```python
import torch

def _softmax(x, axis=-1):
    return torch.softmax(x, dim=axis)
def _gelu(x):
    return torch.nn.functional.gelu(x, approximate="tanh")
def _silu(x):
    return torch.nn.functional.silu(x)
def _sigmoid(x):
    return torch.sigmoid(x)
def _layernorm(x, axis=-1, eps=1e-5):
    m = x.mean(dim=axis, keepdim=True)
    v = x.var(dim=axis, keepdim=True, unbiased=False)
    return (x - m) / torch.sqrt(v + eps)
def _scatter_add(tgt, idx, src, axis=0):
    return tgt.clone().index_add_(axis, idx, src)
def _segment_sum(src, seg, n):
    out = torch.zeros((n,) + src.shape[1:], dtype=src.dtype)
    return out.index_add_(0, seg, src)
def _pad(x, pw):
    flat = tuple(v for pair in reversed(pw) for v in pair)
    return torch.nn.functional.pad(x, flat)

kernel_name = 'standalone__kv_cache_quant_int4_int2_kv_with_per_channel_or_per_token_scales__prompt'

def setup_inputs():

    _w = (lambda a: (lambda t: t.to(torch.bfloat16) if t.is_floating_point() else t)(torch.as_tensor(a)))
    B = 1
    Sq = 2048
    Skv = 2048
    Hq = 32
    Hkv = 32
    Dh = 192
    bits = 8
    G = 64
    if G <= 0 or G >= Skv:
        G = max(1, Skv // 2)
    Skv = (Skv // G) * G
    ngk = Skv // G
    qmax = (1 << bits) - 1
    return {
        "q": _w((lambda *s: torch.randn(*s, dtype=torch.bfloat16) * 0.2)(B, Hq, Sq, Dh)),
        "qk": _w(torch.randint(0, qmax + 1, (B, Hkv, Skv, Dh)).to(torch.int32)),
        "qv": _w(torch.randint(0, qmax + 1, (B, Hkv, Skv, Dh)).to(torch.int32)),
        "k_scales": _w(torch.abs((lambda *s: torch.randn(*s, dtype=torch.bfloat16) * 0.2)(B, Hkv, ngk, Dh)) * 0.05 + 0.02),
        "k_zeros": _w((lambda *s: torch.randn(*s, dtype=torch.bfloat16) * 0.2)(B, Hkv, ngk, Dh) + float(qmax) / 2.0),
        "v_scales": _w(torch.abs((lambda *s: torch.randn(*s, dtype=torch.bfloat16) * 0.05)(B, Hkv, Skv, 1)) * 0.05 + 0.02),
        "v_zeros": _w((lambda *s: torch.randn(*s, dtype=torch.bfloat16) * 0.05)(B, Hkv, Skv, 1) + float(qmax) / 2.0),
    }

def reference(q, qk, qv, k_scales, k_zeros, v_scales, v_zeros):
    B, Hq, Sq, Dh = q.shape
    Hkv, Skv = qk.shape[1], qk.shape[2]
    ngk = k_scales.shape[2]
    G = Skv // ngk
    ks = (lambda x, n, axis: torch.repeat_interleave(x, n, dim=axis))(k_scales, G, axis=2)
    kz = (lambda x, n, axis: torch.repeat_interleave(x, n, dim=axis))(k_zeros, G, axis=2)
    k = ((lambda x, dt: x.to(dt))(qk, q.dtype) - kz) * ks
    v = ((lambda x, dt: x.to(dt))(qv, q.dtype) - v_zeros) * v_scales
    rep = Hq // Hkv
    if rep > 1:
        k = (lambda x, n, axis: torch.repeat_interleave(x, n, dim=axis))(k, rep, axis=1)
        v = (lambda x, n, axis: torch.repeat_interleave(x, n, dim=axis))(v, rep, axis=1)
    scale = float(Dh) ** -0.5
    logits = torch.einsum("bhqd,bhkd->bhqk", q, k) * scale
    p = _softmax(logits, axis=-1)
    o = torch.einsum("bhqk,bhkd->bhqd", p, v)
    return o
```

```cpp
#include <hip/hip_runtime.h>

constexpr int D = 192, NH = 32, S = 2048;
constexpr int SHM = 158208;

struct Args { const unsigned short* q; const int* qk; const int* qv; const unsigned short* ksc; const unsigned short* kzr;
              const unsigned short* vsc; const unsigned short* vzr; char* ws; unsigned short* o; };

__device__ __forceinline__ float bf2f(unsigned short x) { return __uint_as_float((unsigned)x << 16); }
__device__ __forceinline__ float rbf(float x) { return (float)(__bf16)x; }

__device__ __forceinline__ void slow_rows(const Args* a, unsigned h, unsigned row0, unsigned lane) {
  const unsigned short* q = a->q + ((size_t)h * S + row0) * D;
  unsigned short* o = a->o + ((size_t)h * S + row0) * D;
  const int* qk = a->qk + (size_t)h * S * D; const int* qv = a->qv + (size_t)h * S * D;
  const unsigned short* ks = a->ksc + h * 32 * D; const unsigned short* kz = a->kzr + h * 32 * D;
  const unsigned short* vs = a->vsc + h * S; const unsigned short* vz = a->vzr + h * S;
#pragma unroll 1
  for (int r = 0; r < 64; ++r) {
    float m = -INFINITY, l = 0.f, a0 = 0.f, a1 = 0.f, a2 = 0.f;
#pragma unroll 1
    for (int t = 0; t < 32; ++t) {
      const int* kr = qk + (size_t)(t * 64 + lane) * D;
      float s = 0.f;
#pragma unroll 4
      for (int d = 0; d < D; ++d) s = fmaf(bf2f(q[r * D + d]), rbf(rbf(rbf((float)kr[d]) - bf2f(kz[t * D + d])) * bf2f(ks[t * D + d])), s);
      s *= 0.07216878364870322f;
      float mx = s;
      for (int x = 32; x; x >>= 1) mx = fmaxf(mx, __shfl_xor(mx, x));
      const float mn = fmaxf(m, mx), c = __expf(m - mn), p = __expf(s - mn);
      float ps = p;
      for (int x = 32; x; x >>= 1) ps += __shfl_xor(ps, x);
      l = l * c + ps; a0 *= c; a1 *= c; a2 *= c; m = mn;
#pragma unroll 2
      for (int k = 0; k < 64; ++k) {
        const float pk = __shfl(p, k), z = bf2f(vz[t * 64 + k]), sc = bf2f(vs[t * 64 + k]);
        const int* vr = qv + (size_t)(t * 64 + k) * D + lane;
        a0 = fmaf(pk, rbf(rbf(rbf((float)vr[0]) - z) * sc), a0); a1 = fmaf(pk, rbf(rbf(rbf((float)vr[64]) - z) * sc), a1); a2 = fmaf(pk, rbf(rbf(rbf((float)vr[128]) - z) * sc), a2);
      }
    }
    const float i = 1.f / l;
    o[r * D + lane] = __builtin_bit_cast(unsigned short, (__bf16)(a0 * i)); o[r * D + 64 + lane] = __builtin_bit_cast(unsigned short, (__bf16)(a1 * i));
    o[r * D + 128 + lane] = __builtin_bit_cast(unsigned short, (__bf16)(a2 * i));
  }
}

__global__ __launch_bounds__(256, 1) void attn_fwd(const unsigned short* q, const int* qk, const int* qv, const unsigned short* ksc, const unsigned short* kzr,
                                                   const unsigned short* vsc, const unsigned short* vzr, char* ws, unsigned short* o) {
  unsigned long long ka = (unsigned long long)__builtin_amdgcn_kernarg_segment_ptr();
  unsigned bad, b = blockIdx.x;
  const unsigned tid = threadIdx.x;
  asm volatile(
    "s_load_dwordx2 s[20:21], %1, 0\n"
    "s_load_dwordx2 s[36:37], %1, 56\n"
    "s_load_dwordx2 s[24:25], %1, 64\n"
    "v_readfirstlane_b32 s84, %3\n"
    "v_and_b32 v161, 63, %3\n"
    "s_and_b32 s52, %2, 7\n"
    "s_bfe_u32 s72, %2, 0x20003\n"
    "s_lshr_b32 s53, %2, 5\n"
    "s_lshl_b32 s52, s52, 2\n"
    "s_lshr_b32 s84, s84, 6\n"
    "s_or_b32 s52, s52, s72\n"
    "v_lshlrev_b32 v1, 4, v161\n"
    "s_cmp_eq_u32 s84, 1\n"
    "s_cbranch_scc1 .Lp1_1\n"
    "s_cmp_eq_u32 s84, 2\n"
    "s_cbranch_scc1 .Lp1_2\n"
    "s_cmp_eq_u32 s84, 3\n"
    "s_cbranch_scc1 .Lp1_3\n"
    ".Lp1_0:\n"
    "s_load_dwordx2 s[28:29], %1, 8\n"
    "s_load_dwordx2 s[40:41], %1, 24\n"
    "s_load_dwordx2 s[44:45], %1, 32\n"
    "s_mov_b32 s30, 1572864\n"
    "s_mov_b32 s31, 0x20000\n"
    "s_mov_b32 s42, 12288\n"
    "s_mov_b32 s43, 0x20000\n"
    "s_mov_b32 s46, 12288\n"
    "s_mov_b32 s47, 0x20000\n"
    "s_mov_b32 s38, 4224\n"
    "s_mov_b32 s39, 0x20000\n"
    "s_mov_b32 s22, 98304\n"
    "s_mov_b32 s23, 0x20000\n"
    "s_mul_i32 s72, s52, 1572864\n"
    "s_mul_i32 s73, s52, 12288\n"
    "s_mul_i32 s74, s52, 8192\n"
    "s_mul_i32 s75, s52, 786432\n"
    "s_mul_i32 s76, s53, 98304\n"
    "s_add_u32 s75, s75, s76\n"
    "s_mul_i32 s63, s53, 96\n"
    "s_mul_i32 s77, s53, 48\n"
    "v_lshrrev_b32 v232, 2, v161\n"
    "v_and_b32 v233, 3, v161\n"
    "v_mul_u32_u24 v157, 768, v232\n"
    "v_mul_u32_u24 v234, 12, v233\n"
    "v_add3_u32 v157, v157, v234, s77\n"
    "v_add_u32 v157, 0, v157\n"
    "v_and_b32 v235, 3, v232\n"
    "v_mul_u32_u24 v159, 48, v235\n"
    "v_add_u32 v159, v159, v234\n"
    "v_add_u32 v159, 0, v159\n"
    "v_lshlrev_b32 v236, 1, v161\n"
    "v_and_b32 v236, 24, v236\n"
    "v_and_b32 v237, 4, v232\n"
    "v_and_b32 v246, 35, v161\n"
    "v_or3_b32 v246, v246, v236, v237\n"
    "v_mul_u32_u24 v246, 768, v246\n"
    "v_add_u32 v236, 0, v161\n"
    "v_mul_u32_u24 v236, 2731, v236\n"
    "v_lshrrev_b32 v236, 16, v236\n"
    "v_xor_b32 v236, v236, v161\n"
    "v_and_b32 v236, 7, v236\n"
    "v_and_b32 v237, 0x380, v1\n"
    "v_lshl_add_u32 v152, v236, 4, v237\n"
    "v_add_u32 v236, 64, v161\n"
    "v_mul_u32_u24 v236, 2731, v236\n"
    "v_lshrrev_b32 v236, 16, v236\n"
    "v_xor_b32 v236, v236, v161\n"
    "v_and_b32 v236, 7, v236\n"
    "v_and_b32 v237, 0x380, v1\n"
    "v_lshl_add_u32 v153, v236, 4, v237\n"
    "v_add_u32 v236, 128, v161\n"
    "v_mul_u32_u24 v236, 2731, v236\n"
    "v_lshrrev_b32 v236, 16, v236\n"
    "v_xor_b32 v236, v236, v161\n"
    "v_and_b32 v236, 7, v236\n"
    "v_and_b32 v237, 0x380, v1\n"
    "v_lshl_add_u32 v154, v236, 4, v237\n"
    "s_waitcnt lgkmcnt(0)\n"
    "s_mov_b64 s[16:17], s[36:37]\n"
    "s_add_u32 s40, s40, s73\n"
    "s_addc_u32 s41, s41, 0\n"
    "s_add_u32 s44, s44, s73\n"
    "s_addc_u32 s45, s45, 0\n"
    "s_add_u32 s36, s36, s74\n"
    "s_addc_u32 s37, s37, 0\n"
    "s_add_u32 s28, s28, s72\n"
    "s_addc_u32 s29, s29, 0\n"
    "s_add_u32 s20, s20, s75\n"
    "s_addc_u32 s21, s21, 0\n"
    "s_add_u32 s24, s24, s75\n"
    "s_addc_u32 s25, s25, 0\n"
    "buffer_load_dwordx3 v[124:126], v159, s[40:43], 0 offen\n"
    "buffer_load_dwordx3 v[132:134], v159, s[44:47], 0 offen\n"
    "buffer_load_dwordx3 v[120:122], v157, s[40:43], 0 offen\n"
    "buffer_load_dwordx3 v[128:130], v157, s[44:47], 0 offen\n"
    "v_mov_b32 v238, 0\n"
    "buffer_load_dwordx2 v[242:243], v238, s[36:39], 0 offen offset:0 sc1\n"
    "s_add_u32 s80, s63, 98304\n"
    "buffer_load_dwordx4 v[200:203], v246, s[28:31], s80 offen offset:0\n"
    "buffer_load_dwordx4 v[204:207], v246, s[28:31], s80 offen offset:16\n"
    "buffer_load_dwordx4 v[208:211], v246, s[28:31], s80 offen offset:32\n"
    "buffer_load_dwordx4 v[212:215], v246, s[28:31], s80 offen offset:48\n"
    "buffer_load_dwordx4 v[216:219], v246, s[28:31], s80 offen offset:64\n"
    "buffer_load_dwordx4 v[220:223], v246, s[28:31], s80 offen offset:80\n"
    "s_mov_b32 s80, 0\n"
    "buffer_load_dwordx4 v[4:7], v246, s[28:31], s80 offen offset:0\n"
    "buffer_load_dwordx4 v[8:11], v246, s[28:31], s80 offen offset:16\n"
    "buffer_load_dwordx4 v[12:15], v246, s[28:31], s80 offen offset:32\n"
    "buffer_load_dwordx4 v[16:19], v246, s[28:31], s80 offen offset:48\n"
    "buffer_load_dwordx4 v[20:23], v246, s[28:31], s80 offen offset:64\n"
    "buffer_load_dwordx4 v[24:27], v246, s[28:31], s80 offen offset:80\n"
    "s_mov_b32 s80, 96\n"
    "buffer_load_dwordx4 v[28:31], v246, s[28:31], s80 offen offset:0\n"
    "buffer_load_dwordx4 v[32:35], v246, s[28:31], s80 offen offset:16\n"
    "buffer_load_dwordx4 v[36:39], v246, s[28:31], s80 offen offset:32\n"
    "buffer_load_dwordx4 v[40:43], v246, s[28:31], s80 offen offset:48\n"
    "buffer_load_dwordx4 v[44:47], v246, s[28:31], s80 offen offset:64\n"
    "buffer_load_dwordx4 v[48:51], v246, s[28:31], s80 offen offset:80\n"
    "s_mov_b32 s80, 192\n"
    "buffer_load_dwordx4 v[52:55], v246, s[28:31], s80 offen offset:0\n"
    "buffer_load_dwordx4 v[56:59], v246, s[28:31], s80 offen offset:16\n"
    "buffer_load_dwordx4 v[60:63], v246, s[28:31], s80 offen offset:32\n"
    "buffer_load_dwordx4 v[64:67], v246, s[28:31], s80 offen offset:48\n"
    "buffer_load_dwordx4 v[68:71], v246, s[28:31], s80 offen offset:64\n"
    "buffer_load_dwordx4 v[72:75], v246, s[28:31], s80 offen offset:80\n"
    "s_mov_b32 s80, 288\n"
    "buffer_load_dwordx4 v[76:79], v246, s[28:31], s80 offen offset:0\n"
    "buffer_load_dwordx4 v[80:83], v246, s[28:31], s80 offen offset:16\n"
    "buffer_load_dwordx4 v[84:87], v246, s[28:31], s80 offen offset:32\n"
    "buffer_load_dwordx4 v[88:91], v246, s[28:31], s80 offen offset:48\n"
    "buffer_load_dwordx4 v[92:95], v246, s[28:31], s80 offen offset:64\n"
    "buffer_load_dwordx4 v[96:99], v246, s[28:31], s80 offen offset:80\n"
    "s_mov_b32 m0, 49152\n"
    "s_mov_b32 s72, 0\n"
    "buffer_load_dwordx4 v152, s[20:23], s72 offen offset:0 nt lds\n"
    "buffer_load_dwordx4 v153, s[20:23], s72 offen offset:1024 nt lds\n"
    "buffer_load_dwordx4 v154, s[20:23], s72 offen offset:2048 nt lds\n"
    "s_mov_b32 m0, 52224\n"
    "s_mov_b32 s73, 3072\n"
    "buffer_load_dwordx4 v152, s[20:23], s73 offen offset:0 nt lds\n"
    "buffer_load_dwordx4 v153, s[20:23], s73 offen offset:1024 nt lds\n"
    "buffer_load_dwordx4 v154, s[20:23], s73 offen offset:2048 nt lds\n"
    "s_mov_b32 m0, 55296\n"
    "s_mov_b32 s72, 6144\n"
    "buffer_load_dwordx4 v152, s[20:23], s72 offen offset:0 nt lds\n"
    "buffer_load_dwordx4 v153, s[20:23], s72 offen offset:1024 nt lds\n"
    "buffer_load_dwordx4 v154, s[20:23], s72 offen offset:2048 nt lds\n"
    "s_mov_b32 m0, 58368\n"
    "s_mov_b32 s73, 9216\n"
    "buffer_load_dwordx4 v152, s[20:23], s73 offen offset:0 nt lds\n"
    "buffer_load_dwordx4 v153, s[20:23], s73 offen offset:1024 nt lds\n"
    "buffer_load_dwordx4 v154, s[20:23], s73 offen offset:2048 nt lds\n"
    "s_mov_b32 m0, 61440\n"
    "s_mov_b32 s72, 12288\n"
    "buffer_load_dwordx4 v152, s[20:23], s72 offen offset:0 nt lds\n"
    "buffer_load_dwordx4 v153, s[20:23], s72 offen offset:1024 nt lds\n"
    "buffer_load_dwordx4 v154, s[20:23], s72 offen offset:2048 nt lds\n"
    "s_mov_b32 m0, 64512\n"
    "s_mov_b32 s73, 15360\n"
    "buffer_load_dwordx4 v152, s[20:23], s73 offen offset:0 nt lds\n"
    "buffer_load_dwordx4 v153, s[20:23], s73 offen offset:1024 nt lds\n"
    "buffer_load_dwordx4 v154, s[20:23], s73 offen offset:2048 nt lds\n"
    "s_mov_b32 m0, 67584\n"
    "s_mov_b32 s72, 18432\n"
    "buffer_load_dwordx4 v152, s[20:23], s72 offen offset:0 nt lds\n"
    "buffer_load_dwordx4 v153, s[20:23], s72 offen offset:1024 nt lds\n"
    "buffer_load_dwordx4 v154, s[20:23], s72 offen offset:2048 nt lds\n"
    "s_mov_b32 m0, 70656\n"
    "s_mov_b32 s73, 21504\n"
    "buffer_load_dwordx4 v152, s[20:23], s73 offen offset:0 nt lds\n"
    "buffer_load_dwordx4 v153, s[20:23], s73 offen offset:1024 nt lds\n"
    "buffer_load_dwordx4 v154, s[20:23], s73 offen offset:2048 nt lds\n"
    "s_mov_b32 m0, 73728\n"
    "s_mov_b32 s72, 24576\n"
    "buffer_load_dwordx4 v152, s[20:23], s72 offen offset:0 nt lds\n"
    "buffer_load_dwordx4 v153, s[20:23], s72 offen offset:1024 nt lds\n"
    "buffer_load_dwordx4 v154, s[20:23], s72 offen offset:2048 nt lds\n"
    "s_mov_b32 m0, 76800\n"
    "s_mov_b32 s73, 27648\n"
    "buffer_load_dwordx4 v152, s[20:23], s73 offen offset:0 nt lds\n"
    "buffer_load_dwordx4 v153, s[20:23], s73 offen offset:1024 nt lds\n"
    "buffer_load_dwordx4 v154, s[20:23], s73 offen offset:2048 nt lds\n"
    "s_mov_b32 m0, 79872\n"
    "s_mov_b32 s72, 30720\n"
    "buffer_load_dwordx4 v152, s[20:23], s72 offen offset:0 nt lds\n"
    "buffer_load_dwordx4 v153, s[20:23], s72 offen offset:1024 nt lds\n"
    "buffer_load_dwordx4 v154, s[20:23], s72 offen offset:2048 nt lds\n"
    "s_mov_b32 m0, 82944\n"
    "s_mov_b32 s73, 33792\n"
    "buffer_load_dwordx4 v152, s[20:23], s73 offen offset:0 nt lds\n"
    "buffer_load_dwordx4 v153, s[20:23], s73 offen offset:1024 nt lds\n"
    "buffer_load_dwordx4 v154, s[20:23], s73 offen offset:2048 nt lds\n"
    "s_mul_i32 s72, s52, 1576960\n"
    "s_add_u32 s72, s72, 262144\n"
    "s_add_u32 s16, s16, s72\n"
    "s_addc_u32 s17, s17, 0\n"
    "s_mov_b32 s18, 1572864\n"
    "s_mov_b32 s19, 0x20000\n"
    "s_mov_b32 s26, 98304\n"
    "s_mov_b32 s27, 0x20000\n"
    "s_mov_b32 s65, 0\n"
    "s_mov_b32 s66, 0\n"
    "s_mov_b32 s67, 49152\n"
    "s_mov_b32 s68, 49152\n"
    "s_mov_b32 s69, 147456\n"
    "s_mov_b32 s58, 0\n"
    "s_mov_b32 s59, 4\n"
    "s_mul_i32 s73, s53, 3\n"
    "s_add_u32 s74, s73, 0\n"
    "s_lshr_b32 s75, s74, 2\n"
    "s_lshl_b32 s75, s75, 11\n"
    "s_and_b32 s74, s74, 3\n"
    "s_lshl_b32 s74, s74, 8\n"
    "s_or_b32 s60, s74, s75\n"
    "s_add_u32 s74, s73, 1\n"
    "s_lshr_b32 s75, s74, 2\n"
    "s_lshl_b32 s75, s75, 11\n"
    "s_and_b32 s74, s74, 3\n"
    "s_lshl_b32 s74, s74, 8\n"
    "s_or_b32 s61, s74, s75\n"
    "s_add_u32 s74, s73, 2\n"
    "s_lshr_b32 s75, s74, 2\n"
    "s_lshl_b32 s75, s75, 11\n"
    "s_and_b32 s74, s74, 3\n"
    "s_lshl_b32 s74, s74, 8\n"
    "s_or_b32 s62, s74, s75\n"
    "s_cmp_eq_u32 s53, 0\n"
    "s_cselect_b32 s82, 0, 0x7fff0000\n"
    "v_lshrrev_b32 v232, 2, v161\n"
    "v_and_b32 v233, 3, v161\n"
    "v_mul_u32_u24 v158, 192, v232\n"
    "v_mul_u32_u24 v234, 24, v233\n"
    "v_add_u32 v158, v158, v234\n"
    "v_add_u32 v158, 147456, v158\n"
    "v_and_b32 v235, 3, v232\n"
    "v_mul_u32_u24 v160, 192, v235\n"
    "v_add_u32 v160, v160, v234\n"
    "v_add_u32 v160, 150528, v160\n"
    "v_lshrrev_b32 v234, 5, v161\n"
    "v_mul_u32_u24 v248, 12288, v234\n"
    "v_and_b32 v234, 16, v161\n"
    "v_lshl_add_u32 v248, v234, 6, v248\n"
    "v_and_b32 v234, 0xf0, v1\n"
    "v_add_u32 v248, v248, v234\n"
    "v_and_b32 v232, 15, v161\n"
    "v_lshrrev_b32 v233, 4, v161\n"
    "v_and_b32 v234, 7, v161\n"
    "v_xor_b32 v233, v233, v234\n"
    "v_mul_u32_u24 v155, 384, v232\n"
    "v_lshl_add_u32 v155, v233, 4, v155\n"
    "v_add_u32 v155, 49152, v155\n"
    "v_xor_b32 v156, 64, v155\n"
    "v_mov_b32 v2, v1\n"
    "v_add_u32 v3, 49152, v1\n"
    "v_and_b32 v245, 0xf0, v1\n"
    "v_lshrrev_b32 v245, 1, v245\n"
    "v_add_u32 v245, 128, v245\n"
    "s_lshl_b32 s73, s53, 4\n"
    "s_add_u32 s73, s73, 128\n"
    "v_mov_b32 v244, 0x7fff0000\n"
    "v_mov_b32 v239, s73\n"
    "v_cmp_eq_u32 vcc, 0, v1\n"
    "s_nop 1\n"
    "v_cndmask_b32 v244, v244, v239, vcc\n"
    "v_lshlrev_b32 v252, 2, v161\n"
    "v_add_u32 v252, 157952, v252\n"
    "v_mov_b32 v251, 1\n"
    "v_mov_b32 v250, 0\n"
    "ds_write_b32 v252, v250\n"
    "v_accvgpr_write_b32 a0, 0\n"
    "v_accvgpr_write_b32 a1, 0\n"
    "v_accvgpr_write_b32 a2, 0\n"
    "v_accvgpr_write_b32 a3, 0\n"
    "v_accvgpr_write_b32 a4, 0\n"
    "v_accvgpr_write_b32 a5, 0\n"
    "v_accvgpr_write_b32 a6, 0\n"
    "v_accvgpr_write_b32 a7, 0\n"
    "v_accvgpr_write_b32 a8, 0\n"
    "v_accvgpr_write_b32 a9, 0\n"
    "v_accvgpr_write_b32 a10, 0\n"
    "v_accvgpr_write_b32 a11, 0\n"
    "v_accvgpr_write_b32 a12, 0\n"
    "v_accvgpr_write_b32 a13, 0\n"
    "v_accvgpr_write_b32 a14, 0\n"
    "v_accvgpr_write_b32 a15, 0\n"
    "v_accvgpr_write_b32 a16, 0\n"
    "v_accvgpr_write_b32 a17, 0\n"
    "v_accvgpr_write_b32 a18, 0\n"
    "v_accvgpr_write_b32 a19, 0\n"
    "v_accvgpr_write_b32 a20, 0\n"
    "v_accvgpr_write_b32 a21, 0\n"
    "v_accvgpr_write_b32 a22, 0\n"
    "v_accvgpr_write_b32 a23, 0\n"
    "v_accvgpr_write_b32 a24, 0\n"
    "v_accvgpr_write_b32 a25, 0\n"
    "v_accvgpr_write_b32 a26, 0\n"
    "v_accvgpr_write_b32 a27, 0\n"
    "v_accvgpr_write_b32 a28, 0\n"
    "v_accvgpr_write_b32 a29, 0\n"
    "v_accvgpr_write_b32 a30, 0\n"
    "v_accvgpr_write_b32 a31, 0\n"
    "v_accvgpr_write_b32 a32, 0\n"
    "v_accvgpr_write_b32 a33, 0\n"
    "v_accvgpr_write_b32 a34, 0\n"
    "v_accvgpr_write_b32 a35, 0\n"
    "v_accvgpr_write_b32 a36, 0\n"
    "v_accvgpr_write_b32 a37, 0\n"
    "v_accvgpr_write_b32 a38, 0\n"
    "v_accvgpr_write_b32 a39, 0\n"
    "v_accvgpr_write_b32 a40, 0\n"
    "v_accvgpr_write_b32 a41, 0\n"
    "v_accvgpr_write_b32 a42, 0\n"
    "v_accvgpr_write_b32 a43, 0\n"
    "v_accvgpr_write_b32 a44, 0\n"
    "v_accvgpr_write_b32 a45, 0\n"
    "v_accvgpr_write_b32 a46, 0\n"
    "v_accvgpr_write_b32 a47, 0\n"
    "v_accvgpr_write_b32 a48, 0\n"
    "v_accvgpr_write_b32 a49, 0\n"
    "v_accvgpr_write_b32 a50, 0\n"
    "v_accvgpr_write_b32 a51, 0\n"
    "v_accvgpr_write_b32 a52, 0\n"
    "v_accvgpr_write_b32 a53, 0\n"
    "v_accvgpr_write_b32 a54, 0\n"
    "v_accvgpr_write_b32 a55, 0\n"
    "v_accvgpr_write_b32 a56, 0\n"
    "v_accvgpr_write_b32 a57, 0\n"
    "v_accvgpr_write_b32 a58, 0\n"
    "v_accvgpr_write_b32 a59, 0\n"
    "v_accvgpr_write_b32 a60, 0\n"
    "v_accvgpr_write_b32 a61, 0\n"
    "v_accvgpr_write_b32 a62, 0\n"
    "v_accvgpr_write_b32 a63, 0\n"
    "v_accvgpr_write_b32 a64, 0\n"
    "v_accvgpr_write_b32 a65, 0\n"
    "v_accvgpr_write_b32 a66, 0\n"
    "v_accvgpr_write_b32 a67, 0\n"
    "v_accvgpr_write_b32 a68, 0\n"
    "v_accvgpr_write_b32 a69, 0\n"
    "v_accvgpr_write_b32 a70, 0\n"
    "v_accvgpr_write_b32 a71, 0\n"
    "v_accvgpr_write_b32 a72, 0\n"
    "v_accvgpr_write_b32 a73, 0\n"
    "v_accvgpr_write_b32 a74, 0\n"
    "v_accvgpr_write_b32 a75, 0\n"
    "v_accvgpr_write_b32 a76, 0\n"
    "v_accvgpr_write_b32 a77, 0\n"
    "v_accvgpr_write_b32 a78, 0\n"
    "v_accvgpr_write_b32 a79, 0\n"
    "v_accvgpr_write_b32 a80, 0\n"
    "v_accvgpr_write_b32 a81, 0\n"
    "v_accvgpr_write_b32 a82, 0\n"
    "v_accvgpr_write_b32 a83, 0\n"
    "v_accvgpr_write_b32 a84, 0\n"
    "v_accvgpr_write_b32 a85, 0\n"
    "v_accvgpr_write_b32 a86, 0\n"
    "v_accvgpr_write_b32 a87, 0\n"
    "v_accvgpr_write_b32 a88, 0\n"
    "v_accvgpr_write_b32 a89, 0\n"
    "v_accvgpr_write_b32 a90, 0\n"
    "v_accvgpr_write_b32 a91, 0\n"
    "v_accvgpr_write_b32 a92, 0\n"
    "v_accvgpr_write_b32 a93, 0\n"
    "v_accvgpr_write_b32 a94, 0\n"
    "v_accvgpr_write_b32 a95, 0\n"
    "v_accvgpr_write_b32 a96, 0\n"
    "v_accvgpr_write_b32 a97, 0\n"
    "v_accvgpr_write_b32 a98, 0\n"
    "v_accvgpr_write_b32 a99, 0\n"
    "v_accvgpr_write_b32 a100, 0\n"
    "v_accvgpr_write_b32 a101, 0\n"
    "v_accvgpr_write_b32 a102, 0\n"
    "v_accvgpr_write_b32 a103, 0\n"
    "v_accvgpr_write_b32 a104, 0\n"
    "v_accvgpr_write_b32 a105, 0\n"
    "v_accvgpr_write_b32 a106, 0\n"
    "v_accvgpr_write_b32 a107, 0\n"
    "v_accvgpr_write_b32 a108, 0\n"
    "v_accvgpr_write_b32 a109, 0\n"
    "v_accvgpr_write_b32 a110, 0\n"
    "v_accvgpr_write_b32 a111, 0\n"
    "v_accvgpr_write_b32 a112, 0\n"
    "v_accvgpr_write_b32 a113, 0\n"
    "v_accvgpr_write_b32 a114, 0\n"
    "v_accvgpr_write_b32 a115, 0\n"
    "v_accvgpr_write_b32 a116, 0\n"
    "v_accvgpr_write_b32 a117, 0\n"
    "v_accvgpr_write_b32 a118, 0\n"
    "v_accvgpr_write_b32 a119, 0\n"
    "v_accvgpr_write_b32 a120, 0\n"
    "v_accvgpr_write_b32 a121, 0\n"
    "v_accvgpr_write_b32 a122, 0\n"
    "v_accvgpr_write_b32 a123, 0\n"
    "v_accvgpr_write_b32 a124, 0\n"
    "v_accvgpr_write_b32 a125, 0\n"
    "v_accvgpr_write_b32 a126, 0\n"
    "v_accvgpr_write_b32 a127, 0\n"
    "v_accvgpr_write_b32 a128, 0\n"
    "v_accvgpr_write_b32 a129, 0\n"
    "v_accvgpr_write_b32 a130, 0\n"
    "v_accvgpr_write_b32 a131, 0\n"
    "v_accvgpr_write_b32 a132, 0\n"
    "v_accvgpr_write_b32 a133, 0\n"
    "v_accvgpr_write_b32 a134, 0\n"
    "v_accvgpr_write_b32 a135, 0\n"
    "v_accvgpr_write_b32 a136, 0\n"
    "v_accvgpr_write_b32 a137, 0\n"
    "v_accvgpr_write_b32 a138, 0\n"
    "v_accvgpr_write_b32 a139, 0\n"
    "v_accvgpr_write_b32 a140, 0\n"
    "v_accvgpr_write_b32 a141, 0\n"
    "v_accvgpr_write_b32 a142, 0\n"
    "v_accvgpr_write_b32 a143, 0\n"
    "v_accvgpr_write_b32 a144, 0\n"
    "v_accvgpr_write_b32 a145, 0\n"
    "v_accvgpr_write_b32 a146, 0\n"
    "v_accvgpr_write_b32 a147, 0\n"
    "v_accvgpr_write_b32 a148, 0\n"
    "v_accvgpr_write_b32 a149, 0\n"
    "v_accvgpr_write_b32 a150, 0\n"
    "v_accvgpr_write_b32 a151, 0\n"
    "v_accvgpr_write_b32 a152, 0\n"
    "v_accvgpr_write_b32 a153, 0\n"
    "v_accvgpr_write_b32 a154, 0\n"
    "v_accvgpr_write_b32 a155, 0\n"
    "v_accvgpr_write_b32 a156, 0\n"
    "v_accvgpr_write_b32 a157, 0\n"
    "v_accvgpr_write_b32 a158, 0\n"
    "v_accvgpr_write_b32 a159, 0\n"
    "v_accvgpr_write_b32 a160, 0\n"
    "v_accvgpr_write_b32 a161, 0\n"
    "v_accvgpr_write_b32 a162, 0\n"
    "v_accvgpr_write_b32 a163, 0\n"
    "v_accvgpr_write_b32 a164, 0\n"
    "v_accvgpr_write_b32 a165, 0\n"
    "v_accvgpr_write_b32 a166, 0\n"
    "v_accvgpr_write_b32 a167, 0\n"
    "v_accvgpr_write_b32 a168, 0\n"
    "v_accvgpr_write_b32 a169, 0\n"
    "v_accvgpr_write_b32 a170, 0\n"
    "v_accvgpr_write_b32 a171, 0\n"
    "v_accvgpr_write_b32 a172, 0\n"
    "v_accvgpr_write_b32 a173, 0\n"
    "v_accvgpr_write_b32 a174, 0\n"
    "v_accvgpr_write_b32 a175, 0\n"
    "v_accvgpr_write_b32 a176, 0\n"
    "v_accvgpr_write_b32 a177, 0\n"
    "v_accvgpr_write_b32 a178, 0\n"
    "v_accvgpr_write_b32 a179, 0\n"
    "v_accvgpr_write_b32 a180, 0\n"
    "v_accvgpr_write_b32 a181, 0\n"
    "v_accvgpr_write_b32 a182, 0\n"
    "v_accvgpr_write_b32 a183, 0\n"
    "v_accvgpr_write_b32 a184, 0\n"
    "v_accvgpr_write_b32 a185, 0\n"
    "v_accvgpr_write_b32 a186, 0\n"
    "v_accvgpr_write_b32 a187, 0\n"
    "v_accvgpr_write_b32 a188, 0\n"
    "v_accvgpr_write_b32 a189, 0\n"
    "v_accvgpr_write_b32 a190, 0\n"
    "v_accvgpr_write_b32 a191, 0\n"
    "s_waitcnt vmcnt(63)\n"
    "v_add_co_u32 v242, vcc, 0x7f4a7c15, v242\n"
    "v_mov_b32 v238, 0x9e3779b9\n"
    "s_nop 0\n"
    "v_addc_co_u32 v243, vcc, v238, v243, vcc\n"
    "s_mov_b32 s73, 0x3dd53b95\n"
    "v_lshlrev_b32 v136, 16, v124\n"
    "v_lshlrev_b32 v144, 16, v132\n"
    "v_and_b32 v137, 0xffff0000, v124\n"
    "v_and_b32 v145, 0xffff0000, v132\n"
    "v_lshlrev_b32 v138, 16, v125\n"
    "v_lshlrev_b32 v146, 16, v133\n"
    "v_and_b32 v139, 0xffff0000, v125\n"
    "v_and_b32 v147, 0xffff0000, v133\n"
    "v_lshlrev_b32 v140, 16, v126\n"
    "v_lshlrev_b32 v148, 16, v134\n"
    "v_and_b32 v141, 0xffff0000, v126\n"
    "v_and_b32 v149, 0xffff0000, v134\n"
    "v_mul_f32 v136, s73, v136\n"
    "v_mul_f32 v137, s73, v137\n"
    "v_mul_f32 v138, s73, v138\n"
    "v_mul_f32 v139, s73, v139\n"
    "v_mul_f32 v140, s73, v140\n"
    "v_mul_f32 v141, s73, v141\n"
    "v_mul_f32 v144, -v144, v136\n"
    "v_mul_f32 v145, -v145, v137\n"
    "v_mul_f32 v146, -v146, v138\n"
    "v_mul_f32 v147, -v147, v139\n"
    "v_mul_f32 v148, -v148, v140\n"
    "v_mul_f32 v149, -v149, v141\n"
    "ds_write_b64 v160, v[136:137] offset:0\n"
    "ds_write_b64 v160, v[144:145] offset:96\n"
    "ds_write_b64 v160, v[138:139] offset:8\n"
    "ds_write_b64 v160, v[146:147] offset:104\n"
    "ds_write_b64 v160, v[140:141] offset:16\n"
    "ds_write_b64 v160, v[148:149] offset:112\n"
    "v_lshlrev_b32 v136, 16, v120\n"
    "v_lshlrev_b32 v144, 16, v128\n"
    "v_and_b32 v137, 0xffff0000, v120\n"
    "v_and_b32 v145, 0xffff0000, v128\n"
    "v_lshlrev_b32 v138, 16, v121\n"
    "v_lshlrev_b32 v146, 16, v129\n"
    "v_and_b32 v139, 0xffff0000, v121\n"
    "v_and_b32 v147, 0xffff0000, v129\n"
    "v_lshlrev_b32 v140, 16, v122\n"
    "v_lshlrev_b32 v148, 16, v130\n"
    "v_and_b32 v141, 0xffff0000, v122\n"
    "v_and_b32 v149, 0xffff0000, v130\n"
    "v_mul_f32 v136, s73, v136\n"
    "v_mul_f32 v137, s73, v137\n"
    "v_mul_f32 v138, s73, v138\n"
    "v_mul_f32 v139, s73, v139\n"
    "v_mul_f32 v140, s73, v140\n"
    "v_mul_f32 v141, s73, v141\n"
    "v_mul_f32 v144, -v144, v136\n"
    "v_mul_f32 v145, -v145, v137\n"
    "v_mul_f32 v146, -v146, v138\n"
    "v_mul_f32 v147, -v147, v139\n"
    "v_mul_f32 v148, -v148, v140\n"
    "v_mul_f32 v149, -v149, v141\n"
    "ds_write_b64 v158, v[136:137] offset:0\n"
    "ds_write_b64 v158, v[144:145] offset:96\n"
    "ds_write_b64 v158, v[138:139] offset:8\n"
    "ds_write_b64 v158, v[146:147] offset:104\n"
    "ds_write_b64 v158, v[140:141] offset:16\n"
    "ds_write_b64 v158, v[148:149] offset:112\n"
    "s_waitcnt vmcnt(60)\n"
    "s_mov_b32 s71, 98304\n"
    "v_mov_b32 v249, 147648\n"
    "s_add_u32 s76, s71, s60\n"
    "s_add_u32 s77, s71, s61\n"
    "s_add_u32 s78, s71, s62\n"
    "ds_read_b128 v[168:171], v249 offset:0\n"
    "ds_read_b128 v[172:175], v249 offset:96\n"
    "ds_read_b128 v[176:179], v249 offset:16\n"
    "ds_read_b128 v[180:183], v249 offset:112\n"
    "v_cvt_f32_i32 v200, v200\n"
    "v_cvt_f32_i32 v201, v201\n"
    "v_cvt_f32_i32 v202, v202\n"
    "v_cvt_f32_i32 v203, v203\n"
    "v_cvt_f32_i32 v204, v204\n"
    "v_cvt_f32_i32 v205, v205\n"
    "v_cvt_f32_i32 v206, v206\n"
    "v_cvt_f32_i32 v207, v207\n"
    "s_waitcnt lgkmcnt(2)\n"
    "v_fma_f32 v200, v200, v168, v172\n"
    "v_fma_f32 v201, v201, v169, v173\n"
    "v_fma_f32 v202, v202, v170, v174\n"
    "v_fma_f32 v203, v203, v171, v175\n"
    "ds_read_b128 v[168:171], v249 offset:32\n"
    "ds_read_b128 v[172:175], v249 offset:128\n"
    "v_cvt_f32_i32 v208, v208\n"
    "v_cvt_f32_i32 v209, v209\n"
    "v_cvt_f32_i32 v210, v210\n"
    "v_cvt_f32_i32 v211, v211\n"
    "v_cvt_f32_i32 v212, v212\n"
    "v_cvt_f32_i32 v213, v213\n"
    "v_cvt_f32_i32 v214, v214\n"
    "v_cvt_f32_i32 v215, v215\n"
    "s_waitcnt lgkmcnt(2)\n"
    "v_fma_f32 v204, v204, v176, v180\n"
    "v_fma_f32 v205, v205, v177, v181\n"
    "v_fma_f32 v206, v206, v178, v182\n"
    "v_fma_f32 v207, v207, v179, v183\n"
    "ds_read_b128 v[176:179], v249 offset:48\n"
    "ds_read_b128 v[180:183], v249 offset:144\n"
    "v_cvt_pk_bf16_f32 v200, v200, v201\n"
    "v_cvt_pk_bf16_f32 v201, v202, v203\n"
    "v_cvt_pk_bf16_f32 v202, v204, v205\n"
    "v_cvt_pk_bf16_f32 v203, v206, v207\n"
    "buffer_store_dwordx4 v[200:203], v248, s[16:19], s76 offen offset:0 sc1\n"
    "s_waitcnt lgkmcnt(2)\n"
    "v_fma_f32 v208, v208, v168, v172\n"
    "v_fma_f32 v209, v209, v169, v173\n"
    "v_fma_f32 v210, v210, v170, v174\n"
    "v_fma_f32 v211, v211, v171, v175\n"
    "ds_read_b128 v[168:171], v249 offset:64\n"
    "ds_read_b128 v[172:175], v249 offset:160\n"
    "v_cvt_f32_i32 v216, v216\n"
    "v_cvt_f32_i32 v217, v217\n"
    "v_cvt_f32_i32 v218, v218\n"
    "v_cvt_f32_i32 v219, v219\n"
    "v_cvt_f32_i32 v220, v220\n"
    "v_cvt_f32_i32 v221, v221\n"
    "v_cvt_f32_i32 v222, v222\n"
    "v_cvt_f32_i32 v223, v223\n"
    "s_waitcnt lgkmcnt(2)\n"
    "v_fma_f32 v212, v212, v176, v180\n"
    "v_fma_f32 v213, v213, v177, v181\n"
    "v_fma_f32 v214, v214, v178, v182\n"
    "v_fma_f32 v215, v215, v179, v183\n"
    "ds_read_b128 v[176:179], v249 offset:80\n"
    "ds_read_b128 v[180:183], v249 offset:176\n"
    "v_cvt_pk_bf16_f32 v208, v208, v209\n"
    "v_cvt_pk_bf16_f32 v209, v210, v211\n"
    "v_cvt_pk_bf16_f32 v210, v212, v213\n"
    "v_cvt_pk_bf16_f32 v211, v214, v215\n"
    "buffer_store_dwordx4 v[208:211], v248, s[16:19], s77 offen offset:0 sc1\n"
    "s_waitcnt lgkmcnt(2)\n"
    "v_fma_f32 v216, v216, v168, v172\n"
    "v_fma_f32 v217, v217, v169, v173\n"
    "v_fma_f32 v218, v218, v170, v174\n"
    "v_fma_f32 v219, v219, v171, v175\n"
    "s_waitcnt lgkmcnt(0)\n"
    "v_fma_f32 v220, v220, v176, v180\n"
    "v_fma_f32 v221, v221, v177, v181\n"
    "v_fma_f32 v222, v222, v178, v182\n"
    "v_fma_f32 v223, v223, v179, v183\n"
    "v_cvt_pk_bf16_f32 v216, v216, v217\n"
    "v_cvt_pk_bf16_f32 v217, v218, v219\n"
    "v_cvt_pk_bf16_f32 v218, v220, v221\n"
    "v_cvt_pk_bf16_f32 v219, v222, v223\n"
    "buffer_store_dwordx4 v[216:219], v248, s[16:19], s78 offen offset:0 sc1\n"
    "s_waitcnt vmcnt(57)\n"
    "v_mov_b32 v249, 150528\n"
    "ds_read_b128 v[168:171], v249 offset:0\n"
    "ds_read_b128 v[172:175], v249 offset:96\n"
    "ds_read_b128 v[176:179], v249 offset:16\n"
    "ds_read_b128 v[180:183], v249 offset:112\n"
    "v_cvt_f32_i32 v4, v4\n"
    "v_cvt_f32_i32 v5, v5\n"
    "v_cvt_f32_i32 v6, v6\n"
    "v_cvt_f32_i32 v7, v7\n"
    "v_cvt_f32_i32 v8, v8\n"
    "v_cvt_f32_i32 v9, v9\n"
    "v_cvt_f32_i32 v10, v10\n"
    "v_cvt_f32_i32 v11, v11\n"
    "s_waitcnt lgkmcnt(2)\n"
    "v_fma_f32 v4, v4, v168, v172\n"
    "v_fma_f32 v5, v5, v169, v173\n"
    "v_fma_f32 v6, v6, v170, v174\n"
    "v_fma_f32 v7, v7, v171, v175\n"
    "ds_read_b128 v[168:171], v249 offset:32\n"
    "ds_read_b128 v[172:175], v249 offset:128\n"
    "v_cvt_f32_i32 v12, v12\n"
    "v_cvt_f32_i32 v13, v13\n"
    "v_cvt_f32_i32 v14, v14\n"
    "v_cvt_f32_i32 v15, v15\n"
    "v_cvt_f32_i32 v16, v16\n"
    "v_cvt_f32_i32 v17, v17\n"
    "v_cvt_f32_i32 v18, v18\n"
    "v_cvt_f32_i32 v19, v19\n"
    "s_waitcnt lgkmcnt(2)\n"
    "v_fma_f32 v8, v8, v176, v180\n"
    "v_fma_f32 v9, v9, v177, v181\n"
    "v_fma_f32 v10, v10, v178, v182\n"
    "v_fma_f32 v11, v11, v179, v183\n"
    "ds_read_b128 v[176:179], v249 offset:48\n"
    "ds_read_b128 v[180:183], v249 offset:144\n"
    "v_cvt_pk_bf16_f32 v4, v4, v5\n"
    "v_cvt_pk_bf16_f32 v5, v6, v7\n"
    "v_cvt_pk_bf16_f32 v6, v8, v9\n"
    "v_cvt_pk_bf16_f32 v7, v10, v11\n"
    "ds_write_b128 v248, v[4:7] offset:0\n"
    "s_waitcnt lgkmcnt(3)\n"
    "v_fma_f32 v12, v12, v168, v172\n"
    "v_fma_f32 v13, v13, v169, v173\n"
    "v_fma_f32 v14, v14, v170, v174\n"
    "v_fma_f32 v15, v15, v171, v175\n"
    "ds_read_b128 v[168:171], v249 offset:64\n"
    "ds_read_b128 v[172:175], v249 offset:160\n"
    "v_cvt_f32_i32 v20, v20\n"
    "v_cvt_f32_i32 v21, v21\n"
    "v_cvt_f32_i32 v22, v22\n"
    "v_cvt_f32_i32 v23, v23\n"
    "v_cvt_f32_i32 v24, v24\n"
    "v_cvt_f32_i32 v25, v25\n"
    "v_cvt_f32_i32 v26, v26\n"
    "v_cvt_f32_i32 v27, v27\n"
    "s_waitcnt lgkmcnt(3)\n"
    "v_fma_f32 v16, v16, v176, v180\n"
    "v_fma_f32 v17, v17, v177, v181\n"
    "v_fma_f32 v18, v18, v178, v182\n"
    "v_fma_f32 v19, v19, v179, v183\n"
    "ds_read_b128 v[176:179], v249 offset:80\n"
    "ds_read_b128 v[180:183], v249 offset:176\n"
    "v_cvt_pk_bf16_f32 v12, v12, v13\n"
    "v_cvt_pk_bf16_f32 v13, v14, v15\n"
    "v_cvt_pk_bf16_f32 v14, v16, v17\n"
    "v_cvt_pk_bf16_f32 v15, v18, v19\n"
    "ds_write_b128 v248, v[12:15] offset:256\n"
    "s_waitcnt lgkmcnt(3)\n"
    "v_fma_f32 v20, v20, v168, v172\n"
    "v_fma_f32 v21, v21, v169, v173\n"
    "v_fma_f32 v22, v22, v170, v174\n"
    "v_fma_f32 v23, v23, v171, v175\n"
    "s_waitcnt lgkmcnt(1)\n"
    "v_fma_f32 v24, v24, v176, v180\n"
    "v_fma_f32 v25, v25, v177, v181\n"
    "v_fma_f32 v26, v26, v178, v182\n"
    "v_fma_f32 v27, v27, v179, v183\n"
    "v_cvt_pk_bf16_f32 v20, v20, v21\n"
    "v_cvt_pk_bf16_f32 v21, v22, v23\n"
    "v_cvt_pk_bf16_f32 v22, v24, v25\n"
    "v_cvt_pk_bf16_f32 v23, v26, v27\n"
    "ds_write_b128 v248, v[20:23] offset:512\n"
    "s_waitcnt vmcnt(51)\n"
    "v_mov_b32 v249, 150720\n"
    "ds_read_b128 v[168:171], v249 offset:0\n"
    "ds_read_b128 v[172:175], v249 offset:96\n"
    "ds_read_b128 v[176:179], v249 offset:16\n"
    "ds_read_b128 v[180:183], v249 offset:112\n"
    "v_cvt_f32_i32 v28, v28\n"
    "v_cvt_f32_i32 v29, v29\n"
    "v_cvt_f32_i32 v30, v30\n"
    "v_cvt_f32_i32 v31, v31\n"
    "v_cvt_f32_i32 v32, v32\n"
    "v_cvt_f32_i32 v33, v33\n"
    "v_cvt_f32_i32 v34, v34\n"
    "v_cvt_f32_i32 v35, v35\n"
    "s_waitcnt lgkmcnt(2)\n"
    "v_fma_f32 v28, v28, v168, v172\n"
    "v_fma_f32 v29, v29, v169, v173\n"
    "v_fma_f32 v30, v30, v170, v174\n"
    "v_fma_f32 v31, v31, v171, v175\n"
    "ds_read_b128 v[168:171], v249 offset:32\n"
    "ds_read_b128 v[172:175], v249 offset:128\n"
    "v_cvt_f32_i32 v36, v36\n"
    "v_cvt_f32_i32 v37, v37\n"
    "v_cvt_f32_i32 v38, v38\n"
    "v_cvt_f32_i32 v39, v39\n"
    "v_cvt_f32_i32 v40, v40\n"
    "v_cvt_f32_i32 v41, v41\n"
    "v_cvt_f32_i32 v42, v42\n"
    "v_cvt_f32_i32 v43, v43\n"
    "s_waitcnt lgkmcnt(2)\n"
    "v_fma_f32 v32, v32, v176, v180\n"
    "v_fma_f32 v33, v33, v177, v181\n"
    "v_fma_f32 v34, v34, v178, v182\n"
    "v_fma_f32 v35, v35, v179, v183\n"
    "ds_read_b128 v[176:179], v249 offset:48\n"
    "ds_read_b128 v[180:183], v249 offset:144\n"
    "v_cvt_pk_bf16_f32 v28, v28, v29\n"
    "v_cvt_pk_bf16_f32 v29, v30, v31\n"
    "v_cvt_pk_bf16_f32 v30, v32, v33\n"
    "v_cvt_pk_bf16_f32 v31, v34, v35\n"
    "ds_write_b128 v248, v[28:31] offset:768\n"
    "s_waitcnt lgkmcnt(3)\n"
    "v_fma_f32 v36, v36, v168, v172\n"
    "v_fma_f32 v37, v37, v169, v173\n"
    "v_fma_f32 v38, v38, v170, v174\n"
    "v_fma_f32 v39, v39, v171, v175\n"
    "ds_read_b128 v[168:171], v249 offset:64\n"
    "ds_read_b128 v[172:175], v249 offset:160\n"
    "v_cvt_f32_i32 v44, v44\n"
    "v_cvt_f32_i32 v45, v45\n"
    "v_cvt_f32_i32 v46, v46\n"
    "v_cvt_f32_i32 v47, v47\n"
    "v_cvt_f32_i32 v48, v48\n"
    "v_cvt_f32_i32 v49, v49\n"
    "v_cvt_f32_i32 v50, v50\n"
    "v_cvt_f32_i32 v51, v51\n"
    "s_waitcnt lgkmcnt(3)\n"
    "v_fma_f32 v40, v40, v176, v180\n"
    "v_fma_f32 v41, v41, v177, v181\n"
    "v_fma_f32 v42, v42, v178, v182\n"
    "v_fma_f32 v43, v43, v179, v183\n"
    "ds_read_b128 v[176:179], v249 offset:80\n"
    "ds_read_b128 v[180:183], v249 offset:176\n"
    "v_cvt_pk_bf16_f32 v36, v36, v37\n"
    "v_cvt_pk_bf16_f32 v37, v38, v39\n"
    "v_cvt_pk_bf16_f32 v38, v40, v41\n"
    "v_cvt_pk_bf16_f32 v39, v42, v43\n"
    "ds_write_b128 v248, v[36:39] offset:2048\n"
    "s_waitcnt lgkmcnt(3)\n"
    "v_fma_f32 v44, v44, v168, v172\n"
    "v_fma_f32 v45, v45, v169, v173\n"
    "v_fma_f32 v46, v46, v170, v174\n"
    "v_fma_f32 v47, v47, v171, v175\n"
    "s_waitcnt lgkmcnt(1)\n"
    "v_fma_f32 v48, v48, v176, v180\n"
    "v_fma_f32 v49, v49, v177, v181\n"
    "v_fma_f32 v50, v50, v178, v182\n"
    "v_fma_f32 v51, v51, v179, v183\n"
    "v_cvt_pk_bf16_f32 v44, v44, v45\n"
    "v_cvt_pk_bf16_f32 v45, v46, v47\n"
    "v_cvt_pk_bf16_f32 v46, v48, v49\n"
    "v_cvt_pk_bf16_f32 v47, v50, v51\n"
    "ds_write_b128 v248, v[44:47] offset:2304\n"
    "s_waitcnt vmcnt(45)\n"
    "v_mov_b32 v249, 150912\n"
    "ds_read_b128 v[168:171], v249 offset:0\n"
    "ds_read_b128 v[172:175], v249 offset:96\n"
    "ds_read_b128 v[176:179], v249 offset:16\n"
    "ds_read_b128 v[180:183], v249 offset:112\n"
    "v_cvt_f32_i32 v52, v52\n"
    "v_cvt_f32_i32 v53, v53\n"
    "v_cvt_f32_i32 v54, v54\n"
    "v_cvt_f32_i32 v55, v55\n"
    "v_cvt_f32_i32 v56, v56\n"
    "v_cvt_f32_i32 v57, v57\n"
    "v_cvt_f32_i32 v58, v58\n"
    "v_cvt_f32_i32 v59, v59\n"
    "s_waitcnt lgkmcnt(2)\n"
    "v_fma_f32 v52, v52, v168, v172\n"
    "v_fma_f32 v53, v53, v169, v173\n"
    "v_fma_f32 v54, v54, v170, v174\n"
    "v_fma_f32 v55, v55, v171, v175\n"
    "ds_read_b128 v[168:171], v249 offset:32\n"
    "ds_read_b128 v[172:175], v249 offset:128\n"
    "v_cvt_f32_i32 v60, v60\n"
    "v_cvt_f32_i32 v61, v61\n"
    "v_cvt_f32_i32 v62, v62\n"
    "v_cvt_f32_i32 v63, v63\n"
    "v_cvt_f32_i32 v64, v64\n"
    "v_cvt_f32_i32 v65, v65\n"
    "v_cvt_f32_i32 v66, v66\n"
    "v_cvt_f32_i32 v67, v67\n"
    "s_waitcnt lgkmcnt(2)\n"
    "v_fma_f32 v56, v56, v176, v180\n"
    "v_fma_f32 v57, v57, v177, v181\n"
    "v_fma_f32 v58, v58, v178, v182\n"
    "v_fma_f32 v59, v59, v179, v183\n"
    "ds_read_b128 v[176:179], v249 offset:48\n"
    "ds_read_b128 v[180:183], v249 offset:144\n"
    "v_cvt_pk_bf16_f32 v52, v52, v53\n"
    "v_cvt_pk_bf16_f32 v53, v54, v55\n"
    "v_cvt_pk_bf16_f32 v54, v56, v57\n"
    "v_cvt_pk_bf16_f32 v55, v58, v59\n"
    "ds_write_b128 v248, v[52:55] offset:2560\n"
    "s_waitcnt lgkmcnt(3)\n"
    "v_fma_f32 v60, v60, v168, v172\n"
    "v_fma_f32 v61, v61, v169, v173\n"
    "v_fma_f32 v62, v62, v170, v174\n"
    "v_fma_f32 v63, v63, v171, v175\n"
    "ds_read_b128 v[168:171], v249 offset:64\n"
    "ds_read_b128 v[172:175], v249 offset:160\n"
    "v_cvt_f32_i32 v68, v68\n"
    "v_cvt_f32_i32 v69, v69\n"
    "v_cvt_f32_i32 v70, v70\n"
    "v_cvt_f32_i32 v71, v71\n"
    "v_cvt_f32_i32 v72, v72\n"
    "v_cvt_f32_i32 v73, v73\n"
    "v_cvt_f32_i32 v74, v74\n"
    "v_cvt_f32_i32 v75, v75\n"
    "s_waitcnt lgkmcnt(3)\n"
    "v_fma_f32 v64, v64, v176, v180\n"
    "v_fma_f32 v65, v65, v177, v181\n"
    "v_fma_f32 v66, v66, v178, v182\n"
    "v_fma_f32 v67, v67, v179, v183\n"
    "ds_read_b128 v[176:179], v249 offset:80\n"
    "ds_read_b128 v[180:183], v249 offset:176\n"
    "v_cvt_pk_bf16_f32 v60, v60, v61\n"
    "v_cvt_pk_bf16_f32 v61, v62, v63\n"
    "v_cvt_pk_bf16_f32 v62, v64, v65\n"
    "v_cvt_pk_bf16_f32 v63, v66, v67\n"
    "ds_write_b128 v248, v[60:63] offset:2816\n"
    "s_waitcnt lgkmcnt(3)\n"
    "v_fma_f32 v68, v68, v168, v172\n"
    "v_fma_f32 v69, v69, v169, v173\n"
    "v_fma_f32 v70, v70, v170, v174\n"
    "v_fma_f32 v71, v71, v171, v175\n"
    "s_waitcnt lgkmcnt(1)\n"
    "v_fma_f32 v72, v72, v176, v180\n"
    "v_fma_f32 v73, v73, v177, v181\n"
    "v_fma_f32 v74, v74, v178, v182\n"
    "v_fma_f32 v75, v75, v179, v183\n"
    "v_cvt_pk_bf16_f32 v68, v68, v69\n"
    "v_cvt_pk_bf16_f32 v69, v70, v71\n"
    "v_cvt_pk_bf16_f32 v70, v72, v73\n"
    "v_cvt_pk_bf16_f32 v71, v74, v75\n"
    "ds_write_b128 v248, v[68:71] offset:4096\n"
    "s_waitcnt vmcnt(39)\n"
    "v_mov_b32 v249, 151104\n"
    "ds_read_b128 v[168:171], v249 offset:0\n"
    "ds_read_b128 v[172:175], v249 offset:96\n"
    "ds_read_b128 v[176:179], v249 offset:16\n"
    "ds_read_b128 v[180:183], v249 offset:112\n"
    "v_cvt_f32_i32 v76, v76\n"
    "v_cvt_f32_i32 v77, v77\n"
    "v_cvt_f32_i32 v78, v78\n"
    "v_cvt_f32_i32 v79, v79\n"
    "v_cvt_f32_i32 v80, v80\n"
    "v_cvt_f32_i32 v81, v81\n"
    "v_cvt_f32_i32 v82, v82\n"
    "v_cvt_f32_i32 v83, v83\n"
    "s_waitcnt lgkmcnt(2)\n"
    "v_fma_f32 v76, v76, v168, v172\n"
    "v_fma_f32 v77, v77, v169, v173\n"
    "v_fma_f32 v78, v78, v170, v174\n"
    "v_fma_f32 v79, v79, v171, v175\n"
    "ds_read_b128 v[168:171], v249 offset:32\n"
    "ds_read_b128 v[172:175], v249 offset:128\n"
    "v_cvt_f32_i32 v84, v84\n"
    "v_cvt_f32_i32 v85, v85\n"
    "v_cvt_f32_i32 v86, v86\n"
    "v_cvt_f32_i32 v87, v87\n"
    "v_cvt_f32_i32 v88, v88\n"
    "v_cvt_f32_i32 v89, v89\n"
    "v_cvt_f32_i32 v90, v90\n"
    "v_cvt_f32_i32 v91, v91\n"
    "s_waitcnt lgkmcnt(2)\n"
    "v_fma_f32 v80, v80, v176, v180\n"
    "v_fma_f32 v81, v81, v177, v181\n"
    "v_fma_f32 v82, v82, v178, v182\n"
    "v_fma_f32 v83, v83, v179, v183\n"
    "ds_read_b128 v[176:179], v249 offset:48\n"
    "ds_read_b128 v[180:183], v249 offset:144\n"
    "v_cvt_pk_bf16_f32 v76, v76, v77\n"
    "v_cvt_pk_bf16_f32 v77, v78, v79\n"
    "v_cvt_pk_bf16_f32 v78, v80, v81\n"
    "v_cvt_pk_bf16_f32 v79, v82, v83\n"
    "ds_write_b128 v248, v[76:79] offset:4352\n"
    "s_waitcnt lgkmcnt(3)\n"
    "v_fma_f32 v84, v84, v168, v172\n"
    "v_fma_f32 v85, v85, v169, v173\n"
    "v_fma_f32 v86, v86, v170, v174\n"
    "v_fma_f32 v87, v87, v171, v175\n"
    "ds_read_b128 v[168:171], v249 offset:64\n"
    "ds_read_b128 v[172:175], v249 offset:160\n"
    "v_cvt_f32_i32 v92, v92\n"
    "v_cvt_f32_i32 v93, v93\n"
    "v_cvt_f32_i32 v94, v94\n"
    "v_cvt_f32_i32 v95, v95\n"
    "v_cvt_f32_i32 v96, v96\n"
    "v_cvt_f32_i32 v97, v97\n"
    "v_cvt_f32_i32 v98, v98\n"
    "v_cvt_f32_i32 v99, v99\n"
    "s_waitcnt lgkmcnt(3)\n"
    "v_fma_f32 v88, v88, v176, v180\n"
    "v_fma_f32 v89, v89, v177, v181\n"
    "v_fma_f32 v90, v90, v178, v182\n"
    "v_fma_f32 v91, v91, v179, v183\n"
    "ds_read_b128 v[176:179], v249 offset:80\n"
    "ds_read_b128 v[180:183], v249 offset:176\n"
    "v_cvt_pk_bf16_f32 v84, v84, v85\n"
    "v_cvt_pk_bf16_f32 v85, v86, v87\n"
    "v_cvt_pk_bf16_f32 v86, v88, v89\n"
    "v_cvt_pk_bf16_f32 v87, v90, v91\n"
    "ds_write_b128 v248, v[84:87] offset:4608\n"
    "s_waitcnt lgkmcnt(3)\n"
    "v_fma_f32 v92, v92, v168, v172\n"
    "v_fma_f32 v93, v93, v169, v173\n"
    "v_fma_f32 v94, v94, v170, v174\n"
    "v_fma_f32 v95, v95, v171, v175\n"
    "s_waitcnt lgkmcnt(1)\n"
    "v_fma_f32 v96, v96, v176, v180\n"
    "v_fma_f32 v97, v97, v177, v181\n"
    "v_fma_f32 v98, v98, v178, v182\n"
    "v_fma_f32 v99, v99, v179, v183\n"
    "v_cvt_pk_bf16_f32 v92, v92, v93\n"
    "v_cvt_pk_bf16_f32 v93, v94, v95\n"
    "v_cvt_pk_bf16_f32 v94, v96, v97\n"
    "v_cvt_pk_bf16_f32 v95, v98, v99\n"
    "ds_write_b128 v248, v[92:95] offset:4864\n"
    "s_waitcnt vmcnt(3)\n"
    "s_waitcnt lgkmcnt(0)\n"
    "s_barrier\n"
    "ds_read_b128 a[192:195], v155 offset:0\n"
    "ds_read_b128 a[196:199], v156 offset:0\n"
    "ds_read_b128 a[200:203], v155 offset:128\n"
    "ds_read_b128 a[204:207], v156 offset:128\n"
    "ds_read_b128 a[208:211], v155 offset:256\n"
    "ds_read_b128 a[212:215], v156 offset:256\n"
    "ds_read_b128 a[216:219], v155 offset:6144\n"
    "ds_read_b128 a[220:223], v156 offset:6144\n"
    "ds_read_b128 a[224:227], v155 offset:6272\n"
    "ds_read_b128 a[228:231], v156 offset:6272\n"
    "ds_read_b128 a[232:235], v155 offset:6400\n"
    "ds_read_b128 a[236:239], v156 offset:6400\n"
    "ds_read_b128 a[240:243], v155 offset:12288\n"
    "ds_read_b128 a[244:247], v156 offset:12288\n"
    "ds_read_b128 a[248:251], v155 offset:12416\n"
    "ds_read_b128 a[252:255], v156 offset:12416\n"
    "ds_read_b128 v[120:123], v155 offset:12544\n"
    "ds_read_b128 v[124:127], v156 offset:12544\n"
    "ds_read_b128 v[128:131], v155 offset:18432\n"
    "ds_read_b128 v[132:135], v156 offset:18432\n"
    "ds_read_b128 v[136:139], v155 offset:18560\n"
    "ds_read_b128 v[140:143], v156 offset:18560\n"
    "ds_read_b128 v[144:147], v155 offset:18688\n"
    "ds_read_b128 v[148:151], v156 offset:18688\n"
    "s_waitcnt lgkmcnt(0)\n"
    "s_barrier\n"
    "v_and_b32 v68, 0x1f0, v1\n"
    "v_lshrrev_b32 v68, 1, v68\n"
    "v_add_u32 v68, 128, v68\n"
    "s_mov_b32 s81, 0\n"
    "buffer_load_dwordx2 v[240:241], v68, s[36:39], s81 offen offset:0 sc1\n"
    "s_add_u32 s80, s63, 196608\n"
    "buffer_load_dwordx4 v[200:203], v246, s[28:31], s80 offen offset:0\n"
    "buffer_load_dwordx4 v[204:207], v246, s[28:31], s80 offen offset:16\n"
    "buffer_load_dwordx4 v[208:211], v246, s[28:31], s80 offen offset:32\n"
    "buffer_load_dwordx4 v[212:215], v246, s[28:31], s80 offen offset:48\n"
    "buffer_load_dwordx4 v[216:219], v246, s[28:31], s80 offen offset:64\n"
    "buffer_load_dwordx4 v[220:223], v246, s[28:31], s80 offen offset:80\n"
    "ds_read_b128 v[152:155], v2 offset:0\n"
    "ds_read_b128 v[156:159], v2 offset:2048\n"
    "ds_read_b128 v[160:163], v2 offset:4096\n"
    "ds_read_b128 v[164:167], v2 offset:6144\n"
    "ds_read_b128 v[168:171], v2 offset:8192\n"
    "ds_read_b128 v[172:175], v2 offset:10240\n"
    "ds_read_b128 v[176:179], v2 offset:1024\n"
    "ds_read_b128 v[180:183], v2 offset:3072\n"
    "ds_read_b128 v[184:187], v2 offset:5120\n"
    "ds_read_b128 v[188:191], v2 offset:7168\n"
    "ds_read_b128 v[192:195], v2 offset:9216\n"
    "ds_read_b128 v[196:199], v2 offset:11264\n"
    "s_waitcnt lgkmcnt(11)\n"
    "v_mfma_f32_16x16x32_bf16 v[4:7], v[152:155], a[192:195], 0\n"
    "s_waitcnt lgkmcnt(10)\n"
    "v_mfma_f32_16x16x32_bf16 v[4:7], v[156:159], a[196:199], v[4:7]\n"
    "s_waitcnt lgkmcnt(9)\n"
    "v_mfma_f32_16x16x32_bf16 v[4:7], v[160:163], a[200:203], v[4:7]\n"
    "s_waitcnt lgkmcnt(8)\n"
    "v_mfma_f32_16x16x32_bf16 v[4:7], v[164:167], a[204:207], v[4:7]\n"
    "s_waitcnt lgkmcnt(7)\n"
    "v_mfma_f32_16x16x32_bf16 v[4:7], v[168:171], a[208:211], v[4:7]\n"
    "s_waitcnt lgkmcnt(6)\n"
    "v_mfma_f32_16x16x32_bf16 v[4:7], v[172:175], a[212:215], v[4:7]\n"
    "v_mfma_f32_16x16x32_bf16 v[8:11], v[172:175], a[236:239], 0\n"
    "v_mfma_f32_16x16x32_bf16 v[8:11], v[168:171], a[232:235], v[8:11]\n"
    "v_mfma_f32_16x16x32_bf16 v[8:11], v[164:167], a[228:231], v[8:11]\n"
    "v_mfma_f32_16x16x32_bf16 v[8:11], v[160:163], a[224:227], v[8:11]\n"
    "v_mfma_f32_16x16x32_bf16 v[8:11], v[156:159], a[220:223], v[8:11]\n"
    "v_mfma_f32_16x16x32_bf16 v[8:11], v[152:155], a[216:219], v[8:11]\n"
    "v_mfma_f32_16x16x32_bf16 v[12:15], v[152:155], a[240:243], 0\n"
    "v_mfma_f32_16x16x32_bf16 v[12:15], v[156:159], a[244:247], v[12:15]\n"
    "v_mfma_f32_16x16x32_bf16 v[12:15], v[160:163], a[248:251], v[12:15]\n"
    "v_mfma_f32_16x16x32_bf16 v[12:15], v[164:167], a[252:255], v[12:15]\n"
    "v_mfma_f32_16x16x32_bf16 v[12:15], v[168:171], v[120:123], v[12:15]\n"
    "v_mfma_f32_16x16x32_bf16 v[12:15], v[172:175], v[124:127], v[12:15]\n"
    "v_mfma_f32_16x16x32_bf16 v[16:19], v[172:175], v[148:151], 0\n"
    "v_mfma_f32_16x16x32_bf16 v[16:19], v[168:171], v[144:147], v[16:19]\n"
    "v_mfma_f32_16x16x32_bf16 v[16:19], v[164:167], v[140:143], v[16:19]\n"
    "v_mfma_f32_16x16x32_bf16 v[16:19], v[160:163], v[136:139], v[16:19]\n"
    "v_mfma_f32_16x16x32_bf16 v[16:19], v[156:159], v[132:135], v[16:19]\n"
    "v_mfma_f32_16x16x32_bf16 v[16:19], v[152:155], v[128:131], v[16:19]\n"
    "s_waitcnt lgkmcnt(5)\n"
    "v_mfma_f32_16x16x32_bf16 v[20:23], v[176:179], a[192:195], 0\n"
    "s_waitcnt lgkmcnt(4)\n"
    "v_mfma_f32_16x16x32_bf16 v[20:23], v[180:183], a[196:199], v[20:23]\n"
    "s_waitcnt lgkmcnt(3)\n"
    "v_mfma_f32_16x16x32_bf16 v[20:23], v[184:187], a[200:203], v[20:23]\n"
    "s_waitcnt lgkmcnt(2)\n"
    "v_mfma_f32_16x16x32_bf16 v[20:23], v[188:191], a[204:207], v[20:23]\n"
    "s_waitcnt lgkmcnt(1)\n"
    "v_mfma_f32_16x16x32_bf16 v[20:23], v[192:195], a[208:211], v[20:23]\n"
    "s_waitcnt lgkmcnt(0)\n"
    "v_mfma_f32_16x16x32_bf16 v[20:23], v[196:199], a[212:215], v[20:23]\n"
    "v_mfma_f32_16x16x32_bf16 v[24:27], v[196:199], a[236:239], 0\n"
    "v_mfma_f32_16x16x32_bf16 v[24:27], v[192:195], a[232:235], v[24:27]\n"
    "v_mfma_f32_16x16x32_bf16 v[24:27], v[188:191], a[228:231], v[24:27]\n"
    "v_mfma_f32_16x16x32_bf16 v[24:27], v[184:187], a[224:227], v[24:27]\n"
    "v_mfma_f32_16x16x32_bf16 v[24:27], v[180:183], a[220:223], v[24:27]\n"
    "v_mfma_f32_16x16x32_bf16 v[24:27], v[176:179], a[216:219], v[24:27]\n"
    "v_mfma_f32_16x16x32_bf16 v[28:31], v[176:179], a[240:243], 0\n"
    "v_mfma_f32_16x16x32_bf16 v[28:31], v[180:183], a[244:247], v[28:31]\n"
    "v_mfma_f32_16x16x32_bf16 v[28:31], v[184:187], a[248:251], v[28:31]\n"
    "v_mfma_f32_16x16x32_bf16 v[28:31], v[188:191], a[252:255], v[28:31]\n"
    "v_mfma_f32_16x16x32_bf16 v[28:31], v[192:195], v[120:123], v[28:31]\n"
    "v_mfma_f32_16x16x32_bf16 v[28:31], v[196:199], v[124:127], v[28:31]\n"
    "v_mfma_f32_16x16x32_bf16 v[32:35], v[196:199], v[148:151], 0\n"
    "v_mfma_f32_16x16x32_bf16 v[32:35], v[192:195], v[144:147], v[32:35]\n"
    "v_mfma_f32_16x16x32_bf16 v[32:35], v[188:191], v[140:143], v[32:35]\n"
    "v_mfma_f32_16x16x32_bf16 v[32:35], v[184:187], v[136:139], v[32:35]\n"
    "v_mfma_f32_16x16x32_bf16 v[32:35], v[180:183], v[132:135], v[32:35]\n"
    "v_mfma_f32_16x16x32_bf16 v[32:35], v[176:179], v[128:131], v[32:35]\n"
    "s_waitcnt vmcnt(6)\n"
    "s_mov_b32 s80, 256\n"
    "buffer_store_dwordx2 v[242:243], v244, s[36:39], s80 offen offset:0 sc1\n"
    "v_cmp_eq_u64 s[54:55], v[240:241], v[242:243]\n"
    "s_nop 1\n"
    "s_mov_b32 s74, 0x00005555\n"
    "s_mov_b32 s75, 0x00005555\n"
    "s_or_b64 s[54:55], s[54:55], s[74:75]\n"
    "s_cmp_eq_u64 s[54:55], -1\n"
    "s_cbranch_scc0 .LpaK0\n"
    ".LpaK0r:\n"
    "s_add_u32 s72, s65, 24576\n"
    "s_add_u32 s73, s65, 28672\n"
    "s_mov_b32 s81, 256\n"
    "s_add_u32 m0, s68, 0\n"
    "s_nop 0\n"
    "buffer_load_dwordx4 v1, s[16:19], s67 offen offset:0 sc1 lds\n"
    "v_max3_f32 v232, v4, v5, v6\n"
    "v_max3_f32 v232, v232, v7, v20\n"
    "v_max3_f32 v232, v232, v21, v22\n"
    "v_max_f32 v232, v232, v23\n"
    "v_max3_f32 v233, v8, v9, v10\n"
    "v_max3_f32 v233, v233, v11, v24\n"
    "v_max3_f32 v233, v233, v25, v26\n"
    "v_max_f32 v233, v233, v27\n"
    "v_max3_f32 v234, v12, v13, v14\n"
    "v_max3_f32 v234, v234, v15, v28\n"
    "v_max3_f32 v234, v234, v29, v30\n"
    "v_max_f32 v234, v234, v31\n"
    "v_max3_f32 v235, v16, v17, v18\n"
    "buffer_load_dwordx4 v1, s[16:19], s67 offen offset:1024 sc1 lds\n"
    "v_max3_f32 v235, v235, v19, v32\n"
    "v_max3_f32 v235, v235, v33, v34\n"
    "v_max_f32 v235, v235, v35\n"
    "v_mov_b32 v236, v232\n"
    "s_nop 1\n"
    "v_permlane32_swap_b32 v232, v236\n"
    "v_max_f32 v232, v232, v236\n"
    "v_mov_b32 v236, v232\n"
    "s_nop 1\n"
    "v_permlane16_swap_b32 v232, v236\n"
    "v_max_f32 v232, v232, v236\n"
    "v_add_f32 v232, 0x42000000, v232\n"
    "v_sub_f32 v100, 0, v232\n"
    "v_sub_f32 v101, 0, v232\n"
    "v_sub_f32 v102, 0, v232\n"
    "buffer_load_dwordx4 v1, s[16:19], s67 offen offset:2048 sc1 lds\n"
    "v_sub_f32 v103, 0, v232\n"
    "v_mov_b32 v236, v233\n"
    "s_nop 1\n"
    "v_permlane32_swap_b32 v233, v236\n"
    "v_max_f32 v233, v233, v236\n"
    "v_mov_b32 v236, v233\n"
    "s_nop 1\n"
    "v_permlane16_swap_b32 v233, v236\n"
    "v_max_f32 v233, v233, v236\n"
    "v_add_f32 v233, 0x42000000, v233\n"
    "v_sub_f32 v104, 0, v233\n"
    "v_sub_f32 v105, 0, v233\n"
    "v_sub_f32 v106, 0, v233\n"
    "v_sub_f32 v107, 0, v233\n"
    "v_mov_b32 v236, v234\n"
    "buffer_load_dwordx4 v1, s[16:19], s67 offen offset:3072 sc1 lds\n"
    "s_nop 0\n"
    "v_permlane32_swap_b32 v234, v236\n"
    "v_max_f32 v234, v234, v236\n"
    "v_mov_b32 v236, v234\n"
    "s_nop 1\n"
    "v_permlane16_swap_b32 v234, v236\n"
    "v_max_f32 v234, v234, v236\n"
    "v_add_f32 v234, 0x42000000, v234\n"
    "v_sub_f32 v108, 0, v234\n"
    "v_sub_f32 v109, 0, v234\n"
    "v_sub_f32 v110, 0, v234\n"
    "v_sub_f32 v111, 0, v234\n"
    "v_mov_b32 v236, v235\n"
    "s_nop 1\n"
    "v_permlane32_swap_b32 v235, v236\n"
    "v_max_f32 v235, v235, v236\n"
    "s_add_u32 m0, s68, 4096\n"
    "s_add_u32 s83, s67, 4096\n"
    "buffer_load_dwordx4 v1, s[16:19], s83 offen offset:0 sc1 lds\n"
    "v_mov_b32 v236, v235\n"
    "s_nop 1\n"
    "v_permlane16_swap_b32 v235, v236\n"
    "v_max_f32 v235, v235, v236\n"
    "v_add_f32 v235, 0x42000000, v235\n"
    "v_sub_f32 v112, 0, v235\n"
    "v_sub_f32 v113, 0, v235\n"
    "v_sub_f32 v114, 0, v235\n"
    "v_sub_f32 v115, 0, v235\n"
    "v_sub_f32 v4, v4, v232\n"
    "v_sub_f32 v5, v5, v232\n"
    "v_sub_f32 v6, v6, v232\n"
    "v_sub_f32 v7, v7, v232\n"
    "v_sub_f32 v8, v8, v233\n"
    "buffer_load_dwordx4 v1, s[16:19], s83 offen offset:1024 sc1 lds\n"
    "v_sub_f32 v9, v9, v233\n"
    "v_sub_f32 v10, v10, v233\n"
    "v_sub_f32 v11, v11, v233\n"
    "v_sub_f32 v12, v12, v234\n"
    "v_sub_f32 v13, v13, v234\n"
    "v_sub_f32 v14, v14, v234\n"
    "v_sub_f32 v15, v15, v234\n"
    "v_sub_f32 v16, v16, v235\n"
    "v_sub_f32 v17, v17, v235\n"
    "v_sub_f32 v18, v18, v235\n"
    "v_sub_f32 v19, v19, v235\n"
    "v_sub_f32 v20, v20, v232\n"
    "v_sub_f32 v21, v21, v232\n"
    "s_mov_b32 m0, s72\n"
    "s_nop 0\n"
    "buffer_load_dwordx4 v1, s[16:19], s72 offen offset:0 sc1 lds\n"
    "v_sub_f32 v22, v22, v232\n"
    "v_sub_f32 v23, v23, v232\n"
    "v_sub_f32 v24, v24, v233\n"
    "v_sub_f32 v25, v25, v233\n"
    "v_sub_f32 v26, v26, v233\n"
    "v_sub_f32 v27, v27, v233\n"
    "v_sub_f32 v28, v28, v234\n"
    "v_sub_f32 v29, v29, v234\n"
    "v_sub_f32 v30, v30, v234\n"
    "v_sub_f32 v31, v31, v234\n"
    "v_sub_f32 v32, v32, v235\n"
    "v_sub_f32 v33, v33, v235\n"
    "v_sub_f32 v34, v34, v235\n"
    "buffer_load_dwordx4 v1, s[16:19], s72 offen offset:1024 sc1 lds\n"
    "v_sub_f32 v35, v35, v235\n"
    "v_exp_f32 v4, v4\n"
    "v_exp_f32 v5, v5\n"
    "v_exp_f32 v6, v6\n"
    "v_exp_f32 v7, v7\n"
    "v_exp_f32 v8, v8\n"
    "v_exp_f32 v9, v9\n"
    "v_exp_f32 v10, v10\n"
    "v_exp_f32 v11, v11\n"
    "v_exp_f32 v12, v12\n"
    "v_exp_f32 v13, v13\n"
    "v_exp_f32 v14, v14\n"
    "v_exp_f32 v15, v15\n"
    "buffer_load_dwordx4 v1, s[16:19], s72 offen offset:2048 sc1 lds\n"
    "v_exp_f32 v16, v16\n"
    "v_exp_f32 v17, v17\n"
    "v_exp_f32 v18, v18\n"
    "v_exp_f32 v19, v19\n"
    "v_exp_f32 v20, v20\n"
    "v_exp_f32 v21, v21\n"
    "v_exp_f32 v22, v22\n"
    "v_exp_f32 v23, v23\n"
    "v_exp_f32 v24, v24\n"
    "v_exp_f32 v25, v25\n"
    "v_exp_f32 v26, v26\n"
    "v_exp_f32 v27, v27\n"
    "v_exp_f32 v28, v28\n"
    "buffer_load_dwordx4 v1, s[16:19], s72 offen offset:3072 sc1 lds\n"
    "v_exp_f32 v29, v29\n"
    "v_exp_f32 v30, v30\n"
    "v_exp_f32 v31, v31\n"
    "v_exp_f32 v32, v32\n"
    "v_exp_f32 v33, v33\n"
    "v_exp_f32 v34, v34\n"
    "v_exp_f32 v35, v35\n"
    "v_mov_b32 v116, v4\n"
    "v_add_f32 v116, v116, v5\n"
    "v_add_f32 v116, v116, v6\n"
    "v_add_f32 v116, v116, v7\n"
    "v_mov_b32 v117, v8\n"
    "v_add_f32 v117, v117, v9\n"
    "s_mov_b32 m0, s73\n"
    "s_nop 0\n"
    "buffer_load_dwordx4 v1, s[16:19], s73 offen offset:0 sc1 lds\n"
    "v_add_f32 v117, v117, v10\n"
    "v_add_f32 v117, v117, v11\n"
    "v_mov_b32 v118, v12\n"
    "v_add_f32 v118, v118, v13\n"
    "v_add_f32 v118, v118, v14\n"
    "v_add_f32 v118, v118, v15\n"
    "v_mov_b32 v119, v16\n"
    "v_add_f32 v119, v119, v17\n"
    "v_add_f32 v119, v119, v18\n"
    "v_add_f32 v119, v119, v19\n"
    "v_add_f32 v116, v116, v20\n"
    "v_add_f32 v116, v116, v21\n"
    "v_add_f32 v116, v116, v22\n"
    "buffer_load_dwordx4 v1, s[16:19], s73 offen offset:1024 sc1 lds\n"
    "v_add_f32 v116, v116, v23\n"
    "v_add_f32 v117, v117, v24\n"
    "v_add_f32 v117, v117, v25\n"
    "v_add_f32 v117, v117, v26\n"
    "v_add_f32 v117, v117, v27\n"
    "v_add_f32 v118, v118, v28\n"
    "v_add_f32 v118, v118, v29\n"
    "v_add_f32 v118, v118, v30\n"
    "v_add_f32 v118, v118, v31\n"
    "v_add_f32 v119, v119, v32\n"
    "v_add_f32 v119, v119, v33\n"
    "v_add_f32 v119, v119, v34\n"
    "v_add_f32 v119, v119, v35\n"
    "s_mov_b32 s79, 256\n"
    "s_mov_b32 s71, 196608\n"
    "v_mov_b32 v249, 147840\n"
    "s_waitcnt vmcnt(13)\n"
    "ds_read_b128 v[232:235], v249 offset:0\n"
    "ds_read_b128 v[236:239], v249 offset:96\n"
    "v_cvt_f32_i32 v200, v200\n"
    "v_cvt_f32_i32 v201, v201\n"
    "v_cvt_f32_i32 v202, v202\n"
    "v_cvt_f32_i32 v203, v203\n"
    "v_cvt_f32_i32 v204, v204\n"
    "v_cvt_f32_i32 v205, v205\n"
    "v_cvt_f32_i32 v206, v206\n"
    "v_cvt_f32_i32 v207, v207\n"
    "s_waitcnt lgkmcnt(0)\n"
    "v_fma_f32 v200, v200, v232, v236\n"
    "v_fma_f32 v201, v201, v233, v237\n"
    "v_fma_f32 v202, v202, v234, v238\n"
    "v_fma_f32 v203, v203, v235, v239\n"
    "ds_read_b128 v[232:235], v249 offset:16\n"
    "ds_read_b128 v[236:239], v249 offset:112\n"
    "v_cvt_f32_i32 v208, v208\n"
    "v_cvt_f32_i32 v209, v209\n"
    "v_cvt_f32_i32 v210, v210\n"
    "v_cvt_f32_i32 v211, v211\n"
    "v_cvt_f32_i32 v212, v212\n"
    "v_cvt_f32_i32 v213, v213\n"
    "v_cvt_f32_i32 v214, v214\n"
    "v_cvt_f32_i32 v215, v215\n"
    "s_waitcnt lgkmcnt(0)\n"
    "v_fma_f32 v204, v204, v232, v236\n"
    "v_fma_f32 v205, v205, v233, v237\n"
    "v_fma_f32 v206, v206, v234, v238\n"
    "v_fma_f32 v207, v207, v235, v239\n"
    "ds_read_b128 v[232:235], v249 offset:32\n"
    "ds_read_b128 v[236:239], v249 offset:128\n"
    "v_cvt_pk_bf16_f32 v200, v200, v201\n"
    "v_cvt_pk_bf16_f32 v201, v202, v203\n"
    "v_cvt_pk_bf16_f32 v202, v204, v205\n"
    "v_cvt_pk_bf16_f32 v203, v206, v207\n"
    "s_waitcnt lgkmcnt(0)\n"
    "v_fma_f32 v208, v208, v232, v236\n"
    "v_fma_f32 v209, v209, v233, v237\n"
    "v_fma_f32 v210, v210, v234, v238\n"
    "v_fma_f32 v211, v211, v235, v239\n"
    "ds_read_b128 v[232:235], v249 offset:48\n"
    "ds_read_b128 v[236:239], v249 offset:144\n"
    "v_cvt_f32_i32 v216, v216\n"
    "v_cvt_f32_i32 v217, v217\n"
    "v_cvt_f32_i32 v218, v218\n"
    "v_cvt_f32_i32 v219, v219\n"
    "v_cvt_f32_i32 v220, v220\n"
    "v_cvt_f32_i32 v221, v221\n"
    "v_cvt_f32_i32 v222, v222\n"
    "v_cvt_f32_i32 v223, v223\n"
    "s_waitcnt lgkmcnt(0)\n"
    "v_fma_f32 v212, v212, v232, v236\n"
    "v_fma_f32 v213, v213, v233, v237\n"
    "v_fma_f32 v214, v214, v234, v238\n"
    "v_fma_f32 v215, v215, v235, v239\n"
    "buffer_load_dwordx2 v[240:241], v245, s[36:39], s81 offen offset:0 sc1\n"
    "s_add_u32 m0, s68, 24576\n"
    "s_add_u32 s83, s67, 24576\n"
    "buffer_load_dwordx4 v1, s[16:19], s83 offen offset:0 sc1 lds\n"
    "buffer_load_dwordx4 v1, s[16:19], s83 offen offset:1024 sc1 lds\n"
    "buffer_load_dwordx4 v1, s[16:19], s83 offen offset:2048 sc1 lds\n"
    "buffer_load_dwordx4 v1, s[16:19], s83 offen offset:3072 sc1 lds\n"
    "s_add_u32 m0, s68, 28672\n"
    "s_add_u32 s83, s67, 28672\n"
    "buffer_load_dwordx4 v1, s[16:19], s83 offen offset:0 sc1 lds\n"
    "buffer_load_dwordx4 v1, s[16:19], s83 offen offset:1024 sc1 lds\n"
    "s_add_u32 s67, s67, 49152\n"
    "s_waitcnt vmcnt(6)\n"
    "ds_add_u32 v252, v251\n"
    "ds_read_b128 v[152:155], v2 offset:12288\n"
    "ds_read_b128 v[156:159], v2 offset:14336\n"
    "ds_read_b128 v[160:163], v2 offset:16384\n"
    "ds_read_b128 v[164:167], v2 offset:18432\n"
    "ds_read_b128 v[168:171], v2 offset:20480\n"
    "ds_read_b128 v[172:175], v2 offset:22528\n"
    "s_waitcnt lgkmcnt(0)\n"
    "s_nop 7\n"
    "s_branch .LPK\n"
    ".Lp1_1:\n"
    "s_load_dwordx2 s[28:29], %1, 16\n"
    "s_load_dwordx2 s[40:41], %1, 40\n"
    "s_load_dwordx2 s[44:45], %1, 48\n"
    "s_mov_b32 s30, 1572864\n"
    "s_mov_b32 s31, 0x20000\n"
    "s_mov_b32 s42, 4096\n"
    "s_mov_b32 s43, 0x20000\n"
    "s_mov_b32 s46, 4096\n"
    "s_mov_b32 s47, 0x20000\n"
    "s_mov_b32 s38, 4224\n"
    "s_mov_b32 s39, 0x20000\n"
    "s_mov_b32 s22, 98304\n"
    "s_mov_b32 s23, 0x20000\n"
    "s_mul_i32 s72, s52, 1572864\n"
    "s_mul_i32 s73, s52, 4096\n"
    "s_mul_i32 s74, s52, 8192\n"
    "s_mul_i32 s75, s52, 786432\n"
    "s_mul_i32 s76, s53, 98304\n"
    "s_add_u32 s75, s75, s76\n"
    "s_mul_i32 s63, s53, 6144\n"
    "s_mul_i32 s77, s53, 16\n"
    "v_lshrrev_b32 v232, 2, v161\n"
    "v_and_b32 v233, 3, v161\n"
    "v_lshlrev_b32 v157, 8, v232\n"
    "v_lshlrev_b32 v234, 2, v233\n"
    "v_add3_u32 v157, v157, v234, s77\n"
    "v_add_u32 v157, 0, v157\n"
    "v_add_u32 v159, s77, v234\n"
    "v_mul_u32_u24 v246, 12, v161\n"
    "v_add_u32 v247, 3072, v246\n"
    "v_add_u32 v236, 0, v161\n"
    "v_mul_u32_u24 v236, 2731, v236\n"
    "v_lshrrev_b32 v236, 16, v236\n"
    "v_xor_b32 v236, v236, v161\n"
    "v_and_b32 v236, 7, v236\n"
    "v_and_b32 v237, 0x380, v1\n"
    "v_lshl_add_u32 v152, v236, 4, v237\n"
    "v_add_u32 v236, 64, v161\n"
    "v_mul_u32_u24 v236, 2731, v236\n"
    "v_lshrrev_b32 v236, 16, v236\n"
    "v_xor_b32 v236, v236, v161\n"
    "v_and_b32 v236, 7, v236\n"
    "v_and_b32 v237, 0x380, v1\n"
    "v_lshl_add_u32 v153, v236, 4, v237\n"
    "v_add_u32 v236, 128, v161\n"
    "v_mul_u32_u24 v236, 2731, v236\n"
    "v_lshrrev_b32 v236, 16, v236\n"
    "v_xor_b32 v236, v236, v161\n"
    "v_and_b32 v236, 7, v236\n"
    "v_and_b32 v237, 0x380, v1\n"
    "v_lshl_add_u32 v154, v236, 4, v237\n"
    "s_waitcnt lgkmcnt(0)\n"
    "s_mov_b64 s[16:17], s[36:37]\n"
    "s_add_u32 s40, s40, s73\n"
    "s_addc_u32 s41, s41, 0\n"
    "s_add_u32 s44, s44, s73\n"
    "s_addc_u32 s45, s45, 0\n"
    "s_add_u32 s36, s36, s74\n"
    "s_addc_u32 s37, s37, 0\n"
    "s_add_u32 s28, s28, s72\n"
    "s_addc_u32 s29, s29, 0\n"
    "s_add_u32 s20, s20, s75\n"
    "s_addc_u32 s21, s21, 0\n"
    "s_add_u32 s24, s24, s75\n"
    "s_addc_u32 s25, s25, 0\n"
    "buffer_load_dword v120, v157, s[40:43], 0 offen offset:0\n"
    "buffer_load_dword v128, v157, s[44:47], 0 offen offset:0\n"
    "v_mov_b32 v238, 0\n"
    "buffer_load_dwordx2 v[242:243], v238, s[36:39], 0 offen offset:0 sc1\n"
    "s_add_u32 s80, s63, 98304\n"
    "buffer_load_dwordx3 v[200:202], v246, s[28:31], s80 offen offset:0 nt\n"
    "buffer_load_dwordx3 v[204:206], v246, s[28:31], s80 offen offset:768 nt\n"
    "buffer_load_dwordx3 v[208:210], v246, s[28:31], s80 offen offset:1536 nt\n"
    "buffer_load_dwordx3 v[212:214], v246, s[28:31], s80 offen offset:2304 nt\n"
    "buffer_load_dwordx3 v[216:218], v247, s[28:31], s80 offen offset:0 nt\n"
    "buffer_load_dwordx3 v[220:222], v247, s[28:31], s80 offen offset:768 nt\n"
    "buffer_load_dwordx3 v[224:226], v247, s[28:31], s80 offen offset:1536 nt\n"
    "buffer_load_dwordx3 v[228:230], v247, s[28:31], s80 offen offset:2304 nt\n"
    "s_mov_b32 m0, 86016\n"
    "s_mov_b32 s72, 36864\n"
    "buffer_load_dwordx4 v152, s[20:23], s72 offen offset:0 nt lds\n"
    "buffer_load_dwordx4 v153, s[20:23], s72 offen offset:1024 nt lds\n"
    "buffer_load_dwordx4 v154, s[20:23], s72 offen offset:2048 nt lds\n"
    "s_mov_b32 m0, 89088\n"
    "s_mov_b32 s73, 39936\n"
    "buffer_load_dwordx4 v152, s[20:23], s73 offen offset:0 nt lds\n"
    "buffer_load_dwordx4 v153, s[20:23], s73 offen offset:1024 nt lds\n"
    "buffer_load_dwordx4 v154, s[20:23], s73 offen offset:2048 nt lds\n"
    "s_mov_b32 m0, 92160\n"
    "s_mov_b32 s72, 43008\n"
    "buffer_load_dwordx4 v152, s[20:23], s72 offen offset:0 nt lds\n"
    "buffer_load_dwordx4 v153, s[20:23], s72 offen offset:1024 nt lds\n"
    "buffer_load_dwordx4 v154, s[20:23], s72 offen offset:2048 nt lds\n"
    "s_mov_b32 m0, 95232\n"
    "s_mov_b32 s73, 46080\n"
    "buffer_load_dwordx4 v152, s[20:23], s73 offen offset:0 nt lds\n"
    "buffer_load_dwordx4 v153, s[20:23], s73 offen offset:1024 nt lds\n"
    "buffer_load_dwordx4 v154, s[20:23], s73 offen offset:2048 nt lds\n"
    "s_mov_b32 m0, 98304\n"
    "s_mov_b32 s72, 49152\n"
    "buffer_load_dwordx4 v152, s[20:23], s72 offen offset:0 nt lds\n"
    "buffer_load_dwordx4 v153, s[20:23], s72 offen offset:1024 nt lds\n"
    "buffer_load_dwordx4 v154, s[20:23], s72 offen offset:2048 nt lds\n"
    "s_mov_b32 m0, 101376\n"
    "s_mov_b32 s73, 52224\n"
    "buffer_load_dwordx4 v152, s[20:23], s73 offen offset:0 nt lds\n"
    "buffer_load_dwordx4 v153, s[20:23], s73 offen offset:1024 nt lds\n"
    "buffer_load_dwordx4 v154, s[20:23], s73 offen offset:2048 nt lds\n"
    "s_mov_b32 m0, 104448\n"
    "s_mov_b32 s72, 55296\n"
    "buffer_load_dwordx4 v152, s[20:23], s72 offen offset:0 nt lds\n"
    "buffer_load_dwordx4 v153, s[20:23], s72 offen offset:1024 nt lds\n"
    "buffer_load_dwordx4 v154, s[20:23], s72 offen offset:2048 nt lds\n"
    "s_mov_b32 m0, 107520\n"
    "s_mov_b32 s73, 58368\n"
    "buffer_load_dwordx4 v152, s[20:23], s73 offen offset:0 nt lds\n"
    "buffer_load_dwordx4 v153, s[20:23], s73 offen offset:1024 nt lds\n"
    "buffer_load_dwordx4 v154, s[20:23], s73 offen offset:2048 nt lds\n"
    "s_mov_b32 m0, 110592\n"
    "s_mov_b32 s72, 61440\n"
    "buffer_load_dwordx4 v152, s[20:23], s72 offen offset:0 nt lds\n"
    "buffer_load_dwordx4 v153, s[20:23], s72 offen offset:1024 nt lds\n"
    "buffer_load_dwordx4 v154, s[20:23], s72 offen offset:2048 nt lds\n"
    "s_mov_b32 m0, 113664\n"
    "s_mov_b32 s73, 64512\n"
    "buffer_load_dwordx4 v152, s[20:23], s73 offen offset:0 nt lds\n"
    "buffer_load_dwordx4 v153, s[20:23], s73 offen offset:1024 nt lds\n"
    "buffer_load_dwordx4 v154, s[20:23], s73 offen offset:2048 nt lds\n"
    "s_mov_b32 m0, 116736\n"
    "s_mov_b32 s72, 67584\n"
    "buffer_load_dwordx4 v152, s[20:23], s72 offen offset:0 nt lds\n"
    "buffer_load_dwordx4 v153, s[20:23], s72 offen offset:1024 nt lds\n"
    "buffer_load_dwordx4 v154, s[20:23], s72 offen offset:2048 nt lds\n"
    "s_mov_b32 m0, 119808\n"
    "s_mov_b32 s73, 70656\n"
    "buffer_load_dwordx4 v152, s[20:23], s73 offen offset:0 nt lds\n"
    "buffer_load_dwordx4 v153, s[20:23], s73 offen offset:1024 nt lds\n"
    "buffer_load_dwordx4 v154, s[20:23], s73 offen offset:2048 nt lds\n"
    "s_mov_b32 m0, 122880\n"
    "s_mov_b32 s72, 73728\n"
    "buffer_load_dwordx4 v152, s[20:23], s72 offen offset:0 nt lds\n"
    "buffer_load_dwordx4 v153, s[20:23], s72 offen offset:1024 nt lds\n"
    "buffer_load_dwordx4 v154, s[20:23], s72 offen offset:2048 nt lds\n"
    "s_mov_b32 m0, 125952\n"
    "s_mov_b32 s73, 76800\n"
    "buffer_load_dwordx4 v152, s[20:23], s73 offen offset:0 nt lds\n"
    "buffer_load_dwordx4 v153, s[20:23], s73 offen offset:1024 nt lds\n"
    "buffer_load_dwordx4 v154, s[20:23], s73 offen offset:2048 nt lds\n"
    "s_mov_b32 m0, 129024\n"
    "s_mov_b32 s72, 79872\n"
    "buffer_load_dwordx4 v152, s[20:23], s72 offen offset:0 nt lds\n"
    "buffer_load_dwordx4 v153, s[20:23], s72 offen offset:1024 nt lds\n"
    "buffer_load_dwordx4 v154, s[20:23], s72 offen offset:2048 nt lds\n"
    "s_mov_b32 m0, 132096\n"
    "s_mov_b32 s73, 82944\n"
    "buffer_load_dwordx4 v152, s[20:23], s73 offen offset:0 nt lds\n"
    "buffer_load_dwordx4 v153, s[20:23], s73 offen offset:1024 nt lds\n"
    "buffer_load_dwordx4 v154, s[20:23], s73 offen offset:2048 nt lds\n"
    "s_mov_b32 m0, 135168\n"
    "s_mov_b32 s72, 86016\n"
    "buffer_load_dwordx4 v152, s[20:23], s72 offen offset:0 nt lds\n"
    "buffer_load_dwordx4 v153, s[20:23], s72 offen offset:1024 nt lds\n"
    "buffer_load_dwordx4 v154, s[20:23], s72 offen offset:2048 nt lds\n"
    "s_mov_b32 m0, 138240\n"
    "s_mov_b32 s73, 89088\n"
    "buffer_load_dwordx4 v152, s[20:23], s73 offen offset:0 nt lds\n"
    "buffer_load_dwordx4 v153, s[20:23], s73 offen offset:1024 nt lds\n"
    "buffer_load_dwordx4 v154, s[20:23], s73 offen offset:2048 nt lds\n"
    "s_mov_b32 m0, 141312\n"
    "s_mov_b32 s72, 92160\n"
    "buffer_load_dwordx4 v152, s[20:23], s72 offen offset:0 nt lds\n"
    "buffer_load_dwordx4 v153, s[20:23], s72 offen offset:1024 nt lds\n"
    "buffer_load_dwordx4 v154, s[20:23], s72 offen offset:2048 nt lds\n"
    "s_mov_b32 m0, 144384\n"
    "s_mov_b32 s73, 95232\n"
    "buffer_load_dwordx4 v152, s[20:23], s73 offen offset:0 nt lds\n"
    "buffer_load_dwordx4 v153, s[20:23], s73 offen offset:1024 nt lds\n"
    "buffer_load_dwordx4 v154, s[20:23], s73 offen offset:2048 nt lds\n"
    "s_mul_i32 s72, s52, 1576960\n"
    "s_add_u32 s72, s72, 262144\n"
    "s_add_u32 s16, s16, s72\n"
    "s_addc_u32 s17, s17, 0\n"
    "s_mov_b32 s18, 1572864\n"
    "s_mov_b32 s19, 0x20000\n"
    "s_mov_b32 s26, 98304\n"
    "s_mov_b32 s27, 0x20000\n"
    "s_mov_b32 s65, 6144\n"
    "s_mov_b32 s66, 0\n"
    "s_mov_b32 s67, 55296\n"
    "s_mov_b32 s68, 55296\n"
    "s_mov_b32 s69, 153600\n"
    "s_mov_b32 s58, 0\n"
    "s_mov_b32 s59, 4\n"
    "s_lshr_b32 s73, s53, 2\n"
    "s_mul_i32 s73, s73, 12288\n"
    "s_and_b32 s74, s53, 3\n"
    "s_lshl_b32 s74, s74, 8\n"
    "s_add_u32 s73, s73, s74\n"
    "s_add_u32 s60, s73, 24576\n"
    "s_add_u32 s61, s73, 28672\n"
    "s_add_u32 s62, s73, 32768\n"
    "s_mov_b32 s82, 0x7fff0000\n"
    "v_lshrrev_b32 v232, 2, v161\n"
    "v_and_b32 v233, 3, v161\n"
    "v_lshlrev_b32 v158, 6, v232\n"
    "v_lshlrev_b32 v234, 3, v233\n"
    "v_add_u32 v158, v158, v234\n"
    "v_add_u32 v158, 155328, v158\n"
    "v_add_u32 v160, 156352, v234\n"
    "v_and_b32 v234, 48, v161\n"
    "v_lshlrev_b32 v248, 6, v234\n"
    "v_and_b32 v234, 0xf0, v1\n"
    "v_add_u32 v248, v248, v234\n"
    "v_and_b32 v232, 15, v161\n"
    "v_lshrrev_b32 v233, 4, v161\n"
    "v_and_b32 v234, 7, v161\n"
    "v_xor_b32 v233, v233, v234\n"
    "v_mul_u32_u24 v155, 384, v232\n"
    "v_lshl_add_u32 v155, v233, 4, v155\n"
    "v_add_u32 v155, 73728, v155\n"
    "v_xor_b32 v156, 64, v155\n"
    "v_mov_b32 v2, v1\n"
    "v_add_u32 v3, 49152, v1\n"
    "v_and_b32 v245, 0xf0, v1\n"
    "v_lshrrev_b32 v245, 1, v245\n"
    "v_add_u32 v245, 128, v245\n"
    "s_lshl_b32 s73, s53, 4\n"
    "s_add_u32 s73, s73, 136\n"
    "v_mov_b32 v244, 0x7fff0000\n"
    "v_mov_b32 v239, s73\n"
    "v_cmp_eq_u32 vcc, 0, v1\n"
    "s_nop 1\n"
    "v_cndmask_b32 v244, v244, v239, vcc\n"
    "v_lshlrev_b32 v252, 2, v161\n"
    "v_add_u32 v252, 157952, v252\n"
    "v_mov_b32 v251, 1\n"
    "v_accvgpr_write_b32 a0, 0\n"
    "v_accvgpr_write_b32 a1, 0\n"
    "v_accvgpr_write_b32 a2, 0\n"
    "v_accvgpr_write_b32 a3, 0\n"
    "v_accvgpr_write_b32 a4, 0\n"
    "v_accvgpr_write_b32 a5, 0\n"
    "v_accvgpr_write_b32 a6, 0\n"
    "v_accvgpr_write_b32 a7, 0\n"
    "v_accvgpr_write_b32 a8, 0\n"
    "v_accvgpr_write_b32 a9, 0\n"
    "v_accvgpr_write_b32 a10, 0\n"
    "v_accvgpr_write_b32 a11, 0\n"
    "v_accvgpr_write_b32 a12, 0\n"
    "v_accvgpr_write_b32 a13, 0\n"
    "v_accvgpr_write_b32 a14, 0\n"
    "v_accvgpr_write_b32 a15, 0\n"
    "v_accvgpr_write_b32 a16, 0\n"
    "v_accvgpr_write_b32 a17, 0\n"
    "v_accvgpr_write_b32 a18, 0\n"
    "v_accvgpr_write_b32 a19, 0\n"
    "v_accvgpr_write_b32 a20, 0\n"
    "v_accvgpr_write_b32 a21, 0\n"
    "v_accvgpr_write_b32 a22, 0\n"
    "v_accvgpr_write_b32 a23, 0\n"
    "v_accvgpr_write_b32 a24, 0\n"
    "v_accvgpr_write_b32 a25, 0\n"
    "v_accvgpr_write_b32 a26, 0\n"
    "v_accvgpr_write_b32 a27, 0\n"
    "v_accvgpr_write_b32 a28, 0\n"
    "v_accvgpr_write_b32 a29, 0\n"
    "v_accvgpr_write_b32 a30, 0\n"
    "v_accvgpr_write_b32 a31, 0\n"
    "v_accvgpr_write_b32 a32, 0\n"
    "v_accvgpr_write_b32 a33, 0\n"
    "v_accvgpr_write_b32 a34, 0\n"
    "v_accvgpr_write_b32 a35, 0\n"
    "v_accvgpr_write_b32 a36, 0\n"
    "v_accvgpr_write_b32 a37, 0\n"
    "v_accvgpr_write_b32 a38, 0\n"
    "v_accvgpr_write_b32 a39, 0\n"
    "v_accvgpr_write_b32 a40, 0\n"
    "v_accvgpr_write_b32 a41, 0\n"
    "v_accvgpr_write_b32 a42, 0\n"
    "v_accvgpr_write_b32 a43, 0\n"
    "v_accvgpr_write_b32 a44, 0\n"
    "v_accvgpr_write_b32 a45, 0\n"
    "v_accvgpr_write_b32 a46, 0\n"
    "v_accvgpr_write_b32 a47, 0\n"
    "v_accvgpr_write_b32 a48, 0\n"
    "v_accvgpr_write_b32 a49, 0\n"
    "v_accvgpr_write_b32 a50, 0\n"
    "v_accvgpr_write_b32 a51, 0\n"
    "v_accvgpr_write_b32 a52, 0\n"
    "v_accvgpr_write_b32 a53, 0\n"
    "v_accvgpr_write_b32 a54, 0\n"
    "v_accvgpr_write_b32 a55, 0\n"
    "v_accvgpr_write_b32 a56, 0\n"
    "v_accvgpr_write_b32 a57, 0\n"
    "v_accvgpr_write_b32 a58, 0\n"
    "v_accvgpr_write_b32 a59, 0\n"
    "v_accvgpr_write_b32 a60, 0\n"
    "v_accvgpr_write_b32 a61, 0\n"
    "v_accvgpr_write_b32 a62, 0\n"
    "v_accvgpr_write_b32 a63, 0\n"
    "v_accvgpr_write_b32 a64, 0\n"
    "v_accvgpr_write_b32 a65, 0\n"
    "v_accvgpr_write_b32 a66, 0\n"
    "v_accvgpr_write_b32 a67, 0\n"
    "v_accvgpr_write_b32 a68, 0\n"
    "v_accvgpr_write_b32 a69, 0\n"
    "v_accvgpr_write_b32 a70, 0\n"
    "v_accvgpr_write_b32 a71, 0\n"
    "v_accvgpr_write_b32 a72, 0\n"
    "v_accvgpr_write_b32 a73, 0\n"
    "v_accvgpr_write_b32 a74, 0\n"
    "v_accvgpr_write_b32 a75, 0\n"
    "v_accvgpr_write_b32 a76, 0\n"
    "v_accvgpr_write_b32 a77, 0\n"
    "v_accvgpr_write_b32 a78, 0\n"
    "v_accvgpr_write_b32 a79, 0\n"
    "v_accvgpr_write_b32 a80, 0\n"
    "v_accvgpr_write_b32 a81, 0\n"
    "v_accvgpr_write_b32 a82, 0\n"
    "v_accvgpr_write_b32 a83, 0\n"
    "v_accvgpr_write_b32 a84, 0\n"
    "v_accvgpr_write_b32 a85, 0\n"
    "v_accvgpr_write_b32 a86, 0\n"
    "v_accvgpr_write_b32 a87, 0\n"
    "v_accvgpr_write_b32 a88, 0\n"
    "v_accvgpr_write_b32 a89, 0\n"
    "v_accvgpr_write_b32 a90, 0\n"
    "v_accvgpr_write_b32 a91, 0\n"
    "v_accvgpr_write_b32 a92, 0\n"
    "v_accvgpr_write_b32 a93, 0\n"
    "v_accvgpr_write_b32 a94, 0\n"
    "v_accvgpr_write_b32 a95, 0\n"
    "v_accvgpr_write_b32 a96, 0\n"
    "v_accvgpr_write_b32 a97, 0\n"
    "v_accvgpr_write_b32 a98, 0\n"
    "v_accvgpr_write_b32 a99, 0\n"
    "v_accvgpr_write_b32 a100, 0\n"
    "v_accvgpr_write_b32 a101, 0\n"
    "v_accvgpr_write_b32 a102, 0\n"
    "v_accvgpr_write_b32 a103, 0\n"
    "v_accvgpr_write_b32 a104, 0\n"
    "v_accvgpr_write_b32 a105, 0\n"
    "v_accvgpr_write_b32 a106, 0\n"
    "v_accvgpr_write_b32 a107, 0\n"
    "v_accvgpr_write_b32 a108, 0\n"
    "v_accvgpr_write_b32 a109, 0\n"
    "v_accvgpr_write_b32 a110, 0\n"
    "v_accvgpr_write_b32 a111, 0\n"
    "v_accvgpr_write_b32 a112, 0\n"
    "v_accvgpr_write_b32 a113, 0\n"
    "v_accvgpr_write_b32 a114, 0\n"
    "v_accvgpr_write_b32 a115, 0\n"
    "v_accvgpr_write_b32 a116, 0\n"
    "v_accvgpr_write_b32 a117, 0\n"
    "v_accvgpr_write_b32 a118, 0\n"
    "v_accvgpr_write_b32 a119, 0\n"
    "v_accvgpr_write_b32 a120, 0\n"
    "v_accvgpr_write_b32 a121, 0\n"
    "v_accvgpr_write_b32 a122, 0\n"
    "v_accvgpr_write_b32 a123, 0\n"
    "v_accvgpr_write_b32 a124, 0\n"
    "v_accvgpr_write_b32 a125, 0\n"
    "v_accvgpr_write_b32 a126, 0\n"
    "v_accvgpr_write_b32 a127, 0\n"
    "v_accvgpr_write_b32 a128, 0\n"
    "v_accvgpr_write_b32 a129, 0\n"
    "v_accvgpr_write_b32 a130, 0\n"
    "v_accvgpr_write_b32 a131, 0\n"
    "v_accvgpr_write_b32 a132, 0\n"
    "v_accvgpr_write_b32 a133, 0\n"
    "v_accvgpr_write_b32 a134, 0\n"
    "v_accvgpr_write_b32 a135, 0\n"
    "v_accvgpr_write_b32 a136, 0\n"
    "v_accvgpr_write_b32 a137, 0\n"
    "v_accvgpr_write_b32 a138, 0\n"
    "v_accvgpr_write_b32 a139, 0\n"
    "v_accvgpr_write_b32 a140, 0\n"
    "v_accvgpr_write_b32 a141, 0\n"
    "v_accvgpr_write_b32 a142, 0\n"
    "v_accvgpr_write_b32 a143, 0\n"
    "v_accvgpr_write_b32 a144, 0\n"
    "v_accvgpr_write_b32 a145, 0\n"
    "v_accvgpr_write_b32 a146, 0\n"
    "v_accvgpr_write_b32 a147, 0\n"
    "v_accvgpr_write_b32 a148, 0\n"
    "v_accvgpr_write_b32 a149, 0\n"
    "v_accvgpr_write_b32 a150, 0\n"
    "v_accvgpr_write_b32 a151, 0\n"
    "v_accvgpr_write_b32 a152, 0\n"
    "v_accvgpr_write_b32 a153, 0\n"
    "v_accvgpr_write_b32 a154, 0\n"
    "v_accvgpr_write_b32 a155, 0\n"
    "v_accvgpr_write_b32 a156, 0\n"
    "v_accvgpr_write_b32 a157, 0\n"
    "v_accvgpr_write_b32 a158, 0\n"
    "v_accvgpr_write_b32 a159, 0\n"
    "v_accvgpr_write_b32 a160, 0\n"
    "v_accvgpr_write_b32 a161, 0\n"
    "v_accvgpr_write_b32 a162, 0\n"
    "v_accvgpr_write_b32 a163, 0\n"
    "v_accvgpr_write_b32 a164, 0\n"
    "v_accvgpr_write_b32 a165, 0\n"
    "v_accvgpr_write_b32 a166, 0\n"
    "v_accvgpr_write_b32 a167, 0\n"
    "v_accvgpr_write_b32 a168, 0\n"
    "v_accvgpr_write_b32 a169, 0\n"
    "v_accvgpr_write_b32 a170, 0\n"
    "v_accvgpr_write_b32 a171, 0\n"
    "v_accvgpr_write_b32 a172, 0\n"
    "v_accvgpr_write_b32 a173, 0\n"
    "v_accvgpr_write_b32 a174, 0\n"
    "v_accvgpr_write_b32 a175, 0\n"
    "v_accvgpr_write_b32 a176, 0\n"
    "v_accvgpr_write_b32 a177, 0\n"
    "v_accvgpr_write_b32 a178, 0\n"
    "v_accvgpr_write_b32 a179, 0\n"
    "v_accvgpr_write_b32 a180, 0\n"
    "v_accvgpr_write_b32 a181, 0\n"
    "v_accvgpr_write_b32 a182, 0\n"
    "v_accvgpr_write_b32 a183, 0\n"
    "v_accvgpr_write_b32 a184, 0\n"
    "v_accvgpr_write_b32 a185, 0\n"
    "v_accvgpr_write_b32 a186, 0\n"
    "v_accvgpr_write_b32 a187, 0\n"
    "v_accvgpr_write_b32 a188, 0\n"
    "v_accvgpr_write_b32 a189, 0\n"
    "v_accvgpr_write_b32 a190, 0\n"
    "v_accvgpr_write_b32 a191, 0\n"
    "s_waitcnt vmcnt(63)\n"
    "v_add_co_u32 v242, vcc, 0x7f4a7c15, v242\n"
    "v_mov_b32 v238, 0x9e3779b9\n"
    "s_nop 0\n"
    "v_addc_co_u32 v243, vcc, v238, v243, vcc\n"
    "s_mov_b32 s73, 0x3dd53b95\n"
    "v_lshlrev_b32 v136, 16, v120\n"
    "v_lshlrev_b32 v144, 16, v128\n"
    "v_and_b32 v137, 0xffff0000, v120\n"
    "v_and_b32 v145, 0xffff0000, v128\n"
    "v_mul_f32 v144, -v144, v136\n"
    "v_mul_f32 v145, -v145, v137\n"
    "ds_write_b64 v158, v[136:137] offset:0\n"
    "ds_write_b64 v158, v[144:145] offset:32\n"
    "s_waitcnt vmcnt(60)\n"
    "s_mov_b32 s71, 98304\n"
    "v_mov_b32 v249, 155392\n"
    "s_add_u32 s76, s71, s60\n"
    "s_add_u32 s77, s71, s61\n"
    "s_add_u32 s78, s71, s62\n"
    "ds_read_b128 v[168:171], v249 offset:0\n"
    "ds_read_b128 v[172:175], v249 offset:32\n"
    "ds_read_b128 v[176:179], v249 offset:16\n"
    "ds_read_b128 v[180:183], v249 offset:48\n"
    "v_cvt_f32_i32 v200, v200\n"
    "v_cvt_f32_i32 v201, v201\n"
    "v_cvt_f32_i32 v202, v202\n"
    "v_cvt_f32_i32 v204, v204\n"
    "v_cvt_f32_i32 v205, v205\n"
    "v_cvt_f32_i32 v206, v206\n"
    "v_cvt_f32_i32 v208, v208\n"
    "v_cvt_f32_i32 v209, v209\n"
    "v_cvt_f32_i32 v210, v210\n"
    "v_cvt_f32_i32 v212, v212\n"
    "v_cvt_f32_i32 v213, v213\n"
    "v_cvt_f32_i32 v214, v214\n"
    "s_waitcnt lgkmcnt(2)\n"
    "v_fma_f32 v200, v200, v168, v172\n"
    "v_fma_f32 v201, v201, v168, v172\n"
    "v_fma_f32 v202, v202, v168, v172\n"
    "v_fma_f32 v204, v204, v169, v173\n"
    "v_fma_f32 v205, v205, v169, v173\n"
    "v_fma_f32 v206, v206, v169, v173\n"
    "v_fma_f32 v208, v208, v170, v174\n"
    "v_fma_f32 v209, v209, v170, v174\n"
    "v_fma_f32 v210, v210, v170, v174\n"
    "v_fma_f32 v212, v212, v171, v175\n"
    "v_fma_f32 v213, v213, v171, v175\n"
    "v_fma_f32 v214, v214, v171, v175\n"
    "v_cvt_f32_i32 v216, v216\n"
    "v_cvt_f32_i32 v217, v217\n"
    "v_cvt_f32_i32 v218, v218\n"
    "v_cvt_f32_i32 v220, v220\n"
    "v_cvt_f32_i32 v221, v221\n"
    "v_cvt_f32_i32 v222, v222\n"
    "v_cvt_f32_i32 v224, v224\n"
    "v_cvt_f32_i32 v225, v225\n"
    "v_cvt_f32_i32 v226, v226\n"
    "v_cvt_f32_i32 v228, v228\n"
    "v_cvt_f32_i32 v229, v229\n"
    "v_cvt_f32_i32 v230, v230\n"
    "s_waitcnt lgkmcnt(0)\n"
    "v_fma_f32 v216, v216, v176, v180\n"
    "v_fma_f32 v217, v217, v176, v180\n"
    "v_fma_f32 v218, v218, v176, v180\n"
    "v_fma_f32 v220, v220, v177, v181\n"
    "v_fma_f32 v221, v221, v177, v181\n"
    "v_fma_f32 v222, v222, v177, v181\n"
    "v_fma_f32 v224, v224, v178, v182\n"
    "v_fma_f32 v225, v225, v178, v182\n"
    "v_fma_f32 v226, v226, v178, v182\n"
    "v_fma_f32 v228, v228, v179, v183\n"
    "v_fma_f32 v229, v229, v179, v183\n"
    "v_fma_f32 v230, v230, v179, v183\n"
    "v_cvt_pk_bf16_f32 v200, v200, v204\n"
    "v_cvt_pk_bf16_f32 v204, v201, v205\n"
    "v_cvt_pk_bf16_f32 v168, v202, v206\n"
    "v_cvt_pk_bf16_f32 v201, v208, v212\n"
    "v_cvt_pk_bf16_f32 v205, v209, v213\n"
    "v_cvt_pk_bf16_f32 v169, v210, v214\n"
    "v_cvt_pk_bf16_f32 v202, v216, v220\n"
    "v_cvt_pk_bf16_f32 v206, v217, v221\n"
    "v_cvt_pk_bf16_f32 v170, v218, v222\n"
    "v_cvt_pk_bf16_f32 v203, v224, v228\n"
    "v_cvt_pk_bf16_f32 v207, v225, v229\n"
    "v_cvt_pk_bf16_f32 v171, v226, v230\n"
    "buffer_store_dwordx4 v[200:203], v248, s[16:19], s76 offen offset:0 sc1\n"
    "buffer_store_dwordx4 v[204:207], v248, s[16:19], s77 offen offset:0 sc1\n"
    "buffer_store_dwordx4 v[168:171], v248, s[16:19], s78 offen offset:0 sc1\n"
    "s_waitcnt vmcnt(3)\n"
    "s_waitcnt lgkmcnt(0)\n"
    "s_barrier\n"
    "ds_read_b128 a[192:195], v155 offset:0\n"
    "ds_read_b128 a[196:199], v156 offset:0\n"
    "ds_read_b128 a[200:203], v155 offset:128\n"
    "ds_read_b128 a[204:207], v156 offset:128\n"
    "ds_read_b128 a[208:211], v155 offset:256\n"
    "ds_read_b128 a[212:215], v156 offset:256\n"
    "ds_read_b128 a[216:219], v155 offset:6144\n"
    "ds_read_b128 a[220:223], v156 offset:6144\n"
    "ds_read_b128 a[224:227], v155 offset:6272\n"
    "ds_read_b128 a[228:231], v156 offset:6272\n"
    "ds_read_b128 a[232:235], v155 offset:6400\n"
    "ds_read_b128 a[236:239], v156 offset:6400\n"
    "ds_read_b128 a[240:243], v155 offset:12288\n"
    "ds_read_b128 a[244:247], v156 offset:12288\n"
    "ds_read_b128 a[248:251], v155 offset:12416\n"
    "ds_read_b128 a[252:255], v156 offset:12416\n"
    "ds_read_b128 v[120:123], v155 offset:12544\n"
    "ds_read_b128 v[124:127], v156 offset:12544\n"
    "ds_read_b128 v[128:131], v155 offset:18432\n"
    "ds_read_b128 v[132:135], v156 offset:18432\n"
    "ds_read_b128 v[136:139], v155 offset:18560\n"
    "ds_read_b128 v[140:143], v156 offset:18560\n"
    "ds_read_b128 v[144:147], v155 offset:18688\n"
    "ds_read_b128 v[148:151], v156 offset:18688\n"
    "s_waitcnt lgkmcnt(0)\n"
    "s_barrier\n"
    "v_and_b32 v68, 0x1f0, v1\n"
    "v_lshrrev_b32 v68, 1, v68\n"
    "v_add_u32 v68, 128, v68\n"
    "s_mov_b32 s81, 0\n"
    "buffer_load_dwordx2 v[240:241], v68, s[36:39], s81 offen offset:0 sc1\n"
    "s_add_u32 s80, s63, 196608\n"
    "buffer_load_dwordx3 v[200:202], v246, s[28:31], s80 offen offset:0 nt\n"
    "buffer_load_dwordx3 v[204:206], v246, s[28:31], s80 offen offset:768 nt\n"
    "buffer_load_dwordx3 v[208:210], v246, s[28:31], s80 offen offset:1536 nt\n"
    "buffer_load_dwordx3 v[212:214], v246, s[28:31], s80 offen offset:2304 nt\n"
    "buffer_load_dwordx3 v[216:218], v247, s[28:31], s80 offen offset:0 nt\n"
    "buffer_load_dwordx3 v[220:222], v247, s[28:31], s80 offen offset:768 nt\n"
    "buffer_load_dwordx3 v[224:226], v247, s[28:31], s80 offen offset:1536 nt\n"
    "buffer_load_dwordx3 v[228:230], v247, s[28:31], s80 offen offset:2304 nt\n"
    "ds_read_b128 v[152:155], v2 offset:0\n"
    "ds_read_b128 v[156:159], v2 offset:2048\n"
    "ds_read_b128 v[160:163], v2 offset:4096\n"
    "ds_read_b128 v[164:167], v2 offset:6144\n"
    "ds_read_b128 v[168:171], v2 offset:8192\n"
    "ds_read_b128 v[172:175], v2 offset:10240\n"
    "ds_read_b128 v[176:179], v2 offset:1024\n"
    "ds_read_b128 v[180:183], v2 offset:3072\n"
    "ds_read_b128 v[184:187], v2 offset:5120\n"
    "ds_read_b128 v[188:191], v2 offset:7168\n"
    "ds_read_b128 v[192:195], v2 offset:9216\n"
    "ds_read_b128 v[196:199], v2 offset:11264\n"
    "s_waitcnt lgkmcnt(11)\n"
    "v_mfma_f32_16x16x32_bf16 v[4:7], v[152:155], a[192:195], 0\n"
    "s_waitcnt lgkmcnt(10)\n"
    "v_mfma_f32_16x16x32_bf16 v[4:7], v[156:159], a[196:199], v[4:7]\n"
    "s_waitcnt lgkmcnt(9)\n"
    "v_mfma_f32_16x16x32_bf16 v[4:7], v[160:163], a[200:203], v[4:7]\n"
    "s_waitcnt lgkmcnt(8)\n"
    "v_mfma_f32_16x16x32_bf16 v[4:7], v[164:167], a[204:207], v[4:7]\n"
    "s_waitcnt lgkmcnt(7)\n"
    "v_mfma_f32_16x16x32_bf16 v[4:7], v[168:171], a[208:211], v[4:7]\n"
    "s_waitcnt lgkmcnt(6)\n"
    "v_mfma_f32_16x16x32_bf16 v[4:7], v[172:175], a[212:215], v[4:7]\n"
    "v_mfma_f32_16x16x32_bf16 v[8:11], v[172:175], a[236:239], 0\n"
    "v_mfma_f32_16x16x32_bf16 v[8:11], v[168:171], a[232:235], v[8:11]\n"
    "v_mfma_f32_16x16x32_bf16 v[8:11], v[164:167], a[228:231], v[8:11]\n"
    "v_mfma_f32_16x16x32_bf16 v[8:11], v[160:163], a[224:227], v[8:11]\n"
    "v_mfma_f32_16x16x32_bf16 v[8:11], v[156:159], a[220:223], v[8:11]\n"
    "v_mfma_f32_16x16x32_bf16 v[8:11], v[152:155], a[216:219], v[8:11]\n"
    "v_mfma_f32_16x16x32_bf16 v[12:15], v[152:155], a[240:243], 0\n"
    "v_mfma_f32_16x16x32_bf16 v[12:15], v[156:159], a[244:247], v[12:15]\n"
    "v_mfma_f32_16x16x32_bf16 v[12:15], v[160:163], a[248:251], v[12:15]\n"
    "v_mfma_f32_16x16x32_bf16 v[12:15], v[164:167], a[252:255], v[12:15]\n"
    "v_mfma_f32_16x16x32_bf16 v[12:15], v[168:171], v[120:123], v[12:15]\n"
    "v_mfma_f32_16x16x32_bf16 v[12:15], v[172:175], v[124:127], v[12:15]\n"
    "v_mfma_f32_16x16x32_bf16 v[16:19], v[172:175], v[148:151], 0\n"
    "v_mfma_f32_16x16x32_bf16 v[16:19], v[168:171], v[144:147], v[16:19]\n"
    "v_mfma_f32_16x16x32_bf16 v[16:19], v[164:167], v[140:143], v[16:19]\n"
    "v_mfma_f32_16x16x32_bf16 v[16:19], v[160:163], v[136:139], v[16:19]\n"
    "v_mfma_f32_16x16x32_bf16 v[16:19], v[156:159], v[132:135], v[16:19]\n"
    "v_mfma_f32_16x16x32_bf16 v[16:19], v[152:155], v[128:131], v[16:19]\n"
    "s_waitcnt lgkmcnt(5)\n"
    "v_mfma_f32_16x16x32_bf16 v[20:23], v[176:179], a[192:195], 0\n"
    "s_waitcnt lgkmcnt(4)\n"
    "v_mfma_f32_16x16x32_bf16 v[20:23], v[180:183], a[196:199], v[20:23]\n"
    "s_waitcnt lgkmcnt(3)\n"
    "v_mfma_f32_16x16x32_bf16 v[20:23], v[184:187], a[200:203], v[20:23]\n"
    "s_waitcnt lgkmcnt(2)\n"
    "v_mfma_f32_16x16x32_bf16 v[20:23], v[188:191], a[204:207], v[20:23]\n"
    "s_waitcnt lgkmcnt(1)\n"
    "v_mfma_f32_16x16x32_bf16 v[20:23], v[192:195], a[208:211], v[20:23]\n"
    "s_waitcnt lgkmcnt(0)\n"
    "v_mfma_f32_16x16x32_bf16 v[20:23], v[196:199], a[212:215], v[20:23]\n"
    "v_mfma_f32_16x16x32_bf16 v[24:27], v[196:199], a[236:239], 0\n"
    "v_mfma_f32_16x16x32_bf16 v[24:27], v[192:195], a[232:235], v[24:27]\n"
    "v_mfma_f32_16x16x32_bf16 v[24:27], v[188:191], a[228:231], v[24:27]\n"
    "v_mfma_f32_16x16x32_bf16 v[24:27], v[184:187], a[224:227], v[24:27]\n"
    "v_mfma_f32_16x16x32_bf16 v[24:27], v[180:183], a[220:223], v[24:27]\n"
    "v_mfma_f32_16x16x32_bf16 v[24:27], v[176:179], a[216:219], v[24:27]\n"
    "v_mfma_f32_16x16x32_bf16 v[28:31], v[176:179], a[240:243], 0\n"
    "v_mfma_f32_16x16x32_bf16 v[28:31], v[180:183], a[244:247], v[28:31]\n"
    "v_mfma_f32_16x16x32_bf16 v[28:31], v[184:187], a[248:251], v[28:31]\n"
    "v_mfma_f32_16x16x32_bf16 v[28:31], v[188:191], a[252:255], v[28:31]\n"
    "v_mfma_f32_16x16x32_bf16 v[28:31], v[192:195], v[120:123], v[28:31]\n"
    "v_mfma_f32_16x16x32_bf16 v[28:31], v[196:199], v[124:127], v[28:31]\n"
    "v_mfma_f32_16x16x32_bf16 v[32:35], v[196:199], v[148:151], 0\n"
    "v_mfma_f32_16x16x32_bf16 v[32:35], v[192:195], v[144:147], v[32:35]\n"
    "v_mfma_f32_16x16x32_bf16 v[32:35], v[188:191], v[140:143], v[32:35]\n"
    "v_mfma_f32_16x16x32_bf16 v[32:35], v[184:187], v[136:139], v[32:35]\n"
    "v_mfma_f32_16x16x32_bf16 v[32:35], v[180:183], v[132:135], v[32:35]\n"
    "v_mfma_f32_16x16x32_bf16 v[32:35], v[176:179], v[128:131], v[32:35]\n"
    "s_waitcnt vmcnt(8)\n"
    "s_mov_b32 s80, 256\n"
    "buffer_store_dwordx2 v[242:243], v244, s[36:39], s80 offen offset:0 sc1\n"
    "v_cmp_eq_u64 s[54:55], v[240:241], v[242:243]\n"
    "s_nop 1\n"
    "s_mov_b32 s74, 0x00005555\n"
    "s_mov_b32 s75, 0x00005555\n"
    "s_or_b64 s[54:55], s[54:55], s[74:75]\n"
    "s_cmp_eq_u64 s[54:55], -1\n"
    "s_cbranch_scc0 .LpaV0\n"
    ".LpaV0r:\n"
    "s_add_u32 s72, s65, 24576\n"
    "s_add_u32 s73, s65, 28672\n"
    "s_mov_b32 s81, 256\n"
    "s_add_u32 m0, s68, 0\n"
    "s_nop 0\n"
    "buffer_load_dwordx4 v1, s[16:19], s67 offen offset:0 sc1 lds\n"
    "v_max3_f32 v232, v4, v5, v6\n"
    "v_max3_f32 v232, v232, v7, v20\n"
    "v_max3_f32 v232, v232, v21, v22\n"
    "v_max_f32 v232, v232, v23\n"
    "v_max3_f32 v233, v8, v9, v10\n"
    "v_max3_f32 v233, v233, v11, v24\n"
    "v_max3_f32 v233, v233, v25, v26\n"
    "v_max_f32 v233, v233, v27\n"
    "v_max3_f32 v234, v12, v13, v14\n"
    "v_max3_f32 v234, v234, v15, v28\n"
    "v_max3_f32 v234, v234, v29, v30\n"
    "v_max_f32 v234, v234, v31\n"
    "v_max3_f32 v235, v16, v17, v18\n"
    "buffer_load_dwordx4 v1, s[16:19], s67 offen offset:1024 sc1 lds\n"
    "v_max3_f32 v235, v235, v19, v32\n"
    "v_max3_f32 v235, v235, v33, v34\n"
    "v_max_f32 v235, v235, v35\n"
    "v_mov_b32 v236, v232\n"
    "s_nop 1\n"
    "v_permlane32_swap_b32 v232, v236\n"
    "v_max_f32 v232, v232, v236\n"
    "v_mov_b32 v236, v232\n"
    "s_nop 1\n"
    "v_permlane16_swap_b32 v232, v236\n"
    "v_max_f32 v232, v232, v236\n"
    "v_add_f32 v232, 0x42000000, v232\n"
    "v_sub_f32 v100, 0, v232\n"
    "v_sub_f32 v101, 0, v232\n"
    "v_sub_f32 v102, 0, v232\n"
    "buffer_load_dwordx4 v1, s[16:19], s67 offen offset:2048 sc1 lds\n"
    "v_sub_f32 v103, 0, v232\n"
    "v_mov_b32 v236, v233\n"
    "s_nop 1\n"
    "v_permlane32_swap_b32 v233, v236\n"
    "v_max_f32 v233, v233, v236\n"
    "v_mov_b32 v236, v233\n"
    "s_nop 1\n"
    "v_permlane16_swap_b32 v233, v236\n"
    "v_max_f32 v233, v233, v236\n"
    "v_add_f32 v233, 0x42000000, v233\n"
    "v_sub_f32 v104, 0, v233\n"
    "v_sub_f32 v105, 0, v233\n"
    "v_sub_f32 v106, 0, v233\n"
    "v_sub_f32 v107, 0, v233\n"
    "v_mov_b32 v236, v234\n"
    "buffer_load_dwordx4 v1, s[16:19], s67 offen offset:3072 sc1 lds\n"
    "s_nop 0\n"
    "v_permlane32_swap_b32 v234, v236\n"
    "v_max_f32 v234, v234, v236\n"
    "v_mov_b32 v236, v234\n"
    "s_nop 1\n"
    "v_permlane16_swap_b32 v234, v236\n"
    "v_max_f32 v234, v234, v236\n"
    "v_add_f32 v234, 0x42000000, v234\n"
    "v_sub_f32 v108, 0, v234\n"
    "v_sub_f32 v109, 0, v234\n"
    "v_sub_f32 v110, 0, v234\n"
    "v_sub_f32 v111, 0, v234\n"
    "v_mov_b32 v236, v235\n"
    "s_nop 1\n"
    "v_permlane32_swap_b32 v235, v236\n"
    "v_max_f32 v235, v235, v236\n"
    "s_add_u32 m0, s68, 4096\n"
    "s_add_u32 s83, s67, 4096\n"
    "buffer_load_dwordx4 v1, s[16:19], s83 offen offset:0 sc1 lds\n"
    "v_mov_b32 v236, v235\n"
    "s_nop 1\n"
    "v_permlane16_swap_b32 v235, v236\n"
    "v_max_f32 v235, v235, v236\n"
    "v_add_f32 v235, 0x42000000, v235\n"
    "v_sub_f32 v112, 0, v235\n"
    "v_sub_f32 v113, 0, v235\n"
    "v_sub_f32 v114, 0, v235\n"
    "v_sub_f32 v115, 0, v235\n"
    "v_sub_f32 v4, v4, v232\n"
    "v_sub_f32 v5, v5, v232\n"
    "v_sub_f32 v6, v6, v232\n"
    "v_sub_f32 v7, v7, v232\n"
    "v_sub_f32 v8, v8, v233\n"
    "buffer_load_dwordx4 v1, s[16:19], s83 offen offset:1024 sc1 lds\n"
    "v_sub_f32 v9, v9, v233\n"
    "v_sub_f32 v10, v10, v233\n"
    "v_sub_f32 v11, v11, v233\n"
    "v_sub_f32 v12, v12, v234\n"
    "v_sub_f32 v13, v13, v234\n"
    "v_sub_f32 v14, v14, v234\n"
    "v_sub_f32 v15, v15, v234\n"
    "v_sub_f32 v16, v16, v235\n"
    "v_sub_f32 v17, v17, v235\n"
    "v_sub_f32 v18, v18, v235\n"
    "v_sub_f32 v19, v19, v235\n"
    "v_sub_f32 v20, v20, v232\n"
    "v_sub_f32 v21, v21, v232\n"
    "s_mov_b32 m0, s72\n"
    "s_nop 0\n"
    "buffer_load_dwordx4 v1, s[16:19], s72 offen offset:0 sc1 lds\n"
    "v_sub_f32 v22, v22, v232\n"
    "v_sub_f32 v23, v23, v232\n"
    "v_sub_f32 v24, v24, v233\n"
    "v_sub_f32 v25, v25, v233\n"
    "v_sub_f32 v26, v26, v233\n"
    "v_sub_f32 v27, v27, v233\n"
    "v_sub_f32 v28, v28, v234\n"
    "v_sub_f32 v29, v29, v234\n"
    "v_sub_f32 v30, v30, v234\n"
    "v_sub_f32 v31, v31, v234\n"
    "v_sub_f32 v32, v32, v235\n"
    "v_sub_f32 v33, v33, v235\n"
    "v_sub_f32 v34, v34, v235\n"
    "buffer_load_dwordx4 v1, s[16:19], s72 offen offset:1024 sc1 lds\n"
    "v_sub_f32 v35, v35, v235\n"
    "v_exp_f32 v4, v4\n"
    "v_exp_f32 v5, v5\n"
    "v_exp_f32 v6, v6\n"
    "v_exp_f32 v7, v7\n"
    "v_exp_f32 v8, v8\n"
    "v_exp_f32 v9, v9\n"
    "v_exp_f32 v10, v10\n"
    "v_exp_f32 v11, v11\n"
    "v_exp_f32 v12, v12\n"
    "v_exp_f32 v13, v13\n"
    "v_exp_f32 v14, v14\n"
    "v_exp_f32 v15, v15\n"
    "buffer_load_dwordx4 v1, s[16:19], s72 offen offset:2048 sc1 lds\n"
    "v_exp_f32 v16, v16\n"
    "v_exp_f32 v17, v17\n"
    "v_exp_f32 v18, v18\n"
    "v_exp_f32 v19, v19\n"
    "v_exp_f32 v20, v20\n"
    "v_exp_f32 v21, v21\n"
    "v_exp_f32 v22, v22\n"
    "v_exp_f32 v23, v23\n"
    "v_exp_f32 v24, v24\n"
    "v_exp_f32 v25, v25\n"
    "v_exp_f32 v26, v26\n"
    "v_exp_f32 v27, v27\n"
    "v_exp_f32 v28, v28\n"
    "buffer_load_dwordx4 v1, s[16:19], s72 offen offset:3072 sc1 lds\n"
    "v_exp_f32 v29, v29\n"
    "v_exp_f32 v30, v30\n"
    "v_exp_f32 v31, v31\n"
    "v_exp_f32 v32, v32\n"
    "v_exp_f32 v33, v33\n"
    "v_exp_f32 v34, v34\n"
    "v_exp_f32 v35, v35\n"
    "v_mov_b32 v116, v4\n"
    "v_add_f32 v116, v116, v5\n"
    "v_add_f32 v116, v116, v6\n"
    "v_add_f32 v116, v116, v7\n"
    "v_mov_b32 v117, v8\n"
    "v_add_f32 v117, v117, v9\n"
    "s_mov_b32 m0, s73\n"
    "s_nop 0\n"
    "buffer_load_dwordx4 v1, s[16:19], s73 offen offset:0 sc1 lds\n"
    "v_add_f32 v117, v117, v10\n"
    "v_add_f32 v117, v117, v11\n"
    "v_mov_b32 v118, v12\n"
    "v_add_f32 v118, v118, v13\n"
    "v_add_f32 v118, v118, v14\n"
    "v_add_f32 v118, v118, v15\n"
    "v_mov_b32 v119, v16\n"
    "v_add_f32 v119, v119, v17\n"
    "v_add_f32 v119, v119, v18\n"
    "v_add_f32 v119, v119, v19\n"
    "v_add_f32 v116, v116, v20\n"
    "v_add_f32 v116, v116, v21\n"
    "v_add_f32 v116, v116, v22\n"
    "buffer_load_dwordx4 v1, s[16:19], s73 offen offset:1024 sc1 lds\n"
    "v_add_f32 v116, v116, v23\n"
    "v_add_f32 v117, v117, v24\n"
    "v_add_f32 v117, v117, v25\n"
    "v_add_f32 v117, v117, v26\n"
    "v_add_f32 v117, v117, v27\n"
    "v_add_f32 v118, v118, v28\n"
    "v_add_f32 v118, v118, v29\n"
    "v_add_f32 v118, v118, v30\n"
    "v_add_f32 v118, v118, v31\n"
    "v_add_f32 v119, v119, v32\n"
    "v_add_f32 v119, v119, v33\n"
    "v_add_f32 v119, v119, v34\n"
    "v_add_f32 v119, v119, v35\n"
    "s_mov_b32 s79, 256\n"
    "s_mov_b32 s71, 196608\n"
    "v_mov_b32 v249, 155456\n"
    "s_waitcnt vmcnt(13)\n"
    "ds_read_b128 v[232:235], v249 offset:0\n"
    "ds_read_b128 v[236:239], v249 offset:32\n"
    "v_cvt_f32_i32 v200, v200\n"
    "v_cvt_f32_i32 v201, v201\n"
    "v_cvt_f32_i32 v202, v202\n"
    "v_cvt_f32_i32 v204, v204\n"
    "v_cvt_f32_i32 v205, v205\n"
    "v_cvt_f32_i32 v206, v206\n"
    "v_cvt_f32_i32 v208, v208\n"
    "v_cvt_f32_i32 v209, v209\n"
    "v_cvt_f32_i32 v210, v210\n"
    "v_cvt_f32_i32 v212, v212\n"
    "v_cvt_f32_i32 v213, v213\n"
    "v_cvt_f32_i32 v214, v214\n"
    "s_waitcnt lgkmcnt(0)\n"
    "v_fma_f32 v200, v200, v232, v236\n"
    "v_fma_f32 v201, v201, v232, v236\n"
    "v_fma_f32 v202, v202, v232, v236\n"
    "v_fma_f32 v204, v204, v233, v237\n"
    "v_fma_f32 v205, v205, v233, v237\n"
    "v_fma_f32 v206, v206, v233, v237\n"
    "v_fma_f32 v208, v208, v234, v238\n"
    "v_fma_f32 v209, v209, v234, v238\n"
    "v_fma_f32 v210, v210, v234, v238\n"
    "v_fma_f32 v212, v212, v235, v239\n"
    "v_fma_f32 v213, v213, v235, v239\n"
    "v_fma_f32 v214, v214, v235, v239\n"
    "ds_read_b128 v[232:235], v249 offset:16\n"
    "ds_read_b128 v[236:239], v249 offset:48\n"
    "v_cvt_f32_i32 v216, v216\n"
    "v_cvt_f32_i32 v217, v217\n"
    "v_cvt_f32_i32 v218, v218\n"
    "v_cvt_f32_i32 v220, v220\n"
    "v_cvt_f32_i32 v221, v221\n"
    "v_cvt_f32_i32 v222, v222\n"
    "v_cvt_f32_i32 v224, v224\n"
    "v_cvt_f32_i32 v225, v225\n"
    "v_cvt_f32_i32 v226, v226\n"
    "v_cvt_f32_i32 v228, v228\n"
    "v_cvt_f32_i32 v229, v229\n"
    "v_cvt_f32_i32 v230, v230\n"
    "s_waitcnt lgkmcnt(0)\n"
    "v_fma_f32 v216, v216, v232, v236\n"
    "v_fma_f32 v217, v217, v232, v236\n"
    "v_fma_f32 v218, v218, v232, v236\n"
    "v_fma_f32 v220, v220, v233, v237\n"
    "v_fma_f32 v221, v221, v233, v237\n"
    "v_fma_f32 v222, v222, v233, v237\n"
    "v_fma_f32 v224, v224, v234, v238\n"
    "v_fma_f32 v225, v225, v234, v238\n"
    "v_fma_f32 v226, v226, v234, v238\n"
    "v_fma_f32 v228, v228, v235, v239\n"
    "v_fma_f32 v229, v229, v235, v239\n"
    "v_fma_f32 v230, v230, v235, v239\n"
    "buffer_load_dwordx2 v[240:241], v245, s[36:39], s81 offen offset:0 sc1\n"
    "s_add_u32 m0, s68, 24576\n"
    "s_add_u32 s83, s67, 24576\n"
    "buffer_load_dwordx4 v1, s[16:19], s83 offen offset:0 sc1 lds\n"
    "buffer_load_dwordx4 v1, s[16:19], s83 offen offset:1024 sc1 lds\n"
    "buffer_load_dwordx4 v1, s[16:19], s83 offen offset:2048 sc1 lds\n"
    "buffer_load_dwordx4 v1, s[16:19], s83 offen offset:3072 sc1 lds\n"
    "s_add_u32 m0, s68, 28672\n"
    "s_add_u32 s83, s67, 28672\n"
    "buffer_load_dwordx4 v1, s[16:19], s83 offen offset:0 sc1 lds\n"
    "buffer_load_dwordx4 v1, s[16:19], s83 offen offset:1024 sc1 lds\n"
    "s_add_u32 s67, s67, 49152\n"
    "s_waitcnt vmcnt(6)\n"
    "ds_add_u32 v252, v251\n"
    "ds_read_b128 v[152:155], v2 offset:12288\n"
    "ds_read_b128 v[156:159], v2 offset:14336\n"
    "ds_read_b128 v[160:163], v2 offset:16384\n"
    "ds_read_b128 v[164:167], v2 offset:18432\n"
    "ds_read_b128 v[168:171], v2 offset:20480\n"
    "ds_read_b128 v[172:175], v2 offset:22528\n"
    "s_waitcnt lgkmcnt(0)\n"
    "s_nop 7\n"
    "s_branch .LPV\n"
    ".Lp1_2:\n"
    "s_load_dwordx2 s[28:29], %1, 8\n"
    "s_load_dwordx2 s[40:41], %1, 24\n"
    "s_load_dwordx2 s[44:45], %1, 32\n"
    "s_mov_b32 s30, 1572864\n"
    "s_mov_b32 s31, 0x20000\n"
    "s_mov_b32 s42, 12288\n"
    "s_mov_b32 s43, 0x20000\n"
    "s_mov_b32 s46, 12288\n"
    "s_mov_b32 s47, 0x20000\n"
    "s_mov_b32 s38, 4224\n"
    "s_mov_b32 s39, 0x20000\n"
    "s_mov_b32 s22, 98304\n"
    "s_mov_b32 s23, 0x20000\n"
    "s_mul_i32 s72, s52, 1572864\n"
    "s_mul_i32 s73, s52, 12288\n"
    "s_mul_i32 s74, s52, 8192\n"
    "s_mul_i32 s75, s52, 786432\n"
    "s_mul_i32 s76, s53, 98304\n"
    "s_add_u32 s75, s75, s76\n"
    "s_mul_i32 s63, s53, 96\n"
    "s_mul_i32 s77, s53, 48\n"
    "v_lshrrev_b32 v232, 2, v161\n"
    "v_and_b32 v233, 3, v161\n"
    "v_mul_u32_u24 v157, 768, v232\n"
    "v_mul_u32_u24 v234, 12, v233\n"
    "v_add3_u32 v157, v157, v234, s77\n"
    "v_add_u32 v157, 384, v157\n"
    "v_and_b32 v235, 3, v232\n"
    "v_mul_u32_u24 v159, 48, v235\n"
    "v_add_u32 v159, v159, v234\n"
    "v_add_u32 v159, 192, v159\n"
    "v_lshlrev_b32 v236, 1, v161\n"
    "v_and_b32 v236, 24, v236\n"
    "v_and_b32 v237, 4, v232\n"
    "v_and_b32 v246, 35, v161\n"
    "v_or3_b32 v246, v246, v236, v237\n"
    "v_mul_u32_u24 v246, 768, v246\n"
    "s_waitcnt lgkmcnt(0)\n"
    "s_mov_b64 s[16:17], s[36:37]\n"
    "s_add_u32 s40, s40, s73\n"
    "s_addc_u32 s41, s41, 0\n"
    "s_add_u32 s44, s44, s73\n"
    "s_addc_u32 s45, s45, 0\n"
    "s_add_u32 s36, s36, s74\n"
    "s_addc_u32 s37, s37, 0\n"
    "s_add_u32 s28, s28, s72\n"
    "s_addc_u32 s29, s29, 0\n"
    "s_add_u32 s20, s20, s75\n"
    "s_addc_u32 s21, s21, 0\n"
    "s_add_u32 s24, s24, s75\n"
    "s_addc_u32 s25, s25, 0\n"
    "buffer_load_dwordx3 v[124:126], v159, s[40:43], 0 offen\n"
    "buffer_load_dwordx3 v[132:134], v159, s[44:47], 0 offen\n"
    "buffer_load_dwordx3 v[120:122], v157, s[40:43], 0 offen\n"
    "buffer_load_dwordx3 v[128:130], v157, s[44:47], 0 offen\n"
    "v_mov_b32 v238, 0\n"
    "buffer_load_dwordx2 v[242:243], v238, s[36:39], 0 offen offset:0 sc1\n"
    "s_add_u32 s80, s63, 49152\n"
    "buffer_load_dwordx4 v[200:203], v246, s[28:31], s80 offen offset:0\n"
    "buffer_load_dwordx4 v[204:207], v246, s[28:31], s80 offen offset:16\n"
    "buffer_load_dwordx4 v[208:211], v246, s[28:31], s80 offen offset:32\n"
    "buffer_load_dwordx4 v[212:215], v246, s[28:31], s80 offen offset:48\n"
    "buffer_load_dwordx4 v[216:219], v246, s[28:31], s80 offen offset:64\n"
    "buffer_load_dwordx4 v[220:223], v246, s[28:31], s80 offen offset:80\n"
    "s_mul_i32 s72, s52, 1576960\n"
    "s_add_u32 s72, s72, 262144\n"
    "s_add_u32 s16, s16, s72\n"
    "s_addc_u32 s17, s17, 0\n"
    "s_mov_b32 s18, 1572864\n"
    "s_mov_b32 s19, 0x20000\n"
    "s_mov_b32 s26, 98304\n"
    "s_mov_b32 s27, 0x20000\n"
    "s_mov_b32 s65, 12288\n"
    "s_mov_b32 s66, 0\n"
    "s_mov_b32 s67, 61440\n"
    "s_mov_b32 s68, 61440\n"
    "s_mov_b32 s69, 159744\n"
    "s_mov_b32 s58, 0\n"
    "s_mov_b32 s59, 4\n"
    "s_mul_i32 s73, s53, 3\n"
    "s_add_u32 s74, s73, 0\n"
    "s_lshr_b32 s75, s74, 2\n"
    "s_lshl_b32 s75, s75, 11\n"
    "s_and_b32 s74, s74, 3\n"
    "s_lshl_b32 s74, s74, 8\n"
    "s_or_b32 s60, s74, s75\n"
    "s_add_u32 s74, s73, 1\n"
    "s_lshr_b32 s75, s74, 2\n"
    "s_lshl_b32 s75, s75, 11\n"
    "s_and_b32 s74, s74, 3\n"
    "s_lshl_b32 s74, s74, 8\n"
    "s_or_b32 s61, s74, s75\n"
    "s_add_u32 s74, s73, 2\n"
    "s_lshr_b32 s75, s74, 2\n"
    "s_lshl_b32 s75, s75, 11\n"
    "s_and_b32 s74, s74, 3\n"
    "s_lshl_b32 s74, s74, 8\n"
    "s_or_b32 s62, s74, s75\n"
    "s_mov_b32 s82, 0x7fff0000\n"
    "v_lshrrev_b32 v232, 2, v161\n"
    "v_and_b32 v233, 3, v161\n"
    "v_mul_u32_u24 v158, 192, v232\n"
    "v_mul_u32_u24 v234, 24, v233\n"
    "v_add_u32 v158, v158, v234\n"
    "v_add_u32 v158, 151296, v158\n"
    "v_and_b32 v235, 3, v232\n"
    "v_mul_u32_u24 v160, 192, v235\n"
    "v_add_u32 v160, v160, v234\n"
    "v_add_u32 v160, 154368, v160\n"
    "v_lshrrev_b32 v234, 5, v161\n"
    "v_mul_u32_u24 v248, 12288, v234\n"
    "v_and_b32 v234, 16, v161\n"
    "v_lshl_add_u32 v248, v234, 6, v248\n"
    "v_and_b32 v234, 0xf0, v1\n"
    "v_add_u32 v248, v248, v234\n"
    "v_and_b32 v232, 15, v161\n"
    "v_lshrrev_b32 v233, 4, v161\n"
    "v_and_b32 v234, 7, v161\n"
    "v_xor_b32 v233, v233, v234\n"
    "v_mul_u32_u24 v155, 384, v232\n"
    "v_lshl_add_u32 v155, v233, 4, v155\n"
    "v_add_u32 v155, 98304, v155\n"
    "v_xor_b32 v156, 64, v155\n"
    "v_mov_b32 v2, v1\n"
    "v_add_u32 v3, 49152, v1\n"
    "v_and_b32 v245, 0xf0, v1\n"
    "v_lshrrev_b32 v245, 1, v245\n"
    "v_add_u32 v245, 128, v245\n"
    "s_lshl_b32 s73, s53, 4\n"
    "s_add_u32 s73, s73, 128\n"
    "v_mov_b32 v244, 0x7fff0000\n"
    "v_mov_b32 v239, s73\n"
    "v_cmp_eq_u32 vcc, 0, v1\n"
    "s_nop 1\n"
    "v_cndmask_b32 v244, v244, v239, vcc\n"
    "v_lshlrev_b32 v252, 2, v161\n"
    "v_add_u32 v252, 157952, v252\n"
    "v_mov_b32 v251, 1\n"
    "s_waitcnt vmcnt(6)\n"
    "v_add_co_u32 v242, vcc, 0x7f4a7c15, v242\n"
    "v_mov_b32 v238, 0x9e3779b9\n"
    "s_nop 0\n"
    "v_addc_co_u32 v243, vcc, v238, v243, vcc\n"
    "s_mov_b32 s73, 0x3dd53b95\n"
    "v_lshlrev_b32 v136, 16, v124\n"
    "v_lshlrev_b32 v144, 16, v132\n"
    "v_and_b32 v137, 0xffff0000, v124\n"
    "v_and_b32 v145, 0xffff0000, v132\n"
    "v_lshlrev_b32 v138, 16, v125\n"
    "v_lshlrev_b32 v146, 16, v133\n"
    "v_and_b32 v139, 0xffff0000, v125\n"
    "v_and_b32 v147, 0xffff0000, v133\n"
    "v_lshlrev_b32 v140, 16, v126\n"
    "v_lshlrev_b32 v148, 16, v134\n"
    "v_and_b32 v141, 0xffff0000, v126\n"
    "v_and_b32 v149, 0xffff0000, v134\n"
    "v_mul_f32 v136, s73, v136\n"
    "v_mul_f32 v137, s73, v137\n"
    "v_mul_f32 v138, s73, v138\n"
    "v_mul_f32 v139, s73, v139\n"
    "v_mul_f32 v140, s73, v140\n"
    "v_mul_f32 v141, s73, v141\n"
    "v_mul_f32 v144, -v144, v136\n"
    "v_mul_f32 v145, -v145, v137\n"
    "v_mul_f32 v146, -v146, v138\n"
    "v_mul_f32 v147, -v147, v139\n"
    "v_mul_f32 v148, -v148, v140\n"
    "v_mul_f32 v149, -v149, v141\n"
    "ds_write_b64 v160, v[136:137] offset:0\n"
    "ds_write_b64 v160, v[144:145] offset:96\n"
    "ds_write_b64 v160, v[138:139] offset:8\n"
    "ds_write_b64 v160, v[146:147] offset:104\n"
    "ds_write_b64 v160, v[140:141] offset:16\n"
    "ds_write_b64 v160, v[148:149] offset:112\n"
    "v_lshlrev_b32 v136, 16, v120\n"
    "v_lshlrev_b32 v144, 16, v128\n"
    "v_and_b32 v137, 0xffff0000, v120\n"
    "v_and_b32 v145, 0xffff0000, v128\n"
    "v_lshlrev_b32 v138, 16, v121\n"
    "v_lshlrev_b32 v146, 16, v129\n"
    "v_and_b32 v139, 0xffff0000, v121\n"
    "v_and_b32 v147, 0xffff0000, v129\n"
    "v_lshlrev_b32 v140, 16, v122\n"
    "v_lshlrev_b32 v148, 16, v130\n"
    "v_and_b32 v141, 0xffff0000, v122\n"
    "v_and_b32 v149, 0xffff0000, v130\n"
    "v_mul_f32 v136, s73, v136\n"
    "v_mul_f32 v137, s73, v137\n"
    "v_mul_f32 v138, s73, v138\n"
    "v_mul_f32 v139, s73, v139\n"
    "v_mul_f32 v140, s73, v140\n"
    "v_mul_f32 v141, s73, v141\n"
    "v_mul_f32 v144, -v144, v136\n"
    "v_mul_f32 v145, -v145, v137\n"
    "v_mul_f32 v146, -v146, v138\n"
    "v_mul_f32 v147, -v147, v139\n"
    "v_mul_f32 v148, -v148, v140\n"
    "v_mul_f32 v149, -v149, v141\n"
    "ds_write_b64 v158, v[136:137] offset:0\n"
    "ds_write_b64 v158, v[144:145] offset:96\n"
    "ds_write_b64 v158, v[138:139] offset:8\n"
    "ds_write_b64 v158, v[146:147] offset:104\n"
    "ds_write_b64 v158, v[140:141] offset:16\n"
    "ds_write_b64 v158, v[148:149] offset:112\n"
    "s_waitcnt vmcnt(0)\n"
    "s_mov_b32 s71, 49152\n"
    "v_mov_b32 v249, 151296\n"
    "s_add_u32 s76, s71, s60\n"
    "s_add_u32 s77, s71, s61\n"
    "s_add_u32 s78, s71, s62\n"
    "ds_read_b128 v[168:171], v249 offset:0\n"
    "ds_read_b128 v[172:175], v249 offset:96\n"
    "ds_read_b128 v[176:179], v249 offset:16\n"
    "ds_read_b128 v[180:183], v249 offset:112\n"
    "v_cvt_f32_i32 v200, v200\n"
    "v_cvt_f32_i32 v201, v201\n"
    "v_cvt_f32_i32 v202, v202\n"
    "v_cvt_f32_i32 v203, v203\n"
    "v_cvt_f32_i32 v204, v204\n"
    "v_cvt_f32_i32 v205, v205\n"
    "v_cvt_f32_i32 v206, v206\n"
    "v_cvt_f32_i32 v207, v207\n"
    "s_waitcnt lgkmcnt(2)\n"
    "v_fma_f32 v200, v200, v168, v172\n"
    "v_fma_f32 v201, v201, v169, v173\n"
    "v_fma_f32 v202, v202, v170, v174\n"
    "v_fma_f32 v203, v203, v171, v175\n"
    "ds_read_b128 v[168:171], v249 offset:32\n"
    "ds_read_b128 v[172:175], v249 offset:128\n"
    "v_cvt_f32_i32 v208, v208\n"
    "v_cvt_f32_i32 v209, v209\n"
    "v_cvt_f32_i32 v210, v210\n"
    "v_cvt_f32_i32 v211, v211\n"
    "v_cvt_f32_i32 v212, v212\n"
    "v_cvt_f32_i32 v213, v213\n"
    "v_cvt_f32_i32 v214, v214\n"
    "v_cvt_f32_i32 v215, v215\n"
    "s_waitcnt lgkmcnt(2)\n"
    "v_fma_f32 v204, v204, v176, v180\n"
    "v_fma_f32 v205, v205, v177, v181\n"
    "v_fma_f32 v206, v206, v178, v182\n"
    "v_fma_f32 v207, v207, v179, v183\n"
    "ds_read_b128 v[176:179], v249 offset:48\n"
    "ds_read_b128 v[180:183], v249 offset:144\n"
    "v_cvt_pk_bf16_f32 v200, v200, v201\n"
    "v_cvt_pk_bf16_f32 v201, v202, v203\n"
    "v_cvt_pk_bf16_f32 v202, v204, v205\n"
    "v_cvt_pk_bf16_f32 v203, v206, v207\n"
    "buffer_store_dwordx4 v[200:203], v248, s[16:19], s76 offen offset:0 sc1\n"
    "s_waitcnt lgkmcnt(2)\n"
    "v_fma_f32 v208, v208, v168, v172\n"
    "v_fma_f32 v209, v209, v169, v173\n"
    "v_fma_f32 v210, v210, v170, v174\n"
    "v_fma_f32 v211, v211, v171, v175\n"
    "ds_read_b128 v[168:171], v249 offset:64\n"
    "ds_read_b128 v[172:175], v249 offset:160\n"
    "v_cvt_f32_i32 v216, v216\n"
    "v_cvt_f32_i32 v217, v217\n"
    "v_cvt_f32_i32 v218, v218\n"
    "v_cvt_f32_i32 v219, v219\n"
    "v_cvt_f32_i32 v220, v220\n"
    "v_cvt_f32_i32 v221, v221\n"
    "v_cvt_f32_i32 v222, v222\n"
    "v_cvt_f32_i32 v223, v223\n"
    "s_waitcnt lgkmcnt(2)\n"
    "v_fma_f32 v212, v212, v176, v180\n"
    "v_fma_f32 v213, v213, v177, v181\n"
    "v_fma_f32 v214, v214, v178, v182\n"
    "v_fma_f32 v215, v215, v179, v183\n"
    "ds_read_b128 v[176:179], v249 offset:80\n"
    "ds_read_b128 v[180:183], v249 offset:176\n"
    "v_cvt_pk_bf16_f32 v208, v208, v209\n"
    "v_cvt_pk_bf16_f32 v209, v210, v211\n"
    "v_cvt_pk_bf16_f32 v210, v212, v213\n"
    "v_cvt_pk_bf16_f32 v211, v214, v215\n"
    "buffer_store_dwordx4 v[208:211], v248, s[16:19], s77 offen offset:0 sc1\n"
    "s_waitcnt lgkmcnt(2)\n"
    "v_fma_f32 v216, v216, v168, v172\n"
    "v_fma_f32 v217, v217, v169, v173\n"
    "v_fma_f32 v218, v218, v170, v174\n"
    "v_fma_f32 v219, v219, v171, v175\n"
    "s_waitcnt lgkmcnt(0)\n"
    "v_fma_f32 v220, v220, v176, v180\n"
    "v_fma_f32 v221, v221, v177, v181\n"
    "v_fma_f32 v222, v222, v178, v182\n"
    "v_fma_f32 v223, v223, v179, v183\n"
    "v_cvt_pk_bf16_f32 v216, v216, v217\n"
    "v_cvt_pk_bf16_f32 v217, v218, v219\n"
    "v_cvt_pk_bf16_f32 v218, v220, v221\n"
    "v_cvt_pk_bf16_f32 v219, v222, v223\n"
    "buffer_store_dwordx4 v[216:219], v248, s[16:19], s78 offen offset:0 sc1\n"
    "s_mov_b32 s80, 384\n"
    "buffer_load_dwordx4 v[4:7], v246, s[28:31], s80 offen offset:0\n"
    "buffer_load_dwordx4 v[8:11], v246, s[28:31], s80 offen offset:16\n"
    "buffer_load_dwordx4 v[12:15], v246, s[28:31], s80 offen offset:32\n"
    "buffer_load_dwordx4 v[16:19], v246, s[28:31], s80 offen offset:48\n"
    "buffer_load_dwordx4 v[20:23], v246, s[28:31], s80 offen offset:64\n"
    "buffer_load_dwordx4 v[24:27], v246, s[28:31], s80 offen offset:80\n"
    "s_mov_b32 s80, 480\n"
    "buffer_load_dwordx4 v[28:31], v246, s[28:31], s80 offen offset:0\n"
    "buffer_load_dwordx4 v[32:35], v246, s[28:31], s80 offen offset:16\n"
    "buffer_load_dwordx4 v[36:39], v246, s[28:31], s80 offen offset:32\n"
    "buffer_load_dwordx4 v[40:43], v246, s[28:31], s80 offen offset:48\n"
    "buffer_load_dwordx4 v[44:47], v246, s[28:31], s80 offen offset:64\n"
    "buffer_load_dwordx4 v[48:51], v246, s[28:31], s80 offen offset:80\n"
    "s_mov_b32 s80, 576\n"
    "buffer_load_dwordx4 v[52:55], v246, s[28:31], s80 offen offset:0\n"
    "buffer_load_dwordx4 v[56:59], v246, s[28:31], s80 offen offset:16\n"
    "buffer_load_dwordx4 v[60:63], v246, s[28:31], s80 offen offset:32\n"
    "buffer_load_dwordx4 v[64:67], v246, s[28:31], s80 offen offset:48\n"
    "buffer_load_dwordx4 v[68:71], v246, s[28:31], s80 offen offset:64\n"
    "buffer_load_dwordx4 v[72:75], v246, s[28:31], s80 offen offset:80\n"
    "s_mov_b32 s80, 672\n"
    "buffer_load_dwordx4 v[76:79], v246, s[28:31], s80 offen offset:0\n"
    "buffer_load_dwordx4 v[80:83], v246, s[28:31], s80 offen offset:16\n"
    "buffer_load_dwordx4 v[84:87], v246, s[28:31], s80 offen offset:32\n"
    "buffer_load_dwordx4 v[88:91], v246, s[28:31], s80 offen offset:48\n"
    "buffer_load_dwordx4 v[92:95], v246, s[28:31], s80 offen offset:64\n"
    "buffer_load_dwordx4 v[96:99], v246, s[28:31], s80 offen offset:80\n"
    "v_accvgpr_write_b32 a0, 0\n"
    "v_accvgpr_write_b32 a1, 0\n"
    "v_accvgpr_write_b32 a2, 0\n"
    "v_accvgpr_write_b32 a3, 0\n"
    "v_accvgpr_write_b32 a4, 0\n"
    "v_accvgpr_write_b32 a5, 0\n"
    "v_accvgpr_write_b32 a6, 0\n"
    "v_accvgpr_write_b32 a7, 0\n"
    "v_accvgpr_write_b32 a8, 0\n"
    "v_accvgpr_write_b32 a9, 0\n"
    "v_accvgpr_write_b32 a10, 0\n"
    "v_accvgpr_write_b32 a11, 0\n"
    "v_accvgpr_write_b32 a12, 0\n"
    "v_accvgpr_write_b32 a13, 0\n"
    "v_accvgpr_write_b32 a14, 0\n"
    "v_accvgpr_write_b32 a15, 0\n"
    "v_accvgpr_write_b32 a16, 0\n"
    "v_accvgpr_write_b32 a17, 0\n"
    "v_accvgpr_write_b32 a18, 0\n"
    "v_accvgpr_write_b32 a19, 0\n"
    "v_accvgpr_write_b32 a20, 0\n"
    "v_accvgpr_write_b32 a21, 0\n"
    "v_accvgpr_write_b32 a22, 0\n"
    "v_accvgpr_write_b32 a23, 0\n"
    "v_accvgpr_write_b32 a24, 0\n"
    "v_accvgpr_write_b32 a25, 0\n"
    "v_accvgpr_write_b32 a26, 0\n"
    "v_accvgpr_write_b32 a27, 0\n"
    "v_accvgpr_write_b32 a28, 0\n"
    "v_accvgpr_write_b32 a29, 0\n"
    "v_accvgpr_write_b32 a30, 0\n"
    "v_accvgpr_write_b32 a31, 0\n"
    "v_accvgpr_write_b32 a32, 0\n"
    "v_accvgpr_write_b32 a33, 0\n"
    "v_accvgpr_write_b32 a34, 0\n"
    "v_accvgpr_write_b32 a35, 0\n"
    "v_accvgpr_write_b32 a36, 0\n"
    "v_accvgpr_write_b32 a37, 0\n"
    "v_accvgpr_write_b32 a38, 0\n"
    "v_accvgpr_write_b32 a39, 0\n"
    "v_accvgpr_write_b32 a40, 0\n"
    "v_accvgpr_write_b32 a41, 0\n"
    "v_accvgpr_write_b32 a42, 0\n"
    "v_accvgpr_write_b32 a43, 0\n"
    "v_accvgpr_write_b32 a44, 0\n"
    "v_accvgpr_write_b32 a45, 0\n"
    "v_accvgpr_write_b32 a46, 0\n"
    "v_accvgpr_write_b32 a47, 0\n"
    "v_accvgpr_write_b32 a48, 0\n"
    "v_accvgpr_write_b32 a49, 0\n"
    "v_accvgpr_write_b32 a50, 0\n"
    "v_accvgpr_write_b32 a51, 0\n"
    "v_accvgpr_write_b32 a52, 0\n"
    "v_accvgpr_write_b32 a53, 0\n"
    "v_accvgpr_write_b32 a54, 0\n"
    "v_accvgpr_write_b32 a55, 0\n"
    "v_accvgpr_write_b32 a56, 0\n"
    "v_accvgpr_write_b32 a57, 0\n"
    "v_accvgpr_write_b32 a58, 0\n"
    "v_accvgpr_write_b32 a59, 0\n"
    "v_accvgpr_write_b32 a60, 0\n"
    "v_accvgpr_write_b32 a61, 0\n"
    "v_accvgpr_write_b32 a62, 0\n"
    "v_accvgpr_write_b32 a63, 0\n"
    "v_accvgpr_write_b32 a64, 0\n"
    "v_accvgpr_write_b32 a65, 0\n"
    "v_accvgpr_write_b32 a66, 0\n"
    "v_accvgpr_write_b32 a67, 0\n"
    "v_accvgpr_write_b32 a68, 0\n"
    "v_accvgpr_write_b32 a69, 0\n"
    "v_accvgpr_write_b32 a70, 0\n"
    "v_accvgpr_write_b32 a71, 0\n"
    "v_accvgpr_write_b32 a72, 0\n"
    "v_accvgpr_write_b32 a73, 0\n"
    "v_accvgpr_write_b32 a74, 0\n"
    "v_accvgpr_write_b32 a75, 0\n"
    "v_accvgpr_write_b32 a76, 0\n"
    "v_accvgpr_write_b32 a77, 0\n"
    "v_accvgpr_write_b32 a78, 0\n"
    "v_accvgpr_write_b32 a79, 0\n"
    "v_accvgpr_write_b32 a80, 0\n"
    "v_accvgpr_write_b32 a81, 0\n"
    "v_accvgpr_write_b32 a82, 0\n"
    "v_accvgpr_write_b32 a83, 0\n"
    "v_accvgpr_write_b32 a84, 0\n"
    "v_accvgpr_write_b32 a85, 0\n"
    "v_accvgpr_write_b32 a86, 0\n"
    "v_accvgpr_write_b32 a87, 0\n"
    "v_accvgpr_write_b32 a88, 0\n"
    "v_accvgpr_write_b32 a89, 0\n"
    "v_accvgpr_write_b32 a90, 0\n"
    "v_accvgpr_write_b32 a91, 0\n"
    "v_accvgpr_write_b32 a92, 0\n"
    "v_accvgpr_write_b32 a93, 0\n"
    "v_accvgpr_write_b32 a94, 0\n"
    "v_accvgpr_write_b32 a95, 0\n"
    "v_accvgpr_write_b32 a96, 0\n"
    "v_accvgpr_write_b32 a97, 0\n"
    "v_accvgpr_write_b32 a98, 0\n"
    "v_accvgpr_write_b32 a99, 0\n"
    "v_accvgpr_write_b32 a100, 0\n"
    "v_accvgpr_write_b32 a101, 0\n"
    "v_accvgpr_write_b32 a102, 0\n"
    "v_accvgpr_write_b32 a103, 0\n"
    "v_accvgpr_write_b32 a104, 0\n"
    "v_accvgpr_write_b32 a105, 0\n"
    "v_accvgpr_write_b32 a106, 0\n"
    "v_accvgpr_write_b32 a107, 0\n"
    "v_accvgpr_write_b32 a108, 0\n"
    "v_accvgpr_write_b32 a109, 0\n"
    "v_accvgpr_write_b32 a110, 0\n"
    "v_accvgpr_write_b32 a111, 0\n"
    "v_accvgpr_write_b32 a112, 0\n"
    "v_accvgpr_write_b32 a113, 0\n"
    "v_accvgpr_write_b32 a114, 0\n"
    "v_accvgpr_write_b32 a115, 0\n"
    "v_accvgpr_write_b32 a116, 0\n"
    "v_accvgpr_write_b32 a117, 0\n"
    "v_accvgpr_write_b32 a118, 0\n"
    "v_accvgpr_write_b32 a119, 0\n"
    "v_accvgpr_write_b32 a120, 0\n"
    "v_accvgpr_write_b32 a121, 0\n"
    "v_accvgpr_write_b32 a122, 0\n"
    "v_accvgpr_write_b32 a123, 0\n"
    "v_accvgpr_write_b32 a124, 0\n"
    "v_accvgpr_write_b32 a125, 0\n"
    "v_accvgpr_write_b32 a126, 0\n"
    "v_accvgpr_write_b32 a127, 0\n"
    "v_accvgpr_write_b32 a128, 0\n"
    "v_accvgpr_write_b32 a129, 0\n"
    "v_accvgpr_write_b32 a130, 0\n"
    "v_accvgpr_write_b32 a131, 0\n"
    "v_accvgpr_write_b32 a132, 0\n"
    "v_accvgpr_write_b32 a133, 0\n"
    "v_accvgpr_write_b32 a134, 0\n"
    "v_accvgpr_write_b32 a135, 0\n"
    "v_accvgpr_write_b32 a136, 0\n"
    "v_accvgpr_write_b32 a137, 0\n"
    "v_accvgpr_write_b32 a138, 0\n"
    "v_accvgpr_write_b32 a139, 0\n"
    "v_accvgpr_write_b32 a140, 0\n"
    "v_accvgpr_write_b32 a141, 0\n"
    "v_accvgpr_write_b32 a142, 0\n"
    "v_accvgpr_write_b32 a143, 0\n"
    "v_accvgpr_write_b32 a144, 0\n"
    "v_accvgpr_write_b32 a145, 0\n"
    "v_accvgpr_write_b32 a146, 0\n"
    "v_accvgpr_write_b32 a147, 0\n"
    "v_accvgpr_write_b32 a148, 0\n"
    "v_accvgpr_write_b32 a149, 0\n"
    "v_accvgpr_write_b32 a150, 0\n"
    "v_accvgpr_write_b32 a151, 0\n"
    "v_accvgpr_write_b32 a152, 0\n"
    "v_accvgpr_write_b32 a153, 0\n"
    "v_accvgpr_write_b32 a154, 0\n"
    "v_accvgpr_write_b32 a155, 0\n"
    "v_accvgpr_write_b32 a156, 0\n"
    "v_accvgpr_write_b32 a157, 0\n"
    "v_accvgpr_write_b32 a158, 0\n"
    "v_accvgpr_write_b32 a159, 0\n"
    "v_accvgpr_write_b32 a160, 0\n"
    "v_accvgpr_write_b32 a161, 0\n"
    "v_accvgpr_write_b32 a162, 0\n"
    "v_accvgpr_write_b32 a163, 0\n"
    "v_accvgpr_write_b32 a164, 0\n"
    "v_accvgpr_write_b32 a165, 0\n"
    "v_accvgpr_write_b32 a166, 0\n"
    "v_accvgpr_write_b32 a167, 0\n"
    "v_accvgpr_write_b32 a168, 0\n"
    "v_accvgpr_write_b32 a169, 0\n"
    "v_accvgpr_write_b32 a170, 0\n"
    "v_accvgpr_write_b32 a171, 0\n"
    "v_accvgpr_write_b32 a172, 0\n"
    "v_accvgpr_write_b32 a173, 0\n"
    "v_accvgpr_write_b32 a174, 0\n"
    "v_accvgpr_write_b32 a175, 0\n"
    "v_accvgpr_write_b32 a176, 0\n"
    "v_accvgpr_write_b32 a177, 0\n"
    "v_accvgpr_write_b32 a178, 0\n"
    "v_accvgpr_write_b32 a179, 0\n"
    "v_accvgpr_write_b32 a180, 0\n"
    "v_accvgpr_write_b32 a181, 0\n"
    "v_accvgpr_write_b32 a182, 0\n"
    "v_accvgpr_write_b32 a183, 0\n"
    "v_accvgpr_write_b32 a184, 0\n"
    "v_accvgpr_write_b32 a185, 0\n"
    "v_accvgpr_write_b32 a186, 0\n"
    "v_accvgpr_write_b32 a187, 0\n"
    "v_accvgpr_write_b32 a188, 0\n"
    "v_accvgpr_write_b32 a189, 0\n"
    "v_accvgpr_write_b32 a190, 0\n"
    "v_accvgpr_write_b32 a191, 0\n"
    "s_waitcnt vmcnt(24)\n"
    "s_mov_b32 s80, 128\n"
    "buffer_store_dwordx2 v[242:243], v244, s[36:39], s80 offen offset:0 sc1\n"
    "s_waitcnt vmcnt(19)\n"
    "v_mov_b32 v249, 154368\n"
    "ds_read_b128 v[168:171], v249 offset:0\n"
    "ds_read_b128 v[172:175], v249 offset:96\n"
    "ds_read_b128 v[176:179], v249 offset:16\n"
    "ds_read_b128 v[180:183], v249 offset:112\n"
    "v_cvt_f32_i32 v4, v4\n"
    "v_cvt_f32_i32 v5, v5\n"
    "v_cvt_f32_i32 v6, v6\n"
    "v_cvt_f32_i32 v7, v7\n"
    "v_cvt_f32_i32 v8, v8\n"
    "v_cvt_f32_i32 v9, v9\n"
    "v_cvt_f32_i32 v10, v10\n"
    "v_cvt_f32_i32 v11, v11\n"
    "s_waitcnt lgkmcnt(2)\n"
    "v_fma_f32 v4, v4, v168, v172\n"
    "v_fma_f32 v5, v5, v169, v173\n"
    "v_fma_f32 v6, v6, v170, v174\n"
    "v_fma_f32 v7, v7, v171, v175\n"
    "ds_read_b128 v[168:171], v249 offset:32\n"
    "ds_read_b128 v[172:175], v249 offset:128\n"
    "v_cvt_f32_i32 v12, v12\n"
    "v_cvt_f32_i32 v13, v13\n"
    "v_cvt_f32_i32 v14, v14\n"
    "v_cvt_f32_i32 v15, v15\n"
    "v_cvt_f32_i32 v16, v16\n"
    "v_cvt_f32_i32 v17, v17\n"
    "v_cvt_f32_i32 v18, v18\n"
    "v_cvt_f32_i32 v19, v19\n"
    "s_waitcnt lgkmcnt(2)\n"
    "v_fma_f32 v8, v8, v176, v180\n"
    "v_fma_f32 v9, v9, v177, v181\n"
    "v_fma_f32 v10, v10, v178, v182\n"
    "v_fma_f32 v11, v11, v179, v183\n"
    "ds_read_b128 v[176:179], v249 offset:48\n"
    "ds_read_b128 v[180:183], v249 offset:144\n"
    "v_cvt_pk_bf16_f32 v4, v4, v5\n"
    "v_cvt_pk_bf16_f32 v5, v6, v7\n"
    "v_cvt_pk_bf16_f32 v6, v8, v9\n"
    "v_cvt_pk_bf16_f32 v7, v10, v11\n"
    "ds_write_b128 v248, v[4:7] offset:6144\n"
    "s_waitcnt lgkmcnt(3)\n"
    "v_fma_f32 v12, v12, v168, v172\n"
    "v_fma_f32 v13, v13, v169, v173\n"
    "v_fma_f32 v14, v14, v170, v174\n"
    "v_fma_f32 v15, v15, v171, v175\n"
    "ds_read_b128 v[168:171], v249 offset:64\n"
    "ds_read_b128 v[172:175], v249 offset:160\n"
    "v_cvt_f32_i32 v20, v20\n"
    "v_cvt_f32_i32 v21, v21\n"
    "v_cvt_f32_i32 v22, v22\n"
    "v_cvt_f32_i32 v23, v23\n"
    "v_cvt_f32_i32 v24, v24\n"
    "v_cvt_f32_i32 v25, v25\n"
    "v_cvt_f32_i32 v26, v26\n"
    "v_cvt_f32_i32 v27, v27\n"
    "s_waitcnt lgkmcnt(3)\n"
    "v_fma_f32 v16, v16, v176, v180\n"
    "v_fma_f32 v17, v17, v177, v181\n"
    "v_fma_f32 v18, v18, v178, v182\n"
    "v_fma_f32 v19, v19, v179, v183\n"
    "ds_read_b128 v[176:179], v249 offset:80\n"
    "ds_read_b128 v[180:183], v249 offset:176\n"
    "v_cvt_pk_bf16_f32 v12, v12, v13\n"
    "v_cvt_pk_bf16_f32 v13, v14, v15\n"
    "v_cvt_pk_bf16_f32 v14, v16, v17\n"
    "v_cvt_pk_bf16_f32 v15, v18, v19\n"
    "ds_write_b128 v248, v[12:15] offset:6400\n"
    "s_waitcnt lgkmcnt(3)\n"
    "v_fma_f32 v20, v20, v168, v172\n"
    "v_fma_f32 v21, v21, v169, v173\n"
    "v_fma_f32 v22, v22, v170, v174\n"
    "v_fma_f32 v23, v23, v171, v175\n"
    "s_waitcnt lgkmcnt(1)\n"
    "v_fma_f32 v24, v24, v176, v180\n"
    "v_fma_f32 v25, v25, v177, v181\n"
    "v_fma_f32 v26, v26, v178, v182\n"
    "v_fma_f32 v27, v27, v179, v183\n"
    "v_cvt_pk_bf16_f32 v20, v20, v21\n"
    "v_cvt_pk_bf16_f32 v21, v22, v23\n"
    "v_cvt_pk_bf16_f32 v22, v24, v25\n"
    "v_cvt_pk_bf16_f32 v23, v26, v27\n"
    "ds_write_b128 v248, v[20:23] offset:6656\n"
    "s_waitcnt vmcnt(13)\n"
    "v_mov_b32 v249, 154560\n"
    "ds_read_b128 v[168:171], v249 offset:0\n"
    "ds_read_b128 v[172:175], v249 offset:96\n"
    "ds_read_b128 v[176:179], v249 offset:16\n"
    "ds_read_b128 v[180:183], v249 offset:112\n"
    "v_cvt_f32_i32 v28, v28\n"
    "v_cvt_f32_i32 v29, v29\n"
    "v_cvt_f32_i32 v30, v30\n"
    "v_cvt_f32_i32 v31, v31\n"
    "v_cvt_f32_i32 v32, v32\n"
    "v_cvt_f32_i32 v33, v33\n"
    "v_cvt_f32_i32 v34, v34\n"
    "v_cvt_f32_i32 v35, v35\n"
    "s_waitcnt lgkmcnt(2)\n"
    "v_fma_f32 v28, v28, v168, v172\n"
    "v_fma_f32 v29, v29, v169, v173\n"
    "v_fma_f32 v30, v30, v170, v174\n"
    "v_fma_f32 v31, v31, v171, v175\n"
    "ds_read_b128 v[168:171], v249 offset:32\n"
    "ds_read_b128 v[172:175], v249 offset:128\n"
    "v_cvt_f32_i32 v36, v36\n"
    "v_cvt_f32_i32 v37, v37\n"
    "v_cvt_f32_i32 v38, v38\n"
    "v_cvt_f32_i32 v39, v39\n"
    "v_cvt_f32_i32 v40, v40\n"
    "v_cvt_f32_i32 v41, v41\n"
    "v_cvt_f32_i32 v42, v42\n"
    "v_cvt_f32_i32 v43, v43\n"
    "s_waitcnt lgkmcnt(2)\n"
    "v_fma_f32 v32, v32, v176, v180\n"
    "v_fma_f32 v33, v33, v177, v181\n"
    "v_fma_f32 v34, v34, v178, v182\n"
    "v_fma_f32 v35, v35, v179, v183\n"
    "ds_read_b128 v[176:179], v249 offset:48\n"
    "ds_read_b128 v[180:183], v249 offset:144\n"
    "v_cvt_pk_bf16_f32 v28, v28, v29\n"
    "v_cvt_pk_bf16_f32 v29, v30, v31\n"
    "v_cvt_pk_bf16_f32 v30, v32, v33\n"
    "v_cvt_pk_bf16_f32 v31, v34, v35\n"
    "ds_write_b128 v248, v[28:31] offset:6912\n"
    "s_waitcnt lgkmcnt(3)\n"
    "v_fma_f32 v36, v36, v168, v172\n"
    "v_fma_f32 v37, v37, v169, v173\n"
    "v_fma_f32 v38, v38, v170, v174\n"
    "v_fma_f32 v39, v39, v171, v175\n"
    "ds_read_b128 v[168:171], v249 offset:64\n"
    "ds_read_b128 v[172:175], v249 offset:160\n"
    "v_cvt_f32_i32 v44, v44\n"
    "v_cvt_f32_i32 v45, v45\n"
    "v_cvt_f32_i32 v46, v46\n"
    "v_cvt_f32_i32 v47, v47\n"
    "v_cvt_f32_i32 v48, v48\n"
    "v_cvt_f32_i32 v49, v49\n"
    "v_cvt_f32_i32 v50, v50\n"
    "v_cvt_f32_i32 v51, v51\n"
    "s_waitcnt lgkmcnt(3)\n"
    "v_fma_f32 v40, v40, v176, v180\n"
    "v_fma_f32 v41, v41, v177, v181\n"
    "v_fma_f32 v42, v42, v178, v182\n"
    "v_fma_f32 v43, v43, v179, v183\n"
    "ds_read_b128 v[176:179], v249 offset:80\n"
    "ds_read_b128 v[180:183], v249 offset:176\n"
    "v_cvt_pk_bf16_f32 v36, v36, v37\n"
    "v_cvt_pk_bf16_f32 v37, v38, v39\n"
    "v_cvt_pk_bf16_f32 v38, v40, v41\n"
    "v_cvt_pk_bf16_f32 v39, v42, v43\n"
    "ds_write_b128 v248, v[36:39] offset:8192\n"
    "s_waitcnt lgkmcnt(3)\n"
    "v_fma_f32 v44, v44, v168, v172\n"
    "v_fma_f32 v45, v45, v169, v173\n"
    "v_fma_f32 v46, v46, v170, v174\n"
    "v_fma_f32 v47, v47, v171, v175\n"
    "s_waitcnt lgkmcnt(1)\n"
    "v_fma_f32 v48, v48, v176, v180\n"
    "v_fma_f32 v49, v49, v177, v181\n"
    "v_fma_f32 v50, v50, v178, v182\n"
    "v_fma_f32 v51, v51, v179, v183\n"
    "v_cvt_pk_bf16_f32 v44, v44, v45\n"
    "v_cvt_pk_bf16_f32 v45, v46, v47\n"
    "v_cvt_pk_bf16_f32 v46, v48, v49\n"
    "v_cvt_pk_bf16_f32 v47, v50, v51\n"
    "ds_write_b128 v248, v[44:47] offset:8448\n"
    "s_waitcnt vmcnt(7)\n"
    "v_mov_b32 v249, 154752\n"
    "ds_read_b128 v[168:171], v249 offset:0\n"
    "ds_read_b128 v[172:175], v249 offset:96\n"
    "ds_read_b128 v[176:179], v249 offset:16\n"
    "ds_read_b128 v[180:183], v249 offset:112\n"
    "v_cvt_f32_i32 v52, v52\n"
    "v_cvt_f32_i32 v53, v53\n"
    "v_cvt_f32_i32 v54, v54\n"
    "v_cvt_f32_i32 v55, v55\n"
    "v_cvt_f32_i32 v56, v56\n"
    "v_cvt_f32_i32 v57, v57\n"
    "v_cvt_f32_i32 v58, v58\n"
    "v_cvt_f32_i32 v59, v59\n"
    "s_waitcnt lgkmcnt(2)\n"
    "v_fma_f32 v52, v52, v168, v172\n"
    "v_fma_f32 v53, v53, v169, v173\n"
    "v_fma_f32 v54, v54, v170, v174\n"
    "v_fma_f32 v55, v55, v171, v175\n"
    "ds_read_b128 v[168:171], v249 offset:32\n"
    "ds_read_b128 v[172:175], v249 offset:128\n"
    "v_cvt_f32_i32 v60, v60\n"
    "v_cvt_f32_i32 v61, v61\n"
    "v_cvt_f32_i32 v62, v62\n"
    "v_cvt_f32_i32 v63, v63\n"
    "v_cvt_f32_i32 v64, v64\n"
    "v_cvt_f32_i32 v65, v65\n"
    "v_cvt_f32_i32 v66, v66\n"
    "v_cvt_f32_i32 v67, v67\n"
    "s_waitcnt lgkmcnt(2)\n"
    "v_fma_f32 v56, v56, v176, v180\n"
    "v_fma_f32 v57, v57, v177, v181\n"
    "v_fma_f32 v58, v58, v178, v182\n"
    "v_fma_f32 v59, v59, v179, v183\n"
    "ds_read_b128 v[176:179], v249 offset:48\n"
    "ds_read_b128 v[180:183], v249 offset:144\n"
    "v_cvt_pk_bf16_f32 v52, v52, v53\n"
    "v_cvt_pk_bf16_f32 v53, v54, v55\n"
    "v_cvt_pk_bf16_f32 v54, v56, v57\n"
    "v_cvt_pk_bf16_f32 v55, v58, v59\n"
    "ds_write_b128 v248, v[52:55] offset:8704\n"
    "s_waitcnt lgkmcnt(3)\n"
    "v_fma_f32 v60, v60, v168, v172\n"
    "v_fma_f32 v61, v61, v169, v173\n"
    "v_fma_f32 v62, v62, v170, v174\n"
    "v_fma_f32 v63, v63, v171, v175\n"
    "ds_read_b128 v[168:171], v249 offset:64\n"
    "ds_read_b128 v[172:175], v249 offset:160\n"
    "v_cvt_f32_i32 v68, v68\n"
    "v_cvt_f32_i32 v69, v69\n"
    "v_cvt_f32_i32 v70, v70\n"
    "v_cvt_f32_i32 v71, v71\n"
    "v_cvt_f32_i32 v72, v72\n"
    "v_cvt_f32_i32 v73, v73\n"
    "v_cvt_f32_i32 v74, v74\n"
    "v_cvt_f32_i32 v75, v75\n"
    "s_waitcnt lgkmcnt(3)\n"
    "v_fma_f32 v64, v64, v176, v180\n"
    "v_fma_f32 v65, v65, v177, v181\n"
    "v_fma_f32 v66, v66, v178, v182\n"
    "v_fma_f32 v67, v67, v179, v183\n"
    "ds_read_b128 v[176:179], v249 offset:80\n"
    "ds_read_b128 v[180:183], v249 offset:176\n"
    "v_cvt_pk_bf16_f32 v60, v60, v61\n"
    "v_cvt_pk_bf16_f32 v61, v62, v63\n"
    "v_cvt_pk_bf16_f32 v62, v64, v65\n"
    "v_cvt_pk_bf16_f32 v63, v66, v67\n"
    "ds_write_b128 v248, v[60:63] offset:8960\n"
    "s_waitcnt lgkmcnt(3)\n"
    "v_fma_f32 v68, v68, v168, v172\n"
    "v_fma_f32 v69, v69, v169, v173\n"
    "v_fma_f32 v70, v70, v170, v174\n"
    "v_fma_f32 v71, v71, v171, v175\n"
    "s_waitcnt lgkmcnt(1)\n"
    "v_fma_f32 v72, v72, v176, v180\n"
    "v_fma_f32 v73, v73, v177, v181\n"
    "v_fma_f32 v74, v74, v178, v182\n"
    "v_fma_f32 v75, v75, v179, v183\n"
    "v_cvt_pk_bf16_f32 v68, v68, v69\n"
    "v_cvt_pk_bf16_f32 v69, v70, v71\n"
    "v_cvt_pk_bf16_f32 v70, v72, v73\n"
    "v_cvt_pk_bf16_f32 v71, v74, v75\n"
    "ds_write_b128 v248, v[68:71] offset:10240\n"
    "s_waitcnt vmcnt(1)\n"
    "v_mov_b32 v249, 154944\n"
    "ds_read_b128 v[168:171], v249 offset:0\n"
    "ds_read_b128 v[172:175], v249 offset:96\n"
    "ds_read_b128 v[176:179], v249 offset:16\n"
    "ds_read_b128 v[180:183], v249 offset:112\n"
    "v_cvt_f32_i32 v76, v76\n"
    "v_cvt_f32_i32 v77, v77\n"
    "v_cvt_f32_i32 v78, v78\n"
    "v_cvt_f32_i32 v79, v79\n"
    "v_cvt_f32_i32 v80, v80\n"
    "v_cvt_f32_i32 v81, v81\n"
    "v_cvt_f32_i32 v82, v82\n"
    "v_cvt_f32_i32 v83, v83\n"
    "s_waitcnt lgkmcnt(2)\n"
    "v_fma_f32 v76, v76, v168, v172\n"
    "v_fma_f32 v77, v77, v169, v173\n"
    "v_fma_f32 v78, v78, v170, v174\n"
    "v_fma_f32 v79, v79, v171, v175\n"
    "ds_read_b128 v[168:171], v249 offset:32\n"
    "ds_read_b128 v[172:175], v249 offset:128\n"
    "v_cvt_f32_i32 v84, v84\n"
    "v_cvt_f32_i32 v85, v85\n"
    "v_cvt_f32_i32 v86, v86\n"
    "v_cvt_f32_i32 v87, v87\n"
    "v_cvt_f32_i32 v88, v88\n"
    "v_cvt_f32_i32 v89, v89\n"
    "v_cvt_f32_i32 v90, v90\n"
    "v_cvt_f32_i32 v91, v91\n"
    "s_waitcnt lgkmcnt(2)\n"
    "v_fma_f32 v80, v80, v176, v180\n"
    "v_fma_f32 v81, v81, v177, v181\n"
    "v_fma_f32 v82, v82, v178, v182\n"
    "v_fma_f32 v83, v83, v179, v183\n"
    "ds_read_b128 v[176:179], v249 offset:48\n"
    "ds_read_b128 v[180:183], v249 offset:144\n"
    "v_cvt_pk_bf16_f32 v76, v76, v77\n"
    "v_cvt_pk_bf16_f32 v77, v78, v79\n"
    "v_cvt_pk_bf16_f32 v78, v80, v81\n"
    "v_cvt_pk_bf16_f32 v79, v82, v83\n"
    "ds_write_b128 v248, v[76:79] offset:10496\n"
    "s_waitcnt lgkmcnt(3)\n"
    "v_fma_f32 v84, v84, v168, v172\n"
    "v_fma_f32 v85, v85, v169, v173\n"
    "v_fma_f32 v86, v86, v170, v174\n"
    "v_fma_f32 v87, v87, v171, v175\n"
    "ds_read_b128 v[168:171], v249 offset:64\n"
    "ds_read_b128 v[172:175], v249 offset:160\n"
    "v_cvt_f32_i32 v92, v92\n"
    "v_cvt_f32_i32 v93, v93\n"
    "v_cvt_f32_i32 v94, v94\n"
    "v_cvt_f32_i32 v95, v95\n"
    "v_cvt_f32_i32 v96, v96\n"
    "v_cvt_f32_i32 v97, v97\n"
    "v_cvt_f32_i32 v98, v98\n"
    "v_cvt_f32_i32 v99, v99\n"
    "s_waitcnt lgkmcnt(3)\n"
    "v_fma_f32 v88, v88, v176, v180\n"
    "v_fma_f32 v89, v89, v177, v181\n"
    "v_fma_f32 v90, v90, v178, v182\n"
    "v_fma_f32 v91, v91, v179, v183\n"
    "ds_read_b128 v[176:179], v249 offset:80\n"
    "ds_read_b128 v[180:183], v249 offset:176\n"
    "v_cvt_pk_bf16_f32 v84, v84, v85\n"
    "v_cvt_pk_bf16_f32 v85, v86, v87\n"
    "v_cvt_pk_bf16_f32 v86, v88, v89\n"
    "v_cvt_pk_bf16_f32 v87, v90, v91\n"
    "ds_write_b128 v248, v[84:87] offset:10752\n"
    "s_waitcnt lgkmcnt(3)\n"
    "v_fma_f32 v92, v92, v168, v172\n"
    "v_fma_f32 v93, v93, v169, v173\n"
    "v_fma_f32 v94, v94, v170, v174\n"
    "v_fma_f32 v95, v95, v171, v175\n"
    "s_waitcnt lgkmcnt(1)\n"
    "v_fma_f32 v96, v96, v176, v180\n"
    "v_fma_f32 v97, v97, v177, v181\n"
    "v_fma_f32 v98, v98, v178, v182\n"
    "v_fma_f32 v99, v99, v179, v183\n"
    "v_cvt_pk_bf16_f32 v92, v92, v93\n"
    "v_cvt_pk_bf16_f32 v93, v94, v95\n"
    "v_cvt_pk_bf16_f32 v94, v96, v97\n"
    "v_cvt_pk_bf16_f32 v95, v98, v99\n"
    "ds_write_b128 v248, v[92:95] offset:11008\n"
    "s_waitcnt lgkmcnt(0)\n"
    "s_barrier\n"
    "ds_read_b128 a[192:195], v155 offset:0\n"
    "ds_read_b128 a[196:199], v156 offset:0\n"
    "ds_read_b128 a[200:203], v155 offset:128\n"
    "ds_read_b128 a[204:207], v156 offset:128\n"
    "ds_read_b128 a[208:211], v155 offset:256\n"
    "ds_read_b128 a[212:215], v156 offset:256\n"
    "ds_read_b128 a[216:219], v155 offset:6144\n"
    "ds_read_b128 a[220:223], v156 offset:6144\n"
    "ds_read_b128 a[224:227], v155 offset:6272\n"
    "ds_read_b128 a[228:231], v156 offset:6272\n"
    "ds_read_b128 a[232:235], v155 offset:6400\n"
    "ds_read_b128 a[236:239], v156 offset:6400\n"
    "ds_read_b128 a[240:243], v155 offset:12288\n"
    "ds_read_b128 a[244:247], v156 offset:12288\n"
    "ds_read_b128 a[248:251], v155 offset:12416\n"
    "ds_read_b128 a[252:255], v156 offset:12416\n"
    "ds_read_b128 v[120:123], v155 offset:12544\n"
    "ds_read_b128 v[124:127], v156 offset:12544\n"
    "ds_read_b128 v[128:131], v155 offset:18432\n"
    "ds_read_b128 v[132:135], v156 offset:18432\n"
    "ds_read_b128 v[136:139], v155 offset:18560\n"
    "ds_read_b128 v[140:143], v156 offset:18560\n"
    "ds_read_b128 v[144:147], v155 offset:18688\n"
    "ds_read_b128 v[148:151], v156 offset:18688\n"
    "s_waitcnt lgkmcnt(0)\n"
    "s_barrier\n"
    "v_and_b32 v68, 0x1f0, v1\n"
    "v_lshrrev_b32 v68, 1, v68\n"
    "v_add_u32 v68, 128, v68\n"
    "s_mov_b32 s81, 0\n"
    "buffer_load_dwordx2 v[240:241], v68, s[36:39], s81 offen offset:0 sc1\n"
    "s_add_u32 s80, s63, 147456\n"
    "buffer_load_dwordx4 v[36:39], v246, s[28:31], s80 offen offset:0\n"
    "buffer_load_dwordx4 v[40:43], v246, s[28:31], s80 offen offset:16\n"
    "buffer_load_dwordx4 v[44:47], v246, s[28:31], s80 offen offset:32\n"
    "buffer_load_dwordx4 v[48:51], v246, s[28:31], s80 offen offset:48\n"
    "buffer_load_dwordx4 v[52:55], v246, s[28:31], s80 offen offset:64\n"
    "buffer_load_dwordx4 v[56:59], v246, s[28:31], s80 offen offset:80\n"
    "s_add_u32 s80, s63, 245760\n"
    "buffer_load_dwordx4 v[200:203], v246, s[28:31], s80 offen offset:0\n"
    "buffer_load_dwordx4 v[204:207], v246, s[28:31], s80 offen offset:16\n"
    "buffer_load_dwordx4 v[208:211], v246, s[28:31], s80 offen offset:32\n"
    "buffer_load_dwordx4 v[212:215], v246, s[28:31], s80 offen offset:48\n"
    "buffer_load_dwordx4 v[216:219], v246, s[28:31], s80 offen offset:64\n"
    "buffer_load_dwordx4 v[220:223], v246, s[28:31], s80 offen offset:80\n"
    "ds_read_b128 v[152:155], v2 offset:0\n"
    "ds_read_b128 v[156:159], v2 offset:2048\n"
    "ds_read_b128 v[160:163], v2 offset:4096\n"
    "ds_read_b128 v[164:167], v2 offset:6144\n"
    "ds_read_b128 v[168:171], v2 offset:8192\n"
    "ds_read_b128 v[172:175], v2 offset:10240\n"
    "ds_read_b128 v[176:179], v2 offset:1024\n"
    "ds_read_b128 v[180:183], v2 offset:3072\n"
    "ds_read_b128 v[184:187], v2 offset:5120\n"
    "ds_read_b128 v[188:191], v2 offset:7168\n"
    "ds_read_b128 v[192:195], v2 offset:9216\n"
    "ds_read_b128 v[196:199], v2 offset:11264\n"
    "s_waitcnt lgkmcnt(11)\n"
    "v_mfma_f32_16x16x32_bf16 v[4:7], v[152:155], a[192:195], 0\n"
    "s_waitcnt lgkmcnt(10)\n"
    "v_mfma_f32_16x16x32_bf16 v[4:7], v[156:159], a[196:199], v[4:7]\n"
    "s_waitcnt lgkmcnt(9)\n"
    "v_mfma_f32_16x16x32_bf16 v[4:7], v[160:163], a[200:203], v[4:7]\n"
    "s_waitcnt lgkmcnt(8)\n"
    "v_mfma_f32_16x16x32_bf16 v[4:7], v[164:167], a[204:207], v[4:7]\n"
    "s_waitcnt lgkmcnt(7)\n"
    "v_mfma_f32_16x16x32_bf16 v[4:7], v[168:171], a[208:211], v[4:7]\n"
    "s_waitcnt lgkmcnt(6)\n"
    "v_mfma_f32_16x16x32_bf16 v[4:7], v[172:175], a[212:215], v[4:7]\n"
    "v_mfma_f32_16x16x32_bf16 v[8:11], v[172:175], a[236:239], 0\n"
    "v_mfma_f32_16x16x32_bf16 v[8:11], v[168:171], a[232:235], v[8:11]\n"
    "v_mfma_f32_16x16x32_bf16 v[8:11], v[164:167], a[228:231], v[8:11]\n"
    "v_mfma_f32_16x16x32_bf16 v[8:11], v[160:163], a[224:227], v[8:11]\n"
    "v_mfma_f32_16x16x32_bf16 v[8:11], v[156:159], a[220:223], v[8:11]\n"
    "v_mfma_f32_16x16x32_bf16 v[8:11], v[152:155], a[216:219], v[8:11]\n"
    "v_mfma_f32_16x16x32_bf16 v[12:15], v[152:155], a[240:243], 0\n"
    "v_mfma_f32_16x16x32_bf16 v[12:15], v[156:159], a[244:247], v[12:15]\n"
    "v_mfma_f32_16x16x32_bf16 v[12:15], v[160:163], a[248:251], v[12:15]\n"
    "v_mfma_f32_16x16x32_bf16 v[12:15], v[164:167], a[252:255], v[12:15]\n"
    "v_mfma_f32_16x16x32_bf16 v[12:15], v[168:171], v[120:123], v[12:15]\n"
    "v_mfma_f32_16x16x32_bf16 v[12:15], v[172:175], v[124:127], v[12:15]\n"
    "v_mfma_f32_16x16x32_bf16 v[16:19], v[172:175], v[148:151], 0\n"
    "v_mfma_f32_16x16x32_bf16 v[16:19], v[168:171], v[144:147], v[16:19]\n"
    "v_mfma_f32_16x16x32_bf16 v[16:19], v[164:167], v[140:143], v[16:19]\n"
    "v_mfma_f32_16x16x32_bf16 v[16:19], v[160:163], v[136:139], v[16:19]\n"
    "v_mfma_f32_16x16x32_bf16 v[16:19], v[156:159], v[132:135], v[16:19]\n"
    "v_mfma_f32_16x16x32_bf16 v[16:19], v[152:155], v[128:131], v[16:19]\n"
    "s_waitcnt lgkmcnt(5)\n"
    "v_mfma_f32_16x16x32_bf16 v[20:23], v[176:179], a[192:195], 0\n"
    "s_waitcnt lgkmcnt(4)\n"
    "v_mfma_f32_16x16x32_bf16 v[20:23], v[180:183], a[196:199], v[20:23]\n"
    "s_waitcnt lgkmcnt(3)\n"
    "v_mfma_f32_16x16x32_bf16 v[20:23], v[184:187], a[200:203], v[20:23]\n"
    "s_waitcnt lgkmcnt(2)\n"
    "v_mfma_f32_16x16x32_bf16 v[20:23], v[188:191], a[204:207], v[20:23]\n"
    "s_waitcnt lgkmcnt(1)\n"
    "v_mfma_f32_16x16x32_bf16 v[20:23], v[192:195], a[208:211], v[20:23]\n"
    "s_waitcnt lgkmcnt(0)\n"
    "v_mfma_f32_16x16x32_bf16 v[20:23], v[196:199], a[212:215], v[20:23]\n"
    "v_mfma_f32_16x16x32_bf16 v[24:27], v[196:199], a[236:239], 0\n"
    "v_mfma_f32_16x16x32_bf16 v[24:27], v[192:195], a[232:235], v[24:27]\n"
    "v_mfma_f32_16x16x32_bf16 v[24:27], v[188:191], a[228:231], v[24:27]\n"
    "v_mfma_f32_16x16x32_bf16 v[24:27], v[184:187], a[224:227], v[24:27]\n"
    "v_mfma_f32_16x16x32_bf16 v[24:27], v[180:183], a[220:223], v[24:27]\n"
    "v_mfma_f32_16x16x32_bf16 v[24:27], v[176:179], a[216:219], v[24:27]\n"
    "v_mfma_f32_16x16x32_bf16 v[28:31], v[176:179], a[240:243], 0\n"
    "v_mfma_f32_16x16x32_bf16 v[28:31], v[180:183], a[244:247], v[28:31]\n"
    "v_mfma_f32_16x16x32_bf16 v[28:31], v[184:187], a[248:251], v[28:31]\n"
    "v_mfma_f32_16x16x32_bf16 v[28:31], v[188:191], a[252:255], v[28:31]\n"
    "v_mfma_f32_16x16x32_bf16 v[28:31], v[192:195], v[120:123], v[28:31]\n"
    "v_mfma_f32_16x16x32_bf16 v[28:31], v[196:199], v[124:127], v[28:31]\n"
    "v_mfma_f32_16x16x32_bf16 v[32:35], v[196:199], v[148:151], 0\n"
    "v_mfma_f32_16x16x32_bf16 v[32:35], v[192:195], v[144:147], v[32:35]\n"
    "v_mfma_f32_16x16x32_bf16 v[32:35], v[188:191], v[140:143], v[32:35]\n"
    "v_mfma_f32_16x16x32_bf16 v[32:35], v[184:187], v[136:139], v[32:35]\n"
    "v_mfma_f32_16x16x32_bf16 v[32:35], v[180:183], v[132:135], v[32:35]\n"
    "v_mfma_f32_16x16x32_bf16 v[32:35], v[176:179], v[128:131], v[32:35]\n"
    "s_waitcnt vmcnt(12)\n"
    "v_cmp_eq_u64 s[54:55], v[240:241], v[242:243]\n"
    "s_nop 1\n"
    "s_mov_b32 s74, 0x00005555\n"
    "s_mov_b32 s75, 0x00005555\n"
    "s_or_b64 s[54:55], s[54:55], s[74:75]\n"
    "s_cmp_eq_u64 s[54:55], -1\n"
    "s_cbranch_scc0 .LpaK1\n"
    ".LpaK1r:\n"
    "s_add_u32 s72, s65, 24576\n"
    "s_add_u32 s73, s65, 28672\n"
    "s_mov_b32 s81, 256\n"
    "s_add_u32 m0, s68, 0\n"
    "s_nop 0\n"
    "buffer_load_dwordx4 v1, s[16:19], s67 offen offset:0 sc1 lds\n"
    "v_max3_f32 v232, v4, v5, v6\n"
    "v_max3_f32 v232, v232, v7, v20\n"
    "v_max3_f32 v232, v232, v21, v22\n"
    "v_max_f32 v232, v232, v23\n"
    "v_max3_f32 v233, v8, v9, v10\n"
    "v_max3_f32 v233, v233, v11, v24\n"
    "v_max3_f32 v233, v233, v25, v26\n"
    "v_max_f32 v233, v233, v27\n"
    "v_max3_f32 v234, v12, v13, v14\n"
    "v_max3_f32 v234, v234, v15, v28\n"
    "v_max3_f32 v234, v234, v29, v30\n"
    "v_max_f32 v234, v234, v31\n"
    "v_max3_f32 v235, v16, v17, v18\n"
    "buffer_load_dwordx4 v1, s[16:19], s67 offen offset:1024 sc1 lds\n"
    "v_max3_f32 v235, v235, v19, v32\n"
    "v_max3_f32 v235, v235, v33, v34\n"
    "v_max_f32 v235, v235, v35\n"
    "v_mov_b32 v236, v232\n"
    "s_nop 1\n"
    "v_permlane32_swap_b32 v232, v236\n"
    "v_max_f32 v232, v232, v236\n"
    "v_mov_b32 v236, v232\n"
    "s_nop 1\n"
    "v_permlane16_swap_b32 v232, v236\n"
    "v_max_f32 v232, v232, v236\n"
    "v_add_f32 v232, 0x42000000, v232\n"
    "v_sub_f32 v100, 0, v232\n"
    "v_sub_f32 v101, 0, v232\n"
    "v_sub_f32 v102, 0, v232\n"
    "buffer_load_dwordx4 v1, s[16:19], s67 offen offset:2048 sc1 lds\n"
    "v_sub_f32 v103, 0, v232\n"
    "v_mov_b32 v236, v233\n"
    "s_nop 1\n"
    "v_permlane32_swap_b32 v233, v236\n"
    "v_max_f32 v233, v233, v236\n"
    "v_mov_b32 v236, v233\n"
    "s_nop 1\n"
    "v_permlane16_swap_b32 v233, v236\n"
    "v_max_f32 v233, v233, v236\n"
    "v_add_f32 v233, 0x42000000, v233\n"
    "v_sub_f32 v104, 0, v233\n"
    "v_sub_f32 v105, 0, v233\n"
    "v_sub_f32 v106, 0, v233\n"
    "v_sub_f32 v107, 0, v233\n"
    "v_mov_b32 v236, v234\n"
    "buffer_load_dwordx4 v1, s[16:19], s67 offen offset:3072 sc1 lds\n"
    "s_nop 0\n"
    "v_permlane32_swap_b32 v234, v236\n"
    "v_max_f32 v234, v234, v236\n"
    "v_mov_b32 v236, v234\n"
    "s_nop 1\n"
    "v_permlane16_swap_b32 v234, v236\n"
    "v_max_f32 v234, v234, v236\n"
    "v_add_f32 v234, 0x42000000, v234\n"
    "v_sub_f32 v108, 0, v234\n"
    "v_sub_f32 v109, 0, v234\n"
    "v_sub_f32 v110, 0, v234\n"
    "v_sub_f32 v111, 0, v234\n"
    "v_mov_b32 v236, v235\n"
    "s_nop 1\n"
    "v_permlane32_swap_b32 v235, v236\n"
    "v_max_f32 v235, v235, v236\n"
    "s_add_u32 m0, s68, 4096\n"
    "s_add_u32 s83, s67, 4096\n"
    "buffer_load_dwordx4 v1, s[16:19], s83 offen offset:0 sc1 lds\n"
    "v_mov_b32 v236, v235\n"
    "s_nop 1\n"
    "v_permlane16_swap_b32 v235, v236\n"
    "v_max_f32 v235, v235, v236\n"
    "v_add_f32 v235, 0x42000000, v235\n"
    "v_sub_f32 v112, 0, v235\n"
    "v_sub_f32 v113, 0, v235\n"
    "v_sub_f32 v114, 0, v235\n"
    "v_sub_f32 v115, 0, v235\n"
    "v_sub_f32 v4, v4, v232\n"
    "v_sub_f32 v5, v5, v232\n"
    "v_sub_f32 v6, v6, v232\n"
    "v_sub_f32 v7, v7, v232\n"
    "v_sub_f32 v8, v8, v233\n"
    "buffer_load_dwordx4 v1, s[16:19], s83 offen offset:1024 sc1 lds\n"
    "v_sub_f32 v9, v9, v233\n"
    "v_sub_f32 v10, v10, v233\n"
    "v_sub_f32 v11, v11, v233\n"
    "v_sub_f32 v12, v12, v234\n"
    "v_sub_f32 v13, v13, v234\n"
    "v_sub_f32 v14, v14, v234\n"
    "v_sub_f32 v15, v15, v234\n"
    "v_sub_f32 v16, v16, v235\n"
    "v_sub_f32 v17, v17, v235\n"
    "v_sub_f32 v18, v18, v235\n"
    "v_sub_f32 v19, v19, v235\n"
    "v_sub_f32 v20, v20, v232\n"
    "v_sub_f32 v21, v21, v232\n"
    "s_mov_b32 m0, s72\n"
    "s_nop 0\n"
    "buffer_load_dwordx4 v1, s[16:19], s72 offen offset:0 sc1 lds\n"
    "v_sub_f32 v22, v22, v232\n"
    "v_sub_f32 v23, v23, v232\n"
    "v_sub_f32 v24, v24, v233\n"
    "v_sub_f32 v25, v25, v233\n"
    "v_sub_f32 v26, v26, v233\n"
    "v_sub_f32 v27, v27, v233\n"
    "v_sub_f32 v28, v28, v234\n"
    "v_sub_f32 v29, v29, v234\n"
    "v_sub_f32 v30, v30, v234\n"
    "v_sub_f32 v31, v31, v234\n"
    "v_sub_f32 v32, v32, v235\n"
    "v_sub_f32 v33, v33, v235\n"
    "v_sub_f32 v34, v34, v235\n"
    "buffer_load_dwordx4 v1, s[16:19], s72 offen offset:1024 sc1 lds\n"
    "v_sub_f32 v35, v35, v235\n"
    "v_exp_f32 v4, v4\n"
    "v_exp_f32 v5, v5\n"
    "v_exp_f32 v6, v6\n"
    "v_exp_f32 v7, v7\n"
    "v_exp_f32 v8, v8\n"
    "v_exp_f32 v9, v9\n"
    "v_exp_f32 v10, v10\n"
    "v_exp_f32 v11, v11\n"
    "v_exp_f32 v12, v12\n"
    "v_exp_f32 v13, v13\n"
    "v_exp_f32 v14, v14\n"
    "v_exp_f32 v15, v15\n"
    "buffer_load_dwordx4 v1, s[16:19], s72 offen offset:2048 sc1 lds\n"
    "v_exp_f32 v16, v16\n"
    "v_exp_f32 v17, v17\n"
    "v_exp_f32 v18, v18\n"
    "v_exp_f32 v19, v19\n"
    "v_exp_f32 v20, v20\n"
    "v_exp_f32 v21, v21\n"
    "v_exp_f32 v22, v22\n"
    "v_exp_f32 v23, v23\n"
    "v_exp_f32 v24, v24\n"
    "v_exp_f32 v25, v25\n"
    "v_exp_f32 v26, v26\n"
    "v_exp_f32 v27, v27\n"
    "v_exp_f32 v28, v28\n"
    "buffer_load_dwordx4 v1, s[16:19], s72 offen offset:3072 sc1 lds\n"
    "v_exp_f32 v29, v29\n"
    "v_exp_f32 v30, v30\n"
    "v_exp_f32 v31, v31\n"
    "v_exp_f32 v32, v32\n"
    "v_exp_f32 v33, v33\n"
    "v_exp_f32 v34, v34\n"
    "v_exp_f32 v35, v35\n"
    "v_mov_b32 v116, v4\n"
    "v_add_f32 v116, v116, v5\n"
    "v_add_f32 v116, v116, v6\n"
    "v_add_f32 v116, v116, v7\n"
    "v_mov_b32 v117, v8\n"
    "v_add_f32 v117, v117, v9\n"
    "s_mov_b32 m0, s73\n"
    "s_nop 0\n"
    "buffer_load_dwordx4 v1, s[16:19], s73 offen offset:0 sc1 lds\n"
    "v_add_f32 v117, v117, v10\n"
    "v_add_f32 v117, v117, v11\n"
    "v_mov_b32 v118, v12\n"
    "v_add_f32 v118, v118, v13\n"
    "v_add_f32 v118, v118, v14\n"
    "v_add_f32 v118, v118, v15\n"
    "v_mov_b32 v119, v16\n"
    "v_add_f32 v119, v119, v17\n"
    "v_add_f32 v119, v119, v18\n"
    "v_add_f32 v119, v119, v19\n"
    "v_add_f32 v116, v116, v20\n"
    "v_add_f32 v116, v116, v21\n"
    "v_add_f32 v116, v116, v22\n"
    "buffer_load_dwordx4 v1, s[16:19], s73 offen offset:1024 sc1 lds\n"
    "v_add_f32 v116, v116, v23\n"
    "v_add_f32 v117, v117, v24\n"
    "v_add_f32 v117, v117, v25\n"
    "v_add_f32 v117, v117, v26\n"
    "v_add_f32 v117, v117, v27\n"
    "v_add_f32 v118, v118, v28\n"
    "v_add_f32 v118, v118, v29\n"
    "v_add_f32 v118, v118, v30\n"
    "v_add_f32 v118, v118, v31\n"
    "v_add_f32 v119, v119, v32\n"
    "v_add_f32 v119, v119, v33\n"
    "v_add_f32 v119, v119, v34\n"
    "v_add_f32 v119, v119, v35\n"
    "s_mov_b32 s79, 256\n"
    "s_waitcnt vmcnt(18)\n"
    "s_mov_b32 s71, 147456\n"
    "v_mov_b32 v249, 151488\n"
    "s_add_u32 s76, s71, s60\n"
    "s_add_u32 s77, s71, s61\n"
    "s_add_u32 s78, s71, s62\n"
    "ds_read_b128 v[232:235], v249 offset:0\n"
    "ds_read_b128 v[236:239], v249 offset:96\n"
    "v_cvt_f32_i32 v36, v36\n"
    "v_cvt_f32_i32 v37, v37\n"
    "v_cvt_f32_i32 v38, v38\n"
    "v_cvt_f32_i32 v39, v39\n"
    "v_cvt_f32_i32 v40, v40\n"
    "v_cvt_f32_i32 v41, v41\n"
    "v_cvt_f32_i32 v42, v42\n"
    "v_cvt_f32_i32 v43, v43\n"
    "s_waitcnt lgkmcnt(0)\n"
    "v_fma_f32 v36, v36, v232, v236\n"
    "v_fma_f32 v37, v37, v233, v237\n"
    "v_fma_f32 v38, v38, v234, v238\n"
    "v_fma_f32 v39, v39, v235, v239\n"
    "ds_read_b128 v[232:235], v249 offset:16\n"
    "ds_read_b128 v[236:239], v249 offset:112\n"
    "v_cvt_f32_i32 v44, v44\n"
    "v_cvt_f32_i32 v45, v45\n"
    "v_cvt_f32_i32 v46, v46\n"
    "v_cvt_f32_i32 v47, v47\n"
    "v_cvt_f32_i32 v48, v48\n"
    "v_cvt_f32_i32 v49, v49\n"
    "v_cvt_f32_i32 v50, v50\n"
    "v_cvt_f32_i32 v51, v51\n"
    "s_waitcnt lgkmcnt(0)\n"
    "v_fma_f32 v40, v40, v232, v236\n"
    "v_fma_f32 v41, v41, v233, v237\n"
    "v_fma_f32 v42, v42, v234, v238\n"
    "v_fma_f32 v43, v43, v235, v239\n"
    "ds_read_b128 v[232:235], v249 offset:32\n"
    "ds_read_b128 v[236:239], v249 offset:128\n"
    "v_cvt_pk_bf16_f32 v36, v36, v37\n"
    "v_cvt_pk_bf16_f32 v37, v38, v39\n"
    "v_cvt_pk_bf16_f32 v38, v40, v41\n"
    "v_cvt_pk_bf16_f32 v39, v42, v43\n"
    "buffer_store_dwordx4 v[36:39], v248, s[16:19], s76 offen offset:0 sc1\n"
    "s_waitcnt lgkmcnt(0)\n"
    "v_fma_f32 v44, v44, v232, v236\n"
    "v_fma_f32 v45, v45, v233, v237\n"
    "v_fma_f32 v46, v46, v234, v238\n"
    "v_fma_f32 v47, v47, v235, v239\n"
    "ds_read_b128 v[232:235], v249 offset:48\n"
    "ds_read_b128 v[236:239], v249 offset:144\n"
    "v_cvt_f32_i32 v52, v52\n"
    "v_cvt_f32_i32 v53, v53\n"
    "v_cvt_f32_i32 v54, v54\n"
    "v_cvt_f32_i32 v55, v55\n"
    "v_cvt_f32_i32 v56, v56\n"
    "v_cvt_f32_i32 v57, v57\n"
    "v_cvt_f32_i32 v58, v58\n"
    "v_cvt_f32_i32 v59, v59\n"
    "s_waitcnt lgkmcnt(0)\n"
    "v_fma_f32 v48, v48, v232, v236\n"
    "v_fma_f32 v49, v49, v233, v237\n"
    "v_fma_f32 v50, v50, v234, v238\n"
    "v_fma_f32 v51, v51, v235, v239\n"
    "ds_read_b128 v[232:235], v249 offset:64\n"
    "ds_read_b128 v[236:239], v249 offset:160\n"
    "v_cvt_pk_bf16_f32 v44, v44, v45\n"
    "v_cvt_pk_bf16_f32 v45, v46, v47\n"
    "v_cvt_pk_bf16_f32 v46, v48, v49\n"
    "v_cvt_pk_bf16_f32 v47, v50, v51\n"
    "buffer_store_dwordx4 v[44:47], v248, s[16:19], s77 offen offset:0 sc1\n"
    "s_waitcnt lgkmcnt(0)\n"
    "v_fma_f32 v52, v52, v232, v236\n"
    "v_fma_f32 v53, v53, v233, v237\n"
    "v_fma_f32 v54, v54, v234, v238\n"
    "v_fma_f32 v55, v55, v235, v239\n"
    "ds_read_b128 v[232:235], v249 offset:80\n"
    "ds_read_b128 v[236:239], v249 offset:176\n"
    "s_waitcnt lgkmcnt(0)\n"
    "v_fma_f32 v56, v56, v232, v236\n"
    "v_fma_f32 v57, v57, v233, v237\n"
    "v_fma_f32 v58, v58, v234, v238\n"
    "v_fma_f32 v59, v59, v235, v239\n"
    "v_cvt_pk_bf16_f32 v52, v52, v53\n"
    "v_cvt_pk_bf16_f32 v53, v54, v55\n"
    "v_cvt_pk_bf16_f32 v54, v56, v57\n"
    "v_cvt_pk_bf16_f32 v55, v58, v59\n"
    "buffer_store_dwordx4 v[52:55], v248, s[16:19], s78 offen offset:0 sc1\n"
    "s_mov_b32 s71, 245760\n"
    "v_mov_b32 v249, 151680\n"
    "buffer_load_dwordx2 v[240:241], v245, s[36:39], s81 offen offset:0 sc1\n"
    "s_add_u32 m0, s68, 24576\n"
    "s_add_u32 s83, s67, 24576\n"
    "buffer_load_dwordx4 v1, s[16:19], s83 offen offset:0 sc1 lds\n"
    "buffer_load_dwordx4 v1, s[16:19], s83 offen offset:1024 sc1 lds\n"
    "buffer_load_dwordx4 v1, s[16:19], s83 offen offset:2048 sc1 lds\n"
    "buffer_load_dwordx4 v1, s[16:19], s83 offen offset:3072 sc1 lds\n"
    "s_add_u32 m0, s68, 28672\n"
    "s_add_u32 s83, s67, 28672\n"
    "buffer_load_dwordx4 v1, s[16:19], s83 offen offset:0 sc1 lds\n"
    "buffer_load_dwordx4 v1, s[16:19], s83 offen offset:1024 sc1 lds\n"
    "s_add_u32 s67, s67, 49152\n"
    "s_waitcnt vmcnt(6)\n"
    "s_mov_b32 s80, 384\n"
    "buffer_store_dwordx2 v[242:243], v244, s[36:39], s80 offen offset:0 sc1\n"
    "ds_add_u32 v252, v251\n"
    "ds_read_b128 v[152:155], v2 offset:12288\n"
    "ds_read_b128 v[156:159], v2 offset:14336\n"
    "ds_read_b128 v[160:163], v2 offset:16384\n"
    "ds_read_b128 v[164:167], v2 offset:18432\n"
    "ds_read_b128 v[168:171], v2 offset:20480\n"
    "ds_read_b128 v[172:175], v2 offset:22528\n"
    "s_waitcnt lgkmcnt(0)\n"
    "s_nop 7\n"
    "s_branch .LNK\n"
    ".Lp1_3:\n"
    "s_load_dwordx2 s[28:29], %1, 16\n"
    "s_load_dwordx2 s[40:41], %1, 40\n"
    "s_load_dwordx2 s[44:45], %1, 48\n"
    "s_mov_b32 s30, 1572864\n"
    "s_mov_b32 s31, 0x20000\n"
    "s_mov_b32 s42, 4096\n"
    "s_mov_b32 s43, 0x20000\n"
    "s_mov_b32 s46, 4096\n"
    "s_mov_b32 s47, 0x20000\n"
    "s_mov_b32 s38, 4224\n"
    "s_mov_b32 s39, 0x20000\n"
    "s_mov_b32 s22, 98304\n"
    "s_mov_b32 s23, 0x20000\n"
    "s_mul_i32 s72, s52, 1572864\n"
    "s_mul_i32 s73, s52, 4096\n"
    "s_mul_i32 s74, s52, 8192\n"
    "s_mul_i32 s75, s52, 786432\n"
    "s_mul_i32 s76, s53, 98304\n"
    "s_add_u32 s75, s75, s76\n"
    "s_mul_i32 s63, s53, 6144\n"
    "s_mul_i32 s77, s53, 16\n"
    "v_lshrrev_b32 v232, 2, v161\n"
    "v_and_b32 v233, 3, v161\n"
    "v_lshlrev_b32 v157, 8, v232\n"
    "v_lshlrev_b32 v234, 2, v233\n"
    "v_add3_u32 v157, v157, v234, s77\n"
    "v_add_u32 v157, 128, v157\n"
    "v_add_u32 v159, s77, v234\n"
    "v_mul_u32_u24 v246, 12, v161\n"
    "v_add_u32 v247, 3072, v246\n"
    "s_waitcnt lgkmcnt(0)\n"
    "s_mov_b64 s[16:17], s[36:37]\n"
    "s_add_u32 s40, s40, s73\n"
    "s_addc_u32 s41, s41, 0\n"
    "s_add_u32 s44, s44, s73\n"
    "s_addc_u32 s45, s45, 0\n"
    "s_add_u32 s36, s36, s74\n"
    "s_addc_u32 s37, s37, 0\n"
    "s_add_u32 s28, s28, s72\n"
    "s_addc_u32 s29, s29, 0\n"
    "s_add_u32 s20, s20, s75\n"
    "s_addc_u32 s21, s21, 0\n"
    "s_add_u32 s24, s24, s75\n"
    "s_addc_u32 s25, s25, 0\n"
    "buffer_load_dword v124, v159, s[40:43], 0 offen offset:0\n"
    "buffer_load_dword v132, v159, s[44:47], 0 offen offset:0\n"
    "buffer_load_dword v120, v157, s[40:43], 0 offen offset:0\n"
    "buffer_load_dword v128, v157, s[44:47], 0 offen offset:0\n"
    "v_mov_b32 v238, 0\n"
    "buffer_load_dwordx2 v[242:243], v238, s[36:39], 0 offen offset:0 sc1\n"
    "s_add_u32 s80, s63, 0\n"
    "buffer_load_dwordx3 v[4:6], v246, s[28:31], s80 offen offset:0 nt\n"
    "buffer_load_dwordx3 v[8:10], v246, s[28:31], s80 offen offset:768 nt\n"
    "buffer_load_dwordx3 v[12:14], v246, s[28:31], s80 offen offset:1536 nt\n"
    "buffer_load_dwordx3 v[16:18], v246, s[28:31], s80 offen offset:2304 nt\n"
    "buffer_load_dwordx3 v[20:22], v247, s[28:31], s80 offen offset:0 nt\n"
    "buffer_load_dwordx3 v[24:26], v247, s[28:31], s80 offen offset:768 nt\n"
    "buffer_load_dwordx3 v[28:30], v247, s[28:31], s80 offen offset:1536 nt\n"
    "buffer_load_dwordx3 v[32:34], v247, s[28:31], s80 offen offset:2304 nt\n"
    "s_add_u32 s80, s63, 49152\n"
    "buffer_load_dwordx3 v[200:202], v246, s[28:31], s80 offen offset:0 nt\n"
    "buffer_load_dwordx3 v[204:206], v246, s[28:31], s80 offen offset:768 nt\n"
    "buffer_load_dwordx3 v[208:210], v246, s[28:31], s80 offen offset:1536 nt\n"
    "buffer_load_dwordx3 v[212:214], v246, s[28:31], s80 offen offset:2304 nt\n"
    "buffer_load_dwordx3 v[216:218], v247, s[28:31], s80 offen offset:0 nt\n"
    "buffer_load_dwordx3 v[220:222], v247, s[28:31], s80 offen offset:768 nt\n"
    "buffer_load_dwordx3 v[224:226], v247, s[28:31], s80 offen offset:1536 nt\n"
    "buffer_load_dwordx3 v[228:230], v247, s[28:31], s80 offen offset:2304 nt\n"
    "s_mul_i32 s72, s52, 1576960\n"
    "s_add_u32 s72, s72, 262144\n"
    "s_add_u32 s16, s16, s72\n"
    "s_addc_u32 s17, s17, 0\n"
    "s_mov_b32 s18, 1572864\n"
    "s_mov_b32 s19, 0x20000\n"
    "s_mov_b32 s26, 98304\n"
    "s_mov_b32 s27, 0x20000\n"
    "s_mov_b32 s65, 18432\n"
    "s_mov_b32 s66, 0\n"
    "s_mov_b32 s67, 67584\n"
    "s_mov_b32 s68, 67584\n"
    "s_mov_b32 s69, 165888\n"
    "s_mov_b32 s58, 0\n"
    "s_mov_b32 s59, 4\n"
    "s_lshr_b32 s73, s53, 2\n"
    "s_mul_i32 s73, s73, 12288\n"
    "s_and_b32 s74, s53, 3\n"
    "s_lshl_b32 s74, s74, 8\n"
    "s_add_u32 s73, s73, s74\n"
    "s_add_u32 s60, s73, 24576\n"
    "s_add_u32 s61, s73, 28672\n"
    "s_add_u32 s62, s73, 32768\n"
    "s_mov_b32 s82, 0x7fff0000\n"
    "v_lshrrev_b32 v232, 2, v161\n"
    "v_and_b32 v233, 3, v161\n"
    "v_lshlrev_b32 v158, 6, v232\n"
    "v_lshlrev_b32 v234, 3, v233\n"
    "v_add_u32 v158, v158, v234\n"
    "v_add_u32 v158, 156608, v158\n"
    "v_add_u32 v160, 157632, v234\n"
    "v_and_b32 v234, 48, v161\n"
    "v_lshlrev_b32 v248, 6, v234\n"
    "v_and_b32 v234, 0xf0, v1\n"
    "v_add_u32 v248, v248, v234\n"
    "v_and_b32 v232, 15, v161\n"
    "v_lshrrev_b32 v233, 4, v161\n"
    "v_and_b32 v234, 7, v161\n"
    "v_xor_b32 v233, v233, v234\n"
    "v_mul_u32_u24 v155, 384, v232\n"
    "v_lshl_add_u32 v155, v233, 4, v155\n"
    "v_add_u32 v155, 122880, v155\n"
    "v_xor_b32 v156, 64, v155\n"
    "v_mov_b32 v2, v1\n"
    "v_add_u32 v3, 49152, v1\n"
    "v_and_b32 v245, 0xf0, v1\n"
    "v_lshrrev_b32 v245, 1, v245\n"
    "v_add_u32 v245, 128, v245\n"
    "s_lshl_b32 s73, s53, 4\n"
    "s_add_u32 s73, s73, 136\n"
    "v_mov_b32 v244, 0x7fff0000\n"
    "v_mov_b32 v239, s73\n"
    "v_cmp_eq_u32 vcc, 0, v1\n"
    "s_nop 1\n"
    "v_cndmask_b32 v244, v244, v239, vcc\n"
    "v_lshlrev_b32 v252, 2, v161\n"
    "v_add_u32 v252, 157952, v252\n"
    "v_mov_b32 v251, 1\n"
    "s_waitcnt vmcnt(16)\n"
    "v_add_co_u32 v242, vcc, 0x7f4a7c15, v242\n"
    "v_mov_b32 v238, 0x9e3779b9\n"
    "s_nop 0\n"
    "v_addc_co_u32 v243, vcc, v238, v243, vcc\n"
    "s_mov_b32 s73, 0x3dd53b95\n"
    "v_lshlrev_b32 v136, 16, v124\n"
    "v_lshlrev_b32 v144, 16, v132\n"
    "v_and_b32 v137, 0xffff0000, v124\n"
    "v_and_b32 v145, 0xffff0000, v132\n"
    "v_mul_f32 v144, -v144, v136\n"
    "v_mul_f32 v145, -v145, v137\n"
    "ds_write_b64 v160, v[136:137] offset:0\n"
    "ds_write_b64 v160, v[144:145] offset:32\n"
    "v_lshlrev_b32 v136, 16, v120\n"
    "v_lshlrev_b32 v144, 16, v128\n"
    "v_and_b32 v137, 0xffff0000, v120\n"
    "v_and_b32 v145, 0xffff0000, v128\n"
    "v_mul_f32 v144, -v144, v136\n"
    "v_mul_f32 v145, -v145, v137\n"
    "ds_write_b64 v158, v[136:137] offset:0\n"
    "ds_write_b64 v158, v[144:145] offset:32\n"
    "s_waitcnt vmcnt(8)\n"
    "s_mov_b32 s71, 0\n"
    "v_mov_b32 v249, 157632\n"
    "s_add_u32 s76, s71, s60\n"
    "s_add_u32 s77, s71, s61\n"
    "s_add_u32 s78, s71, s62\n"
    "ds_read_b128 v[168:171], v249 offset:0\n"
    "ds_read_b128 v[172:175], v249 offset:32\n"
    "ds_read_b128 v[176:179], v249 offset:16\n"
    "ds_read_b128 v[180:183], v249 offset:48\n"
    "v_cvt_f32_i32 v4, v4\n"
    "v_cvt_f32_i32 v5, v5\n"
    "v_cvt_f32_i32 v6, v6\n"
    "v_cvt_f32_i32 v8, v8\n"
    "v_cvt_f32_i32 v9, v9\n"
    "v_cvt_f32_i32 v10, v10\n"
    "v_cvt_f32_i32 v12, v12\n"
    "v_cvt_f32_i32 v13, v13\n"
    "v_cvt_f32_i32 v14, v14\n"
    "v_cvt_f32_i32 v16, v16\n"
    "v_cvt_f32_i32 v17, v17\n"
    "v_cvt_f32_i32 v18, v18\n"
    "s_waitcnt lgkmcnt(2)\n"
    "v_fma_f32 v4, v4, v168, v172\n"
    "v_fma_f32 v5, v5, v168, v172\n"
    "v_fma_f32 v6, v6, v168, v172\n"
    "v_fma_f32 v8, v8, v169, v173\n"
    "v_fma_f32 v9, v9, v169, v173\n"
    "v_fma_f32 v10, v10, v169, v173\n"
    "v_fma_f32 v12, v12, v170, v174\n"
    "v_fma_f32 v13, v13, v170, v174\n"
    "v_fma_f32 v14, v14, v170, v174\n"
    "v_fma_f32 v16, v16, v171, v175\n"
    "v_fma_f32 v17, v17, v171, v175\n"
    "v_fma_f32 v18, v18, v171, v175\n"
    "v_cvt_f32_i32 v20, v20\n"
    "v_cvt_f32_i32 v21, v21\n"
    "v_cvt_f32_i32 v22, v22\n"
    "v_cvt_f32_i32 v24, v24\n"
    "v_cvt_f32_i32 v25, v25\n"
    "v_cvt_f32_i32 v26, v26\n"
    "v_cvt_f32_i32 v28, v28\n"
    "v_cvt_f32_i32 v29, v29\n"
    "v_cvt_f32_i32 v30, v30\n"
    "v_cvt_f32_i32 v32, v32\n"
    "v_cvt_f32_i32 v33, v33\n"
    "v_cvt_f32_i32 v34, v34\n"
    "s_waitcnt lgkmcnt(0)\n"
    "v_fma_f32 v20, v20, v176, v180\n"
    "v_fma_f32 v21, v21, v176, v180\n"
    "v_fma_f32 v22, v22, v176, v180\n"
    "v_fma_f32 v24, v24, v177, v181\n"
    "v_fma_f32 v25, v25, v177, v181\n"
    "v_fma_f32 v26, v26, v177, v181\n"
    "v_fma_f32 v28, v28, v178, v182\n"
    "v_fma_f32 v29, v29, v178, v182\n"
    "v_fma_f32 v30, v30, v178, v182\n"
    "v_fma_f32 v32, v32, v179, v183\n"
    "v_fma_f32 v33, v33, v179, v183\n"
    "v_fma_f32 v34, v34, v179, v183\n"
    "v_cvt_pk_bf16_f32 v4, v4, v8\n"
    "v_cvt_pk_bf16_f32 v8, v5, v9\n"
    "v_cvt_pk_bf16_f32 v168, v6, v10\n"
    "v_cvt_pk_bf16_f32 v5, v12, v16\n"
    "v_cvt_pk_bf16_f32 v9, v13, v17\n"
    "v_cvt_pk_bf16_f32 v169, v14, v18\n"
    "v_cvt_pk_bf16_f32 v6, v20, v24\n"
    "v_cvt_pk_bf16_f32 v10, v21, v25\n"
    "v_cvt_pk_bf16_f32 v170, v22, v26\n"
    "v_cvt_pk_bf16_f32 v7, v28, v32\n"
    "v_cvt_pk_bf16_f32 v11, v29, v33\n"
    "v_cvt_pk_bf16_f32 v171, v30, v34\n"
    "buffer_store_dwordx4 v[4:7], v248, s[16:19], s76 offen offset:0 sc1\n"
    "buffer_store_dwordx4 v[8:11], v248, s[16:19], s77 offen offset:0 sc1\n"
    "buffer_store_dwordx4 v[168:171], v248, s[16:19], s78 offen offset:0 sc1\n"
    "s_waitcnt vmcnt(3)\n"
    "s_mov_b32 s71, 49152\n"
    "v_mov_b32 v249, 156608\n"
    "s_add_u32 s76, s71, s60\n"
    "s_add_u32 s77, s71, s61\n"
    "s_add_u32 s78, s71, s62\n"
    "ds_read_b128 v[168:171], v249 offset:0\n"
    "ds_read_b128 v[172:175], v249 offset:32\n"
    "ds_read_b128 v[176:179], v249 offset:16\n"
    "ds_read_b128 v[180:183], v249 offset:48\n"
    "v_cvt_f32_i32 v200, v200\n"
    "v_cvt_f32_i32 v201, v201\n"
    "v_cvt_f32_i32 v202, v202\n"
    "v_cvt_f32_i32 v204, v204\n"
    "v_cvt_f32_i32 v205, v205\n"
    "v_cvt_f32_i32 v206, v206\n"
    "v_cvt_f32_i32 v208, v208\n"
    "v_cvt_f32_i32 v209, v209\n"
    "v_cvt_f32_i32 v210, v210\n"
    "v_cvt_f32_i32 v212, v212\n"
    "v_cvt_f32_i32 v213, v213\n"
    "v_cvt_f32_i32 v214, v214\n"
    "s_waitcnt lgkmcnt(2)\n"
    "v_fma_f32 v200, v200, v168, v172\n"
    "v_fma_f32 v201, v201, v168, v172\n"
    "v_fma_f32 v202, v202, v168, v172\n"
    "v_fma_f32 v204, v204, v169, v173\n"
    "v_fma_f32 v205, v205, v169, v173\n"
    "v_fma_f32 v206, v206, v169, v173\n"
    "v_fma_f32 v208, v208, v170, v174\n"
    "v_fma_f32 v209, v209, v170, v174\n"
    "v_fma_f32 v210, v210, v170, v174\n"
    "v_fma_f32 v212, v212, v171, v175\n"
    "v_fma_f32 v213, v213, v171, v175\n"
    "v_fma_f32 v214, v214, v171, v175\n"
    "v_cvt_f32_i32 v216, v216\n"
    "v_cvt_f32_i32 v217, v217\n"
    "v_cvt_f32_i32 v218, v218\n"
    "v_cvt_f32_i32 v220, v220\n"
    "v_cvt_f32_i32 v221, v221\n"
    "v_cvt_f32_i32 v222, v222\n"
    "v_cvt_f32_i32 v224, v224\n"
    "v_cvt_f32_i32 v225, v225\n"
    "v_cvt_f32_i32 v226, v226\n"
    "v_cvt_f32_i32 v228, v228\n"
    "v_cvt_f32_i32 v229, v229\n"
    "v_cvt_f32_i32 v230, v230\n"
    "s_waitcnt lgkmcnt(0)\n"
    "v_fma_f32 v216, v216, v176, v180\n"
    "v_fma_f32 v217, v217, v176, v180\n"
    "v_fma_f32 v218, v218, v176, v180\n"
    "v_fma_f32 v220, v220, v177, v181\n"
    "v_fma_f32 v221, v221, v177, v181\n"
    "v_fma_f32 v222, v222, v177, v181\n"
    "v_fma_f32 v224, v224, v178, v182\n"
    "v_fma_f32 v225, v225, v178, v182\n"
    "v_fma_f32 v226, v226, v178, v182\n"
    "v_fma_f32 v228, v228, v179, v183\n"
    "v_fma_f32 v229, v229, v179, v183\n"
    "v_fma_f32 v230, v230, v179, v183\n"
    "v_cvt_pk_bf16_f32 v200, v200, v204\n"
    "v_cvt_pk_bf16_f32 v204, v201, v205\n"
    "v_cvt_pk_bf16_f32 v168, v202, v206\n"
    "v_cvt_pk_bf16_f32 v201, v208, v212\n"
    "v_cvt_pk_bf16_f32 v205, v209, v213\n"
    "v_cvt_pk_bf16_f32 v169, v210, v214\n"
    "v_cvt_pk_bf16_f32 v202, v216, v220\n"
    "v_cvt_pk_bf16_f32 v206, v217, v221\n"
    "v_cvt_pk_bf16_f32 v170, v218, v222\n"
    "v_cvt_pk_bf16_f32 v203, v224, v228\n"
    "v_cvt_pk_bf16_f32 v207, v225, v229\n"
    "v_cvt_pk_bf16_f32 v171, v226, v230\n"
    "buffer_store_dwordx4 v[200:203], v248, s[16:19], s76 offen offset:0 sc1\n"
    "buffer_store_dwordx4 v[204:207], v248, s[16:19], s77 offen offset:0 sc1\n"
    "buffer_store_dwordx4 v[168:171], v248, s[16:19], s78 offen offset:0 sc1\n"
    "v_accvgpr_write_b32 a0, 0\n"
    "v_accvgpr_write_b32 a1, 0\n"
    "v_accvgpr_write_b32 a2, 0\n"
    "v_accvgpr_write_b32 a3, 0\n"
    "v_accvgpr_write_b32 a4, 0\n"
    "v_accvgpr_write_b32 a5, 0\n"
    "v_accvgpr_write_b32 a6, 0\n"
    "v_accvgpr_write_b32 a7, 0\n"
    "v_accvgpr_write_b32 a8, 0\n"
    "v_accvgpr_write_b32 a9, 0\n"
    "v_accvgpr_write_b32 a10, 0\n"
    "v_accvgpr_write_b32 a11, 0\n"
    "v_accvgpr_write_b32 a12, 0\n"
    "v_accvgpr_write_b32 a13, 0\n"
    "v_accvgpr_write_b32 a14, 0\n"
    "v_accvgpr_write_b32 a15, 0\n"
    "v_accvgpr_write_b32 a16, 0\n"
    "v_accvgpr_write_b32 a17, 0\n"
    "v_accvgpr_write_b32 a18, 0\n"
    "v_accvgpr_write_b32 a19, 0\n"
    "v_accvgpr_write_b32 a20, 0\n"
    "v_accvgpr_write_b32 a21, 0\n"
    "v_accvgpr_write_b32 a22, 0\n"
    "v_accvgpr_write_b32 a23, 0\n"
    "v_accvgpr_write_b32 a24, 0\n"
    "v_accvgpr_write_b32 a25, 0\n"
    "v_accvgpr_write_b32 a26, 0\n"
    "v_accvgpr_write_b32 a27, 0\n"
    "v_accvgpr_write_b32 a28, 0\n"
    "v_accvgpr_write_b32 a29, 0\n"
    "v_accvgpr_write_b32 a30, 0\n"
    "v_accvgpr_write_b32 a31, 0\n"
    "v_accvgpr_write_b32 a32, 0\n"
    "v_accvgpr_write_b32 a33, 0\n"
    "v_accvgpr_write_b32 a34, 0\n"
    "v_accvgpr_write_b32 a35, 0\n"
    "v_accvgpr_write_b32 a36, 0\n"
    "v_accvgpr_write_b32 a37, 0\n"
    "v_accvgpr_write_b32 a38, 0\n"
    "v_accvgpr_write_b32 a39, 0\n"
    "v_accvgpr_write_b32 a40, 0\n"
    "v_accvgpr_write_b32 a41, 0\n"
    "v_accvgpr_write_b32 a42, 0\n"
    "v_accvgpr_write_b32 a43, 0\n"
    "v_accvgpr_write_b32 a44, 0\n"
    "v_accvgpr_write_b32 a45, 0\n"
    "v_accvgpr_write_b32 a46, 0\n"
    "v_accvgpr_write_b32 a47, 0\n"
    "v_accvgpr_write_b32 a48, 0\n"
    "v_accvgpr_write_b32 a49, 0\n"
    "v_accvgpr_write_b32 a50, 0\n"
    "v_accvgpr_write_b32 a51, 0\n"
    "v_accvgpr_write_b32 a52, 0\n"
    "v_accvgpr_write_b32 a53, 0\n"
    "v_accvgpr_write_b32 a54, 0\n"
    "v_accvgpr_write_b32 a55, 0\n"
    "v_accvgpr_write_b32 a56, 0\n"
    "v_accvgpr_write_b32 a57, 0\n"
    "v_accvgpr_write_b32 a58, 0\n"
    "v_accvgpr_write_b32 a59, 0\n"
    "v_accvgpr_write_b32 a60, 0\n"
    "v_accvgpr_write_b32 a61, 0\n"
    "v_accvgpr_write_b32 a62, 0\n"
    "v_accvgpr_write_b32 a63, 0\n"
    "v_accvgpr_write_b32 a64, 0\n"
    "v_accvgpr_write_b32 a65, 0\n"
    "v_accvgpr_write_b32 a66, 0\n"
    "v_accvgpr_write_b32 a67, 0\n"
    "v_accvgpr_write_b32 a68, 0\n"
    "v_accvgpr_write_b32 a69, 0\n"
    "v_accvgpr_write_b32 a70, 0\n"
    "v_accvgpr_write_b32 a71, 0\n"
    "v_accvgpr_write_b32 a72, 0\n"
    "v_accvgpr_write_b32 a73, 0\n"
    "v_accvgpr_write_b32 a74, 0\n"
    "v_accvgpr_write_b32 a75, 0\n"
    "v_accvgpr_write_b32 a76, 0\n"
    "v_accvgpr_write_b32 a77, 0\n"
    "v_accvgpr_write_b32 a78, 0\n"
    "v_accvgpr_write_b32 a79, 0\n"
    "v_accvgpr_write_b32 a80, 0\n"
    "v_accvgpr_write_b32 a81, 0\n"
    "v_accvgpr_write_b32 a82, 0\n"
    "v_accvgpr_write_b32 a83, 0\n"
    "v_accvgpr_write_b32 a84, 0\n"
    "v_accvgpr_write_b32 a85, 0\n"
    "v_accvgpr_write_b32 a86, 0\n"
    "v_accvgpr_write_b32 a87, 0\n"
    "v_accvgpr_write_b32 a88, 0\n"
    "v_accvgpr_write_b32 a89, 0\n"
    "v_accvgpr_write_b32 a90, 0\n"
    "v_accvgpr_write_b32 a91, 0\n"
    "v_accvgpr_write_b32 a92, 0\n"
    "v_accvgpr_write_b32 a93, 0\n"
    "v_accvgpr_write_b32 a94, 0\n"
    "v_accvgpr_write_b32 a95, 0\n"
    "v_accvgpr_write_b32 a96, 0\n"
    "v_accvgpr_write_b32 a97, 0\n"
    "v_accvgpr_write_b32 a98, 0\n"
    "v_accvgpr_write_b32 a99, 0\n"
    "v_accvgpr_write_b32 a100, 0\n"
    "v_accvgpr_write_b32 a101, 0\n"
    "v_accvgpr_write_b32 a102, 0\n"
    "v_accvgpr_write_b32 a103, 0\n"
    "v_accvgpr_write_b32 a104, 0\n"
    "v_accvgpr_write_b32 a105, 0\n"
    "v_accvgpr_write_b32 a106, 0\n"
    "v_accvgpr_write_b32 a107, 0\n"
    "v_accvgpr_write_b32 a108, 0\n"
    "v_accvgpr_write_b32 a109, 0\n"
    "v_accvgpr_write_b32 a110, 0\n"
    "v_accvgpr_write_b32 a111, 0\n"
    "v_accvgpr_write_b32 a112, 0\n"
    "v_accvgpr_write_b32 a113, 0\n"
    "v_accvgpr_write_b32 a114, 0\n"
    "v_accvgpr_write_b32 a115, 0\n"
    "v_accvgpr_write_b32 a116, 0\n"
    "v_accvgpr_write_b32 a117, 0\n"
    "v_accvgpr_write_b32 a118, 0\n"
    "v_accvgpr_write_b32 a119, 0\n"
    "v_accvgpr_write_b32 a120, 0\n"
    "v_accvgpr_write_b32 a121, 0\n"
    "v_accvgpr_write_b32 a122, 0\n"
    "v_accvgpr_write_b32 a123, 0\n"
    "v_accvgpr_write_b32 a124, 0\n"
    "v_accvgpr_write_b32 a125, 0\n"
    "v_accvgpr_write_b32 a126, 0\n"
    "v_accvgpr_write_b32 a127, 0\n"
    "v_accvgpr_write_b32 a128, 0\n"
    "v_accvgpr_write_b32 a129, 0\n"
    "v_accvgpr_write_b32 a130, 0\n"
    "v_accvgpr_write_b32 a131, 0\n"
    "v_accvgpr_write_b32 a132, 0\n"
    "v_accvgpr_write_b32 a133, 0\n"
    "v_accvgpr_write_b32 a134, 0\n"
    "v_accvgpr_write_b32 a135, 0\n"
    "v_accvgpr_write_b32 a136, 0\n"
    "v_accvgpr_write_b32 a137, 0\n"
    "v_accvgpr_write_b32 a138, 0\n"
    "v_accvgpr_write_b32 a139, 0\n"
    "v_accvgpr_write_b32 a140, 0\n"
    "v_accvgpr_write_b32 a141, 0\n"
    "v_accvgpr_write_b32 a142, 0\n"
    "v_accvgpr_write_b32 a143, 0\n"
    "v_accvgpr_write_b32 a144, 0\n"
    "v_accvgpr_write_b32 a145, 0\n"
    "v_accvgpr_write_b32 a146, 0\n"
    "v_accvgpr_write_b32 a147, 0\n"
    "v_accvgpr_write_b32 a148, 0\n"
    "v_accvgpr_write_b32 a149, 0\n"
    "v_accvgpr_write_b32 a150, 0\n"
    "v_accvgpr_write_b32 a151, 0\n"
    "v_accvgpr_write_b32 a152, 0\n"
    "v_accvgpr_write_b32 a153, 0\n"
    "v_accvgpr_write_b32 a154, 0\n"
    "v_accvgpr_write_b32 a155, 0\n"
    "v_accvgpr_write_b32 a156, 0\n"
    "v_accvgpr_write_b32 a157, 0\n"
    "v_accvgpr_write_b32 a158, 0\n"
    "v_accvgpr_write_b32 a159, 0\n"
    "v_accvgpr_write_b32 a160, 0\n"
    "v_accvgpr_write_b32 a161, 0\n"
    "v_accvgpr_write_b32 a162, 0\n"
    "v_accvgpr_write_b32 a163, 0\n"
    "v_accvgpr_write_b32 a164, 0\n"
    "v_accvgpr_write_b32 a165, 0\n"
    "v_accvgpr_write_b32 a166, 0\n"
    "v_accvgpr_write_b32 a167, 0\n"
    "v_accvgpr_write_b32 a168, 0\n"
    "v_accvgpr_write_b32 a169, 0\n"
    "v_accvgpr_write_b32 a170, 0\n"
    "v_accvgpr_write_b32 a171, 0\n"
    "v_accvgpr_write_b32 a172, 0\n"
    "v_accvgpr_write_b32 a173, 0\n"
    "v_accvgpr_write_b32 a174, 0\n"
    "v_accvgpr_write_b32 a175, 0\n"
    "v_accvgpr_write_b32 a176, 0\n"
    "v_accvgpr_write_b32 a177, 0\n"
    "v_accvgpr_write_b32 a178, 0\n"
    "v_accvgpr_write_b32 a179, 0\n"
    "v_accvgpr_write_b32 a180, 0\n"
    "v_accvgpr_write_b32 a181, 0\n"
    "v_accvgpr_write_b32 a182, 0\n"
    "v_accvgpr_write_b32 a183, 0\n"
    "v_accvgpr_write_b32 a184, 0\n"
    "v_accvgpr_write_b32 a185, 0\n"
    "v_accvgpr_write_b32 a186, 0\n"
    "v_accvgpr_write_b32 a187, 0\n"
    "v_accvgpr_write_b32 a188, 0\n"
    "v_accvgpr_write_b32 a189, 0\n"
    "v_accvgpr_write_b32 a190, 0\n"
    "v_accvgpr_write_b32 a191, 0\n"
    "s_waitcnt vmcnt(0)\n"
    "s_mov_b32 s80, 0\n"
    "buffer_store_dwordx2 v[242:243], v244, s[36:39], s80 offen offset:0 sc1\n"
    "s_mov_b32 s80, 128\n"
    "buffer_store_dwordx2 v[242:243], v244, s[36:39], s80 offen offset:0 sc1\n"
    "s_waitcnt lgkmcnt(0)\n"
    "s_barrier\n"
    "ds_read_b128 a[192:195], v155 offset:0\n"
    "ds_read_b128 a[196:199], v156 offset:0\n"
    "ds_read_b128 a[200:203], v155 offset:128\n"
    "ds_read_b128 a[204:207], v156 offset:128\n"
    "ds_read_b128 a[208:211], v155 offset:256\n"
    "ds_read_b128 a[212:215], v156 offset:256\n"
    "ds_read_b128 a[216:219], v155 offset:6144\n"
    "ds_read_b128 a[220:223], v156 offset:6144\n"
    "ds_read_b128 a[224:227], v155 offset:6272\n"
    "ds_read_b128 a[228:231], v156 offset:6272\n"
    "ds_read_b128 a[232:235], v155 offset:6400\n"
    "ds_read_b128 a[236:239], v156 offset:6400\n"
    "ds_read_b128 a[240:243], v155 offset:12288\n"
    "ds_read_b128 a[244:247], v156 offset:12288\n"
    "ds_read_b128 a[248:251], v155 offset:12416\n"
    "ds_read_b128 a[252:255], v156 offset:12416\n"
    "ds_read_b128 v[120:123], v155 offset:12544\n"
    "ds_read_b128 v[124:127], v156 offset:12544\n"
    "ds_read_b128 v[128:131], v155 offset:18432\n"
    "ds_read_b128 v[132:135], v156 offset:18432\n"
    "ds_read_b128 v[136:139], v155 offset:18560\n"
    "ds_read_b128 v[140:143], v156 offset:18560\n"
    "ds_read_b128 v[144:147], v155 offset:18688\n"
    "ds_read_b128 v[148:151], v156 offset:18688\n"
    "s_waitcnt lgkmcnt(0)\n"
    "s_barrier\n"
    "v_and_b32 v68, 0x1f0, v1\n"
    "v_lshrrev_b32 v68, 1, v68\n"
    "v_add_u32 v68, 128, v68\n"
    "s_mov_b32 s81, 0\n"
    "buffer_load_dwordx2 v[240:241], v68, s[36:39], s81 offen offset:0 sc1\n"
    "s_add_u32 s80, s63, 147456\n"
    "buffer_load_dwordx3 v[36:38], v246, s[28:31], s80 offen offset:0 nt\n"
    "buffer_load_dwordx3 v[40:42], v246, s[28:31], s80 offen offset:768 nt\n"
    "buffer_load_dwordx3 v[44:46], v246, s[28:31], s80 offen offset:1536 nt\n"
    "buffer_load_dwordx3 v[48:50], v246, s[28:31], s80 offen offset:2304 nt\n"
    "buffer_load_dwordx3 v[52:54], v247, s[28:31], s80 offen offset:0 nt\n"
    "buffer_load_dwordx3 v[56:58], v247, s[28:31], s80 offen offset:768 nt\n"
    "buffer_load_dwordx3 v[60:62], v247, s[28:31], s80 offen offset:1536 nt\n"
    "buffer_load_dwordx3 v[64:66], v247, s[28:31], s80 offen offset:2304 nt\n"
    "s_add_u32 s80, s63, 245760\n"
    "buffer_load_dwordx3 v[200:202], v246, s[28:31], s80 offen offset:0 nt\n"
    "buffer_load_dwordx3 v[204:206], v246, s[28:31], s80 offen offset:768 nt\n"
    "buffer_load_dwordx3 v[208:210], v246, s[28:31], s80 offen offset:1536 nt\n"
    "buffer_load_dwordx3 v[212:214], v246, s[28:31], s80 offen offset:2304 nt\n"
    "buffer_load_dwordx3 v[216:218], v247, s[28:31], s80 offen offset:0 nt\n"
    "buffer_load_dwordx3 v[220:222], v247, s[28:31], s80 offen offset:768 nt\n"
    "buffer_load_dwordx3 v[224:226], v247, s[28:31], s80 offen offset:1536 nt\n"
    "buffer_load_dwordx3 v[228:230], v247, s[28:31], s80 offen offset:2304 nt\n"
    "ds_read_b128 v[152:155], v2 offset:0\n"
    "ds_read_b128 v[156:159], v2 offset:2048\n"
    "ds_read_b128 v[160:163], v2 offset:4096\n"
    "ds_read_b128 v[164:167], v2 offset:6144\n"
    "ds_read_b128 v[168:171], v2 offset:8192\n"
    "ds_read_b128 v[172:175], v2 offset:10240\n"
    "ds_read_b128 v[176:179], v2 offset:1024\n"
    "ds_read_b128 v[180:183], v2 offset:3072\n"
    "ds_read_b128 v[184:187], v2 offset:5120\n"
    "ds_read_b128 v[188:191], v2 offset:7168\n"
    "ds_read_b128 v[192:195], v2 offset:9216\n"
    "ds_read_b128 v[196:199], v2 offset:11264\n"
    "s_waitcnt lgkmcnt(11)\n"
    "v_mfma_f32_16x16x32_bf16 v[4:7], v[152:155], a[192:195], 0\n"
    "s_waitcnt lgkmcnt(10)\n"
    "v_mfma_f32_16x16x32_bf16 v[4:7], v[156:159], a[196:199], v[4:7]\n"
    "s_waitcnt lgkmcnt(9)\n"
    "v_mfma_f32_16x16x32_bf16 v[4:7], v[160:163], a[200:203], v[4:7]\n"
    "s_waitcnt lgkmcnt(8)\n"
    "v_mfma_f32_16x16x32_bf16 v[4:7], v[164:167], a[204:207], v[4:7]\n"
    "s_waitcnt lgkmcnt(7)\n"
    "v_mfma_f32_16x16x32_bf16 v[4:7], v[168:171], a[208:211], v[4:7]\n"
    "s_waitcnt lgkmcnt(6)\n"
    "v_mfma_f32_16x16x32_bf16 v[4:7], v[172:175], a[212:215], v[4:7]\n"
    "v_mfma_f32_16x16x32_bf16 v[8:11], v[172:175], a[236:239], 0\n"
    "v_mfma_f32_16x16x32_bf16 v[8:11], v[168:171], a[232:235], v[8:11]\n"
    "v_mfma_f32_16x16x32_bf16 v[8:11], v[164:167], a[228:231], v[8:11]\n"
    "v_mfma_f32_16x16x32_bf16 v[8:11], v[160:163], a[224:227], v[8:11]\n"
    "v_mfma_f32_16x16x32_bf16 v[8:11], v[156:159], a[220:223], v[8:11]\n"
    "v_mfma_f32_16x16x32_bf16 v[8:11], v[152:155], a[216:219], v[8:11]\n"
    "v_mfma_f32_16x16x32_bf16 v[12:15], v[152:155], a[240:243], 0\n"
    "v_mfma_f32_16x16x32_bf16 v[12:15], v[156:159], a[244:247], v[12:15]\n"
    "v_mfma_f32_16x16x32_bf16 v[12:15], v[160:163], a[248:251], v[12:15]\n"
    "v_mfma_f32_16x16x32_bf16 v[12:15], v[164:167], a[252:255], v[12:15]\n"
    "v_mfma_f32_16x16x32_bf16 v[12:15], v[168:171], v[120:123], v[12:15]\n"
    "v_mfma_f32_16x16x32_bf16 v[12:15], v[172:175], v[124:127], v[12:15]\n"
    "v_mfma_f32_16x16x32_bf16 v[16:19], v[172:175], v[148:151], 0\n"
    "v_mfma_f32_16x16x32_bf16 v[16:19], v[168:171], v[144:147], v[16:19]\n"
    "v_mfma_f32_16x16x32_bf16 v[16:19], v[164:167], v[140:143], v[16:19]\n"
    "v_mfma_f32_16x16x32_bf16 v[16:19], v[160:163], v[136:139], v[16:19]\n"
    "v_mfma_f32_16x16x32_bf16 v[16:19], v[156:159], v[132:135], v[16:19]\n"
    "v_mfma_f32_16x16x32_bf16 v[16:19], v[152:155], v[128:131], v[16:19]\n"
    "s_waitcnt lgkmcnt(5)\n"
    "v_mfma_f32_16x16x32_bf16 v[20:23], v[176:179], a[192:195], 0\n"
    "s_waitcnt lgkmcnt(4)\n"
    "v_mfma_f32_16x16x32_bf16 v[20:23], v[180:183], a[196:199], v[20:23]\n"
    "s_waitcnt lgkmcnt(3)\n"
    "v_mfma_f32_16x16x32_bf16 v[20:23], v[184:187], a[200:203], v[20:23]\n"
    "s_waitcnt lgkmcnt(2)\n"
    "v_mfma_f32_16x16x32_bf16 v[20:23], v[188:191], a[204:207], v[20:23]\n"
    "s_waitcnt lgkmcnt(1)\n"
    "v_mfma_f32_16x16x32_bf16 v[20:23], v[192:195], a[208:211], v[20:23]\n"
    "s_waitcnt lgkmcnt(0)\n"
    "v_mfma_f32_16x16x32_bf16 v[20:23], v[196:199], a[212:215], v[20:23]\n"
    "v_mfma_f32_16x16x32_bf16 v[24:27], v[196:199], a[236:239], 0\n"
    "v_mfma_f32_16x16x32_bf16 v[24:27], v[192:195], a[232:235], v[24:27]\n"
    "v_mfma_f32_16x16x32_bf16 v[24:27], v[188:191], a[228:231], v[24:27]\n"
    "v_mfma_f32_16x16x32_bf16 v[24:27], v[184:187], a[224:227], v[24:27]\n"
    "v_mfma_f32_16x16x32_bf16 v[24:27], v[180:183], a[220:223], v[24:27]\n"
    "v_mfma_f32_16x16x32_bf16 v[24:27], v[176:179], a[216:219], v[24:27]\n"
    "v_mfma_f32_16x16x32_bf16 v[28:31], v[176:179], a[240:243], 0\n"
    "v_mfma_f32_16x16x32_bf16 v[28:31], v[180:183], a[244:247], v[28:31]\n"
    "v_mfma_f32_16x16x32_bf16 v[28:31], v[184:187], a[248:251], v[28:31]\n"
    "v_mfma_f32_16x16x32_bf16 v[28:31], v[188:191], a[252:255], v[28:31]\n"
    "v_mfma_f32_16x16x32_bf16 v[28:31], v[192:195], v[120:123], v[28:31]\n"
    "v_mfma_f32_16x16x32_bf16 v[28:31], v[196:199], v[124:127], v[28:31]\n"
    "v_mfma_f32_16x16x32_bf16 v[32:35], v[196:199], v[148:151], 0\n"
    "v_mfma_f32_16x16x32_bf16 v[32:35], v[192:195], v[144:147], v[32:35]\n"
    "v_mfma_f32_16x16x32_bf16 v[32:35], v[188:191], v[140:143], v[32:35]\n"
    "v_mfma_f32_16x16x32_bf16 v[32:35], v[184:187], v[136:139], v[32:35]\n"
    "v_mfma_f32_16x16x32_bf16 v[32:35], v[180:183], v[132:135], v[32:35]\n"
    "v_mfma_f32_16x16x32_bf16 v[32:35], v[176:179], v[128:131], v[32:35]\n"
    "s_waitcnt vmcnt(16)\n"
    "v_cmp_eq_u64 s[54:55], v[240:241], v[242:243]\n"
    "s_nop 1\n"
    "s_mov_b32 s74, 0x00005555\n"
    "s_mov_b32 s75, 0x00005555\n"
    "s_or_b64 s[54:55], s[54:55], s[74:75]\n"
    "s_cmp_eq_u64 s[54:55], -1\n"
    "s_cbranch_scc0 .LpaV1\n"
    ".LpaV1r:\n"
    "s_add_u32 s72, s65, 24576\n"
    "s_add_u32 s73, s65, 28672\n"
    "s_mov_b32 s81, 256\n"
    "s_add_u32 m0, s68, 0\n"
    "s_nop 0\n"
    "buffer_load_dwordx4 v1, s[16:19], s67 offen offset:0 sc1 lds\n"
    "v_max3_f32 v232, v4, v5, v6\n"
    "v_max3_f32 v232, v232, v7, v20\n"
    "v_max3_f32 v232, v232, v21, v22\n"
    "v_max_f32 v232, v232, v23\n"
    "v_max3_f32 v233, v8, v9, v10\n"
    "v_max3_f32 v233, v233, v11, v24\n"
    "v_max3_f32 v233, v233, v25, v26\n"
    "v_max_f32 v233, v233, v27\n"
    "v_max3_f32 v234, v12, v13, v14\n"
    "v_max3_f32 v234, v234, v15, v28\n"
    "v_max3_f32 v234, v234, v29, v30\n"
    "v_max_f32 v234, v234, v31\n"
    "v_max3_f32 v235, v16, v17, v18\n"
    "buffer_load_dwordx4 v1, s[16:19], s67 offen offset:1024 sc1 lds\n"
    "v_max3_f32 v235, v235, v19, v32\n"
    "v_max3_f32 v235, v235, v33, v34\n"
    "v_max_f32 v235, v235, v35\n"
    "v_mov_b32 v236, v232\n"
    "s_nop 1\n"
    "v_permlane32_swap_b32 v232, v236\n"
    "v_max_f32 v232, v232, v236\n"
    "v_mov_b32 v236, v232\n"
    "s_nop 1\n"
    "v_permlane16_swap_b32 v232, v236\n"
    "v_max_f32 v232, v232, v236\n"
    "v_add_f32 v232, 0x42000000, v232\n"
    "v_sub_f32 v100, 0, v232\n"
    "v_sub_f32 v101, 0, v232\n"
    "v_sub_f32 v102, 0, v232\n"
    "buffer_load_dwordx4 v1, s[16:19], s67 offen offset:2048 sc1 lds\n"
    "v_sub_f32 v103, 0, v232\n"
    "v_mov_b32 v236, v233\n"
    "s_nop 1\n"
    "v_permlane32_swap_b32 v233, v236\n"
    "v_max_f32 v233, v233, v236\n"
    "v_mov_b32 v236, v233\n"
    "s_nop 1\n"
    "v_permlane16_swap_b32 v233, v236\n"
    "v_max_f32 v233, v233, v236\n"
    "v_add_f32 v233, 0x42000000, v233\n"
    "v_sub_f32 v104, 0, v233\n"
    "v_sub_f32 v105, 0, v233\n"
    "v_sub_f32 v106, 0, v233\n"
    "v_sub_f32 v107, 0, v233\n"
    "v_mov_b32 v236, v234\n"
    "buffer_load_dwordx4 v1, s[16:19], s67 offen offset:3072 sc1 lds\n"
    "s_nop 0\n"
    "v_permlane32_swap_b32 v234, v236\n"
    "v_max_f32 v234, v234, v236\n"
    "v_mov_b32 v236, v234\n"
    "s_nop 1\n"
    "v_permlane16_swap_b32 v234, v236\n"
    "v_max_f32 v234, v234, v236\n"
    "v_add_f32 v234, 0x42000000, v234\n"
    "v_sub_f32 v108, 0, v234\n"
    "v_sub_f32 v109, 0, v234\n"
    "v_sub_f32 v110, 0, v234\n"
    "v_sub_f32 v111, 0, v234\n"
    "v_mov_b32 v236, v235\n"
    "s_nop 1\n"
    "v_permlane32_swap_b32 v235, v236\n"
    "v_max_f32 v235, v235, v236\n"
    "s_add_u32 m0, s68, 4096\n"
    "s_add_u32 s83, s67, 4096\n"
    "buffer_load_dwordx4 v1, s[16:19], s83 offen offset:0 sc1 lds\n"
    "v_mov_b32 v236, v235\n"
    "s_nop 1\n"
    "v_permlane16_swap_b32 v235, v236\n"
    "v_max_f32 v235, v235, v236\n"
    "v_add_f32 v235, 0x42000000, v235\n"
    "v_sub_f32 v112, 0, v235\n"
    "v_sub_f32 v113, 0, v235\n"
    "v_sub_f32 v114, 0, v235\n"
    "v_sub_f32 v115, 0, v235\n"
    "v_sub_f32 v4, v4, v232\n"
    "v_sub_f32 v5, v5, v232\n"
    "v_sub_f32 v6, v6, v232\n"
    "v_sub_f32 v7, v7, v232\n"
    "v_sub_f32 v8, v8, v233\n"
    "buffer_load_dwordx4 v1, s[16:19], s83 offen offset:1024 sc1 lds\n"
    "v_sub_f32 v9, v9, v233\n"
    "v_sub_f32 v10, v10, v233\n"
    "v_sub_f32 v11, v11, v233\n"
    "v_sub_f32 v12, v12, v234\n"
    "v_sub_f32 v13, v13, v234\n"
    "v_sub_f32 v14, v14, v234\n"
    "v_sub_f32 v15, v15, v234\n"
    "v_sub_f32 v16, v16, v235\n"
    "v_sub_f32 v17, v17, v235\n"
    "v_sub_f32 v18, v18, v235\n"
    "v_sub_f32 v19, v19, v235\n"
    "v_sub_f32 v20, v20, v232\n"
    "v_sub_f32 v21, v21, v232\n"
    "s_mov_b32 m0, s72\n"
    "s_nop 0\n"
    "buffer_load_dwordx4 v1, s[16:19], s72 offen offset:0 sc1 lds\n"
    "v_sub_f32 v22, v22, v232\n"
    "v_sub_f32 v23, v23, v232\n"
    "v_sub_f32 v24, v24, v233\n"
    "v_sub_f32 v25, v25, v233\n"
    "v_sub_f32 v26, v26, v233\n"
    "v_sub_f32 v27, v27, v233\n"
    "v_sub_f32 v28, v28, v234\n"
    "v_sub_f32 v29, v29, v234\n"
    "v_sub_f32 v30, v30, v234\n"
    "v_sub_f32 v31, v31, v234\n"
    "v_sub_f32 v32, v32, v235\n"
    "v_sub_f32 v33, v33, v235\n"
    "v_sub_f32 v34, v34, v235\n"
    "buffer_load_dwordx4 v1, s[16:19], s72 offen offset:1024 sc1 lds\n"
    "v_sub_f32 v35, v35, v235\n"
    "v_exp_f32 v4, v4\n"
    "v_exp_f32 v5, v5\n"
    "v_exp_f32 v6, v6\n"
    "v_exp_f32 v7, v7\n"
    "v_exp_f32 v8, v8\n"
    "v_exp_f32 v9, v9\n"
    "v_exp_f32 v10, v10\n"
    "v_exp_f32 v11, v11\n"
    "v_exp_f32 v12, v12\n"
    "v_exp_f32 v13, v13\n"
    "v_exp_f32 v14, v14\n"
    "v_exp_f32 v15, v15\n"
    "buffer_load_dwordx4 v1, s[16:19], s72 offen offset:2048 sc1 lds\n"
    "v_exp_f32 v16, v16\n"
    "v_exp_f32 v17, v17\n"
    "v_exp_f32 v18, v18\n"
    "v_exp_f32 v19, v19\n"
    "v_exp_f32 v20, v20\n"
    "v_exp_f32 v21, v21\n"
    "v_exp_f32 v22, v22\n"
    "v_exp_f32 v23, v23\n"
    "v_exp_f32 v24, v24\n"
    "v_exp_f32 v25, v25\n"
    "v_exp_f32 v26, v26\n"
    "v_exp_f32 v27, v27\n"
    "v_exp_f32 v28, v28\n"
    "buffer_load_dwordx4 v1, s[16:19], s72 offen offset:3072 sc1 lds\n"
    "v_exp_f32 v29, v29\n"
    "v_exp_f32 v30, v30\n"
    "v_exp_f32 v31, v31\n"
    "v_exp_f32 v32, v32\n"
    "v_exp_f32 v33, v33\n"
    "v_exp_f32 v34, v34\n"
    "v_exp_f32 v35, v35\n"
    "v_mov_b32 v116, v4\n"
    "v_add_f32 v116, v116, v5\n"
    "v_add_f32 v116, v116, v6\n"
    "v_add_f32 v116, v116, v7\n"
    "v_mov_b32 v117, v8\n"
    "v_add_f32 v117, v117, v9\n"
    "s_mov_b32 m0, s73\n"
    "s_nop 0\n"
    "buffer_load_dwordx4 v1, s[16:19], s73 offen offset:0 sc1 lds\n"
    "v_add_f32 v117, v117, v10\n"
    "v_add_f32 v117, v117, v11\n"
    "v_mov_b32 v118, v12\n"
    "v_add_f32 v118, v118, v13\n"
    "v_add_f32 v118, v118, v14\n"
    "v_add_f32 v118, v118, v15\n"
    "v_mov_b32 v119, v16\n"
    "v_add_f32 v119, v119, v17\n"
    "v_add_f32 v119, v119, v18\n"
    "v_add_f32 v119, v119, v19\n"
    "v_add_f32 v116, v116, v20\n"
    "v_add_f32 v116, v116, v21\n"
    "v_add_f32 v116, v116, v22\n"
    "buffer_load_dwordx4 v1, s[16:19], s73 offen offset:1024 sc1 lds\n"
    "v_add_f32 v116, v116, v23\n"
    "v_add_f32 v117, v117, v24\n"
    "v_add_f32 v117, v117, v25\n"
    "v_add_f32 v117, v117, v26\n"
    "v_add_f32 v117, v117, v27\n"
    "v_add_f32 v118, v118, v28\n"
    "v_add_f32 v118, v118, v29\n"
    "v_add_f32 v118, v118, v30\n"
    "v_add_f32 v118, v118, v31\n"
    "v_add_f32 v119, v119, v32\n"
    "v_add_f32 v119, v119, v33\n"
    "v_add_f32 v119, v119, v34\n"
    "v_add_f32 v119, v119, v35\n"
    "s_mov_b32 s79, 256\n"
    "s_waitcnt vmcnt(20)\n"
    "s_mov_b32 s71, 147456\n"
    "v_mov_b32 v249, 156672\n"
    "s_add_u32 s76, s71, s60\n"
    "s_add_u32 s77, s71, s61\n"
    "s_add_u32 s78, s71, s62\n"
    "ds_read_b128 v[232:235], v249 offset:0\n"
    "ds_read_b128 v[236:239], v249 offset:32\n"
    "v_cvt_f32_i32 v36, v36\n"
    "v_cvt_f32_i32 v37, v37\n"
    "v_cvt_f32_i32 v38, v38\n"
    "v_cvt_f32_i32 v40, v40\n"
    "v_cvt_f32_i32 v41, v41\n"
    "v_cvt_f32_i32 v42, v42\n"
    "v_cvt_f32_i32 v44, v44\n"
    "v_cvt_f32_i32 v45, v45\n"
    "v_cvt_f32_i32 v46, v46\n"
    "v_cvt_f32_i32 v48, v48\n"
    "v_cvt_f32_i32 v49, v49\n"
    "v_cvt_f32_i32 v50, v50\n"
    "s_waitcnt lgkmcnt(0)\n"
    "v_fma_f32 v36, v36, v232, v236\n"
    "v_fma_f32 v37, v37, v232, v236\n"
    "v_fma_f32 v38, v38, v232, v236\n"
    "v_fma_f32 v40, v40, v233, v237\n"
    "v_fma_f32 v41, v41, v233, v237\n"
    "v_fma_f32 v42, v42, v233, v237\n"
    "v_fma_f32 v44, v44, v234, v238\n"
    "v_fma_f32 v45, v45, v234, v238\n"
    "v_fma_f32 v46, v46, v234, v238\n"
    "v_fma_f32 v48, v48, v235, v239\n"
    "v_fma_f32 v49, v49, v235, v239\n"
    "v_fma_f32 v50, v50, v235, v239\n"
    "ds_read_b128 v[232:235], v249 offset:16\n"
    "ds_read_b128 v[236:239], v249 offset:48\n"
    "v_cvt_f32_i32 v52, v52\n"
    "v_cvt_f32_i32 v53, v53\n"
    "v_cvt_f32_i32 v54, v54\n"
    "v_cvt_f32_i32 v56, v56\n"
    "v_cvt_f32_i32 v57, v57\n"
    "v_cvt_f32_i32 v58, v58\n"
    "v_cvt_f32_i32 v60, v60\n"
    "v_cvt_f32_i32 v61, v61\n"
    "v_cvt_f32_i32 v62, v62\n"
    "v_cvt_f32_i32 v64, v64\n"
    "v_cvt_f32_i32 v65, v65\n"
    "v_cvt_f32_i32 v66, v66\n"
    "s_waitcnt lgkmcnt(0)\n"
    "v_fma_f32 v52, v52, v232, v236\n"
    "v_fma_f32 v53, v53, v232, v236\n"
    "v_fma_f32 v54, v54, v232, v236\n"
    "v_fma_f32 v56, v56, v233, v237\n"
    "v_fma_f32 v57, v57, v233, v237\n"
    "v_fma_f32 v58, v58, v233, v237\n"
    "v_fma_f32 v60, v60, v234, v238\n"
    "v_fma_f32 v61, v61, v234, v238\n"
    "v_fma_f32 v62, v62, v234, v238\n"
    "v_fma_f32 v64, v64, v235, v239\n"
    "v_fma_f32 v65, v65, v235, v239\n"
    "v_fma_f32 v66, v66, v235, v239\n"
    "v_cvt_pk_bf16_f32 v36, v36, v40\n"
    "v_cvt_pk_bf16_f32 v40, v37, v41\n"
    "v_cvt_pk_bf16_f32 v232, v38, v42\n"
    "v_cvt_pk_bf16_f32 v37, v44, v48\n"
    "v_cvt_pk_bf16_f32 v41, v45, v49\n"
    "v_cvt_pk_bf16_f32 v233, v46, v50\n"
    "v_cvt_pk_bf16_f32 v38, v52, v56\n"
    "v_cvt_pk_bf16_f32 v42, v53, v57\n"
    "v_cvt_pk_bf16_f32 v234, v54, v58\n"
    "v_cvt_pk_bf16_f32 v39, v60, v64\n"
    "v_cvt_pk_bf16_f32 v43, v61, v65\n"
    "v_cvt_pk_bf16_f32 v235, v62, v66\n"
    "buffer_store_dwordx4 v[36:39], v248, s[16:19], s76 offen offset:0 sc1\n"
    "buffer_store_dwordx4 v[40:43], v248, s[16:19], s77 offen offset:0 sc1\n"
    "buffer_store_dwordx4 v[232:235], v248, s[16:19], s78 offen offset:0 sc1\n"
    "s_mov_b32 s71, 245760\n"
    "v_mov_b32 v249, 156736\n"
    "buffer_load_dwordx2 v[240:241], v245, s[36:39], s81 offen offset:0 sc1\n"
    "s_add_u32 m0, s68, 24576\n"
    "s_add_u32 s83, s67, 24576\n"
    "buffer_load_dwordx4 v1, s[16:19], s83 offen offset:0 sc1 lds\n"
    "buffer_load_dwordx4 v1, s[16:19], s83 offen offset:1024 sc1 lds\n"
    "buffer_load_dwordx4 v1, s[16:19], s83 offen offset:2048 sc1 lds\n"
    "buffer_load_dwordx4 v1, s[16:19], s83 offen offset:3072 sc1 lds\n"
    "s_add_u32 m0, s68, 28672\n"
    "s_add_u32 s83, s67, 28672\n"
    "buffer_load_dwordx4 v1, s[16:19], s83 offen offset:0 sc1 lds\n"
    "buffer_load_dwordx4 v1, s[16:19], s83 offen offset:1024 sc1 lds\n"
    "s_add_u32 s67, s67, 49152\n"
    "s_waitcnt vmcnt(6)\n"
    "s_mov_b32 s80, 384\n"
    "buffer_store_dwordx2 v[242:243], v244, s[36:39], s80 offen offset:0 sc1\n"
    "ds_add_u32 v252, v251\n"
    "ds_read_b128 v[152:155], v2 offset:12288\n"
    "ds_read_b128 v[156:159], v2 offset:14336\n"
    "ds_read_b128 v[160:163], v2 offset:16384\n"
    "ds_read_b128 v[164:167], v2 offset:18432\n"
    "ds_read_b128 v[168:171], v2 offset:20480\n"
    "ds_read_b128 v[172:175], v2 offset:22528\n"
    "s_waitcnt lgkmcnt(0)\n"
    "s_nop 7\n"
    "s_branch .LNV\n"
    ".p2align 6\n"
    ".LPK:\n"
    "v_mfma_f32_16x16x32_bf16 v[36:39], v[152:155], a[192:195], v[100:103]\n"
    "s_add_u32 s76, s71, s60\n"
    "ds_read_b128 v[176:179], v2 offset:13312\n"
    "v_cvt_pk_bf16_f32 v68, v4, v5\n"
    "v_mfma_f32_16x16x32_bf16 v[36:39], v[156:159], a[196:199], v[36:39]\n"
    "s_add_u32 s77, s71, s61\n"
    "ds_read_b128 v[180:183], v2 offset:15360\n"
    "v_mfma_f32_16x16x32_bf16 v[36:39], v[160:163], a[200:203], v[36:39]\n"
    "s_add_u32 s68, s68, 49152\n"
    "s_cmp_ge_u32 s68, s69\n"
    "s_cselect_b32 s68, s65, s68\n"
    "v_mfma_f32_16x16x32_bf16 v[36:39], v[164:167], a[204:207], v[36:39]\n"
    "s_add_u32 s78, s71, s62\n"
    "ds_read_b128 v[184:187], v2 offset:17408\n"
    "ds_read_b128 v[188:191], v2 offset:19456\n"
    "v_mfma_f32_16x16x32_bf16 v[36:39], v[168:171], a[208:211], v[36:39]\n"
    "ds_read_b128 v[192:195], v2 offset:21504\n"
    "v_cvt_pk_bf16_f32 v69, v6, v7\n"
    "v_mfma_f32_16x16x32_bf16 v[36:39], v[172:175], a[212:215], v[36:39]\n"
    "ds_read_b128 v[196:199], v2 offset:23552\n"
    "ds_read_b128 v[232:235], v249 offset:64\n"
    "v_mfma_f32_16x16x32_bf16 v[40:43], v[172:175], a[236:239], v[104:107]\n"
    "ds_read_b128 v[236:239], v249 offset:160\n"
    "v_cvt_pk_bf16_f32 v208, v208, v209\n"
    "v_mfma_f32_16x16x32_bf16 v[40:43], v[168:171], a[232:235], v[40:43]\n"
    "v_cvt_pk_bf16_f32 v209, v210, v211\n"
    "v_cvt_pk_bf16_f32 v72, v8, v9\n"
    "v_mfma_f32_16x16x32_bf16 v[40:43], v[164:167], a[228:231], v[40:43]\n"
    "s_add_u32 s80, s79, 256\n"
    "v_exp_f32 v36, v36\n"
    "v_mfma_f32_16x16x32_bf16 v[40:43], v[160:163], a[224:227], v[40:43]\n"
    "s_add_u32 s79, s79, 128\n"
    "v_cvt_pk_bf16_f32 v210, v212, v213\n"
    "v_add_f32 v116, v116, v36\n"
    "v_mfma_f32_16x16x32_bf16 v[40:43], v[156:159], a[220:223], v[40:43]\n"
    "v_cvt_pk_bf16_f32 v211, v214, v215\n"
    "v_mfma_f32_16x16x32_bf16 v[40:43], v[152:155], a[216:219], v[40:43]\n"
    "v_cvt_pk_bf16_f32 v73, v10, v11\n"
    "v_mfma_f32_16x16x32_bf16 v[44:47], v[152:155], a[240:243], v[108:111]\n"
    "s_waitcnt lgkmcnt(0)\n"
    "v_fma_f32 v216, v216, v232, v236\n"
    "v_mfma_f32_16x16x32_bf16 v[44:47], v[156:159], a[244:247], v[44:47]\n"
    "v_fma_f32 v217, v217, v233, v237\n"
    "v_mfma_f32_16x16x32_bf16 v[44:47], v[160:163], a[248:251], v[44:47]\n"
    "v_fma_f32 v218, v218, v234, v238\n"
    "v_mfma_f32_16x16x32_bf16 v[44:47], v[164:167], a[252:255], v[44:47]\n"
    "v_fma_f32 v219, v219, v235, v239\n"
    "v_cvt_pk_bf16_f32 v76, v12, v13\n"
    "v_mfma_f32_16x16x32_bf16 v[44:47], v[168:171], v[120:123], v[44:47]\n"
    "ds_read_b128 v[232:235], v249 offset:80\n"
    "v_mfma_f32_16x16x32_bf16 v[44:47], v[172:175], v[124:127], v[44:47]\n"
    "v_exp_f32 v37, v37\n"
    "v_mfma_f32_16x16x32_bf16 v[48:51], v[172:175], v[148:151], v[112:115]\n"
    "ds_read_b32 v250, v252\n"
    "ds_read_b128 v[236:239], v249 offset:176\n"
    "v_mfma_f32_16x16x32_bf16 v[48:51], v[168:171], v[144:147], v[48:51]\n"
    "v_cvt_pk_bf16_f32 v77, v14, v15\n"
    "v_cvt_pk_bf16_f32 v84, v36, v37\n"
    "v_mfma_f32_16x16x32_bf16 v[48:51], v[164:167], v[140:143], v[48:51]\n"
    "v_exp_f32 v38, v38\n"
    "v_mfma_f32_16x16x32_bf16 v[48:51], v[160:163], v[136:139], v[48:51]\n"
    "v_exp_f32 v39, v39\n"
    "v_mfma_f32_16x16x32_bf16 v[48:51], v[156:159], v[132:135], v[48:51]\n"
    "s_add_u32 m0, s68, 0\n"
    "v_cvt_pk_bf16_f32 v80, v16, v17\n"
    "v_cvt_pk_bf16_f32 v85, v38, v39\n"
    "v_mfma_f32_16x16x32_bf16 v[48:51], v[152:155], v[128:131], v[48:51]\n"
    "s_waitcnt vmcnt(0)\n"
    "v_cmp_eq_u64 s[54:55], v[240:241], v[242:243]\n"
    "v_mfma_f32_16x16x32_bf16 v[52:55], v[176:179], a[192:195], v[100:103]\n"
    "s_waitcnt lgkmcnt(1)\n"
    "v_cmp_le_u32 s[56:57], s59, v250\n"
    "v_add_f32 v116, v116, v37\n"
    "v_mfma_f32_16x16x32_bf16 v[52:55], v[180:183], a[196:199], v[52:55]\n"
    "s_waitcnt lgkmcnt(0)\n"
    "v_fma_f32 v220, v220, v232, v236\n"
    "v_fma_f32 v221, v221, v233, v237\n"
    "v_mfma_f32_16x16x32_bf16 v[52:55], v[184:187], a[200:203], v[52:55]\n"
    "s_and_b64 s[54:55], s[54:55], s[56:57]\n"
    "s_cmp_eq_u64 s[54:55], -1\n"
    "v_mfma_f32_16x16x32_bf16 v[52:55], v[188:191], a[204:207], v[52:55]\n"
    "s_cbranch_scc0 .Lpoll1\n"
    ".Lpoll1r:\n"
    "v_mfma_f32_16x16x32_bf16 v[52:55], v[192:195], a[208:211], v[52:55]\n"
    "buffer_load_dwordx4 v1, s[16:19], s67 offen offset:0 sc1 lds\n"
    "v_mfma_f32_16x16x32_bf16 v[52:55], v[196:199], a[212:215], v[52:55]\n"
    "s_add_u32 s59, s59, 4\n"
    "ds_read_b128 v[152:155], v3 offset:0\n"
    "ds_read_b128 v[156:159], v3 offset:2048\n"
    "v_mfma_f32_16x16x32_bf16 v[56:59], v[196:199], a[236:239], v[104:107]\n"
    "ds_read_b128 v[160:163], v3 offset:4096\n"
    "ds_read_b128 v[164:167], v3 offset:6144\n"
    "v_mfma_f32_16x16x32_bf16 v[56:59], v[192:195], a[232:235], v[56:59]\n"
    "s_min_u32 s81, s79, 3968\n"
    "ds_read_b128 v[168:171], v3 offset:8192\n"
    "ds_read_b128 v[172:175], v3 offset:10240\n"
    "v_mfma_f32_16x16x32_bf16 v[56:59], v[188:191], a[228:231], v[56:59]\n"
    "buffer_load_dwordx4 v1, s[16:19], s67 offen offset:1024 sc1 lds\n"
    "v_mfma_f32_16x16x32_bf16 v[56:59], v[184:187], a[224:227], v[56:59]\n"
    "v_fma_f32 v222, v222, v234, v238\n"
    "v_fma_f32 v223, v223, v235, v239\n"
    "v_mfma_f32_16x16x32_bf16 v[56:59], v[180:183], a[220:223], v[56:59]\n"
    "v_cvt_pk_bf16_f32 v216, v216, v217\n"
    "v_cvt_pk_bf16_f32 v217, v218, v219\n"
    "v_mfma_f32_16x16x32_bf16 v[56:59], v[176:179], a[216:219], v[56:59]\n"
    "v_cvt_pk_bf16_f32 v218, v220, v221\n"
    "v_cvt_pk_bf16_f32 v219, v222, v223\n"
    "v_mfma_f32_16x16x32_bf16 v[60:63], v[176:179], a[240:243], v[108:111]\n"
    "buffer_load_dwordx4 v1, s[16:19], s67 offen offset:2048 sc1 lds\n"
    "v_mfma_f32_16x16x32_bf16 v[60:63], v[180:183], a[244:247], v[60:63]\n"
    "buffer_store_dwordx4 v[200:203], v248, s[16:19], s76 offen offset:0 sc1\n"
    "v_mfma_f32_16x16x32_bf16 v[60:63], v[184:187], a[248:251], v[60:63]\n"
    "v_cvt_pk_bf16_f32 v81, v18, v19\n"
    "v_cvt_pk_bf16_f32 v70, v20, v21\n"
    "v_mfma_f32_16x16x32_bf16 v[60:63], v[188:191], a[252:255], v[60:63]\n"
    "buffer_store_dwordx4 v[208:211], v248, s[16:19], s77 offen offset:0 sc1\n"
    "v_mfma_f32_16x16x32_bf16 v[60:63], v[192:195], v[120:123], v[60:63]\n"
    "buffer_load_dwordx4 v1, s[16:19], s67 offen offset:3072 sc1 lds\n"
    "v_mfma_f32_16x16x32_bf16 v[60:63], v[196:199], v[124:127], v[60:63]\n"
    "s_add_u32 m0, s68, 4096\n"
    "s_add_u32 s83, s67, 4096\n"
    "v_cvt_pk_bf16_f32 v71, v22, v23\n"
    "v_mfma_f32_16x16x32_bf16 v[64:67], v[196:199], v[148:151], v[112:115]\n"
    "buffer_store_dwordx4 v[216:219], v248, s[16:19], s78 offen offset:0 sc1\n"
    "v_mfma_f32_16x16x32_bf16 v[64:67], v[192:195], v[144:147], v[64:67]\n"
    "s_add_u32 s71, s71, 98304\n"
    "v_cvt_pk_bf16_f32 v74, v24, v25\n"
    "v_cvt_pk_bf16_f32 v75, v26, v27\n"
    "v_mfma_f32_16x16x32_bf16 v[64:67], v[188:191], v[140:143], v[64:67]\n"
    "buffer_load_dwordx4 v1, s[16:19], s83 offen offset:0 sc1 lds\n"
    "v_mfma_f32_16x16x32_bf16 v[64:67], v[184:187], v[136:139], v[64:67]\n"
    "s_add_u32 s75, s71, s63\n"
    "v_cvt_pk_bf16_f32 v78, v28, v29\n"
    "v_add_u32 v249, 192, v249\n"
    "v_mfma_f32_16x16x32_bf16 v[64:67], v[180:183], v[132:135], v[64:67]\n"
    "v_exp_f32 v40, v40\n"
    "v_mfma_f32_16x16x32_bf16 v[64:67], v[176:179], v[128:131], v[64:67]\n"
    "s_waitcnt lgkmcnt(0)\n"
    "v_exp_f32 v41, v41\n"
    "v_mfma_f32_16x16x32_bf16 v[4:7], v[152:155], a[192:195], v[100:103]\n"
    "buffer_load_dwordx4 v1, s[16:19], s83 offen offset:1024 sc1 lds\n"
    "v_mfma_f32_16x16x32_bf16 v[4:7], v[156:159], a[196:199], v[4:7]\n"
    "ds_read_b128 v[176:179], v3 offset:1024\n"
    "ds_read_b128 v[180:183], v3 offset:3072\n"
    "v_mfma_f32_16x16x32_bf16 v[4:7], v[160:163], a[200:203], v[4:7]\n"
    "ds_read_b128 v[184:187], v3 offset:5120\n"
    "v_cvt_pk_bf16_f32 v79, v30, v31\n"
    "v_mfma_f32_16x16x32_bf16 v[4:7], v[164:167], a[204:207], v[4:7]\n"
    "ds_read_b128 v[188:191], v3 offset:7168\n"
    "v_cvt_pk_bf16_f32 v88, v40, v41\n"
    "v_mfma_f32_16x16x32_bf16 v[4:7], v[168:171], a[208:211], v[4:7]\n"
    "ds_read_b128 v[192:195], v3 offset:9216\n"
    "v_cvt_pk_bf16_f32 v82, v32, v33\n"
    "v_mfma_f32_16x16x32_bf16 v[4:7], v[172:175], a[212:215], v[4:7]\n"
    "ds_read_b128 v[196:199], v3 offset:11264\n"
    "v_add_f32 v116, v116, v38\n"
    "v_mfma_f32_16x16x32_bf16 v[8:11], v[172:175], a[236:239], v[104:107]\n"
    "buffer_load_dwordx4 v[200:203], v246, s[28:31], s75 offen offset:0\n"
    "v_mfma_f32_16x16x32_bf16 v[8:11], v[168:171], a[232:235], v[8:11]\n"
    "v_exp_f32 v52, v52\n"
    "v_mfma_f32_16x16x32_bf16 v[8:11], v[164:167], a[228:231], v[8:11]\n"
    "v_cvt_pk_bf16_f32 v83, v34, v35\n"
    "v_add_f32 v116, v116, v39\n"
    "v_mfma_f32_16x16x32_bf16 v[8:11], v[160:163], a[224:227], v[8:11]\n"
    "buffer_load_dwordx4 v[204:207], v246, s[28:31], s75 offen offset:16\n"
    "v_mfma_f32_16x16x32_bf16 v[8:11], v[156:159], a[220:223], v[8:11]\n"
    "v_exp_f32 v53, v53\n"
    "v_mfma_f32_16x16x32_bf16 v[8:11], v[152:155], a[216:219], v[8:11]\n"
    "v_exp_f32 v54, v54\n"
    "v_mfma_f32_16x16x32_bf16 v[12:15], v[152:155], a[240:243], v[108:111]\n"
    "buffer_load_dwordx4 v[208:211], v246, s[28:31], s75 offen offset:32\n"
    "v_mfma_f32_16x16x32_bf16 v[12:15], v[156:159], a[244:247], v[12:15]\n"
    "v_cvt_pk_bf16_f32 v86, v52, v53\n"
    "v_add_f32 v116, v116, v52\n"
    "v_mfma_f32_16x16x32_bf16 v[12:15], v[160:163], a[248:251], v[12:15]\n"
    "v_exp_f32 v55, v55\n"
    "v_mfma_f32_16x16x32_bf16 v[12:15], v[164:167], a[252:255], v[12:15]\n"
    "buffer_load_dwordx4 v[212:215], v246, s[28:31], s75 offen offset:48\n"
    "v_mfma_f32_16x16x32_bf16 v[12:15], v[168:171], v[120:123], v[12:15]\n"
    "v_cvt_pk_bf16_f32 v87, v54, v55\n"
    "v_add_f32 v116, v116, v53\n"
    "v_mfma_f32_16x16x32_bf16 v[12:15], v[172:175], v[124:127], v[12:15]\n"
    "v_exp_f32 v42, v42\n"
    "v_mfma_f32_16x16x32_bf16 v[16:19], v[172:175], v[148:151], v[112:115]\n"
    "buffer_load_dwordx4 v[216:219], v246, s[28:31], s75 offen offset:64\n"
    "v_mfma_f32_16x16x32_bf16 v[16:19], v[168:171], v[144:147], v[16:19]\n"
    "v_exp_f32 v43, v43\n"
    "v_mfma_f32_16x16x32_bf16 v[16:19], v[164:167], v[140:143], v[16:19]\n"
    "v_exp_f32 v56, v56\n"
    "v_mfma_f32_16x16x32_bf16 v[16:19], v[160:163], v[136:139], v[16:19]\n"
    "buffer_load_dwordx4 v[220:223], v246, s[28:31], s75 offen offset:80\n"
    "v_mfma_f32_16x16x32_bf16 v[16:19], v[156:159], v[132:135], v[16:19]\n"
    "v_exp_f32 v57, v57\n"
    "v_mfma_f32_16x16x32_bf16 v[16:19], v[152:155], v[128:131], v[16:19]\n"
    "s_waitcnt lgkmcnt(0)\n"
    "v_exp_f32 v58, v58\n"
    "v_mfma_f32_16x16x32_bf16 v[20:23], v[176:179], a[192:195], v[100:103]\n"
    "ds_read_b128 v[152:155], v2 offset:24576\n"
    "v_cvt_pk_bf16_f32 v89, v42, v43\n"
    "v_mfma_f32_16x16x32_bf16 v[20:23], v[180:183], a[196:199], v[20:23]\n"
    "ds_read_b128 v[156:159], v2 offset:36864\n"
    "v_cvt_pk_bf16_f32 v90, v56, v57\n"
    "v_mfma_f32_16x16x32_bf16 v[20:23], v[184:187], a[200:203], v[20:23]\n"
    "ds_read_b128 v[160:163], v2 offset:25600\n"
    "v_add_f32 v116, v116, v54\n"
    "v_mfma_f32_16x16x32_bf16 v[20:23], v[188:191], a[204:207], v[20:23]\n"
    "ds_read_b128 v[164:167], v2 offset:37888\n"
    "v_add_f32 v116, v116, v55\n"
    "v_mfma_f32_16x16x32_bf16 v[20:23], v[192:195], a[208:211], v[20:23]\n"
    "ds_read_b128 v[168:171], v2 offset:26624\n"
    "v_add_f32 v117, v117, v40\n"
    "v_mfma_f32_16x16x32_bf16 v[20:23], v[196:199], a[212:215], v[20:23]\n"
    "ds_read_b128 v[172:175], v2 offset:38912\n"
    "v_add_f32 v117, v117, v41\n"
    "v_mfma_f32_16x16x32_bf16 v[24:27], v[196:199], a[236:239], v[104:107]\n"
    "v_exp_f32 v59, v59\n"
    "v_mfma_f32_16x16x32_bf16 v[24:27], v[192:195], a[232:235], v[24:27]\n"
    "v_cvt_pk_bf16_f32 v91, v58, v59\n"
    "v_add_f32 v117, v117, v42\n"
    "v_mfma_f32_16x16x32_bf16 v[24:27], v[188:191], a[228:231], v[24:27]\n"
    "v_exp_f32 v44, v44\n"
    "v_mfma_f32_16x16x32_bf16 v[24:27], v[184:187], a[224:227], v[24:27]\n"
    "v_exp_f32 v45, v45\n"
    "v_mfma_f32_16x16x32_bf16 v[24:27], v[180:183], a[220:223], v[24:27]\n"
    "v_exp_f32 v46, v46\n"
    "v_mfma_f32_16x16x32_bf16 v[24:27], v[176:179], a[216:219], v[24:27]\n"
    "v_exp_f32 v47, v47\n"
    "v_mfma_f32_16x16x32_bf16 v[28:31], v[176:179], a[240:243], v[108:111]\n"
    "v_exp_f32 v60, v60\n"
    "v_mfma_f32_16x16x32_bf16 v[28:31], v[180:183], a[244:247], v[28:31]\n"
    "v_exp_f32 v61, v61\n"
    "v_mfma_f32_16x16x32_bf16 v[28:31], v[184:187], a[248:251], v[28:31]\n"
    "v_exp_f32 v62, v62\n"
    "v_mfma_f32_16x16x32_bf16 v[28:31], v[188:191], a[252:255], v[28:31]\n"
    "v_exp_f32 v63, v63\n"
    "v_mfma_f32_16x16x32_bf16 v[28:31], v[192:195], v[120:123], v[28:31]\n"
    "v_cvt_pk_bf16_f32 v92, v44, v45\n"
    "v_cvt_pk_bf16_f32 v93, v46, v47\n"
    "v_mfma_f32_16x16x32_bf16 v[28:31], v[196:199], v[124:127], v[28:31]\n"
    "v_cvt_pk_bf16_f32 v94, v60, v61\n"
    "v_cvt_pk_bf16_f32 v95, v62, v63\n"
    "v_mfma_f32_16x16x32_bf16 v[32:35], v[196:199], v[148:151], v[112:115]\n"
    "v_exp_f32 v48, v48\n"
    "v_mfma_f32_16x16x32_bf16 v[32:35], v[192:195], v[144:147], v[32:35]\n"
    "v_exp_f32 v49, v49\n"
    "v_mfma_f32_16x16x32_bf16 v[32:35], v[188:191], v[140:143], v[32:35]\n"
    "v_exp_f32 v50, v50\n"
    "v_mfma_f32_16x16x32_bf16 v[32:35], v[184:187], v[136:139], v[32:35]\n"
    "v_exp_f32 v51, v51\n"
    "v_mfma_f32_16x16x32_bf16 v[32:35], v[180:183], v[132:135], v[32:35]\n"
    "s_add_u32 m0, s68, 24576\n"
    "s_add_u32 s83, s67, 24576\n"
    "v_cvt_pk_bf16_f32 v96, v48, v49\n"
    "v_mfma_f32_16x16x32_bf16 v[32:35], v[176:179], v[128:131], v[32:35]\n"
    "s_waitcnt lgkmcnt(0)\n"
    "v_exp_f32 v64, v64\n"
    "v_mfma_f32_16x16x32_bf16 a[0:3], v[152:155], v[68:71], a[0:3]\n"
    "ds_read_b128 v[176:179], v2 offset:27648\n"
    "v_cvt_pk_bf16_f32 v97, v50, v51\n"
    "v_mfma_f32_16x16x32_bf16 a[0:3], v[156:159], v[84:87], a[0:3]\n"
    "ds_read_b128 v[180:183], v2 offset:39936\n"
    "v_add_f32 v117, v117, v43\n"
    "v_mfma_f32_16x16x32_bf16 a[16:19], v[164:167], v[84:87], a[16:19]\n"
    "ds_read_b128 v[184:187], v2 offset:28672\n"
    "v_add_f32 v117, v117, v56\n"
    "v_mfma_f32_16x16x32_bf16 a[16:19], v[160:163], v[68:71], a[16:19]\n"
    "ds_read_b128 v[188:191], v2 offset:40960\n"
    "v_add_f32 v117, v117, v57\n"
    "v_mfma_f32_16x16x32_bf16 a[32:35], v[168:171], v[68:71], a[32:35]\n"
    "buffer_load_dwordx4 v1, s[16:19], s83 offen offset:0 sc1 lds\n"
    "v_mfma_f32_16x16x32_bf16 a[32:35], v[172:175], v[84:87], a[32:35]\n"
    "ds_read_b128 v[192:195], v2 offset:29696\n"
    "ds_read_b128 v[196:199], v2 offset:41984\n"
    "v_mfma_f32_16x16x32_bf16 a[36:39], v[172:175], v[88:91], a[36:39]\n"
    "v_exp_f32 v65, v65\n"
    "v_mfma_f32_16x16x32_bf16 a[36:39], v[168:171], v[72:75], a[36:39]\n"
    "v_exp_f32 v66, v66\n"
    "v_mfma_f32_16x16x32_bf16 a[20:23], v[160:163], v[72:75], a[20:23]\n"
    "v_exp_f32 v67, v67\n"
    "v_mfma_f32_16x16x32_bf16 a[20:23], v[164:167], v[88:91], a[20:23]\n"
    "v_cvt_pk_bf16_f32 v98, v64, v65\n"
    "v_cvt_pk_bf16_f32 v99, v66, v67\n"
    "v_mfma_f32_16x16x32_bf16 a[4:7], v[156:159], v[88:91], a[4:7]\n"
    "buffer_load_dwordx4 v1, s[16:19], s83 offen offset:1024 sc1 lds\n"
    "v_mfma_f32_16x16x32_bf16 a[4:7], v[152:155], v[72:75], a[4:7]\n"
    "v_exp_f32 v4, v4\n"
    "v_mfma_f32_16x16x32_bf16 a[8:11], v[152:155], v[76:79], a[8:11]\n"
    "v_exp_f32 v5, v5\n"
    "v_mfma_f32_16x16x32_bf16 a[8:11], v[156:159], v[92:95], a[8:11]\n"
    "v_exp_f32 v6, v6\n"
    "v_mfma_f32_16x16x32_bf16 a[24:27], v[164:167], v[92:95], a[24:27]\n"
    "v_exp_f32 v7, v7\n"
    "v_mfma_f32_16x16x32_bf16 a[24:27], v[160:163], v[76:79], a[24:27]\n"
    "v_add_f32 v116, v116, v4\n"
    "v_add_f32 v116, v116, v5\n"
    "v_mfma_f32_16x16x32_bf16 a[40:43], v[168:171], v[76:79], a[40:43]\n"
    "buffer_load_dwordx4 v1, s[16:19], s83 offen offset:2048 sc1 lds\n"
    "v_mfma_f32_16x16x32_bf16 a[40:43], v[172:175], v[92:95], a[40:43]\n"
    "v_exp_f32 v8, v8\n"
    "v_mfma_f32_16x16x32_bf16 a[44:47], v[172:175], v[96:99], a[44:47]\n"
    "v_exp_f32 v9, v9\n"
    "v_mfma_f32_16x16x32_bf16 a[44:47], v[168:171], v[80:83], a[44:47]\n"
    "v_add_f32 v116, v116, v6\n"
    "v_add_f32 v116, v116, v7\n"
    "v_mfma_f32_16x16x32_bf16 a[28:31], v[160:163], v[80:83], a[28:31]\n"
    "v_exp_f32 v10, v10\n"
    "v_mfma_f32_16x16x32_bf16 a[28:31], v[164:167], v[96:99], a[28:31]\n"
    "v_exp_f32 v11, v11\n"
    "v_mfma_f32_16x16x32_bf16 a[12:15], v[156:159], v[96:99], a[12:15]\n"
    "buffer_load_dwordx4 v1, s[16:19], s83 offen offset:3072 sc1 lds\n"
    "v_mfma_f32_16x16x32_bf16 a[12:15], v[152:155], v[80:83], a[12:15]\n"
    "s_waitcnt lgkmcnt(0)\n"
    "s_add_u32 m0, s68, 28672\n"
    "s_add_u32 s83, s67, 28672\n"
    "v_mfma_f32_16x16x32_bf16 a[48:51], v[176:179], v[68:71], a[48:51]\n"
    "s_waitcnt vmcnt(12)\n"
    "ds_read_b128 v[152:155], v2 offset:30720\n"
    "v_add_f32 v117, v117, v8\n"
    "v_mfma_f32_16x16x32_bf16 a[48:51], v[180:183], v[84:87], a[48:51]\n"
    "buffer_store_dwordx2 v[242:243], v244, s[36:39], s80 offen offset:0 sc1\n"
    "v_mfma_f32_16x16x32_bf16 a[64:67], v[188:191], v[84:87], a[64:67]\n"
    "ds_read_b128 v[156:159], v2 offset:43008\n"
    "ds_read_b128 v[160:163], v2 offset:31744\n"
    "v_mfma_f32_16x16x32_bf16 a[64:67], v[184:187], v[68:71], a[64:67]\n"
    "ds_read_b128 v[164:167], v2 offset:44032\n"
    "v_add_f32 v117, v117, v9\n"
    "v_mfma_f32_16x16x32_bf16 a[80:83], v[192:195], v[68:71], a[80:83]\n"
    "buffer_load_dwordx4 v1, s[16:19], s83 offen offset:0 sc1 lds\n"
    "v_mfma_f32_16x16x32_bf16 a[80:83], v[196:199], v[84:87], a[80:83]\n"
    "ds_read_b128 v[168:171], v2 offset:32768\n"
    "ds_read_b128 v[172:175], v2 offset:45056\n"
    "v_mfma_f32_16x16x32_bf16 a[84:87], v[196:199], v[88:91], a[84:87]\n"
    "v_exp_f32 v12, v12\n"
    "v_mfma_f32_16x16x32_bf16 a[84:87], v[192:195], v[72:75], a[84:87]\n"
    "v_exp_f32 v13, v13\n"
    "v_mfma_f32_16x16x32_bf16 a[68:71], v[184:187], v[72:75], a[68:71]\n"
    "v_add_f32 v117, v117, v10\n"
    "v_add_f32 v117, v117, v11\n"
    "v_mfma_f32_16x16x32_bf16 a[68:71], v[188:191], v[88:91], a[68:71]\n"
    "v_exp_f32 v14, v14\n"
    "v_mfma_f32_16x16x32_bf16 a[52:55], v[180:183], v[88:91], a[52:55]\n"
    "buffer_load_dwordx4 v1, s[16:19], s83 offen offset:1024 sc1 lds\n"
    "v_mfma_f32_16x16x32_bf16 a[52:55], v[176:179], v[72:75], a[52:55]\n"
    "s_add_u32 s67, s67, 49152\n"
    "v_add_f32 v117, v117, v58\n"
    "v_add_f32 v117, v117, v59\n"
    "v_mfma_f32_16x16x32_bf16 a[56:59], v[176:179], v[76:79], a[56:59]\n"
    "v_exp_f32 v15, v15\n"
    "v_mfma_f32_16x16x32_bf16 a[56:59], v[180:183], v[92:95], a[56:59]\n"
    "v_add_f32 v118, v118, v12\n"
    "v_add_f32 v118, v118, v44\n"
    "v_mfma_f32_16x16x32_bf16 a[72:75], v[188:191], v[92:95], a[72:75]\n"
    "v_exp_f32 v16, v16\n"
    "v_mfma_f32_16x16x32_bf16 a[72:75], v[184:187], v[76:79], a[72:75]\n"
    "v_add_f32 v118, v118, v45\n"
    "v_add_f32 v118, v118, v46\n"
    "v_mfma_f32_16x16x32_bf16 a[88:91], v[192:195], v[76:79], a[88:91]\n"
    "v_add_f32 v118, v118, v47\n"
    "v_add_f32 v118, v118, v60\n"
    "v_mfma_f32_16x16x32_bf16 a[88:91], v[196:199], v[92:95], a[88:91]\n"
    "v_add_f32 v118, v118, v61\n"
    "v_add_f32 v118, v118, v62\n"
    "v_mfma_f32_16x16x32_bf16 a[92:95], v[196:199], v[96:99], a[92:95]\n"
    "v_add_f32 v118, v118, v63\n"
    "v_add_f32 v118, v118, v13\n"
    "v_mfma_f32_16x16x32_bf16 a[92:95], v[192:195], v[80:83], a[92:95]\n"
    "v_exp_f32 v17, v17\n"
    "v_mfma_f32_16x16x32_bf16 a[76:79], v[184:187], v[80:83], a[76:79]\n"
    "v_add_f32 v118, v118, v14\n"
    "v_add_f32 v119, v119, v48\n"
    "v_mfma_f32_16x16x32_bf16 a[76:79], v[188:191], v[96:99], a[76:79]\n"
    "v_exp_f32 v18, v18\n"
    "v_mfma_f32_16x16x32_bf16 a[60:63], v[180:183], v[96:99], a[60:63]\n"
    "v_add_f32 v119, v119, v49\n"
    "v_add_f32 v119, v119, v50\n"
    "v_mfma_f32_16x16x32_bf16 a[60:63], v[176:179], v[80:83], a[60:63]\n"
    "s_waitcnt lgkmcnt(0)\n"
    "v_add_f32 v119, v119, v51\n"
    "v_add_f32 v119, v119, v64\n"
    "v_mfma_f32_16x16x32_bf16 a[96:99], v[152:155], v[68:71], a[96:99]\n"
    "ds_read_b128 v[176:179], v2 offset:33792\n"
    "v_add_f32 v119, v119, v65\n"
    "v_mfma_f32_16x16x32_bf16 a[96:99], v[156:159], v[84:87], a[96:99]\n"
    "ds_read_b128 v[180:183], v2 offset:46080\n"
    "v_add_f32 v119, v119, v66\n"
    "v_mfma_f32_16x16x32_bf16 a[112:115], v[164:167], v[84:87], a[112:115]\n"
    "ds_read_b128 v[184:187], v2 offset:34816\n"
    "v_add_f32 v119, v119, v67\n"
    "v_mfma_f32_16x16x32_bf16 a[112:115], v[160:163], v[68:71], a[112:115]\n"
    "ds_read_b128 v[188:191], v2 offset:47104\n"
    "v_add_f32 v118, v118, v15\n"
    "v_mfma_f32_16x16x32_bf16 a[128:131], v[168:171], v[68:71], a[128:131]\n"
    "ds_read_b128 v[192:195], v2 offset:35840\n"
    "v_add_f32 v119, v119, v16\n"
    "v_mfma_f32_16x16x32_bf16 a[128:131], v[172:175], v[84:87], a[128:131]\n"
    "ds_read_b128 v[196:199], v2 offset:48128\n"
    "v_add_f32 v119, v119, v17\n"
    "v_mfma_f32_16x16x32_bf16 a[132:135], v[172:175], v[88:91], a[132:135]\n"
    "buffer_load_dwordx2 v[240:241], v245, s[36:39], s81 offen offset:0 sc1\n"
    "v_mfma_f32_16x16x32_bf16 a[132:135], v[168:171], v[72:75], a[132:135]\n"
    "v_exp_f32 v19, v19\n"
    "v_mfma_f32_16x16x32_bf16 a[116:119], v[160:163], v[72:75], a[116:119]\n"
    "v_exp_f32 v20, v20\n"
    "v_mfma_f32_16x16x32_bf16 a[116:119], v[164:167], v[88:91], a[116:119]\n"
    "v_exp_f32 v21, v21\n"
    "v_mfma_f32_16x16x32_bf16 a[100:103], v[156:159], v[88:91], a[100:103]\n"
    "v_add_f32 v119, v119, v18\n"
    "v_add_f32 v119, v119, v19\n"
    "v_mfma_f32_16x16x32_bf16 a[100:103], v[152:155], v[72:75], a[100:103]\n"
    "v_exp_f32 v22, v22\n"
    "v_mfma_f32_16x16x32_bf16 a[104:107], v[152:155], v[76:79], a[104:107]\n"
    "v_exp_f32 v23, v23\n"
    "v_mfma_f32_16x16x32_bf16 a[104:107], v[156:159], v[92:95], a[104:107]\n"
    "v_add_f32 v116, v116, v20\n"
    "v_add_f32 v116, v116, v21\n"
    "v_mfma_f32_16x16x32_bf16 a[120:123], v[164:167], v[92:95], a[120:123]\n"
    "v_exp_f32 v24, v24\n"
    "v_mfma_f32_16x16x32_bf16 a[120:123], v[160:163], v[76:79], a[120:123]\n"
    "v_exp_f32 v25, v25\n"
    "v_mfma_f32_16x16x32_bf16 a[136:139], v[168:171], v[76:79], a[136:139]\n"
    "v_add_f32 v116, v116, v22\n"
    "v_add_f32 v116, v116, v23\n"
    "v_mfma_f32_16x16x32_bf16 a[136:139], v[172:175], v[92:95], a[136:139]\n"
    "v_exp_f32 v26, v26\n"
    "v_mfma_f32_16x16x32_bf16 a[140:143], v[172:175], v[96:99], a[140:143]\n"
    "v_exp_f32 v27, v27\n"
    "v_mfma_f32_16x16x32_bf16 a[140:143], v[168:171], v[80:83], a[140:143]\n"
    "v_add_f32 v117, v117, v24\n"
    "v_add_f32 v117, v117, v25\n"
    "v_mfma_f32_16x16x32_bf16 a[124:127], v[160:163], v[80:83], a[124:127]\n"
    "v_exp_f32 v28, v28\n"
    "v_mfma_f32_16x16x32_bf16 a[124:127], v[164:167], v[96:99], a[124:127]\n"
    "v_exp_f32 v29, v29\n"
    "v_mfma_f32_16x16x32_bf16 a[108:111], v[156:159], v[96:99], a[108:111]\n"
    "v_add_f32 v117, v117, v26\n"
    "v_add_f32 v117, v117, v27\n"
    "v_mfma_f32_16x16x32_bf16 a[108:111], v[152:155], v[80:83], a[108:111]\n"
    "s_waitcnt lgkmcnt(0)\n"
    "v_exp_f32 v30, v30\n"
    "v_mfma_f32_16x16x32_bf16 a[144:147], v[176:179], v[68:71], a[144:147]\n"
    "ds_read_b128 v[152:155], v3 offset:12288\n"
    "v_add_f32 v118, v118, v28\n"
    "v_mfma_f32_16x16x32_bf16 a[144:147], v[180:183], v[84:87], a[144:147]\n"
    "ds_read_b128 v[156:159], v3 offset:14336\n"
    "v_add_f32 v118, v118, v29\n"
    "v_mfma_f32_16x16x32_bf16 a[160:163], v[188:191], v[84:87], a[160:163]\n"
    "ds_read_b128 v[160:163], v3 offset:16384\n"
    "v_add_f32 v118, v118, v30\n"
    "v_mfma_f32_16x16x32_bf16 a[160:163], v[184:187], v[68:71], a[160:163]\n"
    "ds_read_b128 v[164:167], v3 offset:18432\n"
    "v_mfma_f32_16x16x32_bf16 a[176:179], v[192:195], v[68:71], a[176:179]\n"
    "ds_read_b128 v[168:171], v3 offset:20480\n"
    "v_mfma_f32_16x16x32_bf16 a[176:179], v[196:199], v[84:87], a[176:179]\n"
    "ds_read_b128 v[172:175], v3 offset:22528\n"
    "v_mfma_f32_16x16x32_bf16 a[180:183], v[196:199], v[88:91], a[180:183]\n"
    "v_exp_f32 v31, v31\n"
    "v_mfma_f32_16x16x32_bf16 a[180:183], v[192:195], v[72:75], a[180:183]\n"
    "v_exp_f32 v32, v32\n"
    "v_mfma_f32_16x16x32_bf16 a[164:167], v[184:187], v[72:75], a[164:167]\n"
    "v_exp_f32 v33, v33\n"
    "v_mfma_f32_16x16x32_bf16 a[164:167], v[188:191], v[88:91], a[164:167]\n"
    "v_exp_f32 v34, v34\n"
    "v_mfma_f32_16x16x32_bf16 a[148:151], v[180:183], v[88:91], a[148:151]\n"
    "v_add_f32 v118, v118, v31\n"
    "v_add_f32 v119, v119, v32\n"
    "v_mfma_f32_16x16x32_bf16 a[148:151], v[176:179], v[72:75], a[148:151]\n"
    "v_exp_f32 v35, v35\n"
    "v_mfma_f32_16x16x32_bf16 a[152:155], v[176:179], v[76:79], a[152:155]\n"
    "v_add_f32 v119, v119, v33\n"
    "v_add_f32 v119, v119, v34\n"
    "v_mfma_f32_16x16x32_bf16 a[152:155], v[180:183], v[92:95], a[152:155]\n"
    "v_add_f32 v119, v119, v35\n"
    "v_mfma_f32_16x16x32_bf16 a[168:171], v[188:191], v[92:95], a[168:171]\n"
    "s_waitcnt vmcnt(14)\n"
    "ds_add_u32 v252, v251\n"
    "v_mfma_f32_16x16x32_bf16 a[168:171], v[184:187], v[76:79], a[168:171]\n"
    "v_mfma_f32_16x16x32_bf16 a[184:187], v[192:195], v[76:79], a[184:187]\n"
    "v_mfma_f32_16x16x32_bf16 a[184:187], v[196:199], v[92:95], a[184:187]\n"
    "v_mfma_f32_16x16x32_bf16 a[188:191], v[196:199], v[96:99], a[188:191]\n"
    "v_mfma_f32_16x16x32_bf16 a[188:191], v[192:195], v[80:83], a[188:191]\n"
    "v_mfma_f32_16x16x32_bf16 a[172:175], v[184:187], v[80:83], a[172:175]\n"
    "s_sub_u32 s74, s68, s65\n"
    "v_mfma_f32_16x16x32_bf16 a[172:175], v[188:191], v[96:99], a[172:175]\n"
    "s_add_u32 s66, s66, 1\n"
    "v_mov_b32 v2, v3\n"
    "v_add_u32 v3, s74, v1\n"
    "v_mfma_f32_16x16x32_bf16 a[156:159], v[180:183], v[96:99], a[156:159]\n"
    "s_waitcnt lgkmcnt(1)\n"
    "s_cmp_eq_u32 s66, 31\n"
    "v_mfma_f32_16x16x32_bf16 a[156:159], v[176:179], v[80:83], a[156:159]\n"
    "s_cbranch_scc1 .Ltail\n"
    ".LNK:\n"
    "v_mfma_f32_16x16x32_bf16 v[36:39], v[152:155], a[192:195], v[100:103]\n"
    "ds_read_b128 v[176:179], v2 offset:13312\n"
    "v_cvt_pk_bf16_f32 v68, v4, v5\n"
    "v_mfma_f32_16x16x32_bf16 v[36:39], v[156:159], a[196:199], v[36:39]\n"
    "ds_read_b128 v[180:183], v2 offset:15360\n"
    "ds_read_b128 v[232:235], v249 offset:0\n"
    "v_mfma_f32_16x16x32_bf16 v[36:39], v[160:163], a[200:203], v[36:39]\n"
    "s_add_u32 s68, s68, 49152\n"
    "s_cmp_ge_u32 s68, s69\n"
    "s_cselect_b32 s68, s65, s68\n"
    "v_mfma_f32_16x16x32_bf16 v[36:39], v[164:167], a[204:207], v[36:39]\n"
    "ds_read_b128 v[184:187], v2 offset:17408\n"
    "ds_read_b128 v[188:191], v2 offset:19456\n"
    "v_mfma_f32_16x16x32_bf16 v[36:39], v[168:171], a[208:211], v[36:39]\n"
    "s_waitcnt vmcnt(8)\n"
    "ds_read_b128 v[192:195], v2 offset:21504\n"
    "v_cvt_pk_bf16_f32 v69, v6, v7\n"
    "v_mfma_f32_16x16x32_bf16 v[36:39], v[172:175], a[212:215], v[36:39]\n"
    "ds_read_b128 v[196:199], v2 offset:23552\n"
    "ds_read_b128 v[236:239], v249 offset:96\n"
    "v_mfma_f32_16x16x32_bf16 v[40:43], v[172:175], a[236:239], v[104:107]\n"
    "v_cvt_f32_i32 v200, v200\n"
    "v_cvt_f32_i32 v201, v201\n"
    "v_mfma_f32_16x16x32_bf16 v[40:43], v[168:171], a[232:235], v[40:43]\n"
    "v_cvt_pk_bf16_f32 v72, v8, v9\n"
    "v_cvt_f32_i32 v202, v202\n"
    "v_mfma_f32_16x16x32_bf16 v[40:43], v[164:167], a[228:231], v[40:43]\n"
    "v_exp_f32 v36, v36\n"
    "v_mfma_f32_16x16x32_bf16 v[40:43], v[160:163], a[224:227], v[40:43]\n"
    "s_add_u32 s79, s79, 128\n"
    "v_cvt_f32_i32 v203, v203\n"
    "v_add_f32 v116, v116, v36\n"
    "v_mfma_f32_16x16x32_bf16 v[40:43], v[156:159], a[220:223], v[40:43]\n"
    "v_exp_f32 v37, v37\n"
    "v_mfma_f32_16x16x32_bf16 v[40:43], v[152:155], a[216:219], v[40:43]\n"
    "v_cvt_pk_bf16_f32 v73, v10, v11\n"
    "v_cvt_pk_bf16_f32 v84, v36, v37\n"
    "v_mfma_f32_16x16x32_bf16 v[44:47], v[152:155], a[240:243], v[108:111]\n"
    "v_cvt_f32_i32 v204, v204\n"
    "v_cvt_f32_i32 v205, v205\n"
    "v_mfma_f32_16x16x32_bf16 v[44:47], v[156:159], a[244:247], v[44:47]\n"
    "v_exp_f32 v38, v38\n"
    "v_mfma_f32_16x16x32_bf16 v[44:47], v[160:163], a[248:251], v[44:47]\n"
    "v_add_f32 v116, v116, v37\n"
    "v_add_f32 v116, v116, v38\n"
    "v_mfma_f32_16x16x32_bf16 v[44:47], v[164:167], a[252:255], v[44:47]\n"
    "v_cvt_pk_bf16_f32 v76, v12, v13\n"
    "v_cvt_f32_i32 v206, v206\n"
    "v_mfma_f32_16x16x32_bf16 v[44:47], v[168:171], v[120:123], v[44:47]\n"
    "v_exp_f32 v39, v39\n"
    "v_mfma_f32_16x16x32_bf16 v[44:47], v[172:175], v[124:127], v[44:47]\n"
    "v_cvt_pk_bf16_f32 v85, v38, v39\n"
    "v_cvt_f32_i32 v207, v207\n"
    "v_mfma_f32_16x16x32_bf16 v[48:51], v[172:175], v[148:151], v[112:115]\n"
    "ds_read_b32 v250, v252\n"
    "v_cvt_pk_bf16_f32 v77, v14, v15\n"
    "v_mfma_f32_16x16x32_bf16 v[48:51], v[168:171], v[144:147], v[48:51]\n"
    "v_exp_f32 v40, v40\n"
    "v_mfma_f32_16x16x32_bf16 v[48:51], v[164:167], v[140:143], v[48:51]\n"
    "s_waitcnt lgkmcnt(1)\n"
    "v_fma_f32 v200, v200, v232, v236\n"
    "v_add_f32 v116, v116, v39\n"
    "v_mfma_f32_16x16x32_bf16 v[48:51], v[160:163], v[136:139], v[48:51]\n"
    "v_add_f32 v117, v117, v40\n"
    "v_mfma_f32_16x16x32_bf16 v[48:51], v[156:159], v[132:135], v[48:51]\n"
    "s_add_u32 m0, s68, 0\n"
    "v_cvt_pk_bf16_f32 v80, v16, v17\n"
    "v_fma_f32 v201, v201, v233, v237\n"
    "v_mfma_f32_16x16x32_bf16 v[48:51], v[152:155], v[128:131], v[48:51]\n"
    "s_waitcnt vmcnt(0)\n"
    "v_cmp_eq_u64 s[54:55], v[240:241], v[242:243]\n"
    "v_mfma_f32_16x16x32_bf16 v[52:55], v[176:179], a[192:195], v[100:103]\n"
    "s_waitcnt lgkmcnt(0)\n"
    "v_cmp_le_u32 s[56:57], s59, v250\n"
    "v_fma_f32 v202, v202, v234, v238\n"
    "v_mfma_f32_16x16x32_bf16 v[52:55], v[180:183], a[196:199], v[52:55]\n"
    "v_exp_f32 v41, v41\n"
    "v_mfma_f32_16x16x32_bf16 v[52:55], v[184:187], a[200:203], v[52:55]\n"
    "s_and_b64 s[54:55], s[54:55], s[56:57]\n"
    "s_cmp_eq_u64 s[54:55], -1\n"
    "v_mfma_f32_16x16x32_bf16 v[52:55], v[188:191], a[204:207], v[52:55]\n"
    "s_cbranch_scc0 .Lpoll2\n"
    ".Lpoll2r:\n"
    "v_mfma_f32_16x16x32_bf16 v[52:55], v[192:195], a[208:211], v[52:55]\n"
    "buffer_load_dwordx4 v1, s[16:19], s67 offen offset:0 sc1 lds\n"
    "v_mfma_f32_16x16x32_bf16 v[52:55], v[196:199], a[212:215], v[52:55]\n"
    "s_add_u32 s59, s59, 4\n"
    "ds_read_b128 v[152:155], v3 offset:0\n"
    "ds_read_b128 v[156:159], v3 offset:2048\n"
    "v_mfma_f32_16x16x32_bf16 v[56:59], v[196:199], a[236:239], v[104:107]\n"
    "ds_read_b128 v[160:163], v3 offset:4096\n"
    "ds_read_b128 v[164:167], v3 offset:6144\n"
    "v_mfma_f32_16x16x32_bf16 v[56:59], v[192:195], a[232:235], v[56:59]\n"
    "s_min_u32 s81, s79, 3968\n"
    "ds_read_b128 v[168:171], v3 offset:8192\n"
    "ds_read_b128 v[172:175], v3 offset:10240\n"
    "v_mfma_f32_16x16x32_bf16 v[56:59], v[188:191], a[228:231], v[56:59]\n"
    "buffer_load_dwordx4 v1, s[16:19], s67 offen offset:1024 sc1 lds\n"
    "v_mfma_f32_16x16x32_bf16 v[56:59], v[184:187], a[224:227], v[56:59]\n"
    "v_cvt_pk_bf16_f32 v81, v18, v19\n"
    "v_cvt_pk_bf16_f32 v70, v20, v21\n"
    "v_mfma_f32_16x16x32_bf16 v[56:59], v[180:183], a[220:223], v[56:59]\n"
    "v_cvt_pk_bf16_f32 v71, v22, v23\n"
    "v_cvt_pk_bf16_f32 v88, v40, v41\n"
    "v_mfma_f32_16x16x32_bf16 v[56:59], v[176:179], a[216:219], v[56:59]\n"
    "v_exp_f32 v42, v42\n"
    "v_mfma_f32_16x16x32_bf16 v[60:63], v[176:179], a[240:243], v[108:111]\n"
    "buffer_load_dwordx4 v1, s[16:19], s67 offen offset:2048 sc1 lds\n"
    "v_mfma_f32_16x16x32_bf16 v[60:63], v[180:183], a[244:247], v[60:63]\n"
    "v_cvt_pk_bf16_f32 v74, v24, v25\n"
    "v_fma_f32 v203, v203, v235, v239\n"
    "v_mfma_f32_16x16x32_bf16 v[60:63], v[184:187], a[248:251], v[60:63]\n"
    "v_exp_f32 v43, v43\n"
    "v_mfma_f32_16x16x32_bf16 v[60:63], v[188:191], a[252:255], v[60:63]\n"
    "ds_read_b128 v[232:235], v249 offset:16\n"
    "v_cvt_pk_bf16_f32 v89, v42, v43\n"
    "v_mfma_f32_16x16x32_bf16 v[60:63], v[192:195], v[120:123], v[60:63]\n"
    "buffer_load_dwordx4 v1, s[16:19], s67 offen offset:3072 sc1 lds\n"
    "v_mfma_f32_16x16x32_bf16 v[60:63], v[196:199], v[124:127], v[60:63]\n"
    "s_add_u32 m0, s68, 4096\n"
    "s_add_u32 s83, s67, 4096\n"
    "v_cvt_pk_bf16_f32 v75, v26, v27\n"
    "v_mfma_f32_16x16x32_bf16 v[64:67], v[196:199], v[148:151], v[112:115]\n"
    "v_exp_f32 v44, v44\n"
    "v_mfma_f32_16x16x32_bf16 v[64:67], v[192:195], v[144:147], v[64:67]\n"
    "v_exp_f32 v45, v45\n"
    "v_mfma_f32_16x16x32_bf16 v[64:67], v[188:191], v[140:143], v[64:67]\n"
    "buffer_load_dwordx4 v1, s[16:19], s83 offen offset:0 sc1 lds\n"
    "v_mfma_f32_16x16x32_bf16 v[64:67], v[184:187], v[136:139], v[64:67]\n"
    "v_cvt_pk_bf16_f32 v78, v28, v29\n"
    "v_cvt_pk_bf16_f32 v92, v44, v45\n"
    "v_mfma_f32_16x16x32_bf16 v[64:67], v[180:183], v[132:135], v[64:67]\n"
    "v_exp_f32 v46, v46\n"
    "v_mfma_f32_16x16x32_bf16 v[64:67], v[176:179], v[128:131], v[64:67]\n"
    "s_waitcnt lgkmcnt(1)\n"
    "v_exp_f32 v47, v47\n"
    "v_mfma_f32_16x16x32_bf16 v[4:7], v[152:155], a[192:195], v[100:103]\n"
    "buffer_load_dwordx4 v1, s[16:19], s83 offen offset:1024 sc1 lds\n"
    "v_mfma_f32_16x16x32_bf16 v[4:7], v[156:159], a[196:199], v[4:7]\n"
    "ds_read_b128 v[176:179], v3 offset:1024\n"
    "ds_read_b128 v[180:183], v3 offset:3072\n"
    "v_mfma_f32_16x16x32_bf16 v[4:7], v[160:163], a[200:203], v[4:7]\n"
    "ds_read_b128 v[184:187], v3 offset:5120\n"
    "v_cvt_pk_bf16_f32 v79, v30, v31\n"
    "v_mfma_f32_16x16x32_bf16 v[4:7], v[164:167], a[204:207], v[4:7]\n"
    "ds_read_b128 v[188:191], v3 offset:7168\n"
    "v_cvt_pk_bf16_f32 v93, v46, v47\n"
    "v_mfma_f32_16x16x32_bf16 v[4:7], v[168:171], a[208:211], v[4:7]\n"
    "ds_read_b128 v[192:195], v3 offset:9216\n"
    "v_cvt_pk_bf16_f32 v82, v32, v33\n"
    "v_mfma_f32_16x16x32_bf16 v[4:7], v[172:175], a[212:215], v[4:7]\n"
    "ds_read_b128 v[196:199], v3 offset:11264\n"
    "ds_read_b128 v[236:239], v249 offset:112\n"
    "v_mfma_f32_16x16x32_bf16 v[8:11], v[172:175], a[236:239], v[104:107]\n"
    "v_exp_f32 v52, v52\n"
    "v_mfma_f32_16x16x32_bf16 v[8:11], v[168:171], a[232:235], v[8:11]\n"
    "v_exp_f32 v48, v48\n"
    "v_mfma_f32_16x16x32_bf16 v[8:11], v[164:167], a[228:231], v[8:11]\n"
    "v_cvt_pk_bf16_f32 v83, v34, v35\n"
    "v_cvt_f32_i32 v208, v208\n"
    "v_mfma_f32_16x16x32_bf16 v[8:11], v[160:163], a[224:227], v[8:11]\n"
    "v_exp_f32 v53, v53\n"
    "v_mfma_f32_16x16x32_bf16 v[8:11], v[156:159], a[220:223], v[8:11]\n"
    "v_cvt_pk_bf16_f32 v86, v52, v53\n"
    "v_cvt_f32_i32 v209, v209\n"
    "v_mfma_f32_16x16x32_bf16 v[8:11], v[152:155], a[216:219], v[8:11]\n"
    "v_exp_f32 v54, v54\n"
    "v_mfma_f32_16x16x32_bf16 v[12:15], v[152:155], a[240:243], v[108:111]\n"
    "v_exp_f32 v49, v49\n"
    "v_mfma_f32_16x16x32_bf16 v[12:15], v[156:159], a[244:247], v[12:15]\n"
    "v_exp_f32 v50, v50\n"
    "v_mfma_f32_16x16x32_bf16 v[12:15], v[160:163], a[248:251], v[12:15]\n"
    "v_exp_f32 v55, v55\n"
    "v_mfma_f32_16x16x32_bf16 v[12:15], v[164:167], a[252:255], v[12:15]\n"
    "v_cvt_pk_bf16_f32 v87, v54, v55\n"
    "v_cvt_pk_bf16_f32 v96, v48, v49\n"
    "v_mfma_f32_16x16x32_bf16 v[12:15], v[168:171], v[120:123], v[12:15]\n"
    "v_exp_f32 v51, v51\n"
    "v_mfma_f32_16x16x32_bf16 v[12:15], v[172:175], v[124:127], v[12:15]\n"
    "v_exp_f32 v56, v56\n"
    "v_mfma_f32_16x16x32_bf16 v[16:19], v[172:175], v[148:151], v[112:115]\n"
    "v_cvt_pk_bf16_f32 v97, v50, v51\n"
    "v_cvt_f32_i32 v210, v210\n"
    "v_mfma_f32_16x16x32_bf16 v[16:19], v[168:171], v[144:147], v[16:19]\n"
    "v_cvt_f32_i32 v211, v211\n"
    "v_cvt_f32_i32 v212, v212\n"
    "v_mfma_f32_16x16x32_bf16 v[16:19], v[164:167], v[140:143], v[16:19]\n"
    "v_exp_f32 v57, v57\n"
    "v_mfma_f32_16x16x32_bf16 v[16:19], v[160:163], v[136:139], v[16:19]\n"
    "v_cvt_pk_bf16_f32 v90, v56, v57\n"
    "v_cvt_f32_i32 v213, v213\n"
    "v_mfma_f32_16x16x32_bf16 v[16:19], v[156:159], v[132:135], v[16:19]\n"
    "v_exp_f32 v4, v4\n"
    "v_mfma_f32_16x16x32_bf16 v[16:19], v[152:155], v[128:131], v[16:19]\n"
    "s_waitcnt lgkmcnt(1)\n"
    "v_exp_f32 v58, v58\n"
    "v_mfma_f32_16x16x32_bf16 v[20:23], v[176:179], a[192:195], v[100:103]\n"
    "ds_read_b128 v[152:155], v2 offset:24576\n"
    "v_add_f32 v116, v116, v4\n"
    "v_mfma_f32_16x16x32_bf16 v[20:23], v[180:183], a[196:199], v[20:23]\n"
    "ds_read_b128 v[156:159], v2 offset:36864\n"
    "v_cvt_f32_i32 v214, v214\n"
    "v_mfma_f32_16x16x32_bf16 v[20:23], v[184:187], a[200:203], v[20:23]\n"
    "ds_read_b128 v[160:163], v2 offset:25600\n"
    "v_cvt_f32_i32 v215, v215\n"
    "v_mfma_f32_16x16x32_bf16 v[20:23], v[188:191], a[204:207], v[20:23]\n"
    "s_waitcnt lgkmcnt(3)\n"
    "ds_read_b128 v[164:167], v2 offset:37888\n"
    "v_fma_f32 v204, v204, v232, v236\n"
    "v_mfma_f32_16x16x32_bf16 v[20:23], v[192:195], a[208:211], v[20:23]\n"
    "ds_read_b128 v[168:171], v2 offset:26624\n"
    "v_fma_f32 v205, v205, v233, v237\n"
    "v_mfma_f32_16x16x32_bf16 v[20:23], v[196:199], a[212:215], v[20:23]\n"
    "ds_read_b128 v[172:175], v2 offset:38912\n"
    "v_add_f32 v116, v116, v52\n"
    "v_mfma_f32_16x16x32_bf16 v[24:27], v[196:199], a[236:239], v[104:107]\n"
    "v_exp_f32 v59, v59\n"
    "v_mfma_f32_16x16x32_bf16 v[24:27], v[192:195], a[232:235], v[24:27]\n"
    "v_cvt_pk_bf16_f32 v91, v58, v59\n"
    "v_add_f32 v116, v116, v53\n"
    "v_mfma_f32_16x16x32_bf16 v[24:27], v[188:191], a[228:231], v[24:27]\n"
    "v_exp_f32 v60, v60\n"
    "v_mfma_f32_16x16x32_bf16 v[24:27], v[184:187], a[224:227], v[24:27]\n"
    "v_exp_f32 v61, v61\n"
    "v_mfma_f32_16x16x32_bf16 v[24:27], v[180:183], a[220:223], v[24:27]\n"
    "v_exp_f32 v62, v62\n"
    "v_mfma_f32_16x16x32_bf16 v[24:27], v[176:179], a[216:219], v[24:27]\n"
    "v_cvt_pk_bf16_f32 v94, v60, v61\n"
    "v_add_f32 v116, v116, v54\n"
    "v_mfma_f32_16x16x32_bf16 v[28:31], v[176:179], a[240:243], v[108:111]\n"
    "v_exp_f32 v5, v5\n"
    "v_mfma_f32_16x16x32_bf16 v[28:31], v[180:183], a[244:247], v[28:31]\n"
    "v_exp_f32 v63, v63\n"
    "v_mfma_f32_16x16x32_bf16 v[28:31], v[184:187], a[248:251], v[28:31]\n"
    "v_cvt_pk_bf16_f32 v95, v62, v63\n"
    "v_add_f32 v116, v116, v5\n"
    "v_mfma_f32_16x16x32_bf16 v[28:31], v[188:191], a[252:255], v[28:31]\n"
    "v_exp_f32 v6, v6\n"
    "v_mfma_f32_16x16x32_bf16 v[28:31], v[192:195], v[120:123], v[28:31]\n"
    "v_exp_f32 v64, v64\n"
    "v_mfma_f32_16x16x32_bf16 v[28:31], v[196:199], v[124:127], v[28:31]\n"
    "v_exp_f32 v7, v7\n"
    "v_mfma_f32_16x16x32_bf16 v[32:35], v[196:199], v[148:151], v[112:115]\n"
    "v_exp_f32 v8, v8\n"
    "v_mfma_f32_16x16x32_bf16 v[32:35], v[192:195], v[144:147], v[32:35]\n"
    "v_exp_f32 v65, v65\n"
    "v_mfma_f32_16x16x32_bf16 v[32:35], v[188:191], v[140:143], v[32:35]\n"
    "v_cvt_pk_bf16_f32 v98, v64, v65\n"
    "v_add_f32 v116, v116, v6\n"
    "v_mfma_f32_16x16x32_bf16 v[32:35], v[184:187], v[136:139], v[32:35]\n"
    "v_exp_f32 v9, v9\n"
    "v_mfma_f32_16x16x32_bf16 v[32:35], v[180:183], v[132:135], v[32:35]\n"
    "s_add_u32 m0, s68, 24576\n"
    "s_add_u32 s83, s67, 24576\n"
    "v_add_f32 v116, v116, v7\n"
    "v_mfma_f32_16x16x32_bf16 v[32:35], v[176:179], v[128:131], v[32:35]\n"
    "s_waitcnt lgkmcnt(0)\n"
    "v_exp_f32 v66, v66\n"
    "v_mfma_f32_16x16x32_bf16 a[0:3], v[152:155], v[68:71], a[0:3]\n"
    "ds_read_b128 v[176:179], v2 offset:27648\n"
    "v_add_f32 v117, v117, v8\n"
    "v_mfma_f32_16x16x32_bf16 a[0:3], v[156:159], v[84:87], a[0:3]\n"
    "ds_read_b128 v[180:183], v2 offset:39936\n"
    "v_add_f32 v117, v117, v9\n"
    "v_mfma_f32_16x16x32_bf16 a[16:19], v[164:167], v[84:87], a[16:19]\n"
    "ds_read_b128 v[184:187], v2 offset:28672\n"
    "v_add_f32 v116, v116, v55\n"
    "v_mfma_f32_16x16x32_bf16 a[16:19], v[160:163], v[68:71], a[16:19]\n"
    "ds_read_b128 v[188:191], v2 offset:40960\n"
    "v_fma_f32 v206, v206, v234, v238\n"
    "v_mfma_f32_16x16x32_bf16 a[32:35], v[168:171], v[68:71], a[32:35]\n"
    "buffer_load_dwordx4 v1, s[16:19], s83 offen offset:0 sc1 lds\n"
    "v_mfma_f32_16x16x32_bf16 a[32:35], v[172:175], v[84:87], a[32:35]\n"
    "ds_read_b128 v[192:195], v2 offset:29696\n"
    "ds_read_b128 v[196:199], v2 offset:41984\n"
    "v_mfma_f32_16x16x32_bf16 a[36:39], v[172:175], v[88:91], a[36:39]\n"
    "v_exp_f32 v67, v67\n"
    "v_mfma_f32_16x16x32_bf16 a[36:39], v[168:171], v[72:75], a[36:39]\n"
    "v_cvt_pk_bf16_f32 v99, v66, v67\n"
    "v_fma_f32 v207, v207, v235, v239\n"
    "v_mfma_f32_16x16x32_bf16 a[20:23], v[160:163], v[72:75], a[20:23]\n"
    "v_exp_f32 v10, v10\n"
    "v_mfma_f32_16x16x32_bf16 a[20:23], v[164:167], v[88:91], a[20:23]\n"
    "v_exp_f32 v11, v11\n"
    "v_mfma_f32_16x16x32_bf16 a[4:7], v[156:159], v[88:91], a[4:7]\n"
    "buffer_load_dwordx4 v1, s[16:19], s83 offen offset:1024 sc1 lds\n"
    "v_mfma_f32_16x16x32_bf16 a[4:7], v[152:155], v[72:75], a[4:7]\n"
    "v_exp_f32 v12, v12\n"
    "v_mfma_f32_16x16x32_bf16 a[8:11], v[152:155], v[76:79], a[8:11]\n"
    "v_exp_f32 v13, v13\n"
    "v_mfma_f32_16x16x32_bf16 a[8:11], v[156:159], v[92:95], a[8:11]\n"
    "v_add_f32 v117, v117, v10\n"
    "v_add_f32 v117, v117, v11\n"
    "v_mfma_f32_16x16x32_bf16 a[24:27], v[164:167], v[92:95], a[24:27]\n"
    "v_exp_f32 v14, v14\n"
    "v_mfma_f32_16x16x32_bf16 a[24:27], v[160:163], v[76:79], a[24:27]\n"
    "v_add_f32 v117, v117, v41\n"
    "v_add_f32 v117, v117, v42\n"
    "v_mfma_f32_16x16x32_bf16 a[40:43], v[168:171], v[76:79], a[40:43]\n"
    "buffer_load_dwordx4 v1, s[16:19], s83 offen offset:2048 sc1 lds\n"
    "v_mfma_f32_16x16x32_bf16 a[40:43], v[172:175], v[92:95], a[40:43]\n"
    "v_add_f32 v117, v117, v43\n"
    "v_add_f32 v117, v117, v56\n"
    "v_mfma_f32_16x16x32_bf16 a[44:47], v[172:175], v[96:99], a[44:47]\n"
    "v_add_f32 v117, v117, v57\n"
    "v_add_f32 v117, v117, v58\n"
    "v_mfma_f32_16x16x32_bf16 a[44:47], v[168:171], v[80:83], a[44:47]\n"
    "ds_read_b128 v[232:235], v249 offset:32\n"
    "v_add_f32 v117, v117, v59\n"
    "v_mfma_f32_16x16x32_bf16 a[28:31], v[160:163], v[80:83], a[28:31]\n"
    "v_exp_f32 v15, v15\n"
    "v_mfma_f32_16x16x32_bf16 a[28:31], v[164:167], v[96:99], a[28:31]\n"
    "ds_read_b128 v[236:239], v249 offset:128\n"
    "v_add_f32 v118, v118, v12\n"
    "v_mfma_f32_16x16x32_bf16 a[12:15], v[156:159], v[96:99], a[12:15]\n"
    "buffer_load_dwordx4 v1, s[16:19], s83 offen offset:3072 sc1 lds\n"
    "v_mfma_f32_16x16x32_bf16 a[12:15], v[152:155], v[80:83], a[12:15]\n"
    "s_waitcnt lgkmcnt(2)\n"
    "s_add_u32 m0, s68, 28672\n"
    "s_add_u32 s83, s67, 28672\n"
    "v_mfma_f32_16x16x32_bf16 a[48:51], v[176:179], v[68:71], a[48:51]\n"
    "ds_read_b128 v[152:155], v2 offset:30720\n"
    "v_add_f32 v118, v118, v44\n"
    "v_mfma_f32_16x16x32_bf16 a[48:51], v[180:183], v[84:87], a[48:51]\n"
    "ds_read_b128 v[156:159], v2 offset:43008\n"
    "v_add_f32 v118, v118, v45\n"
    "v_mfma_f32_16x16x32_bf16 a[64:67], v[188:191], v[84:87], a[64:67]\n"
    "ds_read_b128 v[160:163], v2 offset:31744\n"
    "v_add_f32 v118, v118, v46\n"
    "v_mfma_f32_16x16x32_bf16 a[64:67], v[184:187], v[68:71], a[64:67]\n"
    "ds_read_b128 v[164:167], v2 offset:44032\n"
    "v_add_f32 v118, v118, v47\n"
    "v_mfma_f32_16x16x32_bf16 a[80:83], v[192:195], v[68:71], a[80:83]\n"
    "buffer_load_dwordx4 v1, s[16:19], s83 offen offset:0 sc1 lds\n"
    "v_mfma_f32_16x16x32_bf16 a[80:83], v[196:199], v[84:87], a[80:83]\n"
    "ds_read_b128 v[168:171], v2 offset:32768\n"
    "ds_read_b128 v[172:175], v2 offset:45056\n"
    "v_mfma_f32_16x16x32_bf16 a[84:87], v[196:199], v[88:91], a[84:87]\n"
    "v_exp_f32 v16, v16\n"
    "v_mfma_f32_16x16x32_bf16 a[84:87], v[192:195], v[72:75], a[84:87]\n"
    "v_add_f32 v118, v118, v60\n"
    "v_add_f32 v118, v118, v61\n"
    "v_mfma_f32_16x16x32_bf16 a[68:71], v[184:187], v[72:75], a[68:71]\n"
    "v_add_f32 v118, v118, v62\n"
    "v_add_f32 v118, v118, v63\n"
    "v_mfma_f32_16x16x32_bf16 a[68:71], v[188:191], v[88:91], a[68:71]\n"
    "v_add_f32 v118, v118, v13\n"
    "v_cvt_pk_bf16_f32 v200, v200, v201\n"
    "v_mfma_f32_16x16x32_bf16 a[52:55], v[180:183], v[88:91], a[52:55]\n"
    "buffer_load_dwordx4 v1, s[16:19], s83 offen offset:1024 sc1 lds\n"
    "v_mfma_f32_16x16x32_bf16 a[52:55], v[176:179], v[72:75], a[52:55]\n"
    "s_add_u32 s67, s67, 49152\n"
    "v_exp_f32 v17, v17\n"
    "v_mfma_f32_16x16x32_bf16 a[56:59], v[176:179], v[76:79], a[56:59]\n"
    "v_add_f32 v118, v118, v14\n"
    "v_cvt_pk_bf16_f32 v201, v202, v203\n"
    "v_mfma_f32_16x16x32_bf16 a[56:59], v[180:183], v[92:95], a[56:59]\n"
    "v_exp_f32 v18, v18\n"
    "v_mfma_f32_16x16x32_bf16 a[72:75], v[188:191], v[92:95], a[72:75]\n"
    "v_add_f32 v119, v119, v48\n"
    "v_add_f32 v119, v119, v49\n"
    "v_mfma_f32_16x16x32_bf16 a[72:75], v[184:187], v[76:79], a[72:75]\n"
    "v_add_f32 v119, v119, v50\n"
    "v_add_f32 v119, v119, v51\n"
    "v_mfma_f32_16x16x32_bf16 a[88:91], v[192:195], v[76:79], a[88:91]\n"
    "v_add_f32 v119, v119, v64\n"
    "v_add_f32 v119, v119, v65\n"
    "v_mfma_f32_16x16x32_bf16 a[88:91], v[196:199], v[92:95], a[88:91]\n"
    "v_add_f32 v119, v119, v66\n"
    "v_add_f32 v119, v119, v67\n"
    "v_mfma_f32_16x16x32_bf16 a[92:95], v[196:199], v[96:99], a[92:95]\n"
    "v_cvt_pk_bf16_f32 v202, v204, v205\n"
    "v_add_f32 v118, v118, v15\n"
    "v_mfma_f32_16x16x32_bf16 a[92:95], v[192:195], v[80:83], a[92:95]\n"
    "v_exp_f32 v19, v19\n"
    "v_mfma_f32_16x16x32_bf16 a[76:79], v[184:187], v[80:83], a[76:79]\n"
    "v_cvt_pk_bf16_f32 v203, v206, v207\n"
    "v_add_f32 v119, v119, v16\n"
    "v_mfma_f32_16x16x32_bf16 a[76:79], v[188:191], v[96:99], a[76:79]\n"
    "v_exp_f32 v20, v20\n"
    "v_mfma_f32_16x16x32_bf16 a[60:63], v[180:183], v[96:99], a[60:63]\n"
    "s_waitcnt lgkmcnt(6)\n"
    "v_fma_f32 v208, v208, v232, v236\n"
    "v_add_f32 v119, v119, v17\n"
    "v_mfma_f32_16x16x32_bf16 a[60:63], v[176:179], v[80:83], a[60:63]\n"
    "s_waitcnt lgkmcnt(0)\n"
    "v_exp_f32 v21, v21\n"
    "v_mfma_f32_16x16x32_bf16 a[96:99], v[152:155], v[68:71], a[96:99]\n"
    "ds_read_b128 v[176:179], v2 offset:33792\n"
    "v_fma_f32 v209, v209, v233, v237\n"
    "v_mfma_f32_16x16x32_bf16 a[96:99], v[156:159], v[84:87], a[96:99]\n"
    "ds_read_b128 v[180:183], v2 offset:46080\n"
    "v_add_f32 v119, v119, v18\n"
    "v_mfma_f32_16x16x32_bf16 a[112:115], v[164:167], v[84:87], a[112:115]\n"
    "ds_read_b128 v[184:187], v2 offset:34816\n"
    "v_fma_f32 v210, v210, v234, v238\n"
    "v_mfma_f32_16x16x32_bf16 a[112:115], v[160:163], v[68:71], a[112:115]\n"
    "ds_read_b128 v[188:191], v2 offset:47104\n"
    "v_add_f32 v119, v119, v19\n"
    "v_mfma_f32_16x16x32_bf16 a[128:131], v[168:171], v[68:71], a[128:131]\n"
    "ds_read_b128 v[192:195], v2 offset:35840\n"
    "v_fma_f32 v211, v211, v235, v239\n"
    "v_mfma_f32_16x16x32_bf16 a[128:131], v[172:175], v[84:87], a[128:131]\n"
    "ds_read_b128 v[196:199], v2 offset:48128\n"
    "ds_read_b128 v[232:235], v249 offset:48\n"
    "v_mfma_f32_16x16x32_bf16 a[132:135], v[172:175], v[88:91], a[132:135]\n"
    "buffer_load_dwordx2 v[240:241], v245, s[36:39], s81 offen offset:0 sc1\n"
    "v_mfma_f32_16x16x32_bf16 a[132:135], v[168:171], v[72:75], a[132:135]\n"
    "v_exp_f32 v22, v22\n"
    "v_mfma_f32_16x16x32_bf16 a[116:119], v[160:163], v[72:75], a[116:119]\n"
    "v_exp_f32 v23, v23\n"
    "v_mfma_f32_16x16x32_bf16 a[116:119], v[164:167], v[88:91], a[116:119]\n"
    "ds_read_b128 v[236:239], v249 offset:144\n"
    "v_add_f32 v116, v116, v20\n"
    "v_mfma_f32_16x16x32_bf16 a[100:103], v[156:159], v[88:91], a[100:103]\n"
    "v_exp_f32 v24, v24\n"
    "v_mfma_f32_16x16x32_bf16 a[100:103], v[152:155], v[72:75], a[100:103]\n"
    "v_add_f32 v116, v116, v21\n"
    "v_cvt_f32_i32 v216, v216\n"
    "v_mfma_f32_16x16x32_bf16 a[104:107], v[152:155], v[76:79], a[104:107]\n"
    "v_exp_f32 v25, v25\n"
    "v_mfma_f32_16x16x32_bf16 a[104:107], v[156:159], v[92:95], a[104:107]\n"
    "v_add_f32 v116, v116, v22\n"
    "v_cvt_f32_i32 v217, v217\n"
    "v_mfma_f32_16x16x32_bf16 a[120:123], v[164:167], v[92:95], a[120:123]\n"
    "v_exp_f32 v26, v26\n"
    "v_mfma_f32_16x16x32_bf16 a[120:123], v[160:163], v[76:79], a[120:123]\n"
    "v_add_f32 v116, v116, v23\n"
    "v_cvt_f32_i32 v218, v218\n"
    "v_mfma_f32_16x16x32_bf16 a[136:139], v[168:171], v[76:79], a[136:139]\n"
    "v_exp_f32 v27, v27\n"
    "v_mfma_f32_16x16x32_bf16 a[136:139], v[172:175], v[92:95], a[136:139]\n"
    "v_add_f32 v117, v117, v24\n"
    "v_cvt_f32_i32 v219, v219\n"
    "v_mfma_f32_16x16x32_bf16 a[140:143], v[172:175], v[96:99], a[140:143]\n"
    "v_exp_f32 v28, v28\n"
    "v_mfma_f32_16x16x32_bf16 a[140:143], v[168:171], v[80:83], a[140:143]\n"
    "v_add_f32 v117, v117, v25\n"
    "v_cvt_f32_i32 v220, v220\n"
    "v_mfma_f32_16x16x32_bf16 a[124:127], v[160:163], v[80:83], a[124:127]\n"
    "v_exp_f32 v29, v29\n"
    "v_mfma_f32_16x16x32_bf16 a[124:127], v[164:167], v[96:99], a[124:127]\n"
    "v_cvt_f32_i32 v221, v221\n"
    "v_add_f32 v117, v117, v26\n"
    "v_mfma_f32_16x16x32_bf16 a[108:111], v[156:159], v[96:99], a[108:111]\n"
    "v_exp_f32 v30, v30\n"
    "v_mfma_f32_16x16x32_bf16 a[108:111], v[152:155], v[80:83], a[108:111]\n"
    "s_waitcnt lgkmcnt(2)\n"
    "v_cvt_f32_i32 v222, v222\n"
    "v_add_f32 v117, v117, v27\n"
    "v_mfma_f32_16x16x32_bf16 a[144:147], v[176:179], v[68:71], a[144:147]\n"
    "ds_read_b128 v[152:155], v3 offset:12288\n"
    "v_cvt_f32_i32 v223, v223\n"
    "v_mfma_f32_16x16x32_bf16 a[144:147], v[180:183], v[84:87], a[144:147]\n"
    "ds_read_b128 v[156:159], v3 offset:14336\n"
    "v_add_f32 v118, v118, v28\n"
    "v_mfma_f32_16x16x32_bf16 a[160:163], v[188:191], v[84:87], a[160:163]\n"
    "s_waitcnt lgkmcnt(2)\n"
    "ds_read_b128 v[160:163], v3 offset:16384\n"
    "v_fma_f32 v212, v212, v232, v236\n"
    "v_mfma_f32_16x16x32_bf16 a[160:163], v[184:187], v[68:71], a[160:163]\n"
    "ds_read_b128 v[164:167], v3 offset:18432\n"
    "v_add_f32 v118, v118, v29\n"
    "v_mfma_f32_16x16x32_bf16 a[176:179], v[192:195], v[68:71], a[176:179]\n"
    "ds_read_b128 v[168:171], v3 offset:20480\n"
    "v_fma_f32 v213, v213, v233, v237\n"
    "v_mfma_f32_16x16x32_bf16 a[176:179], v[196:199], v[84:87], a[176:179]\n"
    "ds_read_b128 v[172:175], v3 offset:22528\n"
    "v_add_f32 v118, v118, v30\n"
    "v_mfma_f32_16x16x32_bf16 a[180:183], v[196:199], v[88:91], a[180:183]\n"
    "v_exp_f32 v31, v31\n"
    "v_mfma_f32_16x16x32_bf16 a[180:183], v[192:195], v[72:75], a[180:183]\n"
    "v_exp_f32 v32, v32\n"
    "v_mfma_f32_16x16x32_bf16 a[164:167], v[184:187], v[72:75], a[164:167]\n"
    "v_exp_f32 v33, v33\n"
    "v_mfma_f32_16x16x32_bf16 a[164:167], v[188:191], v[88:91], a[164:167]\n"
    "v_fma_f32 v214, v214, v234, v238\n"
    "v_fma_f32 v215, v215, v235, v239\n"
    "v_mfma_f32_16x16x32_bf16 a[148:151], v[180:183], v[88:91], a[148:151]\n"
    "v_exp_f32 v34, v34\n"
    "v_mfma_f32_16x16x32_bf16 a[148:151], v[176:179], v[72:75], a[148:151]\n"
    "v_add_f32 v118, v118, v31\n"
    "v_add_f32 v119, v119, v32\n"
    "v_mfma_f32_16x16x32_bf16 a[152:155], v[176:179], v[76:79], a[152:155]\n"
    "v_exp_f32 v35, v35\n"
    "v_mfma_f32_16x16x32_bf16 a[152:155], v[180:183], v[92:95], a[152:155]\n"
    "v_add_f32 v119, v119, v33\n"
    "v_add_f32 v119, v119, v34\n"
    "v_mfma_f32_16x16x32_bf16 a[168:171], v[188:191], v[92:95], a[168:171]\n"
    "s_waitcnt vmcnt(7)\n"
    "ds_add_u32 v252, v251\n"
    "v_mfma_f32_16x16x32_bf16 a[168:171], v[184:187], v[76:79], a[168:171]\n"
    "v_add_f32 v119, v119, v35\n"
    "v_mfma_f32_16x16x32_bf16 a[184:187], v[192:195], v[76:79], a[184:187]\n"
    "v_mfma_f32_16x16x32_bf16 a[184:187], v[196:199], v[92:95], a[184:187]\n"
    "v_mfma_f32_16x16x32_bf16 a[188:191], v[196:199], v[96:99], a[188:191]\n"
    "v_mfma_f32_16x16x32_bf16 a[188:191], v[192:195], v[80:83], a[188:191]\n"
    "v_mfma_f32_16x16x32_bf16 a[172:175], v[184:187], v[80:83], a[172:175]\n"
    "s_sub_u32 s74, s68, s65\n"
    "v_mfma_f32_16x16x32_bf16 a[172:175], v[188:191], v[96:99], a[172:175]\n"
    "s_add_u32 s66, s66, 1\n"
    "v_mov_b32 v2, v3\n"
    "v_add_u32 v3, s74, v1\n"
    "v_mfma_f32_16x16x32_bf16 a[156:159], v[180:183], v[96:99], a[156:159]\n"
    "s_waitcnt lgkmcnt(1)\n"
    "s_cmp_lt_u32 s66, 31\n"
    "v_mfma_f32_16x16x32_bf16 a[156:159], v[176:179], v[80:83], a[156:159]\n"
    "s_cbranch_scc1 .LPK\n"
    "s_branch .Ltail\n"
    ".LPV:\n"
    "v_mfma_f32_16x16x32_bf16 v[36:39], v[152:155], a[192:195], v[100:103]\n"
    "s_add_u32 s76, s71, s60\n"
    "ds_read_b128 v[176:179], v2 offset:13312\n"
    "v_cvt_pk_bf16_f32 v68, v4, v5\n"
    "v_mfma_f32_16x16x32_bf16 v[36:39], v[156:159], a[196:199], v[36:39]\n"
    "ds_read_b128 v[180:183], v2 offset:15360\n"
    "v_mfma_f32_16x16x32_bf16 v[36:39], v[160:163], a[200:203], v[36:39]\n"
    "s_add_u32 s68, s68, 49152\n"
    "s_cmp_ge_u32 s68, s69\n"
    "s_cselect_b32 s68, s65, s68\n"
    "v_mfma_f32_16x16x32_bf16 v[36:39], v[164:167], a[204:207], v[36:39]\n"
    "s_add_u32 s77, s71, s61\n"
    "ds_read_b128 v[184:187], v2 offset:17408\n"
    "ds_read_b128 v[188:191], v2 offset:19456\n"
    "v_mfma_f32_16x16x32_bf16 v[36:39], v[168:171], a[208:211], v[36:39]\n"
    "s_add_u32 s78, s71, s62\n"
    "ds_read_b128 v[192:195], v2 offset:21504\n"
    "v_cvt_pk_bf16_f32 v69, v6, v7\n"
    "v_mfma_f32_16x16x32_bf16 v[36:39], v[172:175], a[212:215], v[36:39]\n"
    "ds_read_b128 v[196:199], v2 offset:23552\n"
    "v_mfma_f32_16x16x32_bf16 v[40:43], v[172:175], a[236:239], v[104:107]\n"
    "v_cvt_pk_bf16_f32 v200, v200, v204\n"
    "v_mfma_f32_16x16x32_bf16 v[40:43], v[168:171], a[232:235], v[40:43]\n"
    "v_cvt_pk_bf16_f32 v72, v8, v9\n"
    "v_mfma_f32_16x16x32_bf16 v[40:43], v[164:167], a[228:231], v[40:43]\n"
    "s_add_u32 s80, s79, 256\n"
    "v_cvt_pk_bf16_f32 v204, v201, v205\n"
    "v_mfma_f32_16x16x32_bf16 v[40:43], v[160:163], a[224:227], v[40:43]\n"
    "s_add_u32 s79, s79, 128\n"
    "v_exp_f32 v36, v36\n"
    "v_mfma_f32_16x16x32_bf16 v[40:43], v[156:159], a[220:223], v[40:43]\n"
    "v_cvt_pk_bf16_f32 v232, v202, v206\n"
    "v_add_f32 v116, v116, v36\n"
    "v_mfma_f32_16x16x32_bf16 v[40:43], v[152:155], a[216:219], v[40:43]\n"
    "v_cvt_pk_bf16_f32 v73, v10, v11\n"
    "v_mfma_f32_16x16x32_bf16 v[44:47], v[152:155], a[240:243], v[108:111]\n"
    "v_cvt_pk_bf16_f32 v201, v208, v212\n"
    "v_mfma_f32_16x16x32_bf16 v[44:47], v[156:159], a[244:247], v[44:47]\n"
    "v_exp_f32 v37, v37\n"
    "v_mfma_f32_16x16x32_bf16 v[44:47], v[160:163], a[248:251], v[44:47]\n"
    "v_cvt_pk_bf16_f32 v205, v209, v213\n"
    "v_cvt_pk_bf16_f32 v84, v36, v37\n"
    "v_mfma_f32_16x16x32_bf16 v[44:47], v[164:167], a[252:255], v[44:47]\n"
    "v_cvt_pk_bf16_f32 v76, v12, v13\n"
    "v_add_f32 v116, v116, v37\n"
    "v_mfma_f32_16x16x32_bf16 v[44:47], v[168:171], v[120:123], v[44:47]\n"
    "v_cvt_pk_bf16_f32 v233, v210, v214\n"
    "v_mfma_f32_16x16x32_bf16 v[44:47], v[172:175], v[124:127], v[44:47]\n"
    "v_exp_f32 v38, v38\n"
    "v_mfma_f32_16x16x32_bf16 v[48:51], v[172:175], v[148:151], v[112:115]\n"
    "ds_read_b32 v250, v252\n"
    "v_cvt_pk_bf16_f32 v202, v216, v220\n"
    "v_mfma_f32_16x16x32_bf16 v[48:51], v[168:171], v[144:147], v[48:51]\n"
    "v_cvt_pk_bf16_f32 v77, v14, v15\n"
    "v_add_f32 v116, v116, v38\n"
    "v_mfma_f32_16x16x32_bf16 v[48:51], v[164:167], v[140:143], v[48:51]\n"
    "v_cvt_pk_bf16_f32 v206, v217, v221\n"
    "v_mfma_f32_16x16x32_bf16 v[48:51], v[160:163], v[136:139], v[48:51]\n"
    "v_exp_f32 v39, v39\n"
    "v_mfma_f32_16x16x32_bf16 v[48:51], v[156:159], v[132:135], v[48:51]\n"
    "s_add_u32 m0, s68, 0\n"
    "v_cvt_pk_bf16_f32 v234, v218, v222\n"
    "v_cvt_pk_bf16_f32 v80, v16, v17\n"
    "v_mfma_f32_16x16x32_bf16 v[48:51], v[152:155], v[128:131], v[48:51]\n"
    "s_waitcnt vmcnt(0)\n"
    "s_waitcnt lgkmcnt(1)\n"
    "v_cmp_eq_u64 s[54:55], v[240:241], v[242:243]\n"
    "v_mfma_f32_16x16x32_bf16 v[52:55], v[176:179], a[192:195], v[100:103]\n"
    "s_waitcnt lgkmcnt(0)\n"
    "v_cmp_le_u32 s[56:57], s59, v250\n"
    "v_cvt_pk_bf16_f32 v203, v224, v228\n"
    "v_mfma_f32_16x16x32_bf16 v[52:55], v[180:183], a[196:199], v[52:55]\n"
    "v_cvt_pk_bf16_f32 v85, v38, v39\n"
    "v_add_f32 v116, v116, v39\n"
    "v_mfma_f32_16x16x32_bf16 v[52:55], v[184:187], a[200:203], v[52:55]\n"
    "s_and_b64 s[54:55], s[54:55], s[56:57]\n"
    "s_cmp_eq_u64 s[54:55], -1\n"
    "v_mfma_f32_16x16x32_bf16 v[52:55], v[188:191], a[204:207], v[52:55]\n"
    "s_cbranch_scc0 .Lpoll3\n"
    ".Lpoll3r:\n"
    "v_mfma_f32_16x16x32_bf16 v[52:55], v[192:195], a[208:211], v[52:55]\n"
    "buffer_load_dwordx4 v1, s[16:19], s67 offen offset:0 sc1 lds\n"
    "v_mfma_f32_16x16x32_bf16 v[52:55], v[196:199], a[212:215], v[52:55]\n"
    "s_add_u32 s59, s59, 4\n"
    "ds_read_b128 v[152:155], v3 offset:0\n"
    "ds_read_b128 v[156:159], v3 offset:2048\n"
    "v_mfma_f32_16x16x32_bf16 v[56:59], v[196:199], a[236:239], v[104:107]\n"
    "ds_read_b128 v[160:163], v3 offset:4096\n"
    "ds_read_b128 v[164:167], v3 offset:6144\n"
    "v_mfma_f32_16x16x32_bf16 v[56:59], v[192:195], a[232:235], v[56:59]\n"
    "s_min_u32 s81, s79, 3968\n"
    "ds_read_b128 v[168:171], v3 offset:8192\n"
    "ds_read_b128 v[172:175], v3 offset:10240\n"
    "v_mfma_f32_16x16x32_bf16 v[56:59], v[188:191], a[228:231], v[56:59]\n"
    "buffer_load_dwordx4 v1, s[16:19], s67 offen offset:1024 sc1 lds\n"
    "v_mfma_f32_16x16x32_bf16 v[56:59], v[184:187], a[224:227], v[56:59]\n"
    "v_cvt_pk_bf16_f32 v207, v225, v229\n"
    "v_cvt_pk_bf16_f32 v235, v226, v230\n"
    "v_mfma_f32_16x16x32_bf16 v[56:59], v[180:183], a[220:223], v[56:59]\n"
    "buffer_store_dwordx4 v[200:203], v248, s[16:19], s76 offen offset:0 sc1\n"
    "v_mfma_f32_16x16x32_bf16 v[56:59], v[176:179], a[216:219], v[56:59]\n"
    "v_cvt_pk_bf16_f32 v81, v18, v19\n"
    "v_cvt_pk_bf16_f32 v70, v20, v21\n"
    "v_mfma_f32_16x16x32_bf16 v[60:63], v[176:179], a[240:243], v[108:111]\n"
    "buffer_load_dwordx4 v1, s[16:19], s67 offen offset:2048 sc1 lds\n"
    "v_mfma_f32_16x16x32_bf16 v[60:63], v[180:183], a[244:247], v[60:63]\n"
    "buffer_store_dwordx4 v[204:207], v248, s[16:19], s77 offen offset:0 sc1\n"
    "v_mfma_f32_16x16x32_bf16 v[60:63], v[184:187], a[248:251], v[60:63]\n"
    "v_cvt_pk_bf16_f32 v71, v22, v23\n"
    "v_cvt_pk_bf16_f32 v74, v24, v25\n"
    "v_mfma_f32_16x16x32_bf16 v[60:63], v[188:191], a[252:255], v[60:63]\n"
    "buffer_store_dwordx4 v[232:235], v248, s[16:19], s78 offen offset:0 sc1\n"
    "v_mfma_f32_16x16x32_bf16 v[60:63], v[192:195], v[120:123], v[60:63]\n"
    "buffer_load_dwordx4 v1, s[16:19], s67 offen offset:3072 sc1 lds\n"
    "v_mfma_f32_16x16x32_bf16 v[60:63], v[196:199], v[124:127], v[60:63]\n"
    "s_add_u32 m0, s68, 4096\n"
    "s_add_u32 s83, s67, 4096\n"
    "v_cvt_pk_bf16_f32 v75, v26, v27\n"
    "v_mfma_f32_16x16x32_bf16 v[64:67], v[196:199], v[148:151], v[112:115]\n"
    "s_add_u32 s71, s71, 98304\n"
    "v_exp_f32 v40, v40\n"
    "v_mfma_f32_16x16x32_bf16 v[64:67], v[192:195], v[144:147], v[64:67]\n"
    "s_add_u32 s75, s71, s63\n"
    "v_exp_f32 v41, v41\n"
    "v_mfma_f32_16x16x32_bf16 v[64:67], v[188:191], v[140:143], v[64:67]\n"
    "buffer_load_dwordx4 v1, s[16:19], s83 offen offset:0 sc1 lds\n"
    "v_mfma_f32_16x16x32_bf16 v[64:67], v[184:187], v[136:139], v[64:67]\n"
    "v_cvt_pk_bf16_f32 v78, v28, v29\n"
    "v_cvt_pk_bf16_f32 v88, v40, v41\n"
    "v_mfma_f32_16x16x32_bf16 v[64:67], v[180:183], v[132:135], v[64:67]\n"
    "v_exp_f32 v42, v42\n"
    "v_mfma_f32_16x16x32_bf16 v[64:67], v[176:179], v[128:131], v[64:67]\n"
    "s_waitcnt lgkmcnt(0)\n"
    "v_exp_f32 v43, v43\n"
    "v_mfma_f32_16x16x32_bf16 v[4:7], v[152:155], a[192:195], v[100:103]\n"
    "buffer_load_dwordx4 v1, s[16:19], s83 offen offset:1024 sc1 lds\n"
    "v_mfma_f32_16x16x32_bf16 v[4:7], v[156:159], a[196:199], v[4:7]\n"
    "ds_read_b128 v[176:179], v3 offset:1024\n"
    "ds_read_b128 v[180:183], v3 offset:3072\n"
    "v_mfma_f32_16x16x32_bf16 v[4:7], v[160:163], a[200:203], v[4:7]\n"
    "ds_read_b128 v[184:187], v3 offset:5120\n"
    "v_cvt_pk_bf16_f32 v79, v30, v31\n"
    "v_mfma_f32_16x16x32_bf16 v[4:7], v[164:167], a[204:207], v[4:7]\n"
    "ds_read_b128 v[188:191], v3 offset:7168\n"
    "v_cvt_pk_bf16_f32 v89, v42, v43\n"
    "v_mfma_f32_16x16x32_bf16 v[4:7], v[168:171], a[208:211], v[4:7]\n"
    "ds_read_b128 v[192:195], v3 offset:9216\n"
    "v_cvt_pk_bf16_f32 v82, v32, v33\n"
    "v_mfma_f32_16x16x32_bf16 v[4:7], v[172:175], a[212:215], v[4:7]\n"
    "ds_read_b128 v[196:199], v3 offset:11264\n"
    "v_add_u32 v249, 64, v249\n"
    "v_mfma_f32_16x16x32_bf16 v[8:11], v[172:175], a[236:239], v[104:107]\n"
    "buffer_load_dwordx3 v[200:202], v246, s[28:31], s75 offen offset:0 nt\n"
    "v_mfma_f32_16x16x32_bf16 v[8:11], v[168:171], a[232:235], v[8:11]\n"
    "v_exp_f32 v52, v52\n"
    "v_mfma_f32_16x16x32_bf16 v[8:11], v[164:167], a[228:231], v[8:11]\n"
    "v_cvt_pk_bf16_f32 v83, v34, v35\n"
    "v_add_f32 v116, v116, v52\n"
    "v_mfma_f32_16x16x32_bf16 v[8:11], v[160:163], a[224:227], v[8:11]\n"
    "buffer_load_dwordx3 v[204:206], v246, s[28:31], s75 offen offset:768 nt\n"
    "v_mfma_f32_16x16x32_bf16 v[8:11], v[156:159], a[220:223], v[8:11]\n"
    "v_exp_f32 v53, v53\n"
    "v_mfma_f32_16x16x32_bf16 v[8:11], v[152:155], a[216:219], v[8:11]\n"
    "v_exp_f32 v54, v54\n"
    "v_mfma_f32_16x16x32_bf16 v[12:15], v[152:155], a[240:243], v[108:111]\n"
    "buffer_load_dwordx3 v[208:210], v246, s[28:31], s75 offen offset:1536 nt\n"
    "v_mfma_f32_16x16x32_bf16 v[12:15], v[156:159], a[244:247], v[12:15]\n"
    "v_cvt_pk_bf16_f32 v86, v52, v53\n"
    "v_add_f32 v116, v116, v53\n"
    "v_mfma_f32_16x16x32_bf16 v[12:15], v[160:163], a[248:251], v[12:15]\n"
    "v_exp_f32 v55, v55\n"
    "v_mfma_f32_16x16x32_bf16 v[12:15], v[164:167], a[252:255], v[12:15]\n"
    "buffer_load_dwordx3 v[212:214], v246, s[28:31], s75 offen offset:2304 nt\n"
    "v_mfma_f32_16x16x32_bf16 v[12:15], v[168:171], v[120:123], v[12:15]\n"
    "v_cvt_pk_bf16_f32 v87, v54, v55\n"
    "v_add_f32 v116, v116, v54\n"
    "v_mfma_f32_16x16x32_bf16 v[12:15], v[172:175], v[124:127], v[12:15]\n"
    "v_exp_f32 v56, v56\n"
    "v_mfma_f32_16x16x32_bf16 v[16:19], v[172:175], v[148:151], v[112:115]\n"
    "buffer_load_dwordx3 v[216:218], v247, s[28:31], s75 offen offset:0 nt\n"
    "v_mfma_f32_16x16x32_bf16 v[16:19], v[168:171], v[144:147], v[16:19]\n"
    "v_exp_f32 v44, v44\n"
    "v_mfma_f32_16x16x32_bf16 v[16:19], v[164:167], v[140:143], v[16:19]\n"
    "v_exp_f32 v57, v57\n"
    "v_mfma_f32_16x16x32_bf16 v[16:19], v[160:163], v[136:139], v[16:19]\n"
    "buffer_load_dwordx3 v[220:222], v247, s[28:31], s75 offen offset:768 nt\n"
    "v_mfma_f32_16x16x32_bf16 v[16:19], v[156:159], v[132:135], v[16:19]\n"
    "v_cvt_pk_bf16_f32 v90, v56, v57\n"
    "v_add_f32 v116, v116, v55\n"
    "v_mfma_f32_16x16x32_bf16 v[16:19], v[152:155], v[128:131], v[16:19]\n"
    "s_waitcnt lgkmcnt(0)\n"
    "v_exp_f32 v58, v58\n"
    "v_mfma_f32_16x16x32_bf16 v[20:23], v[176:179], a[192:195], v[100:103]\n"
    "ds_read_b128 v[152:155], v2 offset:24576\n"
    "v_add_f32 v117, v117, v40\n"
    "v_mfma_f32_16x16x32_bf16 v[20:23], v[180:183], a[196:199], v[20:23]\n"
    "ds_read_b128 v[156:159], v2 offset:36864\n"
    "v_add_f32 v117, v117, v41\n"
    "v_mfma_f32_16x16x32_bf16 v[20:23], v[184:187], a[200:203], v[20:23]\n"
    "ds_read_b128 v[160:163], v2 offset:25600\n"
    "v_add_f32 v117, v117, v42\n"
    "v_mfma_f32_16x16x32_bf16 v[20:23], v[188:191], a[204:207], v[20:23]\n"
    "ds_read_b128 v[164:167], v2 offset:37888\n"
    "v_add_f32 v117, v117, v43\n"
    "v_mfma_f32_16x16x32_bf16 v[20:23], v[192:195], a[208:211], v[20:23]\n"
    "ds_read_b128 v[168:171], v2 offset:26624\n"
    "v_add_f32 v117, v117, v56\n"
    "v_mfma_f32_16x16x32_bf16 v[20:23], v[196:199], a[212:215], v[20:23]\n"
    "ds_read_b128 v[172:175], v2 offset:38912\n"
    "v_add_f32 v117, v117, v57\n"
    "v_mfma_f32_16x16x32_bf16 v[24:27], v[196:199], a[236:239], v[104:107]\n"
    "buffer_load_dwordx3 v[224:226], v247, s[28:31], s75 offen offset:1536 nt\n"
    "v_mfma_f32_16x16x32_bf16 v[24:27], v[192:195], a[232:235], v[24:27]\n"
    "v_exp_f32 v59, v59\n"
    "v_mfma_f32_16x16x32_bf16 v[24:27], v[188:191], a[228:231], v[24:27]\n"
    "v_cvt_pk_bf16_f32 v91, v58, v59\n"
    "v_add_f32 v117, v117, v58\n"
    "v_mfma_f32_16x16x32_bf16 v[24:27], v[184:187], a[224:227], v[24:27]\n"
    "buffer_load_dwordx3 v[228:230], v247, s[28:31], s75 offen offset:2304 nt\n"
    "v_mfma_f32_16x16x32_bf16 v[24:27], v[180:183], a[220:223], v[24:27]\n"
    "v_exp_f32 v45, v45\n"
    "v_mfma_f32_16x16x32_bf16 v[24:27], v[176:179], a[216:219], v[24:27]\n"
    "v_exp_f32 v46, v46\n"
    "v_mfma_f32_16x16x32_bf16 v[28:31], v[176:179], a[240:243], v[108:111]\n"
    "v_exp_f32 v47, v47\n"
    "v_mfma_f32_16x16x32_bf16 v[28:31], v[180:183], a[244:247], v[28:31]\n"
    "v_exp_f32 v60, v60\n"
    "v_mfma_f32_16x16x32_bf16 v[28:31], v[184:187], a[248:251], v[28:31]\n"
    "v_exp_f32 v61, v61\n"
    "v_mfma_f32_16x16x32_bf16 v[28:31], v[188:191], a[252:255], v[28:31]\n"
    "v_exp_f32 v62, v62\n"
    "v_mfma_f32_16x16x32_bf16 v[28:31], v[192:195], v[120:123], v[28:31]\n"
    "v_exp_f32 v63, v63\n"
    "v_mfma_f32_16x16x32_bf16 v[28:31], v[196:199], v[124:127], v[28:31]\n"
    "v_cvt_pk_bf16_f32 v92, v44, v45\n"
    "v_cvt_pk_bf16_f32 v93, v46, v47\n"
    "v_mfma_f32_16x16x32_bf16 v[32:35], v[196:199], v[148:151], v[112:115]\n"
    "v_cvt_pk_bf16_f32 v94, v60, v61\n"
    "v_cvt_pk_bf16_f32 v95, v62, v63\n"
    "v_mfma_f32_16x16x32_bf16 v[32:35], v[192:195], v[144:147], v[32:35]\n"
    "v_exp_f32 v48, v48\n"
    "v_mfma_f32_16x16x32_bf16 v[32:35], v[188:191], v[140:143], v[32:35]\n"
    "v_exp_f32 v49, v49\n"
    "v_mfma_f32_16x16x32_bf16 v[32:35], v[184:187], v[136:139], v[32:35]\n"
    "v_exp_f32 v50, v50\n"
    "v_mfma_f32_16x16x32_bf16 v[32:35], v[180:183], v[132:135], v[32:35]\n"
    "s_add_u32 m0, s68, 24576\n"
    "s_add_u32 s83, s67, 24576\n"
    "v_cvt_pk_bf16_f32 v96, v48, v49\n"
    "v_mfma_f32_16x16x32_bf16 v[32:35], v[176:179], v[128:131], v[32:35]\n"
    "s_waitcnt lgkmcnt(0)\n"
    "v_exp_f32 v51, v51\n"
    "v_mfma_f32_16x16x32_bf16 a[0:3], v[152:155], v[68:71], a[0:3]\n"
    "ds_read_b128 v[176:179], v2 offset:27648\n"
    "v_cvt_pk_bf16_f32 v97, v50, v51\n"
    "v_mfma_f32_16x16x32_bf16 a[0:3], v[156:159], v[84:87], a[0:3]\n"
    "ds_read_b128 v[180:183], v2 offset:39936\n"
    "v_add_f32 v117, v117, v59\n"
    "v_mfma_f32_16x16x32_bf16 a[16:19], v[164:167], v[84:87], a[16:19]\n"
    "ds_read_b128 v[184:187], v2 offset:28672\n"
    "v_add_f32 v118, v118, v44\n"
    "v_mfma_f32_16x16x32_bf16 a[16:19], v[160:163], v[68:71], a[16:19]\n"
    "ds_read_b128 v[188:191], v2 offset:40960\n"
    "v_add_f32 v118, v118, v45\n"
    "v_mfma_f32_16x16x32_bf16 a[32:35], v[168:171], v[68:71], a[32:35]\n"
    "buffer_load_dwordx4 v1, s[16:19], s83 offen offset:0 sc1 lds\n"
    "v_mfma_f32_16x16x32_bf16 a[32:35], v[172:175], v[84:87], a[32:35]\n"
    "ds_read_b128 v[192:195], v2 offset:29696\n"
    "ds_read_b128 v[196:199], v2 offset:41984\n"
    "v_mfma_f32_16x16x32_bf16 a[36:39], v[172:175], v[88:91], a[36:39]\n"
    "v_exp_f32 v64, v64\n"
    "v_mfma_f32_16x16x32_bf16 a[36:39], v[168:171], v[72:75], a[36:39]\n"
    "v_exp_f32 v65, v65\n"
    "v_mfma_f32_16x16x32_bf16 a[20:23], v[160:163], v[72:75], a[20:23]\n"
    "v_exp_f32 v66, v66\n"
    "v_mfma_f32_16x16x32_bf16 a[20:23], v[164:167], v[88:91], a[20:23]\n"
    "v_exp_f32 v67, v67\n"
    "v_mfma_f32_16x16x32_bf16 a[4:7], v[156:159], v[88:91], a[4:7]\n"
    "buffer_load_dwordx4 v1, s[16:19], s83 offen offset:1024 sc1 lds\n"
    "v_mfma_f32_16x16x32_bf16 a[4:7], v[152:155], v[72:75], a[4:7]\n"
    "v_cvt_pk_bf16_f32 v98, v64, v65\n"
    "v_cvt_pk_bf16_f32 v99, v66, v67\n"
    "v_mfma_f32_16x16x32_bf16 a[8:11], v[152:155], v[76:79], a[8:11]\n"
    "v_exp_f32 v4, v4\n"
    "v_mfma_f32_16x16x32_bf16 a[8:11], v[156:159], v[92:95], a[8:11]\n"
    "v_exp_f32 v5, v5\n"
    "v_mfma_f32_16x16x32_bf16 a[24:27], v[164:167], v[92:95], a[24:27]\n"
    "v_exp_f32 v6, v6\n"
    "v_mfma_f32_16x16x32_bf16 a[24:27], v[160:163], v[76:79], a[24:27]\n"
    "v_exp_f32 v7, v7\n"
    "v_mfma_f32_16x16x32_bf16 a[40:43], v[168:171], v[76:79], a[40:43]\n"
    "buffer_load_dwordx4 v1, s[16:19], s83 offen offset:2048 sc1 lds\n"
    "v_mfma_f32_16x16x32_bf16 a[40:43], v[172:175], v[92:95], a[40:43]\n"
    "v_add_f32 v116, v116, v4\n"
    "v_add_f32 v116, v116, v5\n"
    "v_mfma_f32_16x16x32_bf16 a[44:47], v[172:175], v[96:99], a[44:47]\n"
    "v_exp_f32 v8, v8\n"
    "v_mfma_f32_16x16x32_bf16 a[44:47], v[168:171], v[80:83], a[44:47]\n"
    "v_exp_f32 v9, v9\n"
    "v_mfma_f32_16x16x32_bf16 a[28:31], v[160:163], v[80:83], a[28:31]\n"
    "v_add_f32 v116, v116, v6\n"
    "v_add_f32 v116, v116, v7\n"
    "v_mfma_f32_16x16x32_bf16 a[28:31], v[164:167], v[96:99], a[28:31]\n"
    "v_exp_f32 v10, v10\n"
    "v_mfma_f32_16x16x32_bf16 a[12:15], v[156:159], v[96:99], a[12:15]\n"
    "buffer_load_dwordx4 v1, s[16:19], s83 offen offset:3072 sc1 lds\n"
    "v_mfma_f32_16x16x32_bf16 a[12:15], v[152:155], v[80:83], a[12:15]\n"
    "s_waitcnt lgkmcnt(0)\n"
    "s_add_u32 m0, s68, 28672\n"
    "s_add_u32 s83, s67, 28672\n"
    "v_mfma_f32_16x16x32_bf16 a[48:51], v[176:179], v[68:71], a[48:51]\n"
    "s_waitcnt vmcnt(15)\n"
    "ds_read_b128 v[152:155], v2 offset:30720\n"
    "v_add_f32 v117, v117, v8\n"
    "v_mfma_f32_16x16x32_bf16 a[48:51], v[180:183], v[84:87], a[48:51]\n"
    "buffer_store_dwordx2 v[242:243], v244, s[36:39], s80 offen offset:0 sc1\n"
    "v_mfma_f32_16x16x32_bf16 a[64:67], v[188:191], v[84:87], a[64:67]\n"
    "ds_read_b128 v[156:159], v2 offset:43008\n"
    "ds_read_b128 v[160:163], v2 offset:31744\n"
    "v_mfma_f32_16x16x32_bf16 a[64:67], v[184:187], v[68:71], a[64:67]\n"
    "ds_read_b128 v[164:167], v2 offset:44032\n"
    "v_add_f32 v117, v117, v9\n"
    "v_mfma_f32_16x16x32_bf16 a[80:83], v[192:195], v[68:71], a[80:83]\n"
    "buffer_load_dwordx4 v1, s[16:19], s83 offen offset:0 sc1 lds\n"
    "v_mfma_f32_16x16x32_bf16 a[80:83], v[196:199], v[84:87], a[80:83]\n"
    "ds_read_b128 v[168:171], v2 offset:32768\n"
    "ds_read_b128 v[172:175], v2 offset:45056\n"
    "v_mfma_f32_16x16x32_bf16 a[84:87], v[196:199], v[88:91], a[84:87]\n"
    "v_exp_f32 v11, v11\n"
    "v_mfma_f32_16x16x32_bf16 a[84:87], v[192:195], v[72:75], a[84:87]\n"
    "v_exp_f32 v12, v12\n"
    "v_mfma_f32_16x16x32_bf16 a[68:71], v[184:187], v[72:75], a[68:71]\n"
    "v_exp_f32 v13, v13\n"
    "v_mfma_f32_16x16x32_bf16 a[68:71], v[188:191], v[88:91], a[68:71]\n"
    "v_add_f32 v117, v117, v10\n"
    "v_add_f32 v117, v117, v11\n"
    "v_mfma_f32_16x16x32_bf16 a[52:55], v[180:183], v[88:91], a[52:55]\n"
    "buffer_load_dwordx4 v1, s[16:19], s83 offen offset:1024 sc1 lds\n"
    "v_mfma_f32_16x16x32_bf16 a[52:55], v[176:179], v[72:75], a[52:55]\n"
    "s_add_u32 s67, s67, 49152\n"
    "v_exp_f32 v14, v14\n"
    "v_mfma_f32_16x16x32_bf16 a[56:59], v[176:179], v[76:79], a[56:59]\n"
    "v_exp_f32 v15, v15\n"
    "v_mfma_f32_16x16x32_bf16 a[56:59], v[180:183], v[92:95], a[56:59]\n"
    "v_add_f32 v118, v118, v12\n"
    "v_add_f32 v118, v118, v46\n"
    "v_mfma_f32_16x16x32_bf16 a[72:75], v[188:191], v[92:95], a[72:75]\n"
    "v_exp_f32 v16, v16\n"
    "v_mfma_f32_16x16x32_bf16 a[72:75], v[184:187], v[76:79], a[72:75]\n"
    "v_add_f32 v118, v118, v47\n"
    "v_add_f32 v118, v118, v60\n"
    "v_mfma_f32_16x16x32_bf16 a[88:91], v[192:195], v[76:79], a[88:91]\n"
    "v_add_f32 v118, v118, v61\n"
    "v_add_f32 v118, v118, v62\n"
    "v_mfma_f32_16x16x32_bf16 a[88:91], v[196:199], v[92:95], a[88:91]\n"
    "v_add_f32 v118, v118, v63\n"
    "v_add_f32 v118, v118, v13\n"
    "v_mfma_f32_16x16x32_bf16 a[92:95], v[196:199], v[96:99], a[92:95]\n"
    "v_exp_f32 v17, v17\n"
    "v_mfma_f32_16x16x32_bf16 a[92:95], v[192:195], v[80:83], a[92:95]\n"
    "v_add_f32 v118, v118, v14\n"
    "v_add_f32 v119, v119, v48\n"
    "v_mfma_f32_16x16x32_bf16 a[76:79], v[184:187], v[80:83], a[76:79]\n"
    "v_exp_f32 v18, v18\n"
    "v_mfma_f32_16x16x32_bf16 a[76:79], v[188:191], v[96:99], a[76:79]\n"
    "v_add_f32 v119, v119, v49\n"
    "v_add_f32 v119, v119, v50\n"
    "v_mfma_f32_16x16x32_bf16 a[60:63], v[180:183], v[96:99], a[60:63]\n"
    "v_add_f32 v119, v119, v51\n"
    "v_add_f32 v119, v119, v64\n"
    "v_mfma_f32_16x16x32_bf16 a[60:63], v[176:179], v[80:83], a[60:63]\n"
    "s_waitcnt lgkmcnt(0)\n"
    "v_add_f32 v119, v119, v65\n"
    "v_add_f32 v119, v119, v66\n"
    "v_mfma_f32_16x16x32_bf16 a[96:99], v[152:155], v[68:71], a[96:99]\n"
    "ds_read_b128 v[176:179], v2 offset:33792\n"
    "v_add_f32 v119, v119, v67\n"
    "v_mfma_f32_16x16x32_bf16 a[96:99], v[156:159], v[84:87], a[96:99]\n"
    "ds_read_b128 v[180:183], v2 offset:46080\n"
    "v_add_f32 v118, v118, v15\n"
    "v_mfma_f32_16x16x32_bf16 a[112:115], v[164:167], v[84:87], a[112:115]\n"
    "ds_read_b128 v[184:187], v2 offset:34816\n"
    "v_add_f32 v119, v119, v16\n"
    "v_mfma_f32_16x16x32_bf16 a[112:115], v[160:163], v[68:71], a[112:115]\n"
    "ds_read_b128 v[188:191], v2 offset:47104\n"
    "v_add_f32 v119, v119, v17\n"
    "v_mfma_f32_16x16x32_bf16 a[128:131], v[168:171], v[68:71], a[128:131]\n"
    "ds_read_b128 v[192:195], v2 offset:35840\n"
    "v_add_f32 v119, v119, v18\n"
    "v_mfma_f32_16x16x32_bf16 a[128:131], v[172:175], v[84:87], a[128:131]\n"
    "ds_read_b128 v[196:199], v2 offset:48128\n"
    "v_mfma_f32_16x16x32_bf16 a[132:135], v[172:175], v[88:91], a[132:135]\n"
    "buffer_load_dwordx2 v[240:241], v245, s[36:39], s81 offen offset:0 sc1\n"
    "v_mfma_f32_16x16x32_bf16 a[132:135], v[168:171], v[72:75], a[132:135]\n"
    "v_exp_f32 v19, v19\n"
    "v_mfma_f32_16x16x32_bf16 a[116:119], v[160:163], v[72:75], a[116:119]\n"
    "v_exp_f32 v20, v20\n"
    "v_mfma_f32_16x16x32_bf16 a[116:119], v[164:167], v[88:91], a[116:119]\n"
    "v_exp_f32 v21, v21\n"
    "v_mfma_f32_16x16x32_bf16 a[100:103], v[156:159], v[88:91], a[100:103]\n"
    "v_exp_f32 v22, v22\n"
    "v_mfma_f32_16x16x32_bf16 a[100:103], v[152:155], v[72:75], a[100:103]\n"
    "v_add_f32 v119, v119, v19\n"
    "v_add_f32 v116, v116, v20\n"
    "v_mfma_f32_16x16x32_bf16 a[104:107], v[152:155], v[76:79], a[104:107]\n"
    "v_exp_f32 v23, v23\n"
    "v_mfma_f32_16x16x32_bf16 a[104:107], v[156:159], v[92:95], a[104:107]\n"
    "v_exp_f32 v24, v24\n"
    "v_mfma_f32_16x16x32_bf16 a[120:123], v[164:167], v[92:95], a[120:123]\n"
    "v_add_f32 v116, v116, v21\n"
    "v_add_f32 v116, v116, v22\n"
    "v_mfma_f32_16x16x32_bf16 a[120:123], v[160:163], v[76:79], a[120:123]\n"
    "v_exp_f32 v25, v25\n"
    "v_mfma_f32_16x16x32_bf16 a[136:139], v[168:171], v[76:79], a[136:139]\n"
    "v_exp_f32 v26, v26\n"
    "v_mfma_f32_16x16x32_bf16 a[136:139], v[172:175], v[92:95], a[136:139]\n"
    "v_add_f32 v116, v116, v23\n"
    "v_add_f32 v117, v117, v24\n"
    "v_mfma_f32_16x16x32_bf16 a[140:143], v[172:175], v[96:99], a[140:143]\n"
    "v_exp_f32 v27, v27\n"
    "v_mfma_f32_16x16x32_bf16 a[140:143], v[168:171], v[80:83], a[140:143]\n"
    "v_exp_f32 v28, v28\n"
    "v_mfma_f32_16x16x32_bf16 a[124:127], v[160:163], v[80:83], a[124:127]\n"
    "v_add_f32 v117, v117, v25\n"
    "v_add_f32 v117, v117, v26\n"
    "v_mfma_f32_16x16x32_bf16 a[124:127], v[164:167], v[96:99], a[124:127]\n"
    "v_exp_f32 v29, v29\n"
    "v_mfma_f32_16x16x32_bf16 a[108:111], v[156:159], v[96:99], a[108:111]\n"
    "v_exp_f32 v30, v30\n"
    "v_mfma_f32_16x16x32_bf16 a[108:111], v[152:155], v[80:83], a[108:111]\n"
    "s_waitcnt lgkmcnt(0)\n"
    "v_add_f32 v117, v117, v27\n"
    "v_add_f32 v118, v118, v28\n"
    "v_mfma_f32_16x16x32_bf16 a[144:147], v[176:179], v[68:71], a[144:147]\n"
    "ds_read_b128 v[152:155], v3 offset:12288\n"
    "v_add_f32 v118, v118, v29\n"
    "v_mfma_f32_16x16x32_bf16 a[144:147], v[180:183], v[84:87], a[144:147]\n"
    "ds_read_b128 v[156:159], v3 offset:14336\n"
    "v_add_f32 v118, v118, v30\n"
    "v_mfma_f32_16x16x32_bf16 a[160:163], v[188:191], v[84:87], a[160:163]\n"
    "ds_read_b128 v[160:163], v3 offset:16384\n"
    "v_mfma_f32_16x16x32_bf16 a[160:163], v[184:187], v[68:71], a[160:163]\n"
    "ds_read_b128 v[164:167], v3 offset:18432\n"
    "v_mfma_f32_16x16x32_bf16 a[176:179], v[192:195], v[68:71], a[176:179]\n"
    "ds_read_b128 v[168:171], v3 offset:20480\n"
    "v_mfma_f32_16x16x32_bf16 a[176:179], v[196:199], v[84:87], a[176:179]\n"
    "ds_read_b128 v[172:175], v3 offset:22528\n"
    "v_mfma_f32_16x16x32_bf16 a[180:183], v[196:199], v[88:91], a[180:183]\n"
    "v_exp_f32 v31, v31\n"
    "v_mfma_f32_16x16x32_bf16 a[180:183], v[192:195], v[72:75], a[180:183]\n"
    "v_exp_f32 v32, v32\n"
    "v_mfma_f32_16x16x32_bf16 a[164:167], v[184:187], v[72:75], a[164:167]\n"
    "v_exp_f32 v33, v33\n"
    "v_mfma_f32_16x16x32_bf16 a[164:167], v[188:191], v[88:91], a[164:167]\n"
    "v_exp_f32 v34, v34\n"
    "v_mfma_f32_16x16x32_bf16 a[148:151], v[180:183], v[88:91], a[148:151]\n"
    "v_add_f32 v118, v118, v31\n"
    "v_add_f32 v119, v119, v32\n"
    "v_mfma_f32_16x16x32_bf16 a[148:151], v[176:179], v[72:75], a[148:151]\n"
    "v_exp_f32 v35, v35\n"
    "v_mfma_f32_16x16x32_bf16 a[152:155], v[176:179], v[76:79], a[152:155]\n"
    "v_add_f32 v119, v119, v33\n"
    "v_add_f32 v119, v119, v34\n"
    "v_mfma_f32_16x16x32_bf16 a[152:155], v[180:183], v[92:95], a[152:155]\n"
    "v_add_f32 v119, v119, v35\n"
    "v_mfma_f32_16x16x32_bf16 a[168:171], v[188:191], v[92:95], a[168:171]\n"
    "s_waitcnt vmcnt(16)\n"
    "ds_add_u32 v252, v251\n"
    "v_mfma_f32_16x16x32_bf16 a[168:171], v[184:187], v[76:79], a[168:171]\n"
    "v_mfma_f32_16x16x32_bf16 a[184:187], v[192:195], v[76:79], a[184:187]\n"
    "v_mfma_f32_16x16x32_bf16 a[184:187], v[196:199], v[92:95], a[184:187]\n"
    "v_mfma_f32_16x16x32_bf16 a[188:191], v[196:199], v[96:99], a[188:191]\n"
    "v_mfma_f32_16x16x32_bf16 a[188:191], v[192:195], v[80:83], a[188:191]\n"
    "v_mfma_f32_16x16x32_bf16 a[172:175], v[184:187], v[80:83], a[172:175]\n"
    "s_sub_u32 s74, s68, s65\n"
    "v_mfma_f32_16x16x32_bf16 a[172:175], v[188:191], v[96:99], a[172:175]\n"
    "s_add_u32 s66, s66, 1\n"
    "v_mov_b32 v2, v3\n"
    "v_add_u32 v3, s74, v1\n"
    "v_mfma_f32_16x16x32_bf16 a[156:159], v[180:183], v[96:99], a[156:159]\n"
    "s_waitcnt lgkmcnt(1)\n"
    "s_cmp_eq_u32 s66, 31\n"
    "v_mfma_f32_16x16x32_bf16 a[156:159], v[176:179], v[80:83], a[156:159]\n"
    "s_cbranch_scc1 .Ltail\n"
    ".LNV:\n"
    "v_mfma_f32_16x16x32_bf16 v[36:39], v[152:155], a[192:195], v[100:103]\n"
    "ds_read_b128 v[176:179], v2 offset:13312\n"
    "v_cvt_pk_bf16_f32 v68, v4, v5\n"
    "v_mfma_f32_16x16x32_bf16 v[36:39], v[156:159], a[196:199], v[36:39]\n"
    "ds_read_b128 v[180:183], v2 offset:15360\n"
    "ds_read_b128 v[232:235], v249 offset:0\n"
    "v_mfma_f32_16x16x32_bf16 v[36:39], v[160:163], a[200:203], v[36:39]\n"
    "s_add_u32 s68, s68, 49152\n"
    "s_cmp_ge_u32 s68, s69\n"
    "s_cselect_b32 s68, s65, s68\n"
    "v_mfma_f32_16x16x32_bf16 v[36:39], v[164:167], a[204:207], v[36:39]\n"
    "ds_read_b128 v[184:187], v2 offset:17408\n"
    "ds_read_b128 v[188:191], v2 offset:19456\n"
    "v_mfma_f32_16x16x32_bf16 v[36:39], v[168:171], a[208:211], v[36:39]\n"
    "s_waitcnt vmcnt(8)\n"
    "ds_read_b128 v[192:195], v2 offset:21504\n"
    "v_cvt_pk_bf16_f32 v69, v6, v7\n"
    "v_mfma_f32_16x16x32_bf16 v[36:39], v[172:175], a[212:215], v[36:39]\n"
    "ds_read_b128 v[196:199], v2 offset:23552\n"
    "ds_read_b128 v[236:239], v249 offset:32\n"
    "v_mfma_f32_16x16x32_bf16 v[40:43], v[172:175], a[236:239], v[104:107]\n"
    "v_cvt_f32_i32 v200, v200\n"
    "v_cvt_f32_i32 v201, v201\n"
    "v_mfma_f32_16x16x32_bf16 v[40:43], v[168:171], a[232:235], v[40:43]\n"
    "v_cvt_pk_bf16_f32 v72, v8, v9\n"
    "v_cvt_f32_i32 v202, v202\n"
    "v_mfma_f32_16x16x32_bf16 v[40:43], v[164:167], a[228:231], v[40:43]\n"
    "v_exp_f32 v36, v36\n"
    "v_mfma_f32_16x16x32_bf16 v[40:43], v[160:163], a[224:227], v[40:43]\n"
    "s_add_u32 s79, s79, 128\n"
    "v_cvt_f32_i32 v204, v204\n"
    "v_add_f32 v116, v116, v36\n"
    "v_mfma_f32_16x16x32_bf16 v[40:43], v[156:159], a[220:223], v[40:43]\n"
    "v_exp_f32 v37, v37\n"
    "v_mfma_f32_16x16x32_bf16 v[40:43], v[152:155], a[216:219], v[40:43]\n"
    "v_cvt_pk_bf16_f32 v73, v10, v11\n"
    "v_cvt_pk_bf16_f32 v84, v36, v37\n"
    "v_mfma_f32_16x16x32_bf16 v[44:47], v[152:155], a[240:243], v[108:111]\n"
    "v_cvt_f32_i32 v205, v205\n"
    "v_cvt_f32_i32 v206, v206\n"
    "v_mfma_f32_16x16x32_bf16 v[44:47], v[156:159], a[244:247], v[44:47]\n"
    "v_exp_f32 v38, v38\n"
    "v_mfma_f32_16x16x32_bf16 v[44:47], v[160:163], a[248:251], v[44:47]\n"
    "v_add_f32 v116, v116, v37\n"
    "v_add_f32 v116, v116, v38\n"
    "v_mfma_f32_16x16x32_bf16 v[44:47], v[164:167], a[252:255], v[44:47]\n"
    "v_cvt_pk_bf16_f32 v76, v12, v13\n"
    "v_cvt_f32_i32 v208, v208\n"
    "v_mfma_f32_16x16x32_bf16 v[44:47], v[168:171], v[120:123], v[44:47]\n"
    "v_exp_f32 v39, v39\n"
    "v_mfma_f32_16x16x32_bf16 v[44:47], v[172:175], v[124:127], v[44:47]\n"
    "v_cvt_pk_bf16_f32 v85, v38, v39\n"
    "v_cvt_f32_i32 v209, v209\n"
    "v_mfma_f32_16x16x32_bf16 v[48:51], v[172:175], v[148:151], v[112:115]\n"
    "ds_read_b32 v250, v252\n"
    "v_cvt_pk_bf16_f32 v77, v14, v15\n"
    "v_mfma_f32_16x16x32_bf16 v[48:51], v[168:171], v[144:147], v[48:51]\n"
    "v_exp_f32 v40, v40\n"
    "v_mfma_f32_16x16x32_bf16 v[48:51], v[164:167], v[140:143], v[48:51]\n"
    "v_cvt_f32_i32 v210, v210\n"
    "v_add_f32 v116, v116, v39\n"
    "v_mfma_f32_16x16x32_bf16 v[48:51], v[160:163], v[136:139], v[48:51]\n"
    "v_add_f32 v117, v117, v40\n"
    "v_mfma_f32_16x16x32_bf16 v[48:51], v[156:159], v[132:135], v[48:51]\n"
    "s_add_u32 m0, s68, 0\n"
    "v_cvt_pk_bf16_f32 v80, v16, v17\n"
    "v_cvt_f32_i32 v212, v212\n"
    "v_mfma_f32_16x16x32_bf16 v[48:51], v[152:155], v[128:131], v[48:51]\n"
    "s_waitcnt vmcnt(0)\n"
    "s_waitcnt lgkmcnt(2)\n"
    "v_cmp_eq_u64 s[54:55], v[240:241], v[242:243]\n"
    "v_mfma_f32_16x16x32_bf16 v[52:55], v[176:179], a[192:195], v[100:103]\n"
    "s_waitcnt lgkmcnt(0)\n"
    "v_cmp_le_u32 s[56:57], s59, v250\n"
    "v_cvt_f32_i32 v213, v213\n"
    "v_mfma_f32_16x16x32_bf16 v[52:55], v[180:183], a[196:199], v[52:55]\n"
    "v_exp_f32 v41, v41\n"
    "v_mfma_f32_16x16x32_bf16 v[52:55], v[184:187], a[200:203], v[52:55]\n"
    "s_and_b64 s[54:55], s[54:55], s[56:57]\n"
    "s_cmp_eq_u64 s[54:55], -1\n"
    "v_mfma_f32_16x16x32_bf16 v[52:55], v[188:191], a[204:207], v[52:55]\n"
    "s_cbranch_scc0 .Lpoll4\n"
    ".Lpoll4r:\n"
    "v_mfma_f32_16x16x32_bf16 v[52:55], v[192:195], a[208:211], v[52:55]\n"
    "buffer_load_dwordx4 v1, s[16:19], s67 offen offset:0 sc1 lds\n"
    "v_mfma_f32_16x16x32_bf16 v[52:55], v[196:199], a[212:215], v[52:55]\n"
    "s_add_u32 s59, s59, 4\n"
    "ds_read_b128 v[152:155], v3 offset:0\n"
    "ds_read_b128 v[156:159], v3 offset:2048\n"
    "v_mfma_f32_16x16x32_bf16 v[56:59], v[196:199], a[236:239], v[104:107]\n"
    "ds_read_b128 v[160:163], v3 offset:4096\n"
    "ds_read_b128 v[164:167], v3 offset:6144\n"
    "v_mfma_f32_16x16x32_bf16 v[56:59], v[192:195], a[232:235], v[56:59]\n"
    "s_min_u32 s81, s79, 3968\n"
    "ds_read_b128 v[168:171], v3 offset:8192\n"
    "ds_read_b128 v[172:175], v3 offset:10240\n"
    "v_mfma_f32_16x16x32_bf16 v[56:59], v[188:191], a[228:231], v[56:59]\n"
    "buffer_load_dwordx4 v1, s[16:19], s67 offen offset:1024 sc1 lds\n"
    "v_mfma_f32_16x16x32_bf16 v[56:59], v[184:187], a[224:227], v[56:59]\n"
    "v_cvt_pk_bf16_f32 v81, v18, v19\n"
    "v_cvt_pk_bf16_f32 v70, v20, v21\n"
    "v_mfma_f32_16x16x32_bf16 v[56:59], v[180:183], a[220:223], v[56:59]\n"
    "v_cvt_pk_bf16_f32 v71, v22, v23\n"
    "v_cvt_pk_bf16_f32 v88, v40, v41\n"
    "v_mfma_f32_16x16x32_bf16 v[56:59], v[176:179], a[216:219], v[56:59]\n"
    "v_exp_f32 v42, v42\n"
    "v_mfma_f32_16x16x32_bf16 v[60:63], v[176:179], a[240:243], v[108:111]\n"
    "buffer_load_dwordx4 v1, s[16:19], s67 offen offset:2048 sc1 lds\n"
    "v_mfma_f32_16x16x32_bf16 v[60:63], v[180:183], a[244:247], v[60:63]\n"
    "v_cvt_pk_bf16_f32 v74, v24, v25\n"
    "v_cvt_f32_i32 v214, v214\n"
    "v_mfma_f32_16x16x32_bf16 v[60:63], v[184:187], a[248:251], v[60:63]\n"
    "v_exp_f32 v43, v43\n"
    "v_mfma_f32_16x16x32_bf16 v[60:63], v[188:191], a[252:255], v[60:63]\n"
    "v_cvt_pk_bf16_f32 v89, v42, v43\n"
    "v_fma_f32 v200, v200, v232, v236\n"
    "v_mfma_f32_16x16x32_bf16 v[60:63], v[192:195], v[120:123], v[60:63]\n"
    "buffer_load_dwordx4 v1, s[16:19], s67 offen offset:3072 sc1 lds\n"
    "v_mfma_f32_16x16x32_bf16 v[60:63], v[196:199], v[124:127], v[60:63]\n"
    "s_add_u32 m0, s68, 4096\n"
    "s_add_u32 s83, s67, 4096\n"
    "v_cvt_pk_bf16_f32 v75, v26, v27\n"
    "v_mfma_f32_16x16x32_bf16 v[64:67], v[196:199], v[148:151], v[112:115]\n"
    "v_exp_f32 v44, v44\n"
    "v_mfma_f32_16x16x32_bf16 v[64:67], v[192:195], v[144:147], v[64:67]\n"
    "v_exp_f32 v45, v45\n"
    "v_mfma_f32_16x16x32_bf16 v[64:67], v[188:191], v[140:143], v[64:67]\n"
    "buffer_load_dwordx4 v1, s[16:19], s83 offen offset:0 sc1 lds\n"
    "v_mfma_f32_16x16x32_bf16 v[64:67], v[184:187], v[136:139], v[64:67]\n"
    "v_cvt_pk_bf16_f32 v78, v28, v29\n"
    "v_cvt_pk_bf16_f32 v92, v44, v45\n"
    "v_mfma_f32_16x16x32_bf16 v[64:67], v[180:183], v[132:135], v[64:67]\n"
    "v_exp_f32 v46, v46\n"
    "v_mfma_f32_16x16x32_bf16 v[64:67], v[176:179], v[128:131], v[64:67]\n"
    "s_waitcnt lgkmcnt(0)\n"
    "v_exp_f32 v47, v47\n"
    "v_mfma_f32_16x16x32_bf16 v[4:7], v[152:155], a[192:195], v[100:103]\n"
    "buffer_load_dwordx4 v1, s[16:19], s83 offen offset:1024 sc1 lds\n"
    "v_mfma_f32_16x16x32_bf16 v[4:7], v[156:159], a[196:199], v[4:7]\n"
    "ds_read_b128 v[176:179], v3 offset:1024\n"
    "ds_read_b128 v[180:183], v3 offset:3072\n"
    "v_mfma_f32_16x16x32_bf16 v[4:7], v[160:163], a[200:203], v[4:7]\n"
    "ds_read_b128 v[184:187], v3 offset:5120\n"
    "v_cvt_pk_bf16_f32 v79, v30, v31\n"
    "v_mfma_f32_16x16x32_bf16 v[4:7], v[164:167], a[204:207], v[4:7]\n"
    "ds_read_b128 v[188:191], v3 offset:7168\n"
    "v_cvt_pk_bf16_f32 v93, v46, v47\n"
    "v_mfma_f32_16x16x32_bf16 v[4:7], v[168:171], a[208:211], v[4:7]\n"
    "ds_read_b128 v[192:195], v3 offset:9216\n"
    "v_cvt_pk_bf16_f32 v82, v32, v33\n"
    "v_mfma_f32_16x16x32_bf16 v[4:7], v[172:175], a[212:215], v[4:7]\n"
    "ds_read_b128 v[196:199], v3 offset:11264\n"
    "v_fma_f32 v201, v201, v232, v236\n"
    "v_mfma_f32_16x16x32_bf16 v[8:11], v[172:175], a[236:239], v[104:107]\n"
    "v_exp_f32 v52, v52\n"
    "v_mfma_f32_16x16x32_bf16 v[8:11], v[168:171], a[232:235], v[8:11]\n"
    "v_exp_f32 v48, v48\n"
    "v_mfma_f32_16x16x32_bf16 v[8:11], v[164:167], a[228:231], v[8:11]\n"
    "v_cvt_pk_bf16_f32 v83, v34, v35\n"
    "v_fma_f32 v202, v202, v232, v236\n"
    "v_mfma_f32_16x16x32_bf16 v[8:11], v[160:163], a[224:227], v[8:11]\n"
    "v_exp_f32 v53, v53\n"
    "v_mfma_f32_16x16x32_bf16 v[8:11], v[156:159], a[220:223], v[8:11]\n"
    "v_cvt_pk_bf16_f32 v86, v52, v53\n"
    "v_fma_f32 v204, v204, v233, v237\n"
    "v_mfma_f32_16x16x32_bf16 v[8:11], v[152:155], a[216:219], v[8:11]\n"
    "v_exp_f32 v54, v54\n"
    "v_mfma_f32_16x16x32_bf16 v[12:15], v[152:155], a[240:243], v[108:111]\n"
    "v_exp_f32 v49, v49\n"
    "v_mfma_f32_16x16x32_bf16 v[12:15], v[156:159], a[244:247], v[12:15]\n"
    "v_exp_f32 v50, v50\n"
    "v_mfma_f32_16x16x32_bf16 v[12:15], v[160:163], a[248:251], v[12:15]\n"
    "v_exp_f32 v55, v55\n"
    "v_mfma_f32_16x16x32_bf16 v[12:15], v[164:167], a[252:255], v[12:15]\n"
    "v_cvt_pk_bf16_f32 v87, v54, v55\n"
    "v_cvt_pk_bf16_f32 v96, v48, v49\n"
    "v_mfma_f32_16x16x32_bf16 v[12:15], v[168:171], v[120:123], v[12:15]\n"
    "v_exp_f32 v51, v51\n"
    "v_mfma_f32_16x16x32_bf16 v[12:15], v[172:175], v[124:127], v[12:15]\n"
    "v_exp_f32 v56, v56\n"
    "v_mfma_f32_16x16x32_bf16 v[16:19], v[172:175], v[148:151], v[112:115]\n"
    "v_cvt_pk_bf16_f32 v97, v50, v51\n"
    "v_fma_f32 v205, v205, v233, v237\n"
    "v_mfma_f32_16x16x32_bf16 v[16:19], v[168:171], v[144:147], v[16:19]\n"
    "v_fma_f32 v206, v206, v233, v237\n"
    "v_fma_f32 v208, v208, v234, v238\n"
    "v_mfma_f32_16x16x32_bf16 v[16:19], v[164:167], v[140:143], v[16:19]\n"
    "v_exp_f32 v57, v57\n"
    "v_mfma_f32_16x16x32_bf16 v[16:19], v[160:163], v[136:139], v[16:19]\n"
    "v_cvt_pk_bf16_f32 v90, v56, v57\n"
    "v_fma_f32 v209, v209, v234, v238\n"
    "v_mfma_f32_16x16x32_bf16 v[16:19], v[156:159], v[132:135], v[16:19]\n"
    "v_exp_f32 v4, v4\n"
    "v_mfma_f32_16x16x32_bf16 v[16:19], v[152:155], v[128:131], v[16:19]\n"
    "s_waitcnt lgkmcnt(0)\n"
    "v_exp_f32 v58, v58\n"
    "v_mfma_f32_16x16x32_bf16 v[20:23], v[176:179], a[192:195], v[100:103]\n"
    "ds_read_b128 v[152:155], v2 offset:24576\n"
    "v_add_f32 v116, v116, v4\n"
    "v_mfma_f32_16x16x32_bf16 v[20:23], v[180:183], a[196:199], v[20:23]\n"
    "ds_read_b128 v[156:159], v2 offset:36864\n"
    "v_fma_f32 v210, v210, v234, v238\n"
    "v_mfma_f32_16x16x32_bf16 v[20:23], v[184:187], a[200:203], v[20:23]\n"
    "ds_read_b128 v[160:163], v2 offset:25600\n"
    "v_fma_f32 v212, v212, v235, v239\n"
    "v_mfma_f32_16x16x32_bf16 v[20:23], v[188:191], a[204:207], v[20:23]\n"
    "ds_read_b128 v[164:167], v2 offset:37888\n"
    "v_fma_f32 v213, v213, v235, v239\n"
    "v_mfma_f32_16x16x32_bf16 v[20:23], v[192:195], a[208:211], v[20:23]\n"
    "ds_read_b128 v[168:171], v2 offset:26624\n"
    "v_fma_f32 v214, v214, v235, v239\n"
    "v_mfma_f32_16x16x32_bf16 v[20:23], v[196:199], a[212:215], v[20:23]\n"
    "ds_read_b128 v[172:175], v2 offset:38912\n"
    "v_add_f32 v116, v116, v52\n"
    "v_mfma_f32_16x16x32_bf16 v[24:27], v[196:199], a[236:239], v[104:107]\n"
    "v_exp_f32 v59, v59\n"
    "v_mfma_f32_16x16x32_bf16 v[24:27], v[192:195], a[232:235], v[24:27]\n"
    "v_cvt_pk_bf16_f32 v91, v58, v59\n"
    "v_add_f32 v116, v116, v53\n"
    "v_mfma_f32_16x16x32_bf16 v[24:27], v[188:191], a[228:231], v[24:27]\n"
    "v_exp_f32 v60, v60\n"
    "v_mfma_f32_16x16x32_bf16 v[24:27], v[184:187], a[224:227], v[24:27]\n"
    "v_exp_f32 v61, v61\n"
    "v_mfma_f32_16x16x32_bf16 v[24:27], v[180:183], a[220:223], v[24:27]\n"
    "v_exp_f32 v62, v62\n"
    "v_mfma_f32_16x16x32_bf16 v[24:27], v[176:179], a[216:219], v[24:27]\n"
    "v_cvt_pk_bf16_f32 v94, v60, v61\n"
    "v_add_f32 v116, v116, v54\n"
    "v_mfma_f32_16x16x32_bf16 v[28:31], v[176:179], a[240:243], v[108:111]\n"
    "v_exp_f32 v5, v5\n"
    "v_mfma_f32_16x16x32_bf16 v[28:31], v[180:183], a[244:247], v[28:31]\n"
    "v_exp_f32 v63, v63\n"
    "v_mfma_f32_16x16x32_bf16 v[28:31], v[184:187], a[248:251], v[28:31]\n"
    "v_cvt_pk_bf16_f32 v95, v62, v63\n"
    "v_add_f32 v116, v116, v5\n"
    "v_mfma_f32_16x16x32_bf16 v[28:31], v[188:191], a[252:255], v[28:31]\n"
    "v_exp_f32 v6, v6\n"
    "v_mfma_f32_16x16x32_bf16 v[28:31], v[192:195], v[120:123], v[28:31]\n"
    "v_exp_f32 v64, v64\n"
    "v_mfma_f32_16x16x32_bf16 v[28:31], v[196:199], v[124:127], v[28:31]\n"
    "v_exp_f32 v7, v7\n"
    "v_mfma_f32_16x16x32_bf16 v[32:35], v[196:199], v[148:151], v[112:115]\n"
    "v_exp_f32 v8, v8\n"
    "v_mfma_f32_16x16x32_bf16 v[32:35], v[192:195], v[144:147], v[32:35]\n"
    "v_exp_f32 v65, v65\n"
    "v_mfma_f32_16x16x32_bf16 v[32:35], v[188:191], v[140:143], v[32:35]\n"
    "v_cvt_pk_bf16_f32 v98, v64, v65\n"
    "v_add_f32 v116, v116, v6\n"
    "v_mfma_f32_16x16x32_bf16 v[32:35], v[184:187], v[136:139], v[32:35]\n"
    "v_exp_f32 v9, v9\n"
    "v_mfma_f32_16x16x32_bf16 v[32:35], v[180:183], v[132:135], v[32:35]\n"
    "s_add_u32 m0, s68, 24576\n"
    "s_add_u32 s83, s67, 24576\n"
    "v_add_f32 v116, v116, v7\n"
    "v_mfma_f32_16x16x32_bf16 v[32:35], v[176:179], v[128:131], v[32:35]\n"
    "s_waitcnt lgkmcnt(0)\n"
    "v_exp_f32 v66, v66\n"
    "v_mfma_f32_16x16x32_bf16 a[0:3], v[152:155], v[68:71], a[0:3]\n"
    "ds_read_b128 v[176:179], v2 offset:27648\n"
    "v_add_f32 v117, v117, v8\n"
    "v_mfma_f32_16x16x32_bf16 a[0:3], v[156:159], v[84:87], a[0:3]\n"
    "ds_read_b128 v[180:183], v2 offset:39936\n"
    "v_add_f32 v117, v117, v9\n"
    "v_mfma_f32_16x16x32_bf16 a[16:19], v[164:167], v[84:87], a[16:19]\n"
    "ds_read_b128 v[184:187], v2 offset:28672\n"
    "v_add_f32 v116, v116, v55\n"
    "v_mfma_f32_16x16x32_bf16 a[16:19], v[160:163], v[68:71], a[16:19]\n"
    "ds_read_b128 v[188:191], v2 offset:40960\n"
    "ds_read_b128 v[232:235], v249 offset:16\n"
    "v_mfma_f32_16x16x32_bf16 a[32:35], v[168:171], v[68:71], a[32:35]\n"
    "buffer_load_dwordx4 v1, s[16:19], s83 offen offset:0 sc1 lds\n"
    "v_mfma_f32_16x16x32_bf16 a[32:35], v[172:175], v[84:87], a[32:35]\n"
    "ds_read_b128 v[192:195], v2 offset:29696\n"
    "ds_read_b128 v[196:199], v2 offset:41984\n"
    "v_mfma_f32_16x16x32_bf16 a[36:39], v[172:175], v[88:91], a[36:39]\n"
    "v_exp_f32 v67, v67\n"
    "v_mfma_f32_16x16x32_bf16 a[36:39], v[168:171], v[72:75], a[36:39]\n"
    "ds_read_b128 v[236:239], v249 offset:48\n"
    "v_cvt_pk_bf16_f32 v99, v66, v67\n"
    "v_mfma_f32_16x16x32_bf16 a[20:23], v[160:163], v[72:75], a[20:23]\n"
    "v_exp_f32 v10, v10\n"
    "v_mfma_f32_16x16x32_bf16 a[20:23], v[164:167], v[88:91], a[20:23]\n"
    "v_exp_f32 v11, v11\n"
    "v_mfma_f32_16x16x32_bf16 a[4:7], v[156:159], v[88:91], a[4:7]\n"
    "buffer_load_dwordx4 v1, s[16:19], s83 offen offset:1024 sc1 lds\n"
    "v_mfma_f32_16x16x32_bf16 a[4:7], v[152:155], v[72:75], a[4:7]\n"
    "v_exp_f32 v12, v12\n"
    "v_mfma_f32_16x16x32_bf16 a[8:11], v[152:155], v[76:79], a[8:11]\n"
    "v_exp_f32 v13, v13\n"
    "v_mfma_f32_16x16x32_bf16 a[8:11], v[156:159], v[92:95], a[8:11]\n"
    "v_add_f32 v117, v117, v10\n"
    "v_add_f32 v117, v117, v11\n"
    "v_mfma_f32_16x16x32_bf16 a[24:27], v[164:167], v[92:95], a[24:27]\n"
    "v_exp_f32 v14, v14\n"
    "v_mfma_f32_16x16x32_bf16 a[24:27], v[160:163], v[76:79], a[24:27]\n"
    "v_add_f32 v117, v117, v41\n"
    "v_add_f32 v117, v117, v42\n"
    "v_mfma_f32_16x16x32_bf16 a[40:43], v[168:171], v[76:79], a[40:43]\n"
    "buffer_load_dwordx4 v1, s[16:19], s83 offen offset:2048 sc1 lds\n"
    "v_mfma_f32_16x16x32_bf16 a[40:43], v[172:175], v[92:95], a[40:43]\n"
    "v_add_f32 v117, v117, v43\n"
    "v_add_f32 v117, v117, v56\n"
    "v_mfma_f32_16x16x32_bf16 a[44:47], v[172:175], v[96:99], a[44:47]\n"
    "v_add_f32 v117, v117, v57\n"
    "v_add_f32 v117, v117, v58\n"
    "v_mfma_f32_16x16x32_bf16 a[44:47], v[168:171], v[80:83], a[44:47]\n"
    "v_add_f32 v117, v117, v59\n"
    "v_cvt_f32_i32 v216, v216\n"
    "v_mfma_f32_16x16x32_bf16 a[28:31], v[160:163], v[80:83], a[28:31]\n"
    "v_exp_f32 v15, v15\n"
    "v_mfma_f32_16x16x32_bf16 a[28:31], v[164:167], v[96:99], a[28:31]\n"
    "v_add_f32 v118, v118, v12\n"
    "v_cvt_f32_i32 v217, v217\n"
    "v_mfma_f32_16x16x32_bf16 a[12:15], v[156:159], v[96:99], a[12:15]\n"
    "buffer_load_dwordx4 v1, s[16:19], s83 offen offset:3072 sc1 lds\n"
    "v_mfma_f32_16x16x32_bf16 a[12:15], v[152:155], v[80:83], a[12:15]\n"
    "s_waitcnt lgkmcnt(1)\n"
    "s_add_u32 m0, s68, 28672\n"
    "s_add_u32 s83, s67, 28672\n"
    "v_mfma_f32_16x16x32_bf16 a[48:51], v[176:179], v[68:71], a[48:51]\n"
    "ds_read_b128 v[152:155], v2 offset:30720\n"
    "v_add_f32 v118, v118, v44\n"
    "v_mfma_f32_16x16x32_bf16 a[48:51], v[180:183], v[84:87], a[48:51]\n"
    "ds_read_b128 v[156:159], v2 offset:43008\n"
    "v_add_f32 v118, v118, v45\n"
    "v_mfma_f32_16x16x32_bf16 a[64:67], v[188:191], v[84:87], a[64:67]\n"
    "ds_read_b128 v[160:163], v2 offset:31744\n"
    "v_add_f32 v118, v118, v46\n"
    "v_mfma_f32_16x16x32_bf16 a[64:67], v[184:187], v[68:71], a[64:67]\n"
    "ds_read_b128 v[164:167], v2 offset:44032\n"
    "v_add_f32 v118, v118, v47\n"
    "v_mfma_f32_16x16x32_bf16 a[80:83], v[192:195], v[68:71], a[80:83]\n"
    "buffer_load_dwordx4 v1, s[16:19], s83 offen offset:0 sc1 lds\n"
    "v_mfma_f32_16x16x32_bf16 a[80:83], v[196:199], v[84:87], a[80:83]\n"
    "ds_read_b128 v[168:171], v2 offset:32768\n"
    "ds_read_b128 v[172:175], v2 offset:45056\n"
    "v_mfma_f32_16x16x32_bf16 a[84:87], v[196:199], v[88:91], a[84:87]\n"
    "v_exp_f32 v16, v16\n"
    "v_mfma_f32_16x16x32_bf16 a[84:87], v[192:195], v[72:75], a[84:87]\n"
    "v_add_f32 v118, v118, v60\n"
    "v_add_f32 v118, v118, v61\n"
    "v_mfma_f32_16x16x32_bf16 a[68:71], v[184:187], v[72:75], a[68:71]\n"
    "v_add_f32 v118, v118, v62\n"
    "v_add_f32 v118, v118, v63\n"
    "v_mfma_f32_16x16x32_bf16 a[68:71], v[188:191], v[88:91], a[68:71]\n"
    "v_add_f32 v118, v118, v13\n"
    "v_cvt_f32_i32 v218, v218\n"
    "v_mfma_f32_16x16x32_bf16 a[52:55], v[180:183], v[88:91], a[52:55]\n"
    "buffer_load_dwordx4 v1, s[16:19], s83 offen offset:1024 sc1 lds\n"
    "v_mfma_f32_16x16x32_bf16 a[52:55], v[176:179], v[72:75], a[52:55]\n"
    "s_add_u32 s67, s67, 49152\n"
    "v_exp_f32 v17, v17\n"
    "v_mfma_f32_16x16x32_bf16 a[56:59], v[176:179], v[76:79], a[56:59]\n"
    "v_add_f32 v118, v118, v14\n"
    "v_cvt_f32_i32 v220, v220\n"
    "v_mfma_f32_16x16x32_bf16 a[56:59], v[180:183], v[92:95], a[56:59]\n"
    "v_exp_f32 v18, v18\n"
    "v_mfma_f32_16x16x32_bf16 a[72:75], v[188:191], v[92:95], a[72:75]\n"
    "v_add_f32 v119, v119, v48\n"
    "v_add_f32 v119, v119, v49\n"
    "v_mfma_f32_16x16x32_bf16 a[72:75], v[184:187], v[76:79], a[72:75]\n"
    "v_add_f32 v119, v119, v50\n"
    "v_add_f32 v119, v119, v51\n"
    "v_mfma_f32_16x16x32_bf16 a[88:91], v[192:195], v[76:79], a[88:91]\n"
    "v_add_f32 v119, v119, v64\n"
    "v_add_f32 v119, v119, v65\n"
    "v_mfma_f32_16x16x32_bf16 a[88:91], v[196:199], v[92:95], a[88:91]\n"
    "v_add_f32 v119, v119, v66\n"
    "v_add_f32 v119, v119, v67\n"
    "v_mfma_f32_16x16x32_bf16 a[92:95], v[196:199], v[96:99], a[92:95]\n"
    "v_cvt_f32_i32 v221, v221\n"
    "v_add_f32 v118, v118, v15\n"
    "v_mfma_f32_16x16x32_bf16 a[92:95], v[192:195], v[80:83], a[92:95]\n"
    "v_exp_f32 v19, v19\n"
    "v_mfma_f32_16x16x32_bf16 a[76:79], v[184:187], v[80:83], a[76:79]\n"
    "v_cvt_f32_i32 v222, v222\n"
    "v_add_f32 v119, v119, v16\n"
    "v_mfma_f32_16x16x32_bf16 a[76:79], v[188:191], v[96:99], a[76:79]\n"
    "v_exp_f32 v20, v20\n"
    "v_mfma_f32_16x16x32_bf16 a[60:63], v[180:183], v[96:99], a[60:63]\n"
    "v_cvt_f32_i32 v224, v224\n"
    "v_add_f32 v119, v119, v17\n"
    "v_mfma_f32_16x16x32_bf16 a[60:63], v[176:179], v[80:83], a[60:63]\n"
    "s_waitcnt lgkmcnt(0)\n"
    "v_exp_f32 v21, v21\n"
    "v_mfma_f32_16x16x32_bf16 a[96:99], v[152:155], v[68:71], a[96:99]\n"
    "ds_read_b128 v[176:179], v2 offset:33792\n"
    "v_cvt_f32_i32 v225, v225\n"
    "v_mfma_f32_16x16x32_bf16 a[96:99], v[156:159], v[84:87], a[96:99]\n"
    "ds_read_b128 v[180:183], v2 offset:46080\n"
    "v_add_f32 v119, v119, v18\n"
    "v_mfma_f32_16x16x32_bf16 a[112:115], v[164:167], v[84:87], a[112:115]\n"
    "ds_read_b128 v[184:187], v2 offset:34816\n"
    "v_cvt_f32_i32 v226, v226\n"
    "v_mfma_f32_16x16x32_bf16 a[112:115], v[160:163], v[68:71], a[112:115]\n"
    "ds_read_b128 v[188:191], v2 offset:47104\n"
    "v_add_f32 v119, v119, v19\n"
    "v_mfma_f32_16x16x32_bf16 a[128:131], v[168:171], v[68:71], a[128:131]\n"
    "ds_read_b128 v[192:195], v2 offset:35840\n"
    "v_cvt_f32_i32 v228, v228\n"
    "v_mfma_f32_16x16x32_bf16 a[128:131], v[172:175], v[84:87], a[128:131]\n"
    "ds_read_b128 v[196:199], v2 offset:48128\n"
    "v_cvt_f32_i32 v229, v229\n"
    "v_mfma_f32_16x16x32_bf16 a[132:135], v[172:175], v[88:91], a[132:135]\n"
    "buffer_load_dwordx2 v[240:241], v245, s[36:39], s81 offen offset:0 sc1\n"
    "v_mfma_f32_16x16x32_bf16 a[132:135], v[168:171], v[72:75], a[132:135]\n"
    "v_exp_f32 v22, v22\n"
    "v_mfma_f32_16x16x32_bf16 a[116:119], v[160:163], v[72:75], a[116:119]\n"
    "v_exp_f32 v23, v23\n"
    "v_mfma_f32_16x16x32_bf16 a[116:119], v[164:167], v[88:91], a[116:119]\n"
    "v_add_f32 v116, v116, v20\n"
    "v_cvt_f32_i32 v230, v230\n"
    "v_mfma_f32_16x16x32_bf16 a[100:103], v[156:159], v[88:91], a[100:103]\n"
    "v_exp_f32 v24, v24\n"
    "v_mfma_f32_16x16x32_bf16 a[100:103], v[152:155], v[72:75], a[100:103]\n"
    "v_add_f32 v116, v116, v21\n"
    "v_fma_f32 v216, v216, v232, v236\n"
    "v_mfma_f32_16x16x32_bf16 a[104:107], v[152:155], v[76:79], a[104:107]\n"
    "v_exp_f32 v25, v25\n"
    "v_mfma_f32_16x16x32_bf16 a[104:107], v[156:159], v[92:95], a[104:107]\n"
    "v_add_f32 v116, v116, v22\n"
    "v_fma_f32 v217, v217, v232, v236\n"
    "v_mfma_f32_16x16x32_bf16 a[120:123], v[164:167], v[92:95], a[120:123]\n"
    "v_exp_f32 v26, v26\n"
    "v_mfma_f32_16x16x32_bf16 a[120:123], v[160:163], v[76:79], a[120:123]\n"
    "v_add_f32 v116, v116, v23\n"
    "v_fma_f32 v218, v218, v232, v236\n"
    "v_mfma_f32_16x16x32_bf16 a[136:139], v[168:171], v[76:79], a[136:139]\n"
    "v_exp_f32 v27, v27\n"
    "v_mfma_f32_16x16x32_bf16 a[136:139], v[172:175], v[92:95], a[136:139]\n"
    "v_add_f32 v117, v117, v24\n"
    "v_fma_f32 v220, v220, v233, v237\n"
    "v_mfma_f32_16x16x32_bf16 a[140:143], v[172:175], v[96:99], a[140:143]\n"
    "v_exp_f32 v28, v28\n"
    "v_mfma_f32_16x16x32_bf16 a[140:143], v[168:171], v[80:83], a[140:143]\n"
    "v_add_f32 v117, v117, v25\n"
    "v_fma_f32 v221, v221, v233, v237\n"
    "v_mfma_f32_16x16x32_bf16 a[124:127], v[160:163], v[80:83], a[124:127]\n"
    "v_exp_f32 v29, v29\n"
    "v_mfma_f32_16x16x32_bf16 a[124:127], v[164:167], v[96:99], a[124:127]\n"
    "v_fma_f32 v222, v222, v233, v237\n"
    "v_add_f32 v117, v117, v26\n"
    "v_mfma_f32_16x16x32_bf16 a[108:111], v[156:159], v[96:99], a[108:111]\n"
    "v_exp_f32 v30, v30\n"
    "v_mfma_f32_16x16x32_bf16 a[108:111], v[152:155], v[80:83], a[108:111]\n"
    "s_waitcnt lgkmcnt(0)\n"
    "v_fma_f32 v224, v224, v234, v238\n"
    "v_add_f32 v117, v117, v27\n"
    "v_mfma_f32_16x16x32_bf16 a[144:147], v[176:179], v[68:71], a[144:147]\n"
    "ds_read_b128 v[152:155], v3 offset:12288\n"
    "v_fma_f32 v225, v225, v234, v238\n"
    "v_mfma_f32_16x16x32_bf16 a[144:147], v[180:183], v[84:87], a[144:147]\n"
    "ds_read_b128 v[156:159], v3 offset:14336\n"
    "v_add_f32 v118, v118, v28\n"
    "v_mfma_f32_16x16x32_bf16 a[160:163], v[188:191], v[84:87], a[160:163]\n"
    "ds_read_b128 v[160:163], v3 offset:16384\n"
    "v_fma_f32 v226, v226, v234, v238\n"
    "v_mfma_f32_16x16x32_bf16 a[160:163], v[184:187], v[68:71], a[160:163]\n"
    "ds_read_b128 v[164:167], v3 offset:18432\n"
    "v_add_f32 v118, v118, v29\n"
    "v_mfma_f32_16x16x32_bf16 a[176:179], v[192:195], v[68:71], a[176:179]\n"
    "ds_read_b128 v[168:171], v3 offset:20480\n"
    "v_fma_f32 v228, v228, v235, v239\n"
    "v_mfma_f32_16x16x32_bf16 a[176:179], v[196:199], v[84:87], a[176:179]\n"
    "ds_read_b128 v[172:175], v3 offset:22528\n"
    "v_add_f32 v118, v118, v30\n"
    "v_mfma_f32_16x16x32_bf16 a[180:183], v[196:199], v[88:91], a[180:183]\n"
    "v_exp_f32 v31, v31\n"
    "v_mfma_f32_16x16x32_bf16 a[180:183], v[192:195], v[72:75], a[180:183]\n"
    "v_exp_f32 v32, v32\n"
    "v_mfma_f32_16x16x32_bf16 a[164:167], v[184:187], v[72:75], a[164:167]\n"
    "v_exp_f32 v33, v33\n"
    "v_mfma_f32_16x16x32_bf16 a[164:167], v[188:191], v[88:91], a[164:167]\n"
    "v_fma_f32 v229, v229, v235, v239\n"
    "v_fma_f32 v230, v230, v235, v239\n"
    "v_mfma_f32_16x16x32_bf16 a[148:151], v[180:183], v[88:91], a[148:151]\n"
    "v_exp_f32 v34, v34\n"
    "v_mfma_f32_16x16x32_bf16 a[148:151], v[176:179], v[72:75], a[148:151]\n"
    "v_add_f32 v118, v118, v31\n"
    "v_add_f32 v119, v119, v32\n"
    "v_mfma_f32_16x16x32_bf16 a[152:155], v[176:179], v[76:79], a[152:155]\n"
    "v_exp_f32 v35, v35\n"
    "v_mfma_f32_16x16x32_bf16 a[152:155], v[180:183], v[92:95], a[152:155]\n"
    "v_add_f32 v119, v119, v33\n"
    "v_add_f32 v119, v119, v34\n"
    "v_mfma_f32_16x16x32_bf16 a[168:171], v[188:191], v[92:95], a[168:171]\n"
    "s_waitcnt vmcnt(7)\n"
    "ds_add_u32 v252, v251\n"
    "v_mfma_f32_16x16x32_bf16 a[168:171], v[184:187], v[76:79], a[168:171]\n"
    "v_add_f32 v119, v119, v35\n"
    "v_mfma_f32_16x16x32_bf16 a[184:187], v[192:195], v[76:79], a[184:187]\n"
    "v_mfma_f32_16x16x32_bf16 a[184:187], v[196:199], v[92:95], a[184:187]\n"
    "v_mfma_f32_16x16x32_bf16 a[188:191], v[196:199], v[96:99], a[188:191]\n"
    "v_mfma_f32_16x16x32_bf16 a[188:191], v[192:195], v[80:83], a[188:191]\n"
    "v_mfma_f32_16x16x32_bf16 a[172:175], v[184:187], v[80:83], a[172:175]\n"
    "s_sub_u32 s74, s68, s65\n"
    "v_mfma_f32_16x16x32_bf16 a[172:175], v[188:191], v[96:99], a[172:175]\n"
    "s_add_u32 s66, s66, 1\n"
    "v_mov_b32 v2, v3\n"
    "v_add_u32 v3, s74, v1\n"
    "v_mfma_f32_16x16x32_bf16 a[156:159], v[180:183], v[96:99], a[156:159]\n"
    "s_waitcnt lgkmcnt(1)\n"
    "s_cmp_lt_u32 s66, 31\n"
    "v_mfma_f32_16x16x32_bf16 a[156:159], v[176:179], v[80:83], a[156:159]\n"
    "s_cbranch_scc1 .LPV\n"
    "s_branch .Ltail\n"
    ".Ltail:\n"
    "v_mfma_f32_16x16x32_bf16 v[36:39], v[152:155], a[192:195], v[100:103]\n"
    "ds_read_b128 v[176:179], v2 offset:13312\n"
    "v_cvt_pk_bf16_f32 v68, v4, v5\n"
    "v_mfma_f32_16x16x32_bf16 v[36:39], v[156:159], a[196:199], v[36:39]\n"
    "ds_read_b128 v[180:183], v2 offset:15360\n"
    "v_mfma_f32_16x16x32_bf16 v[36:39], v[160:163], a[200:203], v[36:39]\n"
    "ds_read_b128 v[184:187], v2 offset:17408\n"
    "v_mfma_f32_16x16x32_bf16 v[36:39], v[164:167], a[204:207], v[36:39]\n"
    "ds_read_b128 v[188:191], v2 offset:19456\n"
    "v_cvt_pk_bf16_f32 v69, v6, v7\n"
    "v_mfma_f32_16x16x32_bf16 v[36:39], v[168:171], a[208:211], v[36:39]\n"
    "ds_read_b128 v[192:195], v2 offset:21504\n"
    "v_mfma_f32_16x16x32_bf16 v[36:39], v[172:175], a[212:215], v[36:39]\n"
    "ds_read_b128 v[196:199], v2 offset:23552\n"
    "v_mfma_f32_16x16x32_bf16 v[40:43], v[172:175], a[236:239], v[104:107]\n"
    "v_mfma_f32_16x16x32_bf16 v[40:43], v[168:171], a[232:235], v[40:43]\n"
    "v_cvt_pk_bf16_f32 v72, v8, v9\n"
    "v_mfma_f32_16x16x32_bf16 v[40:43], v[164:167], a[228:231], v[40:43]\n"
    "s_nop 2\n"
    "v_exp_f32 v36, v36\n"
    "v_mfma_f32_16x16x32_bf16 v[40:43], v[160:163], a[224:227], v[40:43]\n"
    "v_add_f32 v116, v116, v36\n"
    "v_mfma_f32_16x16x32_bf16 v[40:43], v[156:159], a[220:223], v[40:43]\n"
    "v_exp_f32 v37, v37\n"
    "v_mfma_f32_16x16x32_bf16 v[40:43], v[152:155], a[216:219], v[40:43]\n"
    "v_cvt_pk_bf16_f32 v73, v10, v11\n"
    "v_cvt_pk_bf16_f32 v84, v36, v37\n"
    "v_mfma_f32_16x16x32_bf16 v[44:47], v[152:155], a[240:243], v[108:111]\n"
    "v_add_f32 v116, v116, v37\n"
    "v_mfma_f32_16x16x32_bf16 v[44:47], v[156:159], a[244:247], v[44:47]\n"
    "v_exp_f32 v38, v38\n"
    "v_mfma_f32_16x16x32_bf16 v[44:47], v[160:163], a[248:251], v[44:47]\n"
    "v_add_f32 v116, v116, v38\n"
    "v_mfma_f32_16x16x32_bf16 v[44:47], v[164:167], a[252:255], v[44:47]\n"
    "v_cvt_pk_bf16_f32 v76, v12, v13\n"
    "v_mfma_f32_16x16x32_bf16 v[44:47], v[168:171], v[120:123], v[44:47]\n"
    "v_exp_f32 v39, v39\n"
    "v_mfma_f32_16x16x32_bf16 v[44:47], v[172:175], v[124:127], v[44:47]\n"
    "v_cvt_pk_bf16_f32 v85, v38, v39\n"
    "v_add_f32 v116, v116, v39\n"
    "v_mfma_f32_16x16x32_bf16 v[48:51], v[172:175], v[148:151], v[112:115]\n"
    "ds_read_b32 v250, v252\n"
    "v_cvt_pk_bf16_f32 v77, v14, v15\n"
    "v_mfma_f32_16x16x32_bf16 v[48:51], v[168:171], v[144:147], v[48:51]\n"
    "v_exp_f32 v40, v40\n"
    "v_mfma_f32_16x16x32_bf16 v[48:51], v[164:167], v[140:143], v[48:51]\n"
    "v_add_f32 v117, v117, v40\n"
    "v_mfma_f32_16x16x32_bf16 v[48:51], v[160:163], v[136:139], v[48:51]\n"
    "v_mfma_f32_16x16x32_bf16 v[48:51], v[156:159], v[132:135], v[48:51]\n"
    "v_cvt_pk_bf16_f32 v80, v16, v17\n"
    "v_mfma_f32_16x16x32_bf16 v[48:51], v[152:155], v[128:131], v[48:51]\n"
    "s_waitcnt lgkmcnt(1)\n"
    "v_exp_f32 v41, v41\n"
    "v_mfma_f32_16x16x32_bf16 v[52:55], v[176:179], a[192:195], v[100:103]\n"
    "s_waitcnt lgkmcnt(0)\n"
    "v_cmp_le_u32 s[56:57], s59, v250\n"
    "v_cvt_pk_bf16_f32 v88, v40, v41\n"
    "v_mfma_f32_16x16x32_bf16 v[52:55], v[180:183], a[196:199], v[52:55]\n"
    "v_exp_f32 v42, v42\n"
    "v_mfma_f32_16x16x32_bf16 v[52:55], v[184:187], a[200:203], v[52:55]\n"
    "s_cmp_eq_u64 s[56:57], -1\n"
    "v_mfma_f32_16x16x32_bf16 v[52:55], v[188:191], a[204:207], v[52:55]\n"
    "s_cbranch_scc0 .Lpoll5\n"
    ".Lpoll5r:\n"
    "v_mfma_f32_16x16x32_bf16 v[52:55], v[192:195], a[208:211], v[52:55]\n"
    "ds_read_b128 v[152:155], v2 offset:24576\n"
    "ds_read_b128 v[156:159], v2 offset:36864\n"
    "v_mfma_f32_16x16x32_bf16 v[52:55], v[196:199], a[212:215], v[52:55]\n"
    "ds_read_b128 v[160:163], v2 offset:25600\n"
    "ds_read_b128 v[164:167], v2 offset:37888\n"
    "v_mfma_f32_16x16x32_bf16 v[56:59], v[196:199], a[236:239], v[104:107]\n"
    "ds_read_b128 v[168:171], v2 offset:26624\n"
    "ds_read_b128 v[172:175], v2 offset:38912\n"
    "v_mfma_f32_16x16x32_bf16 v[56:59], v[192:195], a[232:235], v[56:59]\n"
    "v_cvt_pk_bf16_f32 v81, v18, v19\n"
    "v_cvt_pk_bf16_f32 v70, v20, v21\n"
    "v_mfma_f32_16x16x32_bf16 v[56:59], v[188:191], a[228:231], v[56:59]\n"
    "v_exp_f32 v43, v43\n"
    "v_mfma_f32_16x16x32_bf16 v[56:59], v[184:187], a[224:227], v[56:59]\n"
    "v_cvt_pk_bf16_f32 v71, v22, v23\n"
    "v_cvt_pk_bf16_f32 v89, v42, v43\n"
    "v_mfma_f32_16x16x32_bf16 v[56:59], v[180:183], a[220:223], v[56:59]\n"
    "v_exp_f32 v44, v44\n"
    "v_mfma_f32_16x16x32_bf16 v[56:59], v[176:179], a[216:219], v[56:59]\n"
    "v_exp_f32 v45, v45\n"
    "v_mfma_f32_16x16x32_bf16 v[60:63], v[176:179], a[240:243], v[108:111]\n"
    "v_exp_f32 v46, v46\n"
    "v_mfma_f32_16x16x32_bf16 v[60:63], v[180:183], a[244:247], v[60:63]\n"
    "v_cvt_pk_bf16_f32 v74, v24, v25\n"
    "v_cvt_pk_bf16_f32 v92, v44, v45\n"
    "v_mfma_f32_16x16x32_bf16 v[60:63], v[184:187], a[248:251], v[60:63]\n"
    "v_add_f32 v117, v117, v41\n"
    "v_add_f32 v117, v117, v42\n"
    "v_mfma_f32_16x16x32_bf16 v[60:63], v[188:191], a[252:255], v[60:63]\n"
    "v_exp_f32 v47, v47\n"
    "v_mfma_f32_16x16x32_bf16 v[60:63], v[192:195], v[120:123], v[60:63]\n"
    "v_cvt_pk_bf16_f32 v93, v46, v47\n"
    "v_add_f32 v117, v117, v43\n"
    "v_mfma_f32_16x16x32_bf16 v[60:63], v[196:199], v[124:127], v[60:63]\n"
    "v_cvt_pk_bf16_f32 v75, v26, v27\n"
    "v_add_f32 v118, v118, v44\n"
    "v_mfma_f32_16x16x32_bf16 v[64:67], v[196:199], v[148:151], v[112:115]\n"
    "v_exp_f32 v48, v48\n"
    "v_mfma_f32_16x16x32_bf16 v[64:67], v[192:195], v[144:147], v[64:67]\n"
    "v_add_f32 v118, v118, v45\n"
    "v_add_f32 v118, v118, v46\n"
    "v_mfma_f32_16x16x32_bf16 v[64:67], v[188:191], v[140:143], v[64:67]\n"
    "v_add_f32 v118, v118, v47\n"
    "v_add_f32 v119, v119, v48\n"
    "v_mfma_f32_16x16x32_bf16 v[64:67], v[184:187], v[136:139], v[64:67]\n"
    "v_cvt_pk_bf16_f32 v78, v28, v29\n"
    "v_mfma_f32_16x16x32_bf16 v[64:67], v[180:183], v[132:135], v[64:67]\n"
    "v_exp_f32 v49, v49\n"
    "v_mfma_f32_16x16x32_bf16 v[64:67], v[176:179], v[128:131], v[64:67]\n"
    "v_cvt_pk_bf16_f32 v96, v48, v49\n"
    "v_add_f32 v119, v119, v49\n"
    "v_cvt_pk_bf16_f32 v79, v30, v31\n"
    "v_exp_f32 v50, v50\n"
    "s_nop 0\n"
    "v_add_f32 v119, v119, v50\n"
    "v_exp_f32 v51, v51\n"
    "v_cvt_pk_bf16_f32 v82, v32, v33\n"
    "v_cvt_pk_bf16_f32 v97, v50, v51\n"
    "v_add_f32 v119, v119, v51\n"
    "v_exp_f32 v52, v52\n"
    "s_nop 0\n"
    "v_add_f32 v116, v116, v52\n"
    "v_cvt_pk_bf16_f32 v83, v34, v35\n"
    "v_exp_f32 v53, v53\n"
    "s_nop 0\n"
    "v_cvt_pk_bf16_f32 v86, v52, v53\n"
    "v_add_f32 v116, v116, v53\n"
    "v_exp_f32 v54, v54\n"
    "s_nop 0\n"
    "v_add_f32 v116, v116, v54\n"
    "v_exp_f32 v55, v55\n"
    "s_nop 0\n"
    "v_cvt_pk_bf16_f32 v87, v54, v55\n"
    "v_add_f32 v116, v116, v55\n"
    "v_exp_f32 v56, v56\n"
    "s_nop 0\n"
    "v_add_f32 v117, v117, v56\n"
    "v_exp_f32 v57, v57\n"
    "s_nop 0\n"
    "v_cvt_pk_bf16_f32 v90, v56, v57\n"
    "v_add_f32 v117, v117, v57\n"
    "v_exp_f32 v58, v58\n"
    "s_nop 0\n"
    "v_add_f32 v117, v117, v58\n"
    "v_exp_f32 v59, v59\n"
    "s_nop 0\n"
    "v_cvt_pk_bf16_f32 v91, v58, v59\n"
    "v_add_f32 v117, v117, v59\n"
    "v_exp_f32 v60, v60\n"
    "s_nop 0\n"
    "v_add_f32 v118, v118, v60\n"
    "v_exp_f32 v61, v61\n"
    "s_nop 0\n"
    "v_cvt_pk_bf16_f32 v94, v60, v61\n"
    "v_add_f32 v118, v118, v61\n"
    "v_exp_f32 v62, v62\n"
    "s_nop 0\n"
    "v_add_f32 v118, v118, v62\n"
    "v_exp_f32 v63, v63\n"
    "s_nop 0\n"
    "v_cvt_pk_bf16_f32 v95, v62, v63\n"
    "v_add_f32 v118, v118, v63\n"
    "v_exp_f32 v64, v64\n"
    "s_nop 0\n"
    "v_add_f32 v119, v119, v64\n"
    "v_exp_f32 v65, v65\n"
    "s_nop 0\n"
    "v_cvt_pk_bf16_f32 v98, v64, v65\n"
    "v_add_f32 v119, v119, v65\n"
    "v_exp_f32 v66, v66\n"
    "s_waitcnt lgkmcnt(0)\n"
    "v_add_f32 v119, v119, v66\n"
    "v_mfma_f32_16x16x32_bf16 a[0:3], v[152:155], v[68:71], a[0:3]\n"
    "ds_read_b128 v[176:179], v2 offset:27648\n"
    "v_mfma_f32_16x16x32_bf16 a[0:3], v[156:159], v[84:87], a[0:3]\n"
    "ds_read_b128 v[180:183], v2 offset:39936\n"
    "v_mfma_f32_16x16x32_bf16 a[16:19], v[164:167], v[84:87], a[16:19]\n"
    "ds_read_b128 v[184:187], v2 offset:28672\n"
    "v_mfma_f32_16x16x32_bf16 a[16:19], v[160:163], v[68:71], a[16:19]\n"
    "ds_read_b128 v[188:191], v2 offset:40960\n"
    "v_mfma_f32_16x16x32_bf16 a[32:35], v[168:171], v[68:71], a[32:35]\n"
    "ds_read_b128 v[192:195], v2 offset:29696\n"
    "v_mfma_f32_16x16x32_bf16 a[32:35], v[172:175], v[84:87], a[32:35]\n"
    "ds_read_b128 v[196:199], v2 offset:41984\n"
    "v_mfma_f32_16x16x32_bf16 a[36:39], v[172:175], v[88:91], a[36:39]\n"
    "v_exp_f32 v67, v67\n"
    "v_mfma_f32_16x16x32_bf16 a[36:39], v[168:171], v[72:75], a[36:39]\n"
    "v_cvt_pk_bf16_f32 v99, v66, v67\n"
    "v_add_f32 v119, v119, v67\n"
    "v_mfma_f32_16x16x32_bf16 a[20:23], v[160:163], v[72:75], a[20:23]\n"
    "v_mfma_f32_16x16x32_bf16 a[20:23], v[164:167], v[88:91], a[20:23]\n"
    "v_mfma_f32_16x16x32_bf16 a[4:7], v[156:159], v[88:91], a[4:7]\n"
    "v_mfma_f32_16x16x32_bf16 a[4:7], v[152:155], v[72:75], a[4:7]\n"
    "v_mfma_f32_16x16x32_bf16 a[8:11], v[152:155], v[76:79], a[8:11]\n"
    "v_mfma_f32_16x16x32_bf16 a[8:11], v[156:159], v[92:95], a[8:11]\n"
    "v_mfma_f32_16x16x32_bf16 a[24:27], v[164:167], v[92:95], a[24:27]\n"
    "v_mfma_f32_16x16x32_bf16 a[24:27], v[160:163], v[76:79], a[24:27]\n"
    "v_mfma_f32_16x16x32_bf16 a[40:43], v[168:171], v[76:79], a[40:43]\n"
    "v_mfma_f32_16x16x32_bf16 a[40:43], v[172:175], v[92:95], a[40:43]\n"
    "v_mfma_f32_16x16x32_bf16 a[44:47], v[172:175], v[96:99], a[44:47]\n"
    "v_mfma_f32_16x16x32_bf16 a[44:47], v[168:171], v[80:83], a[44:47]\n"
    "v_mfma_f32_16x16x32_bf16 a[28:31], v[160:163], v[80:83], a[28:31]\n"
    "v_mfma_f32_16x16x32_bf16 a[28:31], v[164:167], v[96:99], a[28:31]\n"
    "v_mfma_f32_16x16x32_bf16 a[12:15], v[156:159], v[96:99], a[12:15]\n"
    "v_mfma_f32_16x16x32_bf16 a[12:15], v[152:155], v[80:83], a[12:15]\n"
    "s_waitcnt lgkmcnt(0)\n"
    "v_mfma_f32_16x16x32_bf16 a[48:51], v[176:179], v[68:71], a[48:51]\n"
    "ds_read_b128 v[152:155], v2 offset:30720\n"
    "v_mfma_f32_16x16x32_bf16 a[48:51], v[180:183], v[84:87], a[48:51]\n"
    "ds_read_b128 v[156:159], v2 offset:43008\n"
    "v_mfma_f32_16x16x32_bf16 a[64:67], v[188:191], v[84:87], a[64:67]\n"
    "ds_read_b128 v[160:163], v2 offset:31744\n"
    "v_mfma_f32_16x16x32_bf16 a[64:67], v[184:187], v[68:71], a[64:67]\n"
    "ds_read_b128 v[164:167], v2 offset:44032\n"
    "v_mfma_f32_16x16x32_bf16 a[80:83], v[192:195], v[68:71], a[80:83]\n"
    "ds_read_b128 v[168:171], v2 offset:32768\n"
    "v_mfma_f32_16x16x32_bf16 a[80:83], v[196:199], v[84:87], a[80:83]\n"
    "ds_read_b128 v[172:175], v2 offset:45056\n"
    "v_mfma_f32_16x16x32_bf16 a[84:87], v[196:199], v[88:91], a[84:87]\n"
    "v_mfma_f32_16x16x32_bf16 a[84:87], v[192:195], v[72:75], a[84:87]\n"
    "v_mfma_f32_16x16x32_bf16 a[68:71], v[184:187], v[72:75], a[68:71]\n"
    "v_mfma_f32_16x16x32_bf16 a[68:71], v[188:191], v[88:91], a[68:71]\n"
    "v_mfma_f32_16x16x32_bf16 a[52:55], v[180:183], v[88:91], a[52:55]\n"
    "v_mfma_f32_16x16x32_bf16 a[52:55], v[176:179], v[72:75], a[52:55]\n"
    "v_mfma_f32_16x16x32_bf16 a[56:59], v[176:179], v[76:79], a[56:59]\n"
    "v_mfma_f32_16x16x32_bf16 a[56:59], v[180:183], v[92:95], a[56:59]\n"
    "v_mfma_f32_16x16x32_bf16 a[72:75], v[188:191], v[92:95], a[72:75]\n"
    "v_mfma_f32_16x16x32_bf16 a[72:75], v[184:187], v[76:79], a[72:75]\n"
    "v_mfma_f32_16x16x32_bf16 a[88:91], v[192:195], v[76:79], a[88:91]\n"
    "v_mfma_f32_16x16x32_bf16 a[88:91], v[196:199], v[92:95], a[88:91]\n"
    "v_mfma_f32_16x16x32_bf16 a[92:95], v[196:199], v[96:99], a[92:95]\n"
    "v_mfma_f32_16x16x32_bf16 a[92:95], v[192:195], v[80:83], a[92:95]\n"
    "v_mfma_f32_16x16x32_bf16 a[76:79], v[184:187], v[80:83], a[76:79]\n"
    "v_mfma_f32_16x16x32_bf16 a[76:79], v[188:191], v[96:99], a[76:79]\n"
    "v_mfma_f32_16x16x32_bf16 a[60:63], v[180:183], v[96:99], a[60:63]\n"
    "v_mfma_f32_16x16x32_bf16 a[60:63], v[176:179], v[80:83], a[60:63]\n"
    "s_waitcnt lgkmcnt(0)\n"
    "v_mfma_f32_16x16x32_bf16 a[96:99], v[152:155], v[68:71], a[96:99]\n"
    "ds_read_b128 v[176:179], v2 offset:33792\n"
    "v_mfma_f32_16x16x32_bf16 a[96:99], v[156:159], v[84:87], a[96:99]\n"
    "ds_read_b128 v[180:183], v2 offset:46080\n"
    "v_mfma_f32_16x16x32_bf16 a[112:115], v[164:167], v[84:87], a[112:115]\n"
    "ds_read_b128 v[184:187], v2 offset:34816\n"
    "v_mfma_f32_16x16x32_bf16 a[112:115], v[160:163], v[68:71], a[112:115]\n"
    "ds_read_b128 v[188:191], v2 offset:47104\n"
    "v_mfma_f32_16x16x32_bf16 a[128:131], v[168:171], v[68:71], a[128:131]\n"
    "ds_read_b128 v[192:195], v2 offset:35840\n"
    "v_mfma_f32_16x16x32_bf16 a[128:131], v[172:175], v[84:87], a[128:131]\n"
    "ds_read_b128 v[196:199], v2 offset:48128\n"
    "v_mfma_f32_16x16x32_bf16 a[132:135], v[172:175], v[88:91], a[132:135]\n"
    "v_mfma_f32_16x16x32_bf16 a[132:135], v[168:171], v[72:75], a[132:135]\n"
    "v_mfma_f32_16x16x32_bf16 a[116:119], v[160:163], v[72:75], a[116:119]\n"
    "v_mfma_f32_16x16x32_bf16 a[116:119], v[164:167], v[88:91], a[116:119]\n"
    "v_mfma_f32_16x16x32_bf16 a[100:103], v[156:159], v[88:91], a[100:103]\n"
    "v_mfma_f32_16x16x32_bf16 a[100:103], v[152:155], v[72:75], a[100:103]\n"
    "v_mfma_f32_16x16x32_bf16 a[104:107], v[152:155], v[76:79], a[104:107]\n"
    "v_mfma_f32_16x16x32_bf16 a[104:107], v[156:159], v[92:95], a[104:107]\n"
    "v_mfma_f32_16x16x32_bf16 a[120:123], v[164:167], v[92:95], a[120:123]\n"
    "v_mfma_f32_16x16x32_bf16 a[120:123], v[160:163], v[76:79], a[120:123]\n"
    "v_mfma_f32_16x16x32_bf16 a[136:139], v[168:171], v[76:79], a[136:139]\n"
    "v_mfma_f32_16x16x32_bf16 a[136:139], v[172:175], v[92:95], a[136:139]\n"
    "v_mfma_f32_16x16x32_bf16 a[140:143], v[172:175], v[96:99], a[140:143]\n"
    "v_mfma_f32_16x16x32_bf16 a[140:143], v[168:171], v[80:83], a[140:143]\n"
    "v_mfma_f32_16x16x32_bf16 a[124:127], v[160:163], v[80:83], a[124:127]\n"
    "v_mfma_f32_16x16x32_bf16 a[124:127], v[164:167], v[96:99], a[124:127]\n"
    "v_mfma_f32_16x16x32_bf16 a[108:111], v[156:159], v[96:99], a[108:111]\n"
    "v_mfma_f32_16x16x32_bf16 a[108:111], v[152:155], v[80:83], a[108:111]\n"
    "s_waitcnt lgkmcnt(0)\n"
    "v_mfma_f32_16x16x32_bf16 a[144:147], v[176:179], v[68:71], a[144:147]\n"
    "v_mfma_f32_16x16x32_bf16 a[144:147], v[180:183], v[84:87], a[144:147]\n"
    "v_mfma_f32_16x16x32_bf16 a[160:163], v[188:191], v[84:87], a[160:163]\n"
    "v_mfma_f32_16x16x32_bf16 a[160:163], v[184:187], v[68:71], a[160:163]\n"
    "v_mfma_f32_16x16x32_bf16 a[176:179], v[192:195], v[68:71], a[176:179]\n"
    "v_mfma_f32_16x16x32_bf16 a[176:179], v[196:199], v[84:87], a[176:179]\n"
    "v_mfma_f32_16x16x32_bf16 a[180:183], v[196:199], v[88:91], a[180:183]\n"
    "v_mfma_f32_16x16x32_bf16 a[180:183], v[192:195], v[72:75], a[180:183]\n"
    "v_mfma_f32_16x16x32_bf16 a[164:167], v[184:187], v[72:75], a[164:167]\n"
    "v_mfma_f32_16x16x32_bf16 a[164:167], v[188:191], v[88:91], a[164:167]\n"
    "v_mfma_f32_16x16x32_bf16 a[148:151], v[180:183], v[88:91], a[148:151]\n"
    "v_mfma_f32_16x16x32_bf16 a[148:151], v[176:179], v[72:75], a[148:151]\n"
    "v_mfma_f32_16x16x32_bf16 a[152:155], v[176:179], v[76:79], a[152:155]\n"
    "v_mfma_f32_16x16x32_bf16 a[152:155], v[180:183], v[92:95], a[152:155]\n"
    "v_mfma_f32_16x16x32_bf16 a[168:171], v[188:191], v[92:95], a[168:171]\n"
    "v_mfma_f32_16x16x32_bf16 a[168:171], v[184:187], v[76:79], a[168:171]\n"
    "v_mfma_f32_16x16x32_bf16 a[184:187], v[192:195], v[76:79], a[184:187]\n"
    "v_mfma_f32_16x16x32_bf16 a[184:187], v[196:199], v[92:95], a[184:187]\n"
    "v_mfma_f32_16x16x32_bf16 a[188:191], v[196:199], v[96:99], a[188:191]\n"
    "v_mfma_f32_16x16x32_bf16 a[188:191], v[192:195], v[80:83], a[188:191]\n"
    "v_mfma_f32_16x16x32_bf16 a[172:175], v[184:187], v[80:83], a[172:175]\n"
    "v_mfma_f32_16x16x32_bf16 a[172:175], v[188:191], v[96:99], a[172:175]\n"
    "v_mfma_f32_16x16x32_bf16 a[156:159], v[180:183], v[96:99], a[156:159]\n"
    "v_mfma_f32_16x16x32_bf16 a[156:159], v[176:179], v[80:83], a[156:159]\n"
    "s_waitcnt vmcnt(0)\n"
    "v_lshrrev_b32 v232, 4, v1\n"
    "s_mul_i32 s72, s84, 25600\n"
    "s_mul_i32 s73, s84, 24576\n"
    "v_lshlrev_b32 v236, 16, v1\n"
    "buffer_store_dwordx2 v[242:243], v236, s[36:39], s82 offen offset:0 sc1\n"
    "v_and_b32 v234, 15, v232\n"
    "v_lshrrev_b32 v235, 4, v232\n"
    "v_mul_u32_u24 v233, 400, v234\n"
    "v_mul_u32_u24 v235, 24, v235\n"
    "v_add_u32 v233, v235, v233\n"
    "v_add_u32 v233, s72, v233\n"
    "v_add_u32 v234, 0, v232\n"
    "v_mul_u32_u24 v235, 2731, v234\n"
    "v_lshrrev_b32 v235, 16, v235\n"
    "v_lshlrev_b32 v234, 4, v234\n"
    "v_lshl_add_u32 v237, v235, 4, v234\n"
    "v_add_u32 v237, s72, v237\n"
    "v_add_u32 v234, 64, v232\n"
    "v_mul_u32_u24 v235, 2731, v234\n"
    "v_lshrrev_b32 v235, 16, v235\n"
    "v_lshlrev_b32 v234, 4, v234\n"
    "v_lshl_add_u32 v238, v235, 4, v234\n"
    "v_add_u32 v238, s72, v238\n"
    "v_add_u32 v234, 128, v232\n"
    "v_mul_u32_u24 v235, 2731, v234\n"
    "v_lshrrev_b32 v235, 16, v235\n"
    "v_lshlrev_b32 v234, 4, v234\n"
    "v_lshl_add_u32 v239, v235, 4, v234\n"
    "v_add_u32 v239, s72, v239\n"
    "s_mov_b32 s74, 0x5d800000\n"
    "s_mov_b32 s75, 0x17800000\n"
    "s_mov_b64 s[54:55], 0\n"
    "v_mov_b32 v234, v116\n"
    "s_nop 1\n"
    "v_permlane32_swap_b32 v116, v234\n"
    "v_add_f32 v116, v116, v234\n"
    "v_mov_b32 v234, v116\n"
    "s_nop 1\n"
    "v_permlane16_swap_b32 v116, v234\n"
    "v_add_f32 v116, v116, v234\n"
    "v_cmp_ngt_f32 vcc, s74, v116\n"
    "s_nop 0\n"
    "s_or_b64 s[54:55], s[54:55], vcc\n"
    "v_cmp_nlt_f32 vcc, s75, v116\n"
    "s_nop 0\n"
    "s_or_b64 s[54:55], s[54:55], vcc\n"
    "v_mov_b32 v234, v117\n"
    "s_nop 1\n"
    "v_permlane32_swap_b32 v117, v234\n"
    "v_add_f32 v117, v117, v234\n"
    "v_mov_b32 v234, v117\n"
    "s_nop 1\n"
    "v_permlane16_swap_b32 v117, v234\n"
    "v_add_f32 v117, v117, v234\n"
    "v_cmp_ngt_f32 vcc, s74, v117\n"
    "s_nop 0\n"
    "s_or_b64 s[54:55], s[54:55], vcc\n"
    "v_cmp_nlt_f32 vcc, s75, v117\n"
    "s_nop 0\n"
    "s_or_b64 s[54:55], s[54:55], vcc\n"
    "v_mov_b32 v234, v118\n"
    "s_nop 1\n"
    "v_permlane32_swap_b32 v118, v234\n"
    "v_add_f32 v118, v118, v234\n"
    "v_mov_b32 v234, v118\n"
    "s_nop 1\n"
    "v_permlane16_swap_b32 v118, v234\n"
    "v_add_f32 v118, v118, v234\n"
    "v_cmp_ngt_f32 vcc, s74, v118\n"
    "s_nop 0\n"
    "s_or_b64 s[54:55], s[54:55], vcc\n"
    "v_cmp_nlt_f32 vcc, s75, v118\n"
    "s_nop 0\n"
    "s_or_b64 s[54:55], s[54:55], vcc\n"
    "v_mov_b32 v234, v119\n"
    "s_nop 1\n"
    "v_permlane32_swap_b32 v119, v234\n"
    "v_add_f32 v119, v119, v234\n"
    "v_mov_b32 v234, v119\n"
    "s_nop 1\n"
    "v_permlane16_swap_b32 v119, v234\n"
    "v_add_f32 v119, v119, v234\n"
    "v_cmp_ngt_f32 vcc, s74, v119\n"
    "s_nop 0\n"
    "s_or_b64 s[54:55], s[54:55], vcc\n"
    "v_cmp_nlt_f32 vcc, s75, v119\n"
    "s_nop 0\n"
    "s_or_b64 s[54:55], s[54:55], vcc\n"
    "s_or_b32 %0, s54, s55\n"
    "v_rcp_f32 v116, v116\n"
    "v_rcp_f32 v117, v117\n"
    "v_rcp_f32 v118, v118\n"
    "v_rcp_f32 v119, v119\n"
    "s_waitcnt lgkmcnt(0)\n"
    "s_barrier\n"
    "v_accvgpr_read_b32 v4, a0\n"
    "v_accvgpr_read_b32 v7, a1\n"
    "v_accvgpr_read_b32 v10, a2\n"
    "v_accvgpr_read_b32 v13, a3\n"
    "v_accvgpr_read_b32 v5, a64\n"
    "v_accvgpr_read_b32 v8, a65\n"
    "v_accvgpr_read_b32 v11, a66\n"
    "v_accvgpr_read_b32 v14, a67\n"
    "v_accvgpr_read_b32 v6, a128\n"
    "v_accvgpr_read_b32 v9, a129\n"
    "v_accvgpr_read_b32 v12, a130\n"
    "v_accvgpr_read_b32 v15, a131\n"
    "v_pk_mul_f32 v[4:5], v[4:5], v[116:117] op_sel_hi:[1,0]\n"
    "v_pk_mul_f32 v[6:7], v[6:7], v[116:117] op_sel_hi:[1,0]\n"
    "v_pk_mul_f32 v[8:9], v[8:9], v[116:117] op_sel_hi:[1,0]\n"
    "v_pk_mul_f32 v[10:11], v[10:11], v[116:117] op_sel_hi:[1,0]\n"
    "v_pk_mul_f32 v[12:13], v[12:13], v[116:117] op_sel_hi:[1,0]\n"
    "v_pk_mul_f32 v[14:15], v[14:15], v[116:117] op_sel_hi:[1,0]\n"
    "v_cvt_pk_bf16_f32 v16, v4, v5\n"
    "v_cvt_pk_bf16_f32 v17, v6, v7\n"
    "v_cvt_pk_bf16_f32 v18, v8, v9\n"
    "v_cvt_pk_bf16_f32 v19, v10, v11\n"
    "v_cvt_pk_bf16_f32 v20, v12, v13\n"
    "v_cvt_pk_bf16_f32 v21, v14, v15\n"
    "ds_write_b64 v233, v[16:17] offset:0\n"
    "ds_write_b64 v233, v[18:19] offset:8\n"
    "ds_write_b64 v233, v[20:21] offset:16\n"
    "v_accvgpr_read_b32 v22, a16\n"
    "v_accvgpr_read_b32 v25, a17\n"
    "v_accvgpr_read_b32 v28, a18\n"
    "v_accvgpr_read_b32 v31, a19\n"
    "v_accvgpr_read_b32 v23, a80\n"
    "v_accvgpr_read_b32 v26, a81\n"
    "v_accvgpr_read_b32 v29, a82\n"
    "v_accvgpr_read_b32 v32, a83\n"
    "v_accvgpr_read_b32 v24, a144\n"
    "v_accvgpr_read_b32 v27, a145\n"
    "v_accvgpr_read_b32 v30, a146\n"
    "v_accvgpr_read_b32 v33, a147\n"
    "v_pk_mul_f32 v[22:23], v[22:23], v[116:117] op_sel_hi:[1,0]\n"
    "v_pk_mul_f32 v[24:25], v[24:25], v[116:117] op_sel_hi:[1,0]\n"
    "v_pk_mul_f32 v[26:27], v[26:27], v[116:117] op_sel_hi:[1,0]\n"
    "v_pk_mul_f32 v[28:29], v[28:29], v[116:117] op_sel_hi:[1,0]\n"
    "v_pk_mul_f32 v[30:31], v[30:31], v[116:117] op_sel_hi:[1,0]\n"
    "v_pk_mul_f32 v[32:33], v[32:33], v[116:117] op_sel_hi:[1,0]\n"
    "v_cvt_pk_bf16_f32 v34, v22, v23\n"
    "v_cvt_pk_bf16_f32 v35, v24, v25\n"
    "v_cvt_pk_bf16_f32 v36, v26, v27\n"
    "v_cvt_pk_bf16_f32 v37, v28, v29\n"
    "v_cvt_pk_bf16_f32 v38, v30, v31\n"
    "v_cvt_pk_bf16_f32 v39, v32, v33\n"
    "ds_write_b64 v233, v[34:35] offset:96\n"
    "ds_write_b64 v233, v[36:37] offset:104\n"
    "ds_write_b64 v233, v[38:39] offset:112\n"
    "v_accvgpr_read_b32 v40, a32\n"
    "v_accvgpr_read_b32 v43, a33\n"
    "v_accvgpr_read_b32 v46, a34\n"
    "v_accvgpr_read_b32 v49, a35\n"
    "v_accvgpr_read_b32 v41, a96\n"
    "v_accvgpr_read_b32 v44, a97\n"
    "v_accvgpr_read_b32 v47, a98\n"
    "v_accvgpr_read_b32 v50, a99\n"
    "v_accvgpr_read_b32 v42, a160\n"
    "v_accvgpr_read_b32 v45, a161\n"
    "v_accvgpr_read_b32 v48, a162\n"
    "v_accvgpr_read_b32 v51, a163\n"
    "v_pk_mul_f32 v[40:41], v[40:41], v[116:117] op_sel_hi:[1,0]\n"
    "v_pk_mul_f32 v[42:43], v[42:43], v[116:117] op_sel_hi:[1,0]\n"
    "v_pk_mul_f32 v[44:45], v[44:45], v[116:117] op_sel_hi:[1,0]\n"
    "v_pk_mul_f32 v[46:47], v[46:47], v[116:117] op_sel_hi:[1,0]\n"
    "v_pk_mul_f32 v[48:49], v[48:49], v[116:117] op_sel_hi:[1,0]\n"
    "v_pk_mul_f32 v[50:51], v[50:51], v[116:117] op_sel_hi:[1,0]\n"
    "v_cvt_pk_bf16_f32 v52, v40, v41\n"
    "v_cvt_pk_bf16_f32 v53, v42, v43\n"
    "v_cvt_pk_bf16_f32 v54, v44, v45\n"
    "v_cvt_pk_bf16_f32 v55, v46, v47\n"
    "v_cvt_pk_bf16_f32 v56, v48, v49\n"
    "v_cvt_pk_bf16_f32 v57, v50, v51\n"
    "ds_write_b64 v233, v[52:53] offset:192\n"
    "ds_write_b64 v233, v[54:55] offset:200\n"
    "ds_write_b64 v233, v[56:57] offset:208\n"
    "v_accvgpr_read_b32 v58, a48\n"
    "v_accvgpr_read_b32 v61, a49\n"
    "v_accvgpr_read_b32 v64, a50\n"
    "v_accvgpr_read_b32 v67, a51\n"
    "v_accvgpr_read_b32 v59, a112\n"
    "v_accvgpr_read_b32 v62, a113\n"
    "v_accvgpr_read_b32 v65, a114\n"
    "v_accvgpr_read_b32 v68, a115\n"
    "v_accvgpr_read_b32 v60, a176\n"
    "v_accvgpr_read_b32 v63, a177\n"
    "v_accvgpr_read_b32 v66, a178\n"
    "v_accvgpr_read_b32 v69, a179\n"
    "v_pk_mul_f32 v[58:59], v[58:59], v[116:117] op_sel_hi:[1,0]\n"
    "v_pk_mul_f32 v[60:61], v[60:61], v[116:117] op_sel_hi:[1,0]\n"
    "v_pk_mul_f32 v[62:63], v[62:63], v[116:117] op_sel_hi:[1,0]\n"
    "v_pk_mul_f32 v[64:65], v[64:65], v[116:117] op_sel_hi:[1,0]\n"
    "v_pk_mul_f32 v[66:67], v[66:67], v[116:117] op_sel_hi:[1,0]\n"
    "v_pk_mul_f32 v[68:69], v[68:69], v[116:117] op_sel_hi:[1,0]\n"
    "v_cvt_pk_bf16_f32 v70, v58, v59\n"
    "v_cvt_pk_bf16_f32 v71, v60, v61\n"
    "v_cvt_pk_bf16_f32 v72, v62, v63\n"
    "v_cvt_pk_bf16_f32 v73, v64, v65\n"
    "v_cvt_pk_bf16_f32 v74, v66, v67\n"
    "v_cvt_pk_bf16_f32 v75, v68, v69\n"
    "ds_write_b64 v233, v[70:71] offset:288\n"
    "ds_write_b64 v233, v[72:73] offset:296\n"
    "ds_write_b64 v233, v[74:75] offset:304\n"
    "ds_read_b128 v[120:123], v237 offset:0\n"
    "ds_read_b128 v[124:127], v238 offset:0\n"
    "ds_read_b128 v[128:131], v239 offset:0\n"
    "ds_read_b128 v[132:135], v237 offset:3200\n"
    "ds_read_b128 v[136:139], v238 offset:3200\n"
    "ds_read_b128 v[140:143], v239 offset:3200\n"
    "v_accvgpr_read_b32 v76, a4\n"
    "v_accvgpr_read_b32 v79, a5\n"
    "v_accvgpr_read_b32 v82, a6\n"
    "v_accvgpr_read_b32 v85, a7\n"
    "v_accvgpr_read_b32 v77, a68\n"
    "v_accvgpr_read_b32 v80, a69\n"
    "v_accvgpr_read_b32 v83, a70\n"
    "v_accvgpr_read_b32 v86, a71\n"
    "v_accvgpr_read_b32 v78, a132\n"
    "v_accvgpr_read_b32 v81, a133\n"
    "v_accvgpr_read_b32 v84, a134\n"
    "v_accvgpr_read_b32 v87, a135\n"
    "v_pk_mul_f32 v[76:77], v[76:77], v[116:117] op_sel:[0,1] op_sel_hi:[1,1]\n"
    "v_pk_mul_f32 v[78:79], v[78:79], v[116:117] op_sel:[0,1] op_sel_hi:[1,1]\n"
    "v_pk_mul_f32 v[80:81], v[80:81], v[116:117] op_sel:[0,1] op_sel_hi:[1,1]\n"
    "v_pk_mul_f32 v[82:83], v[82:83], v[116:117] op_sel:[0,1] op_sel_hi:[1,1]\n"
    "v_pk_mul_f32 v[84:85], v[84:85], v[116:117] op_sel:[0,1] op_sel_hi:[1,1]\n"
    "v_pk_mul_f32 v[86:87], v[86:87], v[116:117] op_sel:[0,1] op_sel_hi:[1,1]\n"
    "v_cvt_pk_bf16_f32 v88, v76, v77\n"
    "v_cvt_pk_bf16_f32 v89, v78, v79\n"
    "v_cvt_pk_bf16_f32 v90, v80, v81\n"
    "v_cvt_pk_bf16_f32 v91, v82, v83\n"
    "v_cvt_pk_bf16_f32 v92, v84, v85\n"
    "v_cvt_pk_bf16_f32 v93, v86, v87\n"
    "ds_write_b64 v233, v[88:89] offset:6400\n"
    "ds_write_b64 v233, v[90:91] offset:6408\n"
    "ds_write_b64 v233, v[92:93] offset:6416\n"
    "s_waitcnt lgkmcnt(8)\n"
    "buffer_store_dwordx4 v[120:123], v1, s[24:27], s73 offen offset:0 sc1\n"
    "s_waitcnt lgkmcnt(7)\n"
    "buffer_store_dwordx4 v[124:127], v1, s[24:27], s73 offen offset:1024 sc1\n"
    "s_waitcnt lgkmcnt(6)\n"
    "buffer_store_dwordx4 v[128:131], v1, s[24:27], s73 offen offset:2048 sc1\n"
    "s_waitcnt lgkmcnt(5)\n"
    "buffer_store_dwordx4 v[132:135], v1, s[24:27], s73 offen offset:3072 sc1\n"
    "s_add_u32 s73, s73, 4096\n"
    "s_waitcnt lgkmcnt(4)\n"
    "buffer_store_dwordx4 v[136:139], v1, s[24:27], s73 offen offset:0 sc1\n"
    "s_waitcnt lgkmcnt(3)\n"
    "buffer_store_dwordx4 v[140:143], v1, s[24:27], s73 offen offset:1024 sc1\n"
    "v_accvgpr_read_b32 v4, a20\n"
    "v_accvgpr_read_b32 v7, a21\n"
    "v_accvgpr_read_b32 v10, a22\n"
    "v_accvgpr_read_b32 v13, a23\n"
    "v_accvgpr_read_b32 v5, a84\n"
    "v_accvgpr_read_b32 v8, a85\n"
    "v_accvgpr_read_b32 v11, a86\n"
    "v_accvgpr_read_b32 v14, a87\n"
    "v_accvgpr_read_b32 v6, a148\n"
    "v_accvgpr_read_b32 v9, a149\n"
    "v_accvgpr_read_b32 v12, a150\n"
    "v_accvgpr_read_b32 v15, a151\n"
    "v_pk_mul_f32 v[4:5], v[4:5], v[116:117] op_sel:[0,1] op_sel_hi:[1,1]\n"
    "v_pk_mul_f32 v[6:7], v[6:7], v[116:117] op_sel:[0,1] op_sel_hi:[1,1]\n"
    "v_pk_mul_f32 v[8:9], v[8:9], v[116:117] op_sel:[0,1] op_sel_hi:[1,1]\n"
    "v_pk_mul_f32 v[10:11], v[10:11], v[116:117] op_sel:[0,1] op_sel_hi:[1,1]\n"
    "v_pk_mul_f32 v[12:13], v[12:13], v[116:117] op_sel:[0,1] op_sel_hi:[1,1]\n"
    "v_pk_mul_f32 v[14:15], v[14:15], v[116:117] op_sel:[0,1] op_sel_hi:[1,1]\n"
    "v_cvt_pk_bf16_f32 v16, v4, v5\n"
    "v_cvt_pk_bf16_f32 v17, v6, v7\n"
    "v_cvt_pk_bf16_f32 v18, v8, v9\n"
    "v_cvt_pk_bf16_f32 v19, v10, v11\n"
    "v_cvt_pk_bf16_f32 v20, v12, v13\n"
    "v_cvt_pk_bf16_f32 v21, v14, v15\n"
    "ds_write_b64 v233, v[16:17] offset:6496\n"
    "ds_write_b64 v233, v[18:19] offset:6504\n"
    "ds_write_b64 v233, v[20:21] offset:6512\n"
    "v_accvgpr_read_b32 v22, a36\n"
    "v_accvgpr_read_b32 v25, a37\n"
    "v_accvgpr_read_b32 v28, a38\n"
    "v_accvgpr_read_b32 v31, a39\n"
    "v_accvgpr_read_b32 v23, a100\n"
    "v_accvgpr_read_b32 v26, a101\n"
    "v_accvgpr_read_b32 v29, a102\n"
    "v_accvgpr_read_b32 v32, a103\n"
    "v_accvgpr_read_b32 v24, a164\n"
    "v_accvgpr_read_b32 v27, a165\n"
    "v_accvgpr_read_b32 v30, a166\n"
    "v_accvgpr_read_b32 v33, a167\n"
    "v_pk_mul_f32 v[22:23], v[22:23], v[116:117] op_sel:[0,1] op_sel_hi:[1,1]\n"
    "v_pk_mul_f32 v[24:25], v[24:25], v[116:117] op_sel:[0,1] op_sel_hi:[1,1]\n"
    "v_pk_mul_f32 v[26:27], v[26:27], v[116:117] op_sel:[0,1] op_sel_hi:[1,1]\n"
    "v_pk_mul_f32 v[28:29], v[28:29], v[116:117] op_sel:[0,1] op_sel_hi:[1,1]\n"
    "v_pk_mul_f32 v[30:31], v[30:31], v[116:117] op_sel:[0,1] op_sel_hi:[1,1]\n"
    "v_pk_mul_f32 v[32:33], v[32:33], v[116:117] op_sel:[0,1] op_sel_hi:[1,1]\n"
    "v_cvt_pk_bf16_f32 v34, v22, v23\n"
    "v_cvt_pk_bf16_f32 v35, v24, v25\n"
    "v_cvt_pk_bf16_f32 v36, v26, v27\n"
    "v_cvt_pk_bf16_f32 v37, v28, v29\n"
    "v_cvt_pk_bf16_f32 v38, v30, v31\n"
    "v_cvt_pk_bf16_f32 v39, v32, v33\n"
    "ds_write_b64 v233, v[34:35] offset:6592\n"
    "ds_write_b64 v233, v[36:37] offset:6600\n"
    "ds_write_b64 v233, v[38:39] offset:6608\n"
    "v_accvgpr_read_b32 v40, a52\n"
    "v_accvgpr_read_b32 v43, a53\n"
    "v_accvgpr_read_b32 v46, a54\n"
    "v_accvgpr_read_b32 v49, a55\n"
    "v_accvgpr_read_b32 v41, a116\n"
    "v_accvgpr_read_b32 v44, a117\n"
    "v_accvgpr_read_b32 v47, a118\n"
    "v_accvgpr_read_b32 v50, a119\n"
    "v_accvgpr_read_b32 v42, a180\n"
    "v_accvgpr_read_b32 v45, a181\n"
    "v_accvgpr_read_b32 v48, a182\n"
    "v_accvgpr_read_b32 v51, a183\n"
    "v_pk_mul_f32 v[40:41], v[40:41], v[116:117] op_sel:[0,1] op_sel_hi:[1,1]\n"
    "v_pk_mul_f32 v[42:43], v[42:43], v[116:117] op_sel:[0,1] op_sel_hi:[1,1]\n"
    "v_pk_mul_f32 v[44:45], v[44:45], v[116:117] op_sel:[0,1] op_sel_hi:[1,1]\n"
    "v_pk_mul_f32 v[46:47], v[46:47], v[116:117] op_sel:[0,1] op_sel_hi:[1,1]\n"
    "v_pk_mul_f32 v[48:49], v[48:49], v[116:117] op_sel:[0,1] op_sel_hi:[1,1]\n"
    "v_pk_mul_f32 v[50:51], v[50:51], v[116:117] op_sel:[0,1] op_sel_hi:[1,1]\n"
    "v_cvt_pk_bf16_f32 v52, v40, v41\n"
    "v_cvt_pk_bf16_f32 v53, v42, v43\n"
    "v_cvt_pk_bf16_f32 v54, v44, v45\n"
    "v_cvt_pk_bf16_f32 v55, v46, v47\n"
    "v_cvt_pk_bf16_f32 v56, v48, v49\n"
    "v_cvt_pk_bf16_f32 v57, v50, v51\n"
    "ds_write_b64 v233, v[52:53] offset:6688\n"
    "ds_write_b64 v233, v[54:55] offset:6696\n"
    "ds_write_b64 v233, v[56:57] offset:6704\n"
    "ds_read_b128 v[144:147], v237 offset:6400\n"
    "ds_read_b128 v[148:151], v238 offset:6400\n"
    "ds_read_b128 v[152:155], v239 offset:6400\n"
    "ds_read_b128 v[156:159], v237 offset:9600\n"
    "ds_read_b128 v[160:163], v238 offset:9600\n"
    "ds_read_b128 v[164:167], v239 offset:9600\n"
    "v_accvgpr_read_b32 v58, a8\n"
    "v_accvgpr_read_b32 v61, a9\n"
    "v_accvgpr_read_b32 v64, a10\n"
    "v_accvgpr_read_b32 v67, a11\n"
    "v_accvgpr_read_b32 v59, a72\n"
    "v_accvgpr_read_b32 v62, a73\n"
    "v_accvgpr_read_b32 v65, a74\n"
    "v_accvgpr_read_b32 v68, a75\n"
    "v_accvgpr_read_b32 v60, a136\n"
    "v_accvgpr_read_b32 v63, a137\n"
    "v_accvgpr_read_b32 v66, a138\n"
    "v_accvgpr_read_b32 v69, a139\n"
    "v_pk_mul_f32 v[58:59], v[58:59], v[118:119] op_sel_hi:[1,0]\n"
    "v_pk_mul_f32 v[60:61], v[60:61], v[118:119] op_sel_hi:[1,0]\n"
    "v_pk_mul_f32 v[62:63], v[62:63], v[118:119] op_sel_hi:[1,0]\n"
    "v_pk_mul_f32 v[64:65], v[64:65], v[118:119] op_sel_hi:[1,0]\n"
    "v_pk_mul_f32 v[66:67], v[66:67], v[118:119] op_sel_hi:[1,0]\n"
    "v_pk_mul_f32 v[68:69], v[68:69], v[118:119] op_sel_hi:[1,0]\n"
    "v_cvt_pk_bf16_f32 v70, v58, v59\n"
    "v_cvt_pk_bf16_f32 v71, v60, v61\n"
    "v_cvt_pk_bf16_f32 v72, v62, v63\n"
    "v_cvt_pk_bf16_f32 v73, v64, v65\n"
    "v_cvt_pk_bf16_f32 v74, v66, v67\n"
    "v_cvt_pk_bf16_f32 v75, v68, v69\n"
    "ds_write_b64 v233, v[70:71] offset:12800\n"
    "ds_write_b64 v233, v[72:73] offset:12808\n"
    "ds_write_b64 v233, v[74:75] offset:12816\n"
    "s_waitcnt lgkmcnt(8)\n"
    "buffer_store_dwordx4 v[144:147], v1, s[24:27], s73 offen offset:2048 sc1\n"
    "s_waitcnt lgkmcnt(7)\n"
    "buffer_store_dwordx4 v[148:151], v1, s[24:27], s73 offen offset:3072 sc1\n"
    "s_add_u32 s73, s73, 4096\n"
    "s_waitcnt lgkmcnt(6)\n"
    "buffer_store_dwordx4 v[152:155], v1, s[24:27], s73 offen offset:0 sc1\n"
    "s_waitcnt lgkmcnt(5)\n"
    "buffer_store_dwordx4 v[156:159], v1, s[24:27], s73 offen offset:1024 sc1\n"
    "s_waitcnt lgkmcnt(4)\n"
    "buffer_store_dwordx4 v[160:163], v1, s[24:27], s73 offen offset:2048 sc1\n"
    "s_waitcnt lgkmcnt(3)\n"
    "buffer_store_dwordx4 v[164:167], v1, s[24:27], s73 offen offset:3072 sc1\n"
    "v_accvgpr_read_b32 v76, a24\n"
    "v_accvgpr_read_b32 v79, a25\n"
    "v_accvgpr_read_b32 v82, a26\n"
    "v_accvgpr_read_b32 v85, a27\n"
    "v_accvgpr_read_b32 v77, a88\n"
    "v_accvgpr_read_b32 v80, a89\n"
    "v_accvgpr_read_b32 v83, a90\n"
    "v_accvgpr_read_b32 v86, a91\n"
    "v_accvgpr_read_b32 v78, a152\n"
    "v_accvgpr_read_b32 v81, a153\n"
    "v_accvgpr_read_b32 v84, a154\n"
    "v_accvgpr_read_b32 v87, a155\n"
    "v_pk_mul_f32 v[76:77], v[76:77], v[118:119] op_sel_hi:[1,0]\n"
    "v_pk_mul_f32 v[78:79], v[78:79], v[118:119] op_sel_hi:[1,0]\n"
    "v_pk_mul_f32 v[80:81], v[80:81], v[118:119] op_sel_hi:[1,0]\n"
    "v_pk_mul_f32 v[82:83], v[82:83], v[118:119] op_sel_hi:[1,0]\n"
    "v_pk_mul_f32 v[84:85], v[84:85], v[118:119] op_sel_hi:[1,0]\n"
    "v_pk_mul_f32 v[86:87], v[86:87], v[118:119] op_sel_hi:[1,0]\n"
    "v_cvt_pk_bf16_f32 v88, v76, v77\n"
    "v_cvt_pk_bf16_f32 v89, v78, v79\n"
    "v_cvt_pk_bf16_f32 v90, v80, v81\n"
    "v_cvt_pk_bf16_f32 v91, v82, v83\n"
    "v_cvt_pk_bf16_f32 v92, v84, v85\n"
    "v_cvt_pk_bf16_f32 v93, v86, v87\n"
    "ds_write_b64 v233, v[88:89] offset:12896\n"
    "ds_write_b64 v233, v[90:91] offset:12904\n"
    "ds_write_b64 v233, v[92:93] offset:12912\n"
    "v_accvgpr_read_b32 v4, a40\n"
    "v_accvgpr_read_b32 v7, a41\n"
    "v_accvgpr_read_b32 v10, a42\n"
    "v_accvgpr_read_b32 v13, a43\n"
    "v_accvgpr_read_b32 v5, a104\n"
    "v_accvgpr_read_b32 v8, a105\n"
    "v_accvgpr_read_b32 v11, a106\n"
    "v_accvgpr_read_b32 v14, a107\n"
    "v_accvgpr_read_b32 v6, a168\n"
    "v_accvgpr_read_b32 v9, a169\n"
    "v_accvgpr_read_b32 v12, a170\n"
    "v_accvgpr_read_b32 v15, a171\n"
    "v_pk_mul_f32 v[4:5], v[4:5], v[118:119] op_sel_hi:[1,0]\n"
    "v_pk_mul_f32 v[6:7], v[6:7], v[118:119] op_sel_hi:[1,0]\n"
    "v_pk_mul_f32 v[8:9], v[8:9], v[118:119] op_sel_hi:[1,0]\n"
    "v_pk_mul_f32 v[10:11], v[10:11], v[118:119] op_sel_hi:[1,0]\n"
    "v_pk_mul_f32 v[12:13], v[12:13], v[118:119] op_sel_hi:[1,0]\n"
    "v_pk_mul_f32 v[14:15], v[14:15], v[118:119] op_sel_hi:[1,0]\n"
    "v_cvt_pk_bf16_f32 v16, v4, v5\n"
    "v_cvt_pk_bf16_f32 v17, v6, v7\n"
    "v_cvt_pk_bf16_f32 v18, v8, v9\n"
    "v_cvt_pk_bf16_f32 v19, v10, v11\n"
    "v_cvt_pk_bf16_f32 v20, v12, v13\n"
    "v_cvt_pk_bf16_f32 v21, v14, v15\n"
    "ds_write_b64 v233, v[16:17] offset:12992\n"
    "ds_write_b64 v233, v[18:19] offset:13000\n"
    "ds_write_b64 v233, v[20:21] offset:13008\n"
    "v_accvgpr_read_b32 v22, a56\n"
    "v_accvgpr_read_b32 v25, a57\n"
    "v_accvgpr_read_b32 v28, a58\n"
    "v_accvgpr_read_b32 v31, a59\n"
    "v_accvgpr_read_b32 v23, a120\n"
    "v_accvgpr_read_b32 v26, a121\n"
    "v_accvgpr_read_b32 v29, a122\n"
    "v_accvgpr_read_b32 v32, a123\n"
    "v_accvgpr_read_b32 v24, a184\n"
    "v_accvgpr_read_b32 v27, a185\n"
    "v_accvgpr_read_b32 v30, a186\n"
    "v_accvgpr_read_b32 v33, a187\n"
    "v_pk_mul_f32 v[22:23], v[22:23], v[118:119] op_sel_hi:[1,0]\n"
    "v_pk_mul_f32 v[24:25], v[24:25], v[118:119] op_sel_hi:[1,0]\n"
    "v_pk_mul_f32 v[26:27], v[26:27], v[118:119] op_sel_hi:[1,0]\n"
    "v_pk_mul_f32 v[28:29], v[28:29], v[118:119] op_sel_hi:[1,0]\n"
    "v_pk_mul_f32 v[30:31], v[30:31], v[118:119] op_sel_hi:[1,0]\n"
    "v_pk_mul_f32 v[32:33], v[32:33], v[118:119] op_sel_hi:[1,0]\n"
    "v_cvt_pk_bf16_f32 v34, v22, v23\n"
    "v_cvt_pk_bf16_f32 v35, v24, v25\n"
    "v_cvt_pk_bf16_f32 v36, v26, v27\n"
    "v_cvt_pk_bf16_f32 v37, v28, v29\n"
    "v_cvt_pk_bf16_f32 v38, v30, v31\n"
    "v_cvt_pk_bf16_f32 v39, v32, v33\n"
    "ds_write_b64 v233, v[34:35] offset:13088\n"
    "ds_write_b64 v233, v[36:37] offset:13096\n"
    "ds_write_b64 v233, v[38:39] offset:13104\n"
    "ds_read_b128 v[168:171], v237 offset:12800\n"
    "ds_read_b128 v[172:175], v238 offset:12800\n"
    "ds_read_b128 v[176:179], v239 offset:12800\n"
    "ds_read_b128 v[180:183], v237 offset:16000\n"
    "ds_read_b128 v[184:187], v238 offset:16000\n"
    "ds_read_b128 v[188:191], v239 offset:16000\n"
    "v_accvgpr_read_b32 v40, a12\n"
    "v_accvgpr_read_b32 v43, a13\n"
    "v_accvgpr_read_b32 v46, a14\n"
    "v_accvgpr_read_b32 v49, a15\n"
    "v_accvgpr_read_b32 v41, a76\n"
    "v_accvgpr_read_b32 v44, a77\n"
    "v_accvgpr_read_b32 v47, a78\n"
    "v_accvgpr_read_b32 v50, a79\n"
    "v_accvgpr_read_b32 v42, a140\n"
    "v_accvgpr_read_b32 v45, a141\n"
    "v_accvgpr_read_b32 v48, a142\n"
    "v_accvgpr_read_b32 v51, a143\n"
    "v_pk_mul_f32 v[40:41], v[40:41], v[118:119] op_sel:[0,1] op_sel_hi:[1,1]\n"
    "v_pk_mul_f32 v[42:43], v[42:43], v[118:119] op_sel:[0,1] op_sel_hi:[1,1]\n"
    "v_pk_mul_f32 v[44:45], v[44:45], v[118:119] op_sel:[0,1] op_sel_hi:[1,1]\n"
    "v_pk_mul_f32 v[46:47], v[46:47], v[118:119] op_sel:[0,1] op_sel_hi:[1,1]\n"
    "v_pk_mul_f32 v[48:49], v[48:49], v[118:119] op_sel:[0,1] op_sel_hi:[1,1]\n"
    "v_pk_mul_f32 v[50:51], v[50:51], v[118:119] op_sel:[0,1] op_sel_hi:[1,1]\n"
    "v_cvt_pk_bf16_f32 v52, v40, v41\n"
    "v_cvt_pk_bf16_f32 v53, v42, v43\n"
    "v_cvt_pk_bf16_f32 v54, v44, v45\n"
    "v_cvt_pk_bf16_f32 v55, v46, v47\n"
    "v_cvt_pk_bf16_f32 v56, v48, v49\n"
    "v_cvt_pk_bf16_f32 v57, v50, v51\n"
    "ds_write_b64 v233, v[52:53] offset:19200\n"
    "ds_write_b64 v233, v[54:55] offset:19208\n"
    "ds_write_b64 v233, v[56:57] offset:19216\n"
    "s_add_u32 s73, s73, 4096\n"
    "s_waitcnt lgkmcnt(8)\n"
    "buffer_store_dwordx4 v[168:171], v1, s[24:27], s73 offen offset:0 sc1\n"
    "s_waitcnt lgkmcnt(7)\n"
    "buffer_store_dwordx4 v[172:175], v1, s[24:27], s73 offen offset:1024 sc1\n"
    "s_waitcnt lgkmcnt(6)\n"
    "buffer_store_dwordx4 v[176:179], v1, s[24:27], s73 offen offset:2048 sc1\n"
    "s_waitcnt lgkmcnt(5)\n"
    "buffer_store_dwordx4 v[180:183], v1, s[24:27], s73 offen offset:3072 sc1\n"
    "s_add_u32 s73, s73, 4096\n"
    "s_waitcnt lgkmcnt(4)\n"
    "buffer_store_dwordx4 v[184:187], v1, s[24:27], s73 offen offset:0 sc1\n"
    "s_waitcnt lgkmcnt(3)\n"
    "buffer_store_dwordx4 v[188:191], v1, s[24:27], s73 offen offset:1024 sc1\n"
    "v_accvgpr_read_b32 v58, a28\n"
    "v_accvgpr_read_b32 v61, a29\n"
    "v_accvgpr_read_b32 v64, a30\n"
    "v_accvgpr_read_b32 v67, a31\n"
    "v_accvgpr_read_b32 v59, a92\n"
    "v_accvgpr_read_b32 v62, a93\n"
    "v_accvgpr_read_b32 v65, a94\n"
    "v_accvgpr_read_b32 v68, a95\n"
    "v_accvgpr_read_b32 v60, a156\n"
    "v_accvgpr_read_b32 v63, a157\n"
    "v_accvgpr_read_b32 v66, a158\n"
    "v_accvgpr_read_b32 v69, a159\n"
    "v_pk_mul_f32 v[58:59], v[58:59], v[118:119] op_sel:[0,1] op_sel_hi:[1,1]\n"
    "v_pk_mul_f32 v[60:61], v[60:61], v[118:119] op_sel:[0,1] op_sel_hi:[1,1]\n"
    "v_pk_mul_f32 v[62:63], v[62:63], v[118:119] op_sel:[0,1] op_sel_hi:[1,1]\n"
    "v_pk_mul_f32 v[64:65], v[64:65], v[118:119] op_sel:[0,1] op_sel_hi:[1,1]\n"
    "v_pk_mul_f32 v[66:67], v[66:67], v[118:119] op_sel:[0,1] op_sel_hi:[1,1]\n"
    "v_pk_mul_f32 v[68:69], v[68:69], v[118:119] op_sel:[0,1] op_sel_hi:[1,1]\n"
    "v_cvt_pk_bf16_f32 v70, v58, v59\n"
    "v_cvt_pk_bf16_f32 v71, v60, v61\n"
    "v_cvt_pk_bf16_f32 v72, v62, v63\n"
    "v_cvt_pk_bf16_f32 v73, v64, v65\n"
    "v_cvt_pk_bf16_f32 v74, v66, v67\n"
    "v_cvt_pk_bf16_f32 v75, v68, v69\n"
    "ds_write_b64 v233, v[70:71] offset:19296\n"
    "ds_write_b64 v233, v[72:73] offset:19304\n"
    "ds_write_b64 v233, v[74:75] offset:19312\n"
    "v_accvgpr_read_b32 v76, a44\n"
    "v_accvgpr_read_b32 v79, a45\n"
    "v_accvgpr_read_b32 v82, a46\n"
    "v_accvgpr_read_b32 v85, a47\n"
    "v_accvgpr_read_b32 v77, a108\n"
    "v_accvgpr_read_b32 v80, a109\n"
    "v_accvgpr_read_b32 v83, a110\n"
    "v_accvgpr_read_b32 v86, a111\n"
    "v_accvgpr_read_b32 v78, a172\n"
    "v_accvgpr_read_b32 v81, a173\n"
    "v_accvgpr_read_b32 v84, a174\n"
    "v_accvgpr_read_b32 v87, a175\n"
    "v_pk_mul_f32 v[76:77], v[76:77], v[118:119] op_sel:[0,1] op_sel_hi:[1,1]\n"
    "v_pk_mul_f32 v[78:79], v[78:79], v[118:119] op_sel:[0,1] op_sel_hi:[1,1]\n"
    "v_pk_mul_f32 v[80:81], v[80:81], v[118:119] op_sel:[0,1] op_sel_hi:[1,1]\n"
    "v_pk_mul_f32 v[82:83], v[82:83], v[118:119] op_sel:[0,1] op_sel_hi:[1,1]\n"
    "v_pk_mul_f32 v[84:85], v[84:85], v[118:119] op_sel:[0,1] op_sel_hi:[1,1]\n"
    "v_pk_mul_f32 v[86:87], v[86:87], v[118:119] op_sel:[0,1] op_sel_hi:[1,1]\n"
    "v_cvt_pk_bf16_f32 v88, v76, v77\n"
    "v_cvt_pk_bf16_f32 v89, v78, v79\n"
    "v_cvt_pk_bf16_f32 v90, v80, v81\n"
    "v_cvt_pk_bf16_f32 v91, v82, v83\n"
    "v_cvt_pk_bf16_f32 v92, v84, v85\n"
    "v_cvt_pk_bf16_f32 v93, v86, v87\n"
    "ds_write_b64 v233, v[88:89] offset:19392\n"
    "ds_write_b64 v233, v[90:91] offset:19400\n"
    "ds_write_b64 v233, v[92:93] offset:19408\n"
    "v_accvgpr_read_b32 v4, a60\n"
    "v_accvgpr_read_b32 v7, a61\n"
    "v_accvgpr_read_b32 v10, a62\n"
    "v_accvgpr_read_b32 v13, a63\n"
    "v_accvgpr_read_b32 v5, a124\n"
    "v_accvgpr_read_b32 v8, a125\n"
    "v_accvgpr_read_b32 v11, a126\n"
    "v_accvgpr_read_b32 v14, a127\n"
    "v_accvgpr_read_b32 v6, a188\n"
    "v_accvgpr_read_b32 v9, a189\n"
    "v_accvgpr_read_b32 v12, a190\n"
    "v_accvgpr_read_b32 v15, a191\n"
    "v_pk_mul_f32 v[4:5], v[4:5], v[118:119] op_sel:[0,1] op_sel_hi:[1,1]\n"
    "v_pk_mul_f32 v[6:7], v[6:7], v[118:119] op_sel:[0,1] op_sel_hi:[1,1]\n"
    "v_pk_mul_f32 v[8:9], v[8:9], v[118:119] op_sel:[0,1] op_sel_hi:[1,1]\n"
    "v_pk_mul_f32 v[10:11], v[10:11], v[118:119] op_sel:[0,1] op_sel_hi:[1,1]\n"
    "v_pk_mul_f32 v[12:13], v[12:13], v[118:119] op_sel:[0,1] op_sel_hi:[1,1]\n"
    "v_pk_mul_f32 v[14:15], v[14:15], v[118:119] op_sel:[0,1] op_sel_hi:[1,1]\n"
    "v_cvt_pk_bf16_f32 v16, v4, v5\n"
    "v_cvt_pk_bf16_f32 v17, v6, v7\n"
    "v_cvt_pk_bf16_f32 v18, v8, v9\n"
    "v_cvt_pk_bf16_f32 v19, v10, v11\n"
    "v_cvt_pk_bf16_f32 v20, v12, v13\n"
    "v_cvt_pk_bf16_f32 v21, v14, v15\n"
    "ds_write_b64 v233, v[16:17] offset:19488\n"
    "ds_write_b64 v233, v[18:19] offset:19496\n"
    "ds_write_b64 v233, v[20:21] offset:19504\n"
    "ds_read_b128 v[120:123], v237 offset:19200\n"
    "ds_read_b128 v[124:127], v238 offset:19200\n"
    "ds_read_b128 v[128:131], v239 offset:19200\n"
    "ds_read_b128 v[132:135], v237 offset:22400\n"
    "ds_read_b128 v[136:139], v238 offset:22400\n"
    "ds_read_b128 v[140:143], v239 offset:22400\n"
    "s_waitcnt lgkmcnt(5)\n"
    "buffer_store_dwordx4 v[120:123], v1, s[24:27], s73 offen offset:2048 sc1\n"
    "s_waitcnt lgkmcnt(4)\n"
    "buffer_store_dwordx4 v[124:127], v1, s[24:27], s73 offen offset:3072 sc1\n"
    "s_add_u32 s73, s73, 4096\n"
    "s_waitcnt lgkmcnt(3)\n"
    "buffer_store_dwordx4 v[128:131], v1, s[24:27], s73 offen offset:0 sc1\n"
    "s_waitcnt lgkmcnt(2)\n"
    "buffer_store_dwordx4 v[132:135], v1, s[24:27], s73 offen offset:1024 sc1\n"
    "s_waitcnt lgkmcnt(1)\n"
    "buffer_store_dwordx4 v[136:139], v1, s[24:27], s73 offen offset:2048 sc1\n"
    "s_waitcnt lgkmcnt(0)\n"
    "buffer_store_dwordx4 v[140:143], v1, s[24:27], s73 offen offset:3072 sc1\n"
    "s_cmp_eq_u32 %0, 0\n"
    "s_cbranch_scc1 .Lfin\n"
    "s_waitcnt vmcnt(0)\n"
    ".Lfin:\n"
    "s_branch .Lend\n"
    ".Lpoll1:\n"
    "s_sleep 1\n"
    "buffer_load_dwordx2 v[240:241], v245, s[36:39], s81 offen offset:0 sc1\n"
    "ds_read_b32 v250, v252\n"
    "s_waitcnt vmcnt(0) lgkmcnt(0)\n"
    "v_cmp_eq_u64 s[54:55], v[240:241], v[242:243]\n"
    "v_cmp_le_u32 s[56:57], s59, v250\n"
    "s_add_u32 s58, s58, 1\n"
    "s_nop 1\n"
    "s_and_b64 s[54:55], s[54:55], s[56:57]\n"
    "s_cmp_eq_u64 s[54:55], -1\n"
    "s_cbranch_scc1 .Lpoll1r\n"
    "s_cmp_lt_u32 s58, 0x40000\n"
    "s_cbranch_scc1 .Lpoll1\n"
    "s_branch .Lpoll1r\n"
    ".Lpoll2:\n"
    "s_sleep 1\n"
    "buffer_load_dwordx2 v[240:241], v245, s[36:39], s81 offen offset:0 sc1\n"
    "ds_read_b32 v250, v252\n"
    "s_waitcnt vmcnt(0) lgkmcnt(0)\n"
    "v_cmp_eq_u64 s[54:55], v[240:241], v[242:243]\n"
    "v_cmp_le_u32 s[56:57], s59, v250\n"
    "s_add_u32 s58, s58, 1\n"
    "s_nop 1\n"
    "s_and_b64 s[54:55], s[54:55], s[56:57]\n"
    "s_cmp_eq_u64 s[54:55], -1\n"
    "s_cbranch_scc1 .Lpoll2r\n"
    "s_cmp_lt_u32 s58, 0x40000\n"
    "s_cbranch_scc1 .Lpoll2\n"
    "s_branch .Lpoll2r\n"
    ".Lpoll3:\n"
    "s_sleep 1\n"
    "buffer_load_dwordx2 v[240:241], v245, s[36:39], s81 offen offset:0 sc1\n"
    "ds_read_b32 v250, v252\n"
    "s_waitcnt vmcnt(0) lgkmcnt(0)\n"
    "v_cmp_eq_u64 s[54:55], v[240:241], v[242:243]\n"
    "v_cmp_le_u32 s[56:57], s59, v250\n"
    "s_add_u32 s58, s58, 1\n"
    "s_nop 1\n"
    "s_and_b64 s[54:55], s[54:55], s[56:57]\n"
    "s_cmp_eq_u64 s[54:55], -1\n"
    "s_cbranch_scc1 .Lpoll3r\n"
    "s_cmp_lt_u32 s58, 0x40000\n"
    "s_cbranch_scc1 .Lpoll3\n"
    "s_branch .Lpoll3r\n"
    ".Lpoll4:\n"
    "s_sleep 1\n"
    "buffer_load_dwordx2 v[240:241], v245, s[36:39], s81 offen offset:0 sc1\n"
    "ds_read_b32 v250, v252\n"
    "s_waitcnt vmcnt(0) lgkmcnt(0)\n"
    "v_cmp_eq_u64 s[54:55], v[240:241], v[242:243]\n"
    "v_cmp_le_u32 s[56:57], s59, v250\n"
    "s_add_u32 s58, s58, 1\n"
    "s_nop 1\n"
    "s_and_b64 s[54:55], s[54:55], s[56:57]\n"
    "s_cmp_eq_u64 s[54:55], -1\n"
    "s_cbranch_scc1 .Lpoll4r\n"
    "s_cmp_lt_u32 s58, 0x40000\n"
    "s_cbranch_scc1 .Lpoll4\n"
    "s_branch .Lpoll4r\n"
    ".LpaK0:\n"
    "s_sleep 1\n"
    "buffer_load_dwordx2 v[240:241], v68, s[36:39], s81 offen offset:0 sc1\n"
    "s_waitcnt vmcnt(0) lgkmcnt(0)\n"
    "v_cmp_eq_u64 s[54:55], v[240:241], v[242:243]\n"
    "s_add_u32 s58, s58, 1\n"
    "s_nop 1\n"
    "s_mov_b32 s74, 0x00005555\n"
    "s_mov_b32 s75, 0x00005555\n"
    "s_or_b64 s[54:55], s[54:55], s[74:75]\n"
    "s_cmp_eq_u64 s[54:55], -1\n"
    "s_cbranch_scc1 .LpaK0r\n"
    "s_cmp_lt_u32 s58, 0x40000\n"
    "s_cbranch_scc1 .LpaK0\n"
    "s_branch .LpaK0r\n"
    ".LpaV0:\n"
    "s_sleep 1\n"
    "buffer_load_dwordx2 v[240:241], v68, s[36:39], s81 offen offset:0 sc1\n"
    "s_waitcnt vmcnt(0) lgkmcnt(0)\n"
    "v_cmp_eq_u64 s[54:55], v[240:241], v[242:243]\n"
    "s_add_u32 s58, s58, 1\n"
    "s_nop 1\n"
    "s_mov_b32 s74, 0x00005555\n"
    "s_mov_b32 s75, 0x00005555\n"
    "s_or_b64 s[54:55], s[54:55], s[74:75]\n"
    "s_cmp_eq_u64 s[54:55], -1\n"
    "s_cbranch_scc1 .LpaV0r\n"
    "s_cmp_lt_u32 s58, 0x40000\n"
    "s_cbranch_scc1 .LpaV0\n"
    "s_branch .LpaV0r\n"
    ".LpaK1:\n"
    "s_sleep 1\n"
    "buffer_load_dwordx2 v[240:241], v68, s[36:39], s81 offen offset:0 sc1\n"
    "s_waitcnt vmcnt(0) lgkmcnt(0)\n"
    "v_cmp_eq_u64 s[54:55], v[240:241], v[242:243]\n"
    "s_add_u32 s58, s58, 1\n"
    "s_nop 1\n"
    "s_mov_b32 s74, 0x00005555\n"
    "s_mov_b32 s75, 0x00005555\n"
    "s_or_b64 s[54:55], s[54:55], s[74:75]\n"
    "s_cmp_eq_u64 s[54:55], -1\n"
    "s_cbranch_scc1 .LpaK1r\n"
    "s_cmp_lt_u32 s58, 0x40000\n"
    "s_cbranch_scc1 .LpaK1\n"
    "s_branch .LpaK1r\n"
    ".LpaV1:\n"
    "s_sleep 1\n"
    "buffer_load_dwordx2 v[240:241], v68, s[36:39], s81 offen offset:0 sc1\n"
    "s_waitcnt vmcnt(0) lgkmcnt(0)\n"
    "v_cmp_eq_u64 s[54:55], v[240:241], v[242:243]\n"
    "s_add_u32 s58, s58, 1\n"
    "s_nop 1\n"
    "s_mov_b32 s74, 0x00005555\n"
    "s_mov_b32 s75, 0x00005555\n"
    "s_or_b64 s[54:55], s[54:55], s[74:75]\n"
    "s_cmp_eq_u64 s[54:55], -1\n"
    "s_cbranch_scc1 .LpaV1r\n"
    "s_cmp_lt_u32 s58, 0x40000\n"
    "s_cbranch_scc1 .LpaV1\n"
    "s_branch .LpaV1r\n"
    ".Lpoll5:\n"
    "s_sleep 1\n"
    "ds_read_b32 v250, v252\n"
    "s_waitcnt lgkmcnt(0)\n"
    "v_cmp_le_u32 s[56:57], s59, v250\n"
    "s_add_u32 s58, s58, 1\n"
    "s_nop 1\n"
    "s_cmp_eq_u64 s[56:57], -1\n"
    "s_cbranch_scc1 .Lpoll5r\n"
    "s_cmp_lt_u32 s58, 0x40000\n"
    "s_cbranch_scc1 .Lpoll5\n"
    "s_branch .Lpoll5r\n"
    ".Lend:\n"
    : "=&s"(bad), "+s"(ka) : "s"(b), "v"(tid)
    : "memory", "vcc", "s16", "s17", "s18", "s19", "s20", "s21", "s22", "s23", "s24", "s25", "s26", "s27", "s28", "s29", "s30", "s31", "s36", "s37", "s38", "s39", "s40", "s41", "s42", "s43", "s44", "s45", "s46", "s47", "s48", "s49", "s50", "s51", "s52", "s53", "s54", "s55", "s56", "s57", "s58", "s59", "s60", "s61", "s62", "s63", "s64", "s65", "s66", "s67", "s68", "s69", "s70", "s71", "s72", "s73", "s74", "s75", "s76", "s77", "s78", "s79", "s80", "s81", "s82", "s83", "s84", "s85", "s86", "s87", "s88", "s89", "s90", "s91", "s92", "s93", "s94", "s95", "s96", "s97", "s98", "s99", "v1", "v2", "v3", "v4", "v5", "v6", "v7", "v8", "v9", "v10", "v11", "v12", "v13", "v14", "v15", "v16", "v17", "v18", "v19", "v20", "v21", "v22", "v23", "v24", "v25", "v26", "v27", "v28", "v29", "v30", "v31", "v32", "v33", "v34", "v35", "v36", "v37", "v38", "v39", "v40", "v41", "v42", "v43", "v44", "v45", "v46", "v47", "v48", "v49", "v50", "v51", "v52", "v53", "v54", "v55", "v56", "v57", "v58", "v59", "v60", "v61", "v62", "v63", "v64", "v65", "v66", "v67", "v68", "v69", "v70", "v71", "v72", "v73", "v74", "v75", "v76", "v77", "v78", "v79", "v80", "v81", "v82", "v83", "v84", "v85", "v86", "v87", "v88", "v89", "v90", "v91", "v92", "v93", "v94", "v95", "v96", "v97", "v98", "v99", "v100", "v101", "v102", "v103", "v104", "v105", "v106", "v107", "v108", "v109", "v110", "v111", "v112", "v113", "v114", "v115", "v116", "v117", "v118", "v119", "v120", "v121", "v122", "v123", "v124", "v125", "v126", "v127", "v128", "v129", "v130", "v131", "v132", "v133", "v134", "v135", "v136", "v137", "v138", "v139", "v140", "v141", "v142", "v143", "v144", "v145", "v146", "v147", "v148", "v149", "v150", "v151", "v152", "v153", "v154", "v155", "v156", "v157", "v158", "v159", "v160", "v161", "v162", "v163", "v164", "v165", "v166", "v167", "v168", "v169", "v170", "v171", "v172", "v173", "v174", "v175", "v176", "v177", "v178", "v179", "v180", "v181", "v182", "v183", "v184", "v185", "v186", "v187", "v188", "v189", "v190", "v191", "v192", "v193", "v194", "v195", "v196", "v197", "v198", "v199", "v200", "v201", "v202", "v203", "v204", "v205", "v206", "v207", "v208", "v209", "v210", "v211", "v212", "v213", "v214", "v215", "v216", "v217", "v218", "v219", "v220", "v221", "v222", "v223", "v224", "v225", "v226", "v227", "v228", "v229", "v230", "v231", "v232", "v233", "v234", "v235", "v236", "v237", "v238", "v239", "v240", "v241", "v242", "v243", "v244", "v245", "v246", "v247", "v248", "v249", "v250", "v251", "v252", "v253", "v254", "v255", "a0", "a1", "a2", "a3", "a4", "a5", "a6", "a7", "a8", "a9", "a10", "a11", "a12", "a13", "a14", "a15", "a16", "a17", "a18", "a19", "a20", "a21", "a22", "a23", "a24", "a25", "a26", "a27", "a28", "a29", "a30", "a31", "a32", "a33", "a34", "a35", "a36", "a37", "a38", "a39", "a40", "a41", "a42", "a43", "a44", "a45", "a46", "a47", "a48", "a49", "a50", "a51", "a52", "a53", "a54", "a55", "a56", "a57", "a58", "a59", "a60", "a61", "a62", "a63", "a64", "a65", "a66", "a67", "a68", "a69", "a70", "a71", "a72", "a73", "a74", "a75", "a76", "a77", "a78", "a79", "a80", "a81", "a82", "a83", "a84", "a85", "a86", "a87", "a88", "a89", "a90", "a91", "a92", "a93", "a94", "a95", "a96", "a97", "a98", "a99", "a100", "a101", "a102", "a103", "a104", "a105", "a106", "a107", "a108", "a109", "a110", "a111", "a112", "a113", "a114", "a115", "a116", "a117", "a118", "a119", "a120", "a121", "a122", "a123", "a124", "a125", "a126", "a127", "a128", "a129", "a130", "a131", "a132", "a133", "a134", "a135", "a136", "a137", "a138", "a139", "a140", "a141", "a142", "a143", "a144", "a145", "a146", "a147", "a148", "a149", "a150", "a151", "a152", "a153", "a154", "a155", "a156", "a157", "a158", "a159", "a160", "a161", "a162", "a163", "a164", "a165", "a166", "a167", "a168", "a169", "a170", "a171", "a172", "a173", "a174", "a175", "a176", "a177", "a178", "a179", "a180", "a181", "a182", "a183", "a184", "a185", "a186", "a187", "a188", "a189", "a190", "a191", "a192", "a193", "a194", "a195", "a196", "a197", "a198", "a199", "a200", "a201", "a202", "a203", "a204", "a205", "a206", "a207", "a208", "a209", "a210", "a211", "a212", "a213", "a214", "a215", "a216", "a217", "a218", "a219", "a220", "a221", "a222", "a223", "a224", "a225", "a226", "a227", "a228", "a229", "a230", "a231", "a232", "a233", "a234", "a235", "a236", "a237", "a238", "a239", "a240", "a241", "a242", "a243", "a244", "a245", "a246", "a247", "a248", "a249", "a250", "a251", "a252", "a253", "a254", "a255");
  if (bad) {
    slow_rows((const Args*)ka, (b & 7) * 4 + ((b >> 3) & 3), (b >> 5) * 256 + (tid & 192), tid & 63);
  }
}

extern "C" void kernel_launch(void* const* d_in, const int* in_sizes, int n_in,
                              void* d_out, int out_size, void* d_ws, size_t ws_size,
                              hipStream_t stream) {
  (void)hipFuncSetAttribute((const void*)attn_fwd, hipFuncAttributeMaxDynamicSharedMemorySize, SHM);
  attn_fwd<<<256, 256, SHM, stream>>>((const unsigned short*)d_in[0], (const int*)d_in[1], (const int*)d_in[2], (const unsigned short*)d_in[3], (const unsigned short*)d_in[4],
                                      (const unsigned short*)d_in[5], (const unsigned short*)d_in[6], (char*)d_ws, (unsigned short*)d_out);
}
```

```cpp
#include <hip/hip_runtime.h>

constexpr int D = 192, NH = 32, S = 2048;
constexpr int SHM = 154944;

struct Args { const unsigned short* q; const int* qk; const int* qv; const unsigned short* ksc; const unsigned short* kzr;
              const unsigned short* vsc; const unsigned short* vzr; char* ws; unsigned short* o; };

__device__ __forceinline__ float bf2f(unsigned short x) { return __uint_as_float((unsigned)x << 16); }
__device__ __forceinline__ float rbf(float x) { return (float)(__bf16)x; }

__device__ __forceinline__ void slow_rows(const Args* a, unsigned h, unsigned row0, unsigned lane) {
  const unsigned short* q = a->q + ((size_t)h * S + row0) * D;
  unsigned short* o = a->o + ((size_t)h * S + row0) * D;
  const int* qk = a->qk + (size_t)h * S * D; const int* qv = a->qv + (size_t)h * S * D;
  const unsigned short* ks = a->ksc + h * 32 * D; const unsigned short* kz = a->kzr + h * 32 * D;
  const unsigned short* vs = a->vsc + h * S; const unsigned short* vz = a->vzr + h * S;
#pragma unroll 1
  for (int r = 0; r < 64; ++r) {
    float m = -INFINITY, l = 0.f, a0 = 0.f, a1 = 0.f, a2 = 0.f;
#pragma unroll 1
    for (int t = 0; t < 32; ++t) {
      const int* kr = qk + (size_t)(t * 64 + lane) * D;
      float s = 0.f;
#pragma unroll 4
      for (int d = 0; d < D; ++d) s = fmaf(bf2f(q[r * D + d]), rbf(rbf(rbf((float)kr[d]) - bf2f(kz[t * D + d])) * bf2f(ks[t * D + d])), s);
      s *= 0.07216878364870322f;
      float mx = s;
      for (int x = 32; x; x >>= 1) mx = fmaxf(mx, __shfl_xor(mx, x));
      const float mn = fmaxf(m, mx), c = __expf(m - mn), p = __expf(s - mn);
      float ps = p;
      for (int x = 32; x; x >>= 1) ps += __shfl_xor(ps, x);
      l = l * c + ps; a0 *= c; a1 *= c; a2 *= c; m = mn;
#pragma unroll 2
      for (int k = 0; k < 64; ++k) {
        const float pk = __shfl(p, k), z = bf2f(vz[t * 64 + k]), sc = bf2f(vs[t * 64 + k]);
        const int* vr = qv + (size_t)(t * 64 + k) * D + lane;
        a0 = fmaf(pk, rbf(rbf(rbf((float)vr[0]) - z) * sc), a0); a1 = fmaf(pk, rbf(rbf(rbf((float)vr[64]) - z) * sc), a1); a2 = fmaf(pk, rbf(rbf(rbf((float)vr[128]) - z) * sc), a2);
      }
    }
    const float i = 1.f / l;
    o[r * D + lane] = __builtin_bit_cast(unsigned short, (__bf16)(a0 * i)); o[r * D + 64 + lane] = __builtin_bit_cast(unsigned short, (__bf16)(a1 * i));
    o[r * D + 128 + lane] = __builtin_bit_cast(unsigned short, (__bf16)(a2 * i));
  }
}

__global__ __launch_bounds__(256, 1) void attn_fwd(const unsigned short* q, const int* qk, const int* qv, const unsigned short* ksc, const unsigned short* kzr,
                                                   const unsigned short* vsc, const unsigned short* vzr, char* ws, unsigned short* o) {
  unsigned long long ka = (unsigned long long)__builtin_amdgcn_kernarg_segment_ptr();
  unsigned bad, b = blockIdx.x;
  const unsigned tid = threadIdx.x;
  asm volatile(
    "s_load_dwordx16 s[64:79], %1, 0\n"
    "s_load_dwordx2 s[24:25], %1, 64\n"
    "v_readfirstlane_b32 s84, %3\n"
    "v_and_b32 v156, 63, %3\n"
    "s_and_b32 s52, %2, 7\n"
    "s_bfe_u32 s56, %2, 0x20003\n"
    "s_lshr_b32 s53, %2, 5\n"
    "s_lshl_b32 s52, s52, 2\n"
    "s_lshr_b32 s84, s84, 6\n"
    "s_or_b32 s52, s52, s56\n"
    "v_lshlrev_b32 v1, 4, v156\n"
    "s_getpc_b64 s[86:87]\n"
    ".Lpc:\n"
    "v_lshlrev_b32 v246, 7, %3\n"
    "v_min_u32 v246, .Lend-.Lpc-4, v246\n"
    "global_load_dword v246, v246, s[86:87]\n"
    "s_mov_b32 s30, 1572864\n"
    "s_mov_b32 s31, 0x20000\n"
    "s_mov_b32 s42, 1572864\n"
    "s_mov_b32 s43, 0x20000\n"
    "s_mov_b32 s46, 12288\n"
    "s_mov_b32 s47, 0x20000\n"
    "s_mov_b32 s90, 12288\n"
    "s_mov_b32 s91, 0x20000\n"
    "s_mov_b32 s38, 4352\n"
    "s_mov_b32 s39, 0x20000\n"
    "s_mov_b32 s22, 98304\n"
    "s_mov_b32 s23, 0x20000\n"
    "s_mov_b32 s94, 4096\n"
    "s_mov_b32 s95, 0x20000\n"
    "s_mov_b32 s98, 4096\n"
    "s_mov_b32 s99, 0x20000\n"
    "s_mul_i32 s56, s52, 1572864\n"
    "s_mul_i32 s57, s52, 12288\n"
    "s_mul_i32 s54, s52, 786432\n"
    "s_mul_i32 s55, s53, 98304\n"
    "s_add_u32 s54, s54, s55\n"
    "s_lshl_b32 s55, s52, 12\n"
    "s_lshr_b32 s58, s84, 1\n"
    "s_and_b32 s59, s84, 1\n"
    "v_mul_u32_u24 v245, 12, v156\n"
    "v_add_u32 v236, 3072, v245\n"
    "v_lshrrev_b32 v165, 2, v156\n"
    "v_and_b32 v166, 3, v156\n"
    "v_and_b32 v167, 12, v165\n"
    "v_add_u32 v167, v167, v165\n"
    "v_mul_u32_u24 v160, 768, v167\n"
    "v_lshl_add_u32 v160, v166, 5, v160\n"
    "v_lshlrev_b32 v161, 4, v166\n"
    "s_mul_i32 s80, s58, 49152\n"
    "s_mul_i32 s83, s58, 384\n"
    "s_mul_i32 s82, s59, 384\n"
    "s_waitcnt lgkmcnt(0)\n"
    "s_add_u32 s28, s66, s56\n"
    "s_addc_u32 s29, s67, 0\n"
    "s_add_u32 s40, s68, s56\n"
    "s_addc_u32 s41, s69, 0\n"
    "s_add_u32 s44, s70, s57\n"
    "s_addc_u32 s45, s71, 0\n"
    "s_add_u32 s88, s72, s57\n"
    "s_addc_u32 s89, s73, 0\n"
    "s_bitcmp1_b32 s84, 0\n"
    "s_cbranch_scc1 .Lf1b\n"
    "v_lshlrev_b32 v164, 1, v156\n"
    "v_and_b32 v164, 24, v164\n"
    "v_and_b32 v163, 4, v165\n"
    "v_and_b32 v242, 35, v156\n"
    "v_or3_b32 v242, v242, v164, v163\n"
    "v_mul_u32_u24 v242, 768, v242\n"
    "s_mul_i32 s63, s53, 96\n"
    "s_add_u32 s81, s80, s63\n"
    "s_add_u32 s81, s81, 98304\n"
    "buffer_load_dwordx4 v[204:207], v242, s[28:31], s81 offen offset:0\n"
    "buffer_load_dwordx4 v[208:211], v242, s[28:31], s81 offen offset:16\n"
    "buffer_load_dwordx4 v[212:215], v242, s[28:31], s81 offen offset:32\n"
    "buffer_load_dwordx4 v[216:219], v242, s[28:31], s81 offen offset:48\n"
    "buffer_load_dwordx4 v[220:223], v242, s[28:31], s81 offen offset:64\n"
    "buffer_load_dwordx4 v[224:227], v242, s[28:31], s81 offen offset:80\n"
    "v_mul_u32_u24 v163, 768, v165\n"
    "v_mul_u32_u24 v164, 12, v166\n"
    "s_mul_i32 s81, s53, 48\n"
    "v_add_u32 v163, v163, v164\n"
    "s_add_u32 s81, s81, s83\n"
    "buffer_load_dwordx3 v[168:170], v163, s[44:47], s81 offen offset:768\n"
    "buffer_load_dwordx3 v[172:174], v163, s[88:91], s81 offen offset:768\n"
    "s_branch .Lf1e\n"
    ".Lf1b:\n"
    "s_mul_i32 s63, s53, 6144\n"
    "s_sub_u32 s63, s63, 49152\n"
    "s_add_u32 s81, s80, s63\n"
    "s_add_u32 s81, s81, 98304\n"
    "buffer_load_dwordx3 v[204:206], v236, s[40:43], s81 offen offset:1536 nt\n"
    "buffer_load_dwordx3 v[208:210], v236, s[40:43], s81 offen offset:2304 nt\n"
    "buffer_load_dwordx3 v[212:214], v236, s[40:43], s81 offen offset:0 nt\n"
    "buffer_load_dwordx3 v[216:218], v236, s[40:43], s81 offen offset:768 nt\n"
    "buffer_load_dwordx3 v[220:222], v245, s[40:43], s81 offen offset:1536 nt\n"
    "buffer_load_dwordx3 v[224:226], v245, s[40:43], s81 offen offset:2304 nt\n"
    "buffer_load_dwordx3 v[228:230], v245, s[40:43], s81 offen offset:0 nt\n"
    "buffer_load_dwordx3 v[232:234], v245, s[40:43], s81 offen offset:768 nt\n"
    "v_mov_b32 v242, v245\n"
    "v_mov_b32 v243, v236\n"
    ".Lf1e:\n"
    "s_nop 4\n"
    "s_add_u32 s80, s80, s82\n"
    "s_add_u32 s81, s80, 24576\n"
    "s_lshr_b32 s82, s82, 1\n"
    "s_add_u32 s82, s82, s83\n"
    "buffer_load_dwordx4 v[100:103], v161, s[44:47], s82 offen offset:0\n"
    "buffer_load_dwordx4 v[104:107], v161, s[88:91], s82 offen offset:0\n"
    "buffer_load_dwordx4 v[4:7], v160, s[28:31], s80 offen offset:0\n"
    "buffer_load_dwordx4 v[8:11], v160, s[28:31], s80 offen offset:16\n"
    "buffer_load_dwordx4 v[12:15], v160, s[28:31], s80 offen offset:3072\n"
    "buffer_load_dwordx4 v[16:19], v160, s[28:31], s80 offen offset:3088\n"
    "buffer_load_dwordx4 v[20:23], v160, s[28:31], s81 offen offset:0\n"
    "buffer_load_dwordx4 v[24:27], v160, s[28:31], s81 offen offset:16\n"
    "buffer_load_dwordx4 v[28:31], v160, s[28:31], s81 offen offset:3072\n"
    "buffer_load_dwordx4 v[32:35], v160, s[28:31], s81 offen offset:3088\n"
    "s_add_u32 s20, s64, s54\n"
    "s_addc_u32 s21, s65, 0\n"
    "s_add_u32 s92, s74, s55\n"
    "s_addc_u32 s93, s75, 0\n"
    "s_add_u32 s96, s76, s55\n"
    "s_addc_u32 s97, s77, 0\n"
    "s_add_u32 s24, s24, s54\n"
    "s_addc_u32 s25, s25, 0\n"
    "s_lshl_b32 s55, s55, 1\n"
    "s_add_u32 s36, s78, s55\n"
    "s_addc_u32 s37, s79, 0\n"
    "s_mul_i32 s56, s52, 1626112\n"
    "s_add_u32 s56, s56, 262144\n"
    "s_add_u32 s16, s78, s56\n"
    "s_addc_u32 s17, s79, 0\n"
    "buffer_load_dwordx4 v[108:111], v161, s[44:47], s82 offen offset:64\n"
    "buffer_load_dwordx4 v[112:115], v161, s[88:91], s82 offen offset:64\n"
    "buffer_load_dwordx4 v[36:39], v160, s[28:31], s80 offen offset:128\n"
    "buffer_load_dwordx4 v[40:43], v160, s[28:31], s80 offen offset:144\n"
    "buffer_load_dwordx4 v[44:47], v160, s[28:31], s80 offen offset:3200\n"
    "buffer_load_dwordx4 v[48:51], v160, s[28:31], s80 offen offset:3216\n"
    "buffer_load_dwordx4 v[52:55], v160, s[28:31], s81 offen offset:128\n"
    "buffer_load_dwordx4 v[56:59], v160, s[28:31], s81 offen offset:144\n"
    "buffer_load_dwordx4 v[60:63], v160, s[28:31], s81 offen offset:3200\n"
    "buffer_load_dwordx4 v[64:67], v160, s[28:31], s81 offen offset:3216\n"
    "v_add_u32 v167, 0, v156\n"
    "v_mul_u32_u24 v167, 2731, v167\n"
    "v_lshrrev_b32 v167, 16, v167\n"
    "v_xor_b32 v167, v167, v156\n"
    "v_and_b32 v167, 7, v167\n"
    "v_and_b32 v157, 0x380, v1\n"
    "v_lshl_add_u32 v157, v167, 4, v157\n"
    "v_add_u32 v167, 64, v156\n"
    "v_mul_u32_u24 v167, 2731, v167\n"
    "v_lshrrev_b32 v167, 16, v167\n"
    "v_xor_b32 v167, v167, v156\n"
    "v_and_b32 v167, 7, v167\n"
    "v_and_b32 v158, 0x380, v1\n"
    "v_lshl_add_u32 v158, v167, 4, v158\n"
    "v_add_u32 v167, 128, v156\n"
    "v_mul_u32_u24 v167, 2731, v167\n"
    "v_lshrrev_b32 v167, 16, v167\n"
    "v_xor_b32 v167, v167, v156\n"
    "v_and_b32 v167, 7, v167\n"
    "v_and_b32 v159, 0x380, v1\n"
    "v_lshl_add_u32 v159, v167, 4, v159\n"
    "buffer_load_dwordx4 v[116:119], v161, s[44:47], s82 offen offset:128\n"
    "buffer_load_dwordx4 v[120:123], v161, s[88:91], s82 offen offset:128\n"
    "buffer_load_dwordx4 v[68:71], v160, s[28:31], s80 offen offset:256\n"
    "buffer_load_dwordx4 v[72:75], v160, s[28:31], s80 offen offset:272\n"
    "buffer_load_dwordx4 v[76:79], v160, s[28:31], s80 offen offset:3328\n"
    "buffer_load_dwordx4 v[80:83], v160, s[28:31], s80 offen offset:3344\n"
    "buffer_load_dwordx4 v[84:87], v160, s[28:31], s81 offen offset:256\n"
    "buffer_load_dwordx4 v[88:91], v160, s[28:31], s81 offen offset:272\n"
    "buffer_load_dwordx4 v[92:95], v160, s[28:31], s81 offen offset:3328\n"
    "buffer_load_dwordx4 v[96:99], v160, s[28:31], s81 offen offset:3344\n"
    "v_lshlrev_b32 v252, 4, %3\n"
    "v_mov_b32 v247, 0\n"
    "s_mul_i32 s65, s84, 6144\n"
    "s_mul_i32 s85, s84, 24576\n"
    "buffer_load_dwordx4 v[176:179], v252, s[92:95], 0 offen offset:0\n"
    "buffer_load_dwordx4 v[180:183], v252, s[96:99], 0 offen offset:0\n"
    "buffer_load_dwordx2 v[238:239], v247, s[36:39], 0 offen offset:0 sc1\n"
    "s_add_u32 m0, s65, 49152\n"
    "s_add_u32 s72, s85, 0\n"
    "buffer_load_dwordx4 v157, s[20:23], s72 offen offset:0 nt lds\n"
    "buffer_load_dwordx4 v158, s[20:23], s72 offen offset:1024 nt lds\n"
    "buffer_load_dwordx4 v159, s[20:23], s72 offen offset:2048 nt lds\n"
    "s_add_u32 m0, s65, 52224\n"
    "s_add_u32 s73, s85, 3072\n"
    "buffer_load_dwordx4 v157, s[20:23], s73 offen offset:0 nt lds\n"
    "buffer_load_dwordx4 v158, s[20:23], s73 offen offset:1024 nt lds\n"
    "buffer_load_dwordx4 v159, s[20:23], s73 offen offset:2048 nt lds\n"
    "s_add_u32 m0, s65, 98304\n"
    "s_add_u32 s72, s85, 6144\n"
    "buffer_load_dwordx4 v157, s[20:23], s72 offen offset:0 nt lds\n"
    "buffer_load_dwordx4 v158, s[20:23], s72 offen offset:1024 nt lds\n"
    "buffer_load_dwordx4 v159, s[20:23], s72 offen offset:2048 nt lds\n"
    "s_add_u32 m0, s65, 101376\n"
    "s_add_u32 s73, s85, 9216\n"
    "buffer_load_dwordx4 v157, s[20:23], s73 offen offset:0 nt lds\n"
    "buffer_load_dwordx4 v158, s[20:23], s73 offen offset:1024 nt lds\n"
    "buffer_load_dwordx4 v159, s[20:23], s73 offen offset:2048 nt lds\n"
    "s_mul_i32 s74, s84, 12288\n"
    "buffer_load_dwordx3 v[124:126], v236, s[40:43], s74 offen offset:1536\n"
    "buffer_load_dwordx3 v[128:130], v236, s[40:43], s74 offen offset:2304\n"
    "buffer_load_dwordx3 v[132:134], v236, s[40:43], s74 offen offset:0\n"
    "buffer_load_dwordx3 v[136:138], v236, s[40:43], s74 offen offset:768\n"
    "buffer_load_dwordx3 v[140:142], v245, s[40:43], s74 offen offset:1536\n"
    "buffer_load_dwordx3 v[144:146], v245, s[40:43], s74 offen offset:2304\n"
    "buffer_load_dwordx3 v[148:150], v245, s[40:43], s74 offen offset:0\n"
    "buffer_load_dwordx3 v[152:154], v245, s[40:43], s74 offen offset:768\n"
    "s_mov_b32 s18, 1622016\n"
    "s_mov_b32 s19, 0x20000\n"
    "s_mov_b32 s26, 98304\n"
    "s_mov_b32 s27, 0x20000\n"
    "s_mov_b32 s79, s58\n"
    "s_mul_i32 s71, s58, 49152\n"
    "s_add_u32 s71, s71, 98304\n"
    "s_lshl_b32 s80, s58, 7\n"
    "s_add_u32 s80, s80, 256\n"
    "s_mov_b32 s66, 0\n"
    "s_add_u32 s67, s65, 98304\n"
    "s_add_u32 s68, s65, 24576\n"
    "s_add_u32 s69, s65, 73728\n"
    "s_mov_b32 s81, 256\n"
    "s_mul_i32 s72, s58, 24576\n"
    "s_add_u32 s70, s72, s65\n"
    "s_add_u32 s70, s70, 73728\n"
    "s_sub_u32 s64, s65, s72\n"
    "s_add_u32 s64, s64, 98304\n"
    "s_add_u32 s73, s72, 73728\n"
    "v_add_u32 v248, s73, v1\n"
    "s_sub_u32 s73, 98304, s72\n"
    "v_add_u32 v249, s73, v1\n"
    "s_or_b32 s73, s53, s84\n"
    "s_cmp_eq_u32 s73, 0\n"
    "s_cselect_b32 s82, 0, 0x7fff0000\n"
    "v_mov_b32 v2, v1\n"
    "v_add_u32 v3, 24576, v1\n"
    "v_and_b32 v241, 0xf0, v1\n"
    "v_lshrrev_b32 v241, 1, v241\n"
    "v_add_u32 v241, 128, v241\n"
    "s_lshl_b32 s73, s53, 4\n"
    "s_lshl_b32 s75, s59, 3\n"
    "s_add_u32 s73, s73, s75\n"
    "s_add_u32 s73, s73, 128\n"
    "v_mov_b32 v240, 0x7fff0000\n"
    "v_writelane_b32 v240, s73, 0\n"
    "s_mul_i32 s73, s59, 6144\n"
    "s_add_u32 s73, s73, s72\n"
    "v_lshlrev_b32 v162, 4, v165\n"
    "v_lshl_add_u32 v162, v166, 8, v162\n"
    "v_add_u32 v162, s73, v162\n"
    "v_and_b32 v167, 48, v156\n"
    "v_lshlrev_b32 v244, 6, v167\n"
    "v_and_b32 v237, 0xf0, v1\n"
    "v_add_u32 v244, v244, v237\n"
    "s_lshr_b32 s73, s72, 1\n"
    "s_lshl_b32 s75, s59, 9\n"
    "s_add_u32 s73, s73, s75\n"
    "s_add_u32 s73, s73, 73728\n"
    "v_add_u32 v237, s73, v244\n"
    "s_mul_i32 s73, s79, 768\n"
    "s_sub_u32 s73, 130048, s73\n"
    "v_lshlrev_b32 v250, 1, v167\n"
    "v_add_u32 v250, s73, v250\n"
    "v_and_b32 v251, 15, v156\n"
    "v_min_u32 v251, 3, v251\n"
    "v_lshl_add_u32 v251, v251, 7, v167\n"
    "s_add_u32 s73, s73, 256\n"
    "v_add_u32 v251, s73, v251\n"
    "v_and_b32 v253, 15, v156\n"
    "v_lshrrev_b32 v167, 4, v156\n"
    "v_and_b32 v247, 7, v156\n"
    "v_xor_b32 v167, v167, v247\n"
    "v_mul_u32_u24 v253, 384, v253\n"
    "v_lshl_add_u32 v253, v167, 4, v253\n"
    "s_add_u32 s73, s65, 49152\n"
    "v_add_u32 v253, s73, v253\n"
    "v_lshrrev_b32 v252, 3, %3\n"
    "v_mul_u32_u24 v252, 768, v252\n"
    "v_lshl_add_u32 v252, v247, 4, v252\n"
    "v_add_u32 v252, 130048, v252\n"
    "v_lshl_add_u32 v163, v247, 4, v252\n"
    "v_lshlrev_b32 v247, 2, v156\n"
    "v_add_u32 v247, 154624, v247\n"
    "v_mov_b32 v167, 0\n"
    "ds_write_b32 v247, v167\n"
    "v_mov_b32 v167, 0xff800000\n"
    "v_mov_b32 v164, 154880\n"
    "ds_write_b32 v164, v167\n"
    "v_accvgpr_write_b32 a0, 0\n"
    "v_accvgpr_write_b32 a1, 0\n"
    "v_accvgpr_write_b32 a2, 0\n"
    "v_accvgpr_write_b32 a3, 0\n"
    "v_accvgpr_write_b32 a4, 0\n"
    "v_accvgpr_write_b32 a5, 0\n"
    "v_accvgpr_write_b32 a6, 0\n"
    "v_accvgpr_write_b32 a7, 0\n"
    "v_accvgpr_write_b32 a8, 0\n"
    "v_accvgpr_write_b32 a9, 0\n"
    "v_accvgpr_write_b32 a10, 0\n"
    "v_accvgpr_write_b32 a11, 0\n"
    "v_accvgpr_write_b32 a12, 0\n"
    "v_accvgpr_write_b32 a13, 0\n"
    "v_accvgpr_write_b32 a14, 0\n"
    "v_accvgpr_write_b32 a15, 0\n"
    "v_accvgpr_write_b32 a16, 0\n"
    "v_accvgpr_write_b32 a17, 0\n"
    "v_accvgpr_write_b32 a18, 0\n"
    "v_accvgpr_write_b32 a19, 0\n"
    "v_accvgpr_write_b32 a20, 0\n"
    "v_accvgpr_write_b32 a21, 0\n"
    "v_accvgpr_write_b32 a22, 0\n"
    "v_accvgpr_write_b32 a23, 0\n"
    "v_accvgpr_write_b32 a24, 0\n"
    "v_accvgpr_write_b32 a25, 0\n"
    "v_accvgpr_write_b32 a26, 0\n"
    "v_accvgpr_write_b32 a27, 0\n"
    "v_accvgpr_write_b32 a28, 0\n"
    "v_accvgpr_write_b32 a29, 0\n"
    "v_accvgpr_write_b32 a30, 0\n"
    "v_accvgpr_write_b32 a31, 0\n"
    "v_accvgpr_write_b32 a32, 0\n"
    "v_accvgpr_write_b32 a33, 0\n"
    "v_accvgpr_write_b32 a34, 0\n"
    "v_accvgpr_write_b32 a35, 0\n"
    "v_accvgpr_write_b32 a36, 0\n"
    "v_accvgpr_write_b32 a37, 0\n"
    "v_accvgpr_write_b32 a38, 0\n"
    "v_accvgpr_write_b32 a39, 0\n"
    "v_accvgpr_write_b32 a40, 0\n"
    "v_accvgpr_write_b32 a41, 0\n"
    "v_accvgpr_write_b32 a42, 0\n"
    "v_accvgpr_write_b32 a43, 0\n"
    "v_accvgpr_write_b32 a44, 0\n"
    "v_accvgpr_write_b32 a45, 0\n"
    "v_accvgpr_write_b32 a46, 0\n"
    "v_accvgpr_write_b32 a47, 0\n"
    "v_accvgpr_write_b32 a48, 0\n"
    "v_accvgpr_write_b32 a49, 0\n"
    "v_accvgpr_write_b32 a50, 0\n"
    "v_accvgpr_write_b32 a51, 0\n"
    "v_accvgpr_write_b32 a52, 0\n"
    "v_accvgpr_write_b32 a53, 0\n"
    "v_accvgpr_write_b32 a54, 0\n"
    "v_accvgpr_write_b32 a55, 0\n"
    "v_accvgpr_write_b32 a56, 0\n"
    "v_accvgpr_write_b32 a57, 0\n"
    "v_accvgpr_write_b32 a58, 0\n"
    "v_accvgpr_write_b32 a59, 0\n"
    "v_accvgpr_write_b32 a60, 0\n"
    "v_accvgpr_write_b32 a61, 0\n"
    "v_accvgpr_write_b32 a62, 0\n"
    "v_accvgpr_write_b32 a63, 0\n"
    "v_accvgpr_write_b32 a64, 0\n"
    "v_accvgpr_write_b32 a65, 0\n"
    "v_accvgpr_write_b32 a66, 0\n"
    "v_accvgpr_write_b32 a67, 0\n"
    "v_accvgpr_write_b32 a68, 0\n"
    "v_accvgpr_write_b32 a69, 0\n"
    "v_accvgpr_write_b32 a70, 0\n"
    "v_accvgpr_write_b32 a71, 0\n"
    "v_accvgpr_write_b32 a72, 0\n"
    "v_accvgpr_write_b32 a73, 0\n"
    "v_accvgpr_write_b32 a74, 0\n"
    "v_accvgpr_write_b32 a75, 0\n"
    "v_accvgpr_write_b32 a76, 0\n"
    "v_accvgpr_write_b32 a77, 0\n"
    "v_accvgpr_write_b32 a78, 0\n"
    "v_accvgpr_write_b32 a79, 0\n"
    "v_accvgpr_write_b32 a80, 0\n"
    "v_accvgpr_write_b32 a81, 0\n"
    "v_accvgpr_write_b32 a82, 0\n"
    "v_accvgpr_write_b32 a83, 0\n"
    "v_accvgpr_write_b32 a84, 0\n"
    "v_accvgpr_write_b32 a85, 0\n"
    "v_accvgpr_write_b32 a86, 0\n"
    "v_accvgpr_write_b32 a87, 0\n"
    "v_accvgpr_write_b32 a88, 0\n"
    "v_accvgpr_write_b32 a89, 0\n"
    "v_accvgpr_write_b32 a90, 0\n"
    "v_accvgpr_write_b32 a91, 0\n"
    "v_accvgpr_write_b32 a92, 0\n"
    "v_accvgpr_write_b32 a93, 0\n"
    "v_accvgpr_write_b32 a94, 0\n"
    "v_accvgpr_write_b32 a95, 0\n"
    "v_accvgpr_write_b32 a96, 0\n"
    "v_accvgpr_write_b32 a97, 0\n"
    "v_accvgpr_write_b32 a98, 0\n"
    "v_accvgpr_write_b32 a99, 0\n"
    "v_accvgpr_write_b32 a100, 0\n"
    "v_accvgpr_write_b32 a101, 0\n"
    "v_accvgpr_write_b32 a102, 0\n"
    "v_accvgpr_write_b32 a103, 0\n"
    "v_accvgpr_write_b32 a104, 0\n"
    "v_accvgpr_write_b32 a105, 0\n"
    "v_accvgpr_write_b32 a106, 0\n"
    "v_accvgpr_write_b32 a107, 0\n"
    "v_accvgpr_write_b32 a108, 0\n"
    "v_accvgpr_write_b32 a109, 0\n"
    "v_accvgpr_write_b32 a110, 0\n"
    "v_accvgpr_write_b32 a111, 0\n"
    "v_accvgpr_write_b32 a112, 0\n"
    "v_accvgpr_write_b32 a113, 0\n"
    "v_accvgpr_write_b32 a114, 0\n"
    "v_accvgpr_write_b32 a115, 0\n"
    "v_accvgpr_write_b32 a116, 0\n"
    "v_accvgpr_write_b32 a117, 0\n"
    "v_accvgpr_write_b32 a118, 0\n"
    "v_accvgpr_write_b32 a119, 0\n"
    "v_accvgpr_write_b32 a120, 0\n"
    "v_accvgpr_write_b32 a121, 0\n"
    "v_accvgpr_write_b32 a122, 0\n"
    "v_accvgpr_write_b32 a123, 0\n"
    "v_accvgpr_write_b32 a124, 0\n"
    "v_accvgpr_write_b32 a125, 0\n"
    "v_accvgpr_write_b32 a126, 0\n"
    "v_accvgpr_write_b32 a127, 0\n"
    "v_accvgpr_write_b32 a128, 0\n"
    "v_accvgpr_write_b32 a129, 0\n"
    "v_accvgpr_write_b32 a130, 0\n"
    "v_accvgpr_write_b32 a131, 0\n"
    "v_accvgpr_write_b32 a132, 0\n"
    "v_accvgpr_write_b32 a133, 0\n"
    "v_accvgpr_write_b32 a134, 0\n"
    "v_accvgpr_write_b32 a135, 0\n"
    "v_accvgpr_write_b32 a136, 0\n"
    "v_accvgpr_write_b32 a137, 0\n"
    "v_accvgpr_write_b32 a138, 0\n"
    "v_accvgpr_write_b32 a139, 0\n"
    "v_accvgpr_write_b32 a140, 0\n"
    "v_accvgpr_write_b32 a141, 0\n"
    "v_accvgpr_write_b32 a142, 0\n"
    "v_accvgpr_write_b32 a143, 0\n"
    "v_accvgpr_write_b32 a144, 0\n"
    "v_accvgpr_write_b32 a145, 0\n"
    "v_accvgpr_write_b32 a146, 0\n"
    "v_accvgpr_write_b32 a147, 0\n"
    "v_accvgpr_write_b32 a148, 0\n"
    "v_accvgpr_write_b32 a149, 0\n"
    "v_accvgpr_write_b32 a150, 0\n"
    "v_accvgpr_write_b32 a151, 0\n"
    "v_accvgpr_write_b32 a152, 0\n"
    "v_accvgpr_write_b32 a153, 0\n"
    "v_accvgpr_write_b32 a154, 0\n"
    "v_accvgpr_write_b32 a155, 0\n"
    "v_accvgpr_write_b32 a156, 0\n"
    "v_accvgpr_write_b32 a157, 0\n"
    "v_accvgpr_write_b32 a158, 0\n"
    "v_accvgpr_write_b32 a159, 0\n"
    "v_accvgpr_write_b32 a160, 0\n"
    "v_accvgpr_write_b32 a161, 0\n"
    "v_accvgpr_write_b32 a162, 0\n"
    "v_accvgpr_write_b32 a163, 0\n"
    "v_accvgpr_write_b32 a164, 0\n"
    "v_accvgpr_write_b32 a165, 0\n"
    "v_accvgpr_write_b32 a166, 0\n"
    "v_accvgpr_write_b32 a167, 0\n"
    "v_accvgpr_write_b32 a168, 0\n"
    "v_accvgpr_write_b32 a169, 0\n"
    "v_accvgpr_write_b32 a170, 0\n"
    "v_accvgpr_write_b32 a171, 0\n"
    "v_accvgpr_write_b32 a172, 0\n"
    "v_accvgpr_write_b32 a173, 0\n"
    "v_accvgpr_write_b32 a174, 0\n"
    "v_accvgpr_write_b32 a175, 0\n"
    "v_accvgpr_write_b32 a176, 0\n"
    "v_accvgpr_write_b32 a177, 0\n"
    "v_accvgpr_write_b32 a178, 0\n"
    "v_accvgpr_write_b32 a179, 0\n"
    "v_accvgpr_write_b32 a180, 0\n"
    "v_accvgpr_write_b32 a181, 0\n"
    "v_accvgpr_write_b32 a182, 0\n"
    "v_accvgpr_write_b32 a183, 0\n"
    "v_accvgpr_write_b32 a184, 0\n"
    "v_accvgpr_write_b32 a185, 0\n"
    "v_accvgpr_write_b32 a186, 0\n"
    "v_accvgpr_write_b32 a187, 0\n"
    "v_accvgpr_write_b32 a188, 0\n"
    "v_accvgpr_write_b32 a189, 0\n"
    "v_accvgpr_write_b32 a190, 0\n"
    "v_accvgpr_write_b32 a191, 0\n"
    "s_mov_b32 s72, 0x3dd53b95\n"
    "s_bitcmp1_b32 s84, 0\n"
    "s_cbranch_scc1 .Lf2b\n"
    "s_mul_i32 s73, s53, 3\n"
    "s_add_u32 s74, s73, 0\n"
    "s_lshr_b32 s75, s74, 2\n"
    "s_lshl_b32 s75, s75, 11\n"
    "s_and_b32 s74, s74, 3\n"
    "s_lshl_b32 s74, s74, 8\n"
    "s_or_b32 s60, s74, s75\n"
    "s_add_u32 s74, s73, 1\n"
    "s_lshr_b32 s75, s74, 2\n"
    "s_lshl_b32 s75, s75, 11\n"
    "s_and_b32 s74, s74, 3\n"
    "s_lshl_b32 s74, s74, 8\n"
    "s_or_b32 s61, s74, s75\n"
    "s_add_u32 s74, s73, 2\n"
    "s_lshr_b32 s75, s74, 2\n"
    "s_lshl_b32 s75, s75, 11\n"
    "s_and_b32 s74, s74, 3\n"
    "s_lshl_b32 s74, s74, 8\n"
    "s_or_b32 s62, s74, s75\n"
    "s_mul_i32 s73, s79, 3072\n"
    "s_add_u32 s73, s73, 122880\n"
    "v_mul_u32_u24 v164, 192, v165\n"
    "v_mul_u32_u24 v167, 24, v166\n"
    "v_add3_u32 v164, v164, v167, s73\n"
    "v_mov_b32 v243, s73\n"
    "v_lshrrev_b32 v167, 5, v156\n"
    "v_mul_u32_u24 v244, 12288, v167\n"
    "v_and_b32 v167, 16, v156\n"
    "v_lshl_add_u32 v244, v167, 6, v244\n"
    "v_and_b32 v167, 0xf0, v1\n"
    "v_add_u32 v244, v244, v167\n"
    "s_waitcnt vmcnt(53)\n"
    "v_lshlrev_b32 v184, 16, v168\n"
    "v_and_b32 v185, 0xffff0000, v168\n"
    "v_lshlrev_b32 v186, 16, v169\n"
    "v_and_b32 v187, 0xffff0000, v169\n"
    "v_lshlrev_b32 v188, 16, v170\n"
    "v_and_b32 v189, 0xffff0000, v170\n"
    "v_lshlrev_b32 v190, 16, v172\n"
    "v_and_b32 v191, 0xffff0000, v172\n"
    "v_lshlrev_b32 v192, 16, v173\n"
    "v_and_b32 v193, 0xffff0000, v173\n"
    "v_lshlrev_b32 v194, 16, v174\n"
    "v_and_b32 v195, 0xffff0000, v174\n"
    "v_mul_f32 v184, s72, v184\n"
    "v_mul_f32 v185, s72, v185\n"
    "v_mul_f32 v186, s72, v186\n"
    "v_mul_f32 v187, s72, v187\n"
    "v_mul_f32 v188, s72, v188\n"
    "v_mul_f32 v189, s72, v189\n"
    "v_mul_f32 v190, -v190, v184\n"
    "v_mul_f32 v191, -v191, v185\n"
    "v_mul_f32 v192, -v192, v186\n"
    "v_mul_f32 v193, -v193, v187\n"
    "v_mul_f32 v194, -v194, v188\n"
    "v_mul_f32 v195, -v195, v189\n"
    "ds_write_b64 v164, v[184:185] offset:0\n"
    "ds_write_b64 v164, v[190:191] offset:96\n"
    "ds_write_b64 v164, v[186:187] offset:8\n"
    "ds_write_b64 v164, v[192:193] offset:104\n"
    "ds_write_b64 v164, v[188:189] offset:16\n"
    "ds_write_b64 v164, v[194:195] offset:112\n"
    "s_add_u32 s76, s71, s60\n"
    "s_add_u32 s77, s71, s61\n"
    "s_add_u32 s78, s71, s62\n"
    "ds_read_b128 v[184:187], v243 offset:0\n"
    "ds_read_b128 v[188:191], v243 offset:96\n"
    "ds_read_b128 v[192:195], v243 offset:16\n"
    "ds_read_b128 v[196:199], v243 offset:112\n"
    "v_cvt_f32_i32 v204, v204\n"
    "v_cvt_f32_i32 v205, v205\n"
    "v_cvt_f32_i32 v206, v206\n"
    "v_cvt_f32_i32 v207, v207\n"
    "v_cvt_f32_i32 v208, v208\n"
    "v_cvt_f32_i32 v209, v209\n"
    "v_cvt_f32_i32 v210, v210\n"
    "v_cvt_f32_i32 v211, v211\n"
    "s_waitcnt lgkmcnt(2)\n"
    "v_fma_f32 v204, v204, v184, v188\n"
    "v_fma_f32 v205, v205, v185, v189\n"
    "v_fma_f32 v206, v206, v186, v190\n"
    "v_fma_f32 v207, v207, v187, v191\n"
    "ds_read_b128 v[184:187], v243 offset:32\n"
    "ds_read_b128 v[188:191], v243 offset:128\n"
    "v_cvt_f32_i32 v212, v212\n"
    "v_cvt_f32_i32 v213, v213\n"
    "v_cvt_f32_i32 v214, v214\n"
    "v_cvt_f32_i32 v215, v215\n"
    "v_cvt_f32_i32 v216, v216\n"
    "v_cvt_f32_i32 v217, v217\n"
    "v_cvt_f32_i32 v218, v218\n"
    "v_cvt_f32_i32 v219, v219\n"
    "s_waitcnt lgkmcnt(2)\n"
    "v_fma_f32 v208, v208, v192, v196\n"
    "v_fma_f32 v209, v209, v193, v197\n"
    "v_fma_f32 v210, v210, v194, v198\n"
    "v_fma_f32 v211, v211, v195, v199\n"
    "ds_read_b128 v[192:195], v243 offset:48\n"
    "ds_read_b128 v[196:199], v243 offset:144\n"
    "v_cvt_pk_bf16_f32 v204, v204, v205\n"
    "v_cvt_pk_bf16_f32 v205, v206, v207\n"
    "v_cvt_pk_bf16_f32 v206, v208, v209\n"
    "v_cvt_pk_bf16_f32 v207, v210, v211\n"
    "buffer_store_dwordx4 v[204:207], v244, s[16:19], s76 offen offset:0 sc1\n"
    "s_waitcnt lgkmcnt(2)\n"
    "v_fma_f32 v212, v212, v184, v188\n"
    "v_fma_f32 v213, v213, v185, v189\n"
    "v_fma_f32 v214, v214, v186, v190\n"
    "v_fma_f32 v215, v215, v187, v191\n"
    "ds_read_b128 v[184:187], v243 offset:64\n"
    "ds_read_b128 v[188:191], v243 offset:160\n"
    "v_cvt_f32_i32 v220, v220\n"
    "v_cvt_f32_i32 v221, v221\n"
    "v_cvt_f32_i32 v222, v222\n"
    "v_cvt_f32_i32 v223, v223\n"
    "v_cvt_f32_i32 v224, v224\n"
    "v_cvt_f32_i32 v225, v225\n"
    "v_cvt_f32_i32 v226, v226\n"
    "v_cvt_f32_i32 v227, v227\n"
    "s_waitcnt lgkmcnt(2)\n"
    "v_fma_f32 v216, v216, v192, v196\n"
    "v_fma_f32 v217, v217, v193, v197\n"
    "v_fma_f32 v218, v218, v194, v198\n"
    "v_fma_f32 v219, v219, v195, v199\n"
    "ds_read_b128 v[192:195], v243 offset:80\n"
    "ds_read_b128 v[196:199], v243 offset:176\n"
    "v_cvt_pk_bf16_f32 v212, v212, v213\n"
    "v_cvt_pk_bf16_f32 v213, v214, v215\n"
    "v_cvt_pk_bf16_f32 v214, v216, v217\n"
    "v_cvt_pk_bf16_f32 v215, v218, v219\n"
    "buffer_store_dwordx4 v[212:215], v244, s[16:19], s77 offen offset:0 sc1\n"
    "s_waitcnt lgkmcnt(2)\n"
    "v_fma_f32 v220, v220, v184, v188\n"
    "v_fma_f32 v221, v221, v185, v189\n"
    "v_fma_f32 v222, v222, v186, v190\n"
    "v_fma_f32 v223, v223, v187, v191\n"
    "s_waitcnt lgkmcnt(0)\n"
    "v_fma_f32 v224, v224, v192, v196\n"
    "v_fma_f32 v225, v225, v193, v197\n"
    "v_fma_f32 v226, v226, v194, v198\n"
    "v_fma_f32 v227, v227, v195, v199\n"
    "v_cvt_pk_bf16_f32 v220, v220, v221\n"
    "v_cvt_pk_bf16_f32 v221, v222, v223\n"
    "v_cvt_pk_bf16_f32 v222, v224, v225\n"
    "v_cvt_pk_bf16_f32 v223, v226, v227\n"
    "buffer_store_dwordx4 v[220:223], v244, s[16:19], s78 offen offset:0 sc1\n"
    "v_add_u32 v243, 192, v243\n"
    "s_branch .Lf2e\n"
    ".Lf2b:\n"
    "s_lshr_b32 s73, s53, 2\n"
    "s_mul_i32 s73, s73, 12288\n"
    "s_and_b32 s74, s53, 3\n"
    "s_lshl_b32 s74, s74, 8\n"
    "s_add_u32 s73, s73, s74\n"
    "s_add_u32 s60, s73, 24576\n"
    "s_add_u32 s61, s73, 28672\n"
    "s_add_u32 s62, s73, 32768\n"
    "s_waitcnt vmcnt(53)\n"
    "s_add_u32 s76, s71, s60\n"
    "s_add_u32 s77, s71, s61\n"
    "s_add_u32 s78, s71, s62\n"
    "v_cvt_f32_i32 v204, v204\n"
    "v_cvt_f32_i32 v208, v208\n"
    "v_cvt_f32_i32 v205, v205\n"
    "v_cvt_f32_i32 v209, v209\n"
    "v_cvt_f32_i32 v206, v206\n"
    "v_cvt_f32_i32 v210, v210\n"
    "v_cvt_pk_bf16_f32 v207, v204, v208\n"
    "v_cvt_pk_bf16_f32 v211, v205, v209\n"
    "v_cvt_pk_bf16_f32 v215, v206, v210\n"
    "v_cvt_f32_i32 v212, v212\n"
    "v_cvt_f32_i32 v216, v216\n"
    "v_cvt_f32_i32 v213, v213\n"
    "v_cvt_f32_i32 v217, v217\n"
    "v_cvt_f32_i32 v214, v214\n"
    "v_cvt_f32_i32 v218, v218\n"
    "v_cvt_pk_bf16_f32 v206, v212, v216\n"
    "v_cvt_pk_bf16_f32 v210, v213, v217\n"
    "v_cvt_pk_bf16_f32 v214, v214, v218\n"
    "v_cvt_f32_i32 v220, v220\n"
    "v_cvt_f32_i32 v224, v224\n"
    "v_cvt_f32_i32 v221, v221\n"
    "v_cvt_f32_i32 v225, v225\n"
    "v_cvt_f32_i32 v222, v222\n"
    "v_cvt_f32_i32 v226, v226\n"
    "v_cvt_pk_bf16_f32 v205, v220, v224\n"
    "v_cvt_pk_bf16_f32 v209, v221, v225\n"
    "v_cvt_pk_bf16_f32 v213, v222, v226\n"
    "v_cvt_f32_i32 v228, v228\n"
    "v_cvt_f32_i32 v232, v232\n"
    "v_cvt_f32_i32 v229, v229\n"
    "v_cvt_f32_i32 v233, v233\n"
    "v_cvt_f32_i32 v230, v230\n"
    "v_cvt_f32_i32 v234, v234\n"
    "v_cvt_pk_bf16_f32 v204, v228, v232\n"
    "v_cvt_pk_bf16_f32 v208, v229, v233\n"
    "v_cvt_pk_bf16_f32 v212, v230, v234\n"
    "buffer_store_dwordx4 v[204:207], v244, s[16:19], s76 offen offset:0 sc1\n"
    "buffer_store_dwordx4 v[208:211], v244, s[16:19], s77 offen offset:0 sc1\n"
    "buffer_store_dwordx4 v[212:215], v244, s[16:19], s78 offen offset:0 sc1\n"
    ".Lf2e:\n"
    "s_nop 4\n"
    "s_waitcnt vmcnt(46)\n"
    "v_lshlrev_b32 v184, 16, v100\n"
    "v_and_b32 v185, 0xffff0000, v100\n"
    "v_lshlrev_b32 v186, 16, v101\n"
    "v_and_b32 v187, 0xffff0000, v101\n"
    "v_lshlrev_b32 v188, 16, v102\n"
    "v_and_b32 v189, 0xffff0000, v102\n"
    "v_lshlrev_b32 v190, 16, v103\n"
    "v_and_b32 v191, 0xffff0000, v103\n"
    "v_lshlrev_b32 v192, 16, v104\n"
    "v_and_b32 v193, 0xffff0000, v104\n"
    "v_lshlrev_b32 v194, 16, v105\n"
    "v_and_b32 v195, 0xffff0000, v105\n"
    "v_lshlrev_b32 v196, 16, v106\n"
    "v_and_b32 v197, 0xffff0000, v106\n"
    "v_lshlrev_b32 v198, 16, v107\n"
    "v_and_b32 v199, 0xffff0000, v107\n"
    "v_mul_f32 v184, s72, v184\n"
    "v_mul_f32 v185, s72, v185\n"
    "v_mul_f32 v186, s72, v186\n"
    "v_mul_f32 v187, s72, v187\n"
    "v_mul_f32 v188, s72, v188\n"
    "v_mul_f32 v189, s72, v189\n"
    "v_mul_f32 v190, s72, v190\n"
    "v_mul_f32 v191, s72, v191\n"
    "v_mul_f32 v192, -v192, v184\n"
    "v_mul_f32 v193, -v193, v185\n"
    "v_mul_f32 v194, -v194, v186\n"
    "v_mul_f32 v195, -v195, v187\n"
    "v_mul_f32 v196, -v196, v188\n"
    "v_mul_f32 v197, -v197, v189\n"
    "v_mul_f32 v198, -v198, v190\n"
    "v_mul_f32 v199, -v199, v191\n"
    "v_cvt_f32_i32 v4, v4\n"
    "v_cvt_f32_i32 v5, v5\n"
    "v_cvt_f32_i32 v6, v6\n"
    "v_cvt_f32_i32 v7, v7\n"
    "v_cvt_f32_i32 v8, v8\n"
    "v_cvt_f32_i32 v9, v9\n"
    "v_cvt_f32_i32 v10, v10\n"
    "v_cvt_f32_i32 v11, v11\n"
    "v_fma_f32 v4, v4, v184, v192\n"
    "v_fma_f32 v5, v5, v185, v193\n"
    "v_fma_f32 v6, v6, v186, v194\n"
    "v_fma_f32 v7, v7, v187, v195\n"
    "v_fma_f32 v8, v8, v188, v196\n"
    "v_fma_f32 v9, v9, v189, v197\n"
    "v_fma_f32 v10, v10, v190, v198\n"
    "v_fma_f32 v11, v11, v191, v199\n"
    "v_cvt_pk_bf16_f32 v4, v4, v5\n"
    "v_cvt_pk_bf16_f32 v5, v6, v7\n"
    "v_cvt_pk_bf16_f32 v6, v8, v9\n"
    "v_cvt_pk_bf16_f32 v7, v10, v11\n"
    "ds_write_b128 v162, v[4:7] offset:0\n"
    "v_cvt_f32_i32 v12, v12\n"
    "v_cvt_f32_i32 v13, v13\n"
    "v_cvt_f32_i32 v14, v14\n"
    "v_cvt_f32_i32 v15, v15\n"
    "v_cvt_f32_i32 v16, v16\n"
    "v_cvt_f32_i32 v17, v17\n"
    "v_cvt_f32_i32 v18, v18\n"
    "v_cvt_f32_i32 v19, v19\n"
    "v_fma_f32 v12, v12, v184, v192\n"
    "v_fma_f32 v13, v13, v185, v193\n"
    "v_fma_f32 v14, v14, v186, v194\n"
    "v_fma_f32 v15, v15, v187, v195\n"
    "v_fma_f32 v16, v16, v188, v196\n"
    "v_fma_f32 v17, v17, v189, v197\n"
    "v_fma_f32 v18, v18, v190, v198\n"
    "v_fma_f32 v19, v19, v191, v199\n"
    "v_cvt_pk_bf16_f32 v12, v12, v13\n"
    "v_cvt_pk_bf16_f32 v13, v14, v15\n"
    "v_cvt_pk_bf16_f32 v14, v16, v17\n"
    "v_cvt_pk_bf16_f32 v15, v18, v19\n"
    "ds_write_b128 v162, v[12:15] offset:1024\n"
    "v_cvt_f32_i32 v20, v20\n"
    "v_cvt_f32_i32 v21, v21\n"
    "v_cvt_f32_i32 v22, v22\n"
    "v_cvt_f32_i32 v23, v23\n"
    "v_cvt_f32_i32 v24, v24\n"
    "v_cvt_f32_i32 v25, v25\n"
    "v_cvt_f32_i32 v26, v26\n"
    "v_cvt_f32_i32 v27, v27\n"
    "v_fma_f32 v20, v20, v184, v192\n"
    "v_fma_f32 v21, v21, v185, v193\n"
    "v_fma_f32 v22, v22, v186, v194\n"
    "v_fma_f32 v23, v23, v187, v195\n"
    "v_fma_f32 v24, v24, v188, v196\n"
    "v_fma_f32 v25, v25, v189, v197\n"
    "v_fma_f32 v26, v26, v190, v198\n"
    "v_fma_f32 v27, v27, v191, v199\n"
    "v_cvt_pk_bf16_f32 v20, v20, v21\n"
    "v_cvt_pk_bf16_f32 v21, v22, v23\n"
    "v_cvt_pk_bf16_f32 v22, v24, v25\n"
    "v_cvt_pk_bf16_f32 v23, v26, v27\n"
    "ds_write_b128 v162, v[20:23] offset:12288\n"
    "v_cvt_f32_i32 v28, v28\n"
    "v_cvt_f32_i32 v29, v29\n"
    "v_cvt_f32_i32 v30, v30\n"
    "v_cvt_f32_i32 v31, v31\n"
    "v_cvt_f32_i32 v32, v32\n"
    "v_cvt_f32_i32 v33, v33\n"
    "v_cvt_f32_i32 v34, v34\n"
    "v_cvt_f32_i32 v35, v35\n"
    "v_fma_f32 v28, v28, v184, v192\n"
    "v_fma_f32 v29, v29, v185, v193\n"
    "v_fma_f32 v30, v30, v186, v194\n"
    "v_fma_f32 v31, v31, v187, v195\n"
    "v_fma_f32 v32, v32, v188, v196\n"
    "v_fma_f32 v33, v33, v189, v197\n"
    "v_fma_f32 v34, v34, v190, v198\n"
    "v_fma_f32 v35, v35, v191, v199\n"
    "v_cvt_pk_bf16_f32 v28, v28, v29\n"
    "v_cvt_pk_bf16_f32 v29, v30, v31\n"
    "v_cvt_pk_bf16_f32 v30, v32, v33\n"
    "v_cvt_pk_bf16_f32 v31, v34, v35\n"
    "ds_write_b128 v162, v[28:31] offset:13312\n"
    "s_waitcnt vmcnt(36)\n"
    "v_lshlrev_b32 v184, 16, v108\n"
    "v_and_b32 v185, 0xffff0000, v108\n"
    "v_lshlrev_b32 v186, 16, v109\n"
    "v_and_b32 v187, 0xffff0000, v109\n"
    "v_lshlrev_b32 v188, 16, v110\n"
    "v_and_b32 v189, 0xffff0000, v110\n"
    "v_lshlrev_b32 v190, 16, v111\n"
    "v_and_b32 v191, 0xffff0000, v111\n"
    "v_lshlrev_b32 v192, 16, v112\n"
    "v_and_b32 v193, 0xffff0000, v112\n"
    "v_lshlrev_b32 v194, 16, v113\n"
    "v_and_b32 v195, 0xffff0000, v113\n"
    "v_lshlrev_b32 v196, 16, v114\n"
    "v_and_b32 v197, 0xffff0000, v114\n"
    "v_lshlrev_b32 v198, 16, v115\n"
    "v_and_b32 v199, 0xffff0000, v115\n"
    "v_mul_f32 v184, s72, v184\n"
    "v_mul_f32 v185, s72, v185\n"
    "v_mul_f32 v186, s72, v186\n"
    "v_mul_f32 v187, s72, v187\n"
    "v_mul_f32 v188, s72, v188\n"
    "v_mul_f32 v189, s72, v189\n"
    "v_mul_f32 v190, s72, v190\n"
    "v_mul_f32 v191, s72, v191\n"
    "v_mul_f32 v192, -v192, v184\n"
    "v_mul_f32 v193, -v193, v185\n"
    "v_mul_f32 v194, -v194, v186\n"
    "v_mul_f32 v195, -v195, v187\n"
    "v_mul_f32 v196, -v196, v188\n"
    "v_mul_f32 v197, -v197, v189\n"
    "v_mul_f32 v198, -v198, v190\n"
    "v_mul_f32 v199, -v199, v191\n"
    "v_cvt_f32_i32 v36, v36\n"
    "v_cvt_f32_i32 v37, v37\n"
    "v_cvt_f32_i32 v38, v38\n"
    "v_cvt_f32_i32 v39, v39\n"
    "v_cvt_f32_i32 v40, v40\n"
    "v_cvt_f32_i32 v41, v41\n"
    "v_cvt_f32_i32 v42, v42\n"
    "v_cvt_f32_i32 v43, v43\n"
    "v_fma_f32 v36, v36, v184, v192\n"
    "v_fma_f32 v37, v37, v185, v193\n"
    "v_fma_f32 v38, v38, v186, v194\n"
    "v_fma_f32 v39, v39, v187, v195\n"
    "v_fma_f32 v40, v40, v188, v196\n"
    "v_fma_f32 v41, v41, v189, v197\n"
    "v_fma_f32 v42, v42, v190, v198\n"
    "v_fma_f32 v43, v43, v191, v199\n"
    "v_cvt_pk_bf16_f32 v36, v36, v37\n"
    "v_cvt_pk_bf16_f32 v37, v38, v39\n"
    "v_cvt_pk_bf16_f32 v38, v40, v41\n"
    "v_cvt_pk_bf16_f32 v39, v42, v43\n"
    "ds_write_b128 v162, v[36:39] offset:2048\n"
    "v_cvt_f32_i32 v44, v44\n"
    "v_cvt_f32_i32 v45, v45\n"
    "v_cvt_f32_i32 v46, v46\n"
    "v_cvt_f32_i32 v47, v47\n"
    "v_cvt_f32_i32 v48, v48\n"
    "v_cvt_f32_i32 v49, v49\n"
    "v_cvt_f32_i32 v50, v50\n"
    "v_cvt_f32_i32 v51, v51\n"
    "v_fma_f32 v44, v44, v184, v192\n"
    "v_fma_f32 v45, v45, v185, v193\n"
    "v_fma_f32 v46, v46, v186, v194\n"
    "v_fma_f32 v47, v47, v187, v195\n"
    "v_fma_f32 v48, v48, v188, v196\n"
    "v_fma_f32 v49, v49, v189, v197\n"
    "v_fma_f32 v50, v50, v190, v198\n"
    "v_fma_f32 v51, v51, v191, v199\n"
    "v_cvt_pk_bf16_f32 v44, v44, v45\n"
    "v_cvt_pk_bf16_f32 v45, v46, v47\n"
    "v_cvt_pk_bf16_f32 v46, v48, v49\n"
    "v_cvt_pk_bf16_f32 v47, v50, v51\n"
    "ds_write_b128 v162, v[44:47] offset:3072\n"
    "v_cvt_f32_i32 v52, v52\n"
    "v_cvt_f32_i32 v53, v53\n"
    "v_cvt_f32_i32 v54, v54\n"
    "v_cvt_f32_i32 v55, v55\n"
    "v_cvt_f32_i32 v56, v56\n"
    "v_cvt_f32_i32 v57, v57\n"
    "v_cvt_f32_i32 v58, v58\n"
    "v_cvt_f32_i32 v59, v59\n"
    "v_fma_f32 v52, v52, v184, v192\n"
    "v_fma_f32 v53, v53, v185, v193\n"
    "v_fma_f32 v54, v54, v186, v194\n"
    "v_fma_f32 v55, v55, v187, v195\n"
    "v_fma_f32 v56, v56, v188, v196\n"
    "v_fma_f32 v57, v57, v189, v197\n"
    "v_fma_f32 v58, v58, v190, v198\n"
    "v_fma_f32 v59, v59, v191, v199\n"
    "v_cvt_pk_bf16_f32 v52, v52, v53\n"
    "v_cvt_pk_bf16_f32 v53, v54, v55\n"
    "v_cvt_pk_bf16_f32 v54, v56, v57\n"
    "v_cvt_pk_bf16_f32 v55, v58, v59\n"
    "ds_write_b128 v162, v[52:55] offset:14336\n"
    "v_cvt_f32_i32 v60, v60\n"
    "v_cvt_f32_i32 v61, v61\n"
    "v_cvt_f32_i32 v62, v62\n"
    "v_cvt_f32_i32 v63, v63\n"
    "v_cvt_f32_i32 v64, v64\n"
    "v_cvt_f32_i32 v65, v65\n"
    "v_cvt_f32_i32 v66, v66\n"
    "v_cvt_f32_i32 v67, v67\n"
    "v_fma_f32 v60, v60, v184, v192\n"
    "v_fma_f32 v61, v61, v185, v193\n"
    "v_fma_f32 v62, v62, v186, v194\n"
    "v_fma_f32 v63, v63, v187, v195\n"
    "v_fma_f32 v64, v64, v188, v196\n"
    "v_fma_f32 v65, v65, v189, v197\n"
    "v_fma_f32 v66, v66, v190, v198\n"
    "v_fma_f32 v67, v67, v191, v199\n"
    "v_cvt_pk_bf16_f32 v60, v60, v61\n"
    "v_cvt_pk_bf16_f32 v61, v62, v63\n"
    "v_cvt_pk_bf16_f32 v62, v64, v65\n"
    "v_cvt_pk_bf16_f32 v63, v66, v67\n"
    "ds_write_b128 v162, v[60:63] offset:15360\n"
    "s_waitcnt vmcnt(26)\n"
    "v_lshlrev_b32 v184, 16, v116\n"
    "v_and_b32 v185, 0xffff0000, v116\n"
    "v_lshlrev_b32 v186, 16, v117\n"
    "v_and_b32 v187, 0xffff0000, v117\n"
    "v_lshlrev_b32 v188, 16, v118\n"
    "v_and_b32 v189, 0xffff0000, v118\n"
    "v_lshlrev_b32 v190, 16, v119\n"
    "v_and_b32 v191, 0xffff0000, v119\n"
    "v_lshlrev_b32 v192, 16, v120\n"
    "v_and_b32 v193, 0xffff0000, v120\n"
    "v_lshlrev_b32 v194, 16, v121\n"
    "v_and_b32 v195, 0xffff0000, v121\n"
    "v_lshlrev_b32 v196, 16, v122\n"
    "v_and_b32 v197, 0xffff0000, v122\n"
    "v_lshlrev_b32 v198, 16, v123\n"
    "v_and_b32 v199, 0xffff0000, v123\n"
    "v_mul_f32 v184, s72, v184\n"
    "v_mul_f32 v185, s72, v185\n"
    "v_mul_f32 v186, s72, v186\n"
    "v_mul_f32 v187, s72, v187\n"
    "v_mul_f32 v188, s72, v188\n"
    "v_mul_f32 v189, s72, v189\n"
    "v_mul_f32 v190, s72, v190\n"
    "v_mul_f32 v191, s72, v191\n"
    "v_mul_f32 v192, -v192, v184\n"
    "v_mul_f32 v193, -v193, v185\n"
    "v_mul_f32 v194, -v194, v186\n"
    "v_mul_f32 v195, -v195, v187\n"
    "v_mul_f32 v196, -v196, v188\n"
    "v_mul_f32 v197, -v197, v189\n"
    "v_mul_f32 v198, -v198, v190\n"
    "v_mul_f32 v199, -v199, v191\n"
    "v_cvt_f32_i32 v68, v68\n"
    "v_cvt_f32_i32 v69, v69\n"
    "v_cvt_f32_i32 v70, v70\n"
    "v_cvt_f32_i32 v71, v71\n"
    "v_cvt_f32_i32 v72, v72\n"
    "v_cvt_f32_i32 v73, v73\n"
    "v_cvt_f32_i32 v74, v74\n"
    "v_cvt_f32_i32 v75, v75\n"
    "v_fma_f32 v68, v68, v184, v192\n"
    "v_fma_f32 v69, v69, v185, v193\n"
    "v_fma_f32 v70, v70, v186, v194\n"
    "v_fma_f32 v71, v71, v187, v195\n"
    "v_fma_f32 v72, v72, v188, v196\n"
    "v_fma_f32 v73, v73, v189, v197\n"
    "v_fma_f32 v74, v74, v190, v198\n"
    "v_fma_f32 v75, v75, v191, v199\n"
    "v_cvt_pk_bf16_f32 v68, v68, v69\n"
    "v_cvt_pk_bf16_f32 v69, v70, v71\n"
    "v_cvt_pk_bf16_f32 v70, v72, v73\n"
    "v_cvt_pk_bf16_f32 v71, v74, v75\n"
    "ds_write_b128 v162, v[68:71] offset:4096\n"
    "v_cvt_f32_i32 v76, v76\n"
    "v_cvt_f32_i32 v77, v77\n"
    "v_cvt_f32_i32 v78, v78\n"
    "v_cvt_f32_i32 v79, v79\n"
    "v_cvt_f32_i32 v80, v80\n"
    "v_cvt_f32_i32 v81, v81\n"
    "v_cvt_f32_i32 v82, v82\n"
    "v_cvt_f32_i32 v83, v83\n"
    "v_fma_f32 v76, v76, v184, v192\n"
    "v_fma_f32 v77, v77, v185, v193\n"
    "v_fma_f32 v78, v78, v186, v194\n"
    "v_fma_f32 v79, v79, v187, v195\n"
    "v_fma_f32 v80, v80, v188, v196\n"
    "v_fma_f32 v81, v81, v189, v197\n"
    "v_fma_f32 v82, v82, v190, v198\n"
    "v_fma_f32 v83, v83, v191, v199\n"
    "v_cvt_pk_bf16_f32 v76, v76, v77\n"
    "v_cvt_pk_bf16_f32 v77, v78, v79\n"
    "v_cvt_pk_bf16_f32 v78, v80, v81\n"
    "v_cvt_pk_bf16_f32 v79, v82, v83\n"
    "ds_write_b128 v162, v[76:79] offset:5120\n"
    "v_cvt_f32_i32 v84, v84\n"
    "v_cvt_f32_i32 v85, v85\n"
    "v_cvt_f32_i32 v86, v86\n"
    "v_cvt_f32_i32 v87, v87\n"
    "v_cvt_f32_i32 v88, v88\n"
    "v_cvt_f32_i32 v89, v89\n"
    "v_cvt_f32_i32 v90, v90\n"
    "v_cvt_f32_i32 v91, v91\n"
    "v_fma_f32 v84, v84, v184, v192\n"
    "v_fma_f32 v85, v85, v185, v193\n"
    "v_fma_f32 v86, v86, v186, v194\n"
    "v_fma_f32 v87, v87, v187, v195\n"
    "v_fma_f32 v88, v88, v188, v196\n"
    "v_fma_f32 v89, v89, v189, v197\n"
    "v_fma_f32 v90, v90, v190, v198\n"
    "v_fma_f32 v91, v91, v191, v199\n"
    "v_cvt_pk_bf16_f32 v84, v84, v85\n"
    "v_cvt_pk_bf16_f32 v85, v86, v87\n"
    "v_cvt_pk_bf16_f32 v86, v88, v89\n"
    "v_cvt_pk_bf16_f32 v87, v90, v91\n"
    "ds_write_b128 v162, v[84:87] offset:16384\n"
    "v_cvt_f32_i32 v92, v92\n"
    "v_cvt_f32_i32 v93, v93\n"
    "v_cvt_f32_i32 v94, v94\n"
    "v_cvt_f32_i32 v95, v95\n"
    "v_cvt_f32_i32 v96, v96\n"
    "v_cvt_f32_i32 v97, v97\n"
    "v_cvt_f32_i32 v98, v98\n"
    "v_cvt_f32_i32 v99, v99\n"
    "v_fma_f32 v92, v92, v184, v192\n"
    "v_fma_f32 v93, v93, v185, v193\n"
    "v_fma_f32 v94, v94, v186, v194\n"
    "v_fma_f32 v95, v95, v187, v195\n"
    "v_fma_f32 v96, v96, v188, v196\n"
    "v_fma_f32 v97, v97, v189, v197\n"
    "v_fma_f32 v98, v98, v190, v198\n"
    "v_fma_f32 v99, v99, v191, v199\n"
    "v_cvt_pk_bf16_f32 v92, v92, v93\n"
    "v_cvt_pk_bf16_f32 v93, v94, v95\n"
    "v_cvt_pk_bf16_f32 v94, v96, v97\n"
    "v_cvt_pk_bf16_f32 v95, v98, v99\n"
    "ds_write_b128 v162, v[92:95] offset:17408\n"
    "s_add_u32 s74, s85, 12288\n"
    "s_lshr_b32 s74, s74, 1\n"
    "buffer_load_dwordx3 v[36:38], v236, s[40:43], s74 offen offset:1536\n"
    "buffer_load_dwordx3 v[40:42], v236, s[40:43], s74 offen offset:2304\n"
    "buffer_load_dwordx3 v[44:46], v236, s[40:43], s74 offen offset:0\n"
    "buffer_load_dwordx3 v[48:50], v236, s[40:43], s74 offen offset:768\n"
    "buffer_load_dwordx3 v[52:54], v245, s[40:43], s74 offen offset:1536\n"
    "buffer_load_dwordx3 v[56:58], v245, s[40:43], s74 offen offset:2304\n"
    "buffer_load_dwordx3 v[60:62], v245, s[40:43], s74 offen offset:0\n"
    "buffer_load_dwordx3 v[64:66], v245, s[40:43], s74 offen offset:768\n"
    "s_waitcnt vmcnt(31)\n"
    "v_lshlrev_b32 v184, 16, v176\n"
    "v_and_b32 v185, 0xffff0000, v176\n"
    "v_lshlrev_b32 v186, 16, v177\n"
    "v_and_b32 v187, 0xffff0000, v177\n"
    "v_lshlrev_b32 v188, 16, v178\n"
    "v_and_b32 v189, 0xffff0000, v178\n"
    "v_lshlrev_b32 v190, 16, v179\n"
    "v_and_b32 v191, 0xffff0000, v179\n"
    "v_log_f32 v68, v184\n"
    "v_log_f32 v69, v185\n"
    "v_log_f32 v70, v186\n"
    "v_log_f32 v71, v187\n"
    "v_log_f32 v72, v188\n"
    "v_log_f32 v73, v189\n"
    "v_log_f32 v74, v190\n"
    "v_log_f32 v75, v191\n"
    "v_rcp_f32 v184, v184\n"
    "v_rcp_f32 v185, v185\n"
    "v_rcp_f32 v186, v186\n"
    "v_rcp_f32 v187, v187\n"
    "v_rcp_f32 v188, v188\n"
    "v_rcp_f32 v189, v189\n"
    "v_rcp_f32 v190, v190\n"
    "v_rcp_f32 v191, v191\n"
    "s_mov_b32 s73, 0x41a00000\n"
    "v_mov_b32 v167, 0x7fc00000\n"
    "v_cmp_le_f32 vcc, |v68|, s73\n"
    "s_nop 1\n"
    "v_cndmask_b32 v68, v167, v68, vcc\n"
    "v_cmp_le_f32 vcc, |v69|, s73\n"
    "s_nop 1\n"
    "v_cndmask_b32 v69, v167, v69, vcc\n"
    "v_cmp_le_f32 vcc, |v70|, s73\n"
    "s_nop 1\n"
    "v_cndmask_b32 v70, v167, v70, vcc\n"
    "v_cmp_le_f32 vcc, |v71|, s73\n"
    "s_nop 1\n"
    "v_cndmask_b32 v71, v167, v71, vcc\n"
    "v_cmp_le_f32 vcc, |v72|, s73\n"
    "s_nop 1\n"
    "v_cndmask_b32 v72, v167, v72, vcc\n"
    "v_cmp_le_f32 vcc, |v73|, s73\n"
    "s_nop 1\n"
    "v_cndmask_b32 v73, v167, v73, vcc\n"
    "v_cmp_le_f32 vcc, |v74|, s73\n"
    "s_nop 1\n"
    "v_cndmask_b32 v74, v167, v74, vcc\n"
    "v_cmp_le_f32 vcc, |v75|, s73\n"
    "s_nop 1\n"
    "v_cndmask_b32 v75, v167, v75, vcc\n"
    "v_cvt_pk_bf16_f32 v76, v184, v185\n"
    "v_cvt_pk_bf16_f32 v77, v186, v187\n"
    "v_cvt_pk_bf16_f32 v78, v188, v189\n"
    "v_cvt_pk_bf16_f32 v79, v190, v191\n"
    "v_lshlrev_b32 v192, 16, v76\n"
    "v_and_b32 v193, 0xffff0000, v76\n"
    "v_lshlrev_b32 v194, 16, v77\n"
    "v_and_b32 v195, 0xffff0000, v77\n"
    "v_lshlrev_b32 v196, 16, v78\n"
    "v_and_b32 v197, 0xffff0000, v78\n"
    "v_lshlrev_b32 v198, 16, v79\n"
    "v_and_b32 v199, 0xffff0000, v79\n"
    "v_sub_f32 v184, v184, v192\n"
    "v_sub_f32 v185, v185, v193\n"
    "v_sub_f32 v186, v186, v194\n"
    "v_sub_f32 v187, v187, v195\n"
    "v_sub_f32 v188, v188, v196\n"
    "v_sub_f32 v189, v189, v197\n"
    "v_sub_f32 v190, v190, v198\n"
    "v_sub_f32 v191, v191, v199\n"
    "v_cvt_pk_bf16_f32 v80, v184, v185\n"
    "v_cvt_pk_bf16_f32 v81, v186, v187\n"
    "v_cvt_pk_bf16_f32 v82, v188, v189\n"
    "v_cvt_pk_bf16_f32 v83, v190, v191\n"
    "v_mov_b32 v184, 0\n"
    "v_mov_b32 v185, 0\n"
    "v_mov_b32 v186, 0\n"
    "v_mov_b32 v187, 0\n"
    "ds_write_b128 v252, v[76:79] offset:256\n"
    "ds_write_b128 v252, v[80:83] offset:384\n"
    "ds_write_b128 v252, v[180:183] offset:512\n"
    "ds_write_b128 v252, v[184:187] offset:640\n"
    "v_add_co_u32 v238, vcc, 0x7f4a7c15, v238\n"
    "v_mov_b32 v167, 0x9e3779b9\n"
    "s_nop 0\n"
    "v_addc_co_u32 v239, vcc, v167, v239, vcc\n"
    "v_mov_b32 v84, v157\n"
    "v_mov_b32 v85, v158\n"
    "v_mov_b32 v86, v159\n"
    "v_xor_b32 v87, 64, v253\n"
    "v_mov_b32 v252, v163\n"
    "v_mov_b32 v246, 1\n"
    "s_mov_b32 s58, 0\n"
    "s_mov_b32 s59, 4\n"
    "v_mov_b32 v108, 0\n"
    "v_mov_b32 v109, 0\n"
    "v_mov_b32 v110, 0\n"
    "v_mov_b32 v111, 0\n"
    "v_mov_b32 v112, 0\n"
    "v_mov_b32 v113, 0\n"
    "v_mov_b32 v114, 0\n"
    "v_mov_b32 v115, 0\n"
    "v_mov_b32 v116, 0\n"
    "v_mov_b32 v117, 0\n"
    "v_mov_b32 v118, 0\n"
    "v_mov_b32 v119, 0\n"
    "v_mov_b32 v120, 0\n"
    "v_mov_b32 v121, 0\n"
    "v_mov_b32 v122, 0\n"
    "v_mov_b32 v123, 0\n"
    "s_waitcnt lgkmcnt(0)\n"
    "s_barrier\n"
    "ds_read_b128 v[156:159], v2 offset:0\n"
    "ds_read_b128 v[160:163], v2 offset:2048\n"
    "ds_read_b128 v[164:167], v2 offset:4096\n"
    "ds_read_b128 v[168:171], v2 offset:6144\n"
    "ds_read_b128 v[172:175], v2 offset:8192\n"
    "ds_read_b128 v[176:179], v2 offset:10240\n"
    "ds_read_b128 v[180:183], v2 offset:1024\n"
    "ds_read_b128 v[184:187], v2 offset:3072\n"
    "ds_read_b128 v[188:191], v2 offset:5120\n"
    "ds_read_b128 v[192:195], v2 offset:7168\n"
    "ds_read_b128 v[196:199], v2 offset:9216\n"
    "ds_read_b128 v[200:203], v2 offset:11264\n"
    "s_waitcnt vmcnt(25)\n"
    "ds_read_b128 a[192:195], v253 offset:0\n"
    "ds_read_b128 a[196:199], v87 offset:0\n"
    "ds_read_b128 a[200:203], v253 offset:128\n"
    "ds_read_b128 a[204:207], v87 offset:128\n"
    "ds_read_b128 a[208:211], v253 offset:256\n"
    "ds_read_b128 a[212:215], v87 offset:256\n"
    "s_waitcnt lgkmcnt(0)\n"
    "s_add_u32 m0, s65, 49152\n"
    "s_add_u32 s72, s85, 12288\n"
    "buffer_load_dwordx4 v84, s[20:23], s72 offen offset:0 nt lds\n"
    "buffer_load_dwordx4 v85, s[20:23], s72 offen offset:1024 nt lds\n"
    "buffer_load_dwordx4 v86, s[20:23], s72 offen offset:2048 nt lds\n"
    "s_add_u32 m0, s65, 52224\n"
    "s_add_u32 s73, s85, 15360\n"
    "buffer_load_dwordx4 v84, s[20:23], s73 offen offset:0 nt lds\n"
    "buffer_load_dwordx4 v85, s[20:23], s73 offen offset:1024 nt lds\n"
    "buffer_load_dwordx4 v86, s[20:23], s73 offen offset:2048 nt lds\n"
    "v_mfma_f32_16x16x32_bf16 v[4:7], v[156:159], a[192:195], 0\n"
    "v_mfma_f32_16x16x32_bf16 v[4:7], v[160:163], a[196:199], v[4:7]\n"
    "v_mfma_f32_16x16x32_bf16 v[4:7], v[164:167], a[200:203], v[4:7]\n"
    "v_mfma_f32_16x16x32_bf16 v[4:7], v[168:171], a[204:207], v[4:7]\n"
    "v_mfma_f32_16x16x32_bf16 v[4:7], v[172:175], a[208:211], v[4:7]\n"
    "v_mfma_f32_16x16x32_bf16 v[4:7], v[176:179], a[212:215], v[4:7]\n"
    "v_mfma_f32_16x16x32_bf16 v[20:23], v[180:183], a[192:195], 0\n"
    "v_mfma_f32_16x16x32_bf16 v[20:23], v[184:187], a[196:199], v[20:23]\n"
    "v_mfma_f32_16x16x32_bf16 v[20:23], v[188:191], a[200:203], v[20:23]\n"
    "v_mfma_f32_16x16x32_bf16 v[20:23], v[192:195], a[204:207], v[20:23]\n"
    "v_mfma_f32_16x16x32_bf16 v[20:23], v[196:199], a[208:211], v[20:23]\n"
    "v_mfma_f32_16x16x32_bf16 v[20:23], v[200:203], a[212:215], v[20:23]\n"
    "v_mov_b32 v77, 154880\n"
    "s_nop 0\n"
    "v_max3_f32 v76, v4, v5, v6\n"
    "s_nop 4\n"
    "v_max3_f32 v76, v76, v7, v20\n"
    "v_max3_f32 v76, v76, v21, v22\n"
    "v_max_f32 v76, v76, v23\n"
    "v_mov_b32 v78, v76\n"
    "s_nop 1\n"
    "v_permlane32_swap_b32 v76, v78\n"
    "v_max_f32 v76, v76, v78\n"
    "v_mov_b32 v78, v76\n"
    "s_nop 1\n"
    "v_permlane16_swap_b32 v76, v78\n"
    "v_max_f32 v76, v76, v78\n"
    "ds_max_f32 v77, v76\n"
    "s_waitcnt lgkmcnt(0)\n"
    "s_barrier\n"
    "ds_read_b32 v76, v77\n"
    "s_waitcnt lgkmcnt(0)\n"
    "v_sub_f32 v68, v68, v76\n"
    "v_sub_f32 v69, v69, v76\n"
    "v_sub_f32 v70, v70, v76\n"
    "v_sub_f32 v71, v71, v76\n"
    "v_sub_f32 v72, v72, v76\n"
    "v_sub_f32 v73, v73, v76\n"
    "v_sub_f32 v74, v74, v76\n"
    "v_sub_f32 v75, v75, v76\n"
    "ds_write_b128 v252, v[68:71] offset:0\n"
    "ds_write_b128 v252, v[72:75] offset:16\n"
    "s_mul_i32 s73, s79, 768\n"
    "v_add_u32 v77, s73, v250\n"
    "s_waitcnt lgkmcnt(0)\n"
    "s_barrier\n"
    "ds_read_b128 v[100:103], v77 offset:0\n"
    "ds_read_b128 v[104:107], v77 offset:16\n"
    "s_waitcnt vmcnt(25)\n"
    "ds_read_b128 a[216:219], v253 offset:49152\n"
    "ds_read_b128 a[220:223], v87 offset:49152\n"
    "ds_read_b128 a[224:227], v253 offset:49280\n"
    "ds_read_b128 a[228:231], v87 offset:49280\n"
    "ds_read_b128 a[232:235], v253 offset:49408\n"
    "ds_read_b128 a[236:239], v87 offset:49408\n"
    "s_waitcnt lgkmcnt(0)\n"
    "s_add_u32 m0, s65, 98304\n"
    "s_add_u32 s72, s85, 18432\n"
    "buffer_load_dwordx4 v84, s[20:23], s72 offen offset:0 nt lds\n"
    "buffer_load_dwordx4 v85, s[20:23], s72 offen offset:1024 nt lds\n"
    "buffer_load_dwordx4 v86, s[20:23], s72 offen offset:2048 nt lds\n"
    "s_add_u32 m0, s65, 101376\n"
    "s_add_u32 s73, s85, 21504\n"
    "buffer_load_dwordx4 v84, s[20:23], s73 offen offset:0 nt lds\n"
    "buffer_load_dwordx4 v85, s[20:23], s73 offen offset:1024 nt lds\n"
    "buffer_load_dwordx4 v86, s[20:23], s73 offen offset:2048 nt lds\n"
    "s_add_u32 s71, s71, 98304\n"
    "s_add_u32 s75, s71, s63\n"
    "s_bitcmp1_b32 s84, 0\n"
    "s_cbranch_scc1 .Lf3b\n"
    "buffer_load_dwordx4 v[204:207], v242, s[28:31], s75 offen offset:0\n"
    "buffer_load_dwordx4 v[208:211], v242, s[28:31], s75 offen offset:16\n"
    "buffer_load_dwordx4 v[212:215], v242, s[28:31], s75 offen offset:32\n"
    "buffer_load_dwordx4 v[216:219], v242, s[28:31], s75 offen offset:48\n"
    "buffer_load_dwordx4 v[220:223], v242, s[28:31], s75 offen offset:64\n"
    "buffer_load_dwordx4 v[224:227], v242, s[28:31], s75 offen offset:80\n"
    "s_branch .Lf3e\n"
    ".Lf3b:\n"
    "buffer_load_dwordx3 v[204:206], v243, s[40:43], s75 offen offset:1536 nt\n"
    "buffer_load_dwordx3 v[208:210], v243, s[40:43], s75 offen offset:2304 nt\n"
    "buffer_load_dwordx3 v[212:214], v243, s[40:43], s75 offen offset:0 nt\n"
    "buffer_load_dwordx3 v[216:218], v243, s[40:43], s75 offen offset:768 nt\n"
    "buffer_load_dwordx3 v[220:222], v242, s[40:43], s75 offen offset:1536 nt\n"
    "buffer_load_dwordx3 v[224:226], v242, s[40:43], s75 offen offset:2304 nt\n"
    "buffer_load_dwordx3 v[228:230], v242, s[40:43], s75 offen offset:0 nt\n"
    "buffer_load_dwordx3 v[232:234], v242, s[40:43], s75 offen offset:768 nt\n"
    ".Lf3e:\n"
    "s_nop 4\n"
    "v_add_f32 v4, v4, v100\n"
    "v_add_f32 v5, v5, v101\n"
    "v_add_f32 v6, v6, v102\n"
    "v_add_f32 v7, v7, v103\n"
    "v_add_f32 v20, v20, v104\n"
    "v_add_f32 v21, v21, v105\n"
    "v_add_f32 v22, v22, v106\n"
    "v_add_f32 v23, v23, v107\n"
    "v_mfma_f32_16x16x32_bf16 v[8:11], v[176:179], a[236:239], v[100:103]\n"
    "v_mfma_f32_16x16x32_bf16 v[8:11], v[172:175], a[232:235], v[8:11]\n"
    "v_exp_f32 v4, v4\n"
    "v_mfma_f32_16x16x32_bf16 v[8:11], v[168:171], a[228:231], v[8:11]\n"
    "v_exp_f32 v5, v5\n"
    "v_mfma_f32_16x16x32_bf16 v[8:11], v[164:167], a[224:227], v[8:11]\n"
    "v_mfma_f32_16x16x32_bf16 v[8:11], v[160:163], a[220:223], v[8:11]\n"
    "v_exp_f32 v6, v6\n"
    "v_mfma_f32_16x16x32_bf16 v[8:11], v[156:159], a[216:219], v[8:11]\n"
    "v_exp_f32 v7, v7\n"
    "v_mfma_f32_16x16x32_bf16 v[24:27], v[200:203], a[236:239], v[104:107]\n"
    "v_mfma_f32_16x16x32_bf16 v[24:27], v[196:199], a[232:235], v[24:27]\n"
    "v_exp_f32 v20, v20\n"
    "v_mfma_f32_16x16x32_bf16 v[24:27], v[192:195], a[228:231], v[24:27]\n"
    "v_exp_f32 v21, v21\n"
    "v_mfma_f32_16x16x32_bf16 v[24:27], v[188:191], a[224:227], v[24:27]\n"
    "v_mfma_f32_16x16x32_bf16 v[24:27], v[184:187], a[220:223], v[24:27]\n"
    "v_exp_f32 v22, v22\n"
    "v_mfma_f32_16x16x32_bf16 v[24:27], v[180:183], a[216:219], v[24:27]\n"
    "v_exp_f32 v23, v23\n"
    "s_waitcnt vmcnt(29)\n"
    "v_cvt_f32_i32 v124, v124\n"
    "v_cvt_f32_i32 v128, v128\n"
    "v_cvt_f32_i32 v125, v125\n"
    "v_cvt_f32_i32 v129, v129\n"
    "v_cvt_f32_i32 v126, v126\n"
    "v_cvt_f32_i32 v130, v130\n"
    "v_cvt_pk_bf16_f32 v127, v124, v128\n"
    "v_cvt_pk_bf16_f32 v131, v125, v129\n"
    "v_cvt_pk_bf16_f32 v135, v126, v130\n"
    "v_cvt_f32_i32 v132, v132\n"
    "v_cvt_f32_i32 v136, v136\n"
    "v_cvt_f32_i32 v133, v133\n"
    "v_cvt_f32_i32 v137, v137\n"
    "v_cvt_f32_i32 v134, v134\n"
    "v_cvt_f32_i32 v138, v138\n"
    "v_cvt_pk_bf16_f32 v126, v132, v136\n"
    "v_cvt_pk_bf16_f32 v130, v133, v137\n"
    "v_cvt_pk_bf16_f32 v134, v134, v138\n"
    "v_cvt_f32_i32 v140, v140\n"
    "v_cvt_f32_i32 v144, v144\n"
    "v_cvt_f32_i32 v141, v141\n"
    "v_cvt_f32_i32 v145, v145\n"
    "v_cvt_f32_i32 v142, v142\n"
    "v_cvt_f32_i32 v146, v146\n"
    "v_cvt_pk_bf16_f32 v125, v140, v144\n"
    "v_cvt_pk_bf16_f32 v129, v141, v145\n"
    "v_cvt_pk_bf16_f32 v133, v142, v146\n"
    "v_cvt_f32_i32 v148, v148\n"
    "v_cvt_f32_i32 v152, v152\n"
    "v_cvt_f32_i32 v149, v149\n"
    "v_cvt_f32_i32 v153, v153\n"
    "v_cvt_f32_i32 v150, v150\n"
    "v_cvt_f32_i32 v154, v154\n"
    "v_cvt_pk_bf16_f32 v124, v148, v152\n"
    "v_cvt_pk_bf16_f32 v128, v149, v153\n"
    "v_cvt_pk_bf16_f32 v132, v150, v154\n"
    "ds_write_b128 v237, v[124:127] offset:0\n"
    "ds_write_b128 v237, v[128:131] offset:4096\n"
    "ds_write_b128 v237, v[132:135] offset:8192\n"
    "s_waitcnt vmcnt(26)\n"
    "buffer_store_dwordx2 v[238:239], v240, s[36:39], s80 offen offset:0 sc1\n"
    "s_add_u32 s80, s80, 256\n"
    "s_waitcnt vmcnt(19)\n"
    "v_cvt_f32_i32 v36, v36\n"
    "v_cvt_f32_i32 v40, v40\n"
    "v_cvt_f32_i32 v37, v37\n"
    "v_cvt_f32_i32 v41, v41\n"
    "v_cvt_f32_i32 v38, v38\n"
    "v_cvt_f32_i32 v42, v42\n"
    "v_cvt_pk_bf16_f32 v39, v36, v40\n"
    "v_cvt_pk_bf16_f32 v43, v37, v41\n"
    "v_cvt_pk_bf16_f32 v47, v38, v42\n"
    "v_cvt_f32_i32 v44, v44\n"
    "v_cvt_f32_i32 v48, v48\n"
    "v_cvt_f32_i32 v45, v45\n"
    "v_cvt_f32_i32 v49, v49\n"
    "v_cvt_f32_i32 v46, v46\n"
    "v_cvt_f32_i32 v50, v50\n"
    "v_cvt_pk_bf16_f32 v38, v44, v48\n"
    "v_cvt_pk_bf16_f32 v42, v45, v49\n"
    "v_cvt_pk_bf16_f32 v46, v46, v50\n"
    "v_cvt_f32_i32 v52, v52\n"
    "v_cvt_f32_i32 v56, v56\n"
    "v_cvt_f32_i32 v53, v53\n"
    "v_cvt_f32_i32 v57, v57\n"
    "v_cvt_f32_i32 v54, v54\n"
    "v_cvt_f32_i32 v58, v58\n"
    "v_cvt_pk_bf16_f32 v37, v52, v56\n"
    "v_cvt_pk_bf16_f32 v41, v53, v57\n"
    "v_cvt_pk_bf16_f32 v45, v54, v58\n"
    "v_cvt_f32_i32 v60, v60\n"
    "v_cvt_f32_i32 v64, v64\n"
    "v_cvt_f32_i32 v61, v61\n"
    "v_cvt_f32_i32 v65, v65\n"
    "v_cvt_f32_i32 v62, v62\n"
    "v_cvt_f32_i32 v66, v66\n"
    "v_cvt_pk_bf16_f32 v36, v60, v64\n"
    "v_cvt_pk_bf16_f32 v40, v61, v65\n"
    "v_cvt_pk_bf16_f32 v44, v62, v66\n"
    "ds_write_b128 v237, v[36:39] offset:256\n"
    "ds_write_b128 v237, v[40:43] offset:4352\n"
    "ds_write_b128 v237, v[44:47] offset:8448\n"
    "s_waitcnt vmcnt(13)\n"
    "ds_read_b128 a[240:243], v253 offset:0\n"
    "ds_read_b128 a[244:247], v87 offset:0\n"
    "ds_read_b128 a[248:251], v253 offset:128\n"
    "ds_read_b128 a[252:255], v87 offset:128\n"
    "ds_read_b128 v[124:127], v253 offset:256\n"
    "ds_read_b128 v[128:131], v87 offset:256\n"
    "s_waitcnt lgkmcnt(0)\n"
    "v_mfma_f32_16x16x32_bf16 v[12:15], v[156:159], a[240:243], v[100:103]\n"
    "v_mfma_f32_16x16x32_bf16 v[12:15], v[160:163], a[244:247], v[12:15]\n"
    "v_exp_f32 v8, v8\n"
    "v_mfma_f32_16x16x32_bf16 v[12:15], v[164:167], a[248:251], v[12:15]\n"
    "v_exp_f32 v9, v9\n"
    "v_mfma_f32_16x16x32_bf16 v[12:15], v[168:171], a[252:255], v[12:15]\n"
    "v_mfma_f32_16x16x32_bf16 v[12:15], v[172:175], v[124:127], v[12:15]\n"
    "v_exp_f32 v10, v10\n"
    "v_mfma_f32_16x16x32_bf16 v[12:15], v[176:179], v[128:131], v[12:15]\n"
    "v_exp_f32 v11, v11\n"
    "v_mfma_f32_16x16x32_bf16 v[28:31], v[180:183], a[240:243], v[104:107]\n"
    "v_mfma_f32_16x16x32_bf16 v[28:31], v[184:187], a[244:247], v[28:31]\n"
    "v_exp_f32 v24, v24\n"
    "v_mfma_f32_16x16x32_bf16 v[28:31], v[188:191], a[248:251], v[28:31]\n"
    "v_exp_f32 v25, v25\n"
    "v_mfma_f32_16x16x32_bf16 v[28:31], v[192:195], a[252:255], v[28:31]\n"
    "v_mfma_f32_16x16x32_bf16 v[28:31], v[196:199], v[124:127], v[28:31]\n"
    "v_exp_f32 v26, v26\n"
    "v_mfma_f32_16x16x32_bf16 v[28:31], v[200:203], v[128:131], v[28:31]\n"
    "v_exp_f32 v27, v27\n"
    "s_waitcnt vmcnt(7)\n"
    "ds_read_b128 v[132:135], v253 offset:49152\n"
    "ds_read_b128 v[136:139], v87 offset:49152\n"
    "ds_read_b128 v[140:143], v253 offset:49280\n"
    "ds_read_b128 v[144:147], v87 offset:49280\n"
    "ds_read_b128 v[148:151], v253 offset:49408\n"
    "ds_read_b128 v[152:155], v87 offset:49408\n"
    "s_waitcnt lgkmcnt(0)\n"
    "v_mfma_f32_16x16x32_bf16 v[16:19], v[176:179], v[152:155], v[100:103]\n"
    "v_mfma_f32_16x16x32_bf16 v[16:19], v[172:175], v[148:151], v[16:19]\n"
    "v_exp_f32 v12, v12\n"
    "v_mfma_f32_16x16x32_bf16 v[16:19], v[168:171], v[144:147], v[16:19]\n"
    "v_exp_f32 v13, v13\n"
    "v_mfma_f32_16x16x32_bf16 v[16:19], v[164:167], v[140:143], v[16:19]\n"
    "v_mfma_f32_16x16x32_bf16 v[16:19], v[160:163], v[136:139], v[16:19]\n"
    "v_exp_f32 v14, v14\n"
    "v_mfma_f32_16x16x32_bf16 v[16:19], v[156:159], v[132:135], v[16:19]\n"
    "v_exp_f32 v15, v15\n"
    "v_mfma_f32_16x16x32_bf16 v[32:35], v[200:203], v[152:155], v[104:107]\n"
    "v_mfma_f32_16x16x32_bf16 v[32:35], v[196:199], v[148:151], v[32:35]\n"
    "v_exp_f32 v28, v28\n"
    "v_mfma_f32_16x16x32_bf16 v[32:35], v[192:195], v[144:147], v[32:35]\n"
    "v_exp_f32 v29, v29\n"
    "v_mfma_f32_16x16x32_bf16 v[32:35], v[188:191], v[140:143], v[32:35]\n"
    "v_mfma_f32_16x16x32_bf16 v[32:35], v[184:187], v[136:139], v[32:35]\n"
    "v_exp_f32 v30, v30\n"
    "v_mfma_f32_16x16x32_bf16 v[32:35], v[180:183], v[132:135], v[32:35]\n"
    "v_exp_f32 v31, v31\n"
    "ds_read_b128 v[156:159], v2 offset:12288\n"
    "ds_read_b128 v[160:163], v2 offset:14336\n"
    "ds_read_b128 v[164:167], v2 offset:16384\n"
    "ds_read_b128 v[168:171], v2 offset:18432\n"
    "ds_read_b128 v[172:175], v2 offset:20480\n"
    "ds_read_b128 v[176:179], v2 offset:22528\n"
    "ds_read_b128 v[100:103], v77 offset:128\n"
    "s_bitcmp1_b32 s84, 1\n"
    "s_cbranch_scc1 .Lf6b\n"
    "s_bitcmp1_b32 s84, 0\n"
    "s_cbranch_scc1 .Lf4b\n"
    "s_waitcnt vmcnt(1)\n"
    "s_add_u32 s76, s71, s60\n"
    "s_add_u32 s77, s71, s61\n"
    "s_add_u32 s78, s71, s62\n"
    "ds_read_b128 v[36:39], v243 offset:0\n"
    "ds_read_b128 v[40:43], v243 offset:96\n"
    "ds_read_b128 v[44:47], v243 offset:16\n"
    "ds_read_b128 v[48:51], v243 offset:112\n"
    "v_cvt_f32_i32 v204, v204\n"
    "v_cvt_f32_i32 v205, v205\n"
    "v_cvt_f32_i32 v206, v206\n"
    "v_cvt_f32_i32 v207, v207\n"
    "v_cvt_f32_i32 v208, v208\n"
    "v_cvt_f32_i32 v209, v209\n"
    "v_cvt_f32_i32 v210, v210\n"
    "v_cvt_f32_i32 v211, v211\n"
    "s_waitcnt lgkmcnt(2)\n"
    "v_fma_f32 v204, v204, v36, v40\n"
    "v_fma_f32 v205, v205, v37, v41\n"
    "v_fma_f32 v206, v206, v38, v42\n"
    "v_fma_f32 v207, v207, v39, v43\n"
    "ds_read_b128 v[36:39], v243 offset:32\n"
    "ds_read_b128 v[40:43], v243 offset:128\n"
    "v_cvt_f32_i32 v212, v212\n"
    "v_cvt_f32_i32 v213, v213\n"
    "v_cvt_f32_i32 v214, v214\n"
    "v_cvt_f32_i32 v215, v215\n"
    "v_cvt_f32_i32 v216, v216\n"
    "v_cvt_f32_i32 v217, v217\n"
    "v_cvt_f32_i32 v218, v218\n"
    "v_cvt_f32_i32 v219, v219\n"
    "s_waitcnt lgkmcnt(2)\n"
    "v_fma_f32 v208, v208, v44, v48\n"
    "v_fma_f32 v209, v209, v45, v49\n"
    "v_fma_f32 v210, v210, v46, v50\n"
    "v_fma_f32 v211, v211, v47, v51\n"
    "ds_read_b128 v[44:47], v243 offset:48\n"
    "ds_read_b128 v[48:51], v243 offset:144\n"
    "v_cvt_pk_bf16_f32 v204, v204, v205\n"
    "v_cvt_pk_bf16_f32 v205, v206, v207\n"
    "v_cvt_pk_bf16_f32 v206, v208, v209\n"
    "v_cvt_pk_bf16_f32 v207, v210, v211\n"
    "s_waitcnt lgkmcnt(2)\n"
    "v_fma_f32 v212, v212, v36, v40\n"
    "v_fma_f32 v213, v213, v37, v41\n"
    "v_fma_f32 v214, v214, v38, v42\n"
    "v_fma_f32 v215, v215, v39, v43\n"
    "ds_read_b128 v[36:39], v243 offset:64\n"
    "ds_read_b128 v[40:43], v243 offset:160\n"
    "v_cvt_f32_i32 v220, v220\n"
    "v_cvt_f32_i32 v221, v221\n"
    "v_cvt_f32_i32 v222, v222\n"
    "v_cvt_f32_i32 v223, v223\n"
    "v_cvt_f32_i32 v224, v224\n"
    "v_cvt_f32_i32 v225, v225\n"
    "v_cvt_f32_i32 v226, v226\n"
    "v_cvt_f32_i32 v227, v227\n"
    "s_waitcnt lgkmcnt(2)\n"
    "v_fma_f32 v216, v216, v44, v48\n"
    "v_fma_f32 v217, v217, v45, v49\n"
    "v_fma_f32 v218, v218, v46, v50\n"
    "v_fma_f32 v219, v219, v47, v51\n"
    "ds_read_b128 v[44:47], v243 offset:80\n"
    "ds_read_b128 v[48:51], v243 offset:176\n"
    "v_cvt_pk_bf16_f32 v212, v212, v213\n"
    "v_cvt_pk_bf16_f32 v213, v214, v215\n"
    "v_cvt_pk_bf16_f32 v214, v216, v217\n"
    "v_cvt_pk_bf16_f32 v215, v218, v219\n"
    "s_waitcnt lgkmcnt(2)\n"
    "v_fma_f32 v220, v220, v36, v40\n"
    "v_fma_f32 v221, v221, v37, v41\n"
    "v_fma_f32 v222, v222, v38, v42\n"
    "v_fma_f32 v223, v223, v39, v43\n"
    "s_waitcnt lgkmcnt(0)\n"
    "v_fma_f32 v224, v224, v44, v48\n"
    "v_fma_f32 v225, v225, v45, v49\n"
    "v_fma_f32 v226, v226, v46, v50\n"
    "v_fma_f32 v227, v227, v47, v51\n"
    "v_cvt_pk_bf16_f32 v220, v220, v221\n"
    "v_cvt_pk_bf16_f32 v221, v222, v223\n"
    "v_cvt_pk_bf16_f32 v222, v224, v225\n"
    "v_cvt_pk_bf16_f32 v223, v226, v227\n"
    "buffer_load_dwordx2 v[236:237], v241, s[36:39], s81 offen offset:0 sc1\n"
    "buffer_store_dwordx4 v[204:207], v244, s[16:19], s76 offen offset:0 sc1\n"
    "buffer_store_dwordx4 v[212:215], v244, s[16:19], s77 offen offset:0 sc1\n"
    "buffer_store_dwordx4 v[220:223], v244, s[16:19], s78 offen offset:0 sc1\n"
    "v_add_u32 v243, 192, v243\n"
    "v_exp_f32 v16, v16\n"
    "v_exp_f32 v17, v17\n"
    "v_exp_f32 v18, v18\n"
    "v_exp_f32 v19, v19\n"
    "v_exp_f32 v32, v32\n"
    "v_exp_f32 v33, v33\n"
    "v_exp_f32 v34, v34\n"
    "v_exp_f32 v35, v35\n"
    "s_waitcnt lgkmcnt(0)\n"
    "ds_add_u32 v247, v246\n"
    "s_nop 7\n"
    "s_branch .LPK\n"
    "s_branch .Lf4e\n"
    ".Lf4b:\n"
    "s_waitcnt vmcnt(1)\n"
    "s_add_u32 s76, s71, s60\n"
    "s_add_u32 s77, s71, s61\n"
    "s_add_u32 s78, s71, s62\n"
    "v_cvt_f32_i32 v204, v204\n"
    "v_cvt_f32_i32 v208, v208\n"
    "v_cvt_f32_i32 v205, v205\n"
    "v_cvt_f32_i32 v209, v209\n"
    "v_cvt_f32_i32 v206, v206\n"
    "v_cvt_f32_i32 v210, v210\n"
    "v_cvt_pk_bf16_f32 v207, v204, v208\n"
    "v_cvt_pk_bf16_f32 v211, v205, v209\n"
    "v_cvt_pk_bf16_f32 v215, v206, v210\n"
    "v_cvt_f32_i32 v212, v212\n"
    "v_cvt_f32_i32 v216, v216\n"
    "v_cvt_f32_i32 v213, v213\n"
    "v_cvt_f32_i32 v217, v217\n"
    "v_cvt_f32_i32 v214, v214\n"
    "v_cvt_f32_i32 v218, v218\n"
    "v_cvt_pk_bf16_f32 v206, v212, v216\n"
    "v_cvt_pk_bf16_f32 v210, v213, v217\n"
    "v_cvt_pk_bf16_f32 v214, v214, v218\n"
    "v_cvt_f32_i32 v220, v220\n"
    "v_cvt_f32_i32 v224, v224\n"
    "v_cvt_f32_i32 v221, v221\n"
    "v_cvt_f32_i32 v225, v225\n"
    "v_cvt_f32_i32 v222, v222\n"
    "v_cvt_f32_i32 v226, v226\n"
    "v_cvt_pk_bf16_f32 v205, v220, v224\n"
    "v_cvt_pk_bf16_f32 v209, v221, v225\n"
    "v_cvt_pk_bf16_f32 v213, v222, v226\n"
    "v_cvt_f32_i32 v228, v228\n"
    "v_cvt_f32_i32 v232, v232\n"
    "v_cvt_f32_i32 v229, v229\n"
    "v_cvt_f32_i32 v233, v233\n"
    "v_cvt_f32_i32 v230, v230\n"
    "v_cvt_f32_i32 v234, v234\n"
    "v_cvt_pk_bf16_f32 v204, v228, v232\n"
    "v_cvt_pk_bf16_f32 v208, v229, v233\n"
    "v_cvt_pk_bf16_f32 v212, v230, v234\n"
    "buffer_load_dwordx2 v[236:237], v241, s[36:39], s81 offen offset:0 sc1\n"
    "buffer_store_dwordx4 v[204:207], v244, s[16:19], s76 offen offset:0 sc1\n"
    "buffer_store_dwordx4 v[208:211], v244, s[16:19], s77 offen offset:0 sc1\n"
    "buffer_store_dwordx4 v[212:215], v244, s[16:19], s78 offen offset:0 sc1\n"
    "v_exp_f32 v16, v16\n"
    "v_exp_f32 v17, v17\n"
    "v_exp_f32 v18, v18\n"
    "v_exp_f32 v19, v19\n"
    "v_exp_f32 v32, v32\n"
    "v_exp_f32 v33, v33\n"
    "v_exp_f32 v34, v34\n"
    "v_exp_f32 v35, v35\n"
    "s_waitcnt lgkmcnt(0)\n"
    "ds_add_u32 v247, v246\n"
    "s_nop 7\n"
    "s_branch .LPV\n"
    ".Lf4e:\n"
    "s_nop 4\n"
    "s_branch .Lf6e\n"
    ".Lf6b:\n"
    "buffer_load_dwordx2 v[236:237], v241, s[36:39], s81 offen offset:0 sc1\n"
    "s_bitcmp1_b32 s84, 0\n"
    "s_cbranch_scc1 .Lf5b\n"
    "v_exp_f32 v16, v16\n"
    "v_exp_f32 v17, v17\n"
    "v_exp_f32 v18, v18\n"
    "v_exp_f32 v19, v19\n"
    "v_exp_f32 v32, v32\n"
    "v_exp_f32 v33, v33\n"
    "v_exp_f32 v34, v34\n"
    "v_exp_f32 v35, v35\n"
    "s_waitcnt lgkmcnt(0)\n"
    "ds_add_u32 v247, v246\n"
    "s_nop 7\n"
    "s_branch .LNK\n"
    "s_branch .Lf5e\n"
    ".Lf5b:\n"
    "v_exp_f32 v16, v16\n"
    "v_exp_f32 v17, v17\n"
    "v_exp_f32 v18, v18\n"
    "v_exp_f32 v19, v19\n"
    "v_exp_f32 v32, v32\n"
    "v_exp_f32 v33, v33\n"
    "v_exp_f32 v34, v34\n"
    "v_exp_f32 v35, v35\n"
    "s_waitcnt lgkmcnt(0)\n"
    "ds_add_u32 v247, v246\n"
    "s_nop 7\n"
    "s_branch .LNV\n"
    ".Lf5e:\n"
    "s_nop 4\n"
    ".Lf6e:\n"
    "s_nop 4\n"
    ".p2align 6\n"
    ".LPK:\n"
    "v_mfma_f32_16x16x32_bf16 v[36:39], v[156:159], a[192:195], v[100:103]\n"
    "s_add_u32 s71, s71, 98304\n"
    "ds_read_b128 v[180:183], v2 offset:13312\n"
    "v_cvt_pk_bf16_f32 v68, v4, v5\n"
    "v_mfma_f32_16x16x32_bf16 v[36:39], v[160:163], a[196:199], v[36:39]\n"
    "s_add_u32 s75, s71, s63\n"
    "ds_read_b128 v[184:187], v2 offset:15360\n"
    "v_mfma_f32_16x16x32_bf16 v[36:39], v[164:167], a[200:203], v[36:39]\n"
    "ds_read_b128 v[188:191], v2 offset:17408\n"
    "v_mfma_f32_16x16x32_bf16 v[36:39], v[168:171], a[204:207], v[36:39]\n"
    "ds_read_b128 v[192:195], v2 offset:19456\n"
    "v_cvt_pk_bf16_f32 v69, v6, v7\n"
    "v_mfma_f32_16x16x32_bf16 v[36:39], v[172:175], a[208:211], v[36:39]\n"
    "ds_read_b128 v[196:199], v2 offset:21504\n"
    "v_mfma_f32_16x16x32_bf16 v[36:39], v[176:179], a[212:215], v[36:39]\n"
    "ds_read_b128 v[200:203], v2 offset:23552\n"
    "v_mfma_f32_16x16x32_bf16 v[40:43], v[176:179], a[236:239], v[100:103]\n"
    "ds_read_b128 v[104:107], v250 offset:144\n"
    "v_mfma_f32_16x16x32_bf16 v[40:43], v[172:175], a[232:235], v[40:43]\n"
    "s_add_u32 s68, s68, 24576\n"
    "v_cvt_pk_bf16_f32 v72, v8, v9\n"
    "v_mfma_f32_16x16x32_bf16 v[40:43], v[168:171], a[228:231], v[40:43]\n"
    "s_cmp_ge_u32 s68, s69\n"
    "s_cselect_b32 s68, s65, s68\n"
    "v_mfma_f32_16x16x32_bf16 v[40:43], v[164:167], a[224:227], v[40:43]\n"
    "v_exp_f32 v36, v36\n"
    "v_mfma_f32_16x16x32_bf16 v[40:43], v[160:163], a[220:223], v[40:43]\n"
    "v_exp_f32 v37, v37\n"
    "v_mfma_f32_16x16x32_bf16 v[40:43], v[156:159], a[216:219], v[40:43]\n"
    "v_cvt_pk_bf16_f32 v73, v10, v11\n"
    "v_cvt_pk_bf16_f32 v84, v36, v37\n"
    "v_mfma_f32_16x16x32_bf16 v[44:47], v[156:159], a[240:243], v[100:103]\n"
    "v_mfma_f32_16x16x32_bf16 v[44:47], v[160:163], a[244:247], v[44:47]\n"
    "v_exp_f32 v38, v38\n"
    "v_mfma_f32_16x16x32_bf16 v[44:47], v[164:167], a[248:251], v[44:47]\n"
    "v_mfma_f32_16x16x32_bf16 v[44:47], v[168:171], a[252:255], v[44:47]\n"
    "v_cvt_pk_bf16_f32 v76, v12, v13\n"
    "v_mfma_f32_16x16x32_bf16 v[44:47], v[172:175], v[124:127], v[44:47]\n"
    "v_exp_f32 v39, v39\n"
    "v_mfma_f32_16x16x32_bf16 v[44:47], v[176:179], v[128:131], v[44:47]\n"
    "v_cvt_pk_bf16_f32 v85, v38, v39\n"
    "v_mfma_f32_16x16x32_bf16 v[48:51], v[176:179], v[152:155], v[100:103]\n"
    "ds_read_b32 v245, v247\n"
    "v_cvt_pk_bf16_f32 v77, v14, v15\n"
    "v_mfma_f32_16x16x32_bf16 v[48:51], v[172:175], v[148:151], v[48:51]\n"
    "s_add_u32 m0, s68, 0\n"
    "v_exp_f32 v40, v40\n"
    "v_mfma_f32_16x16x32_bf16 v[48:51], v[168:171], v[144:147], v[48:51]\n"
    "v_mfma_f32_16x16x32_bf16 v[48:51], v[164:167], v[140:143], v[48:51]\n"
    "v_mfma_f32_16x16x32_bf16 v[48:51], v[160:163], v[136:139], v[48:51]\n"
    "s_waitcnt vmcnt(3)\n"
    "v_cmp_eq_u64 s[54:55], v[236:237], v[238:239]\n"
    "v_cvt_pk_bf16_f32 v80, v16, v17\n"
    "v_mfma_f32_16x16x32_bf16 v[48:51], v[156:159], v[132:135], v[48:51]\n"
    "s_waitcnt lgkmcnt(1)\n"
    "v_exp_f32 v41, v41\n"
    "v_mfma_f32_16x16x32_bf16 v[52:55], v[180:183], a[192:195], v[104:107]\n"
    "s_waitcnt lgkmcnt(0)\n"
    "v_cmp_le_u32 s[56:57], s59, v245\n"
    "v_cvt_pk_bf16_f32 v88, v40, v41\n"
    "v_mfma_f32_16x16x32_bf16 v[52:55], v[184:187], a[196:199], v[52:55]\n"
    "v_exp_f32 v42, v42\n"
    "v_mfma_f32_16x16x32_bf16 v[52:55], v[188:191], a[200:203], v[52:55]\n"
    "s_and_b64 s[54:55], s[54:55], s[56:57]\n"
    "s_cmp_eq_u64 s[54:55], -1\n"
    "v_mfma_f32_16x16x32_bf16 v[52:55], v[192:195], a[204:207], v[52:55]\n"
    "s_cbranch_scc0 .Lpoll1\n"
    ".Lpoll1r:\n"
    "v_mfma_f32_16x16x32_bf16 v[52:55], v[196:199], a[208:211], v[52:55]\n"
    "buffer_load_dwordx4 v1, s[16:19], s67 offen offset:0 sc1 lds\n"
    "v_mfma_f32_16x16x32_bf16 v[52:55], v[200:203], a[212:215], v[52:55]\n"
    "ds_read_b128 v[156:159], v3 offset:0\n"
    "ds_read_b128 v[160:163], v3 offset:2048\n"
    "v_mfma_f32_16x16x32_bf16 v[56:59], v[200:203], a[236:239], v[104:107]\n"
    "ds_read_b128 v[164:167], v3 offset:4096\n"
    "ds_read_b128 v[168:171], v3 offset:6144\n"
    "v_mfma_f32_16x16x32_bf16 v[56:59], v[196:199], a[232:235], v[56:59]\n"
    "ds_read_b128 v[172:175], v3 offset:8192\n"
    "ds_read_b128 v[176:179], v3 offset:10240\n"
    "v_mfma_f32_16x16x32_bf16 v[56:59], v[192:195], a[228:231], v[56:59]\n"
    "buffer_load_dwordx4 v1, s[16:19], s67 offen offset:1024 sc1 lds\n"
    "v_mfma_f32_16x16x32_bf16 v[56:59], v[188:191], a[224:227], v[56:59]\n"
    "ds_read_b128 v[100:103], v250 offset:768\n"
    "v_cvt_pk_bf16_f32 v81, v18, v19\n"
    "v_mfma_f32_16x16x32_bf16 v[56:59], v[184:187], a[220:223], v[56:59]\n"
    "v_cvt_pk_bf16_f32 v70, v20, v21\n"
    "v_cvt_pk_bf16_f32 v71, v22, v23\n"
    "v_mfma_f32_16x16x32_bf16 v[56:59], v[180:183], a[216:219], v[56:59]\n"
    "v_exp_f32 v43, v43\n"
    "v_mfma_f32_16x16x32_bf16 v[60:63], v[180:183], a[240:243], v[104:107]\n"
    "buffer_load_dwordx4 v1, s[16:19], s67 offen offset:2048 sc1 lds\n"
    "v_mfma_f32_16x16x32_bf16 v[60:63], v[184:187], a[244:247], v[60:63]\n"
    "v_cvt_pk_bf16_f32 v74, v24, v25\n"
    "v_cvt_pk_bf16_f32 v89, v42, v43\n"
    "v_mfma_f32_16x16x32_bf16 v[60:63], v[188:191], a[248:251], v[60:63]\n"
    "v_exp_f32 v44, v44\n"
    "v_mfma_f32_16x16x32_bf16 v[60:63], v[192:195], a[252:255], v[60:63]\n"
    "v_exp_f32 v45, v45\n"
    "v_mfma_f32_16x16x32_bf16 v[60:63], v[196:199], v[124:127], v[60:63]\n"
    "buffer_load_dwordx4 v1, s[16:19], s67 offen offset:3072 sc1 lds\n"
    "v_mfma_f32_16x16x32_bf16 v[60:63], v[200:203], v[128:131], v[60:63]\n"
    "s_add_u32 m0, s68, 4096\n"
    "v_cvt_pk_bf16_f32 v75, v26, v27\n"
    "v_cvt_pk_bf16_f32 v92, v44, v45\n"
    "v_mfma_f32_16x16x32_bf16 v[64:67], v[200:203], v[152:155], v[104:107]\n"
    "s_add_u32 s83, s67, 4096\n"
    "v_exp_f32 v46, v46\n"
    "v_mfma_f32_16x16x32_bf16 v[64:67], v[196:199], v[148:151], v[64:67]\n"
    "v_exp_f32 v47, v47\n"
    "v_mfma_f32_16x16x32_bf16 v[64:67], v[192:195], v[144:147], v[64:67]\n"
    "buffer_load_dwordx4 v1, s[16:19], s83 offen offset:0 sc1 lds\n"
    "v_mfma_f32_16x16x32_bf16 v[64:67], v[188:191], v[140:143], v[64:67]\n"
    "v_cvt_pk_bf16_f32 v78, v28, v29\n"
    "v_cvt_pk_bf16_f32 v93, v46, v47\n"
    "v_mfma_f32_16x16x32_bf16 v[64:67], v[184:187], v[136:139], v[64:67]\n"
    "v_exp_f32 v48, v48\n"
    "v_mfma_f32_16x16x32_bf16 v[64:67], v[180:183], v[132:135], v[64:67]\n"
    "s_waitcnt lgkmcnt(0)\n"
    "v_exp_f32 v49, v49\n"
    "v_mfma_f32_16x16x32_bf16 v[4:7], v[156:159], a[192:195], v[100:103]\n"
    "buffer_load_dwordx4 v1, s[16:19], s83 offen offset:1024 sc1 lds\n"
    "v_mfma_f32_16x16x32_bf16 v[4:7], v[160:163], a[196:199], v[4:7]\n"
    "s_add_u32 m0, s64, 0\n"
    "ds_read_b128 v[180:183], v3 offset:1024\n"
    "ds_read_b128 v[184:187], v3 offset:3072\n"
    "v_mfma_f32_16x16x32_bf16 v[4:7], v[164:167], a[200:203], v[4:7]\n"
    "s_add_u32 s83, s67, 24576\n"
    "ds_read_b128 v[188:191], v3 offset:5120\n"
    "v_cvt_pk_bf16_f32 v79, v30, v31\n"
    "v_mfma_f32_16x16x32_bf16 v[4:7], v[168:171], a[204:207], v[4:7]\n"
    "ds_read_b128 v[192:195], v3 offset:7168\n"
    "v_cvt_pk_bf16_f32 v96, v48, v49\n"
    "v_mfma_f32_16x16x32_bf16 v[4:7], v[172:175], a[208:211], v[4:7]\n"
    "buffer_load_dwordx4 v1, s[16:19], s83 offen offset:0 sc1 lds\n"
    "v_mfma_f32_16x16x32_bf16 v[4:7], v[176:179], a[212:215], v[4:7]\n"
    "ds_read_b128 v[196:199], v3 offset:9216\n"
    "ds_read_b128 v[200:203], v3 offset:11264\n"
    "v_mfma_f32_16x16x32_bf16 v[8:11], v[176:179], a[236:239], v[100:103]\n"
    "ds_read_b128 v[104:107], v250 offset:784\n"
    "v_cvt_pk_bf16_f32 v82, v32, v33\n"
    "v_mfma_f32_16x16x32_bf16 v[8:11], v[172:175], a[232:235], v[8:11]\n"
    "v_exp_f32 v52, v52\n"
    "v_mfma_f32_16x16x32_bf16 v[8:11], v[168:171], a[228:231], v[8:11]\n"
    "buffer_load_dwordx4 v1, s[16:19], s83 offen offset:1024 sc1 lds\n"
    "v_mfma_f32_16x16x32_bf16 v[8:11], v[164:167], a[224:227], v[8:11]\n"
    "v_cvt_pk_bf16_f32 v83, v34, v35\n"
    "v_mfma_f32_16x16x32_bf16 v[8:11], v[160:163], a[220:223], v[8:11]\n"
    "v_exp_f32 v53, v53\n"
    "v_mfma_f32_16x16x32_bf16 v[8:11], v[156:159], a[216:219], v[8:11]\n"
    "v_exp_f32 v54, v54\n"
    "v_mfma_f32_16x16x32_bf16 v[12:15], v[156:159], a[240:243], v[100:103]\n"
    "buffer_load_dwordx4 v1, s[16:19], s83 offen offset:2048 sc1 lds\n"
    "v_mfma_f32_16x16x32_bf16 v[12:15], v[160:163], a[244:247], v[12:15]\n"
    "v_cvt_pk_bf16_f32 v86, v52, v53\n"
    "v_mfma_f32_16x16x32_bf16 v[12:15], v[164:167], a[248:251], v[12:15]\n"
    "v_exp_f32 v55, v55\n"
    "v_mfma_f32_16x16x32_bf16 v[12:15], v[168:171], a[252:255], v[12:15]\n"
    "v_cvt_pk_bf16_f32 v87, v54, v55\n"
    "v_mfma_f32_16x16x32_bf16 v[12:15], v[172:175], v[124:127], v[12:15]\n"
    "buffer_load_dwordx4 v1, s[16:19], s83 offen offset:3072 sc1 lds\n"
    "v_mfma_f32_16x16x32_bf16 v[12:15], v[176:179], v[128:131], v[12:15]\n"
    "s_add_u32 m0, s64, 4096\n"
    "v_exp_f32 v56, v56\n"
    "v_mfma_f32_16x16x32_bf16 v[16:19], v[176:179], v[152:155], v[100:103]\n"
    "s_add_u32 s83, s67, 28672\n"
    "v_exp_f32 v50, v50\n"
    "v_mfma_f32_16x16x32_bf16 v[16:19], v[172:175], v[148:151], v[16:19]\n"
    "v_exp_f32 v51, v51\n"
    "v_mfma_f32_16x16x32_bf16 v[16:19], v[168:171], v[144:147], v[16:19]\n"
    "buffer_load_dwordx4 v1, s[16:19], s83 offen offset:0 sc1 lds\n"
    "v_mfma_f32_16x16x32_bf16 v[16:19], v[164:167], v[140:143], v[16:19]\n"
    "v_exp_f32 v57, v57\n"
    "v_mfma_f32_16x16x32_bf16 v[16:19], v[160:163], v[136:139], v[16:19]\n"
    "v_cvt_pk_bf16_f32 v90, v56, v57\n"
    "v_cvt_pk_bf16_f32 v97, v50, v51\n"
    "v_mfma_f32_16x16x32_bf16 v[16:19], v[156:159], v[132:135], v[16:19]\n"
    "s_waitcnt lgkmcnt(0)\n"
    "v_exp_f32 v58, v58\n"
    "v_mfma_f32_16x16x32_bf16 v[20:23], v[180:183], a[192:195], v[104:107]\n"
    "buffer_load_dwordx4 v1, s[16:19], s83 offen offset:1024 sc1 lds\n"
    "v_mfma_f32_16x16x32_bf16 v[20:23], v[184:187], a[196:199], v[20:23]\n"
    "s_add_u32 s67, s67, 49152\n"
    "ds_read_b128 v[156:159], v248 offset:0\n"
    "ds_read_b128 v[160:163], v248 offset:12288\n"
    "v_mfma_f32_16x16x32_bf16 v[20:23], v[188:191], a[200:203], v[20:23]\n"
    "s_add_u32 s59, s59, 4\n"
    "ds_read_b128 v[164:167], v248 offset:1024\n"
    "v_mfma_f32_16x16x32_bf16 v[20:23], v[192:195], a[204:207], v[20:23]\n"
    "s_add_u32 s81, s81, 128\n"
    "s_waitcnt vmcnt(12)\n"
    "ds_read_b128 v[168:171], v248 offset:13312\n"
    "v_mfma_f32_16x16x32_bf16 v[20:23], v[196:199], a[208:211], v[20:23]\n"
    "s_min_u32 s81, s81, 4096\n"
    "ds_read_b128 v[172:175], v248 offset:2048\n"
    "v_mfma_f32_16x16x32_bf16 v[20:23], v[200:203], a[212:215], v[20:23]\n"
    "buffer_store_dwordx2 v[238:239], v240, s[36:39], s80 offen offset:0 sc1\n"
    "v_mfma_f32_16x16x32_bf16 v[24:27], v[200:203], a[236:239], v[104:107]\n"
    "s_add_u32 s80, s80, 256\n"
    "ds_read_b128 v[176:179], v248 offset:14336\n"
    "ds_read_b128 v[100:103], v251 offset:0\n"
    "v_mfma_f32_16x16x32_bf16 v[24:27], v[196:199], a[232:235], v[24:27]\n"
    "v_exp_f32 v59, v59\n"
    "v_mfma_f32_16x16x32_bf16 v[24:27], v[192:195], a[228:231], v[24:27]\n"
    "v_cvt_pk_bf16_f32 v91, v58, v59\n"
    "v_mfma_f32_16x16x32_bf16 v[24:27], v[188:191], a[224:227], v[24:27]\n"
    "v_exp_f32 v60, v60\n"
    "v_mfma_f32_16x16x32_bf16 v[24:27], v[184:187], a[220:223], v[24:27]\n"
    "v_exp_f32 v61, v61\n"
    "v_mfma_f32_16x16x32_bf16 v[24:27], v[180:183], a[216:219], v[24:27]\n"
    "v_exp_f32 v62, v62\n"
    "v_mfma_f32_16x16x32_bf16 v[28:31], v[180:183], a[240:243], v[104:107]\n"
    "v_cvt_pk_bf16_f32 v94, v60, v61\n"
    "v_mfma_f32_16x16x32_bf16 v[28:31], v[184:187], a[244:247], v[28:31]\n"
    "v_exp_f32 v63, v63\n"
    "v_mfma_f32_16x16x32_bf16 v[28:31], v[188:191], a[248:251], v[28:31]\n"
    "v_cvt_pk_bf16_f32 v95, v62, v63\n"
    "v_mfma_f32_16x16x32_bf16 v[28:31], v[192:195], a[252:255], v[28:31]\n"
    "buffer_load_dwordx4 v[204:207], v242, s[28:31], s75 offen offset:0\n"
    "v_mfma_f32_16x16x32_bf16 v[28:31], v[196:199], v[124:127], v[28:31]\n"
    "v_exp_f32 v64, v64\n"
    "v_mfma_f32_16x16x32_bf16 v[28:31], v[200:203], v[128:131], v[28:31]\n"
    "v_mfma_f32_16x16x32_bf16 v[32:35], v[200:203], v[152:155], v[104:107]\n"
    "buffer_load_dwordx4 v[208:211], v242, s[28:31], s75 offen offset:16\n"
    "v_mfma_f32_16x16x32_bf16 v[32:35], v[196:199], v[148:151], v[32:35]\n"
    "v_exp_f32 v65, v65\n"
    "v_mfma_f32_16x16x32_bf16 v[32:35], v[192:195], v[144:147], v[32:35]\n"
    "v_cvt_pk_bf16_f32 v98, v64, v65\n"
    "v_mfma_f32_16x16x32_bf16 v[32:35], v[188:191], v[140:143], v[32:35]\n"
    "buffer_load_dwordx4 v[212:215], v242, s[28:31], s75 offen offset:32\n"
    "v_mfma_f32_16x16x32_bf16 v[32:35], v[184:187], v[136:139], v[32:35]\n"
    "v_exp_f32 v66, v66\n"
    "v_mfma_f32_16x16x32_bf16 v[32:35], v[180:183], v[132:135], v[32:35]\n"
    "s_waitcnt lgkmcnt(0)\n"
    "v_mfma_f32_16x16x32_bf16 a[0:3], v[156:159], v[68:71], a[0:3]\n"
    "ds_read_b128 v[180:183], v248 offset:3072\n"
    "v_mfma_f32_16x16x32_bf16 a[0:3], v[160:163], v[84:87], a[0:3]\n"
    "ds_read_b128 v[184:187], v248 offset:15360\n"
    "v_mfma_f32_16x16x32_bf16 a[16:19], v[168:171], v[84:87], a[16:19]\n"
    "ds_read_b128 v[188:191], v248 offset:4096\n"
    "v_mfma_f32_16x16x32_bf16 a[16:19], v[164:167], v[68:71], a[16:19]\n"
    "ds_read_b128 v[192:195], v248 offset:16384\n"
    "v_mfma_f32_16x16x32_bf16 a[32:35], v[172:175], v[68:71], a[32:35]\n"
    "ds_read_b128 v[196:199], v248 offset:5120\n"
    "v_mfma_f32_16x16x32_bf16 a[32:35], v[176:179], v[84:87], a[32:35]\n"
    "ds_read_b128 v[200:203], v248 offset:17408\n"
    "v_mfma_f32_16x16x32_bf16 a[36:39], v[176:179], v[88:91], a[36:39]\n"
    "ds_read_b128 v[104:107], v251 offset:64\n"
    "v_mfma_f32_16x16x32_bf16 a[36:39], v[172:175], v[72:75], a[36:39]\n"
    "v_exp_f32 v67, v67\n"
    "v_mfma_f32_16x16x32_bf16 a[20:23], v[164:167], v[72:75], a[20:23]\n"
    "v_cvt_pk_bf16_f32 v99, v66, v67\n"
    "v_mfma_f32_16x16x32_bf16 a[20:23], v[168:171], v[88:91], a[20:23]\n"
    "buffer_load_dwordx4 v[216:219], v242, s[28:31], s75 offen offset:48\n"
    "v_mfma_f32_16x16x32_bf16 a[4:7], v[160:163], v[88:91], a[4:7]\n"
    "v_exp_f32 v4, v4\n"
    "v_mfma_f32_16x16x32_bf16 a[4:7], v[156:159], v[72:75], a[4:7]\n"
    "v_exp_f32 v5, v5\n"
    "v_mfma_f32_16x16x32_bf16 a[8:11], v[156:159], v[76:79], a[8:11]\n"
    "buffer_load_dwordx4 v[220:223], v242, s[28:31], s75 offen offset:64\n"
    "v_mfma_f32_16x16x32_bf16 a[8:11], v[160:163], v[92:95], a[8:11]\n"
    "v_exp_f32 v6, v6\n"
    "v_mfma_f32_16x16x32_bf16 a[24:27], v[168:171], v[92:95], a[24:27]\n"
    "v_exp_f32 v7, v7\n"
    "v_mfma_f32_16x16x32_bf16 a[24:27], v[164:167], v[76:79], a[24:27]\n"
    "buffer_load_dwordx4 v[224:227], v242, s[28:31], s75 offen offset:80\n"
    "v_mfma_f32_16x16x32_bf16 a[40:43], v[172:175], v[76:79], a[40:43]\n"
    "v_exp_f32 v8, v8\n"
    "v_mfma_f32_16x16x32_bf16 a[40:43], v[176:179], v[92:95], a[40:43]\n"
    "v_mfma_f32_16x16x32_bf16 a[44:47], v[176:179], v[96:99], a[44:47]\n"
    "v_exp_f32 v9, v9\n"
    "v_mfma_f32_16x16x32_bf16 a[44:47], v[172:175], v[80:83], a[44:47]\n"
    "v_mfma_f32_16x16x32_bf16 a[28:31], v[164:167], v[80:83], a[28:31]\n"
    "v_exp_f32 v10, v10\n"
    "v_mfma_f32_16x16x32_bf16 a[28:31], v[168:171], v[96:99], a[28:31]\n"
    "v_mfma_f32_16x16x32_bf16 a[12:15], v[160:163], v[96:99], a[12:15]\n"
    "v_mfma_f32_16x16x32_bf16 a[12:15], v[156:159], v[80:83], a[12:15]\n"
    "s_waitcnt lgkmcnt(0)\n"
    "v_exp_f32 v11, v11\n"
    "v_mfma_f32_16x16x32_bf16 a[48:51], v[180:183], v[68:71], a[48:51]\n"
    "ds_read_b128 v[156:159], v248 offset:6144\n"
    "v_mfma_f32_16x16x32_bf16 a[48:51], v[184:187], v[84:87], a[48:51]\n"
    "ds_read_b128 v[160:163], v248 offset:18432\n"
    "v_mfma_f32_16x16x32_bf16 a[64:67], v[192:195], v[84:87], a[64:67]\n"
    "ds_read_b128 v[164:167], v248 offset:7168\n"
    "v_mfma_f32_16x16x32_bf16 a[64:67], v[188:191], v[68:71], a[64:67]\n"
    "ds_read_b128 v[168:171], v248 offset:19456\n"
    "v_mfma_f32_16x16x32_bf16 a[80:83], v[196:199], v[68:71], a[80:83]\n"
    "ds_read_b128 v[172:175], v248 offset:8192\n"
    "v_mfma_f32_16x16x32_bf16 a[80:83], v[200:203], v[84:87], a[80:83]\n"
    "ds_read_b128 v[176:179], v248 offset:20480\n"
    "v_mfma_f32_16x16x32_bf16 v[108:111], v[104:107], v[84:87], v[108:111]\n"
    "v_exp_f32 v12, v12\n"
    "v_mfma_f32_16x16x32_bf16 v[108:111], v[100:103], v[68:71], v[108:111]\n"
    "v_exp_f32 v13, v13\n"
    "v_mfma_f32_16x16x32_bf16 v[112:115], v[100:103], v[72:75], v[112:115]\n"
    "v_exp_f32 v14, v14\n"
    "v_mfma_f32_16x16x32_bf16 v[112:115], v[104:107], v[88:91], v[112:115]\n"
    "v_mfma_f32_16x16x32_bf16 a[84:87], v[200:203], v[88:91], a[84:87]\n"
    "v_exp_f32 v15, v15\n"
    "v_mfma_f32_16x16x32_bf16 a[84:87], v[196:199], v[72:75], a[84:87]\n"
    "v_mfma_f32_16x16x32_bf16 a[68:71], v[188:191], v[72:75], a[68:71]\n"
    "v_mfma_f32_16x16x32_bf16 a[68:71], v[192:195], v[88:91], a[68:71]\n"
    "v_exp_f32 v16, v16\n"
    "v_mfma_f32_16x16x32_bf16 a[52:55], v[184:187], v[88:91], a[52:55]\n"
    "v_mfma_f32_16x16x32_bf16 a[52:55], v[180:183], v[72:75], a[52:55]\n"
    "v_mfma_f32_16x16x32_bf16 a[56:59], v[180:183], v[76:79], a[56:59]\n"
    "v_exp_f32 v17, v17\n"
    "v_mfma_f32_16x16x32_bf16 a[56:59], v[184:187], v[92:95], a[56:59]\n"
    "v_mfma_f32_16x16x32_bf16 a[72:75], v[192:195], v[92:95], a[72:75]\n"
    "v_mfma_f32_16x16x32_bf16 a[72:75], v[188:191], v[76:79], a[72:75]\n"
    "v_exp_f32 v18, v18\n"
    "v_mfma_f32_16x16x32_bf16 a[88:91], v[196:199], v[76:79], a[88:91]\n"
    "v_mfma_f32_16x16x32_bf16 a[88:91], v[200:203], v[92:95], a[88:91]\n"
    "v_mfma_f32_16x16x32_bf16 v[116:119], v[104:107], v[92:95], v[116:119]\n"
    "v_exp_f32 v19, v19\n"
    "v_mfma_f32_16x16x32_bf16 v[116:119], v[100:103], v[76:79], v[116:119]\n"
    "v_mfma_f32_16x16x32_bf16 v[120:123], v[100:103], v[80:83], v[120:123]\n"
    "v_mfma_f32_16x16x32_bf16 v[120:123], v[104:107], v[96:99], v[120:123]\n"
    "v_exp_f32 v20, v20\n"
    "v_mfma_f32_16x16x32_bf16 a[92:95], v[200:203], v[96:99], a[92:95]\n"
    "v_mfma_f32_16x16x32_bf16 a[92:95], v[196:199], v[80:83], a[92:95]\n"
    "v_exp_f32 v21, v21\n"
    "v_mfma_f32_16x16x32_bf16 a[76:79], v[188:191], v[80:83], a[76:79]\n"
    "v_mfma_f32_16x16x32_bf16 a[76:79], v[192:195], v[96:99], a[76:79]\n"
    "v_mfma_f32_16x16x32_bf16 a[60:63], v[184:187], v[96:99], a[60:63]\n"
    "v_exp_f32 v22, v22\n"
    "v_mfma_f32_16x16x32_bf16 a[60:63], v[180:183], v[80:83], a[60:63]\n"
    "s_waitcnt lgkmcnt(0)\n"
    "v_mfma_f32_16x16x32_bf16 a[96:99], v[156:159], v[68:71], a[96:99]\n"
    "ds_read_b128 v[180:183], v248 offset:9216\n"
    "v_mfma_f32_16x16x32_bf16 a[96:99], v[160:163], v[84:87], a[96:99]\n"
    "ds_read_b128 v[184:187], v248 offset:21504\n"
    "v_mfma_f32_16x16x32_bf16 a[112:115], v[168:171], v[84:87], a[112:115]\n"
    "ds_read_b128 v[188:191], v248 offset:10240\n"
    "v_mfma_f32_16x16x32_bf16 a[112:115], v[164:167], v[68:71], a[112:115]\n"
    "ds_read_b128 v[192:195], v248 offset:22528\n"
    "v_mfma_f32_16x16x32_bf16 a[128:131], v[172:175], v[68:71], a[128:131]\n"
    "buffer_load_dwordx2 v[236:237], v241, s[36:39], s81 offen offset:0 sc1\n"
    "v_mfma_f32_16x16x32_bf16 a[128:131], v[176:179], v[84:87], a[128:131]\n"
    "ds_read_b128 v[196:199], v248 offset:11264\n"
    "ds_read_b128 v[200:203], v248 offset:23552\n"
    "v_mfma_f32_16x16x32_bf16 a[132:135], v[176:179], v[88:91], a[132:135]\n"
    "v_exp_f32 v23, v23\n"
    "v_mfma_f32_16x16x32_bf16 a[132:135], v[172:175], v[72:75], a[132:135]\n"
    "v_exp_f32 v24, v24\n"
    "v_mfma_f32_16x16x32_bf16 a[116:119], v[164:167], v[72:75], a[116:119]\n"
    "v_exp_f32 v25, v25\n"
    "v_mfma_f32_16x16x32_bf16 a[116:119], v[168:171], v[88:91], a[116:119]\n"
    "v_exp_f32 v26, v26\n"
    "v_mfma_f32_16x16x32_bf16 a[100:103], v[160:163], v[88:91], a[100:103]\n"
    "v_mfma_f32_16x16x32_bf16 a[100:103], v[156:159], v[72:75], a[100:103]\n"
    "v_mfma_f32_16x16x32_bf16 a[104:107], v[156:159], v[76:79], a[104:107]\n"
    "v_exp_f32 v27, v27\n"
    "v_mfma_f32_16x16x32_bf16 a[104:107], v[160:163], v[92:95], a[104:107]\n"
    "v_mfma_f32_16x16x32_bf16 a[120:123], v[168:171], v[92:95], a[120:123]\n"
    "v_mfma_f32_16x16x32_bf16 a[120:123], v[164:167], v[76:79], a[120:123]\n"
    "v_exp_f32 v28, v28\n"
    "v_mfma_f32_16x16x32_bf16 a[136:139], v[172:175], v[76:79], a[136:139]\n"
    "v_mfma_f32_16x16x32_bf16 a[136:139], v[176:179], v[92:95], a[136:139]\n"
    "v_mfma_f32_16x16x32_bf16 a[140:143], v[176:179], v[96:99], a[140:143]\n"
    "v_exp_f32 v29, v29\n"
    "v_mfma_f32_16x16x32_bf16 a[140:143], v[172:175], v[80:83], a[140:143]\n"
    "v_mfma_f32_16x16x32_bf16 a[124:127], v[164:167], v[80:83], a[124:127]\n"
    "v_mfma_f32_16x16x32_bf16 a[124:127], v[168:171], v[96:99], a[124:127]\n"
    "v_exp_f32 v30, v30\n"
    "v_mfma_f32_16x16x32_bf16 a[108:111], v[160:163], v[96:99], a[108:111]\n"
    "v_mfma_f32_16x16x32_bf16 a[108:111], v[156:159], v[80:83], a[108:111]\n"
    "s_waitcnt lgkmcnt(0)\n"
    "v_exp_f32 v31, v31\n"
    "v_mfma_f32_16x16x32_bf16 a[144:147], v[180:183], v[68:71], a[144:147]\n"
    "ds_read_b128 v[156:159], v3 offset:12288\n"
    "v_mfma_f32_16x16x32_bf16 a[144:147], v[184:187], v[84:87], a[144:147]\n"
    "ds_read_b128 v[160:163], v3 offset:14336\n"
    "v_mfma_f32_16x16x32_bf16 a[160:163], v[192:195], v[84:87], a[160:163]\n"
    "ds_read_b128 v[164:167], v3 offset:16384\n"
    "v_mfma_f32_16x16x32_bf16 a[160:163], v[188:191], v[68:71], a[160:163]\n"
    "ds_read_b128 v[168:171], v3 offset:18432\n"
    "v_mfma_f32_16x16x32_bf16 a[176:179], v[196:199], v[68:71], a[176:179]\n"
    "ds_read_b128 v[172:175], v3 offset:20480\n"
    "v_mfma_f32_16x16x32_bf16 a[176:179], v[200:203], v[84:87], a[176:179]\n"
    "ds_read_b128 v[176:179], v3 offset:22528\n"
    "v_mfma_f32_16x16x32_bf16 a[180:183], v[200:203], v[88:91], a[180:183]\n"
    "ds_read_b128 v[100:103], v250 offset:896\n"
    "v_mfma_f32_16x16x32_bf16 a[180:183], v[196:199], v[72:75], a[180:183]\n"
    "v_exp_f32 v32, v32\n"
    "v_mfma_f32_16x16x32_bf16 a[164:167], v[188:191], v[72:75], a[164:167]\n"
    "v_exp_f32 v33, v33\n"
    "v_mfma_f32_16x16x32_bf16 a[164:167], v[192:195], v[88:91], a[164:167]\n"
    "v_exp_f32 v34, v34\n"
    "v_mfma_f32_16x16x32_bf16 a[148:151], v[184:187], v[88:91], a[148:151]\n"
    "v_mfma_f32_16x16x32_bf16 a[148:151], v[180:183], v[72:75], a[148:151]\n"
    "v_exp_f32 v35, v35\n"
    "v_mfma_f32_16x16x32_bf16 a[152:155], v[180:183], v[76:79], a[152:155]\n"
    "v_mfma_f32_16x16x32_bf16 a[152:155], v[184:187], v[92:95], a[152:155]\n"
    "v_mfma_f32_16x16x32_bf16 a[168:171], v[192:195], v[92:95], a[168:171]\n"
    "s_waitcnt vmcnt(8)\n"
    "ds_add_u32 v247, v246\n"
    "v_mfma_f32_16x16x32_bf16 a[168:171], v[188:191], v[76:79], a[168:171]\n"
    "v_mfma_f32_16x16x32_bf16 a[184:187], v[196:199], v[76:79], a[184:187]\n"
    "v_mfma_f32_16x16x32_bf16 a[184:187], v[200:203], v[92:95], a[184:187]\n"
    "v_mfma_f32_16x16x32_bf16 a[188:191], v[200:203], v[96:99], a[188:191]\n"
    "v_mfma_f32_16x16x32_bf16 a[188:191], v[196:199], v[80:83], a[188:191]\n"
    "v_mfma_f32_16x16x32_bf16 a[172:175], v[188:191], v[80:83], a[172:175]\n"
    "s_sub_u32 s74, s68, s65\n"
    "v_mfma_f32_16x16x32_bf16 a[172:175], v[192:195], v[96:99], a[172:175]\n"
    "s_add_u32 s66, s66, 1\n"
    "v_mov_b32 v2, v3\n"
    "v_add_u32 v3, s74, v1\n"
    "v_mfma_f32_16x16x32_bf16 a[156:159], v[184:187], v[96:99], a[156:159]\n"
    "s_waitcnt lgkmcnt(1)\n"
    "s_cmp_eq_u32 s66, 31\n"
    "v_mfma_f32_16x16x32_bf16 a[156:159], v[180:183], v[80:83], a[156:159]\n"
    "s_cbranch_scc1 .LtailP\n"
    ".LNK:\n"
    "v_mfma_f32_16x16x32_bf16 v[36:39], v[156:159], a[192:195], v[100:103]\n"
    "ds_read_b128 v[180:183], v2 offset:13312\n"
    "v_cvt_pk_bf16_f32 v68, v4, v5\n"
    "v_mfma_f32_16x16x32_bf16 v[36:39], v[160:163], a[196:199], v[36:39]\n"
    "ds_read_b128 v[184:187], v2 offset:15360\n"
    "v_mfma_f32_16x16x32_bf16 v[36:39], v[164:167], a[200:203], v[36:39]\n"
    "ds_read_b128 v[188:191], v2 offset:17408\n"
    "v_mfma_f32_16x16x32_bf16 v[36:39], v[168:171], a[204:207], v[36:39]\n"
    "ds_read_b128 v[192:195], v2 offset:19456\n"
    "v_cvt_pk_bf16_f32 v69, v6, v7\n"
    "v_mfma_f32_16x16x32_bf16 v[36:39], v[172:175], a[208:211], v[36:39]\n"
    "ds_read_b128 v[196:199], v2 offset:21504\n"
    "v_mfma_f32_16x16x32_bf16 v[36:39], v[176:179], a[212:215], v[36:39]\n"
    "ds_read_b128 v[200:203], v2 offset:23552\n"
    "v_mfma_f32_16x16x32_bf16 v[40:43], v[176:179], a[236:239], v[100:103]\n"
    "ds_read_b128 v[104:107], v250 offset:912\n"
    "v_mfma_f32_16x16x32_bf16 v[40:43], v[172:175], a[232:235], v[40:43]\n"
    "s_add_u32 s68, s68, 24576\n"
    "v_cvt_pk_bf16_f32 v72, v8, v9\n"
    "v_mfma_f32_16x16x32_bf16 v[40:43], v[168:171], a[228:231], v[40:43]\n"
    "s_cmp_ge_u32 s68, s69\n"
    "s_cselect_b32 s68, s65, s68\n"
    "v_mfma_f32_16x16x32_bf16 v[40:43], v[164:167], a[224:227], v[40:43]\n"
    "v_exp_f32 v36, v36\n"
    "v_mfma_f32_16x16x32_bf16 v[40:43], v[160:163], a[220:223], v[40:43]\n"
    "v_exp_f32 v37, v37\n"
    "v_mfma_f32_16x16x32_bf16 v[40:43], v[156:159], a[216:219], v[40:43]\n"
    "v_cvt_pk_bf16_f32 v73, v10, v11\n"
    "v_cvt_pk_bf16_f32 v84, v36, v37\n"
    "v_mfma_f32_16x16x32_bf16 v[44:47], v[156:159], a[240:243], v[100:103]\n"
    "v_mfma_f32_16x16x32_bf16 v[44:47], v[160:163], a[244:247], v[44:47]\n"
    "v_exp_f32 v38, v38\n"
    "v_mfma_f32_16x16x32_bf16 v[44:47], v[164:167], a[248:251], v[44:47]\n"
    "v_mfma_f32_16x16x32_bf16 v[44:47], v[168:171], a[252:255], v[44:47]\n"
    "v_cvt_pk_bf16_f32 v76, v12, v13\n"
    "v_mfma_f32_16x16x32_bf16 v[44:47], v[172:175], v[124:127], v[44:47]\n"
    "v_exp_f32 v39, v39\n"
    "v_mfma_f32_16x16x32_bf16 v[44:47], v[176:179], v[128:131], v[44:47]\n"
    "v_cvt_pk_bf16_f32 v85, v38, v39\n"
    "v_mfma_f32_16x16x32_bf16 v[48:51], v[176:179], v[152:155], v[100:103]\n"
    "ds_read_b32 v245, v247\n"
    "v_cvt_pk_bf16_f32 v77, v14, v15\n"
    "v_mfma_f32_16x16x32_bf16 v[48:51], v[172:175], v[148:151], v[48:51]\n"
    "s_add_u32 m0, s68, 0\n"
    "v_exp_f32 v40, v40\n"
    "v_mfma_f32_16x16x32_bf16 v[48:51], v[168:171], v[144:147], v[48:51]\n"
    "v_mfma_f32_16x16x32_bf16 v[48:51], v[164:167], v[140:143], v[48:51]\n"
    "v_mfma_f32_16x16x32_bf16 v[48:51], v[160:163], v[136:139], v[48:51]\n"
    "s_waitcnt vmcnt(0)\n"
    "v_cmp_eq_u64 s[54:55], v[236:237], v[238:239]\n"
    "v_cvt_pk_bf16_f32 v80, v16, v17\n"
    "v_mfma_f32_16x16x32_bf16 v[48:51], v[156:159], v[132:135], v[48:51]\n"
    "s_waitcnt lgkmcnt(1)\n"
    "v_exp_f32 v41, v41\n"
    "v_mfma_f32_16x16x32_bf16 v[52:55], v[180:183], a[192:195], v[104:107]\n"
    "s_waitcnt lgkmcnt(0)\n"
    "v_cmp_le_u32 s[56:57], s59, v245\n"
    "v_cvt_pk_bf16_f32 v88, v40, v41\n"
    "v_mfma_f32_16x16x32_bf16 v[52:55], v[184:187], a[196:199], v[52:55]\n"
    "v_exp_f32 v42, v42\n"
    "v_mfma_f32_16x16x32_bf16 v[52:55], v[188:191], a[200:203], v[52:55]\n"
    "s_and_b64 s[54:55], s[54:55], s[56:57]\n"
    "s_cmp_eq_u64 s[54:55], -1\n"
    "v_mfma_f32_16x16x32_bf16 v[52:55], v[192:195], a[204:207], v[52:55]\n"
    "s_cbranch_scc0 .Lpoll2\n"
    ".Lpoll2r:\n"
    "v_mfma_f32_16x16x32_bf16 v[52:55], v[196:199], a[208:211], v[52:55]\n"
    "buffer_load_dwordx4 v1, s[16:19], s67 offen offset:0 sc1 lds\n"
    "v_mfma_f32_16x16x32_bf16 v[52:55], v[200:203], a[212:215], v[52:55]\n"
    "ds_read_b128 v[156:159], v3 offset:0\n"
    "ds_read_b128 v[160:163], v3 offset:2048\n"
    "v_mfma_f32_16x16x32_bf16 v[56:59], v[200:203], a[236:239], v[104:107]\n"
    "ds_read_b128 v[164:167], v3 offset:4096\n"
    "ds_read_b128 v[168:171], v3 offset:6144\n"
    "v_mfma_f32_16x16x32_bf16 v[56:59], v[196:199], a[232:235], v[56:59]\n"
    "ds_read_b128 v[172:175], v3 offset:8192\n"
    "ds_read_b128 v[176:179], v3 offset:10240\n"
    "v_mfma_f32_16x16x32_bf16 v[56:59], v[192:195], a[228:231], v[56:59]\n"
    "buffer_load_dwordx4 v1, s[16:19], s67 offen offset:1024 sc1 lds\n"
    "v_mfma_f32_16x16x32_bf16 v[56:59], v[188:191], a[224:227], v[56:59]\n"
    "ds_read_b128 v[100:103], v250 offset:1536\n"
    "v_cvt_pk_bf16_f32 v81, v18, v19\n"
    "v_mfma_f32_16x16x32_bf16 v[56:59], v[184:187], a[220:223], v[56:59]\n"
    "v_cvt_pk_bf16_f32 v70, v20, v21\n"
    "v_cvt_pk_bf16_f32 v71, v22, v23\n"
    "v_mfma_f32_16x16x32_bf16 v[56:59], v[180:183], a[216:219], v[56:59]\n"
    "v_exp_f32 v43, v43\n"
    "v_mfma_f32_16x16x32_bf16 v[60:63], v[180:183], a[240:243], v[104:107]\n"
    "buffer_load_dwordx4 v1, s[16:19], s67 offen offset:2048 sc1 lds\n"
    "v_mfma_f32_16x16x32_bf16 v[60:63], v[184:187], a[244:247], v[60:63]\n"
    "v_cvt_pk_bf16_f32 v74, v24, v25\n"
    "v_cvt_pk_bf16_f32 v89, v42, v43\n"
    "v_mfma_f32_16x16x32_bf16 v[60:63], v[188:191], a[248:251], v[60:63]\n"
    "v_exp_f32 v44, v44\n"
    "v_mfma_f32_16x16x32_bf16 v[60:63], v[192:195], a[252:255], v[60:63]\n"
    "v_exp_f32 v45, v45\n"
    "v_mfma_f32_16x16x32_bf16 v[60:63], v[196:199], v[124:127], v[60:63]\n"
    "buffer_load_dwordx4 v1, s[16:19], s67 offen offset:3072 sc1 lds\n"
    "v_mfma_f32_16x16x32_bf16 v[60:63], v[200:203], v[128:131], v[60:63]\n"
    "s_add_u32 m0, s68, 4096\n"
    "v_cvt_pk_bf16_f32 v75, v26, v27\n"
    "v_cvt_pk_bf16_f32 v92, v44, v45\n"
    "v_mfma_f32_16x16x32_bf16 v[64:67], v[200:203], v[152:155], v[104:107]\n"
    "s_add_u32 s83, s67, 4096\n"
    "v_exp_f32 v46, v46\n"
    "v_mfma_f32_16x16x32_bf16 v[64:67], v[196:199], v[148:151], v[64:67]\n"
    "v_exp_f32 v47, v47\n"
    "v_mfma_f32_16x16x32_bf16 v[64:67], v[192:195], v[144:147], v[64:67]\n"
    "buffer_load_dwordx4 v1, s[16:19], s83 offen offset:0 sc1 lds\n"
    "v_mfma_f32_16x16x32_bf16 v[64:67], v[188:191], v[140:143], v[64:67]\n"
    "v_cvt_pk_bf16_f32 v78, v28, v29\n"
    "v_cvt_pk_bf16_f32 v93, v46, v47\n"
    "v_mfma_f32_16x16x32_bf16 v[64:67], v[184:187], v[136:139], v[64:67]\n"
    "v_exp_f32 v48, v48\n"
    "v_mfma_f32_16x16x32_bf16 v[64:67], v[180:183], v[132:135], v[64:67]\n"
    "s_waitcnt lgkmcnt(0)\n"
    "v_exp_f32 v49, v49\n"
    "v_mfma_f32_16x16x32_bf16 v[4:7], v[156:159], a[192:195], v[100:103]\n"
    "buffer_load_dwordx4 v1, s[16:19], s83 offen offset:1024 sc1 lds\n"
    "v_mfma_f32_16x16x32_bf16 v[4:7], v[160:163], a[196:199], v[4:7]\n"
    "s_add_u32 m0, s70, 0\n"
    "ds_read_b128 v[180:183], v3 offset:1024\n"
    "ds_read_b128 v[184:187], v3 offset:3072\n"
    "v_mfma_f32_16x16x32_bf16 v[4:7], v[164:167], a[200:203], v[4:7]\n"
    "s_add_u32 s83, s67, 24576\n"
    "ds_read_b128 v[188:191], v3 offset:5120\n"
    "v_cvt_pk_bf16_f32 v79, v30, v31\n"
    "v_mfma_f32_16x16x32_bf16 v[4:7], v[168:171], a[204:207], v[4:7]\n"
    "ds_read_b128 v[192:195], v3 offset:7168\n"
    "v_cvt_pk_bf16_f32 v96, v48, v49\n"
    "v_mfma_f32_16x16x32_bf16 v[4:7], v[172:175], a[208:211], v[4:7]\n"
    "buffer_load_dwordx4 v1, s[16:19], s83 offen offset:0 sc1 lds\n"
    "v_mfma_f32_16x16x32_bf16 v[4:7], v[176:179], a[212:215], v[4:7]\n"
    "s_add_u32 s76, s71, s60\n"
    "ds_read_b128 v[196:199], v3 offset:9216\n"
    "ds_read_b128 v[200:203], v3 offset:11264\n"
    "v_mfma_f32_16x16x32_bf16 v[8:11], v[176:179], a[236:239], v[100:103]\n"
    "s_add_u32 s77, s71, s61\n"
    "ds_read_b128 v[104:107], v250 offset:1552\n"
    "v_cvt_pk_bf16_f32 v82, v32, v33\n"
    "v_mfma_f32_16x16x32_bf16 v[8:11], v[172:175], a[232:235], v[8:11]\n"
    "s_add_u32 s78, s71, s62\n"
    "v_exp_f32 v52, v52\n"
    "v_mfma_f32_16x16x32_bf16 v[8:11], v[168:171], a[228:231], v[8:11]\n"
    "buffer_load_dwordx4 v1, s[16:19], s83 offen offset:1024 sc1 lds\n"
    "v_mfma_f32_16x16x32_bf16 v[8:11], v[164:167], a[224:227], v[8:11]\n"
    "ds_read_b128 v[228:231], v243 offset:0\n"
    "v_cvt_pk_bf16_f32 v83, v34, v35\n"
    "v_mfma_f32_16x16x32_bf16 v[8:11], v[160:163], a[220:223], v[8:11]\n"
    "v_exp_f32 v53, v53\n"
    "v_mfma_f32_16x16x32_bf16 v[8:11], v[156:159], a[216:219], v[8:11]\n"
    "v_exp_f32 v54, v54\n"
    "v_mfma_f32_16x16x32_bf16 v[12:15], v[156:159], a[240:243], v[100:103]\n"
    "buffer_load_dwordx4 v1, s[16:19], s83 offen offset:2048 sc1 lds\n"
    "v_mfma_f32_16x16x32_bf16 v[12:15], v[160:163], a[244:247], v[12:15]\n"
    "s_waitcnt vmcnt(10)\n"
    "ds_read_b128 v[232:235], v243 offset:96\n"
    "v_cvt_pk_bf16_f32 v86, v52, v53\n"
    "v_mfma_f32_16x16x32_bf16 v[12:15], v[164:167], a[248:251], v[12:15]\n"
    "v_exp_f32 v55, v55\n"
    "v_mfma_f32_16x16x32_bf16 v[12:15], v[168:171], a[252:255], v[12:15]\n"
    "v_cvt_pk_bf16_f32 v87, v54, v55\n"
    "v_cvt_f32_i32 v204, v204\n"
    "v_mfma_f32_16x16x32_bf16 v[12:15], v[172:175], v[124:127], v[12:15]\n"
    "buffer_load_dwordx4 v1, s[16:19], s83 offen offset:3072 sc1 lds\n"
    "v_mfma_f32_16x16x32_bf16 v[12:15], v[176:179], v[128:131], v[12:15]\n"
    "s_add_u32 m0, s70, 4096\n"
    "v_exp_f32 v56, v56\n"
    "v_mfma_f32_16x16x32_bf16 v[16:19], v[176:179], v[152:155], v[100:103]\n"
    "s_add_u32 s83, s67, 28672\n"
    "v_exp_f32 v50, v50\n"
    "v_mfma_f32_16x16x32_bf16 v[16:19], v[172:175], v[148:151], v[16:19]\n"
    "v_exp_f32 v51, v51\n"
    "v_mfma_f32_16x16x32_bf16 v[16:19], v[168:171], v[144:147], v[16:19]\n"
    "buffer_load_dwordx4 v1, s[16:19], s83 offen offset:0 sc1 lds\n"
    "v_mfma_f32_16x16x32_bf16 v[16:19], v[164:167], v[140:143], v[16:19]\n"
    "v_exp_f32 v57, v57\n"
    "v_mfma_f32_16x16x32_bf16 v[16:19], v[160:163], v[136:139], v[16:19]\n"
    "v_cvt_pk_bf16_f32 v90, v56, v57\n"
    "v_cvt_pk_bf16_f32 v97, v50, v51\n"
    "v_mfma_f32_16x16x32_bf16 v[16:19], v[156:159], v[132:135], v[16:19]\n"
    "s_waitcnt lgkmcnt(2)\n"
    "v_exp_f32 v58, v58\n"
    "v_mfma_f32_16x16x32_bf16 v[20:23], v[180:183], a[192:195], v[104:107]\n"
    "buffer_load_dwordx4 v1, s[16:19], s83 offen offset:1024 sc1 lds\n"
    "v_mfma_f32_16x16x32_bf16 v[20:23], v[184:187], a[196:199], v[20:23]\n"
    "s_add_u32 s67, s67, 49152\n"
    "ds_read_b128 v[156:159], v249 offset:0\n"
    "ds_read_b128 v[160:163], v249 offset:12288\n"
    "v_mfma_f32_16x16x32_bf16 v[20:23], v[188:191], a[200:203], v[20:23]\n"
    "s_add_u32 s59, s59, 4\n"
    "ds_read_b128 v[164:167], v249 offset:1024\n"
    "v_cvt_f32_i32 v205, v205\n"
    "v_mfma_f32_16x16x32_bf16 v[20:23], v[192:195], a[204:207], v[20:23]\n"
    "s_add_u32 s81, s81, 128\n"
    "ds_read_b128 v[168:171], v249 offset:13312\n"
    "v_cvt_f32_i32 v206, v206\n"
    "v_mfma_f32_16x16x32_bf16 v[20:23], v[196:199], a[208:211], v[20:23]\n"
    "s_min_u32 s81, s81, 4096\n"
    "ds_read_b128 v[172:175], v249 offset:2048\n"
    "v_cvt_f32_i32 v207, v207\n"
    "v_mfma_f32_16x16x32_bf16 v[20:23], v[200:203], a[212:215], v[20:23]\n"
    "ds_read_b128 v[176:179], v249 offset:14336\n"
    "v_cvt_f32_i32 v208, v208\n"
    "v_mfma_f32_16x16x32_bf16 v[24:27], v[200:203], a[236:239], v[104:107]\n"
    "ds_read_b128 v[100:103], v251 offset:768\n"
    "v_cvt_f32_i32 v209, v209\n"
    "v_mfma_f32_16x16x32_bf16 v[24:27], v[196:199], a[232:235], v[24:27]\n"
    "v_exp_f32 v59, v59\n"
    "v_mfma_f32_16x16x32_bf16 v[24:27], v[192:195], a[228:231], v[24:27]\n"
    "v_cvt_pk_bf16_f32 v91, v58, v59\n"
    "v_cvt_f32_i32 v210, v210\n"
    "v_mfma_f32_16x16x32_bf16 v[24:27], v[188:191], a[224:227], v[24:27]\n"
    "v_exp_f32 v60, v60\n"
    "v_mfma_f32_16x16x32_bf16 v[24:27], v[184:187], a[220:223], v[24:27]\n"
    "v_exp_f32 v61, v61\n"
    "v_mfma_f32_16x16x32_bf16 v[24:27], v[180:183], a[216:219], v[24:27]\n"
    "v_exp_f32 v62, v62\n"
    "v_mfma_f32_16x16x32_bf16 v[28:31], v[180:183], a[240:243], v[104:107]\n"
    "v_cvt_pk_bf16_f32 v94, v60, v61\n"
    "v_cvt_f32_i32 v211, v211\n"
    "v_mfma_f32_16x16x32_bf16 v[28:31], v[184:187], a[244:247], v[28:31]\n"
    "v_exp_f32 v63, v63\n"
    "v_mfma_f32_16x16x32_bf16 v[28:31], v[188:191], a[248:251], v[28:31]\n"
    "s_waitcnt lgkmcnt(7)\n"
    "v_cvt_pk_bf16_f32 v95, v62, v63\n"
    "v_fma_f32 v204, v204, v228, v232\n"
    "v_mfma_f32_16x16x32_bf16 v[28:31], v[192:195], a[252:255], v[28:31]\n"
    "v_fma_f32 v205, v205, v229, v233\n"
    "v_fma_f32 v206, v206, v230, v234\n"
    "v_mfma_f32_16x16x32_bf16 v[28:31], v[196:199], v[124:127], v[28:31]\n"
    "v_exp_f32 v64, v64\n"
    "v_mfma_f32_16x16x32_bf16 v[28:31], v[200:203], v[128:131], v[28:31]\n"
    "v_fma_f32 v207, v207, v231, v235\n"
    "v_cvt_f32_i32 v212, v212\n"
    "v_mfma_f32_16x16x32_bf16 v[32:35], v[200:203], v[152:155], v[104:107]\n"
    "ds_read_b128 v[228:231], v243 offset:16\n"
    "ds_read_b128 v[232:235], v243 offset:112\n"
    "v_mfma_f32_16x16x32_bf16 v[32:35], v[196:199], v[148:151], v[32:35]\n"
    "v_exp_f32 v65, v65\n"
    "v_mfma_f32_16x16x32_bf16 v[32:35], v[192:195], v[144:147], v[32:35]\n"
    "v_cvt_pk_bf16_f32 v98, v64, v65\n"
    "v_cvt_f32_i32 v213, v213\n"
    "v_mfma_f32_16x16x32_bf16 v[32:35], v[188:191], v[140:143], v[32:35]\n"
    "v_cvt_f32_i32 v214, v214\n"
    "v_cvt_f32_i32 v215, v215\n"
    "v_mfma_f32_16x16x32_bf16 v[32:35], v[184:187], v[136:139], v[32:35]\n"
    "v_exp_f32 v66, v66\n"
    "v_mfma_f32_16x16x32_bf16 v[32:35], v[180:183], v[132:135], v[32:35]\n"
    "s_waitcnt lgkmcnt(2)\n"
    "v_cvt_f32_i32 v216, v216\n"
    "v_cvt_f32_i32 v217, v217\n"
    "v_mfma_f32_16x16x32_bf16 a[0:3], v[156:159], v[68:71], a[0:3]\n"
    "ds_read_b128 v[180:183], v249 offset:3072\n"
    "v_cvt_f32_i32 v218, v218\n"
    "v_mfma_f32_16x16x32_bf16 a[0:3], v[160:163], v[84:87], a[0:3]\n"
    "ds_read_b128 v[184:187], v249 offset:15360\n"
    "v_cvt_f32_i32 v219, v219\n"
    "v_mfma_f32_16x16x32_bf16 a[16:19], v[168:171], v[84:87], a[16:19]\n"
    "s_waitcnt lgkmcnt(2)\n"
    "ds_read_b128 v[188:191], v249 offset:4096\n"
    "v_fma_f32 v208, v208, v228, v232\n"
    "v_mfma_f32_16x16x32_bf16 a[16:19], v[164:167], v[68:71], a[16:19]\n"
    "ds_read_b128 v[192:195], v249 offset:16384\n"
    "v_fma_f32 v209, v209, v229, v233\n"
    "v_mfma_f32_16x16x32_bf16 a[32:35], v[172:175], v[68:71], a[32:35]\n"
    "ds_read_b128 v[196:199], v249 offset:5120\n"
    "v_fma_f32 v210, v210, v230, v234\n"
    "v_mfma_f32_16x16x32_bf16 a[32:35], v[176:179], v[84:87], a[32:35]\n"
    "ds_read_b128 v[200:203], v249 offset:17408\n"
    "v_fma_f32 v211, v211, v231, v235\n"
    "v_mfma_f32_16x16x32_bf16 a[36:39], v[176:179], v[88:91], a[36:39]\n"
    "ds_read_b128 v[104:107], v251 offset:832\n"
    "ds_read_b128 v[228:231], v243 offset:32\n"
    "v_mfma_f32_16x16x32_bf16 a[36:39], v[172:175], v[72:75], a[36:39]\n"
    "v_exp_f32 v67, v67\n"
    "v_mfma_f32_16x16x32_bf16 a[20:23], v[164:167], v[72:75], a[20:23]\n"
    "ds_read_b128 v[232:235], v243 offset:128\n"
    "v_cvt_pk_bf16_f32 v99, v66, v67\n"
    "v_mfma_f32_16x16x32_bf16 a[20:23], v[168:171], v[88:91], a[20:23]\n"
    "v_exp_f32 v4, v4\n"
    "v_mfma_f32_16x16x32_bf16 a[4:7], v[160:163], v[88:91], a[4:7]\n"
    "v_cvt_pk_bf16_f32 v204, v204, v205\n"
    "v_cvt_pk_bf16_f32 v205, v206, v207\n"
    "v_mfma_f32_16x16x32_bf16 a[4:7], v[156:159], v[72:75], a[4:7]\n"
    "v_exp_f32 v5, v5\n"
    "v_mfma_f32_16x16x32_bf16 a[8:11], v[156:159], v[76:79], a[8:11]\n"
    "v_cvt_pk_bf16_f32 v206, v208, v209\n"
    "v_cvt_pk_bf16_f32 v207, v210, v211\n"
    "v_mfma_f32_16x16x32_bf16 a[8:11], v[160:163], v[92:95], a[8:11]\n"
    "v_exp_f32 v6, v6\n"
    "v_mfma_f32_16x16x32_bf16 a[24:27], v[168:171], v[92:95], a[24:27]\n"
    "s_waitcnt lgkmcnt(0)\n"
    "v_fma_f32 v212, v212, v228, v232\n"
    "v_fma_f32 v213, v213, v229, v233\n"
    "v_mfma_f32_16x16x32_bf16 a[24:27], v[164:167], v[76:79], a[24:27]\n"
    "v_exp_f32 v7, v7\n"
    "v_mfma_f32_16x16x32_bf16 a[40:43], v[172:175], v[76:79], a[40:43]\n"
    "v_fma_f32 v214, v214, v230, v234\n"
    "v_fma_f32 v215, v215, v231, v235\n"
    "v_mfma_f32_16x16x32_bf16 a[40:43], v[176:179], v[92:95], a[40:43]\n"
    "v_exp_f32 v8, v8\n"
    "v_mfma_f32_16x16x32_bf16 a[44:47], v[176:179], v[96:99], a[44:47]\n"
    "ds_read_b128 v[228:231], v243 offset:48\n"
    "ds_read_b128 v[232:235], v243 offset:144\n"
    "v_mfma_f32_16x16x32_bf16 a[44:47], v[172:175], v[80:83], a[44:47]\n"
    "v_exp_f32 v9, v9\n"
    "v_mfma_f32_16x16x32_bf16 a[28:31], v[164:167], v[80:83], a[28:31]\n"
    "v_cvt_f32_i32 v220, v220\n"
    "v_cvt_f32_i32 v221, v221\n"
    "v_mfma_f32_16x16x32_bf16 a[28:31], v[168:171], v[96:99], a[28:31]\n"
    "v_exp_f32 v10, v10\n"
    "v_mfma_f32_16x16x32_bf16 a[12:15], v[160:163], v[96:99], a[12:15]\n"
    "v_cvt_f32_i32 v222, v222\n"
    "v_cvt_f32_i32 v223, v223\n"
    "v_mfma_f32_16x16x32_bf16 a[12:15], v[156:159], v[80:83], a[12:15]\n"
    "v_exp_f32 v11, v11\n"
    "v_mfma_f32_16x16x32_bf16 a[48:51], v[180:183], v[68:71], a[48:51]\n"
    "ds_read_b128 v[156:159], v249 offset:6144\n"
    "v_cvt_f32_i32 v224, v224\n"
    "v_mfma_f32_16x16x32_bf16 a[48:51], v[184:187], v[84:87], a[48:51]\n"
    "ds_read_b128 v[160:163], v249 offset:18432\n"
    "v_cvt_f32_i32 v225, v225\n"
    "v_mfma_f32_16x16x32_bf16 a[64:67], v[192:195], v[84:87], a[64:67]\n"
    "ds_read_b128 v[164:167], v249 offset:7168\n"
    "v_cvt_f32_i32 v226, v226\n"
    "v_mfma_f32_16x16x32_bf16 a[64:67], v[188:191], v[68:71], a[64:67]\n"
    "ds_read_b128 v[168:171], v249 offset:19456\n"
    "v_cvt_f32_i32 v227, v227\n"
    "v_mfma_f32_16x16x32_bf16 a[80:83], v[196:199], v[68:71], a[80:83]\n"
    "s_waitcnt lgkmcnt(4)\n"
    "ds_read_b128 v[172:175], v249 offset:8192\n"
    "v_fma_f32 v216, v216, v228, v232\n"
    "v_mfma_f32_16x16x32_bf16 a[80:83], v[200:203], v[84:87], a[80:83]\n"
    "ds_read_b128 v[176:179], v249 offset:20480\n"
    "v_fma_f32 v217, v217, v229, v233\n"
    "v_mfma_f32_16x16x32_bf16 v[108:111], v[104:107], v[84:87], v[108:111]\n"
    "v_exp_f32 v12, v12\n"
    "v_mfma_f32_16x16x32_bf16 v[108:111], v[100:103], v[68:71], v[108:111]\n"
    "v_exp_f32 v13, v13\n"
    "v_mfma_f32_16x16x32_bf16 v[112:115], v[100:103], v[72:75], v[112:115]\n"
    "v_exp_f32 v14, v14\n"
    "v_mfma_f32_16x16x32_bf16 v[112:115], v[104:107], v[88:91], v[112:115]\n"
    "v_fma_f32 v218, v218, v230, v234\n"
    "v_fma_f32 v219, v219, v231, v235\n"
    "v_mfma_f32_16x16x32_bf16 a[84:87], v[200:203], v[88:91], a[84:87]\n"
    "v_exp_f32 v15, v15\n"
    "v_mfma_f32_16x16x32_bf16 a[84:87], v[196:199], v[72:75], a[84:87]\n"
    "ds_read_b128 v[228:231], v243 offset:64\n"
    "ds_read_b128 v[232:235], v243 offset:160\n"
    "v_mfma_f32_16x16x32_bf16 a[68:71], v[188:191], v[72:75], a[68:71]\n"
    "v_cvt_pk_bf16_f32 v212, v212, v213\n"
    "v_mfma_f32_16x16x32_bf16 a[68:71], v[192:195], v[88:91], a[68:71]\n"
    "v_exp_f32 v16, v16\n"
    "v_mfma_f32_16x16x32_bf16 a[52:55], v[184:187], v[88:91], a[52:55]\n"
    "v_cvt_pk_bf16_f32 v213, v214, v215\n"
    "v_mfma_f32_16x16x32_bf16 a[52:55], v[180:183], v[72:75], a[52:55]\n"
    "v_cvt_pk_bf16_f32 v214, v216, v217\n"
    "v_mfma_f32_16x16x32_bf16 a[56:59], v[180:183], v[76:79], a[56:59]\n"
    "v_exp_f32 v17, v17\n"
    "v_mfma_f32_16x16x32_bf16 a[56:59], v[184:187], v[92:95], a[56:59]\n"
    "v_cvt_pk_bf16_f32 v215, v218, v219\n"
    "v_mfma_f32_16x16x32_bf16 a[72:75], v[192:195], v[92:95], a[72:75]\n"
    "s_waitcnt lgkmcnt(0)\n"
    "v_fma_f32 v220, v220, v228, v232\n"
    "v_mfma_f32_16x16x32_bf16 a[72:75], v[188:191], v[76:79], a[72:75]\n"
    "v_exp_f32 v18, v18\n"
    "v_mfma_f32_16x16x32_bf16 a[88:91], v[196:199], v[76:79], a[88:91]\n"
    "v_fma_f32 v221, v221, v229, v233\n"
    "v_mfma_f32_16x16x32_bf16 a[88:91], v[200:203], v[92:95], a[88:91]\n"
    "v_fma_f32 v222, v222, v230, v234\n"
    "v_mfma_f32_16x16x32_bf16 v[116:119], v[104:107], v[92:95], v[116:119]\n"
    "v_exp_f32 v19, v19\n"
    "v_mfma_f32_16x16x32_bf16 v[116:119], v[100:103], v[76:79], v[116:119]\n"
    "v_fma_f32 v223, v223, v231, v235\n"
    "v_mfma_f32_16x16x32_bf16 v[120:123], v[100:103], v[80:83], v[120:123]\n"
    "ds_read_b128 v[228:231], v243 offset:80\n"
    "v_mfma_f32_16x16x32_bf16 v[120:123], v[104:107], v[96:99], v[120:123]\n"
    "v_exp_f32 v20, v20\n"
    "v_mfma_f32_16x16x32_bf16 a[92:95], v[200:203], v[96:99], a[92:95]\n"
    "ds_read_b128 v[232:235], v243 offset:176\n"
    "v_mfma_f32_16x16x32_bf16 a[92:95], v[196:199], v[80:83], a[92:95]\n"
    "v_add_u32 v243, 192, v243\n"
    "v_mfma_f32_16x16x32_bf16 a[76:79], v[188:191], v[80:83], a[76:79]\n"
    "v_exp_f32 v21, v21\n"
    "v_mfma_f32_16x16x32_bf16 a[76:79], v[192:195], v[96:99], a[76:79]\n"
    "v_mfma_f32_16x16x32_bf16 a[60:63], v[184:187], v[96:99], a[60:63]\n"
    "v_exp_f32 v22, v22\n"
    "v_mfma_f32_16x16x32_bf16 a[60:63], v[180:183], v[80:83], a[60:63]\n"
    "v_mfma_f32_16x16x32_bf16 a[96:99], v[156:159], v[68:71], a[96:99]\n"
    "s_waitcnt lgkmcnt(0)\n"
    "ds_read_b128 v[180:183], v249 offset:9216\n"
    "v_fma_f32 v224, v224, v228, v232\n"
    "v_mfma_f32_16x16x32_bf16 a[96:99], v[160:163], v[84:87], a[96:99]\n"
    "ds_read_b128 v[184:187], v249 offset:21504\n"
    "v_fma_f32 v225, v225, v229, v233\n"
    "v_mfma_f32_16x16x32_bf16 a[112:115], v[168:171], v[84:87], a[112:115]\n"
    "ds_read_b128 v[188:191], v249 offset:10240\n"
    "v_fma_f32 v226, v226, v230, v234\n"
    "v_mfma_f32_16x16x32_bf16 a[112:115], v[164:167], v[68:71], a[112:115]\n"
    "ds_read_b128 v[192:195], v249 offset:22528\n"
    "v_fma_f32 v227, v227, v231, v235\n"
    "v_mfma_f32_16x16x32_bf16 a[128:131], v[172:175], v[68:71], a[128:131]\n"
    "buffer_load_dwordx2 v[236:237], v241, s[36:39], s81 offen offset:0 sc1\n"
    "v_mfma_f32_16x16x32_bf16 a[128:131], v[176:179], v[84:87], a[128:131]\n"
    "ds_read_b128 v[196:199], v249 offset:11264\n"
    "ds_read_b128 v[200:203], v249 offset:23552\n"
    "v_mfma_f32_16x16x32_bf16 a[132:135], v[176:179], v[88:91], a[132:135]\n"
    "v_exp_f32 v23, v23\n"
    "v_mfma_f32_16x16x32_bf16 a[132:135], v[172:175], v[72:75], a[132:135]\n"
    "v_cvt_pk_bf16_f32 v220, v220, v221\n"
    "v_cvt_pk_bf16_f32 v221, v222, v223\n"
    "v_mfma_f32_16x16x32_bf16 a[116:119], v[164:167], v[72:75], a[116:119]\n"
    "v_exp_f32 v24, v24\n"
    "v_mfma_f32_16x16x32_bf16 a[116:119], v[168:171], v[88:91], a[116:119]\n"
    "v_cvt_pk_bf16_f32 v222, v224, v225\n"
    "v_cvt_pk_bf16_f32 v223, v226, v227\n"
    "v_mfma_f32_16x16x32_bf16 a[100:103], v[160:163], v[88:91], a[100:103]\n"
    "v_exp_f32 v25, v25\n"
    "v_mfma_f32_16x16x32_bf16 a[100:103], v[156:159], v[72:75], a[100:103]\n"
    "v_exp_f32 v26, v26\n"
    "v_mfma_f32_16x16x32_bf16 a[104:107], v[156:159], v[76:79], a[104:107]\n"
    "v_exp_f32 v27, v27\n"
    "v_mfma_f32_16x16x32_bf16 a[104:107], v[160:163], v[92:95], a[104:107]\n"
    "v_mfma_f32_16x16x32_bf16 a[120:123], v[168:171], v[92:95], a[120:123]\n"
    "v_mfma_f32_16x16x32_bf16 a[120:123], v[164:167], v[76:79], a[120:123]\n"
    "v_exp_f32 v28, v28\n"
    "v_mfma_f32_16x16x32_bf16 a[136:139], v[172:175], v[76:79], a[136:139]\n"
    "v_mfma_f32_16x16x32_bf16 a[136:139], v[176:179], v[92:95], a[136:139]\n"
    "v_mfma_f32_16x16x32_bf16 a[140:143], v[176:179], v[96:99], a[140:143]\n"
    "buffer_store_dwordx4 v[204:207], v244, s[16:19], s76 offen offset:0 sc1\n"
    "v_mfma_f32_16x16x32_bf16 a[140:143], v[172:175], v[80:83], a[140:143]\n"
    "v_exp_f32 v29, v29\n"
    "v_mfma_f32_16x16x32_bf16 a[124:127], v[164:167], v[80:83], a[124:127]\n"
    "buffer_store_dwordx4 v[212:215], v244, s[16:19], s77 offen offset:0 sc1\n"
    "v_mfma_f32_16x16x32_bf16 a[124:127], v[168:171], v[96:99], a[124:127]\n"
    "v_exp_f32 v30, v30\n"
    "v_mfma_f32_16x16x32_bf16 a[108:111], v[160:163], v[96:99], a[108:111]\n"
    "buffer_store_dwordx4 v[220:223], v244, s[16:19], s78 offen offset:0 sc1\n"
    "v_mfma_f32_16x16x32_bf16 a[108:111], v[156:159], v[80:83], a[108:111]\n"
    "s_waitcnt lgkmcnt(0)\n"
    "v_exp_f32 v31, v31\n"
    "v_mfma_f32_16x16x32_bf16 a[144:147], v[180:183], v[68:71], a[144:147]\n"
    "ds_read_b128 v[156:159], v3 offset:12288\n"
    "v_mfma_f32_16x16x32_bf16 a[144:147], v[184:187], v[84:87], a[144:147]\n"
    "ds_read_b128 v[160:163], v3 offset:14336\n"
    "v_mfma_f32_16x16x32_bf16 a[160:163], v[192:195], v[84:87], a[160:163]\n"
    "ds_read_b128 v[164:167], v3 offset:16384\n"
    "v_mfma_f32_16x16x32_bf16 a[160:163], v[188:191], v[68:71], a[160:163]\n"
    "ds_read_b128 v[168:171], v3 offset:18432\n"
    "v_mfma_f32_16x16x32_bf16 a[176:179], v[196:199], v[68:71], a[176:179]\n"
    "ds_read_b128 v[172:175], v3 offset:20480\n"
    "v_mfma_f32_16x16x32_bf16 a[176:179], v[200:203], v[84:87], a[176:179]\n"
    "ds_read_b128 v[176:179], v3 offset:22528\n"
    "v_mfma_f32_16x16x32_bf16 a[180:183], v[200:203], v[88:91], a[180:183]\n"
    "ds_read_b128 v[100:103], v250 offset:1664\n"
    "v_mfma_f32_16x16x32_bf16 a[180:183], v[196:199], v[72:75], a[180:183]\n"
    "v_exp_f32 v32, v32\n"
    "v_mfma_f32_16x16x32_bf16 a[164:167], v[188:191], v[72:75], a[164:167]\n"
    "v_exp_f32 v33, v33\n"
    "v_mfma_f32_16x16x32_bf16 a[164:167], v[192:195], v[88:91], a[164:167]\n"
    "v_exp_f32 v34, v34\n"
    "v_mfma_f32_16x16x32_bf16 a[148:151], v[184:187], v[88:91], a[148:151]\n"
    "v_mfma_f32_16x16x32_bf16 a[148:151], v[180:183], v[72:75], a[148:151]\n"
    "v_exp_f32 v35, v35\n"
    "v_mfma_f32_16x16x32_bf16 a[152:155], v[180:183], v[76:79], a[152:155]\n"
    "v_mfma_f32_16x16x32_bf16 a[152:155], v[184:187], v[92:95], a[152:155]\n"
    "v_mfma_f32_16x16x32_bf16 a[168:171], v[192:195], v[92:95], a[168:171]\n"
    "s_waitcnt vmcnt(4)\n"
    "ds_add_u32 v247, v246\n"
    "v_mfma_f32_16x16x32_bf16 a[168:171], v[188:191], v[76:79], a[168:171]\n"
    "v_mfma_f32_16x16x32_bf16 a[184:187], v[196:199], v[76:79], a[184:187]\n"
    "v_mfma_f32_16x16x32_bf16 a[184:187], v[200:203], v[92:95], a[184:187]\n"
    "v_mfma_f32_16x16x32_bf16 a[188:191], v[200:203], v[96:99], a[188:191]\n"
    "v_mfma_f32_16x16x32_bf16 a[188:191], v[196:199], v[80:83], a[188:191]\n"
    "v_mfma_f32_16x16x32_bf16 a[172:175], v[188:191], v[80:83], a[172:175]\n"
    "s_sub_u32 s74, s68, s65\n"
    "v_add_u32 v250, 1536, v250\n"
    "v_add_u32 v251, 1536, v251\n"
    "v_mfma_f32_16x16x32_bf16 a[172:175], v[192:195], v[96:99], a[172:175]\n"
    "s_add_u32 s66, s66, 1\n"
    "v_mov_b32 v2, v3\n"
    "v_add_u32 v3, s74, v1\n"
    "v_mfma_f32_16x16x32_bf16 a[156:159], v[184:187], v[96:99], a[156:159]\n"
    "s_waitcnt lgkmcnt(1)\n"
    "s_cmp_lt_u32 s66, 31\n"
    "v_mfma_f32_16x16x32_bf16 a[156:159], v[180:183], v[80:83], a[156:159]\n"
    "s_cbranch_scc1 .LPK\n"
    "s_branch .Ltail\n"
    ".LPV:\n"
    "v_mfma_f32_16x16x32_bf16 v[36:39], v[156:159], a[192:195], v[100:103]\n"
    "s_add_u32 s71, s71, 98304\n"
    "ds_read_b128 v[180:183], v2 offset:13312\n"
    "v_cvt_pk_bf16_f32 v68, v4, v5\n"
    "v_mfma_f32_16x16x32_bf16 v[36:39], v[160:163], a[196:199], v[36:39]\n"
    "s_add_u32 s75, s71, s63\n"
    "ds_read_b128 v[184:187], v2 offset:15360\n"
    "v_mfma_f32_16x16x32_bf16 v[36:39], v[164:167], a[200:203], v[36:39]\n"
    "ds_read_b128 v[188:191], v2 offset:17408\n"
    "v_mfma_f32_16x16x32_bf16 v[36:39], v[168:171], a[204:207], v[36:39]\n"
    "ds_read_b128 v[192:195], v2 offset:19456\n"
    "v_cvt_pk_bf16_f32 v69, v6, v7\n"
    "v_mfma_f32_16x16x32_bf16 v[36:39], v[172:175], a[208:211], v[36:39]\n"
    "ds_read_b128 v[196:199], v2 offset:21504\n"
    "v_mfma_f32_16x16x32_bf16 v[36:39], v[176:179], a[212:215], v[36:39]\n"
    "ds_read_b128 v[200:203], v2 offset:23552\n"
    "v_mfma_f32_16x16x32_bf16 v[40:43], v[176:179], a[236:239], v[100:103]\n"
    "ds_read_b128 v[104:107], v250 offset:144\n"
    "v_mfma_f32_16x16x32_bf16 v[40:43], v[172:175], a[232:235], v[40:43]\n"
    "s_add_u32 s68, s68, 24576\n"
    "v_cvt_pk_bf16_f32 v72, v8, v9\n"
    "v_mfma_f32_16x16x32_bf16 v[40:43], v[168:171], a[228:231], v[40:43]\n"
    "s_cmp_ge_u32 s68, s69\n"
    "s_cselect_b32 s68, s65, s68\n"
    "v_mfma_f32_16x16x32_bf16 v[40:43], v[164:167], a[224:227], v[40:43]\n"
    "v_exp_f32 v36, v36\n"
    "v_mfma_f32_16x16x32_bf16 v[40:43], v[160:163], a[220:223], v[40:43]\n"
    "v_exp_f32 v37, v37\n"
    "v_mfma_f32_16x16x32_bf16 v[40:43], v[156:159], a[216:219], v[40:43]\n"
    "v_cvt_pk_bf16_f32 v73, v10, v11\n"
    "v_cvt_pk_bf16_f32 v84, v36, v37\n"
    "v_mfma_f32_16x16x32_bf16 v[44:47], v[156:159], a[240:243], v[100:103]\n"
    "v_mfma_f32_16x16x32_bf16 v[44:47], v[160:163], a[244:247], v[44:47]\n"
    "v_exp_f32 v38, v38\n"
    "v_mfma_f32_16x16x32_bf16 v[44:47], v[164:167], a[248:251], v[44:47]\n"
    "v_mfma_f32_16x16x32_bf16 v[44:47], v[168:171], a[252:255], v[44:47]\n"
    "v_cvt_pk_bf16_f32 v76, v12, v13\n"
    "v_mfma_f32_16x16x32_bf16 v[44:47], v[172:175], v[124:127], v[44:47]\n"
    "v_exp_f32 v39, v39\n"
    "v_mfma_f32_16x16x32_bf16 v[44:47], v[176:179], v[128:131], v[44:47]\n"
    "v_cvt_pk_bf16_f32 v85, v38, v39\n"
    "v_mfma_f32_16x16x32_bf16 v[48:51], v[176:179], v[152:155], v[100:103]\n"
    "ds_read_b32 v245, v247\n"
    "v_cvt_pk_bf16_f32 v77, v14, v15\n"
    "v_mfma_f32_16x16x32_bf16 v[48:51], v[172:175], v[148:151], v[48:51]\n"
    "s_add_u32 m0, s68, 0\n"
    "v_exp_f32 v40, v40\n"
    "v_mfma_f32_16x16x32_bf16 v[48:51], v[168:171], v[144:147], v[48:51]\n"
    "v_mfma_f32_16x16x32_bf16 v[48:51], v[164:167], v[140:143], v[48:51]\n"
    "v_mfma_f32_16x16x32_bf16 v[48:51], v[160:163], v[136:139], v[48:51]\n"
    "s_waitcnt vmcnt(3)\n"
    "v_cmp_eq_u64 s[54:55], v[236:237], v[238:239]\n"
    "v_cvt_pk_bf16_f32 v80, v16, v17\n"
    "v_mfma_f32_16x16x32_bf16 v[48:51], v[156:159], v[132:135], v[48:51]\n"
    "s_waitcnt lgkmcnt(1)\n"
    "v_exp_f32 v41, v41\n"
    "v_mfma_f32_16x16x32_bf16 v[52:55], v[180:183], a[192:195], v[104:107]\n"
    "s_waitcnt lgkmcnt(0)\n"
    "v_cmp_le_u32 s[56:57], s59, v245\n"
    "v_cvt_pk_bf16_f32 v88, v40, v41\n"
    "v_mfma_f32_16x16x32_bf16 v[52:55], v[184:187], a[196:199], v[52:55]\n"
    "v_exp_f32 v42, v42\n"
    "v_mfma_f32_16x16x32_bf16 v[52:55], v[188:191], a[200:203], v[52:55]\n"
    "s_and_b64 s[54:55], s[54:55], s[56:57]\n"
    "s_cmp_eq_u64 s[54:55], -1\n"
    "v_mfma_f32_16x16x32_bf16 v[52:55], v[192:195], a[204:207], v[52:55]\n"
    "s_cbranch_scc0 .Lpoll3\n"
    ".Lpoll3r:\n"
    "v_mfma_f32_16x16x32_bf16 v[52:55], v[196:199], a[208:211], v[52:55]\n"
    "buffer_load_dwordx4 v1, s[16:19], s67 offen offset:0 sc1 lds\n"
    "v_mfma_f32_16x16x32_bf16 v[52:55], v[200:203], a[212:215], v[52:55]\n"
    "ds_read_b128 v[156:159], v3 offset:0\n"
    "ds_read_b128 v[160:163], v3 offset:2048\n"
    "v_mfma_f32_16x16x32_bf16 v[56:59], v[200:203], a[236:239], v[104:107]\n"
    "ds_read_b128 v[164:167], v3 offset:4096\n"
    "ds_read_b128 v[168:171], v3 offset:6144\n"
    "v_mfma_f32_16x16x32_bf16 v[56:59], v[196:199], a[232:235], v[56:59]\n"
    "ds_read_b128 v[172:175], v3 offset:8192\n"
    "ds_read_b128 v[176:179], v3 offset:10240\n"
    "v_mfma_f32_16x16x32_bf16 v[56:59], v[192:195], a[228:231], v[56:59]\n"
    "buffer_load_dwordx4 v1, s[16:19], s67 offen offset:1024 sc1 lds\n"
    "v_mfma_f32_16x16x32_bf16 v[56:59], v[188:191], a[224:227], v[56:59]\n"
    "ds_read_b128 v[100:103], v250 offset:768\n"
    "v_cvt_pk_bf16_f32 v81, v18, v19\n"
    "v_mfma_f32_16x16x32_bf16 v[56:59], v[184:187], a[220:223], v[56:59]\n"
    "v_cvt_pk_bf16_f32 v70, v20, v21\n"
    "v_cvt_pk_bf16_f32 v71, v22, v23\n"
    "v_mfma_f32_16x16x32_bf16 v[56:59], v[180:183], a[216:219], v[56:59]\n"
    "v_exp_f32 v43, v43\n"
    "v_mfma_f32_16x16x32_bf16 v[60:63], v[180:183], a[240:243], v[104:107]\n"
    "buffer_load_dwordx4 v1, s[16:19], s67 offen offset:2048 sc1 lds\n"
    "v_mfma_f32_16x16x32_bf16 v[60:63], v[184:187], a[244:247], v[60:63]\n"
    "v_cvt_pk_bf16_f32 v74, v24, v25\n"
    "v_cvt_pk_bf16_f32 v89, v42, v43\n"
    "v_mfma_f32_16x16x32_bf16 v[60:63], v[188:191], a[248:251], v[60:63]\n"
    "v_exp_f32 v44, v44\n"
    "v_mfma_f32_16x16x32_bf16 v[60:63], v[192:195], a[252:255], v[60:63]\n"
    "v_exp_f32 v45, v45\n"
    "v_mfma_f32_16x16x32_bf16 v[60:63], v[196:199], v[124:127], v[60:63]\n"
    "buffer_load_dwordx4 v1, s[16:19], s67 offen offset:3072 sc1 lds\n"
    "v_mfma_f32_16x16x32_bf16 v[60:63], v[200:203], v[128:131], v[60:63]\n"
    "s_add_u32 m0, s68, 4096\n"
    "v_cvt_pk_bf16_f32 v75, v26, v27\n"
    "v_cvt_pk_bf16_f32 v92, v44, v45\n"
    "v_mfma_f32_16x16x32_bf16 v[64:67], v[200:203], v[152:155], v[104:107]\n"
    "s_add_u32 s83, s67, 4096\n"
    "v_exp_f32 v46, v46\n"
    "v_mfma_f32_16x16x32_bf16 v[64:67], v[196:199], v[148:151], v[64:67]\n"
    "v_exp_f32 v47, v47\n"
    "v_mfma_f32_16x16x32_bf16 v[64:67], v[192:195], v[144:147], v[64:67]\n"
    "buffer_load_dwordx4 v1, s[16:19], s83 offen offset:0 sc1 lds\n"
    "v_mfma_f32_16x16x32_bf16 v[64:67], v[188:191], v[140:143], v[64:67]\n"
    "v_cvt_pk_bf16_f32 v78, v28, v29\n"
    "v_cvt_pk_bf16_f32 v93, v46, v47\n"
    "v_mfma_f32_16x16x32_bf16 v[64:67], v[184:187], v[136:139], v[64:67]\n"
    "v_exp_f32 v48, v48\n"
    "v_mfma_f32_16x16x32_bf16 v[64:67], v[180:183], v[132:135], v[64:67]\n"
    "s_waitcnt lgkmcnt(0)\n"
    "v_exp_f32 v49, v49\n"
    "v_mfma_f32_16x16x32_bf16 v[4:7], v[156:159], a[192:195], v[100:103]\n"
    "buffer_load_dwordx4 v1, s[16:19], s83 offen offset:1024 sc1 lds\n"
    "v_mfma_f32_16x16x32_bf16 v[4:7], v[160:163], a[196:199], v[4:7]\n"
    "s_add_u32 m0, s64, 0\n"
    "ds_read_b128 v[180:183], v3 offset:1024\n"
    "ds_read_b128 v[184:187], v3 offset:3072\n"
    "v_mfma_f32_16x16x32_bf16 v[4:7], v[164:167], a[200:203], v[4:7]\n"
    "s_add_u32 s83, s67, 24576\n"
    "ds_read_b128 v[188:191], v3 offset:5120\n"
    "v_cvt_pk_bf16_f32 v79, v30, v31\n"
    "v_mfma_f32_16x16x32_bf16 v[4:7], v[168:171], a[204:207], v[4:7]\n"
    "ds_read_b128 v[192:195], v3 offset:7168\n"
    "v_cvt_pk_bf16_f32 v96, v48, v49\n"
    "v_mfma_f32_16x16x32_bf16 v[4:7], v[172:175], a[208:211], v[4:7]\n"
    "buffer_load_dwordx4 v1, s[16:19], s83 offen offset:0 sc1 lds\n"
    "v_mfma_f32_16x16x32_bf16 v[4:7], v[176:179], a[212:215], v[4:7]\n"
    "ds_read_b128 v[196:199], v3 offset:9216\n"
    "ds_read_b128 v[200:203], v3 offset:11264\n"
    "v_mfma_f32_16x16x32_bf16 v[8:11], v[176:179], a[236:239], v[100:103]\n"
    "ds_read_b128 v[104:107], v250 offset:784\n"
    "v_cvt_pk_bf16_f32 v82, v32, v33\n"
    "v_mfma_f32_16x16x32_bf16 v[8:11], v[172:175], a[232:235], v[8:11]\n"
    "v_exp_f32 v52, v52\n"
    "v_mfma_f32_16x16x32_bf16 v[8:11], v[168:171], a[228:231], v[8:11]\n"
    "buffer_load_dwordx4 v1, s[16:19], s83 offen offset:1024 sc1 lds\n"
    "v_mfma_f32_16x16x32_bf16 v[8:11], v[164:167], a[224:227], v[8:11]\n"
    "v_cvt_pk_bf16_f32 v83, v34, v35\n"
    "v_mfma_f32_16x16x32_bf16 v[8:11], v[160:163], a[220:223], v[8:11]\n"
    "v_exp_f32 v53, v53\n"
    "v_mfma_f32_16x16x32_bf16 v[8:11], v[156:159], a[216:219], v[8:11]\n"
    "v_exp_f32 v54, v54\n"
    "v_mfma_f32_16x16x32_bf16 v[12:15], v[156:159], a[240:243], v[100:103]\n"
    "buffer_load_dwordx4 v1, s[16:19], s83 offen offset:2048 sc1 lds\n"
    "v_mfma_f32_16x16x32_bf16 v[12:15], v[160:163], a[244:247], v[12:15]\n"
    "v_cvt_pk_bf16_f32 v86, v52, v53\n"
    "v_mfma_f32_16x16x32_bf16 v[12:15], v[164:167], a[248:251], v[12:15]\n"
    "v_exp_f32 v55, v55\n"
    "v_mfma_f32_16x16x32_bf16 v[12:15], v[168:171], a[252:255], v[12:15]\n"
    "v_cvt_pk_bf16_f32 v87, v54, v55\n"
    "v_mfma_f32_16x16x32_bf16 v[12:15], v[172:175], v[124:127], v[12:15]\n"
    "buffer_load_dwordx4 v1, s[16:19], s83 offen offset:3072 sc1 lds\n"
    "v_mfma_f32_16x16x32_bf16 v[12:15], v[176:179], v[128:131], v[12:15]\n"
    "s_add_u32 m0, s64, 4096\n"
    "v_exp_f32 v56, v56\n"
    "v_mfma_f32_16x16x32_bf16 v[16:19], v[176:179], v[152:155], v[100:103]\n"
    "s_add_u32 s83, s67, 28672\n"
    "v_exp_f32 v50, v50\n"
    "v_mfma_f32_16x16x32_bf16 v[16:19], v[172:175], v[148:151], v[16:19]\n"
    "v_exp_f32 v51, v51\n"
    "v_mfma_f32_16x16x32_bf16 v[16:19], v[168:171], v[144:147], v[16:19]\n"
    "buffer_load_dwordx4 v1, s[16:19], s83 offen offset:0 sc1 lds\n"
    "v_mfma_f32_16x16x32_bf16 v[16:19], v[164:167], v[140:143], v[16:19]\n"
    "v_exp_f32 v57, v57\n"
    "v_mfma_f32_16x16x32_bf16 v[16:19], v[160:163], v[136:139], v[16:19]\n"
    "v_cvt_pk_bf16_f32 v90, v56, v57\n"
    "v_cvt_pk_bf16_f32 v97, v50, v51\n"
    "v_mfma_f32_16x16x32_bf16 v[16:19], v[156:159], v[132:135], v[16:19]\n"
    "s_waitcnt lgkmcnt(0)\n"
    "v_exp_f32 v58, v58\n"
    "v_mfma_f32_16x16x32_bf16 v[20:23], v[180:183], a[192:195], v[104:107]\n"
    "buffer_load_dwordx4 v1, s[16:19], s83 offen offset:1024 sc1 lds\n"
    "v_mfma_f32_16x16x32_bf16 v[20:23], v[184:187], a[196:199], v[20:23]\n"
    "s_add_u32 s67, s67, 49152\n"
    "ds_read_b128 v[156:159], v248 offset:0\n"
    "ds_read_b128 v[160:163], v248 offset:12288\n"
    "v_mfma_f32_16x16x32_bf16 v[20:23], v[188:191], a[200:203], v[20:23]\n"
    "s_add_u32 s59, s59, 4\n"
    "ds_read_b128 v[164:167], v248 offset:1024\n"
    "v_mfma_f32_16x16x32_bf16 v[20:23], v[192:195], a[204:207], v[20:23]\n"
    "s_add_u32 s81, s81, 128\n"
    "s_waitcnt vmcnt(12)\n"
    "ds_read_b128 v[168:171], v248 offset:13312\n"
    "v_mfma_f32_16x16x32_bf16 v[20:23], v[196:199], a[208:211], v[20:23]\n"
    "s_min_u32 s81, s81, 4096\n"
    "ds_read_b128 v[172:175], v248 offset:2048\n"
    "v_mfma_f32_16x16x32_bf16 v[20:23], v[200:203], a[212:215], v[20:23]\n"
    "buffer_store_dwordx2 v[238:239], v240, s[36:39], s80 offen offset:0 sc1\n"
    "v_mfma_f32_16x16x32_bf16 v[24:27], v[200:203], a[236:239], v[104:107]\n"
    "s_add_u32 s80, s80, 256\n"
    "ds_read_b128 v[176:179], v248 offset:14336\n"
    "ds_read_b128 v[100:103], v251 offset:0\n"
    "v_mfma_f32_16x16x32_bf16 v[24:27], v[196:199], a[232:235], v[24:27]\n"
    "v_exp_f32 v59, v59\n"
    "v_mfma_f32_16x16x32_bf16 v[24:27], v[192:195], a[228:231], v[24:27]\n"
    "v_cvt_pk_bf16_f32 v91, v58, v59\n"
    "v_mfma_f32_16x16x32_bf16 v[24:27], v[188:191], a[224:227], v[24:27]\n"
    "v_exp_f32 v60, v60\n"
    "v_mfma_f32_16x16x32_bf16 v[24:27], v[184:187], a[220:223], v[24:27]\n"
    "v_exp_f32 v61, v61\n"
    "v_mfma_f32_16x16x32_bf16 v[24:27], v[180:183], a[216:219], v[24:27]\n"
    "v_exp_f32 v62, v62\n"
    "v_mfma_f32_16x16x32_bf16 v[28:31], v[180:183], a[240:243], v[104:107]\n"
    "v_cvt_pk_bf16_f32 v94, v60, v61\n"
    "v_mfma_f32_16x16x32_bf16 v[28:31], v[184:187], a[244:247], v[28:31]\n"
    "v_exp_f32 v63, v63\n"
    "v_mfma_f32_16x16x32_bf16 v[28:31], v[188:191], a[248:251], v[28:31]\n"
    "v_cvt_pk_bf16_f32 v95, v62, v63\n"
    "v_mfma_f32_16x16x32_bf16 v[28:31], v[192:195], a[252:255], v[28:31]\n"
    "buffer_load_dwordx3 v[204:206], v243, s[40:43], s75 offen offset:1536 nt\n"
    "v_mfma_f32_16x16x32_bf16 v[28:31], v[196:199], v[124:127], v[28:31]\n"
    "v_exp_f32 v64, v64\n"
    "v_mfma_f32_16x16x32_bf16 v[28:31], v[200:203], v[128:131], v[28:31]\n"
    "v_mfma_f32_16x16x32_bf16 v[32:35], v[200:203], v[152:155], v[104:107]\n"
    "buffer_load_dwordx3 v[208:210], v243, s[40:43], s75 offen offset:2304 nt\n"
    "v_mfma_f32_16x16x32_bf16 v[32:35], v[196:199], v[148:151], v[32:35]\n"
    "v_exp_f32 v65, v65\n"
    "v_mfma_f32_16x16x32_bf16 v[32:35], v[192:195], v[144:147], v[32:35]\n"
    "v_cvt_pk_bf16_f32 v98, v64, v65\n"
    "v_mfma_f32_16x16x32_bf16 v[32:35], v[188:191], v[140:143], v[32:35]\n"
    "buffer_load_dwordx3 v[212:214], v243, s[40:43], s75 offen offset:0 nt\n"
    "v_mfma_f32_16x16x32_bf16 v[32:35], v[184:187], v[136:139], v[32:35]\n"
    "v_exp_f32 v66, v66\n"
    "v_mfma_f32_16x16x32_bf16 v[32:35], v[180:183], v[132:135], v[32:35]\n"
    "s_waitcnt lgkmcnt(0)\n"
    "v_mfma_f32_16x16x32_bf16 a[0:3], v[156:159], v[68:71], a[0:3]\n"
    "ds_read_b128 v[180:183], v248 offset:3072\n"
    "v_mfma_f32_16x16x32_bf16 a[0:3], v[160:163], v[84:87], a[0:3]\n"
    "ds_read_b128 v[184:187], v248 offset:15360\n"
    "v_mfma_f32_16x16x32_bf16 a[16:19], v[168:171], v[84:87], a[16:19]\n"
    "ds_read_b128 v[188:191], v248 offset:4096\n"
    "v_mfma_f32_16x16x32_bf16 a[16:19], v[164:167], v[68:71], a[16:19]\n"
    "ds_read_b128 v[192:195], v248 offset:16384\n"
    "v_mfma_f32_16x16x32_bf16 a[32:35], v[172:175], v[68:71], a[32:35]\n"
    "ds_read_b128 v[196:199], v248 offset:5120\n"
    "v_mfma_f32_16x16x32_bf16 a[32:35], v[176:179], v[84:87], a[32:35]\n"
    "ds_read_b128 v[200:203], v248 offset:17408\n"
    "v_mfma_f32_16x16x32_bf16 a[36:39], v[176:179], v[88:91], a[36:39]\n"
    "ds_read_b128 v[104:107], v251 offset:64\n"
    "v_mfma_f32_16x16x32_bf16 a[36:39], v[172:175], v[72:75], a[36:39]\n"
    "v_exp_f32 v67, v67\n"
    "v_mfma_f32_16x16x32_bf16 a[20:23], v[164:167], v[72:75], a[20:23]\n"
    "v_cvt_pk_bf16_f32 v99, v66, v67\n"
    "v_mfma_f32_16x16x32_bf16 a[20:23], v[168:171], v[88:91], a[20:23]\n"
    "buffer_load_dwordx3 v[216:218], v243, s[40:43], s75 offen offset:768 nt\n"
    "v_mfma_f32_16x16x32_bf16 a[4:7], v[160:163], v[88:91], a[4:7]\n"
    "v_exp_f32 v4, v4\n"
    "v_mfma_f32_16x16x32_bf16 a[4:7], v[156:159], v[72:75], a[4:7]\n"
    "v_exp_f32 v5, v5\n"
    "v_mfma_f32_16x16x32_bf16 a[8:11], v[156:159], v[76:79], a[8:11]\n"
    "buffer_load_dwordx3 v[220:222], v242, s[40:43], s75 offen offset:1536 nt\n"
    "v_mfma_f32_16x16x32_bf16 a[8:11], v[160:163], v[92:95], a[8:11]\n"
    "v_exp_f32 v6, v6\n"
    "v_mfma_f32_16x16x32_bf16 a[24:27], v[168:171], v[92:95], a[24:27]\n"
    "v_exp_f32 v7, v7\n"
    "v_mfma_f32_16x16x32_bf16 a[24:27], v[164:167], v[76:79], a[24:27]\n"
    "buffer_load_dwordx3 v[224:226], v242, s[40:43], s75 offen offset:2304 nt\n"
    "v_mfma_f32_16x16x32_bf16 a[40:43], v[172:175], v[76:79], a[40:43]\n"
    "v_exp_f32 v8, v8\n"
    "v_mfma_f32_16x16x32_bf16 a[40:43], v[176:179], v[92:95], a[40:43]\n"
    "v_mfma_f32_16x16x32_bf16 a[44:47], v[176:179], v[96:99], a[44:47]\n"
    "buffer_load_dwordx3 v[228:230], v242, s[40:43], s75 offen offset:0 nt\n"
    "v_mfma_f32_16x16x32_bf16 a[44:47], v[172:175], v[80:83], a[44:47]\n"
    "v_exp_f32 v9, v9\n"
    "v_mfma_f32_16x16x32_bf16 a[28:31], v[164:167], v[80:83], a[28:31]\n"
    "v_exp_f32 v10, v10\n"
    "v_mfma_f32_16x16x32_bf16 a[28:31], v[168:171], v[96:99], a[28:31]\n"
    "buffer_load_dwordx3 v[232:234], v242, s[40:43], s75 offen offset:768 nt\n"
    "v_mfma_f32_16x16x32_bf16 a[12:15], v[160:163], v[96:99], a[12:15]\n"
    "v_mfma_f32_16x16x32_bf16 a[12:15], v[156:159], v[80:83], a[12:15]\n"
    "s_waitcnt lgkmcnt(0)\n"
    "v_exp_f32 v11, v11\n"
    "v_mfma_f32_16x16x32_bf16 a[48:51], v[180:183], v[68:71], a[48:51]\n"
    "ds_read_b128 v[156:159], v248 offset:6144\n"
    "v_mfma_f32_16x16x32_bf16 a[48:51], v[184:187], v[84:87], a[48:51]\n"
    "ds_read_b128 v[160:163], v248 offset:18432\n"
    "v_mfma_f32_16x16x32_bf16 a[64:67], v[192:195], v[84:87], a[64:67]\n"
    "ds_read_b128 v[164:167], v248 offset:7168\n"
    "v_mfma_f32_16x16x32_bf16 a[64:67], v[188:191], v[68:71], a[64:67]\n"
    "ds_read_b128 v[168:171], v248 offset:19456\n"
    "v_mfma_f32_16x16x32_bf16 a[80:83], v[196:199], v[68:71], a[80:83]\n"
    "ds_read_b128 v[172:175], v248 offset:8192\n"
    "v_mfma_f32_16x16x32_bf16 a[80:83], v[200:203], v[84:87], a[80:83]\n"
    "ds_read_b128 v[176:179], v248 offset:20480\n"
    "v_mfma_f32_16x16x32_bf16 v[108:111], v[104:107], v[84:87], v[108:111]\n"
    "v_exp_f32 v12, v12\n"
    "v_mfma_f32_16x16x32_bf16 v[108:111], v[100:103], v[68:71], v[108:111]\n"
    "v_exp_f32 v13, v13\n"
    "v_mfma_f32_16x16x32_bf16 v[112:115], v[100:103], v[72:75], v[112:115]\n"
    "v_exp_f32 v14, v14\n"
    "v_mfma_f32_16x16x32_bf16 v[112:115], v[104:107], v[88:91], v[112:115]\n"
    "v_mfma_f32_16x16x32_bf16 a[84:87], v[200:203], v[88:91], a[84:87]\n"
    "v_exp_f32 v15, v15\n"
    "v_mfma_f32_16x16x32_bf16 a[84:87], v[196:199], v[72:75], a[84:87]\n"
    "v_mfma_f32_16x16x32_bf16 a[68:71], v[188:191], v[72:75], a[68:71]\n"
    "v_mfma_f32_16x16x32_bf16 a[68:71], v[192:195], v[88:91], a[68:71]\n"
    "v_exp_f32 v16, v16\n"
    "v_mfma_f32_16x16x32_bf16 a[52:55], v[184:187], v[88:91], a[52:55]\n"
    "v_mfma_f32_16x16x32_bf16 a[52:55], v[180:183], v[72:75], a[52:55]\n"
    "v_mfma_f32_16x16x32_bf16 a[56:59], v[180:183], v[76:79], a[56:59]\n"
    "v_exp_f32 v17, v17\n"
    "v_mfma_f32_16x16x32_bf16 a[56:59], v[184:187], v[92:95], a[56:59]\n"
    "v_mfma_f32_16x16x32_bf16 a[72:75], v[192:195], v[92:95], a[72:75]\n"
    "v_mfma_f32_16x16x32_bf16 a[72:75], v[188:191], v[76:79], a[72:75]\n"
    "v_exp_f32 v18, v18\n"
    "v_mfma_f32_16x16x32_bf16 a[88:91], v[196:199], v[76:79], a[88:91]\n"
    "v_mfma_f32_16x16x32_bf16 a[88:91], v[200:203], v[92:95], a[88:91]\n"
    "v_mfma_f32_16x16x32_bf16 v[116:119], v[104:107], v[92:95], v[116:119]\n"
    "v_exp_f32 v19, v19\n"
    "v_mfma_f32_16x16x32_bf16 v[116:119], v[100:103], v[76:79], v[116:119]\n"
    "v_mfma_f32_16x16x32_bf16 v[120:123], v[100:103], v[80:83], v[120:123]\n"
    "v_mfma_f32_16x16x32_bf16 v[120:123], v[104:107], v[96:99], v[120:123]\n"
    "v_exp_f32 v20, v20\n"
    "v_mfma_f32_16x16x32_bf16 a[92:95], v[200:203], v[96:99], a[92:95]\n"
    "v_mfma_f32_16x16x32_bf16 a[92:95], v[196:199], v[80:83], a[92:95]\n"
    "v_exp_f32 v21, v21\n"
    "v_mfma_f32_16x16x32_bf16 a[76:79], v[188:191], v[80:83], a[76:79]\n"
    "v_mfma_f32_16x16x32_bf16 a[76:79], v[192:195], v[96:99], a[76:79]\n"
    "v_mfma_f32_16x16x32_bf16 a[60:63], v[184:187], v[96:99], a[60:63]\n"
    "v_exp_f32 v22, v22\n"
    "v_mfma_f32_16x16x32_bf16 a[60:63], v[180:183], v[80:83], a[60:63]\n"
    "s_waitcnt lgkmcnt(0)\n"
    "v_mfma_f32_16x16x32_bf16 a[96:99], v[156:159], v[68:71], a[96:99]\n"
    "ds_read_b128 v[180:183], v248 offset:9216\n"
    "v_mfma_f32_16x16x32_bf16 a[96:99], v[160:163], v[84:87], a[96:99]\n"
    "ds_read_b128 v[184:187], v248 offset:21504\n"
    "v_mfma_f32_16x16x32_bf16 a[112:115], v[168:171], v[84:87], a[112:115]\n"
    "ds_read_b128 v[188:191], v248 offset:10240\n"
    "v_mfma_f32_16x16x32_bf16 a[112:115], v[164:167], v[68:71], a[112:115]\n"
    "ds_read_b128 v[192:195], v248 offset:22528\n"
    "v_mfma_f32_16x16x32_bf16 a[128:131], v[172:175], v[68:71], a[128:131]\n"
    "buffer_load_dwordx2 v[236:237], v241, s[36:39], s81 offen offset:0 sc1\n"
    "v_mfma_f32_16x16x32_bf16 a[128:131], v[176:179], v[84:87], a[128:131]\n"
    "ds_read_b128 v[196:199], v248 offset:11264\n"
    "ds_read_b128 v[200:203], v248 offset:23552\n"
    "v_mfma_f32_16x16x32_bf16 a[132:135], v[176:179], v[88:91], a[132:135]\n"
    "v_exp_f32 v23, v23\n"
    "v_mfma_f32_16x16x32_bf16 a[132:135], v[172:175], v[72:75], a[132:135]\n"
    "v_exp_f32 v24, v24\n"
    "v_mfma_f32_16x16x32_bf16 a[116:119], v[164:167], v[72:75], a[116:119]\n"
    "v_exp_f32 v25, v25\n"
    "v_mfma_f32_16x16x32_bf16 a[116:119], v[168:171], v[88:91], a[116:119]\n"
    "v_exp_f32 v26, v26\n"
    "v_mfma_f32_16x16x32_bf16 a[100:103], v[160:163], v[88:91], a[100:103]\n"
    "v_mfma_f32_16x16x32_bf16 a[100:103], v[156:159], v[72:75], a[100:103]\n"
    "v_mfma_f32_16x16x32_bf16 a[104:107], v[156:159], v[76:79], a[104:107]\n"
    "v_exp_f32 v27, v27\n"
    "v_mfma_f32_16x16x32_bf16 a[104:107], v[160:163], v[92:95], a[104:107]\n"
    "v_mfma_f32_16x16x32_bf16 a[120:123], v[168:171], v[92:95], a[120:123]\n"
    "v_mfma_f32_16x16x32_bf16 a[120:123], v[164:167], v[76:79], a[120:123]\n"
    "v_exp_f32 v28, v28\n"
    "v_mfma_f32_16x16x32_bf16 a[136:139], v[172:175], v[76:79], a[136:139]\n"
    "v_mfma_f32_16x16x32_bf16 a[136:139], v[176:179], v[92:95], a[136:139]\n"
    "v_mfma_f32_16x16x32_bf16 a[140:143], v[176:179], v[96:99], a[140:143]\n"
    "v_exp_f32 v29, v29\n"
    "v_mfma_f32_16x16x32_bf16 a[140:143], v[172:175], v[80:83], a[140:143]\n"
    "v_mfma_f32_16x16x32_bf16 a[124:127], v[164:167], v[80:83], a[124:127]\n"
    "v_mfma_f32_16x16x32_bf16 a[124:127], v[168:171], v[96:99], a[124:127]\n"
    "v_exp_f32 v30, v30\n"
    "v_mfma_f32_16x16x32_bf16 a[108:111], v[160:163], v[96:99], a[108:111]\n"
    "v_mfma_f32_16x16x32_bf16 a[108:111], v[156:159], v[80:83], a[108:111]\n"
    "s_waitcnt lgkmcnt(0)\n"
    "v_exp_f32 v31, v31\n"
    "v_mfma_f32_16x16x32_bf16 a[144:147], v[180:183], v[68:71], a[144:147]\n"
    "ds_read_b128 v[156:159], v3 offset:12288\n"
    "v_mfma_f32_16x16x32_bf16 a[144:147], v[184:187], v[84:87], a[144:147]\n"
    "ds_read_b128 v[160:163], v3 offset:14336\n"
    "v_mfma_f32_16x16x32_bf16 a[160:163], v[192:195], v[84:87], a[160:163]\n"
    "ds_read_b128 v[164:167], v3 offset:16384\n"
    "v_mfma_f32_16x16x32_bf16 a[160:163], v[188:191], v[68:71], a[160:163]\n"
    "ds_read_b128 v[168:171], v3 offset:18432\n"
    "v_mfma_f32_16x16x32_bf16 a[176:179], v[196:199], v[68:71], a[176:179]\n"
    "ds_read_b128 v[172:175], v3 offset:20480\n"
    "v_mfma_f32_16x16x32_bf16 a[176:179], v[200:203], v[84:87], a[176:179]\n"
    "ds_read_b128 v[176:179], v3 offset:22528\n"
    "v_mfma_f32_16x16x32_bf16 a[180:183], v[200:203], v[88:91], a[180:183]\n"
    "ds_read_b128 v[100:103], v250 offset:896\n"
    "v_mfma_f32_16x16x32_bf16 a[180:183], v[196:199], v[72:75], a[180:183]\n"
    "v_exp_f32 v32, v32\n"
    "v_mfma_f32_16x16x32_bf16 a[164:167], v[188:191], v[72:75], a[164:167]\n"
    "v_exp_f32 v33, v33\n"
    "v_mfma_f32_16x16x32_bf16 a[164:167], v[192:195], v[88:91], a[164:167]\n"
    "v_exp_f32 v34, v34\n"
    "v_mfma_f32_16x16x32_bf16 a[148:151], v[184:187], v[88:91], a[148:151]\n"
    "v_mfma_f32_16x16x32_bf16 a[148:151], v[180:183], v[72:75], a[148:151]\n"
    "v_exp_f32 v35, v35\n"
    "v_mfma_f32_16x16x32_bf16 a[152:155], v[180:183], v[76:79], a[152:155]\n"
    "v_mfma_f32_16x16x32_bf16 a[152:155], v[184:187], v[92:95], a[152:155]\n"
    "v_mfma_f32_16x16x32_bf16 a[168:171], v[192:195], v[92:95], a[168:171]\n"
    "s_waitcnt vmcnt(10)\n"
    "ds_add_u32 v247, v246\n"
    "v_mfma_f32_16x16x32_bf16 a[168:171], v[188:191], v[76:79], a[168:171]\n"
    "v_mfma_f32_16x16x32_bf16 a[184:187], v[196:199], v[76:79], a[184:187]\n"
    "v_mfma_f32_16x16x32_bf16 a[184:187], v[200:203], v[92:95], a[184:187]\n"
    "v_mfma_f32_16x16x32_bf16 a[188:191], v[200:203], v[96:99], a[188:191]\n"
    "v_mfma_f32_16x16x32_bf16 a[188:191], v[196:199], v[80:83], a[188:191]\n"
    "v_mfma_f32_16x16x32_bf16 a[172:175], v[188:191], v[80:83], a[172:175]\n"
    "s_sub_u32 s74, s68, s65\n"
    "v_mfma_f32_16x16x32_bf16 a[172:175], v[192:195], v[96:99], a[172:175]\n"
    "s_add_u32 s66, s66, 1\n"
    "v_mov_b32 v2, v3\n"
    "v_add_u32 v3, s74, v1\n"
    "v_mfma_f32_16x16x32_bf16 a[156:159], v[184:187], v[96:99], a[156:159]\n"
    "s_waitcnt lgkmcnt(1)\n"
    "s_cmp_eq_u32 s66, 31\n"
    "v_mfma_f32_16x16x32_bf16 a[156:159], v[180:183], v[80:83], a[156:159]\n"
    "s_cbranch_scc1 .LtailP\n"
    ".LNV:\n"
    "v_mfma_f32_16x16x32_bf16 v[36:39], v[156:159], a[192:195], v[100:103]\n"
    "ds_read_b128 v[180:183], v2 offset:13312\n"
    "v_cvt_pk_bf16_f32 v68, v4, v5\n"
    "v_mfma_f32_16x16x32_bf16 v[36:39], v[160:163], a[196:199], v[36:39]\n"
    "ds_read_b128 v[184:187], v2 offset:15360\n"
    "v_mfma_f32_16x16x32_bf16 v[36:39], v[164:167], a[200:203], v[36:39]\n"
    "ds_read_b128 v[188:191], v2 offset:17408\n"
    "v_mfma_f32_16x16x32_bf16 v[36:39], v[168:171], a[204:207], v[36:39]\n"
    "ds_read_b128 v[192:195], v2 offset:19456\n"
    "v_cvt_pk_bf16_f32 v69, v6, v7\n"
    "v_mfma_f32_16x16x32_bf16 v[36:39], v[172:175], a[208:211], v[36:39]\n"
    "ds_read_b128 v[196:199], v2 offset:21504\n"
    "v_mfma_f32_16x16x32_bf16 v[36:39], v[176:179], a[212:215], v[36:39]\n"
    "ds_read_b128 v[200:203], v2 offset:23552\n"
    "v_mfma_f32_16x16x32_bf16 v[40:43], v[176:179], a[236:239], v[100:103]\n"
    "ds_read_b128 v[104:107], v250 offset:912\n"
    "v_mfma_f32_16x16x32_bf16 v[40:43], v[172:175], a[232:235], v[40:43]\n"
    "s_add_u32 s68, s68, 24576\n"
    "v_cvt_pk_bf16_f32 v72, v8, v9\n"
    "v_mfma_f32_16x16x32_bf16 v[40:43], v[168:171], a[228:231], v[40:43]\n"
    "s_cmp_ge_u32 s68, s69\n"
    "s_cselect_b32 s68, s65, s68\n"
    "v_mfma_f32_16x16x32_bf16 v[40:43], v[164:167], a[224:227], v[40:43]\n"
    "v_exp_f32 v36, v36\n"
    "v_mfma_f32_16x16x32_bf16 v[40:43], v[160:163], a[220:223], v[40:43]\n"
    "v_exp_f32 v37, v37\n"
    "v_mfma_f32_16x16x32_bf16 v[40:43], v[156:159], a[216:219], v[40:43]\n"
    "v_cvt_pk_bf16_f32 v73, v10, v11\n"
    "v_cvt_pk_bf16_f32 v84, v36, v37\n"
    "v_mfma_f32_16x16x32_bf16 v[44:47], v[156:159], a[240:243], v[100:103]\n"
    "v_mfma_f32_16x16x32_bf16 v[44:47], v[160:163], a[244:247], v[44:47]\n"
    "v_exp_f32 v38, v38\n"
    "v_mfma_f32_16x16x32_bf16 v[44:47], v[164:167], a[248:251], v[44:47]\n"
    "v_mfma_f32_16x16x32_bf16 v[44:47], v[168:171], a[252:255], v[44:47]\n"
    "v_cvt_pk_bf16_f32 v76, v12, v13\n"
    "v_mfma_f32_16x16x32_bf16 v[44:47], v[172:175], v[124:127], v[44:47]\n"
    "v_exp_f32 v39, v39\n"
    "v_mfma_f32_16x16x32_bf16 v[44:47], v[176:179], v[128:131], v[44:47]\n"
    "v_cvt_pk_bf16_f32 v85, v38, v39\n"
    "v_mfma_f32_16x16x32_bf16 v[48:51], v[176:179], v[152:155], v[100:103]\n"
    "ds_read_b32 v245, v247\n"
    "v_cvt_pk_bf16_f32 v77, v14, v15\n"
    "v_mfma_f32_16x16x32_bf16 v[48:51], v[172:175], v[148:151], v[48:51]\n"
    "s_add_u32 m0, s68, 0\n"
    "v_exp_f32 v40, v40\n"
    "v_mfma_f32_16x16x32_bf16 v[48:51], v[168:171], v[144:147], v[48:51]\n"
    "v_mfma_f32_16x16x32_bf16 v[48:51], v[164:167], v[140:143], v[48:51]\n"
    "v_mfma_f32_16x16x32_bf16 v[48:51], v[160:163], v[136:139], v[48:51]\n"
    "s_waitcnt vmcnt(0)\n"
    "v_cmp_eq_u64 s[54:55], v[236:237], v[238:239]\n"
    "v_cvt_pk_bf16_f32 v80, v16, v17\n"
    "v_mfma_f32_16x16x32_bf16 v[48:51], v[156:159], v[132:135], v[48:51]\n"
    "s_waitcnt lgkmcnt(1)\n"
    "v_exp_f32 v41, v41\n"
    "v_mfma_f32_16x16x32_bf16 v[52:55], v[180:183], a[192:195], v[104:107]\n"
    "s_waitcnt lgkmcnt(0)\n"
    "v_cmp_le_u32 s[56:57], s59, v245\n"
    "v_cvt_pk_bf16_f32 v88, v40, v41\n"
    "v_mfma_f32_16x16x32_bf16 v[52:55], v[184:187], a[196:199], v[52:55]\n"
    "v_exp_f32 v42, v42\n"
    "v_mfma_f32_16x16x32_bf16 v[52:55], v[188:191], a[200:203], v[52:55]\n"
    "s_and_b64 s[54:55], s[54:55], s[56:57]\n"
    "s_cmp_eq_u64 s[54:55], -1\n"
    "v_mfma_f32_16x16x32_bf16 v[52:55], v[192:195], a[204:207], v[52:55]\n"
    "s_cbranch_scc0 .Lpoll4\n"
    ".Lpoll4r:\n"
    "v_mfma_f32_16x16x32_bf16 v[52:55], v[196:199], a[208:211], v[52:55]\n"
    "buffer_load_dwordx4 v1, s[16:19], s67 offen offset:0 sc1 lds\n"
    "v_mfma_f32_16x16x32_bf16 v[52:55], v[200:203], a[212:215], v[52:55]\n"
    "ds_read_b128 v[156:159], v3 offset:0\n"
    "ds_read_b128 v[160:163], v3 offset:2048\n"
    "v_mfma_f32_16x16x32_bf16 v[56:59], v[200:203], a[236:239], v[104:107]\n"
    "ds_read_b128 v[164:167], v3 offset:4096\n"
    "ds_read_b128 v[168:171], v3 offset:6144\n"
    "v_mfma_f32_16x16x32_bf16 v[56:59], v[196:199], a[232:235], v[56:59]\n"
    "ds_read_b128 v[172:175], v3 offset:8192\n"
    "ds_read_b128 v[176:179], v3 offset:10240\n"
    "v_mfma_f32_16x16x32_bf16 v[56:59], v[192:195], a[228:231], v[56:59]\n"
    "buffer_load_dwordx4 v1, s[16:19], s67 offen offset:1024 sc1 lds\n"
    "v_mfma_f32_16x16x32_bf16 v[56:59], v[188:191], a[224:227], v[56:59]\n"
    "ds_read_b128 v[100:103], v250 offset:1536\n"
    "v_cvt_pk_bf16_f32 v81, v18, v19\n"
    "v_mfma_f32_16x16x32_bf16 v[56:59], v[184:187], a[220:223], v[56:59]\n"
    "v_cvt_pk_bf16_f32 v70, v20, v21\n"
    "v_cvt_pk_bf16_f32 v71, v22, v23\n"
    "v_mfma_f32_16x16x32_bf16 v[56:59], v[180:183], a[216:219], v[56:59]\n"
    "v_exp_f32 v43, v43\n"
    "v_mfma_f32_16x16x32_bf16 v[60:63], v[180:183], a[240:243], v[104:107]\n"
    "buffer_load_dwordx4 v1, s[16:19], s67 offen offset:2048 sc1 lds\n"
    "v_mfma_f32_16x16x32_bf16 v[60:63], v[184:187], a[244:247], v[60:63]\n"
    "v_cvt_pk_bf16_f32 v74, v24, v25\n"
    "v_cvt_pk_bf16_f32 v89, v42, v43\n"
    "v_mfma_f32_16x16x32_bf16 v[60:63], v[188:191], a[248:251], v[60:63]\n"
    "v_exp_f32 v44, v44\n"
    "v_mfma_f32_16x16x32_bf16 v[60:63], v[192:195], a[252:255], v[60:63]\n"
    "v_exp_f32 v45, v45\n"
    "v_mfma_f32_16x16x32_bf16 v[60:63], v[196:199], v[124:127], v[60:63]\n"
    "buffer_load_dwordx4 v1, s[16:19], s67 offen offset:3072 sc1 lds\n"
    "v_mfma_f32_16x16x32_bf16 v[60:63], v[200:203], v[128:131], v[60:63]\n"
    "s_add_u32 m0, s68, 4096\n"
    "v_cvt_pk_bf16_f32 v75, v26, v27\n"
    "v_cvt_pk_bf16_f32 v92, v44, v45\n"
    "v_mfma_f32_16x16x32_bf16 v[64:67], v[200:203], v[152:155], v[104:107]\n"
    "s_add_u32 s83, s67, 4096\n"
    "v_exp_f32 v46, v46\n"
    "v_mfma_f32_16x16x32_bf16 v[64:67], v[196:199], v[148:151], v[64:67]\n"
    "v_exp_f32 v47, v47\n"
    "v_mfma_f32_16x16x32_bf16 v[64:67], v[192:195], v[144:147], v[64:67]\n"
    "buffer_load_dwordx4 v1, s[16:19], s83 offen offset:0 sc1 lds\n"
    "v_mfma_f32_16x16x32_bf16 v[64:67], v[188:191], v[140:143], v[64:67]\n"
    "v_cvt_pk_bf16_f32 v78, v28, v29\n"
    "v_cvt_pk_bf16_f32 v93, v46, v47\n"
    "v_mfma_f32_16x16x32_bf16 v[64:67], v[184:187], v[136:139], v[64:67]\n"
    "v_exp_f32 v48, v48\n"
    "v_mfma_f32_16x16x32_bf16 v[64:67], v[180:183], v[132:135], v[64:67]\n"
    "s_waitcnt lgkmcnt(0)\n"
    "v_exp_f32 v49, v49\n"
    "v_mfma_f32_16x16x32_bf16 v[4:7], v[156:159], a[192:195], v[100:103]\n"
    "buffer_load_dwordx4 v1, s[16:19], s83 offen offset:1024 sc1 lds\n"
    "v_mfma_f32_16x16x32_bf16 v[4:7], v[160:163], a[196:199], v[4:7]\n"
    "s_add_u32 m0, s70, 0\n"
    "ds_read_b128 v[180:183], v3 offset:1024\n"
    "ds_read_b128 v[184:187], v3 offset:3072\n"
    "v_mfma_f32_16x16x32_bf16 v[4:7], v[164:167], a[200:203], v[4:7]\n"
    "s_add_u32 s83, s67, 24576\n"
    "ds_read_b128 v[188:191], v3 offset:5120\n"
    "v_cvt_pk_bf16_f32 v79, v30, v31\n"
    "v_mfma_f32_16x16x32_bf16 v[4:7], v[168:171], a[204:207], v[4:7]\n"
    "ds_read_b128 v[192:195], v3 offset:7168\n"
    "v_cvt_pk_bf16_f32 v96, v48, v49\n"
    "v_mfma_f32_16x16x32_bf16 v[4:7], v[172:175], a[208:211], v[4:7]\n"
    "buffer_load_dwordx4 v1, s[16:19], s83 offen offset:0 sc1 lds\n"
    "v_mfma_f32_16x16x32_bf16 v[4:7], v[176:179], a[212:215], v[4:7]\n"
    "s_add_u32 s76, s71, s60\n"
    "ds_read_b128 v[196:199], v3 offset:9216\n"
    "ds_read_b128 v[200:203], v3 offset:11264\n"
    "v_mfma_f32_16x16x32_bf16 v[8:11], v[176:179], a[236:239], v[100:103]\n"
    "s_add_u32 s77, s71, s61\n"
    "ds_read_b128 v[104:107], v250 offset:1552\n"
    "v_cvt_pk_bf16_f32 v82, v32, v33\n"
    "v_mfma_f32_16x16x32_bf16 v[8:11], v[172:175], a[232:235], v[8:11]\n"
    "v_exp_f32 v52, v52\n"
    "v_mfma_f32_16x16x32_bf16 v[8:11], v[168:171], a[228:231], v[8:11]\n"
    "buffer_load_dwordx4 v1, s[16:19], s83 offen offset:1024 sc1 lds\n"
    "v_mfma_f32_16x16x32_bf16 v[8:11], v[164:167], a[224:227], v[8:11]\n"
    "s_add_u32 s78, s71, s62\n"
    "v_cvt_pk_bf16_f32 v83, v34, v35\n"
    "v_mfma_f32_16x16x32_bf16 v[8:11], v[160:163], a[220:223], v[8:11]\n"
    "v_exp_f32 v53, v53\n"
    "v_mfma_f32_16x16x32_bf16 v[8:11], v[156:159], a[216:219], v[8:11]\n"
    "v_exp_f32 v54, v54\n"
    "v_mfma_f32_16x16x32_bf16 v[12:15], v[156:159], a[240:243], v[100:103]\n"
    "buffer_load_dwordx4 v1, s[16:19], s83 offen offset:2048 sc1 lds\n"
    "v_mfma_f32_16x16x32_bf16 v[12:15], v[160:163], a[244:247], v[12:15]\n"
    "s_waitcnt vmcnt(10)\n"
    "v_cvt_pk_bf16_f32 v86, v52, v53\n"
    "v_cvt_f32_i32 v204, v204\n"
    "v_mfma_f32_16x16x32_bf16 v[12:15], v[164:167], a[248:251], v[12:15]\n"
    "v_exp_f32 v55, v55\n"
    "v_mfma_f32_16x16x32_bf16 v[12:15], v[168:171], a[252:255], v[12:15]\n"
    "v_cvt_pk_bf16_f32 v87, v54, v55\n"
    "v_cvt_f32_i32 v208, v208\n"
    "v_mfma_f32_16x16x32_bf16 v[12:15], v[172:175], v[124:127], v[12:15]\n"
    "buffer_load_dwordx4 v1, s[16:19], s83 offen offset:3072 sc1 lds\n"
    "v_mfma_f32_16x16x32_bf16 v[12:15], v[176:179], v[128:131], v[12:15]\n"
    "s_add_u32 m0, s70, 4096\n"
    "v_exp_f32 v56, v56\n"
    "v_mfma_f32_16x16x32_bf16 v[16:19], v[176:179], v[152:155], v[100:103]\n"
    "s_add_u32 s83, s67, 28672\n"
    "v_exp_f32 v50, v50\n"
    "v_mfma_f32_16x16x32_bf16 v[16:19], v[172:175], v[148:151], v[16:19]\n"
    "v_exp_f32 v51, v51\n"
    "v_mfma_f32_16x16x32_bf16 v[16:19], v[168:171], v[144:147], v[16:19]\n"
    "buffer_load_dwordx4 v1, s[16:19], s83 offen offset:0 sc1 lds\n"
    "v_mfma_f32_16x16x32_bf16 v[16:19], v[164:167], v[140:143], v[16:19]\n"
    "v_exp_f32 v57, v57\n"
    "v_mfma_f32_16x16x32_bf16 v[16:19], v[160:163], v[136:139], v[16:19]\n"
    "v_cvt_pk_bf16_f32 v90, v56, v57\n"
    "v_cvt_pk_bf16_f32 v97, v50, v51\n"
    "v_mfma_f32_16x16x32_bf16 v[16:19], v[156:159], v[132:135], v[16:19]\n"
    "s_waitcnt lgkmcnt(0)\n"
    "v_exp_f32 v58, v58\n"
    "v_mfma_f32_16x16x32_bf16 v[20:23], v[180:183], a[192:195], v[104:107]\n"
    "buffer_load_dwordx4 v1, s[16:19], s83 offen offset:1024 sc1 lds\n"
    "v_mfma_f32_16x16x32_bf16 v[20:23], v[184:187], a[196:199], v[20:23]\n"
    "s_add_u32 s67, s67, 49152\n"
    "ds_read_b128 v[156:159], v249 offset:0\n"
    "ds_read_b128 v[160:163], v249 offset:12288\n"
    "v_mfma_f32_16x16x32_bf16 v[20:23], v[188:191], a[200:203], v[20:23]\n"
    "s_add_u32 s59, s59, 4\n"
    "ds_read_b128 v[164:167], v249 offset:1024\n"
    "v_cvt_f32_i32 v205, v205\n"
    "v_mfma_f32_16x16x32_bf16 v[20:23], v[192:195], a[204:207], v[20:23]\n"
    "s_add_u32 s81, s81, 128\n"
    "ds_read_b128 v[168:171], v249 offset:13312\n"
    "v_cvt_f32_i32 v209, v209\n"
    "v_mfma_f32_16x16x32_bf16 v[20:23], v[196:199], a[208:211], v[20:23]\n"
    "s_min_u32 s81, s81, 4096\n"
    "ds_read_b128 v[172:175], v249 offset:2048\n"
    "v_cvt_f32_i32 v206, v206\n"
    "v_mfma_f32_16x16x32_bf16 v[20:23], v[200:203], a[212:215], v[20:23]\n"
    "ds_read_b128 v[176:179], v249 offset:14336\n"
    "v_cvt_f32_i32 v210, v210\n"
    "v_mfma_f32_16x16x32_bf16 v[24:27], v[200:203], a[236:239], v[104:107]\n"
    "ds_read_b128 v[100:103], v251 offset:768\n"
    "v_cvt_pk_bf16_f32 v207, v204, v208\n"
    "v_mfma_f32_16x16x32_bf16 v[24:27], v[196:199], a[232:235], v[24:27]\n"
    "v_exp_f32 v59, v59\n"
    "v_mfma_f32_16x16x32_bf16 v[24:27], v[192:195], a[228:231], v[24:27]\n"
    "v_cvt_pk_bf16_f32 v91, v58, v59\n"
    "v_cvt_pk_bf16_f32 v211, v205, v209\n"
    "v_mfma_f32_16x16x32_bf16 v[24:27], v[188:191], a[224:227], v[24:27]\n"
    "v_exp_f32 v60, v60\n"
    "v_mfma_f32_16x16x32_bf16 v[24:27], v[184:187], a[220:223], v[24:27]\n"
    "v_exp_f32 v61, v61\n"
    "v_mfma_f32_16x16x32_bf16 v[24:27], v[180:183], a[216:219], v[24:27]\n"
    "v_exp_f32 v62, v62\n"
    "v_mfma_f32_16x16x32_bf16 v[28:31], v[180:183], a[240:243], v[104:107]\n"
    "v_cvt_pk_bf16_f32 v94, v60, v61\n"
    "v_cvt_pk_bf16_f32 v215, v206, v210\n"
    "v_mfma_f32_16x16x32_bf16 v[28:31], v[184:187], a[244:247], v[28:31]\n"
    "v_exp_f32 v63, v63\n"
    "v_mfma_f32_16x16x32_bf16 v[28:31], v[188:191], a[248:251], v[28:31]\n"
    "v_cvt_pk_bf16_f32 v95, v62, v63\n"
    "v_cvt_f32_i32 v212, v212\n"
    "v_mfma_f32_16x16x32_bf16 v[28:31], v[192:195], a[252:255], v[28:31]\n"
    "v_cvt_f32_i32 v216, v216\n"
    "v_mfma_f32_16x16x32_bf16 v[28:31], v[196:199], v[124:127], v[28:31]\n"
    "v_exp_f32 v64, v64\n"
    "v_mfma_f32_16x16x32_bf16 v[28:31], v[200:203], v[128:131], v[28:31]\n"
    "v_mfma_f32_16x16x32_bf16 v[32:35], v[200:203], v[152:155], v[104:107]\n"
    "v_cvt_f32_i32 v213, v213\n"
    "v_mfma_f32_16x16x32_bf16 v[32:35], v[196:199], v[148:151], v[32:35]\n"
    "v_exp_f32 v65, v65\n"
    "v_mfma_f32_16x16x32_bf16 v[32:35], v[192:195], v[144:147], v[32:35]\n"
    "v_cvt_pk_bf16_f32 v98, v64, v65\n"
    "v_cvt_f32_i32 v217, v217\n"
    "v_mfma_f32_16x16x32_bf16 v[32:35], v[188:191], v[140:143], v[32:35]\n"
    "v_mfma_f32_16x16x32_bf16 v[32:35], v[184:187], v[136:139], v[32:35]\n"
    "v_exp_f32 v66, v66\n"
    "v_mfma_f32_16x16x32_bf16 v[32:35], v[180:183], v[132:135], v[32:35]\n"
    "s_waitcnt lgkmcnt(0)\n"
    "v_cvt_f32_i32 v214, v214\n"
    "v_mfma_f32_16x16x32_bf16 a[0:3], v[156:159], v[68:71], a[0:3]\n"
    "ds_read_b128 v[180:183], v249 offset:3072\n"
    "v_mfma_f32_16x16x32_bf16 a[0:3], v[160:163], v[84:87], a[0:3]\n"
    "ds_read_b128 v[184:187], v249 offset:15360\n"
    "v_mfma_f32_16x16x32_bf16 a[16:19], v[168:171], v[84:87], a[16:19]\n"
    "ds_read_b128 v[188:191], v249 offset:4096\n"
    "v_cvt_f32_i32 v218, v218\n"
    "v_mfma_f32_16x16x32_bf16 a[16:19], v[164:167], v[68:71], a[16:19]\n"
    "ds_read_b128 v[192:195], v249 offset:16384\n"
    "v_mfma_f32_16x16x32_bf16 a[32:35], v[172:175], v[68:71], a[32:35]\n"
    "ds_read_b128 v[196:199], v249 offset:5120\n"
    "v_cvt_pk_bf16_f32 v206, v212, v216\n"
    "v_mfma_f32_16x16x32_bf16 a[32:35], v[176:179], v[84:87], a[32:35]\n"
    "ds_read_b128 v[200:203], v249 offset:17408\n"
    "v_mfma_f32_16x16x32_bf16 a[36:39], v[176:179], v[88:91], a[36:39]\n"
    "ds_read_b128 v[104:107], v251 offset:832\n"
    "v_mfma_f32_16x16x32_bf16 a[36:39], v[172:175], v[72:75], a[36:39]\n"
    "v_exp_f32 v67, v67\n"
    "v_mfma_f32_16x16x32_bf16 a[20:23], v[164:167], v[72:75], a[20:23]\n"
    "v_cvt_pk_bf16_f32 v99, v66, v67\n"
    "v_cvt_pk_bf16_f32 v210, v213, v217\n"
    "v_mfma_f32_16x16x32_bf16 a[20:23], v[168:171], v[88:91], a[20:23]\n"
    "v_exp_f32 v4, v4\n"
    "v_mfma_f32_16x16x32_bf16 a[4:7], v[160:163], v[88:91], a[4:7]\n"
    "v_exp_f32 v5, v5\n"
    "v_mfma_f32_16x16x32_bf16 a[4:7], v[156:159], v[72:75], a[4:7]\n"
    "v_exp_f32 v6, v6\n"
    "v_mfma_f32_16x16x32_bf16 a[8:11], v[156:159], v[76:79], a[8:11]\n"
    "v_exp_f32 v7, v7\n"
    "v_mfma_f32_16x16x32_bf16 a[8:11], v[160:163], v[92:95], a[8:11]\n"
    "v_cvt_pk_bf16_f32 v214, v214, v218\n"
    "v_cvt_f32_i32 v220, v220\n"
    "v_mfma_f32_16x16x32_bf16 a[24:27], v[168:171], v[92:95], a[24:27]\n"
    "v_mfma_f32_16x16x32_bf16 a[24:27], v[164:167], v[76:79], a[24:27]\n"
    "v_exp_f32 v8, v8\n"
    "v_mfma_f32_16x16x32_bf16 a[40:43], v[172:175], v[76:79], a[40:43]\n"
    "v_cvt_f32_i32 v224, v224\n"
    "v_mfma_f32_16x16x32_bf16 a[40:43], v[176:179], v[92:95], a[40:43]\n"
    "v_mfma_f32_16x16x32_bf16 a[44:47], v[176:179], v[96:99], a[44:47]\n"
    "v_exp_f32 v9, v9\n"
    "v_mfma_f32_16x16x32_bf16 a[44:47], v[172:175], v[80:83], a[44:47]\n"
    "v_cvt_f32_i32 v221, v221\n"
    "v_mfma_f32_16x16x32_bf16 a[28:31], v[164:167], v[80:83], a[28:31]\n"
    "v_exp_f32 v10, v10\n"
    "v_mfma_f32_16x16x32_bf16 a[28:31], v[168:171], v[96:99], a[28:31]\n"
    "v_cvt_f32_i32 v225, v225\n"
    "v_mfma_f32_16x16x32_bf16 a[12:15], v[160:163], v[96:99], a[12:15]\n"
    "v_mfma_f32_16x16x32_bf16 a[12:15], v[156:159], v[80:83], a[12:15]\n"
    "s_waitcnt lgkmcnt(0)\n"
    "v_exp_f32 v11, v11\n"
    "v_mfma_f32_16x16x32_bf16 a[48:51], v[180:183], v[68:71], a[48:51]\n"
    "ds_read_b128 v[156:159], v249 offset:6144\n"
    "v_cvt_f32_i32 v222, v222\n"
    "v_mfma_f32_16x16x32_bf16 a[48:51], v[184:187], v[84:87], a[48:51]\n"
    "ds_read_b128 v[160:163], v249 offset:18432\n"
    "v_mfma_f32_16x16x32_bf16 a[64:67], v[192:195], v[84:87], a[64:67]\n"
    "ds_read_b128 v[164:167], v249 offset:7168\n"
    "v_cvt_f32_i32 v226, v226\n"
    "v_mfma_f32_16x16x32_bf16 a[64:67], v[188:191], v[68:71], a[64:67]\n"
    "ds_read_b128 v[168:171], v249 offset:19456\n"
    "v_mfma_f32_16x16x32_bf16 a[80:83], v[196:199], v[68:71], a[80:83]\n"
    "ds_read_b128 v[172:175], v249 offset:8192\n"
    "v_mfma_f32_16x16x32_bf16 a[80:83], v[200:203], v[84:87], a[80:83]\n"
    "ds_read_b128 v[176:179], v249 offset:20480\n"
    "v_cvt_pk_bf16_f32 v205, v220, v224\n"
    "v_mfma_f32_16x16x32_bf16 v[108:111], v[104:107], v[84:87], v[108:111]\n"
    "v_exp_f32 v12, v12\n"
    "v_mfma_f32_16x16x32_bf16 v[108:111], v[100:103], v[68:71], v[108:111]\n"
    "v_exp_f32 v13, v13\n"
    "v_mfma_f32_16x16x32_bf16 v[112:115], v[100:103], v[72:75], v[112:115]\n"
    "v_exp_f32 v14, v14\n"
    "v_mfma_f32_16x16x32_bf16 v[112:115], v[104:107], v[88:91], v[112:115]\n"
    "v_cvt_pk_bf16_f32 v209, v221, v225\n"
    "v_mfma_f32_16x16x32_bf16 a[84:87], v[200:203], v[88:91], a[84:87]\n"
    "v_exp_f32 v15, v15\n"
    "v_mfma_f32_16x16x32_bf16 a[84:87], v[196:199], v[72:75], a[84:87]\n"
    "v_cvt_pk_bf16_f32 v213, v222, v226\n"
    "v_mfma_f32_16x16x32_bf16 a[68:71], v[188:191], v[72:75], a[68:71]\n"
    "v_mfma_f32_16x16x32_bf16 a[68:71], v[192:195], v[88:91], a[68:71]\n"
    "v_exp_f32 v16, v16\n"
    "v_mfma_f32_16x16x32_bf16 a[52:55], v[184:187], v[88:91], a[52:55]\n"
    "v_cvt_f32_i32 v228, v228\n"
    "v_mfma_f32_16x16x32_bf16 a[52:55], v[180:183], v[72:75], a[52:55]\n"
    "v_mfma_f32_16x16x32_bf16 a[56:59], v[180:183], v[76:79], a[56:59]\n"
    "v_exp_f32 v17, v17\n"
    "v_mfma_f32_16x16x32_bf16 a[56:59], v[184:187], v[92:95], a[56:59]\n"
    "v_cvt_f32_i32 v232, v232\n"
    "v_mfma_f32_16x16x32_bf16 a[72:75], v[192:195], v[92:95], a[72:75]\n"
    "v_cvt_f32_i32 v229, v229\n"
    "v_mfma_f32_16x16x32_bf16 a[72:75], v[188:191], v[76:79], a[72:75]\n"
    "v_exp_f32 v18, v18\n"
    "v_mfma_f32_16x16x32_bf16 a[88:91], v[196:199], v[76:79], a[88:91]\n"
    "v_mfma_f32_16x16x32_bf16 a[88:91], v[200:203], v[92:95], a[88:91]\n"
    "v_cvt_f32_i32 v233, v233\n"
    "v_mfma_f32_16x16x32_bf16 v[116:119], v[104:107], v[92:95], v[116:119]\n"
    "v_exp_f32 v19, v19\n"
    "v_mfma_f32_16x16x32_bf16 v[116:119], v[100:103], v[76:79], v[116:119]\n"
    "v_mfma_f32_16x16x32_bf16 v[120:123], v[100:103], v[80:83], v[120:123]\n"
    "v_cvt_f32_i32 v230, v230\n"
    "v_mfma_f32_16x16x32_bf16 v[120:123], v[104:107], v[96:99], v[120:123]\n"
    "v_exp_f32 v20, v20\n"
    "v_mfma_f32_16x16x32_bf16 a[92:95], v[200:203], v[96:99], a[92:95]\n"
    "v_mfma_f32_16x16x32_bf16 a[92:95], v[196:199], v[80:83], a[92:95]\n"
    "v_exp_f32 v21, v21\n"
    "v_mfma_f32_16x16x32_bf16 a[76:79], v[188:191], v[80:83], a[76:79]\n"
    "v_cvt_f32_i32 v234, v234\n"
    "v_mfma_f32_16x16x32_bf16 a[76:79], v[192:195], v[96:99], a[76:79]\n"
    "v_cvt_pk_bf16_f32 v204, v228, v232\n"
    "v_mfma_f32_16x16x32_bf16 a[60:63], v[184:187], v[96:99], a[60:63]\n"
    "v_exp_f32 v22, v22\n"
    "v_mfma_f32_16x16x32_bf16 a[60:63], v[180:183], v[80:83], a[60:63]\n"
    "s_waitcnt lgkmcnt(0)\n"
    "v_mfma_f32_16x16x32_bf16 a[96:99], v[156:159], v[68:71], a[96:99]\n"
    "ds_read_b128 v[180:183], v249 offset:9216\n"
    "v_cvt_pk_bf16_f32 v208, v229, v233\n"
    "v_mfma_f32_16x16x32_bf16 a[96:99], v[160:163], v[84:87], a[96:99]\n"
    "ds_read_b128 v[184:187], v249 offset:21504\n"
    "v_mfma_f32_16x16x32_bf16 a[112:115], v[168:171], v[84:87], a[112:115]\n"
    "ds_read_b128 v[188:191], v249 offset:10240\n"
    "v_mfma_f32_16x16x32_bf16 a[112:115], v[164:167], v[68:71], a[112:115]\n"
    "ds_read_b128 v[192:195], v249 offset:22528\n"
    "v_cvt_pk_bf16_f32 v212, v230, v234\n"
    "v_mfma_f32_16x16x32_bf16 a[128:131], v[172:175], v[68:71], a[128:131]\n"
    "buffer_load_dwordx2 v[236:237], v241, s[36:39], s81 offen offset:0 sc1\n"
    "v_mfma_f32_16x16x32_bf16 a[128:131], v[176:179], v[84:87], a[128:131]\n"
    "ds_read_b128 v[196:199], v249 offset:11264\n"
    "ds_read_b128 v[200:203], v249 offset:23552\n"
    "v_mfma_f32_16x16x32_bf16 a[132:135], v[176:179], v[88:91], a[132:135]\n"
    "v_exp_f32 v23, v23\n"
    "v_mfma_f32_16x16x32_bf16 a[132:135], v[172:175], v[72:75], a[132:135]\n"
    "v_exp_f32 v24, v24\n"
    "v_mfma_f32_16x16x32_bf16 a[116:119], v[164:167], v[72:75], a[116:119]\n"
    "v_exp_f32 v25, v25\n"
    "v_mfma_f32_16x16x32_bf16 a[116:119], v[168:171], v[88:91], a[116:119]\n"
    "v_exp_f32 v26, v26\n"
    "v_mfma_f32_16x16x32_bf16 a[100:103], v[160:163], v[88:91], a[100:103]\n"
    "v_mfma_f32_16x16x32_bf16 a[100:103], v[156:159], v[72:75], a[100:103]\n"
    "v_mfma_f32_16x16x32_bf16 a[104:107], v[156:159], v[76:79], a[104:107]\n"
    "v_exp_f32 v27, v27\n"
    "v_mfma_f32_16x16x32_bf16 a[104:107], v[160:163], v[92:95], a[104:107]\n"
    "v_mfma_f32_16x16x32_bf16 a[120:123], v[168:171], v[92:95], a[120:123]\n"
    "v_mfma_f32_16x16x32_bf16 a[120:123], v[164:167], v[76:79], a[120:123]\n"
    "v_exp_f32 v28, v28\n"
    "v_mfma_f32_16x16x32_bf16 a[136:139], v[172:175], v[76:79], a[136:139]\n"
    "v_mfma_f32_16x16x32_bf16 a[136:139], v[176:179], v[92:95], a[136:139]\n"
    "v_mfma_f32_16x16x32_bf16 a[140:143], v[176:179], v[96:99], a[140:143]\n"
    "buffer_store_dwordx4 v[204:207], v244, s[16:19], s76 offen offset:0 sc1\n"
    "v_mfma_f32_16x16x32_bf16 a[140:143], v[172:175], v[80:83], a[140:143]\n"
    "v_exp_f32 v29, v29\n"
    "v_mfma_f32_16x16x32_bf16 a[124:127], v[164:167], v[80:83], a[124:127]\n"
    "buffer_store_dwordx4 v[208:211], v244, s[16:19], s77 offen offset:0 sc1\n"
    "v_mfma_f32_16x16x32_bf16 a[124:127], v[168:171], v[96:99], a[124:127]\n"
    "v_exp_f32 v30, v30\n"
    "v_mfma_f32_16x16x32_bf16 a[108:111], v[160:163], v[96:99], a[108:111]\n"
    "buffer_store_dwordx4 v[212:215], v244, s[16:19], s78 offen offset:0 sc1\n"
    "v_mfma_f32_16x16x32_bf16 a[108:111], v[156:159], v[80:83], a[108:111]\n"
    "s_waitcnt lgkmcnt(0)\n"
    "v_exp_f32 v31, v31\n"
    "v_mfma_f32_16x16x32_bf16 a[144:147], v[180:183], v[68:71], a[144:147]\n"
    "ds_read_b128 v[156:159], v3 offset:12288\n"
    "v_mfma_f32_16x16x32_bf16 a[144:147], v[184:187], v[84:87], a[144:147]\n"
    "ds_read_b128 v[160:163], v3 offset:14336\n"
    "v_mfma_f32_16x16x32_bf16 a[160:163], v[192:195], v[84:87], a[160:163]\n"
    "ds_read_b128 v[164:167], v3 offset:16384\n"
    "v_mfma_f32_16x16x32_bf16 a[160:163], v[188:191], v[68:71], a[160:163]\n"
    "ds_read_b128 v[168:171], v3 offset:18432\n"
    "v_mfma_f32_16x16x32_bf16 a[176:179], v[196:199], v[68:71], a[176:179]\n"
    "ds_read_b128 v[172:175], v3 offset:20480\n"
    "v_mfma_f32_16x16x32_bf16 a[176:179], v[200:203], v[84:87], a[176:179]\n"
    "ds_read_b128 v[176:179], v3 offset:22528\n"
    "v_mfma_f32_16x16x32_bf16 a[180:183], v[200:203], v[88:91], a[180:183]\n"
    "ds_read_b128 v[100:103], v250 offset:1664\n"
    "v_mfma_f32_16x16x32_bf16 a[180:183], v[196:199], v[72:75], a[180:183]\n"
    "v_exp_f32 v32, v32\n"
    "v_mfma_f32_16x16x32_bf16 a[164:167], v[188:191], v[72:75], a[164:167]\n"
    "v_exp_f32 v33, v33\n"
    "v_mfma_f32_16x16x32_bf16 a[164:167], v[192:195], v[88:91], a[164:167]\n"
    "v_exp_f32 v34, v34\n"
    "v_mfma_f32_16x16x32_bf16 a[148:151], v[184:187], v[88:91], a[148:151]\n"
    "v_mfma_f32_16x16x32_bf16 a[148:151], v[180:183], v[72:75], a[148:151]\n"
    "v_exp_f32 v35, v35\n"
    "v_mfma_f32_16x16x32_bf16 a[152:155], v[180:183], v[76:79], a[152:155]\n"
    "v_mfma_f32_16x16x32_bf16 a[152:155], v[184:187], v[92:95], a[152:155]\n"
    "v_mfma_f32_16x16x32_bf16 a[168:171], v[192:195], v[92:95], a[168:171]\n"
    "s_waitcnt vmcnt(4)\n"
    "ds_add_u32 v247, v246\n"
    "v_mfma_f32_16x16x32_bf16 a[168:171], v[188:191], v[76:79], a[168:171]\n"
    "v_mfma_f32_16x16x32_bf16 a[184:187], v[196:199], v[76:79], a[184:187]\n"
    "v_mfma_f32_16x16x32_bf16 a[184:187], v[200:203], v[92:95], a[184:187]\n"
    "v_mfma_f32_16x16x32_bf16 a[188:191], v[200:203], v[96:99], a[188:191]\n"
    "v_mfma_f32_16x16x32_bf16 a[188:191], v[196:199], v[80:83], a[188:191]\n"
    "v_mfma_f32_16x16x32_bf16 a[172:175], v[188:191], v[80:83], a[172:175]\n"
    "s_sub_u32 s74, s68, s65\n"
    "v_add_u32 v250, 1536, v250\n"
    "v_add_u32 v251, 1536, v251\n"
    "v_mfma_f32_16x16x32_bf16 a[172:175], v[192:195], v[96:99], a[172:175]\n"
    "s_add_u32 s66, s66, 1\n"
    "v_mov_b32 v2, v3\n"
    "v_add_u32 v3, s74, v1\n"
    "v_mfma_f32_16x16x32_bf16 a[156:159], v[184:187], v[96:99], a[156:159]\n"
    "s_waitcnt lgkmcnt(1)\n"
    "s_cmp_lt_u32 s66, 31\n"
    "v_mfma_f32_16x16x32_bf16 a[156:159], v[180:183], v[80:83], a[156:159]\n"
    "s_cbranch_scc1 .LPV\n"
    "s_branch .Ltail\n"
    ".LtailP:\n"
    "v_mov_b32 v248, v249\n"
    "v_add_u32 v250, 768, v250\n"
    "v_add_u32 v251, 768, v251\n"
    ".Ltail:\n"
    "v_mfma_f32_16x16x32_bf16 v[36:39], v[156:159], a[192:195], v[100:103]\n"
    "ds_read_b128 v[180:183], v2 offset:13312\n"
    "v_cvt_pk_bf16_f32 v68, v4, v5\n"
    "v_mfma_f32_16x16x32_bf16 v[36:39], v[160:163], a[196:199], v[36:39]\n"
    "ds_read_b128 v[184:187], v2 offset:15360\n"
    "v_mfma_f32_16x16x32_bf16 v[36:39], v[164:167], a[200:203], v[36:39]\n"
    "ds_read_b128 v[188:191], v2 offset:17408\n"
    "v_mfma_f32_16x16x32_bf16 v[36:39], v[168:171], a[204:207], v[36:39]\n"
    "ds_read_b128 v[192:195], v2 offset:19456\n"
    "v_cvt_pk_bf16_f32 v69, v6, v7\n"
    "v_mfma_f32_16x16x32_bf16 v[36:39], v[172:175], a[208:211], v[36:39]\n"
    "ds_read_b128 v[196:199], v2 offset:21504\n"
    "v_mfma_f32_16x16x32_bf16 v[36:39], v[176:179], a[212:215], v[36:39]\n"
    "ds_read_b128 v[200:203], v2 offset:23552\n"
    "v_mfma_f32_16x16x32_bf16 v[40:43], v[176:179], a[236:239], v[100:103]\n"
    "ds_read_b128 v[104:107], v250 offset:144\n"
    "v_mfma_f32_16x16x32_bf16 v[40:43], v[172:175], a[232:235], v[40:43]\n"
    "v_cvt_pk_bf16_f32 v72, v8, v9\n"
    "v_mfma_f32_16x16x32_bf16 v[40:43], v[168:171], a[228:231], v[40:43]\n"
    "v_mfma_f32_16x16x32_bf16 v[40:43], v[164:167], a[224:227], v[40:43]\n"
    "s_nop 0\n"
    "v_exp_f32 v36, v36\n"
    "v_mfma_f32_16x16x32_bf16 v[40:43], v[160:163], a[220:223], v[40:43]\n"
    "v_exp_f32 v37, v37\n"
    "v_mfma_f32_16x16x32_bf16 v[40:43], v[156:159], a[216:219], v[40:43]\n"
    "v_cvt_pk_bf16_f32 v73, v10, v11\n"
    "v_cvt_pk_bf16_f32 v84, v36, v37\n"
    "v_mfma_f32_16x16x32_bf16 v[44:47], v[156:159], a[240:243], v[100:103]\n"
    "v_exp_f32 v38, v38\n"
    "v_mfma_f32_16x16x32_bf16 v[44:47], v[160:163], a[244:247], v[44:47]\n"
    "v_exp_f32 v39, v39\n"
    "v_mfma_f32_16x16x32_bf16 v[44:47], v[164:167], a[248:251], v[44:47]\n"
    "v_cvt_pk_bf16_f32 v85, v38, v39\n"
    "v_mfma_f32_16x16x32_bf16 v[44:47], v[168:171], a[252:255], v[44:47]\n"
    "v_cvt_pk_bf16_f32 v76, v12, v13\n"
    "v_mfma_f32_16x16x32_bf16 v[44:47], v[172:175], v[124:127], v[44:47]\n"
    "v_exp_f32 v40, v40\n"
    "v_mfma_f32_16x16x32_bf16 v[44:47], v[176:179], v[128:131], v[44:47]\n"
    "v_exp_f32 v41, v41\n"
    "v_mfma_f32_16x16x32_bf16 v[48:51], v[176:179], v[152:155], v[100:103]\n"
    "ds_read_b32 v245, v247\n"
    "v_cvt_pk_bf16_f32 v77, v14, v15\n"
    "v_mfma_f32_16x16x32_bf16 v[48:51], v[172:175], v[148:151], v[48:51]\n"
    "v_exp_f32 v42, v42\n"
    "v_mfma_f32_16x16x32_bf16 v[48:51], v[168:171], v[144:147], v[48:51]\n"
    "v_exp_f32 v43, v43\n"
    "v_mfma_f32_16x16x32_bf16 v[48:51], v[164:167], v[140:143], v[48:51]\n"
    "v_cvt_pk_bf16_f32 v88, v40, v41\n"
    "v_cvt_pk_bf16_f32 v89, v42, v43\n"
    "v_mfma_f32_16x16x32_bf16 v[48:51], v[160:163], v[136:139], v[48:51]\n"
    "v_cvt_pk_bf16_f32 v80, v16, v17\n"
    "v_mfma_f32_16x16x32_bf16 v[48:51], v[156:159], v[132:135], v[48:51]\n"
    "s_waitcnt lgkmcnt(1)\n"
    "v_exp_f32 v44, v44\n"
    "v_mfma_f32_16x16x32_bf16 v[52:55], v[180:183], a[192:195], v[104:107]\n"
    "s_waitcnt lgkmcnt(0)\n"
    "v_cmp_le_u32 s[56:57], s59, v245\n"
    "v_mfma_f32_16x16x32_bf16 v[52:55], v[184:187], a[196:199], v[52:55]\n"
    "v_exp_f32 v45, v45\n"
    "v_mfma_f32_16x16x32_bf16 v[52:55], v[188:191], a[200:203], v[52:55]\n"
    "s_cmp_eq_u64 s[56:57], -1\n"
    "v_mfma_f32_16x16x32_bf16 v[52:55], v[192:195], a[204:207], v[52:55]\n"
    "s_cbranch_scc0 .Lpoll5\n"
    ".Lpoll5r:\n"
    "v_mfma_f32_16x16x32_bf16 v[52:55], v[196:199], a[208:211], v[52:55]\n"
    "ds_read_b128 v[156:159], v248 offset:0\n"
    "ds_read_b128 v[160:163], v248 offset:12288\n"
    "v_mfma_f32_16x16x32_bf16 v[52:55], v[200:203], a[212:215], v[52:55]\n"
    "ds_read_b128 v[164:167], v248 offset:1024\n"
    "ds_read_b128 v[168:171], v248 offset:13312\n"
    "v_mfma_f32_16x16x32_bf16 v[56:59], v[200:203], a[236:239], v[104:107]\n"
    "ds_read_b128 v[172:175], v248 offset:2048\n"
    "ds_read_b128 v[176:179], v248 offset:14336\n"
    "v_mfma_f32_16x16x32_bf16 v[56:59], v[196:199], a[232:235], v[56:59]\n"
    "ds_read_b128 v[100:103], v251 offset:0\n"
    "v_cvt_pk_bf16_f32 v81, v18, v19\n"
    "v_mfma_f32_16x16x32_bf16 v[56:59], v[192:195], a[228:231], v[56:59]\n"
    "v_cvt_pk_bf16_f32 v70, v20, v21\n"
    "v_cvt_pk_bf16_f32 v92, v44, v45\n"
    "v_mfma_f32_16x16x32_bf16 v[56:59], v[188:191], a[224:227], v[56:59]\n"
    "v_cvt_pk_bf16_f32 v71, v22, v23\n"
    "v_mfma_f32_16x16x32_bf16 v[56:59], v[184:187], a[220:223], v[56:59]\n"
    "v_exp_f32 v52, v52\n"
    "v_mfma_f32_16x16x32_bf16 v[56:59], v[180:183], a[216:219], v[56:59]\n"
    "v_exp_f32 v53, v53\n"
    "v_mfma_f32_16x16x32_bf16 v[60:63], v[180:183], a[240:243], v[104:107]\n"
    "v_exp_f32 v54, v54\n"
    "v_mfma_f32_16x16x32_bf16 v[60:63], v[184:187], a[244:247], v[60:63]\n"
    "v_cvt_pk_bf16_f32 v74, v24, v25\n"
    "v_cvt_pk_bf16_f32 v86, v52, v53\n"
    "v_mfma_f32_16x16x32_bf16 v[60:63], v[188:191], a[248:251], v[60:63]\n"
    "v_exp_f32 v55, v55\n"
    "v_mfma_f32_16x16x32_bf16 v[60:63], v[192:195], a[252:255], v[60:63]\n"
    "v_cvt_pk_bf16_f32 v87, v54, v55\n"
    "v_mfma_f32_16x16x32_bf16 v[60:63], v[196:199], v[124:127], v[60:63]\n"
    "v_exp_f32 v56, v56\n"
    "v_mfma_f32_16x16x32_bf16 v[60:63], v[200:203], v[128:131], v[60:63]\n"
    "v_cvt_pk_bf16_f32 v75, v26, v27\n"
    "v_mfma_f32_16x16x32_bf16 v[64:67], v[200:203], v[152:155], v[104:107]\n"
    "v_exp_f32 v57, v57\n"
    "v_mfma_f32_16x16x32_bf16 v[64:67], v[196:199], v[148:151], v[64:67]\n"
    "v_exp_f32 v58, v58\n"
    "v_mfma_f32_16x16x32_bf16 v[64:67], v[192:195], v[144:147], v[64:67]\n"
    "v_exp_f32 v59, v59\n"
    "v_mfma_f32_16x16x32_bf16 v[64:67], v[188:191], v[140:143], v[64:67]\n"
    "v_cvt_pk_bf16_f32 v78, v28, v29\n"
    "v_cvt_pk_bf16_f32 v90, v56, v57\n"
    "v_mfma_f32_16x16x32_bf16 v[64:67], v[184:187], v[136:139], v[64:67]\n"
    "v_cvt_pk_bf16_f32 v91, v58, v59\n"
    "v_mfma_f32_16x16x32_bf16 v[64:67], v[180:183], v[132:135], v[64:67]\n"
    "v_exp_f32 v46, v46\n"
    "v_cvt_pk_bf16_f32 v79, v30, v31\n"
    "v_exp_f32 v47, v47\n"
    "v_exp_f32 v60, v60\n"
    "v_exp_f32 v61, v61\n"
    "v_cvt_pk_bf16_f32 v82, v32, v33\n"
    "v_cvt_pk_bf16_f32 v93, v46, v47\n"
    "v_exp_f32 v62, v62\n"
    "v_exp_f32 v63, v63\n"
    "v_cvt_pk_bf16_f32 v94, v60, v61\n"
    "v_cvt_pk_bf16_f32 v95, v62, v63\n"
    "v_cvt_pk_bf16_f32 v83, v34, v35\n"
    "v_exp_f32 v48, v48\n"
    "v_exp_f32 v49, v49\n"
    "v_exp_f32 v50, v50\n"
    "v_exp_f32 v51, v51\n"
    "v_exp_f32 v64, v64\n"
    "v_exp_f32 v65, v65\n"
    "v_exp_f32 v66, v66\n"
    "v_exp_f32 v67, v67\n"
    "v_cvt_pk_bf16_f32 v96, v48, v49\n"
    "v_cvt_pk_bf16_f32 v97, v50, v51\n"
    "v_cvt_pk_bf16_f32 v98, v64, v65\n"
    "v_cvt_pk_bf16_f32 v99, v66, v67\n"
    "s_waitcnt lgkmcnt(0)\n"
    "v_mfma_f32_16x16x32_bf16 a[0:3], v[156:159], v[68:71], a[0:3]\n"
    "ds_read_b128 v[180:183], v248 offset:3072\n"
    "v_mfma_f32_16x16x32_bf16 a[0:3], v[160:163], v[84:87], a[0:3]\n"
    "ds_read_b128 v[184:187], v248 offset:15360\n"
    "v_mfma_f32_16x16x32_bf16 a[16:19], v[168:171], v[84:87], a[16:19]\n"
    "ds_read_b128 v[188:191], v248 offset:4096\n"
    "v_mfma_f32_16x16x32_bf16 a[16:19], v[164:167], v[68:71], a[16:19]\n"
    "ds_read_b128 v[192:195], v248 offset:16384\n"
    "v_mfma_f32_16x16x32_bf16 a[32:35], v[172:175], v[68:71], a[32:35]\n"
    "ds_read_b128 v[196:199], v248 offset:5120\n"
    "v_mfma_f32_16x16x32_bf16 a[32:35], v[176:179], v[84:87], a[32:35]\n"
    "ds_read_b128 v[200:203], v248 offset:17408\n"
    "v_mfma_f32_16x16x32_bf16 a[36:39], v[176:179], v[88:91], a[36:39]\n"
    "ds_read_b128 v[104:107], v251 offset:64\n"
    "v_mfma_f32_16x16x32_bf16 a[36:39], v[172:175], v[72:75], a[36:39]\n"
    "v_mfma_f32_16x16x32_bf16 a[20:23], v[164:167], v[72:75], a[20:23]\n"
    "v_mfma_f32_16x16x32_bf16 a[20:23], v[168:171], v[88:91], a[20:23]\n"
    "v_mfma_f32_16x16x32_bf16 a[4:7], v[160:163], v[88:91], a[4:7]\n"
    "v_mfma_f32_16x16x32_bf16 a[4:7], v[156:159], v[72:75], a[4:7]\n"
    "v_mfma_f32_16x16x32_bf16 a[8:11], v[156:159], v[76:79], a[8:11]\n"
    "v_mfma_f32_16x16x32_bf16 a[8:11], v[160:163], v[92:95], a[8:11]\n"
    "v_mfma_f32_16x16x32_bf16 a[24:27], v[168:171], v[92:95], a[24:27]\n"
    "s_waitcnt vmcnt(0)\n"
    "v_mfma_f32_16x16x32_bf16 a[24:27], v[164:167], v[76:79], a[24:27]\n"
    "v_lshrrev_b32 v204, 4, v1\n"
    "v_mfma_f32_16x16x32_bf16 a[40:43], v[172:175], v[76:79], a[40:43]\n"
    "s_mul_i32 s72, s84, 25600\n"
    "s_mul_i32 s73, s84, 24576\n"
    "v_mfma_f32_16x16x32_bf16 a[40:43], v[176:179], v[92:95], a[40:43]\n"
    "v_and_b32 v206, 15, v204\n"
    "v_lshrrev_b32 v207, 4, v204\n"
    "v_mfma_f32_16x16x32_bf16 a[44:47], v[176:179], v[96:99], a[44:47]\n"
    "v_mul_u32_u24 v205, 400, v206\n"
    "v_mul_u32_u24 v207, 24, v207\n"
    "v_mfma_f32_16x16x32_bf16 a[44:47], v[172:175], v[80:83], a[44:47]\n"
    "v_add_u32 v205, v207, v205\n"
    "v_add_u32 v205, s72, v205\n"
    "v_mfma_f32_16x16x32_bf16 a[28:31], v[164:167], v[80:83], a[28:31]\n"
    "v_add_u32 v206, 0, v204\n"
    "v_mul_u32_u24 v207, 2731, v206\n"
    "v_mfma_f32_16x16x32_bf16 a[28:31], v[168:171], v[96:99], a[28:31]\n"
    "v_lshrrev_b32 v207, 16, v207\n"
    "v_lshlrev_b32 v206, 4, v206\n"
    "v_mfma_f32_16x16x32_bf16 a[12:15], v[160:163], v[96:99], a[12:15]\n"
    "v_lshl_add_u32 v209, v207, 4, v206\n"
    "v_add_u32 v209, s72, v209\n"
    "v_mfma_f32_16x16x32_bf16 a[12:15], v[156:159], v[80:83], a[12:15]\n"
    "s_waitcnt lgkmcnt(0)\n"
    "v_add_u32 v206, 64, v204\n"
    "v_mul_u32_u24 v207, 2731, v206\n"
    "v_mfma_f32_16x16x32_bf16 a[48:51], v[180:183], v[68:71], a[48:51]\n"
    "ds_read_b128 v[156:159], v248 offset:6144\n"
    "v_lshrrev_b32 v207, 16, v207\n"
    "v_mfma_f32_16x16x32_bf16 a[48:51], v[184:187], v[84:87], a[48:51]\n"
    "ds_read_b128 v[160:163], v248 offset:18432\n"
    "v_lshlrev_b32 v206, 4, v206\n"
    "v_mfma_f32_16x16x32_bf16 a[64:67], v[192:195], v[84:87], a[64:67]\n"
    "ds_read_b128 v[164:167], v248 offset:7168\n"
    "v_lshl_add_u32 v210, v207, 4, v206\n"
    "v_mfma_f32_16x16x32_bf16 a[64:67], v[188:191], v[68:71], a[64:67]\n"
    "ds_read_b128 v[168:171], v248 offset:19456\n"
    "v_add_u32 v210, s72, v210\n"
    "v_mfma_f32_16x16x32_bf16 a[80:83], v[196:199], v[68:71], a[80:83]\n"
    "ds_read_b128 v[172:175], v248 offset:8192\n"
    "v_add_u32 v206, 128, v204\n"
    "v_mfma_f32_16x16x32_bf16 a[80:83], v[200:203], v[84:87], a[80:83]\n"
    "ds_read_b128 v[176:179], v248 offset:20480\n"
    "v_mul_u32_u24 v207, 2731, v206\n"
    "v_mfma_f32_16x16x32_bf16 v[108:111], v[104:107], v[84:87], v[108:111]\n"
    "v_lshrrev_b32 v207, 16, v207\n"
    "v_lshlrev_b32 v206, 4, v206\n"
    "v_mfma_f32_16x16x32_bf16 v[108:111], v[100:103], v[68:71], v[108:111]\n"
    "v_lshl_add_u32 v211, v207, 4, v206\n"
    "v_add_u32 v211, s72, v211\n"
    "v_mfma_f32_16x16x32_bf16 v[112:115], v[100:103], v[72:75], v[112:115]\n"
    "v_lshlrev_b32 v208, 16, v1\n"
    "v_mfma_f32_16x16x32_bf16 v[112:115], v[104:107], v[88:91], v[112:115]\n"
    "buffer_store_dwordx2 v[238:239], v208, s[36:39], s82 offen offset:0 sc1\n"
    "v_mfma_f32_16x16x32_bf16 a[84:87], v[200:203], v[88:91], a[84:87]\n"
    "v_mfma_f32_16x16x32_bf16 a[84:87], v[196:199], v[72:75], a[84:87]\n"
    "v_mfma_f32_16x16x32_bf16 a[68:71], v[188:191], v[72:75], a[68:71]\n"
    "v_mfma_f32_16x16x32_bf16 a[68:71], v[192:195], v[88:91], a[68:71]\n"
    "v_mfma_f32_16x16x32_bf16 a[52:55], v[184:187], v[88:91], a[52:55]\n"
    "s_mov_b32 s74, 0x5b800000\n"
    "v_mov_b32 v212, 0\n"
    "v_add_f32 v108, v108, v109\n"
    "v_mfma_f32_16x16x32_bf16 a[52:55], v[180:183], v[72:75], a[52:55]\n"
    "v_add_f32 v112, v112, v113\n"
    "v_mov_b32 v111, v110\n"
    "v_mfma_f32_16x16x32_bf16 a[56:59], v[180:183], v[76:79], a[56:59]\n"
    "v_mov_b32 v115, v114\n"
    "v_mov_b32 v109, v108\n"
    "v_mfma_f32_16x16x32_bf16 a[56:59], v[184:187], v[92:95], a[56:59]\n"
    "v_mov_b32 v113, v112\n"
    "v_permlane32_swap_b32 v110, v111\n"
    "v_mfma_f32_16x16x32_bf16 a[72:75], v[192:195], v[92:95], a[72:75]\n"
    "v_permlane32_swap_b32 v114, v115\n"
    "v_permlane32_swap_b32 v108, v109\n"
    "v_mfma_f32_16x16x32_bf16 a[72:75], v[188:191], v[76:79], a[72:75]\n"
    "v_permlane32_swap_b32 v112, v113\n"
    "v_add_f32 v110, v110, v111\n"
    "v_mfma_f32_16x16x32_bf16 a[88:91], v[196:199], v[76:79], a[88:91]\n"
    "v_add_f32 v114, v114, v115\n"
    "v_add_f32 v108, v108, v109\n"
    "v_mfma_f32_16x16x32_bf16 a[88:91], v[200:203], v[92:95], a[88:91]\n"
    "v_add_f32 v112, v112, v113\n"
    "v_mov_b32 v111, v110\n"
    "v_mfma_f32_16x16x32_bf16 v[116:119], v[104:107], v[92:95], v[116:119]\n"
    "v_mov_b32 v115, v114\n"
    "v_mov_b32 v109, v108\n"
    "v_mfma_f32_16x16x32_bf16 v[116:119], v[100:103], v[76:79], v[116:119]\n"
    "v_mov_b32 v113, v112\n"
    "v_permlane16_swap_b32 v110, v111\n"
    "v_mfma_f32_16x16x32_bf16 v[120:123], v[100:103], v[80:83], v[120:123]\n"
    "v_permlane16_swap_b32 v114, v115\n"
    "v_permlane16_swap_b32 v108, v109\n"
    "v_mfma_f32_16x16x32_bf16 v[120:123], v[104:107], v[96:99], v[120:123]\n"
    "v_permlane16_swap_b32 v112, v113\n"
    "v_add_f32 v110, v110, v111\n"
    "v_mfma_f32_16x16x32_bf16 a[92:95], v[200:203], v[96:99], a[92:95]\n"
    "v_add_f32 v114, v114, v115\n"
    "v_add_f32 v108, v108, v109\n"
    "v_mfma_f32_16x16x32_bf16 a[92:95], v[196:199], v[80:83], a[92:95]\n"
    "v_add_f32 v112, v112, v113\n"
    "v_fma_f32 v212, |v110|, 4.0, v212\n"
    "v_mfma_f32_16x16x32_bf16 a[76:79], v[188:191], v[80:83], a[76:79]\n"
    "v_fma_f32 v212, v108, s74, v212\n"
    "v_fma_f32 v212, |v114|, 4.0, v212\n"
    "v_mfma_f32_16x16x32_bf16 a[76:79], v[192:195], v[96:99], a[76:79]\n"
    "v_fma_f32 v212, v112, s74, v212\n"
    "v_mfma_f32_16x16x32_bf16 a[60:63], v[184:187], v[96:99], a[60:63]\n"
    "v_rcp_f32 v108, v108\n"
    "v_mfma_f32_16x16x32_bf16 a[60:63], v[180:183], v[80:83], a[60:63]\n"
    "s_waitcnt lgkmcnt(0)\n"
    "v_rcp_f32 v112, v112\n"
    "v_mfma_f32_16x16x32_bf16 a[96:99], v[156:159], v[68:71], a[96:99]\n"
    "ds_read_b128 v[180:183], v248 offset:9216\n"
    "v_fma_f32 v212, v108, s74, v212\n"
    "v_mfma_f32_16x16x32_bf16 a[96:99], v[160:163], v[84:87], a[96:99]\n"
    "ds_read_b128 v[184:187], v248 offset:21504\n"
    "v_mul_f32 v110, -v110, v108\n"
    "v_mfma_f32_16x16x32_bf16 a[112:115], v[168:171], v[84:87], a[112:115]\n"
    "ds_read_b128 v[188:191], v248 offset:10240\n"
    "v_fma_f32 v212, v112, s74, v212\n"
    "v_mfma_f32_16x16x32_bf16 a[112:115], v[164:167], v[68:71], a[112:115]\n"
    "ds_read_b128 v[192:195], v248 offset:22528\n"
    "v_mul_f32 v114, -v114, v112\n"
    "v_mfma_f32_16x16x32_bf16 a[128:131], v[172:175], v[68:71], a[128:131]\n"
    "ds_read_b128 v[196:199], v248 offset:11264\n"
    "v_add_f32 v116, v116, v117\n"
    "v_mfma_f32_16x16x32_bf16 a[128:131], v[176:179], v[84:87], a[128:131]\n"
    "ds_read_b128 v[200:203], v248 offset:23552\n"
    "v_add_f32 v120, v120, v121\n"
    "v_mfma_f32_16x16x32_bf16 a[132:135], v[176:179], v[88:91], a[132:135]\n"
    "v_mov_b32 v119, v118\n"
    "v_mov_b32 v123, v122\n"
    "v_mfma_f32_16x16x32_bf16 a[132:135], v[172:175], v[72:75], a[132:135]\n"
    "v_mov_b32 v117, v116\n"
    "v_mov_b32 v121, v120\n"
    "v_mfma_f32_16x16x32_bf16 a[116:119], v[164:167], v[72:75], a[116:119]\n"
    "v_permlane32_swap_b32 v118, v119\n"
    "v_permlane32_swap_b32 v122, v123\n"
    "v_mfma_f32_16x16x32_bf16 a[116:119], v[168:171], v[88:91], a[116:119]\n"
    "v_permlane32_swap_b32 v116, v117\n"
    "v_permlane32_swap_b32 v120, v121\n"
    "v_mfma_f32_16x16x32_bf16 a[100:103], v[160:163], v[88:91], a[100:103]\n"
    "v_add_f32 v118, v118, v119\n"
    "v_add_f32 v122, v122, v123\n"
    "v_mfma_f32_16x16x32_bf16 a[100:103], v[156:159], v[72:75], a[100:103]\n"
    "v_add_f32 v116, v116, v117\n"
    "v_add_f32 v120, v120, v121\n"
    "v_mfma_f32_16x16x32_bf16 a[104:107], v[156:159], v[76:79], a[104:107]\n"
    "v_mov_b32 v119, v118\n"
    "v_mov_b32 v123, v122\n"
    "v_mfma_f32_16x16x32_bf16 a[104:107], v[160:163], v[92:95], a[104:107]\n"
    "v_mov_b32 v117, v116\n"
    "v_mov_b32 v121, v120\n"
    "v_mfma_f32_16x16x32_bf16 a[120:123], v[168:171], v[92:95], a[120:123]\n"
    "v_permlane16_swap_b32 v118, v119\n"
    "v_permlane16_swap_b32 v122, v123\n"
    "v_mfma_f32_16x16x32_bf16 a[120:123], v[164:167], v[76:79], a[120:123]\n"
    "v_permlane16_swap_b32 v116, v117\n"
    "v_permlane16_swap_b32 v120, v121\n"
    "v_mfma_f32_16x16x32_bf16 a[136:139], v[172:175], v[76:79], a[136:139]\n"
    "v_add_f32 v118, v118, v119\n"
    "v_add_f32 v122, v122, v123\n"
    "v_mfma_f32_16x16x32_bf16 a[136:139], v[176:179], v[92:95], a[136:139]\n"
    "v_add_f32 v116, v116, v117\n"
    "v_add_f32 v120, v120, v121\n"
    "v_mfma_f32_16x16x32_bf16 a[140:143], v[176:179], v[96:99], a[140:143]\n"
    "v_fma_f32 v212, |v118|, 4.0, v212\n"
    "v_fma_f32 v212, v116, s74, v212\n"
    "v_mfma_f32_16x16x32_bf16 a[140:143], v[172:175], v[80:83], a[140:143]\n"
    "v_fma_f32 v212, |v122|, 4.0, v212\n"
    "v_fma_f32 v212, v120, s74, v212\n"
    "v_mfma_f32_16x16x32_bf16 a[124:127], v[164:167], v[80:83], a[124:127]\n"
    "v_rcp_f32 v116, v116\n"
    "v_mfma_f32_16x16x32_bf16 a[124:127], v[168:171], v[96:99], a[124:127]\n"
    "v_rcp_f32 v120, v120\n"
    "v_mfma_f32_16x16x32_bf16 a[108:111], v[160:163], v[96:99], a[108:111]\n"
    "v_fma_f32 v212, v116, s74, v212\n"
    "v_mul_f32 v118, -v118, v116\n"
    "v_mfma_f32_16x16x32_bf16 a[108:111], v[156:159], v[80:83], a[108:111]\n"
    "s_waitcnt lgkmcnt(0)\n"
    "v_fma_f32 v212, v120, s74, v212\n"
    "v_mul_f32 v122, -v122, v120\n"
    "v_mfma_f32_16x16x32_bf16 a[144:147], v[180:183], v[68:71], a[144:147]\n"
    "s_mov_b32 s74, 0x7f800000\n"
    "v_cmp_nlt_f32 s[54:55], |v212|, s74\n"
    "v_mfma_f32_16x16x32_bf16 a[144:147], v[184:187], v[84:87], a[144:147]\n"
    "s_or_b32 %0, s54, s55\n"
    "v_mfma_f32_16x16x32_bf16 a[160:163], v[192:195], v[84:87], a[160:163]\n"
    "v_mfma_f32_16x16x32_bf16 a[160:163], v[188:191], v[68:71], a[160:163]\n"
    "v_mfma_f32_16x16x32_bf16 a[176:179], v[196:199], v[68:71], a[176:179]\n"
    "v_mfma_f32_16x16x32_bf16 a[176:179], v[200:203], v[84:87], a[176:179]\n"
    "v_mfma_f32_16x16x32_bf16 a[180:183], v[200:203], v[88:91], a[180:183]\n"
    "v_mfma_f32_16x16x32_bf16 a[180:183], v[196:199], v[72:75], a[180:183]\n"
    "v_mfma_f32_16x16x32_bf16 a[164:167], v[188:191], v[72:75], a[164:167]\n"
    "v_mfma_f32_16x16x32_bf16 a[164:167], v[192:195], v[88:91], a[164:167]\n"
    "v_mfma_f32_16x16x32_bf16 a[148:151], v[184:187], v[88:91], a[148:151]\n"
    "v_mfma_f32_16x16x32_bf16 a[148:151], v[180:183], v[72:75], a[148:151]\n"
    "v_mfma_f32_16x16x32_bf16 a[152:155], v[180:183], v[76:79], a[152:155]\n"
    "v_mfma_f32_16x16x32_bf16 a[152:155], v[184:187], v[92:95], a[152:155]\n"
    "v_mfma_f32_16x16x32_bf16 a[168:171], v[192:195], v[92:95], a[168:171]\n"
    "v_mfma_f32_16x16x32_bf16 a[168:171], v[188:191], v[76:79], a[168:171]\n"
    "v_mfma_f32_16x16x32_bf16 a[184:187], v[196:199], v[76:79], a[184:187]\n"
    "v_mfma_f32_16x16x32_bf16 a[184:187], v[200:203], v[92:95], a[184:187]\n"
    "v_mfma_f32_16x16x32_bf16 a[188:191], v[200:203], v[96:99], a[188:191]\n"
    "v_mfma_f32_16x16x32_bf16 a[188:191], v[196:199], v[80:83], a[188:191]\n"
    "v_mfma_f32_16x16x32_bf16 a[172:175], v[188:191], v[80:83], a[172:175]\n"
    "v_mfma_f32_16x16x32_bf16 a[172:175], v[192:195], v[96:99], a[172:175]\n"
    "v_mfma_f32_16x16x32_bf16 a[156:159], v[184:187], v[96:99], a[156:159]\n"
    "v_mfma_f32_16x16x32_bf16 a[156:159], v[180:183], v[80:83], a[156:159]\n"
    "s_waitcnt lgkmcnt(0)\n"
    "s_barrier\n"
    "v_accvgpr_read_b32 v4, a0\n"
    "v_accvgpr_read_b32 v7, a1\n"
    "v_accvgpr_read_b32 v10, a2\n"
    "v_accvgpr_read_b32 v13, a3\n"
    "v_accvgpr_read_b32 v5, a64\n"
    "v_accvgpr_read_b32 v8, a65\n"
    "v_accvgpr_read_b32 v11, a66\n"
    "v_accvgpr_read_b32 v14, a67\n"
    "v_accvgpr_read_b32 v6, a128\n"
    "v_accvgpr_read_b32 v9, a129\n"
    "v_accvgpr_read_b32 v12, a130\n"
    "v_accvgpr_read_b32 v15, a131\n"
    "v_pk_fma_f32 v[4:5], v[4:5], v[108:109], v[110:111] op_sel_hi:[1,0,0]\n"
    "v_pk_fma_f32 v[6:7], v[6:7], v[108:109], v[110:111] op_sel_hi:[1,0,0]\n"
    "v_pk_fma_f32 v[8:9], v[8:9], v[108:109], v[110:111] op_sel_hi:[1,0,0]\n"
    "v_pk_fma_f32 v[10:11], v[10:11], v[108:109], v[110:111] op_sel_hi:[1,0,0]\n"
    "v_pk_fma_f32 v[12:13], v[12:13], v[108:109], v[110:111] op_sel_hi:[1,0,0]\n"
    "v_pk_fma_f32 v[14:15], v[14:15], v[108:109], v[110:111] op_sel_hi:[1,0,0]\n"
    "v_cvt_pk_bf16_f32 v16, v4, v5\n"
    "v_cvt_pk_bf16_f32 v17, v6, v7\n"
    "v_cvt_pk_bf16_f32 v18, v8, v9\n"
    "v_cvt_pk_bf16_f32 v19, v10, v11\n"
    "v_cvt_pk_bf16_f32 v20, v12, v13\n"
    "v_cvt_pk_bf16_f32 v21, v14, v15\n"
    "ds_write_b64 v205, v[16:17] offset:0\n"
    "ds_write_b64 v205, v[18:19] offset:8\n"
    "ds_write_b64 v205, v[20:21] offset:16\n"
    "v_accvgpr_read_b32 v22, a16\n"
    "v_accvgpr_read_b32 v25, a17\n"
    "v_accvgpr_read_b32 v28, a18\n"
    "v_accvgpr_read_b32 v31, a19\n"
    "v_accvgpr_read_b32 v23, a80\n"
    "v_accvgpr_read_b32 v26, a81\n"
    "v_accvgpr_read_b32 v29, a82\n"
    "v_accvgpr_read_b32 v32, a83\n"
    "v_accvgpr_read_b32 v24, a144\n"
    "v_accvgpr_read_b32 v27, a145\n"
    "v_accvgpr_read_b32 v30, a146\n"
    "v_accvgpr_read_b32 v33, a147\n"
    "v_pk_fma_f32 v[22:23], v[22:23], v[108:109], v[110:111] op_sel_hi:[1,0,0]\n"
    "v_pk_fma_f32 v[24:25], v[24:25], v[108:109], v[110:111] op_sel_hi:[1,0,0]\n"
    "v_pk_fma_f32 v[26:27], v[26:27], v[108:109], v[110:111] op_sel_hi:[1,0,0]\n"
    "v_pk_fma_f32 v[28:29], v[28:29], v[108:109], v[110:111] op_sel_hi:[1,0,0]\n"
    "v_pk_fma_f32 v[30:31], v[30:31], v[108:109], v[110:111] op_sel_hi:[1,0,0]\n"
    "v_pk_fma_f32 v[32:33], v[32:33], v[108:109], v[110:111] op_sel_hi:[1,0,0]\n"
    "v_cvt_pk_bf16_f32 v34, v22, v23\n"
    "v_cvt_pk_bf16_f32 v35, v24, v25\n"
    "v_cvt_pk_bf16_f32 v36, v26, v27\n"
    "v_cvt_pk_bf16_f32 v37, v28, v29\n"
    "v_cvt_pk_bf16_f32 v38, v30, v31\n"
    "v_cvt_pk_bf16_f32 v39, v32, v33\n"
    "ds_write_b64 v205, v[34:35] offset:96\n"
    "ds_write_b64 v205, v[36:37] offset:104\n"
    "ds_write_b64 v205, v[38:39] offset:112\n"
    "v_accvgpr_read_b32 v40, a32\n"
    "v_accvgpr_read_b32 v43, a33\n"
    "v_accvgpr_read_b32 v46, a34\n"
    "v_accvgpr_read_b32 v49, a35\n"
    "v_accvgpr_read_b32 v41, a96\n"
    "v_accvgpr_read_b32 v44, a97\n"
    "v_accvgpr_read_b32 v47, a98\n"
    "v_accvgpr_read_b32 v50, a99\n"
    "v_accvgpr_read_b32 v42, a160\n"
    "v_accvgpr_read_b32 v45, a161\n"
    "v_accvgpr_read_b32 v48, a162\n"
    "v_accvgpr_read_b32 v51, a163\n"
    "v_pk_fma_f32 v[40:41], v[40:41], v[108:109], v[110:111] op_sel_hi:[1,0,0]\n"
    "v_pk_fma_f32 v[42:43], v[42:43], v[108:109], v[110:111] op_sel_hi:[1,0,0]\n"
    "v_pk_fma_f32 v[44:45], v[44:45], v[108:109], v[110:111] op_sel_hi:[1,0,0]\n"
    "v_pk_fma_f32 v[46:47], v[46:47], v[108:109], v[110:111] op_sel_hi:[1,0,0]\n"
    "v_pk_fma_f32 v[48:49], v[48:49], v[108:109], v[110:111] op_sel_hi:[1,0,0]\n"
    "v_pk_fma_f32 v[50:51], v[50:51], v[108:109], v[110:111] op_sel_hi:[1,0,0]\n"
    "v_cvt_pk_bf16_f32 v52, v40, v41\n"
    "v_cvt_pk_bf16_f32 v53, v42, v43\n"
    "v_cvt_pk_bf16_f32 v54, v44, v45\n"
    "v_cvt_pk_bf16_f32 v55, v46, v47\n"
    "v_cvt_pk_bf16_f32 v56, v48, v49\n"
    "v_cvt_pk_bf16_f32 v57, v50, v51\n"
    "ds_write_b64 v205, v[52:53] offset:192\n"
    "ds_write_b64 v205, v[54:55] offset:200\n"
    "ds_write_b64 v205, v[56:57] offset:208\n"
    "v_accvgpr_read_b32 v58, a48\n"
    "v_accvgpr_read_b32 v61, a49\n"
    "v_accvgpr_read_b32 v64, a50\n"
    "v_accvgpr_read_b32 v67, a51\n"
    "v_accvgpr_read_b32 v59, a112\n"
    "v_accvgpr_read_b32 v62, a113\n"
    "v_accvgpr_read_b32 v65, a114\n"
    "v_accvgpr_read_b32 v68, a115\n"
    "v_accvgpr_read_b32 v60, a176\n"
    "v_accvgpr_read_b32 v63, a177\n"
    "v_accvgpr_read_b32 v66, a178\n"
    "v_accvgpr_read_b32 v69, a179\n"
    "v_pk_fma_f32 v[58:59], v[58:59], v[108:109], v[110:111] op_sel_hi:[1,0,0]\n"
    "v_pk_fma_f32 v[60:61], v[60:61], v[108:109], v[110:111] op_sel_hi:[1,0,0]\n"
    "v_pk_fma_f32 v[62:63], v[62:63], v[108:109], v[110:111] op_sel_hi:[1,0,0]\n"
    "v_pk_fma_f32 v[64:65], v[64:65], v[108:109], v[110:111] op_sel_hi:[1,0,0]\n"
    "v_pk_fma_f32 v[66:67], v[66:67], v[108:109], v[110:111] op_sel_hi:[1,0,0]\n"
    "v_pk_fma_f32 v[68:69], v[68:69], v[108:109], v[110:111] op_sel_hi:[1,0,0]\n"
    "v_cvt_pk_bf16_f32 v70, v58, v59\n"
    "v_cvt_pk_bf16_f32 v71, v60, v61\n"
    "v_cvt_pk_bf16_f32 v72, v62, v63\n"
    "v_cvt_pk_bf16_f32 v73, v64, v65\n"
    "v_cvt_pk_bf16_f32 v74, v66, v67\n"
    "v_cvt_pk_bf16_f32 v75, v68, v69\n"
    "ds_write_b64 v205, v[70:71] offset:288\n"
    "ds_write_b64 v205, v[72:73] offset:296\n"
    "ds_write_b64 v205, v[74:75] offset:304\n"
    "ds_read_b128 v[124:127], v209 offset:0\n"
    "ds_read_b128 v[128:131], v210 offset:0\n"
    "ds_read_b128 v[132:135], v211 offset:0\n"
    "ds_read_b128 v[136:139], v209 offset:3200\n"
    "ds_read_b128 v[140:143], v210 offset:3200\n"
    "ds_read_b128 v[144:147], v211 offset:3200\n"
    "v_accvgpr_read_b32 v76, a4\n"
    "v_accvgpr_read_b32 v79, a5\n"
    "v_accvgpr_read_b32 v82, a6\n"
    "v_accvgpr_read_b32 v85, a7\n"
    "v_accvgpr_read_b32 v77, a68\n"
    "v_accvgpr_read_b32 v80, a69\n"
    "v_accvgpr_read_b32 v83, a70\n"
    "v_accvgpr_read_b32 v86, a71\n"
    "v_accvgpr_read_b32 v78, a132\n"
    "v_accvgpr_read_b32 v81, a133\n"
    "v_accvgpr_read_b32 v84, a134\n"
    "v_accvgpr_read_b32 v87, a135\n"
    "v_pk_fma_f32 v[76:77], v[76:77], v[112:113], v[114:115] op_sel_hi:[1,0,0]\n"
    "v_pk_fma_f32 v[78:79], v[78:79], v[112:113], v[114:115] op_sel_hi:[1,0,0]\n"
    "v_pk_fma_f32 v[80:81], v[80:81], v[112:113], v[114:115] op_sel_hi:[1,0,0]\n"
    "v_pk_fma_f32 v[82:83], v[82:83], v[112:113], v[114:115] op_sel_hi:[1,0,0]\n"
    "v_pk_fma_f32 v[84:85], v[84:85], v[112:113], v[114:115] op_sel_hi:[1,0,0]\n"
    "v_pk_fma_f32 v[86:87], v[86:87], v[112:113], v[114:115] op_sel_hi:[1,0,0]\n"
    "v_cvt_pk_bf16_f32 v88, v76, v77\n"
    "v_cvt_pk_bf16_f32 v89, v78, v79\n"
    "v_cvt_pk_bf16_f32 v90, v80, v81\n"
    "v_cvt_pk_bf16_f32 v91, v82, v83\n"
    "v_cvt_pk_bf16_f32 v92, v84, v85\n"
    "v_cvt_pk_bf16_f32 v93, v86, v87\n"
    "ds_write_b64 v205, v[88:89] offset:6400\n"
    "ds_write_b64 v205, v[90:91] offset:6408\n"
    "ds_write_b64 v205, v[92:93] offset:6416\n"
    "s_waitcnt lgkmcnt(8)\n"
    "buffer_store_dwordx4 v[124:127], v1, s[24:27], s73 offen offset:0 sc1\n"
    "s_waitcnt lgkmcnt(7)\n"
    "buffer_store_dwordx4 v[128:131], v1, s[24:27], s73 offen offset:1024 sc1\n"
    "s_waitcnt lgkmcnt(6)\n"
    "buffer_store_dwordx4 v[132:135], v1, s[24:27], s73 offen offset:2048 sc1\n"
    "s_waitcnt lgkmcnt(5)\n"
    "buffer_store_dwordx4 v[136:139], v1, s[24:27], s73 offen offset:3072 sc1\n"
    "s_add_u32 s73, s73, 4096\n"
    "s_waitcnt lgkmcnt(4)\n"
    "buffer_store_dwordx4 v[140:143], v1, s[24:27], s73 offen offset:0 sc1\n"
    "s_waitcnt lgkmcnt(3)\n"
    "buffer_store_dwordx4 v[144:147], v1, s[24:27], s73 offen offset:1024 sc1\n"
    "v_accvgpr_read_b32 v4, a20\n"
    "v_accvgpr_read_b32 v7, a21\n"
    "v_accvgpr_read_b32 v10, a22\n"
    "v_accvgpr_read_b32 v13, a23\n"
    "v_accvgpr_read_b32 v5, a84\n"
    "v_accvgpr_read_b32 v8, a85\n"
    "v_accvgpr_read_b32 v11, a86\n"
    "v_accvgpr_read_b32 v14, a87\n"
    "v_accvgpr_read_b32 v6, a148\n"
    "v_accvgpr_read_b32 v9, a149\n"
    "v_accvgpr_read_b32 v12, a150\n"
    "v_accvgpr_read_b32 v15, a151\n"
    "v_pk_fma_f32 v[4:5], v[4:5], v[112:113], v[114:115] op_sel_hi:[1,0,0]\n"
    "v_pk_fma_f32 v[6:7], v[6:7], v[112:113], v[114:115] op_sel_hi:[1,0,0]\n"
    "v_pk_fma_f32 v[8:9], v[8:9], v[112:113], v[114:115] op_sel_hi:[1,0,0]\n"
    "v_pk_fma_f32 v[10:11], v[10:11], v[112:113], v[114:115] op_sel_hi:[1,0,0]\n"
    "v_pk_fma_f32 v[12:13], v[12:13], v[112:113], v[114:115] op_sel_hi:[1,0,0]\n"
    "v_pk_fma_f32 v[14:15], v[14:15], v[112:113], v[114:115] op_sel_hi:[1,0,0]\n"
    "v_cvt_pk_bf16_f32 v16, v4, v5\n"
    "v_cvt_pk_bf16_f32 v17, v6, v7\n"
    "v_cvt_pk_bf16_f32 v18, v8, v9\n"
    "v_cvt_pk_bf16_f32 v19, v10, v11\n"
    "v_cvt_pk_bf16_f32 v20, v12, v13\n"
    "v_cvt_pk_bf16_f32 v21, v14, v15\n"
    "ds_write_b64 v205, v[16:17] offset:6496\n"
    "ds_write_b64 v205, v[18:19] offset:6504\n"
    "ds_write_b64 v205, v[20:21] offset:6512\n"
    "v_accvgpr_read_b32 v22, a36\n"
    "v_accvgpr_read_b32 v25, a37\n"
    "v_accvgpr_read_b32 v28, a38\n"
    "v_accvgpr_read_b32 v31, a39\n"
    "v_accvgpr_read_b32 v23, a100\n"
    "v_accvgpr_read_b32 v26, a101\n"
    "v_accvgpr_read_b32 v29, a102\n"
    "v_accvgpr_read_b32 v32, a103\n"
    "v_accvgpr_read_b32 v24, a164\n"
    "v_accvgpr_read_b32 v27, a165\n"
    "v_accvgpr_read_b32 v30, a166\n"
    "v_accvgpr_read_b32 v33, a167\n"
    "v_pk_fma_f32 v[22:23], v[22:23], v[112:113], v[114:115] op_sel_hi:[1,0,0]\n"
    "v_pk_fma_f32 v[24:25], v[24:25], v[112:113], v[114:115] op_sel_hi:[1,0,0]\n"
    "v_pk_fma_f32 v[26:27], v[26:27], v[112:113], v[114:115] op_sel_hi:[1,0,0]\n"
    "v_pk_fma_f32 v[28:29], v[28:29], v[112:113], v[114:115] op_sel_hi:[1,0,0]\n"
    "v_pk_fma_f32 v[30:31], v[30:31], v[112:113], v[114:115] op_sel_hi:[1,0,0]\n"
    "v_pk_fma_f32 v[32:33], v[32:33], v[112:113], v[114:115] op_sel_hi:[1,0,0]\n"
    "v_cvt_pk_bf16_f32 v34, v22, v23\n"
    "v_cvt_pk_bf16_f32 v35, v24, v25\n"
    "v_cvt_pk_bf16_f32 v36, v26, v27\n"
    "v_cvt_pk_bf16_f32 v37, v28, v29\n"
    "v_cvt_pk_bf16_f32 v38, v30, v31\n"
    "v_cvt_pk_bf16_f32 v39, v32, v33\n"
    "ds_write_b64 v205, v[34:35] offset:6592\n"
    "ds_write_b64 v205, v[36:37] offset:6600\n"
    "ds_write_b64 v205, v[38:39] offset:6608\n"
    "v_accvgpr_read_b32 v40, a52\n"
    "v_accvgpr_read_b32 v43, a53\n"
    "v_accvgpr_read_b32 v46, a54\n"
    "v_accvgpr_read_b32 v49, a55\n"
    "v_accvgpr_read_b32 v41, a116\n"
    "v_accvgpr_read_b32 v44, a117\n"
    "v_accvgpr_read_b32 v47, a118\n"
    "v_accvgpr_read_b32 v50, a119\n"
    "v_accvgpr_read_b32 v42, a180\n"
    "v_accvgpr_read_b32 v45, a181\n"
    "v_accvgpr_read_b32 v48, a182\n"
    "v_accvgpr_read_b32 v51, a183\n"
    "v_pk_fma_f32 v[40:41], v[40:41], v[112:113], v[114:115] op_sel_hi:[1,0,0]\n"
    "v_pk_fma_f32 v[42:43], v[42:43], v[112:113], v[114:115] op_sel_hi:[1,0,0]\n"
    "v_pk_fma_f32 v[44:45], v[44:45], v[112:113], v[114:115] op_sel_hi:[1,0,0]\n"
    "v_pk_fma_f32 v[46:47], v[46:47], v[112:113], v[114:115] op_sel_hi:[1,0,0]\n"
    "v_pk_fma_f32 v[48:49], v[48:49], v[112:113], v[114:115] op_sel_hi:[1,0,0]\n"
    "v_pk_fma_f32 v[50:51], v[50:51], v[112:113], v[114:115] op_sel_hi:[1,0,0]\n"
    "v_cvt_pk_bf16_f32 v52, v40, v41\n"
    "v_cvt_pk_bf16_f32 v53, v42, v43\n"
    "v_cvt_pk_bf16_f32 v54, v44, v45\n"
    "v_cvt_pk_bf16_f32 v55, v46, v47\n"
    "v_cvt_pk_bf16_f32 v56, v48, v49\n"
    "v_cvt_pk_bf16_f32 v57, v50, v51\n"
    "ds_write_b64 v205, v[52:53] offset:6688\n"
    "ds_write_b64 v205, v[54:55] offset:6696\n"
    "ds_write_b64 v205, v[56:57] offset:6704\n"
    "ds_read_b128 v[148:151], v209 offset:6400\n"
    "ds_read_b128 v[152:155], v210 offset:6400\n"
    "ds_read_b128 v[156:159], v211 offset:6400\n"
    "ds_read_b128 v[160:163], v209 offset:9600\n"
    "ds_read_b128 v[164:167], v210 offset:9600\n"
    "ds_read_b128 v[168:171], v211 offset:9600\n"
    "v_accvgpr_read_b32 v58, a8\n"
    "v_accvgpr_read_b32 v61, a9\n"
    "v_accvgpr_read_b32 v64, a10\n"
    "v_accvgpr_read_b32 v67, a11\n"
    "v_accvgpr_read_b32 v59, a72\n"
    "v_accvgpr_read_b32 v62, a73\n"
    "v_accvgpr_read_b32 v65, a74\n"
    "v_accvgpr_read_b32 v68, a75\n"
    "v_accvgpr_read_b32 v60, a136\n"
    "v_accvgpr_read_b32 v63, a137\n"
    "v_accvgpr_read_b32 v66, a138\n"
    "v_accvgpr_read_b32 v69, a139\n"
    "v_pk_fma_f32 v[58:59], v[58:59], v[116:117], v[118:119] op_sel_hi:[1,0,0]\n"
    "v_pk_fma_f32 v[60:61], v[60:61], v[116:117], v[118:119] op_sel_hi:[1,0,0]\n"
    "v_pk_fma_f32 v[62:63], v[62:63], v[116:117], v[118:119] op_sel_hi:[1,0,0]\n"
    "v_pk_fma_f32 v[64:65], v[64:65], v[116:117], v[118:119] op_sel_hi:[1,0,0]\n"
    "v_pk_fma_f32 v[66:67], v[66:67], v[116:117], v[118:119] op_sel_hi:[1,0,0]\n"
    "v_pk_fma_f32 v[68:69], v[68:69], v[116:117], v[118:119] op_sel_hi:[1,0,0]\n"
    "v_cvt_pk_bf16_f32 v70, v58, v59\n"
    "v_cvt_pk_bf16_f32 v71, v60, v61\n"
    "v_cvt_pk_bf16_f32 v72, v62, v63\n"
    "v_cvt_pk_bf16_f32 v73, v64, v65\n"
    "v_cvt_pk_bf16_f32 v74, v66, v67\n"
    "v_cvt_pk_bf16_f32 v75, v68, v69\n"
    "ds_write_b64 v205, v[70:71] offset:12800\n"
    "ds_write_b64 v205, v[72:73] offset:12808\n"
    "ds_write_b64 v205, v[74:75] offset:12816\n"
    "s_waitcnt lgkmcnt(8)\n"
    "buffer_store_dwordx4 v[148:151], v1, s[24:27], s73 offen offset:2048 sc1\n"
    "s_waitcnt lgkmcnt(7)\n"
    "buffer_store_dwordx4 v[152:155], v1, s[24:27], s73 offen offset:3072 sc1\n"
    "s_add_u32 s73, s73, 4096\n"
    "s_waitcnt lgkmcnt(6)\n"
    "buffer_store_dwordx4 v[156:159], v1, s[24:27], s73 offen offset:0 sc1\n"
    "s_waitcnt lgkmcnt(5)\n"
    "buffer_store_dwordx4 v[160:163], v1, s[24:27], s73 offen offset:1024 sc1\n"
    "s_waitcnt lgkmcnt(4)\n"
    "buffer_store_dwordx4 v[164:167], v1, s[24:27], s73 offen offset:2048 sc1\n"
    "s_waitcnt lgkmcnt(3)\n"
    "buffer_store_dwordx4 v[168:171], v1, s[24:27], s73 offen offset:3072 sc1\n"
    "v_accvgpr_read_b32 v76, a24\n"
    "v_accvgpr_read_b32 v79, a25\n"
    "v_accvgpr_read_b32 v82, a26\n"
    "v_accvgpr_read_b32 v85, a27\n"
    "v_accvgpr_read_b32 v77, a88\n"
    "v_accvgpr_read_b32 v80, a89\n"
    "v_accvgpr_read_b32 v83, a90\n"
    "v_accvgpr_read_b32 v86, a91\n"
    "v_accvgpr_read_b32 v78, a152\n"
    "v_accvgpr_read_b32 v81, a153\n"
    "v_accvgpr_read_b32 v84, a154\n"
    "v_accvgpr_read_b32 v87, a155\n"
    "v_pk_fma_f32 v[76:77], v[76:77], v[116:117], v[118:119] op_sel_hi:[1,0,0]\n"
    "v_pk_fma_f32 v[78:79], v[78:79], v[116:117], v[118:119] op_sel_hi:[1,0,0]\n"
    "v_pk_fma_f32 v[80:81], v[80:81], v[116:117], v[118:119] op_sel_hi:[1,0,0]\n"
    "v_pk_fma_f32 v[82:83], v[82:83], v[116:117], v[118:119] op_sel_hi:[1,0,0]\n"
    "v_pk_fma_f32 v[84:85], v[84:85], v[116:117], v[118:119] op_sel_hi:[1,0,0]\n"
    "v_pk_fma_f32 v[86:87], v[86:87], v[116:117], v[118:119] op_sel_hi:[1,0,0]\n"
    "v_cvt_pk_bf16_f32 v88, v76, v77\n"
    "v_cvt_pk_bf16_f32 v89, v78, v79\n"
    "v_cvt_pk_bf16_f32 v90, v80, v81\n"
    "v_cvt_pk_bf16_f32 v91, v82, v83\n"
    "v_cvt_pk_bf16_f32 v92, v84, v85\n"
    "v_cvt_pk_bf16_f32 v93, v86, v87\n"
    "ds_write_b64 v205, v[88:89] offset:12896\n"
    "ds_write_b64 v205, v[90:91] offset:12904\n"
    "ds_write_b64 v205, v[92:93] offset:12912\n"
    "v_accvgpr_read_b32 v4, a40\n"
    "v_accvgpr_read_b32 v7, a41\n"
    "v_accvgpr_read_b32 v10, a42\n"
    "v_accvgpr_read_b32 v13, a43\n"
    "v_accvgpr_read_b32 v5, a104\n"
    "v_accvgpr_read_b32 v8, a105\n"
    "v_accvgpr_read_b32 v11, a106\n"
    "v_accvgpr_read_b32 v14, a107\n"
    "v_accvgpr_read_b32 v6, a168\n"
    "v_accvgpr_read_b32 v9, a169\n"
    "v_accvgpr_read_b32 v12, a170\n"
    "v_accvgpr_read_b32 v15, a171\n"
    "v_pk_fma_f32 v[4:5], v[4:5], v[116:117], v[118:119] op_sel_hi:[1,0,0]\n"
    "v_pk_fma_f32 v[6:7], v[6:7], v[116:117], v[118:119] op_sel_hi:[1,0,0]\n"
    "v_pk_fma_f32 v[8:9], v[8:9], v[116:117], v[118:119] op_sel_hi:[1,0,0]\n"
    "v_pk_fma_f32 v[10:11], v[10:11], v[116:117], v[118:119] op_sel_hi:[1,0,0]\n"
    "v_pk_fma_f32 v[12:13], v[12:13], v[116:117], v[118:119] op_sel_hi:[1,0,0]\n"
    "v_pk_fma_f32 v[14:15], v[14:15], v[116:117], v[118:119] op_sel_hi:[1,0,0]\n"
    "v_cvt_pk_bf16_f32 v16, v4, v5\n"
    "v_cvt_pk_bf16_f32 v17, v6, v7\n"
    "v_cvt_pk_bf16_f32 v18, v8, v9\n"
    "v_cvt_pk_bf16_f32 v19, v10, v11\n"
    "v_cvt_pk_bf16_f32 v20, v12, v13\n"
    "v_cvt_pk_bf16_f32 v21, v14, v15\n"
    "ds_write_b64 v205, v[16:17] offset:12992\n"
    "ds_write_b64 v205, v[18:19] offset:13000\n"
    "ds_write_b64 v205, v[20:21] offset:13008\n"
    "v_accvgpr_read_b32 v22, a56\n"
    "v_accvgpr_read_b32 v25, a57\n"
    "v_accvgpr_read_b32 v28, a58\n"
    "v_accvgpr_read_b32 v31, a59\n"
    "v_accvgpr_read_b32 v23, a120\n"
    "v_accvgpr_read_b32 v26, a121\n"
    "v_accvgpr_read_b32 v29, a122\n"
    "v_accvgpr_read_b32 v32, a123\n"
    "v_accvgpr_read_b32 v24, a184\n"
    "v_accvgpr_read_b32 v27, a185\n"
    "v_accvgpr_read_b32 v30, a186\n"
    "v_accvgpr_read_b32 v33, a187\n"
    "v_pk_fma_f32 v[22:23], v[22:23], v[116:117], v[118:119] op_sel_hi:[1,0,0]\n"
    "v_pk_fma_f32 v[24:25], v[24:25], v[116:117], v[118:119] op_sel_hi:[1,0,0]\n"
    "v_pk_fma_f32 v[26:27], v[26:27], v[116:117], v[118:119] op_sel_hi:[1,0,0]\n"
    "v_pk_fma_f32 v[28:29], v[28:29], v[116:117], v[118:119] op_sel_hi:[1,0,0]\n"
    "v_pk_fma_f32 v[30:31], v[30:31], v[116:117], v[118:119] op_sel_hi:[1,0,0]\n"
    "v_pk_fma_f32 v[32:33], v[32:33], v[116:117], v[118:119] op_sel_hi:[1,0,0]\n"
    "v_cvt_pk_bf16_f32 v34, v22, v23\n"
    "v_cvt_pk_bf16_f32 v35, v24, v25\n"
    "v_cvt_pk_bf16_f32 v36, v26, v27\n"
    "v_cvt_pk_bf16_f32 v37, v28, v29\n"
    "v_cvt_pk_bf16_f32 v38, v30, v31\n"
    "v_cvt_pk_bf16_f32 v39, v32, v33\n"
    "ds_write_b64 v205, v[34:35] offset:13088\n"
    "ds_write_b64 v205, v[36:37] offset:13096\n"
    "ds_write_b64 v205, v[38:39] offset:13104\n"
    "ds_read_b128 v[172:175], v209 offset:12800\n"
    "ds_read_b128 v[176:179], v210 offset:12800\n"
    "ds_read_b128 v[180:183], v211 offset:12800\n"
    "ds_read_b128 v[184:187], v209 offset:16000\n"
    "ds_read_b128 v[188:191], v210 offset:16000\n"
    "ds_read_b128 v[192:195], v211 offset:16000\n"
    "v_accvgpr_read_b32 v40, a12\n"
    "v_accvgpr_read_b32 v43, a13\n"
    "v_accvgpr_read_b32 v46, a14\n"
    "v_accvgpr_read_b32 v49, a15\n"
    "v_accvgpr_read_b32 v41, a76\n"
    "v_accvgpr_read_b32 v44, a77\n"
    "v_accvgpr_read_b32 v47, a78\n"
    "v_accvgpr_read_b32 v50, a79\n"
    "v_accvgpr_read_b32 v42, a140\n"
    "v_accvgpr_read_b32 v45, a141\n"
    "v_accvgpr_read_b32 v48, a142\n"
    "v_accvgpr_read_b32 v51, a143\n"
    "v_pk_fma_f32 v[40:41], v[40:41], v[120:121], v[122:123] op_sel_hi:[1,0,0]\n"
    "v_pk_fma_f32 v[42:43], v[42:43], v[120:121], v[122:123] op_sel_hi:[1,0,0]\n"
    "v_pk_fma_f32 v[44:45], v[44:45], v[120:121], v[122:123] op_sel_hi:[1,0,0]\n"
    "v_pk_fma_f32 v[46:47], v[46:47], v[120:121], v[122:123] op_sel_hi:[1,0,0]\n"
    "v_pk_fma_f32 v[48:49], v[48:49], v[120:121], v[122:123] op_sel_hi:[1,0,0]\n"
    "v_pk_fma_f32 v[50:51], v[50:51], v[120:121], v[122:123] op_sel_hi:[1,0,0]\n"
    "v_cvt_pk_bf16_f32 v52, v40, v41\n"
    "v_cvt_pk_bf16_f32 v53, v42, v43\n"
    "v_cvt_pk_bf16_f32 v54, v44, v45\n"
    "v_cvt_pk_bf16_f32 v55, v46, v47\n"
    "v_cvt_pk_bf16_f32 v56, v48, v49\n"
    "v_cvt_pk_bf16_f32 v57, v50, v51\n"
    "ds_write_b64 v205, v[52:53] offset:19200\n"
    "ds_write_b64 v205, v[54:55] offset:19208\n"
    "ds_write_b64 v205, v[56:57] offset:19216\n"
    "s_add_u32 s73, s73, 4096\n"
    "s_waitcnt lgkmcnt(8)\n"
    "buffer_store_dwordx4 v[172:175], v1, s[24:27], s73 offen offset:0 sc1\n"
    "s_waitcnt lgkmcnt(7)\n"
    "buffer_store_dwordx4 v[176:179], v1, s[24:27], s73 offen offset:1024 sc1\n"
    "s_waitcnt lgkmcnt(6)\n"
    "buffer_store_dwordx4 v[180:183], v1, s[24:27], s73 offen offset:2048 sc1\n"
    "s_waitcnt lgkmcnt(5)\n"
    "buffer_store_dwordx4 v[184:187], v1, s[24:27], s73 offen offset:3072 sc1\n"
    "s_add_u32 s73, s73, 4096\n"
    "s_waitcnt lgkmcnt(4)\n"
    "buffer_store_dwordx4 v[188:191], v1, s[24:27], s73 offen offset:0 sc1\n"
    "s_waitcnt lgkmcnt(3)\n"
    "buffer_store_dwordx4 v[192:195], v1, s[24:27], s73 offen offset:1024 sc1\n"
    "v_accvgpr_read_b32 v58, a28\n"
    "v_accvgpr_read_b32 v61, a29\n"
    "v_accvgpr_read_b32 v64, a30\n"
    "v_accvgpr_read_b32 v67, a31\n"
    "v_accvgpr_read_b32 v59, a92\n"
    "v_accvgpr_read_b32 v62, a93\n"
    "v_accvgpr_read_b32 v65, a94\n"
    "v_accvgpr_read_b32 v68, a95\n"
    "v_accvgpr_read_b32 v60, a156\n"
    "v_accvgpr_read_b32 v63, a157\n"
    "v_accvgpr_read_b32 v66, a158\n"
    "v_accvgpr_read_b32 v69, a159\n"
    "v_pk_fma_f32 v[58:59], v[58:59], v[120:121], v[122:123] op_sel_hi:[1,0,0]\n"
    "v_pk_fma_f32 v[60:61], v[60:61], v[120:121], v[122:123] op_sel_hi:[1,0,0]\n"
    "v_pk_fma_f32 v[62:63], v[62:63], v[120:121], v[122:123] op_sel_hi:[1,0,0]\n"
    "v_pk_fma_f32 v[64:65], v[64:65], v[120:121], v[122:123] op_sel_hi:[1,0,0]\n"
    "v_pk_fma_f32 v[66:67], v[66:67], v[120:121], v[122:123] op_sel_hi:[1,0,0]\n"
    "v_pk_fma_f32 v[68:69], v[68:69], v[120:121], v[122:123] op_sel_hi:[1,0,0]\n"
    "v_cvt_pk_bf16_f32 v70, v58, v59\n"
    "v_cvt_pk_bf16_f32 v71, v60, v61\n"
    "v_cvt_pk_bf16_f32 v72, v62, v63\n"
    "v_cvt_pk_bf16_f32 v73, v64, v65\n"
    "v_cvt_pk_bf16_f32 v74, v66, v67\n"
    "v_cvt_pk_bf16_f32 v75, v68, v69\n"
    "ds_write_b64 v205, v[70:71] offset:19296\n"
    "ds_write_b64 v205, v[72:73] offset:19304\n"
    "ds_write_b64 v205, v[74:75] offset:19312\n"
    "v_accvgpr_read_b32 v76, a44\n"
    "v_accvgpr_read_b32 v79, a45\n"
    "v_accvgpr_read_b32 v82, a46\n"
    "v_accvgpr_read_b32 v85, a47\n"
    "v_accvgpr_read_b32 v77, a108\n"
    "v_accvgpr_read_b32 v80, a109\n"
    "v_accvgpr_read_b32 v83, a110\n"
    "v_accvgpr_read_b32 v86, a111\n"
    "v_accvgpr_read_b32 v78, a172\n"
    "v_accvgpr_read_b32 v81, a173\n"
    "v_accvgpr_read_b32 v84, a174\n"
    "v_accvgpr_read_b32 v87, a175\n"
    "v_pk_fma_f32 v[76:77], v[76:77], v[120:121], v[122:123] op_sel_hi:[1,0,0]\n"
    "v_pk_fma_f32 v[78:79], v[78:79], v[120:121], v[122:123] op_sel_hi:[1,0,0]\n"
    "v_pk_fma_f32 v[80:81], v[80:81], v[120:121], v[122:123] op_sel_hi:[1,0,0]\n"
    "v_pk_fma_f32 v[82:83], v[82:83], v[120:121], v[122:123] op_sel_hi:[1,0,0]\n"
    "v_pk_fma_f32 v[84:85], v[84:85], v[120:121], v[122:123] op_sel_hi:[1,0,0]\n"
    "v_pk_fma_f32 v[86:87], v[86:87], v[120:121], v[122:123] op_sel_hi:[1,0,0]\n"
    "v_cvt_pk_bf16_f32 v88, v76, v77\n"
    "v_cvt_pk_bf16_f32 v89, v78, v79\n"
    "v_cvt_pk_bf16_f32 v90, v80, v81\n"
    "v_cvt_pk_bf16_f32 v91, v82, v83\n"
    "v_cvt_pk_bf16_f32 v92, v84, v85\n"
    "v_cvt_pk_bf16_f32 v93, v86, v87\n"
    "ds_write_b64 v205, v[88:89] offset:19392\n"
    "ds_write_b64 v205, v[90:91] offset:19400\n"
    "ds_write_b64 v205, v[92:93] offset:19408\n"
    "v_accvgpr_read_b32 v4, a60\n"
    "v_accvgpr_read_b32 v7, a61\n"
    "v_accvgpr_read_b32 v10, a62\n"
    "v_accvgpr_read_b32 v13, a63\n"
    "v_accvgpr_read_b32 v5, a124\n"
    "v_accvgpr_read_b32 v8, a125\n"
    "v_accvgpr_read_b32 v11, a126\n"
    "v_accvgpr_read_b32 v14, a127\n"
    "v_accvgpr_read_b32 v6, a188\n"
    "v_accvgpr_read_b32 v9, a189\n"
    "v_accvgpr_read_b32 v12, a190\n"
    "v_accvgpr_read_b32 v15, a191\n"
    "v_pk_fma_f32 v[4:5], v[4:5], v[120:121], v[122:123] op_sel_hi:[1,0,0]\n"
    "v_pk_fma_f32 v[6:7], v[6:7], v[120:121], v[122:123] op_sel_hi:[1,0,0]\n"
    "v_pk_fma_f32 v[8:9], v[8:9], v[120:121], v[122:123] op_sel_hi:[1,0,0]\n"
    "v_pk_fma_f32 v[10:11], v[10:11], v[120:121], v[122:123] op_sel_hi:[1,0,0]\n"
    "v_pk_fma_f32 v[12:13], v[12:13], v[120:121], v[122:123] op_sel_hi:[1,0,0]\n"
    "v_pk_fma_f32 v[14:15], v[14:15], v[120:121], v[122:123] op_sel_hi:[1,0,0]\n"
    "v_cvt_pk_bf16_f32 v16, v4, v5\n"
    "v_cvt_pk_bf16_f32 v17, v6, v7\n"
    "v_cvt_pk_bf16_f32 v18, v8, v9\n"
    "v_cvt_pk_bf16_f32 v19, v10, v11\n"
    "v_cvt_pk_bf16_f32 v20, v12, v13\n"
    "v_cvt_pk_bf16_f32 v21, v14, v15\n"
    "ds_write_b64 v205, v[16:17] offset:19488\n"
    "ds_write_b64 v205, v[18:19] offset:19496\n"
    "ds_write_b64 v205, v[20:21] offset:19504\n"
    "ds_read_b128 v[124:127], v209 offset:19200\n"
    "ds_read_b128 v[128:131], v210 offset:19200\n"
    "ds_read_b128 v[132:135], v211 offset:19200\n"
    "ds_read_b128 v[136:139], v209 offset:22400\n"
    "ds_read_b128 v[140:143], v210 offset:22400\n"
    "ds_read_b128 v[144:147], v211 offset:22400\n"
    "s_waitcnt lgkmcnt(5)\n"
    "buffer_store_dwordx4 v[124:127], v1, s[24:27], s73 offen offset:2048 sc1\n"
    "s_waitcnt lgkmcnt(4)\n"
    "buffer_store_dwordx4 v[128:131], v1, s[24:27], s73 offen offset:3072 sc1\n"
    "s_add_u32 s73, s73, 4096\n"
    "s_waitcnt lgkmcnt(3)\n"
    "buffer_store_dwordx4 v[132:135], v1, s[24:27], s73 offen offset:0 sc1\n"
    "s_waitcnt lgkmcnt(2)\n"
    "buffer_store_dwordx4 v[136:139], v1, s[24:27], s73 offen offset:1024 sc1\n"
    "s_waitcnt lgkmcnt(1)\n"
    "buffer_store_dwordx4 v[140:143], v1, s[24:27], s73 offen offset:2048 sc1\n"
    "s_waitcnt lgkmcnt(0)\n"
    "buffer_store_dwordx4 v[144:147], v1, s[24:27], s73 offen offset:3072 sc1\n"
    "s_cmp_eq_u32 %0, 0\n"
    "s_cbranch_scc1 .Lfin\n"
    "s_waitcnt vmcnt(0)\n"
    ".Lfin:\n"
    "s_branch .Lend\n"
    ".Lpoll1:\n"
    "s_sleep 1\n"
    "v_cmp_eq_u64 s[54:55], v[236:237], v[238:239]\n"
    "s_nop 1\n"
    "s_cmp_eq_u64 s[54:55], -1\n"
    "s_cbranch_scc1 .Lpoll1l\n"
    "buffer_load_dwordx2 v[236:237], v241, s[36:39], s81 offen offset:0 sc1\n"
    "s_waitcnt vmcnt(0)\n"
    "v_cmp_eq_u64 s[54:55], v[236:237], v[238:239]\n"
    ".Lpoll1l:\n"
    "ds_read_b32 v245, v247\n"
    "s_waitcnt lgkmcnt(0)\n"
    "v_cmp_le_u32 s[56:57], s59, v245\n"
    "s_add_u32 s58, s58, 1\n"
    "s_nop 1\n"
    "s_and_b64 s[54:55], s[54:55], s[56:57]\n"
    "s_cmp_eq_u64 s[54:55], -1\n"
    "s_cbranch_scc1 .Lpoll1r\n"
    "s_cmp_lt_u32 s58, 0x80000\n"
    "s_cbranch_scc1 .Lpoll1\n"
    "s_branch .Lpoll1r\n"
    ".Lpoll2:\n"
    "s_sleep 1\n"
    "v_cmp_eq_u64 s[54:55], v[236:237], v[238:239]\n"
    "s_nop 1\n"
    "s_cmp_eq_u64 s[54:55], -1\n"
    "s_cbranch_scc1 .Lpoll2l\n"
    "buffer_load_dwordx2 v[236:237], v241, s[36:39], s81 offen offset:0 sc1\n"
    "s_waitcnt vmcnt(0)\n"
    "v_cmp_eq_u64 s[54:55], v[236:237], v[238:239]\n"
    ".Lpoll2l:\n"
    "ds_read_b32 v245, v247\n"
    "s_waitcnt lgkmcnt(0)\n"
    "v_cmp_le_u32 s[56:57], s59, v245\n"
    "s_add_u32 s58, s58, 1\n"
    "s_nop 1\n"
    "s_and_b64 s[54:55], s[54:55], s[56:57]\n"
    "s_cmp_eq_u64 s[54:55], -1\n"
    "s_cbranch_scc1 .Lpoll2r\n"
    "s_cmp_lt_u32 s58, 0x80000\n"
    "s_cbranch_scc1 .Lpoll2\n"
    "s_branch .Lpoll2r\n"
    ".Lpoll3:\n"
    "s_sleep 1\n"
    "v_cmp_eq_u64 s[54:55], v[236:237], v[238:239]\n"
    "s_nop 1\n"
    "s_cmp_eq_u64 s[54:55], -1\n"
    "s_cbranch_scc1 .Lpoll3l\n"
    "buffer_load_dwordx2 v[236:237], v241, s[36:39], s81 offen offset:0 sc1\n"
    "s_waitcnt vmcnt(0)\n"
    "v_cmp_eq_u64 s[54:55], v[236:237], v[238:239]\n"
    ".Lpoll3l:\n"
    "ds_read_b32 v245, v247\n"
    "s_waitcnt lgkmcnt(0)\n"
    "v_cmp_le_u32 s[56:57], s59, v245\n"
    "s_add_u32 s58, s58, 1\n"
    "s_nop 1\n"
    "s_and_b64 s[54:55], s[54:55], s[56:57]\n"
    "s_cmp_eq_u64 s[54:55], -1\n"
    "s_cbranch_scc1 .Lpoll3r\n"
    "s_cmp_lt_u32 s58, 0x80000\n"
    "s_cbranch_scc1 .Lpoll3\n"
    "s_branch .Lpoll3r\n"
    ".Lpoll4:\n"
    "s_sleep 1\n"
    "v_cmp_eq_u64 s[54:55], v[236:237], v[238:239]\n"
    "s_nop 1\n"
    "s_cmp_eq_u64 s[54:55], -1\n"
    "s_cbranch_scc1 .Lpoll4l\n"
    "buffer_load_dwordx2 v[236:237], v241, s[36:39], s81 offen offset:0 sc1\n"
    "s_waitcnt vmcnt(0)\n"
    "v_cmp_eq_u64 s[54:55], v[236:237], v[238:239]\n"
    ".Lpoll4l:\n"
    "ds_read_b32 v245, v247\n"
    "s_waitcnt lgkmcnt(0)\n"
    "v_cmp_le_u32 s[56:57], s59, v245\n"
    "s_add_u32 s58, s58, 1\n"
    "s_nop 1\n"
    "s_and_b64 s[54:55], s[54:55], s[56:57]\n"
    "s_cmp_eq_u64 s[54:55], -1\n"
    "s_cbranch_scc1 .Lpoll4r\n"
    "s_cmp_lt_u32 s58, 0x80000\n"
    "s_cbranch_scc1 .Lpoll4\n"
    "s_branch .Lpoll4r\n"
    ".Lpoll5:\n"
    "s_sleep 1\n"
    "ds_read_b32 v245, v247\n"
    "s_waitcnt lgkmcnt(0)\n"
    "v_cmp_le_u32 s[56:57], s59, v245\n"
    "s_add_u32 s58, s58, 1\n"
    "s_nop 1\n"
    "s_cmp_eq_u64 s[56:57], -1\n"
    "s_cbranch_scc1 .Lpoll5r\n"
    "s_cmp_lt_u32 s58, 0x80000\n"
    "s_cbranch_scc1 .Lpoll5\n"
    "s_branch .Lpoll5r\n"
    ".Lend:\n"
    : "=&s"(bad), "+s"(ka) : "s"(b), "v"(tid)
    : "memory", "vcc", "s16", "s17", "s18", "s19", "s20", "s21", "s22", "s23", "s24", "s25", "s26", "s27", "s28", "s29", "s30", "s31", "s36", "s37", "s38", "s39", "s40", "s41", "s42", "s43", "s44", "s45", "s46", "s47", "s48", "s49", "s50", "s51", "s52", "s53", "s54", "s55", "s56", "s57", "s58", "s59", "s60", "s61", "s62", "s63", "s64", "s65", "s66", "s67", "s68", "s69", "s70", "s71", "s72", "s73", "s74", "s75", "s76", "s77", "s78", "s79", "s80", "s81", "s82", "s83", "s84", "s85", "s86", "s87", "s88", "s89", "s90", "s91", "s92", "s93", "s94", "s95", "s96", "s97", "s98", "s99", "v1", "v2", "v3", "v4", "v5", "v6", "v7", "v8", "v9", "v10", "v11", "v12", "v13", "v14", "v15", "v16", "v17", "v18", "v19", "v20", "v21", "v22", "v23", "v24", "v25", "v26", "v27", "v28", "v29", "v30", "v31", "v32", "v33", "v34", "v35", "v36", "v37", "v38", "v39", "v40", "v41", "v42", "v43", "v44", "v45", "v46", "v47", "v48", "v49", "v50", "v51", "v52", "v53", "v54", "v55", "v56", "v57", "v58", "v59", "v60", "v61", "v62", "v63", "v64", "v65", "v66", "v67", "v68", "v69", "v70", "v71", "v72", "v73", "v74", "v75", "v76", "v77", "v78", "v79", "v80", "v81", "v82", "v83", "v84", "v85", "v86", "v87", "v88", "v89", "v90", "v91", "v92", "v93", "v94", "v95", "v96", "v97", "v98", "v99", "v100", "v101", "v102", "v103", "v104", "v105", "v106", "v107", "v108", "v109", "v110", "v111", "v112", "v113", "v114", "v115", "v116", "v117", "v118", "v119", "v120", "v121", "v122", "v123", "v124", "v125", "v126", "v127", "v128", "v129", "v130", "v131", "v132", "v133", "v134", "v135", "v136", "v137", "v138", "v139", "v140", "v141", "v142", "v143", "v144", "v145", "v146", "v147", "v148", "v149", "v150", "v151", "v152", "v153", "v154", "v155", "v156", "v157", "v158", "v159", "v160", "v161", "v162", "v163", "v164", "v165", "v166", "v167", "v168", "v169", "v170", "v171", "v172", "v173", "v174", "v175", "v176", "v177", "v178", "v179", "v180", "v181", "v182", "v183", "v184", "v185", "v186", "v187", "v188", "v189", "v190", "v191", "v192", "v193", "v194", "v195", "v196", "v197", "v198", "v199", "v200", "v201", "v202", "v203", "v204", "v205", "v206", "v207", "v208", "v209", "v210", "v211", "v212", "v213", "v214", "v215", "v216", "v217", "v218", "v219", "v220", "v221", "v222", "v223", "v224", "v225", "v226", "v227", "v228", "v229", "v230", "v231", "v232", "v233", "v234", "v235", "v236", "v237", "v238", "v239", "v240", "v241", "v242", "v243", "v244", "v245", "v246", "v247", "v248", "v249", "v250", "v251", "v252", "v253", "v254", "v255", "a0", "a1", "a2", "a3", "a4", "a5", "a6", "a7", "a8", "a9", "a10", "a11", "a12", "a13", "a14", "a15", "a16", "a17", "a18", "a19", "a20", "a21", "a22", "a23", "a24", "a25", "a26", "a27", "a28", "a29", "a30", "a31", "a32", "a33", "a34", "a35", "a36", "a37", "a38", "a39", "a40", "a41", "a42", "a43", "a44", "a45", "a46", "a47", "a48", "a49", "a50", "a51", "a52", "a53", "a54", "a55", "a56", "a57", "a58", "a59", "a60", "a61", "a62", "a63", "a64", "a65", "a66", "a67", "a68", "a69", "a70", "a71", "a72", "a73", "a74", "a75", "a76", "a77", "a78", "a79", "a80", "a81", "a82", "a83", "a84", "a85", "a86", "a87", "a88", "a89", "a90", "a91", "a92", "a93", "a94", "a95", "a96", "a97", "a98", "a99", "a100", "a101", "a102", "a103", "a104", "a105", "a106", "a107", "a108", "a109", "a110", "a111", "a112", "a113", "a114", "a115", "a116", "a117", "a118", "a119", "a120", "a121", "a122", "a123", "a124", "a125", "a126", "a127", "a128", "a129", "a130", "a131", "a132", "a133", "a134", "a135", "a136", "a137", "a138", "a139", "a140", "a141", "a142", "a143", "a144", "a145", "a146", "a147", "a148", "a149", "a150", "a151", "a152", "a153", "a154", "a155", "a156", "a157", "a158", "a159", "a160", "a161", "a162", "a163", "a164", "a165", "a166", "a167", "a168", "a169", "a170", "a171", "a172", "a173", "a174", "a175", "a176", "a177", "a178", "a179", "a180", "a181", "a182", "a183", "a184", "a185", "a186", "a187", "a188", "a189", "a190", "a191", "a192", "a193", "a194", "a195", "a196", "a197", "a198", "a199", "a200", "a201", "a202", "a203", "a204", "a205", "a206", "a207", "a208", "a209", "a210", "a211", "a212", "a213", "a214", "a215", "a216", "a217", "a218", "a219", "a220", "a221", "a222", "a223", "a224", "a225", "a226", "a227", "a228", "a229", "a230", "a231", "a232", "a233", "a234", "a235", "a236", "a237", "a238", "a239", "a240", "a241", "a242", "a243", "a244", "a245", "a246", "a247", "a248", "a249", "a250", "a251", "a252", "a253", "a254", "a255");
  if (bad) {
    slow_rows((const Args*)ka, (b & 7) * 4 + ((b >> 3) & 3), (b >> 5) * 256 + (tid & 192), tid & 63);
  }
}

extern "C" void kernel_launch(void* const* d_in, const int* in_sizes, int n_in,
                              void* d_out, int out_size, void* d_ws, size_t ws_size,
                              hipStream_t stream) {
  (void)hipFuncSetAttribute((const void*)attn_fwd, hipFuncAttributeMaxDynamicSharedMemorySize, SHM);
  attn_fwd<<<256, 256, SHM, stream>>>((const unsigned short*)d_in[0], (const int*)d_in[1], (const int*)d_in[2], (const unsigned short*)d_in[3], (const unsigned short*)d_in[4],
                                      (const unsigned short*)d_in[5], (const unsigned short*)d_in[6], (char*)d_ws, (unsigned short*)d_out);
}
```

```cpp
#include <hip/hip_runtime.h>

constexpr int D = 192, NH = 32, S = 2048;
constexpr int SHM = 155968;

struct Args { const unsigned short* q; const int* qk; const int* qv; const unsigned short* ksc; const unsigned short* kzr;
              const unsigned short* vsc; const unsigned short* vzr; char* ws; unsigned short* o; };

__device__ __forceinline__ float bf2f(unsigned short x) { return __uint_as_float((unsigned)x << 16); }
__device__ __forceinline__ float rbf(float x) { return (float)(__bf16)x; }

__device__ __forceinline__ void slow_rows(const Args* a, unsigned h, unsigned row0, unsigned lane) {
  const unsigned short* q = a->q + ((size_t)h * S + row0) * D;
  unsigned short* o = a->o + ((size_t)h * S + row0) * D;
  const int* qk = a->qk + (size_t)h * S * D; const int* qv = a->qv + (size_t)h * S * D;
  const unsigned short* ks = a->ksc + h * 32 * D; const unsigned short* kz = a->kzr + h * 32 * D;
  const unsigned short* vs = a->vsc + h * S; const unsigned short* vz = a->vzr + h * S;
#pragma unroll 1
  for (int r = 0; r < 64; ++r) {
    float m = -INFINITY, l = 0.f, a0 = 0.f, a1 = 0.f, a2 = 0.f;
#pragma unroll 1
    for (int t = 0; t < 32; ++t) {
      const int* kr = qk + (size_t)(t * 64 + lane) * D;
      float s = 0.f;
#pragma unroll 4
      for (int d = 0; d < D; ++d) s = fmaf(bf2f(q[r * D + d]), rbf(rbf(rbf((float)kr[d]) - bf2f(kz[t * D + d])) * bf2f(ks[t * D + d])), s);
      s *= 0.07216878364870322f;
      float mx = s;
      for (int x = 32; x; x >>= 1) mx = fmaxf(mx, __shfl_xor(mx, x));
      const float mn = fmaxf(m, mx), c = __expf(m - mn), p = __expf(s - mn);
      float ps = p;
      for (int x = 32; x; x >>= 1) ps += __shfl_xor(ps, x);
      l = l * c + ps; a0 *= c; a1 *= c; a2 *= c; m = mn;
#pragma unroll 2
      for (int k = 0; k < 64; ++k) {
        const float pk = __shfl(p, k), z = bf2f(vz[t * 64 + k]), sc = bf2f(vs[t * 64 + k]);
        const int* vr = qv + (size_t)(t * 64 + k) * D + lane;
        a0 = fmaf(pk, rbf(rbf(rbf((float)vr[0]) - z) * sc), a0); a1 = fmaf(pk, rbf(rbf(rbf((float)vr[64]) - z) * sc), a1); a2 = fmaf(pk, rbf(rbf(rbf((float)vr[128]) - z) * sc), a2);
      }
    }
    const float i = 1.f / l;
    o[r * D + lane] = __builtin_bit_cast(unsigned short, (__bf16)(a0 * i)); o[r * D + 64 + lane] = __builtin_bit_cast(unsigned short, (__bf16)(a1 * i));
    o[r * D + 128 + lane] = __builtin_bit_cast(unsigned short, (__bf16)(a2 * i));
  }
}

__global__ __launch_bounds__(256, 1) void attn_fwd(const unsigned short* q, const int* qk, const int* qv, const unsigned short* ksc, const unsigned short* kzr,
                                                   const unsigned short* vsc, const unsigned short* vzr, char* ws, unsigned short* o) {
  unsigned long long ka = (unsigned long long)__builtin_amdgcn_kernarg_segment_ptr();
  unsigned bad, b = blockIdx.x;
  const unsigned tid = threadIdx.x;
  asm volatile(
    "s_load_dwordx2 s[28:29], %1, 8\n"
    "s_load_dwordx2 s[40:41], %1, 16\n"
    "s_load_dwordx2 s[44:45], %1, 24\n"
    "s_load_dwordx2 s[88:89], %1, 32\n"
    "s_load_dwordx2 s[36:37], %1, 56\n"
    "s_load_dwordx2 s[92:93], %1, 40\n"
    "s_load_dwordx2 s[96:97], %1, 48\n"
    "s_load_dwordx2 s[20:21], %1, 0\n"
    "s_load_dwordx2 s[16:17], %1, 56\n"
    "s_load_dwordx2 s[24:25], %1, 64\n"
    "v_readfirstlane_b32 s84, %3\n"
    "v_and_b32 v156, 63, %3\n"
    "s_and_b32 s52, %2, 7\n"
    "s_bfe_u32 s72, %2, 0x20003\n"
    "s_lshr_b32 s53, %2, 5\n"
    "s_lshl_b32 s52, s52, 2\n"
    "s_lshr_b32 s84, s84, 6\n"
    "s_or_b32 s52, s52, s72\n"
    "v_lshlrev_b32 v1, 4, v156\n"
    "s_getpc_b64 s[54:55]\n"
    ".Lpc:\n"
    "s_lshl_b32 s72, s72, 13\n"
    "v_lshl_add_u32 v246, v156, 7, s72\n"
    "v_min_u32 v246, .Lend-.Lpc-4, v246\n"
    "global_load_dword v246, v246, s[54:55]\n"
    "s_mov_b32 s30, 1572864\n"
    "s_mov_b32 s31, 0x20000\n"
    "s_mov_b32 s42, 1572864\n"
    "s_mov_b32 s43, 0x20000\n"
    "s_mov_b32 s46, 12288\n"
    "s_mov_b32 s47, 0x20000\n"
    "s_mov_b32 s90, 12288\n"
    "s_mov_b32 s91, 0x20000\n"
    "s_mov_b32 s38, 4864\n"
    "s_mov_b32 s39, 0x20000\n"
    "s_mov_b32 s94, 4096\n"
    "s_mov_b32 s95, 0x20000\n"
    "s_mov_b32 s98, 4096\n"
    "s_mov_b32 s99, 0x20000\n"
    "s_mov_b32 s22, 98304\n"
    "s_mov_b32 s23, 0x20000\n"
    "s_mov_b32 s18, 1622016\n"
    "s_mov_b32 s19, 0x20000\n"
    "s_mov_b32 s26, 98304\n"
    "s_mov_b32 s27, 0x20000\n"
    "s_lshr_b32 s79, s84, 1\n"
    "s_and_b32 s59, s84, 1\n"
    "s_cmp_ge_u32 s53, 6\n"
    "s_cselect_b32 s83, 1, 0\n"
    "s_or_b32 s83, s83, s84\n"
    "s_xor_b32 s58, s79, 0\n"
    "s_mul_i32 s71, s58, 49152\n"
    "s_lshl_b32 s80, s58, 7\n"
    "s_add_u32 s80, s80, 256\n"
    "s_mul_i32 s72, s52, 1572864\n"
    "s_mul_i32 s73, s52, 12288\n"
    "s_lshl_b32 s74, s52, 13\n"
    "v_lshrrev_b32 v157, 2, v156\n"
    "v_and_b32 v158, 3, v156\n"
    "v_lshrrev_b32 v162, 1, v156\n"
    "v_and_b32 v162, 16, v162\n"
    "v_bfe_u32 v159, v156, 3, 2\n"
    "v_or_b32 v162, v162, v159\n"
    "v_mul_u32_u24 v162, 768, v162\n"
    "v_and_b32 v163, 0x70, v1\n"
    "v_add_u32 v162, v162, v163\n"
    "s_mul_i32 s69, s59, 384\n"
    "s_mul_i32 s68, s79, 6144\n"
    "s_add_u32 s68, s68, s69\n"
    "s_bitcmp1_b32 s83, 0\n"
    "s_cbranch_scc1 .Lf2b\n"
    "v_mov_b32 v242, v162\n"
    "v_mov_b32 v243, v163\n"
    "s_lshl_b32 s63, s53, 7\n"
    "v_mul_u32_u24 v160, 768, v157\n"
    "v_lshl_add_u32 v160, v158, 4, v160\n"
    "s_mul_i32 s81, s58, 384\n"
    "s_lshl_b32 s75, s53, 6\n"
    "s_add_u32 s81, s81, s75\n"
    "s_add_u32 s75, s71, s63\n"
    "s_add_u32 s76, s75, 6144\n"
    "s_add_u32 s77, s75, 24576\n"
    "s_add_u32 s78, s75, 30720\n"
    "s_cmp_eq_u32 s58, 0\n"
    "s_cselect_b32 s75, 0x7f000000, s75\n"
    "s_cselect_b32 s76, 0x7f000000, s76\n"
    "s_waitcnt lgkmcnt(0)\n"
    "s_add_u32 s44, s44, s73\n"
    "s_addc_u32 s45, s45, 0\n"
    "s_add_u32 s88, s88, s73\n"
    "s_addc_u32 s89, s89, 0\n"
    "buffer_load_dwordx4 v[164:167], v160, s[44:47], s81 offen offset:0\n"
    "buffer_load_dwordx4 v[168:171], v160, s[88:91], s81 offen offset:0\n"
    "s_add_u32 s28, s28, s72\n"
    "s_addc_u32 s29, s29, 0\n"
    "buffer_load_dwordx4 v[204:207], v242, s[28:31], s75 offen offset:0 nt\n"
    "buffer_load_dwordx4 v[208:211], v242, s[28:31], s75 offen offset:3072 nt\n"
    "buffer_load_dwordx4 v[212:215], v242, s[28:31], s76 offen offset:0 nt\n"
    "buffer_load_dwordx4 v[216:219], v242, s[28:31], s76 offen offset:3072 nt\n"
    "buffer_load_dwordx4 v[220:223], v242, s[28:31], s77 offen offset:0 nt\n"
    "buffer_load_dwordx4 v[224:227], v242, s[28:31], s77 offen offset:3072 nt\n"
    "buffer_load_dwordx4 v[228:231], v242, s[28:31], s78 offen offset:0 nt\n"
    "buffer_load_dwordx4 v[232:235], v242, s[28:31], s78 offen offset:3072 nt\n"
    "s_branch .Lf2e\n"
    ".Lf2b:\n"
    "v_mul_u32_u24 v242, 12, v156\n"
    "v_add_u32 v243, 3072, v242\n"
    "s_mul_i32 s63, s53, 6144\n"
    "s_sub_u32 s63, s63, 49152\n"
    "s_cmp_eq_u32 s59, 0\n"
    "s_cselect_b32 s63, 0x7f000000, s63\n"
    "s_add_u32 s75, s71, s63\n"
    "s_waitcnt lgkmcnt(0)\n"
    "s_add_u32 s40, s40, s72\n"
    "s_addc_u32 s41, s41, 0\n"
    "buffer_load_dwordx3 v[204:206], v243, s[40:43], s75 offen offset:1536 nt\n"
    "buffer_load_dwordx3 v[208:210], v243, s[40:43], s75 offen offset:2304 nt\n"
    "buffer_load_dwordx3 v[212:214], v243, s[40:43], s75 offen offset:0 nt\n"
    "buffer_load_dwordx3 v[216:218], v243, s[40:43], s75 offen offset:768 nt\n"
    "buffer_load_dwordx3 v[220:222], v242, s[40:43], s75 offen offset:1536 nt\n"
    "buffer_load_dwordx3 v[224:226], v242, s[40:43], s75 offen offset:2304 nt\n"
    "buffer_load_dwordx3 v[228:230], v242, s[40:43], s75 offen offset:0 nt\n"
    "buffer_load_dwordx3 v[232:234], v242, s[40:43], s75 offen offset:768 nt\n"
    "s_add_u32 s44, s44, s73\n"
    "s_addc_u32 s45, s45, 0\n"
    "s_add_u32 s88, s88, s73\n"
    "s_addc_u32 s89, s89, 0\n"
    "s_add_u32 s28, s28, s72\n"
    "s_addc_u32 s29, s29, 0\n"
    ".Lf2e:\n"
    "s_nop 4\n"
    "buffer_load_dwordx4 v[4:7], v162, s[28:31], s68 offen offset:0\n"
    "buffer_load_dwordx4 v[8:11], v162, s[28:31], s68 offen offset:3072\n"
    "buffer_load_dwordx4 v[12:15], v162, s[28:31], s68 offen offset:128\n"
    "buffer_load_dwordx4 v[16:19], v162, s[28:31], s68 offen offset:3200\n"
    "buffer_load_dwordx4 v[20:23], v162, s[28:31], s68 offen offset:256\n"
    "buffer_load_dwordx4 v[24:27], v162, s[28:31], s68 offen offset:3328\n"
    "s_add_u32 s36, s36, s74\n"
    "s_addc_u32 s37, s37, 0\n"
    "s_lshr_b32 s74, s74, 1\n"
    "s_add_u32 s92, s92, s74\n"
    "s_addc_u32 s93, s93, 0\n"
    "s_add_u32 s96, s96, s74\n"
    "s_addc_u32 s97, s97, 0\n"
    "s_mul_i32 s74, s52, 786432\n"
    "s_mul_i32 s75, s53, 98304\n"
    "s_add_u32 s74, s74, s75\n"
    "s_add_u32 s20, s20, s74\n"
    "s_addc_u32 s21, s21, 0\n"
    "s_add_u32 s24, s24, s74\n"
    "s_addc_u32 s25, s25, 0\n"
    "s_mul_i32 s72, s52, 1626112\n"
    "s_add_u32 s72, s72, 262144\n"
    "s_add_u32 s16, s16, s72\n"
    "s_addc_u32 s17, s17, 0\n"
    "v_add_u32 v159, 0, v156\n"
    "v_mul_u32_u24 v159, 2731, v159\n"
    "v_lshrrev_b32 v159, 16, v159\n"
    "v_xor_b32 v159, v159, v156\n"
    "v_and_b32 v159, 7, v159\n"
    "v_and_b32 v88, 0x380, v1\n"
    "v_lshl_add_u32 v88, v159, 4, v88\n"
    "v_add_u32 v159, 64, v156\n"
    "v_mul_u32_u24 v159, 2731, v159\n"
    "v_lshrrev_b32 v159, 16, v159\n"
    "v_xor_b32 v159, v159, v156\n"
    "v_and_b32 v159, 7, v159\n"
    "v_and_b32 v89, 0x380, v1\n"
    "v_lshl_add_u32 v89, v159, 4, v89\n"
    "v_add_u32 v159, 128, v156\n"
    "v_mul_u32_u24 v159, 2731, v159\n"
    "v_lshrrev_b32 v159, 16, v159\n"
    "v_xor_b32 v159, v159, v156\n"
    "v_and_b32 v159, 7, v159\n"
    "v_and_b32 v90, 0x380, v1\n"
    "v_lshl_add_u32 v90, v159, 4, v90\n"
    "s_mul_i32 s65, s84, 6144\n"
    "s_mul_i32 s85, s84, 24576\n"
    "s_add_u32 s67, s65, 98304\n"
    "s_add_u32 s68, s65, 24576\n"
    "s_add_u32 s69, s65, 73728\n"
    "s_mov_b32 s81, 256\n"
    "s_mul_i32 s72, s79, 24576\n"
    "s_add_u32 s70, s72, s65\n"
    "s_add_u32 s70, s70, 73728\n"
    "s_sub_u32 s64, s65, s72\n"
    "s_add_u32 s64, s64, 98304\n"
    "s_add_u32 s73, s72, 73728\n"
    "v_add_u32 v248, s73, v1\n"
    "s_sub_u32 s73, 98304, s72\n"
    "v_add_u32 v249, s73, v1\n"
    "s_or_b32 s73, s53, s84\n"
    "s_cmp_eq_u32 s73, 0\n"
    "s_cselect_b32 s82, 0, 0x7fff0000\n"
    "v_and_b32 v252, 15, v156\n"
    "v_lshlrev_b32 v252, 6, v252\n"
    "v_lshlrev_b32 v159, 2, v156\n"
    "v_and_b32 v159, 32, v159\n"
    "v_xor_b32 v159, v159, v156\n"
    "v_and_or_b32 v252, v159, 48, v252\n"
    "v_mov_b32 v2, v252\n"
    "v_add_u32 v3, 24576, v252\n"
    "v_and_b32 v87, 0x70, v1\n"
    "v_lshl_add_u32 v87, v87, 2, 64\n"
    "v_add_u32 v87, 64, v87\n"
    "v_and_b32 v241, 0xf0, v1\n"
    "v_lshrrev_b32 v241, 1, v241\n"
    "v_add_u32 v241, 640, v241\n"
    "v_lshrrev_b32 v91, 3, v156\n"
    "v_lshlrev_b32 v91, 24, v91\n"
    "v_lshl_add_u32 v91, v156, 3, v91\n"
    "s_lshl_b32 s73, s53, 4\n"
    "s_lshl_b32 s75, s59, 3\n"
    "s_add_u32 s73, s73, s75\n"
    "s_add_u32 s73, s73, 640\n"
    "v_mov_b32 v240, 0x7fff0000\n"
    "v_writelane_b32 v240, s73, 0\n"
    "v_and_b32 v159, 48, v156\n"
    "s_mul_i32 s73, s79, 768\n"
    "s_sub_u32 s73, 131072, s73\n"
    "v_lshlrev_b32 v250, 1, v159\n"
    "v_add_u32 v250, s73, v250\n"
    "v_and_b32 v251, 15, v156\n"
    "v_min_u32 v251, 3, v251\n"
    "v_lshl_add_u32 v251, v251, 7, v159\n"
    "s_add_u32 s73, s73, 256\n"
    "v_add_u32 v251, s73, v251\n"
    "v_and_b32 v84, 15, v156\n"
    "v_lshrrev_b32 v159, 4, v156\n"
    "v_and_b32 v247, 7, v156\n"
    "v_xor_b32 v159, v159, v247\n"
    "v_mul_u32_u24 v84, 384, v84\n"
    "v_lshl_add_u32 v84, v159, 4, v84\n"
    "s_add_u32 s73, s65, 49152\n"
    "v_add_u32 v84, s73, v84\n"
    "v_xor_b32 v85, 64, v84\n"
    "v_subrev_u32 v154, 24576, v84\n"
    "v_subrev_u32 v155, 24576, v85\n"
    "v_lshrrev_b32 v144, 3, %3\n"
    "v_mul_u32_u24 v144, 768, v144\n"
    "v_lshl_add_u32 v144, v247, 4, v144\n"
    "v_add_u32 v144, 131072, v144\n"
    "v_lshl_add_u32 v86, v247, 4, v144\n"
    "v_lshlrev_b32 v247, 2, v156\n"
    "v_add_u32 v247, 155648, v247\n"
    "v_mov_b32 v159, 0\n"
    "ds_write_b32 v247, v159\n"
    "v_mov_b32 v159, 0xff800000\n"
    "v_mov_b32 v160, 155904\n"
    "ds_write_b32 v160, v159\n"
    "s_mul_i32 s73, s84, 3072\n"
    "s_add_u32 s73, s73, 12288\n"
    "v_add_u32 v131, s73, v1\n"
    "s_mul_i32 s74, s59, 384\n"
    "s_add_u32 s73, s73, s74\n"
    "v_add_u32 v132, s73, v163\n"
    "v_and_b32 v159, 32, v156\n"
    "v_lshlrev_b32 v244, 3, v156\n"
    "v_xor_b32 v244, v244, v159\n"
    "v_lshl_add_u32 v244, v159, 3, v244\n"
    "s_lshl_b32 s73, s79, 8\n"
    "s_mul_i32 s74, s59, 6144\n"
    "s_add_u32 s73, s73, s74\n"
    "v_add_u32 v163, s73, v244\n"
    "s_bitcmp1_b32 s83, 0\n"
    "s_cbranch_scc1 .Lf3b\n"
    "s_lshl_b32 s60, s53, 11\n"
    "s_add_u32 s61, s60, 12288\n"
    "s_lshl_b32 s73, s58, 8\n"
    "s_add_u32 s73, s73, 122880\n"
    "v_add_u32 v243, s73, v243\n"
    "v_lshlrev_b32 v160, 9, v157\n"
    "v_lshl_add_u32 v160, v158, 5, v160\n"
    "v_add_u32 v160, s73, v160\n"
    "v_accvgpr_write_b32 a0, 0\n"
    "v_accvgpr_write_b32 a1, 0\n"
    "v_accvgpr_write_b32 a2, 0\n"
    "v_accvgpr_write_b32 a3, 0\n"
    "v_accvgpr_write_b32 a4, 0\n"
    "v_accvgpr_write_b32 a5, 0\n"
    "v_accvgpr_write_b32 a6, 0\n"
    "v_accvgpr_write_b32 a7, 0\n"
    "v_accvgpr_write_b32 a8, 0\n"
    "v_accvgpr_write_b32 a9, 0\n"
    "v_accvgpr_write_b32 a10, 0\n"
    "v_accvgpr_write_b32 a11, 0\n"
    "v_accvgpr_write_b32 a12, 0\n"
    "v_accvgpr_write_b32 a13, 0\n"
    "v_accvgpr_write_b32 a14, 0\n"
    "v_accvgpr_write_b32 a15, 0\n"
    "v_accvgpr_write_b32 a16, 0\n"
    "v_accvgpr_write_b32 a17, 0\n"
    "v_accvgpr_write_b32 a18, 0\n"
    "v_accvgpr_write_b32 a19, 0\n"
    "v_accvgpr_write_b32 a20, 0\n"
    "v_accvgpr_write_b32 a21, 0\n"
    "v_accvgpr_write_b32 a22, 0\n"
    "v_accvgpr_write_b32 a23, 0\n"
    "v_accvgpr_write_b32 a24, 0\n"
    "v_accvgpr_write_b32 a25, 0\n"
    "v_accvgpr_write_b32 a26, 0\n"
    "v_accvgpr_write_b32 a27, 0\n"
    "v_accvgpr_write_b32 a28, 0\n"
    "v_accvgpr_write_b32 a29, 0\n"
    "v_accvgpr_write_b32 a30, 0\n"
    "v_accvgpr_write_b32 a31, 0\n"
    "v_accvgpr_write_b32 a32, 0\n"
    "v_accvgpr_write_b32 a33, 0\n"
    "v_accvgpr_write_b32 a34, 0\n"
    "v_accvgpr_write_b32 a35, 0\n"
    "v_accvgpr_write_b32 a36, 0\n"
    "v_accvgpr_write_b32 a37, 0\n"
    "v_accvgpr_write_b32 a38, 0\n"
    "v_accvgpr_write_b32 a39, 0\n"
    "v_accvgpr_write_b32 a40, 0\n"
    "v_accvgpr_write_b32 a41, 0\n"
    "v_accvgpr_write_b32 a42, 0\n"
    "v_accvgpr_write_b32 a43, 0\n"
    "v_accvgpr_write_b32 a44, 0\n"
    "v_accvgpr_write_b32 a45, 0\n"
    "v_accvgpr_write_b32 a46, 0\n"
    "v_accvgpr_write_b32 a47, 0\n"
    "v_accvgpr_write_b32 a48, 0\n"
    "v_accvgpr_write_b32 a49, 0\n"
    "v_accvgpr_write_b32 a50, 0\n"
    "v_accvgpr_write_b32 a51, 0\n"
    "v_accvgpr_write_b32 a52, 0\n"
    "v_accvgpr_write_b32 a53, 0\n"
    "v_accvgpr_write_b32 a54, 0\n"
    "v_accvgpr_write_b32 a55, 0\n"
    "v_accvgpr_write_b32 a56, 0\n"
    "v_accvgpr_write_b32 a57, 0\n"
    "v_accvgpr_write_b32 a58, 0\n"
    "v_accvgpr_write_b32 a59, 0\n"
    "s_waitcnt vmcnt(14)\n"
    "v_lshlrev_b32 v180, 16, v164\n"
    "v_and_b32 v181, 0xffff0000, v164\n"
    "v_lshlrev_b32 v182, 16, v165\n"
    "v_and_b32 v183, 0xffff0000, v165\n"
    "v_lshlrev_b32 v184, 16, v166\n"
    "v_and_b32 v185, 0xffff0000, v166\n"
    "v_lshlrev_b32 v186, 16, v167\n"
    "v_and_b32 v187, 0xffff0000, v167\n"
    "v_lshlrev_b32 v188, 16, v168\n"
    "v_and_b32 v189, 0xffff0000, v168\n"
    "v_lshlrev_b32 v190, 16, v169\n"
    "v_and_b32 v191, 0xffff0000, v169\n"
    "v_lshlrev_b32 v192, 16, v170\n"
    "v_and_b32 v193, 0xffff0000, v170\n"
    "v_lshlrev_b32 v194, 16, v171\n"
    "v_and_b32 v195, 0xffff0000, v171\n"
    "v_mul_f32 v180, 0x3dd53b95, v180\n"
    "v_mul_f32 v181, 0x3dd53b95, v181\n"
    "v_mul_f32 v182, 0x3dd53b95, v182\n"
    "v_mul_f32 v183, 0x3dd53b95, v183\n"
    "v_mul_f32 v184, 0x3dd53b95, v184\n"
    "v_mul_f32 v185, 0x3dd53b95, v185\n"
    "v_mul_f32 v186, 0x3dd53b95, v186\n"
    "v_mul_f32 v187, 0x3dd53b95, v187\n"
    "v_mul_f32 v188, -v188, v180\n"
    "v_mul_f32 v189, -v189, v181\n"
    "v_mul_f32 v190, -v190, v182\n"
    "v_mul_f32 v191, -v191, v183\n"
    "v_mul_f32 v192, -v192, v184\n"
    "v_mul_f32 v193, -v193, v185\n"
    "v_mul_f32 v194, -v194, v186\n"
    "v_mul_f32 v195, -v195, v187\n"
    "ds_write_b128 v160, v[180:183] offset:0\n"
    "ds_write_b128 v160, v[184:187] offset:16\n"
    "ds_write_b128 v160, v[188:191] offset:128\n"
    "ds_write_b128 v160, v[192:195] offset:144\n"
    "s_branch .Lf3e\n"
    ".Lf3b:\n"
    "s_lshr_b32 s73, s53, 2\n"
    "s_mul_i32 s73, s73, 12288\n"
    "s_and_b32 s74, s53, 3\n"
    "s_lshl_b32 s74, s74, 8\n"
    "s_add_u32 s73, s73, s74\n"
    "s_cmp_eq_u32 s59, 0\n"
    "s_cselect_b32 s73, 0x7f000000, s73\n"
    "s_add_u32 s60, s73, 24576\n"
    "s_add_u32 s61, s73, 28672\n"
    "s_add_u32 s62, s73, 32768\n"
    "v_and_b32 v159, 48, v156\n"
    "v_lshlrev_b32 v244, 6, v159\n"
    "v_and_b32 v159, 0xf0, v1\n"
    "v_add_u32 v244, v244, v159\n"
    "v_accvgpr_write_b32 a0, 0\n"
    "v_accvgpr_write_b32 a1, 0\n"
    "v_accvgpr_write_b32 a2, 0\n"
    "v_accvgpr_write_b32 a3, 0\n"
    "v_accvgpr_write_b32 a4, 0\n"
    "v_accvgpr_write_b32 a5, 0\n"
    "v_accvgpr_write_b32 a6, 0\n"
    "v_accvgpr_write_b32 a7, 0\n"
    "v_accvgpr_write_b32 a8, 0\n"
    "v_accvgpr_write_b32 a9, 0\n"
    "v_accvgpr_write_b32 a10, 0\n"
    "v_accvgpr_write_b32 a11, 0\n"
    "v_accvgpr_write_b32 a12, 0\n"
    "v_accvgpr_write_b32 a13, 0\n"
    "v_accvgpr_write_b32 a14, 0\n"
    "v_accvgpr_write_b32 a15, 0\n"
    "v_accvgpr_write_b32 a16, 0\n"
    "v_accvgpr_write_b32 a17, 0\n"
    "v_accvgpr_write_b32 a18, 0\n"
    "v_accvgpr_write_b32 a19, 0\n"
    "v_accvgpr_write_b32 a20, 0\n"
    "v_accvgpr_write_b32 a21, 0\n"
    "v_accvgpr_write_b32 a22, 0\n"
    "v_accvgpr_write_b32 a23, 0\n"
    "v_accvgpr_write_b32 a24, 0\n"
    "v_accvgpr_write_b32 a25, 0\n"
    "v_accvgpr_write_b32 a26, 0\n"
    "v_accvgpr_write_b32 a27, 0\n"
    "v_accvgpr_write_b32 a28, 0\n"
    "v_accvgpr_write_b32 a29, 0\n"
    "v_accvgpr_write_b32 a30, 0\n"
    "v_accvgpr_write_b32 a31, 0\n"
    "v_accvgpr_write_b32 a32, 0\n"
    "v_accvgpr_write_b32 a33, 0\n"
    "v_accvgpr_write_b32 a34, 0\n"
    "v_accvgpr_write_b32 a35, 0\n"
    "v_accvgpr_write_b32 a36, 0\n"
    "v_accvgpr_write_b32 a37, 0\n"
    "v_accvgpr_write_b32 a38, 0\n"
    "v_accvgpr_write_b32 a39, 0\n"
    "v_accvgpr_write_b32 a40, 0\n"
    "v_accvgpr_write_b32 a41, 0\n"
    "v_accvgpr_write_b32 a42, 0\n"
    "v_accvgpr_write_b32 a43, 0\n"
    "v_accvgpr_write_b32 a44, 0\n"
    "v_accvgpr_write_b32 a45, 0\n"
    "v_accvgpr_write_b32 a46, 0\n"
    "v_accvgpr_write_b32 a47, 0\n"
    "v_accvgpr_write_b32 a48, 0\n"
    "v_accvgpr_write_b32 a49, 0\n"
    "v_accvgpr_write_b32 a50, 0\n"
    "v_accvgpr_write_b32 a51, 0\n"
    "v_accvgpr_write_b32 a52, 0\n"
    "v_accvgpr_write_b32 a53, 0\n"
    "v_accvgpr_write_b32 a54, 0\n"
    "v_accvgpr_write_b32 a55, 0\n"
    "v_accvgpr_write_b32 a56, 0\n"
    "v_accvgpr_write_b32 a57, 0\n"
    "v_accvgpr_write_b32 a58, 0\n"
    "v_accvgpr_write_b32 a59, 0\n"
    ".Lf3e:\n"
    "s_nop 4\n"
    "s_mov_b32 s58, 0\n"
    "s_mov_b32 s59, 8\n"
    "s_waitcnt lgkmcnt(0)\n"
    "s_barrier\n"
    "v_lshlrev_b32 v128, 3, v156\n"
    "v_mov_b32 v129, 0\n"
    "v_lshlrev_b32 v130, 4, %3\n"
    "buffer_load_dwordx2 v[124:125], v128, s[44:47], 0 offen offset:0\n"
    "buffer_load_dwordx2 v[126:127], v128, s[88:91], 0 offen offset:0\n"
    "buffer_load_dwordx2 v[238:239], v129, s[36:39], 0 offen offset:0 sc1\n"
    "s_mul_i32 s44, s84, 3072\n"
    "s_add_u32 m0, s65, 24576\n"
    "s_add_u32 s72, s85, 0\n"
    "buffer_load_dwordx4 v88, s[20:23], s72 offen offset:0 nt lds\n"
    "s_add_u32 m0, s65, 25600\n"
    "s_add_u32 s72, s85, 1024\n"
    "buffer_load_dwordx4 v89, s[20:23], s72 offen offset:0 nt lds\n"
    "s_add_u32 m0, s65, 26624\n"
    "s_add_u32 s72, s85, 2048\n"
    "buffer_load_dwordx4 v90, s[20:23], s72 offen offset:0 nt lds\n"
    "s_add_u32 m0, s65, 27648\n"
    "s_add_u32 s72, s85, 3072\n"
    "buffer_load_dwordx4 v88, s[20:23], s72 offen offset:0 nt lds\n"
    "s_add_u32 m0, s65, 28672\n"
    "s_add_u32 s72, s85, 4096\n"
    "buffer_load_dwordx4 v89, s[20:23], s72 offen offset:0 nt lds\n"
    "s_add_u32 m0, s65, 29696\n"
    "s_add_u32 s72, s85, 5120\n"
    "buffer_load_dwordx4 v90, s[20:23], s72 offen offset:0 nt lds\n"
    "s_waitcnt vmcnt(15)\n"
    "s_bitcmp1_b32 s83, 0\n"
    "s_cbranch_scc1 .Lf4b\n"
    "s_add_u32 s76, s71, s60\n"
    "s_add_u32 s77, s71, s61\n"
    "v_cvt_f32_i32 v204, v204\n"
    "v_cvt_f32_i32 v205, v205\n"
    "v_cvt_f32_i32 v206, v206\n"
    "v_cvt_f32_i32 v207, v207\n"
    "v_cvt_f32_i32 v208, v208\n"
    "v_cvt_f32_i32 v209, v209\n"
    "v_cvt_f32_i32 v210, v210\n"
    "v_cvt_f32_i32 v211, v211\n"
    "v_cvt_f32_i32 v212, v212\n"
    "v_cvt_f32_i32 v213, v213\n"
    "v_cvt_f32_i32 v214, v214\n"
    "v_cvt_f32_i32 v215, v215\n"
    "ds_read_b128 v[92:95], v243 offset:0\n"
    "ds_read_b128 v[96:99], v243 offset:128\n"
    "v_cvt_f32_i32 v216, v216\n"
    "v_cvt_f32_i32 v217, v217\n"
    "v_cvt_f32_i32 v218, v218\n"
    "v_cvt_f32_i32 v219, v219\n"
    "v_cvt_f32_i32 v220, v220\n"
    "v_cvt_f32_i32 v221, v221\n"
    "v_cvt_f32_i32 v222, v222\n"
    "v_cvt_f32_i32 v223, v223\n"
    "v_cvt_f32_i32 v224, v224\n"
    "v_cvt_f32_i32 v225, v225\n"
    "v_cvt_f32_i32 v226, v226\n"
    "v_cvt_f32_i32 v227, v227\n"
    "s_waitcnt lgkmcnt(0)\n"
    "v_fma_f32 v204, v204, v92, v96\n"
    "v_fma_f32 v205, v205, v93, v97\n"
    "v_fma_f32 v206, v206, v94, v98\n"
    "v_fma_f32 v207, v207, v95, v99\n"
    "v_cvt_pk_bf16_f32 v204, v204, v205\n"
    "v_cvt_pk_bf16_f32 v205, v206, v207\n"
    "v_cvt_f32_i32 v228, v228\n"
    "v_cvt_f32_i32 v229, v229\n"
    "v_cvt_f32_i32 v230, v230\n"
    "v_cvt_f32_i32 v231, v231\n"
    "buffer_store_dwordx2 v[204:205], v244, s[16:19], s76 offen offset:0 sc1\n"
    "v_fma_f32 v208, v208, v92, v96\n"
    "v_fma_f32 v209, v209, v93, v97\n"
    "v_fma_f32 v210, v210, v94, v98\n"
    "v_fma_f32 v211, v211, v95, v99\n"
    "v_cvt_pk_bf16_f32 v208, v208, v209\n"
    "v_cvt_pk_bf16_f32 v209, v210, v211\n"
    "v_cvt_f32_i32 v232, v232\n"
    "v_cvt_f32_i32 v233, v233\n"
    "v_cvt_f32_i32 v234, v234\n"
    "v_cvt_f32_i32 v235, v235\n"
    "buffer_store_dwordx2 v[208:209], v244, s[16:19], s76 offen offset:1024 sc1\n"
    "v_fma_f32 v212, v212, v92, v96\n"
    "v_fma_f32 v213, v213, v93, v97\n"
    "v_fma_f32 v214, v214, v94, v98\n"
    "v_fma_f32 v215, v215, v95, v99\n"
    "v_cvt_pk_bf16_f32 v212, v212, v213\n"
    "v_cvt_pk_bf16_f32 v213, v214, v215\n"
    "buffer_store_dwordx2 v[212:213], v244, s[16:19], s76 offen offset:256 sc1\n"
    "v_fma_f32 v216, v216, v92, v96\n"
    "v_fma_f32 v217, v217, v93, v97\n"
    "v_fma_f32 v218, v218, v94, v98\n"
    "v_fma_f32 v219, v219, v95, v99\n"
    "v_cvt_pk_bf16_f32 v216, v216, v217\n"
    "v_cvt_pk_bf16_f32 v217, v218, v219\n"
    "buffer_store_dwordx2 v[216:217], v244, s[16:19], s76 offen offset:1280 sc1\n"
    "v_fma_f32 v220, v220, v92, v96\n"
    "v_fma_f32 v221, v221, v93, v97\n"
    "v_fma_f32 v222, v222, v94, v98\n"
    "v_fma_f32 v223, v223, v95, v99\n"
    "v_cvt_pk_bf16_f32 v220, v220, v221\n"
    "v_cvt_pk_bf16_f32 v221, v222, v223\n"
    "buffer_store_dwordx2 v[220:221], v244, s[16:19], s77 offen offset:0 sc1\n"
    "v_fma_f32 v224, v224, v92, v96\n"
    "v_fma_f32 v225, v225, v93, v97\n"
    "v_fma_f32 v226, v226, v94, v98\n"
    "v_fma_f32 v227, v227, v95, v99\n"
    "v_cvt_pk_bf16_f32 v224, v224, v225\n"
    "v_cvt_pk_bf16_f32 v225, v226, v227\n"
    "buffer_store_dwordx2 v[224:225], v244, s[16:19], s77 offen offset:1024 sc1\n"
    "v_fma_f32 v228, v228, v92, v96\n"
    "v_fma_f32 v229, v229, v93, v97\n"
    "v_fma_f32 v230, v230, v94, v98\n"
    "v_fma_f32 v231, v231, v95, v99\n"
    "v_cvt_pk_bf16_f32 v228, v228, v229\n"
    "v_cvt_pk_bf16_f32 v229, v230, v231\n"
    "buffer_store_dwordx2 v[228:229], v244, s[16:19], s77 offen offset:256 sc1\n"
    "v_fma_f32 v232, v232, v92, v96\n"
    "v_fma_f32 v233, v233, v93, v97\n"
    "v_fma_f32 v234, v234, v94, v98\n"
    "v_fma_f32 v235, v235, v95, v99\n"
    "v_cvt_pk_bf16_f32 v232, v232, v233\n"
    "v_cvt_pk_bf16_f32 v233, v234, v235\n"
    "buffer_store_dwordx2 v[232:233], v244, s[16:19], s77 offen offset:1280 sc1\n"
    "v_add_u32 v243, 512, v243\n"
    "s_branch .Lf4e\n"
    ".Lf4b:\n"
    "s_add_u32 s76, s71, s60\n"
    "s_add_u32 s77, s71, s61\n"
    "s_add_u32 s78, s71, s62\n"
    "v_cvt_f32_i32 v204, v204\n"
    "v_cvt_f32_i32 v208, v208\n"
    "v_cvt_f32_i32 v205, v205\n"
    "v_cvt_f32_i32 v209, v209\n"
    "v_cvt_f32_i32 v206, v206\n"
    "v_cvt_f32_i32 v210, v210\n"
    "v_cvt_pk_bf16_f32 v207, v204, v208\n"
    "v_cvt_pk_bf16_f32 v211, v205, v209\n"
    "v_cvt_pk_bf16_f32 v215, v206, v210\n"
    "v_cvt_f32_i32 v212, v212\n"
    "v_cvt_f32_i32 v216, v216\n"
    "v_cvt_f32_i32 v213, v213\n"
    "v_cvt_f32_i32 v217, v217\n"
    "v_cvt_f32_i32 v214, v214\n"
    "v_cvt_f32_i32 v218, v218\n"
    "v_cvt_pk_bf16_f32 v206, v212, v216\n"
    "v_cvt_pk_bf16_f32 v210, v213, v217\n"
    "v_cvt_pk_bf16_f32 v214, v214, v218\n"
    "v_cvt_f32_i32 v220, v220\n"
    "v_cvt_f32_i32 v224, v224\n"
    "v_cvt_f32_i32 v221, v221\n"
    "v_cvt_f32_i32 v225, v225\n"
    "v_cvt_f32_i32 v222, v222\n"
    "v_cvt_f32_i32 v226, v226\n"
    "v_cvt_pk_bf16_f32 v205, v220, v224\n"
    "v_cvt_pk_bf16_f32 v209, v221, v225\n"
    "v_cvt_pk_bf16_f32 v213, v222, v226\n"
    "v_cvt_f32_i32 v228, v228\n"
    "v_cvt_f32_i32 v232, v232\n"
    "v_cvt_f32_i32 v229, v229\n"
    "v_cvt_f32_i32 v233, v233\n"
    "v_cvt_f32_i32 v230, v230\n"
    "v_cvt_f32_i32 v234, v234\n"
    "v_cvt_pk_bf16_f32 v204, v228, v232\n"
    "v_cvt_pk_bf16_f32 v208, v229, v233\n"
    "v_cvt_pk_bf16_f32 v212, v230, v234\n"
    "buffer_store_dwordx4 v[204:207], v244, s[16:19], s76 offen offset:0 sc1\n"
    "buffer_store_dwordx4 v[208:211], v244, s[16:19], s77 offen offset:0 sc1\n"
    "buffer_store_dwordx4 v[212:215], v244, s[16:19], s78 offen offset:0 sc1\n"
    ".Lf4e:\n"
    "s_nop 4\n"
    "s_add_u32 s71, s71, 98304\n"
    "buffer_load_dwordx4 v[136:139], v130, s[92:95], 0 offen offset:0\n"
    "buffer_load_dwordx4 v[140:143], v130, s[96:99], 0 offen offset:0\n"
    "s_bitcmp1_b32 s83, 0\n"
    "s_cbranch_scc1 .Lf5b\n"
    "s_add_u32 s75, s71, s63\n"
    "s_add_u32 s76, s75, 6144\n"
    "s_add_u32 s77, s75, 24576\n"
    "s_add_u32 s78, s75, 30720\n"
    "buffer_load_dwordx4 v[36:39], v242, s[28:31], s75 offen offset:0 nt\n"
    "buffer_load_dwordx4 v[40:43], v242, s[28:31], s75 offen offset:3072 nt\n"
    "buffer_load_dwordx4 v[44:47], v242, s[28:31], s76 offen offset:0 nt\n"
    "buffer_load_dwordx4 v[48:51], v242, s[28:31], s76 offen offset:3072 nt\n"
    "buffer_load_dwordx4 v[52:55], v242, s[28:31], s77 offen offset:0 nt\n"
    "buffer_load_dwordx4 v[56:59], v242, s[28:31], s77 offen offset:3072 nt\n"
    "buffer_load_dwordx4 v[60:63], v242, s[28:31], s78 offen offset:0 nt\n"
    "buffer_load_dwordx4 v[64:67], v242, s[28:31], s78 offen offset:3072 nt\n"
    "s_branch .Lf5e\n"
    ".Lf5b:\n"
    "s_add_u32 s75, s71, s63\n"
    "buffer_load_dwordx3 v[36:38], v243, s[40:43], s75 offen offset:1536 nt\n"
    "buffer_load_dwordx3 v[40:42], v243, s[40:43], s75 offen offset:2304 nt\n"
    "buffer_load_dwordx3 v[44:46], v243, s[40:43], s75 offen offset:0 nt\n"
    "buffer_load_dwordx3 v[48:50], v243, s[40:43], s75 offen offset:768 nt\n"
    "buffer_load_dwordx3 v[52:54], v242, s[40:43], s75 offen offset:1536 nt\n"
    "buffer_load_dwordx3 v[56:58], v242, s[40:43], s75 offen offset:2304 nt\n"
    "buffer_load_dwordx3 v[60:62], v242, s[40:43], s75 offen offset:0 nt\n"
    "buffer_load_dwordx3 v[64:66], v242, s[40:43], s75 offen offset:768 nt\n"
    ".Lf5e:\n"
    "s_nop 4\n"
    "s_waitcnt vmcnt(20)\n"
    "v_lshlrev_b32 v180, 16, v124\n"
    "v_and_b32 v181, 0xffff0000, v124\n"
    "v_lshlrev_b32 v182, 16, v125\n"
    "v_and_b32 v183, 0xffff0000, v125\n"
    "v_lshlrev_b32 v184, 16, v126\n"
    "v_and_b32 v185, 0xffff0000, v126\n"
    "v_lshlrev_b32 v186, 16, v127\n"
    "v_and_b32 v187, 0xffff0000, v127\n"
    "v_mul_f32 v180, 0x3dd53b95, v180\n"
    "v_mul_f32 v181, 0x3dd53b95, v181\n"
    "v_mul_f32 v182, 0x3dd53b95, v182\n"
    "v_mul_f32 v183, 0x3dd53b95, v183\n"
    "v_mul_f32 v184, -v184, v180\n"
    "v_mul_f32 v185, -v185, v181\n"
    "v_mul_f32 v186, -v186, v182\n"
    "v_mul_f32 v187, -v187, v183\n"
    "ds_write_b128 v131, v[180:183] offset:0\n"
    "ds_write_b128 v131, v[184:187] offset:1024\n"
    "ds_read_b128 v[180:183], v132 offset:0\n"
    "ds_read_b128 v[184:187], v132 offset:1024\n"
    "ds_read_b128 v[188:191], v132 offset:128\n"
    "ds_read_b128 v[192:195], v132 offset:1152\n"
    "ds_read_b128 v[196:199], v132 offset:256\n"
    "ds_read_b128 v[200:203], v132 offset:1280\n"
    "v_cvt_f32_i32 v4, v4\n"
    "v_cvt_f32_i32 v5, v5\n"
    "v_cvt_f32_i32 v6, v6\n"
    "v_cvt_f32_i32 v7, v7\n"
    "v_cvt_f32_i32 v8, v8\n"
    "v_cvt_f32_i32 v9, v9\n"
    "v_cvt_f32_i32 v10, v10\n"
    "v_cvt_f32_i32 v11, v11\n"
    "v_cvt_f32_i32 v12, v12\n"
    "v_cvt_f32_i32 v13, v13\n"
    "v_cvt_f32_i32 v14, v14\n"
    "v_cvt_f32_i32 v15, v15\n"
    "v_cvt_f32_i32 v16, v16\n"
    "v_cvt_f32_i32 v17, v17\n"
    "v_cvt_f32_i32 v18, v18\n"
    "v_cvt_f32_i32 v19, v19\n"
    "v_cvt_f32_i32 v20, v20\n"
    "v_cvt_f32_i32 v21, v21\n"
    "v_cvt_f32_i32 v22, v22\n"
    "v_cvt_f32_i32 v23, v23\n"
    "v_cvt_f32_i32 v24, v24\n"
    "v_cvt_f32_i32 v25, v25\n"
    "v_cvt_f32_i32 v26, v26\n"
    "v_cvt_f32_i32 v27, v27\n"
    "s_waitcnt lgkmcnt(4)\n"
    "v_fma_f32 v4, v4, v180, v184\n"
    "v_fma_f32 v5, v5, v181, v185\n"
    "v_fma_f32 v6, v6, v182, v186\n"
    "v_fma_f32 v7, v7, v183, v187\n"
    "v_fma_f32 v8, v8, v180, v184\n"
    "v_fma_f32 v9, v9, v181, v185\n"
    "v_fma_f32 v10, v10, v182, v186\n"
    "v_fma_f32 v11, v11, v183, v187\n"
    "v_cvt_pk_bf16_f32 v4, v4, v5\n"
    "v_cvt_pk_bf16_f32 v5, v6, v7\n"
    "v_cvt_pk_bf16_f32 v6, v8, v9\n"
    "v_cvt_pk_bf16_f32 v7, v10, v11\n"
    "ds_write_b64 v163, v[4:5] offset:0\n"
    "ds_write_b64 v163, v[6:7] offset:1024\n"
    "s_waitcnt lgkmcnt(4)\n"
    "v_fma_f32 v12, v12, v188, v192\n"
    "v_fma_f32 v13, v13, v189, v193\n"
    "v_fma_f32 v14, v14, v190, v194\n"
    "v_fma_f32 v15, v15, v191, v195\n"
    "v_fma_f32 v16, v16, v188, v192\n"
    "v_fma_f32 v17, v17, v189, v193\n"
    "v_fma_f32 v18, v18, v190, v194\n"
    "v_fma_f32 v19, v19, v191, v195\n"
    "v_cvt_pk_bf16_f32 v12, v12, v13\n"
    "v_cvt_pk_bf16_f32 v13, v14, v15\n"
    "v_cvt_pk_bf16_f32 v14, v16, v17\n"
    "v_cvt_pk_bf16_f32 v15, v18, v19\n"
    "ds_write_b64 v163, v[12:13] offset:2048\n"
    "ds_write_b64 v163, v[14:15] offset:3072\n"
    "s_waitcnt lgkmcnt(4)\n"
    "v_fma_f32 v20, v20, v196, v200\n"
    "v_fma_f32 v21, v21, v197, v201\n"
    "v_fma_f32 v22, v22, v198, v202\n"
    "v_fma_f32 v23, v23, v199, v203\n"
    "v_fma_f32 v24, v24, v196, v200\n"
    "v_fma_f32 v25, v25, v197, v201\n"
    "v_fma_f32 v26, v26, v198, v202\n"
    "v_fma_f32 v27, v27, v199, v203\n"
    "v_cvt_pk_bf16_f32 v20, v20, v21\n"
    "v_cvt_pk_bf16_f32 v21, v22, v23\n"
    "v_cvt_pk_bf16_f32 v22, v24, v25\n"
    "v_cvt_pk_bf16_f32 v23, v26, v27\n"
    "ds_write_b64 v163, v[20:21] offset:4096\n"
    "ds_write_b64 v163, v[22:23] offset:5120\n"
    "v_accvgpr_write_b32 a60, 0\n"
    "v_accvgpr_write_b32 a61, 0\n"
    "v_accvgpr_write_b32 a62, 0\n"
    "v_accvgpr_write_b32 a63, 0\n"
    "v_accvgpr_write_b32 a64, 0\n"
    "v_accvgpr_write_b32 a65, 0\n"
    "v_accvgpr_write_b32 a66, 0\n"
    "v_accvgpr_write_b32 a67, 0\n"
    "v_accvgpr_write_b32 a68, 0\n"
    "v_accvgpr_write_b32 a69, 0\n"
    "v_accvgpr_write_b32 a70, 0\n"
    "v_accvgpr_write_b32 a71, 0\n"
    "v_accvgpr_write_b32 a72, 0\n"
    "v_accvgpr_write_b32 a73, 0\n"
    "v_accvgpr_write_b32 a74, 0\n"
    "v_accvgpr_write_b32 a75, 0\n"
    "v_accvgpr_write_b32 a76, 0\n"
    "v_accvgpr_write_b32 a77, 0\n"
    "v_accvgpr_write_b32 a78, 0\n"
    "v_accvgpr_write_b32 a79, 0\n"
    "v_accvgpr_write_b32 a80, 0\n"
    "v_accvgpr_write_b32 a81, 0\n"
    "v_accvgpr_write_b32 a82, 0\n"
    "v_accvgpr_write_b32 a83, 0\n"
    "v_accvgpr_write_b32 a84, 0\n"
    "v_accvgpr_write_b32 a85, 0\n"
    "v_accvgpr_write_b32 a86, 0\n"
    "v_accvgpr_write_b32 a87, 0\n"
    "v_accvgpr_write_b32 a88, 0\n"
    "v_accvgpr_write_b32 a89, 0\n"
    "v_accvgpr_write_b32 a90, 0\n"
    "v_accvgpr_write_b32 a91, 0\n"
    "v_accvgpr_write_b32 a92, 0\n"
    "v_accvgpr_write_b32 a93, 0\n"
    "v_accvgpr_write_b32 a94, 0\n"
    "v_accvgpr_write_b32 a95, 0\n"
    "v_accvgpr_write_b32 a96, 0\n"
    "v_accvgpr_write_b32 a97, 0\n"
    "v_accvgpr_write_b32 a98, 0\n"
    "v_accvgpr_write_b32 a99, 0\n"
    "v_accvgpr_write_b32 a100, 0\n"
    "v_accvgpr_write_b32 a101, 0\n"
    "v_accvgpr_write_b32 a102, 0\n"
    "v_accvgpr_write_b32 a103, 0\n"
    "v_accvgpr_write_b32 a104, 0\n"
    "v_accvgpr_write_b32 a105, 0\n"
    "v_accvgpr_write_b32 a106, 0\n"
    "v_accvgpr_write_b32 a107, 0\n"
    "v_accvgpr_write_b32 a108, 0\n"
    "v_accvgpr_write_b32 a109, 0\n"
    "v_accvgpr_write_b32 a110, 0\n"
    "v_accvgpr_write_b32 a111, 0\n"
    "v_accvgpr_write_b32 a112, 0\n"
    "v_accvgpr_write_b32 a113, 0\n"
    "v_accvgpr_write_b32 a114, 0\n"
    "v_accvgpr_write_b32 a115, 0\n"
    "v_accvgpr_write_b32 a116, 0\n"
    "v_accvgpr_write_b32 a117, 0\n"
    "v_accvgpr_write_b32 a118, 0\n"
    "v_accvgpr_write_b32 a119, 0\n"
    "v_accvgpr_write_b32 a120, 0\n"
    "v_accvgpr_write_b32 a121, 0\n"
    "v_accvgpr_write_b32 a122, 0\n"
    "v_accvgpr_write_b32 a123, 0\n"
    "v_accvgpr_write_b32 a124, 0\n"
    "v_accvgpr_write_b32 a125, 0\n"
    "v_accvgpr_write_b32 a126, 0\n"
    "v_accvgpr_write_b32 a127, 0\n"
    "v_accvgpr_write_b32 a128, 0\n"
    "v_accvgpr_write_b32 a129, 0\n"
    "v_accvgpr_write_b32 a130, 0\n"
    "v_accvgpr_write_b32 a131, 0\n"
    "v_accvgpr_write_b32 a132, 0\n"
    "v_accvgpr_write_b32 a133, 0\n"
    "v_accvgpr_write_b32 a134, 0\n"
    "v_accvgpr_write_b32 a135, 0\n"
    "v_accvgpr_write_b32 a136, 0\n"
    "v_accvgpr_write_b32 a137, 0\n"
    "v_accvgpr_write_b32 a138, 0\n"
    "v_accvgpr_write_b32 a139, 0\n"
    "v_accvgpr_write_b32 a140, 0\n"
    "v_accvgpr_write_b32 a141, 0\n"
    "v_accvgpr_write_b32 a142, 0\n"
    "v_accvgpr_write_b32 a143, 0\n"
    "v_accvgpr_write_b32 a144, 0\n"
    "v_accvgpr_write_b32 a145, 0\n"
    "v_accvgpr_write_b32 a146, 0\n"
    "v_accvgpr_write_b32 a147, 0\n"
    "v_accvgpr_write_b32 a148, 0\n"
    "v_accvgpr_write_b32 a149, 0\n"
    "v_accvgpr_write_b32 a150, 0\n"
    "v_accvgpr_write_b32 a151, 0\n"
    "v_accvgpr_write_b32 a152, 0\n"
    "v_accvgpr_write_b32 a153, 0\n"
    "v_accvgpr_write_b32 a154, 0\n"
    "v_accvgpr_write_b32 a155, 0\n"
    "v_accvgpr_write_b32 a156, 0\n"
    "v_accvgpr_write_b32 a157, 0\n"
    "v_accvgpr_write_b32 a158, 0\n"
    "v_accvgpr_write_b32 a159, 0\n"
    "s_waitcnt vmcnt(19)\n"
    "v_add_co_u32 v238, vcc, 0x7f4a7c15, v238\n"
    "v_mov_b32 v159, 0x9e3779b9\n"
    "s_nop 0\n"
    "v_addc_co_u32 v239, vcc, v159, v239, vcc\n"
    "v_mov_b32 v246, 1\n"
    "s_add_u32 m0, s65, 73728\n"
    "s_add_u32 s72, s85, 6144\n"
    "buffer_load_dwordx4 v88, s[20:23], s72 offen offset:0 nt lds\n"
    "s_add_u32 m0, s65, 74752\n"
    "s_add_u32 s72, s85, 7168\n"
    "buffer_load_dwordx4 v89, s[20:23], s72 offen offset:0 nt lds\n"
    "s_waitcnt vmcnt(15)\n"
    "ds_read_b128 a[192:195], v154 offset:0\n"
    "ds_read_b128 a[196:199], v155 offset:0\n"
    "ds_read_b128 a[200:203], v154 offset:128\n"
    "ds_read_b128 a[204:207], v155 offset:128\n"
    "ds_read_b128 a[208:211], v154 offset:256\n"
    "ds_read_b128 a[212:215], v155 offset:256\n"
    "s_waitcnt lgkmcnt(0)\n"
    "s_waitcnt vmcnt(12)\n"
    "ds_add_rtn_u32 v245, v247, v246\n"
    "s_lshl_b32 s73, s53, 6\n"
    "s_add_u32 s73, s73, 128\n"
    "s_waitcnt lgkmcnt(0)\n"
    "v_readfirstlane_b32 s74, v245\n"
    "s_nop 0\n"
    "s_cmp_eq_u32 s74, 3\n"
    "s_cselect_b32 s73, s73, 0x7fff0000\n"
    "buffer_store_dwordx2 v[238:239], v91, s[36:39], s73 offen offset:0 sc1\n"
    "s_barrier\n"
    "ds_read_b128 v[156:159], v2 offset:0\n"
    "ds_read_b128 v[160:163], v2 offset:2048\n"
    "ds_read_b128 v[164:167], v2 offset:4096\n"
    "ds_read_b128 v[168:171], v2 offset:6144\n"
    "ds_read_b128 v[172:175], v2 offset:8192\n"
    "ds_read_b128 v[176:179], v2 offset:10240\n"
    "ds_read_b128 v[180:183], v2 offset:1024\n"
    "ds_read_b128 v[184:187], v2 offset:3072\n"
    "ds_read_b128 v[188:191], v2 offset:5120\n"
    "ds_read_b128 v[192:195], v2 offset:7168\n"
    "ds_read_b128 v[196:199], v2 offset:9216\n"
    "ds_read_b128 v[200:203], v2 offset:11264\n"
    "s_waitcnt lgkmcnt(11)\n"
    "v_mfma_f32_16x16x32_bf16 v[4:7], v[156:159], a[192:195], 0\n"
    "s_waitcnt lgkmcnt(10)\n"
    "v_mfma_f32_16x16x32_bf16 v[4:7], v[160:163], a[196:199], v[4:7]\n"
    "s_waitcnt lgkmcnt(9)\n"
    "v_mfma_f32_16x16x32_bf16 v[4:7], v[164:167], a[200:203], v[4:7]\n"
    "s_waitcnt lgkmcnt(8)\n"
    "v_mfma_f32_16x16x32_bf16 v[4:7], v[168:171], a[204:207], v[4:7]\n"
    "s_waitcnt lgkmcnt(7)\n"
    "v_mfma_f32_16x16x32_bf16 v[4:7], v[172:175], a[208:211], v[4:7]\n"
    "s_waitcnt lgkmcnt(6)\n"
    "v_mfma_f32_16x16x32_bf16 v[4:7], v[176:179], a[212:215], v[4:7]\n"
    "s_waitcnt lgkmcnt(5)\n"
    "v_mfma_f32_16x16x32_bf16 v[20:23], v[180:183], a[192:195], 0\n"
    "s_waitcnt lgkmcnt(4)\n"
    "v_mfma_f32_16x16x32_bf16 v[20:23], v[184:187], a[196:199], v[20:23]\n"
    "s_waitcnt lgkmcnt(3)\n"
    "v_mfma_f32_16x16x32_bf16 v[20:23], v[188:191], a[200:203], v[20:23]\n"
    "s_waitcnt lgkmcnt(2)\n"
    "v_mfma_f32_16x16x32_bf16 v[20:23], v[192:195], a[204:207], v[20:23]\n"
    "s_waitcnt lgkmcnt(1)\n"
    "v_mfma_f32_16x16x32_bf16 v[20:23], v[196:199], a[208:211], v[20:23]\n"
    "s_waitcnt lgkmcnt(0)\n"
    "v_mfma_f32_16x16x32_bf16 v[20:23], v[200:203], a[212:215], v[20:23]\n"
    "v_mov_b32 v77, 155904\n"
    "v_max3_f32 v76, v4, v5, v6\n"
    "s_nop 5\n"
    "v_max3_f32 v76, v76, v7, v20\n"
    "v_max3_f32 v76, v76, v21, v22\n"
    "v_max_f32 v76, v76, v23\n"
    "v_mov_b32 v78, v76\n"
    "s_nop 1\n"
    "v_permlane32_swap_b32 v76, v78\n"
    "v_max_f32 v76, v76, v78\n"
    "v_mov_b32 v78, v76\n"
    "s_nop 1\n"
    "v_permlane16_swap_b32 v76, v78\n"
    "v_max_f32 v76, v76, v78\n"
    "ds_max_f32 v77, v76\n"
    "s_add_u32 m0, s65, 75776\n"
    "s_add_u32 s72, s85, 8192\n"
    "buffer_load_dwordx4 v90, s[20:23], s72 offen offset:0 nt lds\n"
    "s_waitcnt vmcnt(12)\n"
    "v_lshlrev_b32 v204, 16, v136\n"
    "v_and_b32 v205, 0xffff0000, v136\n"
    "v_lshlrev_b32 v206, 16, v137\n"
    "v_and_b32 v207, 0xffff0000, v137\n"
    "v_lshlrev_b32 v208, 16, v138\n"
    "v_and_b32 v209, 0xffff0000, v138\n"
    "v_lshlrev_b32 v210, 16, v139\n"
    "v_and_b32 v211, 0xffff0000, v139\n"
    "v_log_f32 v68, v204\n"
    "v_log_f32 v69, v205\n"
    "v_log_f32 v70, v206\n"
    "v_log_f32 v71, v207\n"
    "v_log_f32 v72, v208\n"
    "v_log_f32 v73, v209\n"
    "v_log_f32 v74, v210\n"
    "v_log_f32 v75, v211\n"
    "v_rcp_f32 v204, v204\n"
    "s_add_u32 m0, s65, 76800\n"
    "s_add_u32 s72, s85, 9216\n"
    "buffer_load_dwordx4 v88, s[20:23], s72 offen offset:0 nt lds\n"
    "v_rcp_f32 v205, v205\n"
    "v_rcp_f32 v206, v206\n"
    "v_rcp_f32 v207, v207\n"
    "v_rcp_f32 v208, v208\n"
    "v_rcp_f32 v209, v209\n"
    "v_rcp_f32 v210, v210\n"
    "v_rcp_f32 v211, v211\n"
    "s_mov_b32 s73, 0x41a00000\n"
    "v_mov_b32 v228, 0x7fc00000\n"
    "v_cmp_le_f32 vcc, |v68|, s73\n"
    "s_nop 1\n"
    "v_cndmask_b32 v68, v228, v68, vcc\n"
    "v_cmp_le_f32 vcc, |v69|, s73\n"
    "s_nop 1\n"
    "v_cndmask_b32 v69, v228, v69, vcc\n"
    "v_cmp_le_f32 vcc, |v70|, s73\n"
    "s_nop 1\n"
    "v_cndmask_b32 v70, v228, v70, vcc\n"
    "v_cmp_le_f32 vcc, |v71|, s73\n"
    "s_nop 1\n"
    "v_cndmask_b32 v71, v228, v71, vcc\n"
    "v_cmp_le_f32 vcc, |v72|, s73\n"
    "s_nop 1\n"
    "v_cndmask_b32 v72, v228, v72, vcc\n"
    "s_add_u32 m0, s65, 77824\n"
    "s_add_u32 s72, s85, 10240\n"
    "buffer_load_dwordx4 v89, s[20:23], s72 offen offset:0 nt lds\n"
    "v_cmp_le_f32 vcc, |v73|, s73\n"
    "s_nop 1\n"
    "v_cndmask_b32 v73, v228, v73, vcc\n"
    "v_cmp_le_f32 vcc, |v74|, s73\n"
    "s_nop 1\n"
    "v_cndmask_b32 v74, v228, v74, vcc\n"
    "v_cmp_le_f32 vcc, |v75|, s73\n"
    "s_nop 1\n"
    "v_cndmask_b32 v75, v228, v75, vcc\n"
    "v_cvt_pk_bf16_f32 v220, v204, v205\n"
    "v_cvt_pk_bf16_f32 v221, v206, v207\n"
    "v_cvt_pk_bf16_f32 v222, v208, v209\n"
    "v_cvt_pk_bf16_f32 v223, v210, v211\n"
    "v_lshlrev_b32 v212, 16, v220\n"
    "v_and_b32 v213, 0xffff0000, v220\n"
    "v_lshlrev_b32 v214, 16, v221\n"
    "v_and_b32 v215, 0xffff0000, v221\n"
    "v_lshlrev_b32 v216, 16, v222\n"
    "v_and_b32 v217, 0xffff0000, v222\n"
    "v_lshlrev_b32 v218, 16, v223\n"
    "v_and_b32 v219, 0xffff0000, v223\n"
    "v_sub_f32 v204, v204, v212\n"
    "s_add_u32 m0, s65, 78848\n"
    "s_add_u32 s72, s85, 11264\n"
    "buffer_load_dwordx4 v90, s[20:23], s72 offen offset:0 nt lds\n"
    "v_sub_f32 v205, v205, v213\n"
    "v_sub_f32 v206, v206, v214\n"
    "v_sub_f32 v207, v207, v215\n"
    "v_sub_f32 v208, v208, v216\n"
    "v_sub_f32 v209, v209, v217\n"
    "v_sub_f32 v210, v210, v218\n"
    "v_sub_f32 v211, v211, v219\n"
    "v_cvt_pk_bf16_f32 v224, v204, v205\n"
    "v_cvt_pk_bf16_f32 v225, v206, v207\n"
    "v_cvt_pk_bf16_f32 v226, v208, v209\n"
    "v_cvt_pk_bf16_f32 v227, v210, v211\n"
    "v_mov_b32 v204, 0\n"
    "v_mov_b32 v205, 0\n"
    "v_mov_b32 v206, 0\n"
    "v_mov_b32 v207, 0\n"
    "ds_write_b128 v144, v[220:223] offset:256\n"
    "ds_write_b128 v144, v[224:227] offset:384\n"
    "ds_write_b128 v144, v[140:143] offset:512\n"
    "ds_write_b128 v144, v[204:207] offset:640\n"
    "s_waitcnt lgkmcnt(0)\n"
    "s_barrier\n"
    "ds_read_b32 v76, v77\n"
    "s_waitcnt lgkmcnt(0)\n"
    "v_sub_f32 v68, v68, v76\n"
    "v_sub_f32 v69, v69, v76\n"
    "v_sub_f32 v70, v70, v76\n"
    "v_sub_f32 v71, v71, v76\n"
    "v_sub_f32 v72, v72, v76\n"
    "v_sub_f32 v73, v73, v76\n"
    "v_sub_f32 v74, v74, v76\n"
    "v_sub_f32 v75, v75, v76\n"
    "ds_write_b128 v86, v[68:71] offset:0\n"
    "ds_write_b128 v86, v[72:75] offset:16\n"
    "s_mul_i32 s73, s79, 768\n"
    "v_add_u32 v77, s73, v250\n"
    "s_waitcnt lgkmcnt(0)\n"
    "s_barrier\n"
    "ds_read_b128 v[100:103], v77 offset:0\n"
    "ds_read_b128 v[104:107], v77 offset:16\n"
    "s_waitcnt lgkmcnt(1)\n"
    "v_add_f32 v4, v4, v100\n"
    "v_add_f32 v5, v5, v101\n"
    "v_add_f32 v6, v6, v102\n"
    "v_add_f32 v7, v7, v103\n"
    "s_waitcnt lgkmcnt(0)\n"
    "v_add_f32 v20, v20, v104\n"
    "v_add_f32 v21, v21, v105\n"
    "v_add_f32 v22, v22, v106\n"
    "v_add_f32 v23, v23, v107\n"
    "v_exp_f32 v4, v4\n"
    "v_exp_f32 v5, v5\n"
    "v_exp_f32 v6, v6\n"
    "v_exp_f32 v7, v7\n"
    "v_exp_f32 v20, v20\n"
    "v_exp_f32 v21, v21\n"
    "v_exp_f32 v22, v22\n"
    "v_exp_f32 v23, v23\n"
    "s_waitcnt vmcnt(7)\n"
    "s_bitcmp1_b32 s83, 0\n"
    "s_cbranch_scc1 .Lf6b\n"
    "s_add_u32 m0, s65, 49152\n"
    "s_add_u32 s72, s85, 12288\n"
    "buffer_load_dwordx4 v88, s[20:23], s72 offen offset:0 nt lds\n"
    "s_add_u32 s76, s71, s60\n"
    "s_add_u32 s77, s71, s61\n"
    "v_cvt_f32_i32 v36, v36\n"
    "v_cvt_f32_i32 v37, v37\n"
    "v_cvt_f32_i32 v38, v38\n"
    "v_cvt_f32_i32 v39, v39\n"
    "v_cvt_f32_i32 v40, v40\n"
    "v_cvt_f32_i32 v41, v41\n"
    "v_cvt_f32_i32 v42, v42\n"
    "v_cvt_f32_i32 v43, v43\n"
    "v_cvt_f32_i32 v44, v44\n"
    "v_cvt_f32_i32 v45, v45\n"
    "v_cvt_f32_i32 v46, v46\n"
    "v_cvt_f32_i32 v47, v47\n"
    "ds_read_b128 v[92:95], v243 offset:0\n"
    "ds_read_b128 v[96:99], v243 offset:128\n"
    "v_cvt_f32_i32 v48, v48\n"
    "v_cvt_f32_i32 v49, v49\n"
    "v_cvt_f32_i32 v50, v50\n"
    "v_cvt_f32_i32 v51, v51\n"
    "v_cvt_f32_i32 v52, v52\n"
    "v_cvt_f32_i32 v53, v53\n"
    "v_cvt_f32_i32 v54, v54\n"
    "v_cvt_f32_i32 v55, v55\n"
    "v_cvt_f32_i32 v56, v56\n"
    "v_cvt_f32_i32 v57, v57\n"
    "v_cvt_f32_i32 v58, v58\n"
    "v_cvt_f32_i32 v59, v59\n"
    "s_waitcnt lgkmcnt(0)\n"
    "v_fma_f32 v36, v36, v92, v96\n"
    "v_fma_f32 v37, v37, v93, v97\n"
    "v_fma_f32 v38, v38, v94, v98\n"
    "v_fma_f32 v39, v39, v95, v99\n"
    "v_cvt_pk_bf16_f32 v36, v36, v37\n"
    "v_cvt_pk_bf16_f32 v37, v38, v39\n"
    "v_cvt_f32_i32 v60, v60\n"
    "v_cvt_f32_i32 v61, v61\n"
    "v_cvt_f32_i32 v62, v62\n"
    "v_cvt_f32_i32 v63, v63\n"
    "buffer_store_dwordx2 v[36:37], v244, s[16:19], s76 offen offset:0 sc1\n"
    "v_fma_f32 v40, v40, v92, v96\n"
    "v_fma_f32 v41, v41, v93, v97\n"
    "v_fma_f32 v42, v42, v94, v98\n"
    "v_fma_f32 v43, v43, v95, v99\n"
    "v_cvt_pk_bf16_f32 v40, v40, v41\n"
    "v_cvt_pk_bf16_f32 v41, v42, v43\n"
    "s_add_u32 m0, s65, 50176\n"
    "s_add_u32 s72, s85, 13312\n"
    "buffer_load_dwordx4 v89, s[20:23], s72 offen offset:0 nt lds\n"
    "v_cvt_f32_i32 v64, v64\n"
    "v_cvt_f32_i32 v65, v65\n"
    "v_cvt_f32_i32 v66, v66\n"
    "v_cvt_f32_i32 v67, v67\n"
    "buffer_store_dwordx2 v[40:41], v244, s[16:19], s76 offen offset:1024 sc1\n"
    "v_fma_f32 v44, v44, v92, v96\n"
    "v_fma_f32 v45, v45, v93, v97\n"
    "v_fma_f32 v46, v46, v94, v98\n"
    "v_fma_f32 v47, v47, v95, v99\n"
    "v_cvt_pk_bf16_f32 v44, v44, v45\n"
    "v_cvt_pk_bf16_f32 v45, v46, v47\n"
    "buffer_store_dwordx2 v[44:45], v244, s[16:19], s76 offen offset:256 sc1\n"
    "v_fma_f32 v48, v48, v92, v96\n"
    "v_fma_f32 v49, v49, v93, v97\n"
    "v_fma_f32 v50, v50, v94, v98\n"
    "v_fma_f32 v51, v51, v95, v99\n"
    "v_cvt_pk_bf16_f32 v48, v48, v49\n"
    "v_cvt_pk_bf16_f32 v49, v50, v51\n"
    "buffer_store_dwordx2 v[48:49], v244, s[16:19], s76 offen offset:1280 sc1\n"
    "v_fma_f32 v52, v52, v92, v96\n"
    "v_fma_f32 v53, v53, v93, v97\n"
    "v_fma_f32 v54, v54, v94, v98\n"
    "v_fma_f32 v55, v55, v95, v99\n"
    "v_cvt_pk_bf16_f32 v52, v52, v53\n"
    "v_cvt_pk_bf16_f32 v53, v54, v55\n"
    "buffer_store_dwordx2 v[52:53], v244, s[16:19], s77 offen offset:0 sc1\n"
    "v_fma_f32 v56, v56, v92, v96\n"
    "v_fma_f32 v57, v57, v93, v97\n"
    "v_fma_f32 v58, v58, v94, v98\n"
    "v_fma_f32 v59, v59, v95, v99\n"
    "v_cvt_pk_bf16_f32 v56, v56, v57\n"
    "v_cvt_pk_bf16_f32 v57, v58, v59\n"
    "buffer_store_dwordx2 v[56:57], v244, s[16:19], s77 offen offset:1024 sc1\n"
    "v_fma_f32 v60, v60, v92, v96\n"
    "v_fma_f32 v61, v61, v93, v97\n"
    "v_fma_f32 v62, v62, v94, v98\n"
    "v_fma_f32 v63, v63, v95, v99\n"
    "v_cvt_pk_bf16_f32 v60, v60, v61\n"
    "v_cvt_pk_bf16_f32 v61, v62, v63\n"
    "buffer_store_dwordx2 v[60:61], v244, s[16:19], s77 offen offset:256 sc1\n"
    "v_fma_f32 v64, v64, v92, v96\n"
    "v_fma_f32 v65, v65, v93, v97\n"
    "v_fma_f32 v66, v66, v94, v98\n"
    "v_fma_f32 v67, v67, v95, v99\n"
    "v_cvt_pk_bf16_f32 v64, v64, v65\n"
    "v_cvt_pk_bf16_f32 v65, v66, v67\n"
    "buffer_store_dwordx2 v[64:65], v244, s[16:19], s77 offen offset:1280 sc1\n"
    "v_add_u32 v243, 512, v243\n"
    "s_branch .Lf6e\n"
    ".Lf6b:\n"
    "s_add_u32 m0, s65, 49152\n"
    "s_add_u32 s72, s85, 12288\n"
    "buffer_load_dwordx4 v88, s[20:23], s72 offen offset:0 nt lds\n"
    "s_add_u32 s76, s71, s60\n"
    "s_add_u32 s77, s71, s61\n"
    "s_add_u32 s78, s71, s62\n"
    "v_cvt_f32_i32 v36, v36\n"
    "v_cvt_f32_i32 v40, v40\n"
    "v_cvt_f32_i32 v37, v37\n"
    "v_cvt_f32_i32 v41, v41\n"
    "v_cvt_f32_i32 v38, v38\n"
    "v_cvt_f32_i32 v42, v42\n"
    "v_cvt_pk_bf16_f32 v39, v36, v40\n"
    "v_cvt_pk_bf16_f32 v43, v37, v41\n"
    "v_cvt_pk_bf16_f32 v47, v38, v42\n"
    "v_cvt_f32_i32 v44, v44\n"
    "v_cvt_f32_i32 v48, v48\n"
    "v_cvt_f32_i32 v45, v45\n"
    "v_cvt_f32_i32 v49, v49\n"
    "v_cvt_f32_i32 v46, v46\n"
    "v_cvt_f32_i32 v50, v50\n"
    "v_cvt_pk_bf16_f32 v38, v44, v48\n"
    "s_add_u32 m0, s65, 50176\n"
    "s_add_u32 s72, s85, 13312\n"
    "buffer_load_dwordx4 v89, s[20:23], s72 offen offset:0 nt lds\n"
    "v_cvt_pk_bf16_f32 v42, v45, v49\n"
    "v_cvt_pk_bf16_f32 v46, v46, v50\n"
    "v_cvt_f32_i32 v52, v52\n"
    "v_cvt_f32_i32 v56, v56\n"
    "v_cvt_f32_i32 v53, v53\n"
    "v_cvt_f32_i32 v57, v57\n"
    "v_cvt_f32_i32 v54, v54\n"
    "v_cvt_f32_i32 v58, v58\n"
    "v_cvt_pk_bf16_f32 v37, v52, v56\n"
    "v_cvt_pk_bf16_f32 v41, v53, v57\n"
    "v_cvt_pk_bf16_f32 v45, v54, v58\n"
    "v_cvt_f32_i32 v60, v60\n"
    "v_cvt_f32_i32 v64, v64\n"
    "v_cvt_f32_i32 v61, v61\n"
    "v_cvt_f32_i32 v65, v65\n"
    "v_cvt_f32_i32 v62, v62\n"
    "v_cvt_f32_i32 v66, v66\n"
    "v_cvt_pk_bf16_f32 v36, v60, v64\n"
    "v_cvt_pk_bf16_f32 v40, v61, v65\n"
    "v_cvt_pk_bf16_f32 v44, v62, v66\n"
    "buffer_store_dwordx4 v[36:39], v244, s[16:19], s76 offen offset:0 sc1\n"
    "buffer_store_dwordx4 v[40:43], v244, s[16:19], s77 offen offset:0 sc1\n"
    "buffer_store_dwordx4 v[44:47], v244, s[16:19], s78 offen offset:0 sc1\n"
    ".Lf6e:\n"
    "s_nop 4\n"
    "s_mov_b32 s45, 0x7fff0000\n"
    "s_add_u32 m0, s65, 51200\n"
    "s_add_u32 s72, s85, 14336\n"
    "buffer_load_dwordx4 v90, s[20:23], s72 offen offset:0 nt lds\n"
    "s_add_u32 m0, s65, 52224\n"
    "s_add_u32 s72, s85, 15360\n"
    "buffer_load_dwordx4 v88, s[20:23], s72 offen offset:0 nt lds\n"
    "v_accvgpr_write_b32 a160, 0\n"
    "v_accvgpr_write_b32 a161, 0\n"
    "v_accvgpr_write_b32 a162, 0\n"
    "v_accvgpr_write_b32 a163, 0\n"
    "v_accvgpr_write_b32 a164, 0\n"
    "v_accvgpr_write_b32 a165, 0\n"
    "v_accvgpr_write_b32 a166, 0\n"
    "v_accvgpr_write_b32 a167, 0\n"
    "v_accvgpr_write_b32 a168, 0\n"
    "v_accvgpr_write_b32 a169, 0\n"
    "v_accvgpr_write_b32 a170, 0\n"
    "v_accvgpr_write_b32 a171, 0\n"
    "v_accvgpr_write_b32 a172, 0\n"
    "v_accvgpr_write_b32 a173, 0\n"
    "v_accvgpr_write_b32 a174, 0\n"
    "v_accvgpr_write_b32 a175, 0\n"
    "v_accvgpr_write_b32 a176, 0\n"
    "v_accvgpr_write_b32 a177, 0\n"
    "v_accvgpr_write_b32 a178, 0\n"
    "v_accvgpr_write_b32 a179, 0\n"
    "v_accvgpr_write_b32 a180, 0\n"
    "v_accvgpr_write_b32 a181, 0\n"
    "v_accvgpr_write_b32 a182, 0\n"
    "v_accvgpr_write_b32 a183, 0\n"
    "v_accvgpr_write_b32 a184, 0\n"
    "v_accvgpr_write_b32 a185, 0\n"
    "v_accvgpr_write_b32 a186, 0\n"
    "v_accvgpr_write_b32 a187, 0\n"
    "v_accvgpr_write_b32 a188, 0\n"
    "v_accvgpr_write_b32 a189, 0\n"
    "v_accvgpr_write_b32 a190, 0\n"
    "v_accvgpr_write_b32 a191, 0\n"
    "v_mov_b32 v108, 0\n"
    "v_mov_b32 v109, 0\n"
    "v_mov_b32 v110, 0\n"
    "v_mov_b32 v111, 0\n"
    "v_mov_b32 v112, 0\n"
    "v_mov_b32 v113, 0\n"
    "v_mov_b32 v114, 0\n"
    "v_mov_b32 v115, 0\n"
    "v_mov_b32 v116, 0\n"
    "v_mov_b32 v117, 0\n"
    "v_mov_b32 v118, 0\n"
    "v_mov_b32 v119, 0\n"
    "v_mov_b32 v120, 0\n"
    "v_mov_b32 v121, 0\n"
    "v_mov_b32 v122, 0\n"
    "v_mov_b32 v123, 0\n"
    "buffer_load_dwordx2 v[236:237], v87, s[36:39], 0 offen offset:0 sc1\n"
    "s_add_u32 m0, s65, 53248\n"
    "s_add_u32 s72, s85, 16384\n"
    "buffer_load_dwordx4 v89, s[20:23], s72 offen offset:0 nt lds\n"
    "s_add_u32 m0, s65, 54272\n"
    "s_add_u32 s72, s85, 17408\n"
    "buffer_load_dwordx4 v90, s[20:23], s72 offen offset:0 nt lds\n"
    "s_waitcnt vmcnt(10)\n"
    "ds_read_b128 a[216:219], v84 offset:24576\n"
    "ds_read_b128 a[220:223], v85 offset:24576\n"
    "ds_read_b128 a[224:227], v84 offset:24704\n"
    "ds_read_b128 a[228:231], v85 offset:24704\n"
    "ds_read_b128 a[232:235], v84 offset:24832\n"
    "ds_read_b128 a[236:239], v85 offset:24832\n"
    "s_waitcnt lgkmcnt(0)\n"
    "v_mfma_f32_16x16x32_bf16 v[8:11], v[176:179], a[236:239], v[100:103]\n"
    "v_mfma_f32_16x16x32_bf16 v[8:11], v[172:175], a[232:235], v[8:11]\n"
    "v_mfma_f32_16x16x32_bf16 v[8:11], v[168:171], a[228:231], v[8:11]\n"
    "v_mfma_f32_16x16x32_bf16 v[8:11], v[164:167], a[224:227], v[8:11]\n"
    "v_mfma_f32_16x16x32_bf16 v[8:11], v[160:163], a[220:223], v[8:11]\n"
    "v_mfma_f32_16x16x32_bf16 v[8:11], v[156:159], a[216:219], v[8:11]\n"
    "v_mfma_f32_16x16x32_bf16 v[24:27], v[200:203], a[236:239], v[104:107]\n"
    "v_mfma_f32_16x16x32_bf16 v[24:27], v[196:199], a[232:235], v[24:27]\n"
    "v_mfma_f32_16x16x32_bf16 v[24:27], v[192:195], a[228:231], v[24:27]\n"
    "v_mfma_f32_16x16x32_bf16 v[24:27], v[188:191], a[224:227], v[24:27]\n"
    "v_mfma_f32_16x16x32_bf16 v[24:27], v[184:187], a[220:223], v[24:27]\n"
    "v_mfma_f32_16x16x32_bf16 v[24:27], v[180:183], a[216:219], v[24:27]\n"
    "s_add_u32 s71, s71, 98304\n"
    "s_bitcmp1_b32 s83, 1\n"
    "s_cbranch_scc1 .Lf8b\n"
    "s_bitcmp1_b32 s83, 0\n"
    "s_cbranch_scc1 .Lf7b\n"
    "s_add_u32 s75, s71, s63\n"
    "s_add_u32 s76, s75, 6144\n"
    "s_add_u32 s77, s75, 24576\n"
    "s_add_u32 s78, s75, 30720\n"
    "buffer_load_dwordx4 v[36:39], v242, s[28:31], s75 offen offset:0 nt\n"
    "buffer_load_dwordx4 v[40:43], v242, s[28:31], s75 offen offset:3072 nt\n"
    "buffer_load_dwordx4 v[44:47], v242, s[28:31], s76 offen offset:0 nt\n"
    "buffer_load_dwordx4 v[48:51], v242, s[28:31], s76 offen offset:3072 nt\n"
    "buffer_load_dwordx4 v[52:55], v242, s[28:31], s77 offen offset:0 nt\n"
    "buffer_load_dwordx4 v[56:59], v242, s[28:31], s77 offen offset:3072 nt\n"
    "buffer_load_dwordx4 v[60:63], v242, s[28:31], s78 offen offset:0 nt\n"
    "buffer_load_dwordx4 v[64:67], v242, s[28:31], s78 offen offset:3072 nt\n"
    "s_branch .Lf7e\n"
    ".Lf7b:\n"
    "s_add_u32 s75, s71, s63\n"
    "buffer_load_dwordx3 v[36:38], v243, s[40:43], s75 offen offset:1536 nt\n"
    "buffer_load_dwordx3 v[40:42], v243, s[40:43], s75 offen offset:2304 nt\n"
    "buffer_load_dwordx3 v[44:46], v243, s[40:43], s75 offen offset:0 nt\n"
    "buffer_load_dwordx3 v[48:50], v243, s[40:43], s75 offen offset:768 nt\n"
    "buffer_load_dwordx3 v[52:54], v242, s[40:43], s75 offen offset:1536 nt\n"
    "buffer_load_dwordx3 v[56:58], v242, s[40:43], s75 offen offset:2304 nt\n"
    "buffer_load_dwordx3 v[60:62], v242, s[40:43], s75 offen offset:0 nt\n"
    "buffer_load_dwordx3 v[64:66], v242, s[40:43], s75 offen offset:768 nt\n"
    ".Lf7e:\n"
    "s_nop 4\n"
    "s_branch .Lf8e\n"
    ".Lf8b:\n"
    "buffer_load_dword v36, v240, s[36:39], s45 offen offset:0\n"
    "buffer_load_dword v36, v240, s[36:39], s45 offen offset:0\n"
    "buffer_load_dword v36, v240, s[36:39], s45 offen offset:0\n"
    "buffer_load_dword v36, v240, s[36:39], s45 offen offset:0\n"
    "buffer_load_dword v36, v240, s[36:39], s45 offen offset:0\n"
    "buffer_load_dword v36, v240, s[36:39], s45 offen offset:0\n"
    "buffer_load_dword v36, v240, s[36:39], s45 offen offset:0\n"
    "buffer_load_dword v36, v240, s[36:39], s45 offen offset:0\n"
    ".Lf8e:\n"
    "s_nop 4\n"
    "v_exp_f32 v8, v8\n"
    "v_exp_f32 v9, v9\n"
    "v_exp_f32 v10, v10\n"
    "v_exp_f32 v11, v11\n"
    "v_exp_f32 v24, v24\n"
    "v_exp_f32 v25, v25\n"
    "v_exp_f32 v26, v26\n"
    "v_exp_f32 v27, v27\n"
    "s_waitcnt vmcnt(13)\n"
    "buffer_store_dwordx2 v[238:239], v240, s[36:39], s80 offen offset:0 sc1\n"
    "s_add_u32 s80, s80, 256\n"
    "s_add_u32 m0, s65, 98304\n"
    "s_add_u32 s72, s85, 18432\n"
    "buffer_load_dwordx4 v88, s[20:23], s72 offen offset:0 nt lds\n"
    "s_add_u32 m0, s65, 99328\n"
    "s_add_u32 s72, s85, 19456\n"
    "buffer_load_dwordx4 v89, s[20:23], s72 offen offset:0 nt lds\n"
    "s_waitcnt vmcnt(13)\n"
    "v_cmp_eq_u64 s[54:55], v[236:237], v[238:239]\n"
    "s_nop 0\n"
    "s_cmp_eq_u64 s[54:55], -1\n"
    "s_cbranch_scc0 .LpollA\n"
    ".LpollAr:\n"
    "s_add_u32 m0, s44, 12288\n"
    "s_add_u32 s72, s44, 12288\n"
    "buffer_load_dwordx4 v1, s[16:19], s72 offen offset:0 sc1 lds\n"
    "s_add_u32 m0, s44, 13312\n"
    "s_add_u32 s72, s44, 13312\n"
    "buffer_load_dwordx4 v1, s[16:19], s72 offen offset:0 sc1 lds\n"
    "s_add_u32 m0, s44, 14336\n"
    "s_add_u32 s72, s44, 14336\n"
    "buffer_load_dwordx4 v1, s[16:19], s72 offen offset:0 sc1 lds\n"
    "s_add_u32 m0, s65, 24576\n"
    "s_add_u32 s72, s65, 49152\n"
    "buffer_load_dwordx4 v1, s[16:19], s72 offen offset:0 sc1 lds\n"
    "s_add_u32 m0, s65, 25600\n"
    "s_add_u32 s72, s65, 50176\n"
    "buffer_load_dwordx4 v1, s[16:19], s72 offen offset:0 sc1 lds\n"
    "s_add_u32 m0, s65, 26624\n"
    "s_add_u32 s72, s65, 51200\n"
    "buffer_load_dwordx4 v1, s[16:19], s72 offen offset:0 sc1 lds\n"
    "s_add_u32 m0, s65, 27648\n"
    "s_add_u32 s72, s65, 52224\n"
    "buffer_load_dwordx4 v1, s[16:19], s72 offen offset:0 sc1 lds\n"
    "s_add_u32 m0, s65, 28672\n"
    "s_add_u32 s72, s65, 53248\n"
    "buffer_load_dwordx4 v1, s[16:19], s72 offen offset:0 sc1 lds\n"
    "s_add_u32 m0, s65, 29696\n"
    "s_add_u32 s72, s65, 54272\n"
    "buffer_load_dwordx4 v1, s[16:19], s72 offen offset:0 sc1 lds\n"
    "s_add_u32 m0, s65, 100352\n"
    "s_add_u32 s72, s85, 20480\n"
    "buffer_load_dwordx4 v90, s[20:23], s72 offen offset:0 nt lds\n"
    "s_add_u32 m0, s65, 101376\n"
    "s_add_u32 s72, s85, 21504\n"
    "buffer_load_dwordx4 v88, s[20:23], s72 offen offset:0 nt lds\n"
    "s_add_u32 m0, s65, 102400\n"
    "s_add_u32 s72, s85, 22528\n"
    "buffer_load_dwordx4 v89, s[20:23], s72 offen offset:0 nt lds\n"
    "s_add_u32 m0, s65, 103424\n"
    "s_add_u32 s72, s85, 23552\n"
    "buffer_load_dwordx4 v90, s[20:23], s72 offen offset:0 nt lds\n"
    "s_waitcnt vmcnt(24)\n"
    "ds_read_b128 a[240:243], v84 offset:0\n"
    "ds_read_b128 a[244:247], v85 offset:0\n"
    "ds_read_b128 a[248:251], v84 offset:128\n"
    "ds_read_b128 a[252:255], v85 offset:128\n"
    "ds_read_b128 v[124:127], v84 offset:256\n"
    "ds_read_b128 v[128:131], v85 offset:256\n"
    "s_waitcnt lgkmcnt(0)\n"
    "v_mfma_f32_16x16x32_bf16 v[12:15], v[156:159], a[240:243], v[100:103]\n"
    "v_mfma_f32_16x16x32_bf16 v[12:15], v[160:163], a[244:247], v[12:15]\n"
    "v_mfma_f32_16x16x32_bf16 v[12:15], v[164:167], a[248:251], v[12:15]\n"
    "v_mfma_f32_16x16x32_bf16 v[12:15], v[168:171], a[252:255], v[12:15]\n"
    "v_mfma_f32_16x16x32_bf16 v[12:15], v[172:175], v[124:127], v[12:15]\n"
    "v_mfma_f32_16x16x32_bf16 v[12:15], v[176:179], v[128:131], v[12:15]\n"
    "v_mfma_f32_16x16x32_bf16 v[28:31], v[180:183], a[240:243], v[104:107]\n"
    "v_mfma_f32_16x16x32_bf16 v[28:31], v[184:187], a[244:247], v[28:31]\n"
    "v_mfma_f32_16x16x32_bf16 v[28:31], v[188:191], a[248:251], v[28:31]\n"
    "v_mfma_f32_16x16x32_bf16 v[28:31], v[192:195], a[252:255], v[28:31]\n"
    "v_mfma_f32_16x16x32_bf16 v[28:31], v[196:199], v[124:127], v[28:31]\n"
    "v_mfma_f32_16x16x32_bf16 v[28:31], v[200:203], v[128:131], v[28:31]\n"
    "s_add_u32 m0, s65, 73728\n"
    "s_add_u32 s72, s65, 73728\n"
    "buffer_load_dwordx4 v1, s[16:19], s72 offen offset:0 sc1 lds\n"
    "s_add_u32 m0, s65, 74752\n"
    "s_add_u32 s72, s65, 74752\n"
    "buffer_load_dwordx4 v1, s[16:19], s72 offen offset:0 sc1 lds\n"
    "s_add_u32 m0, s65, 75776\n"
    "s_add_u32 s72, s65, 75776\n"
    "buffer_load_dwordx4 v1, s[16:19], s72 offen offset:0 sc1 lds\n"
    "s_add_u32 m0, s65, 76800\n"
    "s_add_u32 s72, s65, 76800\n"
    "buffer_load_dwordx4 v1, s[16:19], s72 offen offset:0 sc1 lds\n"
    "s_add_u32 m0, s65, 77824\n"
    "s_add_u32 s72, s65, 77824\n"
    "buffer_load_dwordx4 v1, s[16:19], s72 offen offset:0 sc1 lds\n"
    "s_add_u32 m0, s65, 78848\n"
    "s_add_u32 s72, s65, 78848\n"
    "buffer_load_dwordx4 v1, s[16:19], s72 offen offset:0 sc1 lds\n"
    "s_bitcmp1_b32 s83, 1\n"
    "s_cbranch_scc1 .Lf10b\n"
    "buffer_load_dword v204, v240, s[36:39], s45 offen offset:0\n"
    "buffer_load_dword v204, v240, s[36:39], s45 offen offset:0\n"
    "buffer_load_dword v204, v240, s[36:39], s45 offen offset:0\n"
    "buffer_load_dword v204, v240, s[36:39], s45 offen offset:0\n"
    "buffer_load_dword v204, v240, s[36:39], s45 offen offset:0\n"
    "buffer_load_dword v204, v240, s[36:39], s45 offen offset:0\n"
    "buffer_load_dword v204, v240, s[36:39], s45 offen offset:0\n"
    "buffer_load_dword v204, v240, s[36:39], s45 offen offset:0\n"
    "s_branch .Lf10e\n"
    ".Lf10b:\n"
    "s_bitcmp1_b32 s83, 0\n"
    "s_cbranch_scc1 .Lf9b\n"
    "s_add_u32 s75, s71, s63\n"
    "s_add_u32 s76, s75, 6144\n"
    "s_add_u32 s77, s75, 24576\n"
    "s_add_u32 s78, s75, 30720\n"
    "buffer_load_dwordx4 v[204:207], v242, s[28:31], s75 offen offset:0 nt\n"
    "buffer_load_dwordx4 v[208:211], v242, s[28:31], s75 offen offset:3072 nt\n"
    "buffer_load_dwordx4 v[212:215], v242, s[28:31], s76 offen offset:0 nt\n"
    "buffer_load_dwordx4 v[216:219], v242, s[28:31], s76 offen offset:3072 nt\n"
    "buffer_load_dwordx4 v[220:223], v242, s[28:31], s77 offen offset:0 nt\n"
    "buffer_load_dwordx4 v[224:227], v242, s[28:31], s77 offen offset:3072 nt\n"
    "buffer_load_dwordx4 v[228:231], v242, s[28:31], s78 offen offset:0 nt\n"
    "buffer_load_dwordx4 v[232:235], v242, s[28:31], s78 offen offset:3072 nt\n"
    "s_branch .Lf9e\n"
    ".Lf9b:\n"
    "s_add_u32 s75, s71, s63\n"
    "buffer_load_dwordx3 v[204:206], v243, s[40:43], s75 offen offset:1536 nt\n"
    "buffer_load_dwordx3 v[208:210], v243, s[40:43], s75 offen offset:2304 nt\n"
    "buffer_load_dwordx3 v[212:214], v243, s[40:43], s75 offen offset:0 nt\n"
    "buffer_load_dwordx3 v[216:218], v243, s[40:43], s75 offen offset:768 nt\n"
    "buffer_load_dwordx3 v[220:222], v242, s[40:43], s75 offen offset:1536 nt\n"
    "buffer_load_dwordx3 v[224:226], v242, s[40:43], s75 offen offset:2304 nt\n"
    "buffer_load_dwordx3 v[228:230], v242, s[40:43], s75 offen offset:0 nt\n"
    "buffer_load_dwordx3 v[232:234], v242, s[40:43], s75 offen offset:768 nt\n"
    ".Lf9e:\n"
    "s_nop 4\n"
    ".Lf10e:\n"
    "s_nop 4\n"
    "v_exp_f32 v12, v12\n"
    "v_exp_f32 v13, v13\n"
    "v_exp_f32 v14, v14\n"
    "v_exp_f32 v15, v15\n"
    "v_exp_f32 v28, v28\n"
    "v_exp_f32 v29, v29\n"
    "v_exp_f32 v30, v30\n"
    "v_exp_f32 v31, v31\n"
    "s_waitcnt vmcnt(30)\n"
    "s_bitcmp1_b32 s83, 1\n"
    "s_cbranch_scc1 .Lf13b\n"
    "s_bitcmp1_b32 s83, 0\n"
    "s_cbranch_scc1 .Lf12b\n"
    "s_add_u32 s76, s71, s60\n"
    "s_add_u32 s77, s71, s61\n"
    "v_cvt_f32_i32 v36, v36\n"
    "v_cvt_f32_i32 v37, v37\n"
    "v_cvt_f32_i32 v38, v38\n"
    "v_cvt_f32_i32 v39, v39\n"
    "v_cvt_f32_i32 v40, v40\n"
    "v_cvt_f32_i32 v41, v41\n"
    "v_cvt_f32_i32 v42, v42\n"
    "v_cvt_f32_i32 v43, v43\n"
    "v_cvt_f32_i32 v44, v44\n"
    "v_cvt_f32_i32 v45, v45\n"
    "v_cvt_f32_i32 v46, v46\n"
    "v_cvt_f32_i32 v47, v47\n"
    "ds_read_b128 v[92:95], v243 offset:0\n"
    "ds_read_b128 v[96:99], v243 offset:128\n"
    "v_cvt_f32_i32 v48, v48\n"
    "v_cvt_f32_i32 v49, v49\n"
    "v_cvt_f32_i32 v50, v50\n"
    "v_cvt_f32_i32 v51, v51\n"
    "v_cvt_f32_i32 v52, v52\n"
    "v_cvt_f32_i32 v53, v53\n"
    "v_cvt_f32_i32 v54, v54\n"
    "v_cvt_f32_i32 v55, v55\n"
    "v_cvt_f32_i32 v56, v56\n"
    "v_cvt_f32_i32 v57, v57\n"
    "v_cvt_f32_i32 v58, v58\n"
    "v_cvt_f32_i32 v59, v59\n"
    "s_waitcnt lgkmcnt(0)\n"
    "v_fma_f32 v36, v36, v92, v96\n"
    "v_fma_f32 v37, v37, v93, v97\n"
    "v_fma_f32 v38, v38, v94, v98\n"
    "v_fma_f32 v39, v39, v95, v99\n"
    "v_cvt_pk_bf16_f32 v36, v36, v37\n"
    "v_cvt_pk_bf16_f32 v37, v38, v39\n"
    "v_cvt_f32_i32 v60, v60\n"
    "v_cvt_f32_i32 v61, v61\n"
    "v_cvt_f32_i32 v62, v62\n"
    "v_cvt_f32_i32 v63, v63\n"
    "v_fma_f32 v40, v40, v92, v96\n"
    "v_fma_f32 v41, v41, v93, v97\n"
    "v_fma_f32 v42, v42, v94, v98\n"
    "v_fma_f32 v43, v43, v95, v99\n"
    "v_cvt_pk_bf16_f32 v40, v40, v41\n"
    "v_cvt_pk_bf16_f32 v41, v42, v43\n"
    "v_cvt_f32_i32 v64, v64\n"
    "v_cvt_f32_i32 v65, v65\n"
    "v_cvt_f32_i32 v66, v66\n"
    "v_cvt_f32_i32 v67, v67\n"
    "v_fma_f32 v44, v44, v92, v96\n"
    "v_fma_f32 v45, v45, v93, v97\n"
    "v_fma_f32 v46, v46, v94, v98\n"
    "v_fma_f32 v47, v47, v95, v99\n"
    "v_cvt_pk_bf16_f32 v44, v44, v45\n"
    "v_cvt_pk_bf16_f32 v45, v46, v47\n"
    "v_fma_f32 v48, v48, v92, v96\n"
    "v_fma_f32 v49, v49, v93, v97\n"
    "v_fma_f32 v50, v50, v94, v98\n"
    "v_fma_f32 v51, v51, v95, v99\n"
    "v_cvt_pk_bf16_f32 v48, v48, v49\n"
    "v_cvt_pk_bf16_f32 v49, v50, v51\n"
    "v_fma_f32 v52, v52, v92, v96\n"
    "v_fma_f32 v53, v53, v93, v97\n"
    "v_fma_f32 v54, v54, v94, v98\n"
    "v_fma_f32 v55, v55, v95, v99\n"
    "v_cvt_pk_bf16_f32 v52, v52, v53\n"
    "v_cvt_pk_bf16_f32 v53, v54, v55\n"
    "v_fma_f32 v56, v56, v92, v96\n"
    "v_fma_f32 v57, v57, v93, v97\n"
    "v_fma_f32 v58, v58, v94, v98\n"
    "v_fma_f32 v59, v59, v95, v99\n"
    "v_cvt_pk_bf16_f32 v56, v56, v57\n"
    "v_cvt_pk_bf16_f32 v57, v58, v59\n"
    "v_fma_f32 v60, v60, v92, v96\n"
    "v_fma_f32 v61, v61, v93, v97\n"
    "v_fma_f32 v62, v62, v94, v98\n"
    "v_fma_f32 v63, v63, v95, v99\n"
    "v_cvt_pk_bf16_f32 v60, v60, v61\n"
    "v_cvt_pk_bf16_f32 v61, v62, v63\n"
    "v_fma_f32 v64, v64, v92, v96\n"
    "v_fma_f32 v65, v65, v93, v97\n"
    "v_fma_f32 v66, v66, v94, v98\n"
    "v_fma_f32 v67, v67, v95, v99\n"
    "v_cvt_pk_bf16_f32 v64, v64, v65\n"
    "v_cvt_pk_bf16_f32 v65, v66, v67\n"
    "v_add_u32 v243, 512, v243\n"
    "s_branch .Lf12e\n"
    ".Lf12b:\n"
    "s_add_u32 s76, s71, s60\n"
    "s_add_u32 s77, s71, s61\n"
    "s_add_u32 s78, s71, s62\n"
    "v_cvt_f32_i32 v36, v36\n"
    "v_cvt_f32_i32 v40, v40\n"
    "v_cvt_f32_i32 v37, v37\n"
    "v_cvt_f32_i32 v41, v41\n"
    "v_cvt_f32_i32 v38, v38\n"
    "v_cvt_f32_i32 v42, v42\n"
    "v_cvt_pk_bf16_f32 v39, v36, v40\n"
    "v_cvt_pk_bf16_f32 v43, v37, v41\n"
    "v_cvt_pk_bf16_f32 v47, v38, v42\n"
    "v_cvt_f32_i32 v44, v44\n"
    "v_cvt_f32_i32 v48, v48\n"
    "v_cvt_f32_i32 v45, v45\n"
    "v_cvt_f32_i32 v49, v49\n"
    "v_cvt_f32_i32 v46, v46\n"
    "v_cvt_f32_i32 v50, v50\n"
    "v_cvt_pk_bf16_f32 v38, v44, v48\n"
    "v_cvt_pk_bf16_f32 v42, v45, v49\n"
    "v_cvt_pk_bf16_f32 v46, v46, v50\n"
    "v_cvt_f32_i32 v52, v52\n"
    "v_cvt_f32_i32 v56, v56\n"
    "v_cvt_f32_i32 v53, v53\n"
    "v_cvt_f32_i32 v57, v57\n"
    "v_cvt_f32_i32 v54, v54\n"
    "v_cvt_f32_i32 v58, v58\n"
    "v_cvt_pk_bf16_f32 v37, v52, v56\n"
    "v_cvt_pk_bf16_f32 v41, v53, v57\n"
    "v_cvt_pk_bf16_f32 v45, v54, v58\n"
    "v_cvt_f32_i32 v60, v60\n"
    "v_cvt_f32_i32 v64, v64\n"
    "v_cvt_f32_i32 v61, v61\n"
    "v_cvt_f32_i32 v65, v65\n"
    "v_cvt_f32_i32 v62, v62\n"
    "v_cvt_f32_i32 v66, v66\n"
    "v_cvt_pk_bf16_f32 v36, v60, v64\n"
    "v_cvt_pk_bf16_f32 v40, v61, v65\n"
    "v_cvt_pk_bf16_f32 v44, v62, v66\n"
    ".Lf12e:\n"
    "s_nop 4\n"
    "s_branch .Lf13e\n"
    ".Lf13b:\n"
    ".Lf13e:\n"
    "s_nop 4\n"
    "s_waitcnt vmcnt(24)\n"
    "s_barrier\n"
    "s_waitcnt vmcnt(14)\n"
    "ds_read_b128 v[132:135], v84 offset:49152\n"
    "ds_read_b128 v[136:139], v85 offset:49152\n"
    "ds_read_b128 v[140:143], v84 offset:49280\n"
    "ds_read_b128 v[144:147], v85 offset:49280\n"
    "ds_read_b128 v[148:151], v84 offset:49408\n"
    "ds_read_b128 v[152:155], v85 offset:49408\n"
    "s_waitcnt lgkmcnt(0)\n"
    "v_mfma_f32_16x16x32_bf16 v[16:19], v[176:179], v[152:155], v[100:103]\n"
    "v_mfma_f32_16x16x32_bf16 v[16:19], v[172:175], v[148:151], v[16:19]\n"
    "v_mfma_f32_16x16x32_bf16 v[16:19], v[168:171], v[144:147], v[16:19]\n"
    "v_mfma_f32_16x16x32_bf16 v[16:19], v[164:167], v[140:143], v[16:19]\n"
    "v_mfma_f32_16x16x32_bf16 v[16:19], v[160:163], v[136:139], v[16:19]\n"
    "v_mfma_f32_16x16x32_bf16 v[16:19], v[156:159], v[132:135], v[16:19]\n"
    "v_mfma_f32_16x16x32_bf16 v[32:35], v[200:203], v[152:155], v[104:107]\n"
    "v_mfma_f32_16x16x32_bf16 v[32:35], v[196:199], v[148:151], v[32:35]\n"
    "v_mfma_f32_16x16x32_bf16 v[32:35], v[192:195], v[144:147], v[32:35]\n"
    "v_mfma_f32_16x16x32_bf16 v[32:35], v[188:191], v[140:143], v[32:35]\n"
    "v_mfma_f32_16x16x32_bf16 v[32:35], v[184:187], v[136:139], v[32:35]\n"
    "v_mfma_f32_16x16x32_bf16 v[32:35], v[180:183], v[132:135], v[32:35]\n"
    "s_waitcnt vmcnt(8)\n"
    "buffer_load_dwordx2 v[236:237], v241, s[36:39], s81 offen offset:0 sc1\n"
    "s_bitcmp1_b32 s83, 1\n"
    "s_cbranch_scc1 .Lf15b\n"
    "s_bitcmp1_b32 s83, 0\n"
    "s_cbranch_scc1 .Lf14b\n"
    "buffer_store_dwordx2 v[36:37], v244, s[16:19], s76 offen offset:0 sc1\n"
    "buffer_store_dwordx2 v[40:41], v244, s[16:19], s76 offen offset:1024 sc1\n"
    "buffer_store_dwordx2 v[44:45], v244, s[16:19], s76 offen offset:256 sc1\n"
    "buffer_store_dwordx2 v[48:49], v244, s[16:19], s76 offen offset:1280 sc1\n"
    "buffer_store_dwordx2 v[52:53], v244, s[16:19], s77 offen offset:0 sc1\n"
    "buffer_store_dwordx2 v[56:57], v244, s[16:19], s77 offen offset:1024 sc1\n"
    "buffer_store_dwordx2 v[60:61], v244, s[16:19], s77 offen offset:256 sc1\n"
    "buffer_store_dwordx2 v[64:65], v244, s[16:19], s77 offen offset:1280 sc1\n"
    "s_branch .Lf14e\n"
    ".Lf14b:\n"
    "buffer_store_dwordx4 v[36:39], v244, s[16:19], s76 offen offset:0 sc1\n"
    "buffer_store_dwordx4 v[40:43], v244, s[16:19], s77 offen offset:0 sc1\n"
    "buffer_store_dwordx4 v[44:47], v244, s[16:19], s78 offen offset:0 sc1\n"
    ".Lf14e:\n"
    "s_nop 4\n"
    "s_branch .Lf15e\n"
    ".Lf15b:\n"
    ".Lf15e:\n"
    "s_nop 4\n"
    "ds_read_b128 v[156:159], v2 offset:12288\n"
    "ds_read_b128 v[160:163], v2 offset:14336\n"
    "ds_read_b128 v[164:167], v2 offset:16384\n"
    "ds_read_b128 v[168:171], v2 offset:18432\n"
    "ds_read_b128 v[172:175], v2 offset:20480\n"
    "ds_read_b128 v[176:179], v2 offset:22528\n"
    "ds_read_b128 v[100:103], v77 offset:128\n"
    "v_exp_f32 v16, v16\n"
    "v_exp_f32 v17, v17\n"
    "v_exp_f32 v18, v18\n"
    "v_exp_f32 v19, v19\n"
    "v_exp_f32 v32, v32\n"
    "v_exp_f32 v33, v33\n"
    "v_exp_f32 v34, v34\n"
    "v_exp_f32 v35, v35\n"
    "s_mov_b32 s66, 0\n"
    "s_waitcnt lgkmcnt(0)\n"
    "ds_add_u32 v247, v246\n"
    "s_bitcmp1_b32 s83, 1\n"
    "s_cbranch_scc1 .Lf18b\n"
    "s_bitcmp1_b32 s83, 0\n"
    "s_cbranch_scc1 .Lf16b\n"
    "s_nop 7\n"
    "s_branch .LPK\n"
    "s_branch .Lf16e\n"
    ".Lf16b:\n"
    "s_nop 7\n"
    "s_branch .LPV\n"
    ".Lf16e:\n"
    "s_nop 4\n"
    "s_branch .Lf18e\n"
    ".Lf18b:\n"
    "s_bitcmp1_b32 s83, 0\n"
    "s_cbranch_scc1 .Lf17b\n"
    "s_nop 7\n"
    "s_branch .LNK\n"
    "s_branch .Lf17e\n"
    ".Lf17b:\n"
    "s_nop 7\n"
    "s_branch .LNV\n"
    ".Lf17e:\n"
    "s_nop 4\n"
    ".Lf18e:\n"
    "s_nop 4\n"
    ".p2align 6\n"
    ".LPK:\n"
    "v_mfma_f32_16x16x32_bf16 v[36:39], v[156:159], a[192:195], v[100:103]\n"
    "s_add_u32 s71, s71, 98304\n"
    "ds_read_b128 v[180:183], v2 offset:13312\n"
    "v_cvt_pk_bf16_f32 v68, v4, v5\n"
    "v_mfma_f32_16x16x32_bf16 v[36:39], v[160:163], a[196:199], v[36:39]\n"
    "s_add_u32 s75, s71, s63\n"
    "ds_read_b128 v[184:187], v2 offset:15360\n"
    "v_mfma_f32_16x16x32_bf16 v[36:39], v[164:167], a[200:203], v[36:39]\n"
    "s_add_u32 s76, s75, 6144\n"
    "ds_read_b128 v[188:191], v2 offset:17408\n"
    "v_mfma_f32_16x16x32_bf16 v[36:39], v[168:171], a[204:207], v[36:39]\n"
    "s_add_u32 s77, s75, 24576\n"
    "ds_read_b128 v[192:195], v2 offset:19456\n"
    "v_cvt_pk_bf16_f32 v69, v6, v7\n"
    "v_mfma_f32_16x16x32_bf16 v[36:39], v[172:175], a[208:211], v[36:39]\n"
    "s_add_u32 s78, s75, 30720\n"
    "ds_read_b128 v[196:199], v2 offset:21504\n"
    "v_mfma_f32_16x16x32_bf16 v[36:39], v[176:179], a[212:215], v[36:39]\n"
    "ds_read_b128 v[200:203], v2 offset:23552\n"
    "v_mfma_f32_16x16x32_bf16 v[40:43], v[176:179], a[236:239], v[100:103]\n"
    "ds_read_b128 v[104:107], v250 offset:144\n"
    "v_mfma_f32_16x16x32_bf16 v[40:43], v[172:175], a[232:235], v[40:43]\n"
    "s_add_u32 s68, s68, 24576\n"
    "v_cvt_pk_bf16_f32 v72, v8, v9\n"
    "v_mfma_f32_16x16x32_bf16 v[40:43], v[168:171], a[228:231], v[40:43]\n"
    "s_cmp_ge_u32 s68, s69\n"
    "s_cselect_b32 s68, s65, s68\n"
    "v_mfma_f32_16x16x32_bf16 v[40:43], v[164:167], a[224:227], v[40:43]\n"
    "v_exp_f32 v36, v36\n"
    "v_mfma_f32_16x16x32_bf16 v[40:43], v[160:163], a[220:223], v[40:43]\n"
    "v_exp_f32 v37, v37\n"
    "v_mfma_f32_16x16x32_bf16 v[40:43], v[156:159], a[216:219], v[40:43]\n"
    "v_cvt_pk_bf16_f32 v73, v10, v11\n"
    "v_cvt_pk_bf16_f32 v84, v36, v37\n"
    "v_mfma_f32_16x16x32_bf16 v[44:47], v[156:159], a[240:243], v[100:103]\n"
    "v_mfma_f32_16x16x32_bf16 v[44:47], v[160:163], a[244:247], v[44:47]\n"
    "v_exp_f32 v38, v38\n"
    "v_mfma_f32_16x16x32_bf16 v[44:47], v[164:167], a[248:251], v[44:47]\n"
    "v_mfma_f32_16x16x32_bf16 v[44:47], v[168:171], a[252:255], v[44:47]\n"
    "v_cvt_pk_bf16_f32 v76, v12, v13\n"
    "v_mfma_f32_16x16x32_bf16 v[44:47], v[172:175], v[124:127], v[44:47]\n"
    "v_exp_f32 v39, v39\n"
    "v_mfma_f32_16x16x32_bf16 v[44:47], v[176:179], v[128:131], v[44:47]\n"
    "v_cvt_pk_bf16_f32 v85, v38, v39\n"
    "v_mfma_f32_16x16x32_bf16 v[48:51], v[176:179], v[152:155], v[100:103]\n"
    "ds_read_b32 v245, v247\n"
    "v_cvt_pk_bf16_f32 v77, v14, v15\n"
    "v_mfma_f32_16x16x32_bf16 v[48:51], v[172:175], v[148:151], v[48:51]\n"
    "s_add_u32 m0, s68, 0\n"
    "v_exp_f32 v40, v40\n"
    "v_mfma_f32_16x16x32_bf16 v[48:51], v[168:171], v[144:147], v[48:51]\n"
    "v_mfma_f32_16x16x32_bf16 v[48:51], v[164:167], v[140:143], v[48:51]\n"
    "v_mfma_f32_16x16x32_bf16 v[48:51], v[160:163], v[136:139], v[48:51]\n"
    "s_waitcnt vmcnt(8)\n"
    "v_cmp_eq_u64 s[54:55], v[236:237], v[238:239]\n"
    "v_cvt_pk_bf16_f32 v80, v16, v17\n"
    "v_mfma_f32_16x16x32_bf16 v[48:51], v[156:159], v[132:135], v[48:51]\n"
    "s_waitcnt lgkmcnt(1)\n"
    "v_exp_f32 v41, v41\n"
    "v_mfma_f32_16x16x32_bf16 v[52:55], v[180:183], a[192:195], v[104:107]\n"
    "s_waitcnt lgkmcnt(0)\n"
    "v_cmp_le_u32 s[56:57], s59, v245\n"
    "v_cvt_pk_bf16_f32 v88, v40, v41\n"
    "v_mfma_f32_16x16x32_bf16 v[52:55], v[184:187], a[196:199], v[52:55]\n"
    "v_exp_f32 v42, v42\n"
    "v_mfma_f32_16x16x32_bf16 v[52:55], v[188:191], a[200:203], v[52:55]\n"
    "s_and_b64 s[54:55], s[54:55], s[56:57]\n"
    "s_cmp_eq_u64 s[54:55], -1\n"
    "v_mfma_f32_16x16x32_bf16 v[52:55], v[192:195], a[204:207], v[52:55]\n"
    "s_cbranch_scc0 .Lpoll1\n"
    ".Lpoll1r:\n"
    "v_mfma_f32_16x16x32_bf16 v[52:55], v[196:199], a[208:211], v[52:55]\n"
    "buffer_load_dwordx4 v1, s[16:19], s67 offen offset:0 sc1 lds\n"
    "v_mfma_f32_16x16x32_bf16 v[52:55], v[200:203], a[212:215], v[52:55]\n"
    "ds_read_b128 v[156:159], v3 offset:0\n"
    "ds_read_b128 v[160:163], v3 offset:2048\n"
    "v_mfma_f32_16x16x32_bf16 v[56:59], v[200:203], a[236:239], v[104:107]\n"
    "ds_read_b128 v[164:167], v3 offset:4096\n"
    "ds_read_b128 v[168:171], v3 offset:6144\n"
    "v_mfma_f32_16x16x32_bf16 v[56:59], v[196:199], a[232:235], v[56:59]\n"
    "ds_read_b128 v[172:175], v3 offset:8192\n"
    "ds_read_b128 v[176:179], v3 offset:10240\n"
    "v_mfma_f32_16x16x32_bf16 v[56:59], v[192:195], a[228:231], v[56:59]\n"
    "buffer_load_dwordx4 v1, s[16:19], s67 offen offset:1024 sc1 lds\n"
    "v_mfma_f32_16x16x32_bf16 v[56:59], v[188:191], a[224:227], v[56:59]\n"
    "ds_read_b128 v[100:103], v250 offset:768\n"
    "v_cvt_pk_bf16_f32 v81, v18, v19\n"
    "v_mfma_f32_16x16x32_bf16 v[56:59], v[184:187], a[220:223], v[56:59]\n"
    "v_cvt_pk_bf16_f32 v70, v20, v21\n"
    "v_cvt_pk_bf16_f32 v71, v22, v23\n"
    "v_mfma_f32_16x16x32_bf16 v[56:59], v[180:183], a[216:219], v[56:59]\n"
    "v_exp_f32 v43, v43\n"
    "v_mfma_f32_16x16x32_bf16 v[60:63], v[180:183], a[240:243], v[104:107]\n"
    "buffer_load_dwordx4 v1, s[16:19], s67 offen offset:2048 sc1 lds\n"
    "v_mfma_f32_16x16x32_bf16 v[60:63], v[184:187], a[244:247], v[60:63]\n"
    "v_cvt_pk_bf16_f32 v74, v24, v25\n"
    "v_cvt_pk_bf16_f32 v89, v42, v43\n"
    "v_mfma_f32_16x16x32_bf16 v[60:63], v[188:191], a[248:251], v[60:63]\n"
    "v_exp_f32 v44, v44\n"
    "v_mfma_f32_16x16x32_bf16 v[60:63], v[192:195], a[252:255], v[60:63]\n"
    "v_exp_f32 v45, v45\n"
    "v_mfma_f32_16x16x32_bf16 v[60:63], v[196:199], v[124:127], v[60:63]\n"
    "buffer_load_dwordx4 v1, s[16:19], s67 offen offset:3072 sc1 lds\n"
    "v_mfma_f32_16x16x32_bf16 v[60:63], v[200:203], v[128:131], v[60:63]\n"
    "s_add_u32 m0, s68, 4096\n"
    "v_cvt_pk_bf16_f32 v75, v26, v27\n"
    "v_cvt_pk_bf16_f32 v92, v44, v45\n"
    "v_mfma_f32_16x16x32_bf16 v[64:67], v[200:203], v[152:155], v[104:107]\n"
    "s_add_u32 s83, s67, 4096\n"
    "v_exp_f32 v46, v46\n"
    "v_mfma_f32_16x16x32_bf16 v[64:67], v[196:199], v[148:151], v[64:67]\n"
    "v_exp_f32 v47, v47\n"
    "v_mfma_f32_16x16x32_bf16 v[64:67], v[192:195], v[144:147], v[64:67]\n"
    "buffer_load_dwordx4 v1, s[16:19], s83 offen offset:0 sc1 lds\n"
    "v_mfma_f32_16x16x32_bf16 v[64:67], v[188:191], v[140:143], v[64:67]\n"
    "v_cvt_pk_bf16_f32 v78, v28, v29\n"
    "v_cvt_pk_bf16_f32 v93, v46, v47\n"
    "v_mfma_f32_16x16x32_bf16 v[64:67], v[184:187], v[136:139], v[64:67]\n"
    "v_exp_f32 v48, v48\n"
    "v_mfma_f32_16x16x32_bf16 v[64:67], v[180:183], v[132:135], v[64:67]\n"
    "s_waitcnt lgkmcnt(0)\n"
    "v_exp_f32 v49, v49\n"
    "v_mfma_f32_16x16x32_bf16 v[4:7], v[156:159], a[192:195], v[100:103]\n"
    "buffer_load_dwordx4 v1, s[16:19], s83 offen offset:1024 sc1 lds\n"
    "v_mfma_f32_16x16x32_bf16 v[4:7], v[160:163], a[196:199], v[4:7]\n"
    "s_add_u32 m0, s64, 0\n"
    "ds_read_b128 v[180:183], v3 offset:1024\n"
    "ds_read_b128 v[184:187], v3 offset:3072\n"
    "v_mfma_f32_16x16x32_bf16 v[4:7], v[164:167], a[200:203], v[4:7]\n"
    "s_add_u32 s83, s67, 24576\n"
    "ds_read_b128 v[188:191], v3 offset:5120\n"
    "v_cvt_pk_bf16_f32 v79, v30, v31\n"
    "v_mfma_f32_16x16x32_bf16 v[4:7], v[168:171], a[204:207], v[4:7]\n"
    "ds_read_b128 v[192:195], v3 offset:7168\n"
    "v_cvt_pk_bf16_f32 v96, v48, v49\n"
    "v_mfma_f32_16x16x32_bf16 v[4:7], v[172:175], a[208:211], v[4:7]\n"
    "buffer_load_dwordx4 v1, s[16:19], s83 offen offset:0 sc1 lds\n"
    "v_mfma_f32_16x16x32_bf16 v[4:7], v[176:179], a[212:215], v[4:7]\n"
    "ds_read_b128 v[196:199], v3 offset:9216\n"
    "ds_read_b128 v[200:203], v3 offset:11264\n"
    "v_mfma_f32_16x16x32_bf16 v[8:11], v[176:179], a[236:239], v[100:103]\n"
    "ds_read_b128 v[104:107], v250 offset:784\n"
    "v_cvt_pk_bf16_f32 v82, v32, v33\n"
    "v_mfma_f32_16x16x32_bf16 v[8:11], v[172:175], a[232:235], v[8:11]\n"
    "v_exp_f32 v52, v52\n"
    "v_mfma_f32_16x16x32_bf16 v[8:11], v[168:171], a[228:231], v[8:11]\n"
    "buffer_load_dwordx4 v1, s[16:19], s83 offen offset:1024 sc1 lds\n"
    "v_mfma_f32_16x16x32_bf16 v[8:11], v[164:167], a[224:227], v[8:11]\n"
    "v_cvt_pk_bf16_f32 v83, v34, v35\n"
    "v_mfma_f32_16x16x32_bf16 v[8:11], v[160:163], a[220:223], v[8:11]\n"
    "v_exp_f32 v53, v53\n"
    "v_mfma_f32_16x16x32_bf16 v[8:11], v[156:159], a[216:219], v[8:11]\n"
    "v_exp_f32 v54, v54\n"
    "v_mfma_f32_16x16x32_bf16 v[12:15], v[156:159], a[240:243], v[100:103]\n"
    "buffer_load_dwordx4 v1, s[16:19], s83 offen offset:2048 sc1 lds\n"
    "v_mfma_f32_16x16x32_bf16 v[12:15], v[160:163], a[244:247], v[12:15]\n"
    "v_cvt_pk_bf16_f32 v86, v52, v53\n"
    "v_mfma_f32_16x16x32_bf16 v[12:15], v[164:167], a[248:251], v[12:15]\n"
    "v_exp_f32 v55, v55\n"
    "v_mfma_f32_16x16x32_bf16 v[12:15], v[168:171], a[252:255], v[12:15]\n"
    "v_cvt_pk_bf16_f32 v87, v54, v55\n"
    "v_mfma_f32_16x16x32_bf16 v[12:15], v[172:175], v[124:127], v[12:15]\n"
    "buffer_load_dwordx4 v1, s[16:19], s83 offen offset:3072 sc1 lds\n"
    "v_mfma_f32_16x16x32_bf16 v[12:15], v[176:179], v[128:131], v[12:15]\n"
    "s_add_u32 m0, s64, 4096\n"
    "v_exp_f32 v56, v56\n"
    "v_mfma_f32_16x16x32_bf16 v[16:19], v[176:179], v[152:155], v[100:103]\n"
    "s_add_u32 s83, s67, 28672\n"
    "v_exp_f32 v50, v50\n"
    "v_mfma_f32_16x16x32_bf16 v[16:19], v[172:175], v[148:151], v[16:19]\n"
    "v_exp_f32 v51, v51\n"
    "v_mfma_f32_16x16x32_bf16 v[16:19], v[168:171], v[144:147], v[16:19]\n"
    "buffer_load_dwordx4 v1, s[16:19], s83 offen offset:0 sc1 lds\n"
    "v_mfma_f32_16x16x32_bf16 v[16:19], v[164:167], v[140:143], v[16:19]\n"
    "v_exp_f32 v57, v57\n"
    "v_mfma_f32_16x16x32_bf16 v[16:19], v[160:163], v[136:139], v[16:19]\n"
    "v_cvt_pk_bf16_f32 v90, v56, v57\n"
    "v_cvt_pk_bf16_f32 v97, v50, v51\n"
    "v_mfma_f32_16x16x32_bf16 v[16:19], v[156:159], v[132:135], v[16:19]\n"
    "s_waitcnt lgkmcnt(0)\n"
    "v_exp_f32 v58, v58\n"
    "v_mfma_f32_16x16x32_bf16 v[20:23], v[180:183], a[192:195], v[104:107]\n"
    "buffer_load_dwordx4 v1, s[16:19], s83 offen offset:1024 sc1 lds\n"
    "v_mfma_f32_16x16x32_bf16 v[20:23], v[184:187], a[196:199], v[20:23]\n"
    "s_add_u32 s67, s67, 49152\n"
    "ds_read_b128 v[156:159], v248 offset:0\n"
    "ds_read_b128 v[160:163], v248 offset:12288\n"
    "v_mfma_f32_16x16x32_bf16 v[20:23], v[188:191], a[200:203], v[20:23]\n"
    "s_add_u32 s59, s59, 4\n"
    "ds_read_b128 v[164:167], v248 offset:1024\n"
    "v_mfma_f32_16x16x32_bf16 v[20:23], v[192:195], a[204:207], v[20:23]\n"
    "s_add_u32 s81, s81, 128\n"
    "s_waitcnt vmcnt(12)\n"
    "ds_read_b128 v[168:171], v248 offset:13312\n"
    "v_mfma_f32_16x16x32_bf16 v[20:23], v[196:199], a[208:211], v[20:23]\n"
    "s_min_u32 s81, s81, 4096\n"
    "ds_read_b128 v[172:175], v248 offset:2048\n"
    "v_mfma_f32_16x16x32_bf16 v[20:23], v[200:203], a[212:215], v[20:23]\n"
    "buffer_store_dwordx2 v[238:239], v240, s[36:39], s80 offen offset:0 sc1\n"
    "v_mfma_f32_16x16x32_bf16 v[24:27], v[200:203], a[236:239], v[104:107]\n"
    "s_add_u32 s80, s80, 256\n"
    "ds_read_b128 v[176:179], v248 offset:14336\n"
    "ds_read_b128 v[100:103], v251 offset:0\n"
    "v_mfma_f32_16x16x32_bf16 v[24:27], v[196:199], a[232:235], v[24:27]\n"
    "v_exp_f32 v59, v59\n"
    "v_mfma_f32_16x16x32_bf16 v[24:27], v[192:195], a[228:231], v[24:27]\n"
    "v_cvt_pk_bf16_f32 v91, v58, v59\n"
    "v_mfma_f32_16x16x32_bf16 v[24:27], v[188:191], a[224:227], v[24:27]\n"
    "v_exp_f32 v60, v60\n"
    "v_mfma_f32_16x16x32_bf16 v[24:27], v[184:187], a[220:223], v[24:27]\n"
    "v_exp_f32 v61, v61\n"
    "v_mfma_f32_16x16x32_bf16 v[24:27], v[180:183], a[216:219], v[24:27]\n"
    "v_exp_f32 v62, v62\n"
    "v_mfma_f32_16x16x32_bf16 v[28:31], v[180:183], a[240:243], v[104:107]\n"
    "v_cvt_pk_bf16_f32 v94, v60, v61\n"
    "v_mfma_f32_16x16x32_bf16 v[28:31], v[184:187], a[244:247], v[28:31]\n"
    "v_exp_f32 v63, v63\n"
    "v_mfma_f32_16x16x32_bf16 v[28:31], v[188:191], a[248:251], v[28:31]\n"
    "v_cvt_pk_bf16_f32 v95, v62, v63\n"
    "v_mfma_f32_16x16x32_bf16 v[28:31], v[192:195], a[252:255], v[28:31]\n"
    "buffer_load_dwordx4 v[204:207], v242, s[28:31], s75 offen offset:0 nt\n"
    "v_mfma_f32_16x16x32_bf16 v[28:31], v[196:199], v[124:127], v[28:31]\n"
    "v_exp_f32 v64, v64\n"
    "v_mfma_f32_16x16x32_bf16 v[28:31], v[200:203], v[128:131], v[28:31]\n"
    "v_mfma_f32_16x16x32_bf16 v[32:35], v[200:203], v[152:155], v[104:107]\n"
    "buffer_load_dwordx4 v[208:211], v242, s[28:31], s75 offen offset:3072 nt\n"
    "v_mfma_f32_16x16x32_bf16 v[32:35], v[196:199], v[148:151], v[32:35]\n"
    "v_exp_f32 v65, v65\n"
    "v_mfma_f32_16x16x32_bf16 v[32:35], v[192:195], v[144:147], v[32:35]\n"
    "v_cvt_pk_bf16_f32 v98, v64, v65\n"
    "v_mfma_f32_16x16x32_bf16 v[32:35], v[188:191], v[140:143], v[32:35]\n"
    "buffer_load_dwordx4 v[212:215], v242, s[28:31], s76 offen offset:0 nt\n"
    "v_mfma_f32_16x16x32_bf16 v[32:35], v[184:187], v[136:139], v[32:35]\n"
    "v_exp_f32 v66, v66\n"
    "v_mfma_f32_16x16x32_bf16 v[32:35], v[180:183], v[132:135], v[32:35]\n"
    "s_waitcnt lgkmcnt(0)\n"
    "v_mfma_f32_16x16x32_bf16 a[0:3], v[156:159], v[68:71], a[0:3]\n"
    "ds_read_b128 v[180:183], v248 offset:3072\n"
    "v_mfma_f32_16x16x32_bf16 a[0:3], v[160:163], v[84:87], a[0:3]\n"
    "ds_read_b128 v[184:187], v248 offset:15360\n"
    "v_mfma_f32_16x16x32_bf16 a[16:19], v[168:171], v[84:87], a[16:19]\n"
    "ds_read_b128 v[188:191], v248 offset:4096\n"
    "v_mfma_f32_16x16x32_bf16 a[16:19], v[164:167], v[68:71], a[16:19]\n"
    "ds_read_b128 v[192:195], v248 offset:16384\n"
    "v_mfma_f32_16x16x32_bf16 a[32:35], v[172:175], v[68:71], a[32:35]\n"
    "ds_read_b128 v[196:199], v248 offset:5120\n"
    "v_mfma_f32_16x16x32_bf16 a[32:35], v[176:179], v[84:87], a[32:35]\n"
    "ds_read_b128 v[200:203], v248 offset:17408\n"
    "v_mfma_f32_16x16x32_bf16 a[36:39], v[176:179], v[88:91], a[36:39]\n"
    "ds_read_b128 v[104:107], v251 offset:64\n"
    "v_mfma_f32_16x16x32_bf16 a[36:39], v[172:175], v[72:75], a[36:39]\n"
    "v_exp_f32 v67, v67\n"
    "v_mfma_f32_16x16x32_bf16 a[20:23], v[164:167], v[72:75], a[20:23]\n"
    "v_cvt_pk_bf16_f32 v99, v66, v67\n"
    "v_mfma_f32_16x16x32_bf16 a[20:23], v[168:171], v[88:91], a[20:23]\n"
    "buffer_load_dwordx4 v[216:219], v242, s[28:31], s76 offen offset:3072 nt\n"
    "v_mfma_f32_16x16x32_bf16 a[4:7], v[160:163], v[88:91], a[4:7]\n"
    "v_exp_f32 v4, v4\n"
    "v_mfma_f32_16x16x32_bf16 a[4:7], v[156:159], v[72:75], a[4:7]\n"
    "v_exp_f32 v5, v5\n"
    "v_mfma_f32_16x16x32_bf16 a[8:11], v[156:159], v[76:79], a[8:11]\n"
    "buffer_load_dwordx4 v[220:223], v242, s[28:31], s77 offen offset:0 nt\n"
    "v_mfma_f32_16x16x32_bf16 a[8:11], v[160:163], v[92:95], a[8:11]\n"
    "v_exp_f32 v6, v6\n"
    "v_mfma_f32_16x16x32_bf16 a[24:27], v[168:171], v[92:95], a[24:27]\n"
    "v_exp_f32 v7, v7\n"
    "v_mfma_f32_16x16x32_bf16 a[24:27], v[164:167], v[76:79], a[24:27]\n"
    "buffer_load_dwordx4 v[224:227], v242, s[28:31], s77 offen offset:3072 nt\n"
    "v_mfma_f32_16x16x32_bf16 a[40:43], v[172:175], v[76:79], a[40:43]\n"
    "v_exp_f32 v8, v8\n"
    "v_mfma_f32_16x16x32_bf16 a[40:43], v[176:179], v[92:95], a[40:43]\n"
    "v_mfma_f32_16x16x32_bf16 a[44:47], v[176:179], v[96:99], a[44:47]\n"
    "buffer_load_dwordx4 v[228:231], v242, s[28:31], s78 offen offset:0 nt\n"
    "v_mfma_f32_16x16x32_bf16 a[44:47], v[172:175], v[80:83], a[44:47]\n"
    "v_exp_f32 v9, v9\n"
    "v_mfma_f32_16x16x32_bf16 a[28:31], v[164:167], v[80:83], a[28:31]\n"
    "v_exp_f32 v10, v10\n"
    "v_mfma_f32_16x16x32_bf16 a[28:31], v[168:171], v[96:99], a[28:31]\n"
    "buffer_load_dwordx4 v[232:235], v242, s[28:31], s78 offen offset:3072 nt\n"
    "v_mfma_f32_16x16x32_bf16 a[12:15], v[160:163], v[96:99], a[12:15]\n"
    "v_mfma_f32_16x16x32_bf16 a[12:15], v[156:159], v[80:83], a[12:15]\n"
    "s_waitcnt lgkmcnt(0)\n"
    "v_exp_f32 v11, v11\n"
    "v_mfma_f32_16x16x32_bf16 a[48:51], v[180:183], v[68:71], a[48:51]\n"
    "ds_read_b128 v[156:159], v248 offset:6144\n"
    "v_mfma_f32_16x16x32_bf16 a[48:51], v[184:187], v[84:87], a[48:51]\n"
    "ds_read_b128 v[160:163], v248 offset:18432\n"
    "v_mfma_f32_16x16x32_bf16 a[64:67], v[192:195], v[84:87], a[64:67]\n"
    "ds_read_b128 v[164:167], v248 offset:7168\n"
    "v_mfma_f32_16x16x32_bf16 a[64:67], v[188:191], v[68:71], a[64:67]\n"
    "ds_read_b128 v[168:171], v248 offset:19456\n"
    "v_mfma_f32_16x16x32_bf16 a[80:83], v[196:199], v[68:71], a[80:83]\n"
    "ds_read_b128 v[172:175], v248 offset:8192\n"
    "v_mfma_f32_16x16x32_bf16 a[80:83], v[200:203], v[84:87], a[80:83]\n"
    "ds_read_b128 v[176:179], v248 offset:20480\n"
    "v_mfma_f32_16x16x32_bf16 v[108:111], v[104:107], v[84:87], v[108:111]\n"
    "v_exp_f32 v12, v12\n"
    "v_mfma_f32_16x16x32_bf16 v[108:111], v[100:103], v[68:71], v[108:111]\n"
    "v_exp_f32 v13, v13\n"
    "v_mfma_f32_16x16x32_bf16 v[112:115], v[100:103], v[72:75], v[112:115]\n"
    "v_exp_f32 v14, v14\n"
    "v_mfma_f32_16x16x32_bf16 v[112:115], v[104:107], v[88:91], v[112:115]\n"
    "v_mfma_f32_16x16x32_bf16 a[84:87], v[200:203], v[88:91], a[84:87]\n"
    "v_exp_f32 v15, v15\n"
    "v_mfma_f32_16x16x32_bf16 a[84:87], v[196:199], v[72:75], a[84:87]\n"
    "v_mfma_f32_16x16x32_bf16 a[68:71], v[188:191], v[72:75], a[68:71]\n"
    "v_mfma_f32_16x16x32_bf16 a[68:71], v[192:195], v[88:91], a[68:71]\n"
    "v_exp_f32 v16, v16\n"
    "v_mfma_f32_16x16x32_bf16 a[52:55], v[184:187], v[88:91], a[52:55]\n"
    "v_mfma_f32_16x16x32_bf16 a[52:55], v[180:183], v[72:75], a[52:55]\n"
    "v_mfma_f32_16x16x32_bf16 a[56:59], v[180:183], v[76:79], a[56:59]\n"
    "v_exp_f32 v17, v17\n"
    "v_mfma_f32_16x16x32_bf16 a[56:59], v[184:187], v[92:95], a[56:59]\n"
    "v_mfma_f32_16x16x32_bf16 a[72:75], v[192:195], v[92:95], a[72:75]\n"
    "v_mfma_f32_16x16x32_bf16 a[72:75], v[188:191], v[76:79], a[72:75]\n"
    "v_exp_f32 v18, v18\n"
    "v_mfma_f32_16x16x32_bf16 a[88:91], v[196:199], v[76:79], a[88:91]\n"
    "v_mfma_f32_16x16x32_bf16 a[88:91], v[200:203], v[92:95], a[88:91]\n"
    "v_mfma_f32_16x16x32_bf16 v[116:119], v[104:107], v[92:95], v[116:119]\n"
    "v_exp_f32 v19, v19\n"
    "v_mfma_f32_16x16x32_bf16 v[116:119], v[100:103], v[76:79], v[116:119]\n"
    "v_mfma_f32_16x16x32_bf16 v[120:123], v[100:103], v[80:83], v[120:123]\n"
    "v_mfma_f32_16x16x32_bf16 v[120:123], v[104:107], v[96:99], v[120:123]\n"
    "v_exp_f32 v20, v20\n"
    "v_mfma_f32_16x16x32_bf16 a[92:95], v[200:203], v[96:99], a[92:95]\n"
    "v_mfma_f32_16x16x32_bf16 a[92:95], v[196:199], v[80:83], a[92:95]\n"
    "v_exp_f32 v21, v21\n"
    "v_mfma_f32_16x16x32_bf16 a[76:79], v[188:191], v[80:83], a[76:79]\n"
    "v_mfma_f32_16x16x32_bf16 a[76:79], v[192:195], v[96:99], a[76:79]\n"
    "v_mfma_f32_16x16x32_bf16 a[60:63], v[184:187], v[96:99], a[60:63]\n"
    "v_exp_f32 v22, v22\n"
    "v_mfma_f32_16x16x32_bf16 a[60:63], v[180:183], v[80:83], a[60:63]\n"
    "s_waitcnt lgkmcnt(0)\n"
    "v_mfma_f32_16x16x32_bf16 a[96:99], v[156:159], v[68:71], a[96:99]\n"
    "ds_read_b128 v[180:183], v248 offset:9216\n"
    "v_mfma_f32_16x16x32_bf16 a[96:99], v[160:163], v[84:87], a[96:99]\n"
    "ds_read_b128 v[184:187], v248 offset:21504\n"
    "v_mfma_f32_16x16x32_bf16 a[112:115], v[168:171], v[84:87], a[112:115]\n"
    "ds_read_b128 v[188:191], v248 offset:10240\n"
    "v_mfma_f32_16x16x32_bf16 a[112:115], v[164:167], v[68:71], a[112:115]\n"
    "ds_read_b128 v[192:195], v248 offset:22528\n"
    "v_mfma_f32_16x16x32_bf16 a[128:131], v[172:175], v[68:71], a[128:131]\n"
    "buffer_load_dwordx2 v[236:237], v241, s[36:39], s81 offen offset:0 sc1\n"
    "v_mfma_f32_16x16x32_bf16 a[128:131], v[176:179], v[84:87], a[128:131]\n"
    "ds_read_b128 v[196:199], v248 offset:11264\n"
    "ds_read_b128 v[200:203], v248 offset:23552\n"
    "v_mfma_f32_16x16x32_bf16 a[132:135], v[176:179], v[88:91], a[132:135]\n"
    "v_exp_f32 v23, v23\n"
    "v_mfma_f32_16x16x32_bf16 a[132:135], v[172:175], v[72:75], a[132:135]\n"
    "v_exp_f32 v24, v24\n"
    "v_mfma_f32_16x16x32_bf16 a[116:119], v[164:167], v[72:75], a[116:119]\n"
    "v_exp_f32 v25, v25\n"
    "v_mfma_f32_16x16x32_bf16 a[116:119], v[168:171], v[88:91], a[116:119]\n"
    "v_exp_f32 v26, v26\n"
    "v_mfma_f32_16x16x32_bf16 a[100:103], v[160:163], v[88:91], a[100:103]\n"
    "v_mfma_f32_16x16x32_bf16 a[100:103], v[156:159], v[72:75], a[100:103]\n"
    "v_mfma_f32_16x16x32_bf16 a[104:107], v[156:159], v[76:79], a[104:107]\n"
    "v_exp_f32 v27, v27\n"
    "v_mfma_f32_16x16x32_bf16 a[104:107], v[160:163], v[92:95], a[104:107]\n"
    "v_mfma_f32_16x16x32_bf16 a[120:123], v[168:171], v[92:95], a[120:123]\n"
    "v_mfma_f32_16x16x32_bf16 a[120:123], v[164:167], v[76:79], a[120:123]\n"
    "v_exp_f32 v28, v28\n"
    "v_mfma_f32_16x16x32_bf16 a[136:139], v[172:175], v[76:79], a[136:139]\n"
    "v_mfma_f32_16x16x32_bf16 a[136:139], v[176:179], v[92:95], a[136:139]\n"
    "v_mfma_f32_16x16x32_bf16 a[140:143], v[176:179], v[96:99], a[140:143]\n"
    "v_exp_f32 v29, v29\n"
    "v_mfma_f32_16x16x32_bf16 a[140:143], v[172:175], v[80:83], a[140:143]\n"
    "v_mfma_f32_16x16x32_bf16 a[124:127], v[164:167], v[80:83], a[124:127]\n"
    "v_mfma_f32_16x16x32_bf16 a[124:127], v[168:171], v[96:99], a[124:127]\n"
    "v_exp_f32 v30, v30\n"
    "v_mfma_f32_16x16x32_bf16 a[108:111], v[160:163], v[96:99], a[108:111]\n"
    "v_mfma_f32_16x16x32_bf16 a[108:111], v[156:159], v[80:83], a[108:111]\n"
    "s_waitcnt lgkmcnt(0)\n"
    "v_exp_f32 v31, v31\n"
    "v_mfma_f32_16x16x32_bf16 a[144:147], v[180:183], v[68:71], a[144:147]\n"
    "ds_read_b128 v[156:159], v3 offset:12288\n"
    "v_mfma_f32_16x16x32_bf16 a[144:147], v[184:187], v[84:87], a[144:147]\n"
    "ds_read_b128 v[160:163], v3 offset:14336\n"
    "v_mfma_f32_16x16x32_bf16 a[160:163], v[192:195], v[84:87], a[160:163]\n"
    "ds_read_b128 v[164:167], v3 offset:16384\n"
    "v_mfma_f32_16x16x32_bf16 a[160:163], v[188:191], v[68:71], a[160:163]\n"
    "ds_read_b128 v[168:171], v3 offset:18432\n"
    "v_mfma_f32_16x16x32_bf16 a[176:179], v[196:199], v[68:71], a[176:179]\n"
    "ds_read_b128 v[172:175], v3 offset:20480\n"
    "v_mfma_f32_16x16x32_bf16 a[176:179], v[200:203], v[84:87], a[176:179]\n"
    "ds_read_b128 v[176:179], v3 offset:22528\n"
    "v_mfma_f32_16x16x32_bf16 a[180:183], v[200:203], v[88:91], a[180:183]\n"
    "ds_read_b128 v[100:103], v250 offset:896\n"
    "v_mfma_f32_16x16x32_bf16 a[180:183], v[196:199], v[72:75], a[180:183]\n"
    "v_exp_f32 v32, v32\n"
    "v_mfma_f32_16x16x32_bf16 a[164:167], v[188:191], v[72:75], a[164:167]\n"
    "v_exp_f32 v33, v33\n"
    "v_mfma_f32_16x16x32_bf16 a[164:167], v[192:195], v[88:91], a[164:167]\n"
    "v_exp_f32 v34, v34\n"
    "v_mfma_f32_16x16x32_bf16 a[148:151], v[184:187], v[88:91], a[148:151]\n"
    "v_mfma_f32_16x16x32_bf16 a[148:151], v[180:183], v[72:75], a[148:151]\n"
    "v_exp_f32 v35, v35\n"
    "v_mfma_f32_16x16x32_bf16 a[152:155], v[180:183], v[76:79], a[152:155]\n"
    "v_mfma_f32_16x16x32_bf16 a[152:155], v[184:187], v[92:95], a[152:155]\n"
    "v_mfma_f32_16x16x32_bf16 a[168:171], v[192:195], v[92:95], a[168:171]\n"
    "s_waitcnt vmcnt(10)\n"
    "ds_add_u32 v247, v246\n"
    "v_mfma_f32_16x16x32_bf16 a[168:171], v[188:191], v[76:79], a[168:171]\n"
    "v_mfma_f32_16x16x32_bf16 a[184:187], v[196:199], v[76:79], a[184:187]\n"
    "v_mfma_f32_16x16x32_bf16 a[184:187], v[200:203], v[92:95], a[184:187]\n"
    "v_mfma_f32_16x16x32_bf16 a[188:191], v[200:203], v[96:99], a[188:191]\n"
    "v_mfma_f32_16x16x32_bf16 a[188:191], v[196:199], v[80:83], a[188:191]\n"
    "v_mfma_f32_16x16x32_bf16 a[172:175], v[188:191], v[80:83], a[172:175]\n"
    "s_sub_u32 s74, s68, s65\n"
    "v_mfma_f32_16x16x32_bf16 a[172:175], v[192:195], v[96:99], a[172:175]\n"
    "s_add_u32 s66, s66, 1\n"
    "v_mov_b32 v2, v3\n"
    "v_add_u32 v3, s74, v252\n"
    "v_mfma_f32_16x16x32_bf16 a[156:159], v[184:187], v[96:99], a[156:159]\n"
    "s_waitcnt lgkmcnt(1)\n"
    "s_cmp_eq_u32 s66, 31\n"
    "v_mfma_f32_16x16x32_bf16 a[156:159], v[180:183], v[80:83], a[156:159]\n"
    "s_cbranch_scc1 .LtailP\n"
    ".LNK:\n"
    "v_mfma_f32_16x16x32_bf16 v[36:39], v[156:159], a[192:195], v[100:103]\n"
    "ds_read_b128 v[180:183], v2 offset:13312\n"
    "v_cvt_pk_bf16_f32 v68, v4, v5\n"
    "v_mfma_f32_16x16x32_bf16 v[36:39], v[160:163], a[196:199], v[36:39]\n"
    "ds_read_b128 v[184:187], v2 offset:15360\n"
    "v_mfma_f32_16x16x32_bf16 v[36:39], v[164:167], a[200:203], v[36:39]\n"
    "ds_read_b128 v[188:191], v2 offset:17408\n"
    "v_mfma_f32_16x16x32_bf16 v[36:39], v[168:171], a[204:207], v[36:39]\n"
    "ds_read_b128 v[192:195], v2 offset:19456\n"
    "v_cvt_pk_bf16_f32 v69, v6, v7\n"
    "v_mfma_f32_16x16x32_bf16 v[36:39], v[172:175], a[208:211], v[36:39]\n"
    "ds_read_b128 v[196:199], v2 offset:21504\n"
    "v_mfma_f32_16x16x32_bf16 v[36:39], v[176:179], a[212:215], v[36:39]\n"
    "ds_read_b128 v[200:203], v2 offset:23552\n"
    "v_mfma_f32_16x16x32_bf16 v[40:43], v[176:179], a[236:239], v[100:103]\n"
    "ds_read_b128 v[104:107], v250 offset:912\n"
    "v_mfma_f32_16x16x32_bf16 v[40:43], v[172:175], a[232:235], v[40:43]\n"
    "s_add_u32 s68, s68, 24576\n"
    "v_cvt_pk_bf16_f32 v72, v8, v9\n"
    "v_mfma_f32_16x16x32_bf16 v[40:43], v[168:171], a[228:231], v[40:43]\n"
    "s_cmp_ge_u32 s68, s69\n"
    "s_cselect_b32 s68, s65, s68\n"
    "v_mfma_f32_16x16x32_bf16 v[40:43], v[164:167], a[224:227], v[40:43]\n"
    "v_exp_f32 v36, v36\n"
    "v_mfma_f32_16x16x32_bf16 v[40:43], v[160:163], a[220:223], v[40:43]\n"
    "v_exp_f32 v37, v37\n"
    "v_mfma_f32_16x16x32_bf16 v[40:43], v[156:159], a[216:219], v[40:43]\n"
    "v_cvt_pk_bf16_f32 v73, v10, v11\n"
    "v_cvt_pk_bf16_f32 v84, v36, v37\n"
    "v_mfma_f32_16x16x32_bf16 v[44:47], v[156:159], a[240:243], v[100:103]\n"
    "v_mfma_f32_16x16x32_bf16 v[44:47], v[160:163], a[244:247], v[44:47]\n"
    "v_exp_f32 v38, v38\n"
    "v_mfma_f32_16x16x32_bf16 v[44:47], v[164:167], a[248:251], v[44:47]\n"
    "v_mfma_f32_16x16x32_bf16 v[44:47], v[168:171], a[252:255], v[44:47]\n"
    "v_cvt_pk_bf16_f32 v76, v12, v13\n"
    "v_mfma_f32_16x16x32_bf16 v[44:47], v[172:175], v[124:127], v[44:47]\n"
    "v_exp_f32 v39, v39\n"
    "v_mfma_f32_16x16x32_bf16 v[44:47], v[176:179], v[128:131], v[44:47]\n"
    "v_cvt_pk_bf16_f32 v85, v38, v39\n"
    "v_mfma_f32_16x16x32_bf16 v[48:51], v[176:179], v[152:155], v[100:103]\n"
    "ds_read_b32 v245, v247\n"
    "v_cvt_pk_bf16_f32 v77, v14, v15\n"
    "v_mfma_f32_16x16x32_bf16 v[48:51], v[172:175], v[148:151], v[48:51]\n"
    "s_add_u32 m0, s68, 0\n"
    "v_exp_f32 v40, v40\n"
    "v_mfma_f32_16x16x32_bf16 v[48:51], v[168:171], v[144:147], v[48:51]\n"
    "v_mfma_f32_16x16x32_bf16 v[48:51], v[164:167], v[140:143], v[48:51]\n"
    "v_mfma_f32_16x16x32_bf16 v[48:51], v[160:163], v[136:139], v[48:51]\n"
    "s_waitcnt vmcnt(0)\n"
    "v_cmp_eq_u64 s[54:55], v[236:237], v[238:239]\n"
    "v_cvt_pk_bf16_f32 v80, v16, v17\n"
    "v_mfma_f32_16x16x32_bf16 v[48:51], v[156:159], v[132:135], v[48:51]\n"
    "s_waitcnt lgkmcnt(1)\n"
    "v_exp_f32 v41, v41\n"
    "v_mfma_f32_16x16x32_bf16 v[52:55], v[180:183], a[192:195], v[104:107]\n"
    "s_waitcnt lgkmcnt(0)\n"
    "v_cmp_le_u32 s[56:57], s59, v245\n"
    "v_cvt_pk_bf16_f32 v88, v40, v41\n"
    "v_mfma_f32_16x16x32_bf16 v[52:55], v[184:187], a[196:199], v[52:55]\n"
    "v_exp_f32 v42, v42\n"
    "v_mfma_f32_16x16x32_bf16 v[52:55], v[188:191], a[200:203], v[52:55]\n"
    "s_and_b64 s[54:55], s[54:55], s[56:57]\n"
    "s_cmp_eq_u64 s[54:55], -1\n"
    "v_mfma_f32_16x16x32_bf16 v[52:55], v[192:195], a[204:207], v[52:55]\n"
    "s_cbranch_scc0 .Lpoll2\n"
    ".Lpoll2r:\n"
    "v_mfma_f32_16x16x32_bf16 v[52:55], v[196:199], a[208:211], v[52:55]\n"
    "buffer_load_dwordx4 v1, s[16:19], s67 offen offset:0 sc1 lds\n"
    "v_mfma_f32_16x16x32_bf16 v[52:55], v[200:203], a[212:215], v[52:55]\n"
    "ds_read_b128 v[156:159], v3 offset:0\n"
    "ds_read_b128 v[160:163], v3 offset:2048\n"
    "v_mfma_f32_16x16x32_bf16 v[56:59], v[200:203], a[236:239], v[104:107]\n"
    "ds_read_b128 v[164:167], v3 offset:4096\n"
    "ds_read_b128 v[168:171], v3 offset:6144\n"
    "v_mfma_f32_16x16x32_bf16 v[56:59], v[196:199], a[232:235], v[56:59]\n"
    "ds_read_b128 v[172:175], v3 offset:8192\n"
    "ds_read_b128 v[176:179], v3 offset:10240\n"
    "v_mfma_f32_16x16x32_bf16 v[56:59], v[192:195], a[228:231], v[56:59]\n"
    "buffer_load_dwordx4 v1, s[16:19], s67 offen offset:1024 sc1 lds\n"
    "v_mfma_f32_16x16x32_bf16 v[56:59], v[188:191], a[224:227], v[56:59]\n"
    "ds_read_b128 v[100:103], v250 offset:1536\n"
    "v_cvt_pk_bf16_f32 v81, v18, v19\n"
    "v_mfma_f32_16x16x32_bf16 v[56:59], v[184:187], a[220:223], v[56:59]\n"
    "v_cvt_pk_bf16_f32 v70, v20, v21\n"
    "v_cvt_pk_bf16_f32 v71, v22, v23\n"
    "v_mfma_f32_16x16x32_bf16 v[56:59], v[180:183], a[216:219], v[56:59]\n"
    "v_exp_f32 v43, v43\n"
    "v_mfma_f32_16x16x32_bf16 v[60:63], v[180:183], a[240:243], v[104:107]\n"
    "buffer_load_dwordx4 v1, s[16:19], s67 offen offset:2048 sc1 lds\n"
    "v_mfma_f32_16x16x32_bf16 v[60:63], v[184:187], a[244:247], v[60:63]\n"
    "v_cvt_pk_bf16_f32 v74, v24, v25\n"
    "v_cvt_pk_bf16_f32 v89, v42, v43\n"
    "v_mfma_f32_16x16x32_bf16 v[60:63], v[188:191], a[248:251], v[60:63]\n"
    "v_exp_f32 v44, v44\n"
    "v_mfma_f32_16x16x32_bf16 v[60:63], v[192:195], a[252:255], v[60:63]\n"
    "v_exp_f32 v45, v45\n"
    "v_mfma_f32_16x16x32_bf16 v[60:63], v[196:199], v[124:127], v[60:63]\n"
    "buffer_load_dwordx4 v1, s[16:19], s67 offen offset:3072 sc1 lds\n"
    "v_mfma_f32_16x16x32_bf16 v[60:63], v[200:203], v[128:131], v[60:63]\n"
    "s_add_u32 m0, s68, 4096\n"
    "v_cvt_pk_bf16_f32 v75, v26, v27\n"
    "v_cvt_pk_bf16_f32 v92, v44, v45\n"
    "v_mfma_f32_16x16x32_bf16 v[64:67], v[200:203], v[152:155], v[104:107]\n"
    "s_add_u32 s83, s67, 4096\n"
    "v_exp_f32 v46, v46\n"
    "v_mfma_f32_16x16x32_bf16 v[64:67], v[196:199], v[148:151], v[64:67]\n"
    "v_exp_f32 v47, v47\n"
    "v_mfma_f32_16x16x32_bf16 v[64:67], v[192:195], v[144:147], v[64:67]\n"
    "buffer_load_dwordx4 v1, s[16:19], s83 offen offset:0 sc1 lds\n"
    "v_mfma_f32_16x16x32_bf16 v[64:67], v[188:191], v[140:143], v[64:67]\n"
    "v_cvt_pk_bf16_f32 v78, v28, v29\n"
    "v_cvt_pk_bf16_f32 v93, v46, v47\n"
    "v_mfma_f32_16x16x32_bf16 v[64:67], v[184:187], v[136:139], v[64:67]\n"
    "v_exp_f32 v48, v48\n"
    "v_mfma_f32_16x16x32_bf16 v[64:67], v[180:183], v[132:135], v[64:67]\n"
    "s_waitcnt lgkmcnt(0)\n"
    "v_exp_f32 v49, v49\n"
    "v_mfma_f32_16x16x32_bf16 v[4:7], v[156:159], a[192:195], v[100:103]\n"
    "buffer_load_dwordx4 v1, s[16:19], s83 offen offset:1024 sc1 lds\n"
    "v_mfma_f32_16x16x32_bf16 v[4:7], v[160:163], a[196:199], v[4:7]\n"
    "s_add_u32 m0, s70, 0\n"
    "ds_read_b128 v[180:183], v3 offset:1024\n"
    "ds_read_b128 v[184:187], v3 offset:3072\n"
    "v_mfma_f32_16x16x32_bf16 v[4:7], v[164:167], a[200:203], v[4:7]\n"
    "s_add_u32 s83, s67, 24576\n"
    "ds_read_b128 v[188:191], v3 offset:5120\n"
    "v_cvt_pk_bf16_f32 v79, v30, v31\n"
    "v_mfma_f32_16x16x32_bf16 v[4:7], v[168:171], a[204:207], v[4:7]\n"
    "ds_read_b128 v[192:195], v3 offset:7168\n"
    "v_cvt_pk_bf16_f32 v96, v48, v49\n"
    "v_mfma_f32_16x16x32_bf16 v[4:7], v[172:175], a[208:211], v[4:7]\n"
    "buffer_load_dwordx4 v1, s[16:19], s83 offen offset:0 sc1 lds\n"
    "v_mfma_f32_16x16x32_bf16 v[4:7], v[176:179], a[212:215], v[4:7]\n"
    "ds_read_b128 v[196:199], v3 offset:9216\n"
    "ds_read_b128 v[200:203], v3 offset:11264\n"
    "v_mfma_f32_16x16x32_bf16 v[8:11], v[176:179], a[236:239], v[100:103]\n"
    "ds_read_b128 v[104:107], v250 offset:1552\n"
    "v_cvt_pk_bf16_f32 v82, v32, v33\n"
    "v_mfma_f32_16x16x32_bf16 v[8:11], v[172:175], a[232:235], v[8:11]\n"
    "v_exp_f32 v52, v52\n"
    "v_mfma_f32_16x16x32_bf16 v[8:11], v[168:171], a[228:231], v[8:11]\n"
    "buffer_load_dwordx4 v1, s[16:19], s83 offen offset:1024 sc1 lds\n"
    "v_mfma_f32_16x16x32_bf16 v[8:11], v[164:167], a[224:227], v[8:11]\n"
    "v_cvt_pk_bf16_f32 v83, v34, v35\n"
    "v_mfma_f32_16x16x32_bf16 v[8:11], v[160:163], a[220:223], v[8:11]\n"
    "v_exp_f32 v53, v53\n"
    "v_mfma_f32_16x16x32_bf16 v[8:11], v[156:159], a[216:219], v[8:11]\n"
    "v_exp_f32 v54, v54\n"
    "v_mfma_f32_16x16x32_bf16 v[12:15], v[156:159], a[240:243], v[100:103]\n"
    "buffer_load_dwordx4 v1, s[16:19], s83 offen offset:2048 sc1 lds\n"
    "v_mfma_f32_16x16x32_bf16 v[12:15], v[160:163], a[244:247], v[12:15]\n"
    "s_waitcnt vmcnt(10)\n"
    "s_add_u32 s76, s71, s60\n"
    "v_cvt_pk_bf16_f32 v86, v52, v53\n"
    "v_mfma_f32_16x16x32_bf16 v[12:15], v[164:167], a[248:251], v[12:15]\n"
    "s_add_u32 s77, s71, s61\n"
    "v_exp_f32 v55, v55\n"
    "v_mfma_f32_16x16x32_bf16 v[12:15], v[168:171], a[252:255], v[12:15]\n"
    "v_cvt_pk_bf16_f32 v87, v54, v55\n"
    "v_cvt_f32_i32 v204, v204\n"
    "v_mfma_f32_16x16x32_bf16 v[12:15], v[172:175], v[124:127], v[12:15]\n"
    "buffer_load_dwordx4 v1, s[16:19], s83 offen offset:3072 sc1 lds\n"
    "v_mfma_f32_16x16x32_bf16 v[12:15], v[176:179], v[128:131], v[12:15]\n"
    "s_add_u32 m0, s70, 4096\n"
    "v_exp_f32 v56, v56\n"
    "v_mfma_f32_16x16x32_bf16 v[16:19], v[176:179], v[152:155], v[100:103]\n"
    "s_add_u32 s83, s67, 28672\n"
    "v_exp_f32 v50, v50\n"
    "v_mfma_f32_16x16x32_bf16 v[16:19], v[172:175], v[148:151], v[16:19]\n"
    "v_exp_f32 v51, v51\n"
    "v_mfma_f32_16x16x32_bf16 v[16:19], v[168:171], v[144:147], v[16:19]\n"
    "buffer_load_dwordx4 v1, s[16:19], s83 offen offset:0 sc1 lds\n"
    "v_mfma_f32_16x16x32_bf16 v[16:19], v[164:167], v[140:143], v[16:19]\n"
    "v_exp_f32 v57, v57\n"
    "v_mfma_f32_16x16x32_bf16 v[16:19], v[160:163], v[136:139], v[16:19]\n"
    "v_cvt_pk_bf16_f32 v90, v56, v57\n"
    "v_cvt_pk_bf16_f32 v97, v50, v51\n"
    "v_mfma_f32_16x16x32_bf16 v[16:19], v[156:159], v[132:135], v[16:19]\n"
    "s_waitcnt lgkmcnt(0)\n"
    "v_exp_f32 v58, v58\n"
    "v_mfma_f32_16x16x32_bf16 v[20:23], v[180:183], a[192:195], v[104:107]\n"
    "buffer_load_dwordx4 v1, s[16:19], s83 offen offset:1024 sc1 lds\n"
    "v_mfma_f32_16x16x32_bf16 v[20:23], v[184:187], a[196:199], v[20:23]\n"
    "s_add_u32 s67, s67, 49152\n"
    "ds_read_b128 v[156:159], v249 offset:0\n"
    "ds_read_b128 v[160:163], v249 offset:12288\n"
    "v_mfma_f32_16x16x32_bf16 v[20:23], v[188:191], a[200:203], v[20:23]\n"
    "s_add_u32 s59, s59, 4\n"
    "ds_read_b128 v[164:167], v249 offset:1024\n"
    "v_cvt_f32_i32 v205, v205\n"
    "v_mfma_f32_16x16x32_bf16 v[20:23], v[192:195], a[204:207], v[20:23]\n"
    "s_add_u32 s81, s81, 128\n"
    "ds_read_b128 v[168:171], v249 offset:13312\n"
    "v_cvt_f32_i32 v206, v206\n"
    "v_mfma_f32_16x16x32_bf16 v[20:23], v[196:199], a[208:211], v[20:23]\n"
    "s_min_u32 s81, s81, 4096\n"
    "ds_read_b128 v[172:175], v249 offset:2048\n"
    "v_cvt_f32_i32 v207, v207\n"
    "v_mfma_f32_16x16x32_bf16 v[20:23], v[200:203], a[212:215], v[20:23]\n"
    "ds_read_b128 v[176:179], v249 offset:14336\n"
    "v_cvt_f32_i32 v208, v208\n"
    "v_mfma_f32_16x16x32_bf16 v[24:27], v[200:203], a[236:239], v[104:107]\n"
    "ds_read_b128 v[100:103], v251 offset:768\n"
    "v_cvt_f32_i32 v209, v209\n"
    "v_mfma_f32_16x16x32_bf16 v[24:27], v[196:199], a[232:235], v[24:27]\n"
    "v_exp_f32 v59, v59\n"
    "v_mfma_f32_16x16x32_bf16 v[24:27], v[192:195], a[228:231], v[24:27]\n"
    "v_cvt_pk_bf16_f32 v91, v58, v59\n"
    "v_cvt_f32_i32 v210, v210\n"
    "v_mfma_f32_16x16x32_bf16 v[24:27], v[188:191], a[224:227], v[24:27]\n"
    "v_exp_f32 v60, v60\n"
    "v_mfma_f32_16x16x32_bf16 v[24:27], v[184:187], a[220:223], v[24:27]\n"
    "v_exp_f32 v61, v61\n"
    "v_mfma_f32_16x16x32_bf16 v[24:27], v[180:183], a[216:219], v[24:27]\n"
    "v_exp_f32 v62, v62\n"
    "v_mfma_f32_16x16x32_bf16 v[28:31], v[180:183], a[240:243], v[104:107]\n"
    "v_cvt_pk_bf16_f32 v94, v60, v61\n"
    "v_cvt_f32_i32 v211, v211\n"
    "v_mfma_f32_16x16x32_bf16 v[28:31], v[184:187], a[244:247], v[28:31]\n"
    "v_exp_f32 v63, v63\n"
    "v_mfma_f32_16x16x32_bf16 v[28:31], v[188:191], a[248:251], v[28:31]\n"
    "v_cvt_pk_bf16_f32 v95, v62, v63\n"
    "v_cvt_f32_i32 v212, v212\n"
    "v_mfma_f32_16x16x32_bf16 v[28:31], v[192:195], a[252:255], v[28:31]\n"
    "v_cvt_f32_i32 v213, v213\n"
    "v_cvt_f32_i32 v214, v214\n"
    "v_mfma_f32_16x16x32_bf16 v[28:31], v[196:199], v[124:127], v[28:31]\n"
    "v_exp_f32 v64, v64\n"
    "v_mfma_f32_16x16x32_bf16 v[28:31], v[200:203], v[128:131], v[28:31]\n"
    "ds_read_b128 v[36:39], v243 offset:0\n"
    "v_cvt_f32_i32 v215, v215\n"
    "v_mfma_f32_16x16x32_bf16 v[32:35], v[200:203], v[152:155], v[104:107]\n"
    "ds_read_b128 v[40:43], v243 offset:128\n"
    "v_cvt_f32_i32 v216, v216\n"
    "v_mfma_f32_16x16x32_bf16 v[32:35], v[196:199], v[148:151], v[32:35]\n"
    "v_exp_f32 v65, v65\n"
    "v_mfma_f32_16x16x32_bf16 v[32:35], v[192:195], v[144:147], v[32:35]\n"
    "v_cvt_pk_bf16_f32 v98, v64, v65\n"
    "v_add_u32 v243, 512, v243\n"
    "v_mfma_f32_16x16x32_bf16 v[32:35], v[188:191], v[140:143], v[32:35]\n"
    "v_cvt_f32_i32 v217, v217\n"
    "v_cvt_f32_i32 v218, v218\n"
    "v_mfma_f32_16x16x32_bf16 v[32:35], v[184:187], v[136:139], v[32:35]\n"
    "v_exp_f32 v66, v66\n"
    "v_mfma_f32_16x16x32_bf16 v[32:35], v[180:183], v[132:135], v[32:35]\n"
    "s_waitcnt lgkmcnt(2)\n"
    "v_cvt_f32_i32 v219, v219\n"
    "v_cvt_f32_i32 v220, v220\n"
    "v_mfma_f32_16x16x32_bf16 a[0:3], v[156:159], v[68:71], a[0:3]\n"
    "ds_read_b128 v[180:183], v249 offset:3072\n"
    "v_cvt_f32_i32 v221, v221\n"
    "v_mfma_f32_16x16x32_bf16 a[0:3], v[160:163], v[84:87], a[0:3]\n"
    "ds_read_b128 v[184:187], v249 offset:15360\n"
    "v_cvt_f32_i32 v222, v222\n"
    "v_mfma_f32_16x16x32_bf16 a[16:19], v[168:171], v[84:87], a[16:19]\n"
    "ds_read_b128 v[188:191], v249 offset:4096\n"
    "v_cvt_f32_i32 v223, v223\n"
    "v_mfma_f32_16x16x32_bf16 a[16:19], v[164:167], v[68:71], a[16:19]\n"
    "ds_read_b128 v[192:195], v249 offset:16384\n"
    "v_cvt_f32_i32 v224, v224\n"
    "v_mfma_f32_16x16x32_bf16 a[32:35], v[172:175], v[68:71], a[32:35]\n"
    "ds_read_b128 v[196:199], v249 offset:5120\n"
    "v_cvt_f32_i32 v225, v225\n"
    "v_mfma_f32_16x16x32_bf16 a[32:35], v[176:179], v[84:87], a[32:35]\n"
    "ds_read_b128 v[200:203], v249 offset:17408\n"
    "v_cvt_f32_i32 v226, v226\n"
    "v_mfma_f32_16x16x32_bf16 a[36:39], v[176:179], v[88:91], a[36:39]\n"
    "ds_read_b128 v[104:107], v251 offset:832\n"
    "v_cvt_f32_i32 v227, v227\n"
    "v_mfma_f32_16x16x32_bf16 a[36:39], v[172:175], v[72:75], a[36:39]\n"
    "v_exp_f32 v67, v67\n"
    "v_mfma_f32_16x16x32_bf16 a[20:23], v[164:167], v[72:75], a[20:23]\n"
    "s_waitcnt lgkmcnt(7)\n"
    "v_cvt_pk_bf16_f32 v99, v66, v67\n"
    "v_fma_f32 v204, v204, v36, v40\n"
    "v_mfma_f32_16x16x32_bf16 a[20:23], v[168:171], v[88:91], a[20:23]\n"
    "v_exp_f32 v4, v4\n"
    "v_mfma_f32_16x16x32_bf16 a[4:7], v[160:163], v[88:91], a[4:7]\n"
    "v_fma_f32 v205, v205, v37, v41\n"
    "v_fma_f32 v206, v206, v38, v42\n"
    "v_mfma_f32_16x16x32_bf16 a[4:7], v[156:159], v[72:75], a[4:7]\n"
    "v_exp_f32 v5, v5\n"
    "v_mfma_f32_16x16x32_bf16 a[8:11], v[156:159], v[76:79], a[8:11]\n"
    "v_fma_f32 v207, v207, v39, v43\n"
    "v_cvt_pk_bf16_f32 v204, v204, v205\n"
    "v_mfma_f32_16x16x32_bf16 a[8:11], v[160:163], v[92:95], a[8:11]\n"
    "v_exp_f32 v6, v6\n"
    "v_mfma_f32_16x16x32_bf16 a[24:27], v[168:171], v[92:95], a[24:27]\n"
    "v_cvt_pk_bf16_f32 v205, v206, v207\n"
    "v_cvt_f32_i32 v228, v228\n"
    "v_mfma_f32_16x16x32_bf16 a[24:27], v[164:167], v[76:79], a[24:27]\n"
    "v_exp_f32 v7, v7\n"
    "v_mfma_f32_16x16x32_bf16 a[40:43], v[172:175], v[76:79], a[40:43]\n"
    "v_cvt_f32_i32 v229, v229\n"
    "v_cvt_f32_i32 v230, v230\n"
    "v_mfma_f32_16x16x32_bf16 a[40:43], v[176:179], v[92:95], a[40:43]\n"
    "v_cvt_f32_i32 v231, v231\n"
    "v_fma_f32 v208, v208, v36, v40\n"
    "v_mfma_f32_16x16x32_bf16 a[44:47], v[176:179], v[96:99], a[44:47]\n"
    "v_exp_f32 v8, v8\n"
    "v_mfma_f32_16x16x32_bf16 a[44:47], v[172:175], v[80:83], a[44:47]\n"
    "v_fma_f32 v209, v209, v37, v41\n"
    "v_fma_f32 v210, v210, v38, v42\n"
    "v_mfma_f32_16x16x32_bf16 a[28:31], v[164:167], v[80:83], a[28:31]\n"
    "v_exp_f32 v9, v9\n"
    "v_mfma_f32_16x16x32_bf16 a[28:31], v[168:171], v[96:99], a[28:31]\n"
    "v_fma_f32 v211, v211, v39, v43\n"
    "v_cvt_pk_bf16_f32 v208, v208, v209\n"
    "v_mfma_f32_16x16x32_bf16 a[12:15], v[160:163], v[96:99], a[12:15]\n"
    "v_exp_f32 v10, v10\n"
    "v_mfma_f32_16x16x32_bf16 a[12:15], v[156:159], v[80:83], a[12:15]\n"
    "s_waitcnt lgkmcnt(0)\n"
    "v_cvt_pk_bf16_f32 v209, v210, v211\n"
    "v_cvt_f32_i32 v232, v232\n"
    "v_mfma_f32_16x16x32_bf16 a[48:51], v[180:183], v[68:71], a[48:51]\n"
    "ds_read_b128 v[156:159], v249 offset:6144\n"
    "v_cvt_f32_i32 v233, v233\n"
    "v_mfma_f32_16x16x32_bf16 a[48:51], v[184:187], v[84:87], a[48:51]\n"
    "ds_read_b128 v[160:163], v249 offset:18432\n"
    "v_cvt_f32_i32 v234, v234\n"
    "v_mfma_f32_16x16x32_bf16 a[64:67], v[192:195], v[84:87], a[64:67]\n"
    "ds_read_b128 v[164:167], v249 offset:7168\n"
    "v_cvt_f32_i32 v235, v235\n"
    "v_mfma_f32_16x16x32_bf16 a[64:67], v[188:191], v[68:71], a[64:67]\n"
    "ds_read_b128 v[168:171], v249 offset:19456\n"
    "v_fma_f32 v212, v212, v36, v40\n"
    "v_mfma_f32_16x16x32_bf16 a[80:83], v[196:199], v[68:71], a[80:83]\n"
    "ds_read_b128 v[172:175], v249 offset:8192\n"
    "v_fma_f32 v213, v213, v37, v41\n"
    "v_mfma_f32_16x16x32_bf16 a[80:83], v[200:203], v[84:87], a[80:83]\n"
    "ds_read_b128 v[176:179], v249 offset:20480\n"
    "v_fma_f32 v214, v214, v38, v42\n"
    "v_mfma_f32_16x16x32_bf16 v[108:111], v[104:107], v[84:87], v[108:111]\n"
    "v_exp_f32 v11, v11\n"
    "v_mfma_f32_16x16x32_bf16 v[108:111], v[100:103], v[68:71], v[108:111]\n"
    "v_exp_f32 v12, v12\n"
    "v_mfma_f32_16x16x32_bf16 v[112:115], v[100:103], v[72:75], v[112:115]\n"
    "v_exp_f32 v13, v13\n"
    "v_mfma_f32_16x16x32_bf16 v[112:115], v[104:107], v[88:91], v[112:115]\n"
    "v_exp_f32 v14, v14\n"
    "v_mfma_f32_16x16x32_bf16 a[84:87], v[200:203], v[88:91], a[84:87]\n"
    "v_fma_f32 v215, v215, v39, v43\n"
    "v_cvt_pk_bf16_f32 v212, v212, v213\n"
    "v_mfma_f32_16x16x32_bf16 a[84:87], v[196:199], v[72:75], a[84:87]\n"
    "v_exp_f32 v15, v15\n"
    "v_mfma_f32_16x16x32_bf16 a[68:71], v[188:191], v[72:75], a[68:71]\n"
    "v_cvt_pk_bf16_f32 v213, v214, v215\n"
    "v_fma_f32 v216, v216, v36, v40\n"
    "v_mfma_f32_16x16x32_bf16 a[68:71], v[192:195], v[88:91], a[68:71]\n"
    "v_exp_f32 v16, v16\n"
    "v_mfma_f32_16x16x32_bf16 a[52:55], v[184:187], v[88:91], a[52:55]\n"
    "v_fma_f32 v217, v217, v37, v41\n"
    "v_fma_f32 v218, v218, v38, v42\n"
    "v_mfma_f32_16x16x32_bf16 a[52:55], v[180:183], v[72:75], a[52:55]\n"
    "v_fma_f32 v219, v219, v39, v43\n"
    "v_cvt_pk_bf16_f32 v216, v216, v217\n"
    "v_mfma_f32_16x16x32_bf16 a[56:59], v[180:183], v[76:79], a[56:59]\n"
    "v_exp_f32 v17, v17\n"
    "v_mfma_f32_16x16x32_bf16 a[56:59], v[184:187], v[92:95], a[56:59]\n"
    "v_cvt_pk_bf16_f32 v217, v218, v219\n"
    "v_fma_f32 v220, v220, v36, v40\n"
    "v_mfma_f32_16x16x32_bf16 a[72:75], v[192:195], v[92:95], a[72:75]\n"
    "v_fma_f32 v221, v221, v37, v41\n"
    "v_mfma_f32_16x16x32_bf16 a[72:75], v[188:191], v[76:79], a[72:75]\n"
    "v_exp_f32 v18, v18\n"
    "v_mfma_f32_16x16x32_bf16 a[88:91], v[196:199], v[76:79], a[88:91]\n"
    "v_fma_f32 v222, v222, v38, v42\n"
    "v_mfma_f32_16x16x32_bf16 a[88:91], v[200:203], v[92:95], a[88:91]\n"
    "v_fma_f32 v223, v223, v39, v43\n"
    "v_mfma_f32_16x16x32_bf16 v[116:119], v[104:107], v[92:95], v[116:119]\n"
    "v_exp_f32 v19, v19\n"
    "v_mfma_f32_16x16x32_bf16 v[116:119], v[100:103], v[76:79], v[116:119]\n"
    "v_cvt_pk_bf16_f32 v220, v220, v221\n"
    "v_cvt_pk_bf16_f32 v221, v222, v223\n"
    "v_mfma_f32_16x16x32_bf16 v[120:123], v[100:103], v[80:83], v[120:123]\n"
    "v_fma_f32 v224, v224, v36, v40\n"
    "v_mfma_f32_16x16x32_bf16 v[120:123], v[104:107], v[96:99], v[120:123]\n"
    "v_exp_f32 v20, v20\n"
    "v_mfma_f32_16x16x32_bf16 a[92:95], v[200:203], v[96:99], a[92:95]\n"
    "v_fma_f32 v225, v225, v37, v41\n"
    "v_mfma_f32_16x16x32_bf16 a[92:95], v[196:199], v[80:83], a[92:95]\n"
    "v_exp_f32 v21, v21\n"
    "v_mfma_f32_16x16x32_bf16 a[76:79], v[188:191], v[80:83], a[76:79]\n"
    "v_fma_f32 v226, v226, v38, v42\n"
    "v_fma_f32 v227, v227, v39, v43\n"
    "v_mfma_f32_16x16x32_bf16 a[76:79], v[192:195], v[96:99], a[76:79]\n"
    "v_mfma_f32_16x16x32_bf16 a[60:63], v[184:187], v[96:99], a[60:63]\n"
    "v_exp_f32 v22, v22\n"
    "v_mfma_f32_16x16x32_bf16 a[60:63], v[180:183], v[80:83], a[60:63]\n"
    "s_waitcnt lgkmcnt(0)\n"
    "v_cvt_pk_bf16_f32 v224, v224, v225\n"
    "v_cvt_pk_bf16_f32 v225, v226, v227\n"
    "v_mfma_f32_16x16x32_bf16 a[96:99], v[156:159], v[68:71], a[96:99]\n"
    "ds_read_b128 v[180:183], v249 offset:9216\n"
    "v_fma_f32 v228, v228, v36, v40\n"
    "v_mfma_f32_16x16x32_bf16 a[96:99], v[160:163], v[84:87], a[96:99]\n"
    "ds_read_b128 v[184:187], v249 offset:21504\n"
    "v_fma_f32 v229, v229, v37, v41\n"
    "v_mfma_f32_16x16x32_bf16 a[112:115], v[168:171], v[84:87], a[112:115]\n"
    "ds_read_b128 v[188:191], v249 offset:10240\n"
    "v_mfma_f32_16x16x32_bf16 a[112:115], v[164:167], v[68:71], a[112:115]\n"
    "ds_read_b128 v[192:195], v249 offset:22528\n"
    "v_fma_f32 v230, v230, v38, v42\n"
    "v_mfma_f32_16x16x32_bf16 a[128:131], v[172:175], v[68:71], a[128:131]\n"
    "buffer_load_dwordx2 v[236:237], v241, s[36:39], s81 offen offset:0 sc1\n"
    "v_mfma_f32_16x16x32_bf16 a[128:131], v[176:179], v[84:87], a[128:131]\n"
    "ds_read_b128 v[196:199], v249 offset:11264\n"
    "ds_read_b128 v[200:203], v249 offset:23552\n"
    "v_mfma_f32_16x16x32_bf16 a[132:135], v[176:179], v[88:91], a[132:135]\n"
    "v_exp_f32 v23, v23\n"
    "v_mfma_f32_16x16x32_bf16 a[132:135], v[172:175], v[72:75], a[132:135]\n"
    "v_exp_f32 v24, v24\n"
    "v_mfma_f32_16x16x32_bf16 a[116:119], v[164:167], v[72:75], a[116:119]\n"
    "buffer_store_dwordx2 v[204:205], v244, s[16:19], s76 offen offset:0 sc1\n"
    "v_mfma_f32_16x16x32_bf16 a[116:119], v[168:171], v[88:91], a[116:119]\n"
    "v_exp_f32 v25, v25\n"
    "v_mfma_f32_16x16x32_bf16 a[100:103], v[160:163], v[88:91], a[100:103]\n"
    "buffer_store_dwordx2 v[208:209], v244, s[16:19], s76 offen offset:1024 sc1\n"
    "v_mfma_f32_16x16x32_bf16 a[100:103], v[156:159], v[72:75], a[100:103]\n"
    "v_fma_f32 v231, v231, v39, v43\n"
    "v_cvt_pk_bf16_f32 v228, v228, v229\n"
    "v_mfma_f32_16x16x32_bf16 a[104:107], v[156:159], v[76:79], a[104:107]\n"
    "buffer_store_dwordx2 v[212:213], v244, s[16:19], s76 offen offset:256 sc1\n"
    "v_mfma_f32_16x16x32_bf16 a[104:107], v[160:163], v[92:95], a[104:107]\n"
    "v_exp_f32 v26, v26\n"
    "v_mfma_f32_16x16x32_bf16 a[120:123], v[168:171], v[92:95], a[120:123]\n"
    "buffer_store_dwordx2 v[216:217], v244, s[16:19], s76 offen offset:1280 sc1\n"
    "v_mfma_f32_16x16x32_bf16 a[120:123], v[164:167], v[76:79], a[120:123]\n"
    "v_cvt_pk_bf16_f32 v229, v230, v231\n"
    "v_fma_f32 v232, v232, v36, v40\n"
    "v_mfma_f32_16x16x32_bf16 a[136:139], v[172:175], v[76:79], a[136:139]\n"
    "buffer_store_dwordx2 v[220:221], v244, s[16:19], s77 offen offset:0 sc1\n"
    "v_mfma_f32_16x16x32_bf16 a[136:139], v[176:179], v[92:95], a[136:139]\n"
    "v_exp_f32 v27, v27\n"
    "v_mfma_f32_16x16x32_bf16 a[140:143], v[176:179], v[96:99], a[140:143]\n"
    "buffer_store_dwordx2 v[224:225], v244, s[16:19], s77 offen offset:1024 sc1\n"
    "v_mfma_f32_16x16x32_bf16 a[140:143], v[172:175], v[80:83], a[140:143]\n"
    "v_fma_f32 v233, v233, v37, v41\n"
    "v_fma_f32 v234, v234, v38, v42\n"
    "v_mfma_f32_16x16x32_bf16 a[124:127], v[164:167], v[80:83], a[124:127]\n"
    "buffer_store_dwordx2 v[228:229], v244, s[16:19], s77 offen offset:256 sc1\n"
    "v_mfma_f32_16x16x32_bf16 a[124:127], v[168:171], v[96:99], a[124:127]\n"
    "v_exp_f32 v28, v28\n"
    "v_mfma_f32_16x16x32_bf16 a[108:111], v[160:163], v[96:99], a[108:111]\n"
    "v_fma_f32 v235, v235, v39, v43\n"
    "v_cvt_pk_bf16_f32 v232, v232, v233\n"
    "v_mfma_f32_16x16x32_bf16 a[108:111], v[156:159], v[80:83], a[108:111]\n"
    "s_waitcnt lgkmcnt(0)\n"
    "v_cvt_pk_bf16_f32 v233, v234, v235\n"
    "v_mfma_f32_16x16x32_bf16 a[144:147], v[180:183], v[68:71], a[144:147]\n"
    "ds_read_b128 v[156:159], v3 offset:12288\n"
    "v_mfma_f32_16x16x32_bf16 a[144:147], v[184:187], v[84:87], a[144:147]\n"
    "ds_read_b128 v[160:163], v3 offset:14336\n"
    "v_mfma_f32_16x16x32_bf16 a[160:163], v[192:195], v[84:87], a[160:163]\n"
    "ds_read_b128 v[164:167], v3 offset:16384\n"
    "v_mfma_f32_16x16x32_bf16 a[160:163], v[188:191], v[68:71], a[160:163]\n"
    "ds_read_b128 v[168:171], v3 offset:18432\n"
    "v_mfma_f32_16x16x32_bf16 a[176:179], v[196:199], v[68:71], a[176:179]\n"
    "ds_read_b128 v[172:175], v3 offset:20480\n"
    "v_mfma_f32_16x16x32_bf16 a[176:179], v[200:203], v[84:87], a[176:179]\n"
    "ds_read_b128 v[176:179], v3 offset:22528\n"
    "v_mfma_f32_16x16x32_bf16 a[180:183], v[200:203], v[88:91], a[180:183]\n"
    "ds_read_b128 v[100:103], v250 offset:1664\n"
    "v_mfma_f32_16x16x32_bf16 a[180:183], v[196:199], v[72:75], a[180:183]\n"
    "buffer_store_dwordx2 v[232:233], v244, s[16:19], s77 offen offset:1280 sc1\n"
    "v_mfma_f32_16x16x32_bf16 a[164:167], v[188:191], v[72:75], a[164:167]\n"
    "v_exp_f32 v29, v29\n"
    "v_mfma_f32_16x16x32_bf16 a[164:167], v[192:195], v[88:91], a[164:167]\n"
    "v_exp_f32 v30, v30\n"
    "v_mfma_f32_16x16x32_bf16 a[148:151], v[184:187], v[88:91], a[148:151]\n"
    "v_exp_f32 v31, v31\n"
    "v_mfma_f32_16x16x32_bf16 a[148:151], v[180:183], v[72:75], a[148:151]\n"
    "v_exp_f32 v32, v32\n"
    "v_mfma_f32_16x16x32_bf16 a[152:155], v[180:183], v[76:79], a[152:155]\n"
    "v_exp_f32 v33, v33\n"
    "v_mfma_f32_16x16x32_bf16 a[152:155], v[184:187], v[92:95], a[152:155]\n"
    "v_exp_f32 v34, v34\n"
    "v_mfma_f32_16x16x32_bf16 a[168:171], v[192:195], v[92:95], a[168:171]\n"
    "s_waitcnt vmcnt(9)\n"
    "ds_add_u32 v247, v246\n"
    "v_mfma_f32_16x16x32_bf16 a[168:171], v[188:191], v[76:79], a[168:171]\n"
    "v_exp_f32 v35, v35\n"
    "v_mfma_f32_16x16x32_bf16 a[184:187], v[196:199], v[76:79], a[184:187]\n"
    "v_mfma_f32_16x16x32_bf16 a[184:187], v[200:203], v[92:95], a[184:187]\n"
    "v_mfma_f32_16x16x32_bf16 a[188:191], v[200:203], v[96:99], a[188:191]\n"
    "v_mfma_f32_16x16x32_bf16 a[188:191], v[196:199], v[80:83], a[188:191]\n"
    "v_mfma_f32_16x16x32_bf16 a[172:175], v[188:191], v[80:83], a[172:175]\n"
    "s_sub_u32 s74, s68, s65\n"
    "v_add_u32 v250, 1536, v250\n"
    "v_add_u32 v251, 1536, v251\n"
    "v_mfma_f32_16x16x32_bf16 a[172:175], v[192:195], v[96:99], a[172:175]\n"
    "s_add_u32 s66, s66, 1\n"
    "v_mov_b32 v2, v3\n"
    "v_add_u32 v3, s74, v252\n"
    "v_mfma_f32_16x16x32_bf16 a[156:159], v[184:187], v[96:99], a[156:159]\n"
    "s_waitcnt lgkmcnt(1)\n"
    "s_cmp_lt_u32 s66, 31\n"
    "v_mfma_f32_16x16x32_bf16 a[156:159], v[180:183], v[80:83], a[156:159]\n"
    "s_cbranch_scc1 .LPK\n"
    "s_branch .Ltail\n"
    ".LPV:\n"
    "v_mfma_f32_16x16x32_bf16 v[36:39], v[156:159], a[192:195], v[100:103]\n"
    "s_add_u32 s71, s71, 98304\n"
    "ds_read_b128 v[180:183], v2 offset:13312\n"
    "v_cvt_pk_bf16_f32 v68, v4, v5\n"
    "v_mfma_f32_16x16x32_bf16 v[36:39], v[160:163], a[196:199], v[36:39]\n"
    "s_add_u32 s75, s71, s63\n"
    "ds_read_b128 v[184:187], v2 offset:15360\n"
    "v_mfma_f32_16x16x32_bf16 v[36:39], v[164:167], a[200:203], v[36:39]\n"
    "ds_read_b128 v[188:191], v2 offset:17408\n"
    "v_mfma_f32_16x16x32_bf16 v[36:39], v[168:171], a[204:207], v[36:39]\n"
    "ds_read_b128 v[192:195], v2 offset:19456\n"
    "v_cvt_pk_bf16_f32 v69, v6, v7\n"
    "v_mfma_f32_16x16x32_bf16 v[36:39], v[172:175], a[208:211], v[36:39]\n"
    "ds_read_b128 v[196:199], v2 offset:21504\n"
    "v_mfma_f32_16x16x32_bf16 v[36:39], v[176:179], a[212:215], v[36:39]\n"
    "ds_read_b128 v[200:203], v2 offset:23552\n"
    "v_mfma_f32_16x16x32_bf16 v[40:43], v[176:179], a[236:239], v[100:103]\n"
    "ds_read_b128 v[104:107], v250 offset:144\n"
    "v_mfma_f32_16x16x32_bf16 v[40:43], v[172:175], a[232:235], v[40:43]\n"
    "s_add_u32 s68, s68, 24576\n"
    "v_cvt_pk_bf16_f32 v72, v8, v9\n"
    "v_mfma_f32_16x16x32_bf16 v[40:43], v[168:171], a[228:231], v[40:43]\n"
    "s_cmp_ge_u32 s68, s69\n"
    "s_cselect_b32 s68, s65, s68\n"
    "v_mfma_f32_16x16x32_bf16 v[40:43], v[164:167], a[224:227], v[40:43]\n"
    "v_exp_f32 v36, v36\n"
    "v_mfma_f32_16x16x32_bf16 v[40:43], v[160:163], a[220:223], v[40:43]\n"
    "v_exp_f32 v37, v37\n"
    "v_mfma_f32_16x16x32_bf16 v[40:43], v[156:159], a[216:219], v[40:43]\n"
    "v_cvt_pk_bf16_f32 v73, v10, v11\n"
    "v_cvt_pk_bf16_f32 v84, v36, v37\n"
    "v_mfma_f32_16x16x32_bf16 v[44:47], v[156:159], a[240:243], v[100:103]\n"
    "v_mfma_f32_16x16x32_bf16 v[44:47], v[160:163], a[244:247], v[44:47]\n"
    "v_exp_f32 v38, v38\n"
    "v_mfma_f32_16x16x32_bf16 v[44:47], v[164:167], a[248:251], v[44:47]\n"
    "v_mfma_f32_16x16x32_bf16 v[44:47], v[168:171], a[252:255], v[44:47]\n"
    "v_cvt_pk_bf16_f32 v76, v12, v13\n"
    "v_mfma_f32_16x16x32_bf16 v[44:47], v[172:175], v[124:127], v[44:47]\n"
    "v_exp_f32 v39, v39\n"
    "v_mfma_f32_16x16x32_bf16 v[44:47], v[176:179], v[128:131], v[44:47]\n"
    "v_cvt_pk_bf16_f32 v85, v38, v39\n"
    "v_mfma_f32_16x16x32_bf16 v[48:51], v[176:179], v[152:155], v[100:103]\n"
    "ds_read_b32 v245, v247\n"
    "v_cvt_pk_bf16_f32 v77, v14, v15\n"
    "v_mfma_f32_16x16x32_bf16 v[48:51], v[172:175], v[148:151], v[48:51]\n"
    "s_add_u32 m0, s68, 0\n"
    "v_exp_f32 v40, v40\n"
    "v_mfma_f32_16x16x32_bf16 v[48:51], v[168:171], v[144:147], v[48:51]\n"
    "v_mfma_f32_16x16x32_bf16 v[48:51], v[164:167], v[140:143], v[48:51]\n"
    "v_mfma_f32_16x16x32_bf16 v[48:51], v[160:163], v[136:139], v[48:51]\n"
    "s_waitcnt vmcnt(3)\n"
    "v_cmp_eq_u64 s[54:55], v[236:237], v[238:239]\n"
    "v_cvt_pk_bf16_f32 v80, v16, v17\n"
    "v_mfma_f32_16x16x32_bf16 v[48:51], v[156:159], v[132:135], v[48:51]\n"
    "s_waitcnt lgkmcnt(1)\n"
    "v_exp_f32 v41, v41\n"
    "v_mfma_f32_16x16x32_bf16 v[52:55], v[180:183], a[192:195], v[104:107]\n"
    "s_waitcnt lgkmcnt(0)\n"
    "v_cmp_le_u32 s[56:57], s59, v245\n"
    "v_cvt_pk_bf16_f32 v88, v40, v41\n"
    "v_mfma_f32_16x16x32_bf16 v[52:55], v[184:187], a[196:199], v[52:55]\n"
    "v_exp_f32 v42, v42\n"
    "v_mfma_f32_16x16x32_bf16 v[52:55], v[188:191], a[200:203], v[52:55]\n"
    "s_and_b64 s[54:55], s[54:55], s[56:57]\n"
    "s_cmp_eq_u64 s[54:55], -1\n"
    "v_mfma_f32_16x16x32_bf16 v[52:55], v[192:195], a[204:207], v[52:55]\n"
    "s_cbranch_scc0 .Lpoll3\n"
    ".Lpoll3r:\n"
    "v_mfma_f32_16x16x32_bf16 v[52:55], v[196:199], a[208:211], v[52:55]\n"
    "buffer_load_dwordx4 v1, s[16:19], s67 offen offset:0 sc1 lds\n"
    "v_mfma_f32_16x16x32_bf16 v[52:55], v[200:203], a[212:215], v[52:55]\n"
    "ds_read_b128 v[156:159], v3 offset:0\n"
    "ds_read_b128 v[160:163], v3 offset:2048\n"
    "v_mfma_f32_16x16x32_bf16 v[56:59], v[200:203], a[236:239], v[104:107]\n"
    "ds_read_b128 v[164:167], v3 offset:4096\n"
    "ds_read_b128 v[168:171], v3 offset:6144\n"
    "v_mfma_f32_16x16x32_bf16 v[56:59], v[196:199], a[232:235], v[56:59]\n"
    "ds_read_b128 v[172:175], v3 offset:8192\n"
    "ds_read_b128 v[176:179], v3 offset:10240\n"
    "v_mfma_f32_16x16x32_bf16 v[56:59], v[192:195], a[228:231], v[56:59]\n"
    "buffer_load_dwordx4 v1, s[16:19], s67 offen offset:1024 sc1 lds\n"
    "v_mfma_f32_16x16x32_bf16 v[56:59], v[188:191], a[224:227], v[56:59]\n"
    "ds_read_b128 v[100:103], v250 offset:768\n"
    "v_cvt_pk_bf16_f32 v81, v18, v19\n"
    "v_mfma_f32_16x16x32_bf16 v[56:59], v[184:187], a[220:223], v[56:59]\n"
    "v_cvt_pk_bf16_f32 v70, v20, v21\n"
    "v_cvt_pk_bf16_f32 v71, v22, v23\n"
    "v_mfma_f32_16x16x32_bf16 v[56:59], v[180:183], a[216:219], v[56:59]\n"
    "v_exp_f32 v43, v43\n"
    "v_mfma_f32_16x16x32_bf16 v[60:63], v[180:183], a[240:243], v[104:107]\n"
    "buffer_load_dwordx4 v1, s[16:19], s67 offen offset:2048 sc1 lds\n"
    "v_mfma_f32_16x16x32_bf16 v[60:63], v[184:187], a[244:247], v[60:63]\n"
    "v_cvt_pk_bf16_f32 v74, v24, v25\n"
    "v_cvt_pk_bf16_f32 v89, v42, v43\n"
    "v_mfma_f32_16x16x32_bf16 v[60:63], v[188:191], a[248:251], v[60:63]\n"
    "v_exp_f32 v44, v44\n"
    "v_mfma_f32_16x16x32_bf16 v[60:63], v[192:195], a[252:255], v[60:63]\n"
    "v_exp_f32 v45, v45\n"
    "v_mfma_f32_16x16x32_bf16 v[60:63], v[196:199], v[124:127], v[60:63]\n"
    "buffer_load_dwordx4 v1, s[16:19], s67 offen offset:3072 sc1 lds\n"
    "v_mfma_f32_16x16x32_bf16 v[60:63], v[200:203], v[128:131], v[60:63]\n"
    "s_add_u32 m0, s68, 4096\n"
    "v_cvt_pk_bf16_f32 v75, v26, v27\n"
    "v_cvt_pk_bf16_f32 v92, v44, v45\n"
    "v_mfma_f32_16x16x32_bf16 v[64:67], v[200:203], v[152:155], v[104:107]\n"
    "s_add_u32 s83, s67, 4096\n"
    "v_exp_f32 v46, v46\n"
    "v_mfma_f32_16x16x32_bf16 v[64:67], v[196:199], v[148:151], v[64:67]\n"
    "v_exp_f32 v47, v47\n"
    "v_mfma_f32_16x16x32_bf16 v[64:67], v[192:195], v[144:147], v[64:67]\n"
    "buffer_load_dwordx4 v1, s[16:19], s83 offen offset:0 sc1 lds\n"
    "v_mfma_f32_16x16x32_bf16 v[64:67], v[188:191], v[140:143], v[64:67]\n"
    "v_cvt_pk_bf16_f32 v78, v28, v29\n"
    "v_cvt_pk_bf16_f32 v93, v46, v47\n"
    "v_mfma_f32_16x16x32_bf16 v[64:67], v[184:187], v[136:139], v[64:67]\n"
    "v_exp_f32 v48, v48\n"
    "v_mfma_f32_16x16x32_bf16 v[64:67], v[180:183], v[132:135], v[64:67]\n"
    "s_waitcnt lgkmcnt(0)\n"
    "v_exp_f32 v49, v49\n"
    "v_mfma_f32_16x16x32_bf16 v[4:7], v[156:159], a[192:195], v[100:103]\n"
    "buffer_load_dwordx4 v1, s[16:19], s83 offen offset:1024 sc1 lds\n"
    "v_mfma_f32_16x16x32_bf16 v[4:7], v[160:163], a[196:199], v[4:7]\n"
    "s_add_u32 m0, s64, 0\n"
    "ds_read_b128 v[180:183], v3 offset:1024\n"
    "ds_read_b128 v[184:187], v3 offset:3072\n"
    "v_mfma_f32_16x16x32_bf16 v[4:7], v[164:167], a[200:203], v[4:7]\n"
    "s_add_u32 s83, s67, 24576\n"
    "ds_read_b128 v[188:191], v3 offset:5120\n"
    "v_cvt_pk_bf16_f32 v79, v30, v31\n"
    "v_mfma_f32_16x16x32_bf16 v[4:7], v[168:171], a[204:207], v[4:7]\n"
    "ds_read_b128 v[192:195], v3 offset:7168\n"
    "v_cvt_pk_bf16_f32 v96, v48, v49\n"
    "v_mfma_f32_16x16x32_bf16 v[4:7], v[172:175], a[208:211], v[4:7]\n"
    "buffer_load_dwordx4 v1, s[16:19], s83 offen offset:0 sc1 lds\n"
    "v_mfma_f32_16x16x32_bf16 v[4:7], v[176:179], a[212:215], v[4:7]\n"
    "ds_read_b128 v[196:199], v3 offset:9216\n"
    "ds_read_b128 v[200:203], v3 offset:11264\n"
    "v_mfma_f32_16x16x32_bf16 v[8:11], v[176:179], a[236:239], v[100:103]\n"
    "ds_read_b128 v[104:107], v250 offset:784\n"
    "v_cvt_pk_bf16_f32 v82, v32, v33\n"
    "v_mfma_f32_16x16x32_bf16 v[8:11], v[172:175], a[232:235], v[8:11]\n"
    "v_exp_f32 v52, v52\n"
    "v_mfma_f32_16x16x32_bf16 v[8:11], v[168:171], a[228:231], v[8:11]\n"
    "buffer_load_dwordx4 v1, s[16:19], s83 offen offset:1024 sc1 lds\n"
    "v_mfma_f32_16x16x32_bf16 v[8:11], v[164:167], a[224:227], v[8:11]\n"
    "v_cvt_pk_bf16_f32 v83, v34, v35\n"
    "v_mfma_f32_16x16x32_bf16 v[8:11], v[160:163], a[220:223], v[8:11]\n"
    "v_exp_f32 v53, v53\n"
    "v_mfma_f32_16x16x32_bf16 v[8:11], v[156:159], a[216:219], v[8:11]\n"
    "v_exp_f32 v54, v54\n"
    "v_mfma_f32_16x16x32_bf16 v[12:15], v[156:159], a[240:243], v[100:103]\n"
    "buffer_load_dwordx4 v1, s[16:19], s83 offen offset:2048 sc1 lds\n"
    "v_mfma_f32_16x16x32_bf16 v[12:15], v[160:163], a[244:247], v[12:15]\n"
    "v_cvt_pk_bf16_f32 v86, v52, v53\n"
    "v_mfma_f32_16x16x32_bf16 v[12:15], v[164:167], a[248:251], v[12:15]\n"
    "v_exp_f32 v55, v55\n"
    "v_mfma_f32_16x16x32_bf16 v[12:15], v[168:171], a[252:255], v[12:15]\n"
    "v_cvt_pk_bf16_f32 v87, v54, v55\n"
    "v_mfma_f32_16x16x32_bf16 v[12:15], v[172:175], v[124:127], v[12:15]\n"
    "buffer_load_dwordx4 v1, s[16:19], s83 offen offset:3072 sc1 lds\n"
    "v_mfma_f32_16x16x32_bf16 v[12:15], v[176:179], v[128:131], v[12:15]\n"
    "s_add_u32 m0, s64, 4096\n"
    "v_exp_f32 v56, v56\n"
    "v_mfma_f32_16x16x32_bf16 v[16:19], v[176:179], v[152:155], v[100:103]\n"
    "s_add_u32 s83, s67, 28672\n"
    "v_exp_f32 v50, v50\n"
    "v_mfma_f32_16x16x32_bf16 v[16:19], v[172:175], v[148:151], v[16:19]\n"
    "v_exp_f32 v51, v51\n"
    "v_mfma_f32_16x16x32_bf16 v[16:19], v[168:171], v[144:147], v[16:19]\n"
    "buffer_load_dwordx4 v1, s[16:19], s83 offen offset:0 sc1 lds\n"
    "v_mfma_f32_16x16x32_bf16 v[16:19], v[164:167], v[140:143], v[16:19]\n"
    "v_exp_f32 v57, v57\n"
    "v_mfma_f32_16x16x32_bf16 v[16:19], v[160:163], v[136:139], v[16:19]\n"
    "v_cvt_pk_bf16_f32 v90, v56, v57\n"
    "v_cvt_pk_bf16_f32 v97, v50, v51\n"
    "v_mfma_f32_16x16x32_bf16 v[16:19], v[156:159], v[132:135], v[16:19]\n"
    "s_waitcnt lgkmcnt(0)\n"
    "v_exp_f32 v58, v58\n"
    "v_mfma_f32_16x16x32_bf16 v[20:23], v[180:183], a[192:195], v[104:107]\n"
    "buffer_load_dwordx4 v1, s[16:19], s83 offen offset:1024 sc1 lds\n"
    "v_mfma_f32_16x16x32_bf16 v[20:23], v[184:187], a[196:199], v[20:23]\n"
    "s_add_u32 s67, s67, 49152\n"
    "ds_read_b128 v[156:159], v248 offset:0\n"
    "ds_read_b128 v[160:163], v248 offset:12288\n"
    "v_mfma_f32_16x16x32_bf16 v[20:23], v[188:191], a[200:203], v[20:23]\n"
    "s_add_u32 s59, s59, 4\n"
    "ds_read_b128 v[164:167], v248 offset:1024\n"
    "v_mfma_f32_16x16x32_bf16 v[20:23], v[192:195], a[204:207], v[20:23]\n"
    "s_add_u32 s81, s81, 128\n"
    "s_waitcnt vmcnt(12)\n"
    "ds_read_b128 v[168:171], v248 offset:13312\n"
    "v_mfma_f32_16x16x32_bf16 v[20:23], v[196:199], a[208:211], v[20:23]\n"
    "s_min_u32 s81, s81, 4096\n"
    "ds_read_b128 v[172:175], v248 offset:2048\n"
    "v_mfma_f32_16x16x32_bf16 v[20:23], v[200:203], a[212:215], v[20:23]\n"
    "buffer_store_dwordx2 v[238:239], v240, s[36:39], s80 offen offset:0 sc1\n"
    "v_mfma_f32_16x16x32_bf16 v[24:27], v[200:203], a[236:239], v[104:107]\n"
    "s_add_u32 s80, s80, 256\n"
    "ds_read_b128 v[176:179], v248 offset:14336\n"
    "ds_read_b128 v[100:103], v251 offset:0\n"
    "v_mfma_f32_16x16x32_bf16 v[24:27], v[196:199], a[232:235], v[24:27]\n"
    "v_exp_f32 v59, v59\n"
    "v_mfma_f32_16x16x32_bf16 v[24:27], v[192:195], a[228:231], v[24:27]\n"
    "v_cvt_pk_bf16_f32 v91, v58, v59\n"
    "v_mfma_f32_16x16x32_bf16 v[24:27], v[188:191], a[224:227], v[24:27]\n"
    "v_exp_f32 v60, v60\n"
    "v_mfma_f32_16x16x32_bf16 v[24:27], v[184:187], a[220:223], v[24:27]\n"
    "v_exp_f32 v61, v61\n"
    "v_mfma_f32_16x16x32_bf16 v[24:27], v[180:183], a[216:219], v[24:27]\n"
    "v_exp_f32 v62, v62\n"
    "v_mfma_f32_16x16x32_bf16 v[28:31], v[180:183], a[240:243], v[104:107]\n"
    "v_cvt_pk_bf16_f32 v94, v60, v61\n"
    "v_mfma_f32_16x16x32_bf16 v[28:31], v[184:187], a[244:247], v[28:31]\n"
    "v_exp_f32 v63, v63\n"
    "v_mfma_f32_16x16x32_bf16 v[28:31], v[188:191], a[248:251], v[28:31]\n"
    "v_cvt_pk_bf16_f32 v95, v62, v63\n"
    "v_mfma_f32_16x16x32_bf16 v[28:31], v[192:195], a[252:255], v[28:31]\n"
    "buffer_load_dwordx3 v[204:206], v243, s[40:43], s75 offen offset:1536 nt\n"
    "v_mfma_f32_16x16x32_bf16 v[28:31], v[196:199], v[124:127], v[28:31]\n"
    "v_exp_f32 v64, v64\n"
    "v_mfma_f32_16x16x32_bf16 v[28:31], v[200:203], v[128:131], v[28:31]\n"
    "v_mfma_f32_16x16x32_bf16 v[32:35], v[200:203], v[152:155], v[104:107]\n"
    "buffer_load_dwordx3 v[208:210], v243, s[40:43], s75 offen offset:2304 nt\n"
    "v_mfma_f32_16x16x32_bf16 v[32:35], v[196:199], v[148:151], v[32:35]\n"
    "v_exp_f32 v65, v65\n"
    "v_mfma_f32_16x16x32_bf16 v[32:35], v[192:195], v[144:147], v[32:35]\n"
    "v_cvt_pk_bf16_f32 v98, v64, v65\n"
    "v_mfma_f32_16x16x32_bf16 v[32:35], v[188:191], v[140:143], v[32:35]\n"
    "buffer_load_dwordx3 v[212:214], v243, s[40:43], s75 offen offset:0 nt\n"
    "v_mfma_f32_16x16x32_bf16 v[32:35], v[184:187], v[136:139], v[32:35]\n"
    "v_exp_f32 v66, v66\n"
    "v_mfma_f32_16x16x32_bf16 v[32:35], v[180:183], v[132:135], v[32:35]\n"
    "s_waitcnt lgkmcnt(0)\n"
    "v_mfma_f32_16x16x32_bf16 a[0:3], v[156:159], v[68:71], a[0:3]\n"
    "ds_read_b128 v[180:183], v248 offset:3072\n"
    "v_mfma_f32_16x16x32_bf16 a[0:3], v[160:163], v[84:87], a[0:3]\n"
    "ds_read_b128 v[184:187], v248 offset:15360\n"
    "v_mfma_f32_16x16x32_bf16 a[16:19], v[168:171], v[84:87], a[16:19]\n"
    "ds_read_b128 v[188:191], v248 offset:4096\n"
    "v_mfma_f32_16x16x32_bf16 a[16:19], v[164:167], v[68:71], a[16:19]\n"
    "ds_read_b128 v[192:195], v248 offset:16384\n"
    "v_mfma_f32_16x16x32_bf16 a[32:35], v[172:175], v[68:71], a[32:35]\n"
    "ds_read_b128 v[196:199], v248 offset:5120\n"
    "v_mfma_f32_16x16x32_bf16 a[32:35], v[176:179], v[84:87], a[32:35]\n"
    "ds_read_b128 v[200:203], v248 offset:17408\n"
    "v_mfma_f32_16x16x32_bf16 a[36:39], v[176:179], v[88:91], a[36:39]\n"
    "ds_read_b128 v[104:107], v251 offset:64\n"
    "v_mfma_f32_16x16x32_bf16 a[36:39], v[172:175], v[72:75], a[36:39]\n"
    "v_exp_f32 v67, v67\n"
    "v_mfma_f32_16x16x32_bf16 a[20:23], v[164:167], v[72:75], a[20:23]\n"
    "v_cvt_pk_bf16_f32 v99, v66, v67\n"
    "v_mfma_f32_16x16x32_bf16 a[20:23], v[168:171], v[88:91], a[20:23]\n"
    "buffer_load_dwordx3 v[216:218], v243, s[40:43], s75 offen offset:768 nt\n"
    "v_mfma_f32_16x16x32_bf16 a[4:7], v[160:163], v[88:91], a[4:7]\n"
    "v_exp_f32 v4, v4\n"
    "v_mfma_f32_16x16x32_bf16 a[4:7], v[156:159], v[72:75], a[4:7]\n"
    "v_exp_f32 v5, v5\n"
    "v_mfma_f32_16x16x32_bf16 a[8:11], v[156:159], v[76:79], a[8:11]\n"
    "buffer_load_dwordx3 v[220:222], v242, s[40:43], s75 offen offset:1536 nt\n"
    "v_mfma_f32_16x16x32_bf16 a[8:11], v[160:163], v[92:95], a[8:11]\n"
    "v_exp_f32 v6, v6\n"
    "v_mfma_f32_16x16x32_bf16 a[24:27], v[168:171], v[92:95], a[24:27]\n"
    "v_exp_f32 v7, v7\n"
    "v_mfma_f32_16x16x32_bf16 a[24:27], v[164:167], v[76:79], a[24:27]\n"
    "buffer_load_dwordx3 v[224:226], v242, s[40:43], s75 offen offset:2304 nt\n"
    "v_mfma_f32_16x16x32_bf16 a[40:43], v[172:175], v[76:79], a[40:43]\n"
    "v_exp_f32 v8, v8\n"
    "v_mfma_f32_16x16x32_bf16 a[40:43], v[176:179], v[92:95], a[40:43]\n"
    "v_mfma_f32_16x16x32_bf16 a[44:47], v[176:179], v[96:99], a[44:47]\n"
    "buffer_load_dwordx3 v[228:230], v242, s[40:43], s75 offen offset:0 nt\n"
    "v_mfma_f32_16x16x32_bf16 a[44:47], v[172:175], v[80:83], a[44:47]\n"
    "v_exp_f32 v9, v9\n"
    "v_mfma_f32_16x16x32_bf16 a[28:31], v[164:167], v[80:83], a[28:31]\n"
    "v_exp_f32 v10, v10\n"
    "v_mfma_f32_16x16x32_bf16 a[28:31], v[168:171], v[96:99], a[28:31]\n"
    "buffer_load_dwordx3 v[232:234], v242, s[40:43], s75 offen offset:768 nt\n"
    "v_mfma_f32_16x16x32_bf16 a[12:15], v[160:163], v[96:99], a[12:15]\n"
    "v_mfma_f32_16x16x32_bf16 a[12:15], v[156:159], v[80:83], a[12:15]\n"
    "s_waitcnt lgkmcnt(0)\n"
    "v_exp_f32 v11, v11\n"
    "v_mfma_f32_16x16x32_bf16 a[48:51], v[180:183], v[68:71], a[48:51]\n"
    "ds_read_b128 v[156:159], v248 offset:6144\n"
    "v_mfma_f32_16x16x32_bf16 a[48:51], v[184:187], v[84:87], a[48:51]\n"
    "ds_read_b128 v[160:163], v248 offset:18432\n"
    "v_mfma_f32_16x16x32_bf16 a[64:67], v[192:195], v[84:87], a[64:67]\n"
    "ds_read_b128 v[164:167], v248 offset:7168\n"
    "v_mfma_f32_16x16x32_bf16 a[64:67], v[188:191], v[68:71], a[64:67]\n"
    "ds_read_b128 v[168:171], v248 offset:19456\n"
    "v_mfma_f32_16x16x32_bf16 a[80:83], v[196:199], v[68:71], a[80:83]\n"
    "ds_read_b128 v[172:175], v248 offset:8192\n"
    "v_mfma_f32_16x16x32_bf16 a[80:83], v[200:203], v[84:87], a[80:83]\n"
    "ds_read_b128 v[176:179], v248 offset:20480\n"
    "v_mfma_f32_16x16x32_bf16 v[108:111], v[104:107], v[84:87], v[108:111]\n"
    "v_exp_f32 v12, v12\n"
    "v_mfma_f32_16x16x32_bf16 v[108:111], v[100:103], v[68:71], v[108:111]\n"
    "v_exp_f32 v13, v13\n"
    "v_mfma_f32_16x16x32_bf16 v[112:115], v[100:103], v[72:75], v[112:115]\n"
    "v_exp_f32 v14, v14\n"
    "v_mfma_f32_16x16x32_bf16 v[112:115], v[104:107], v[88:91], v[112:115]\n"
    "v_mfma_f32_16x16x32_bf16 a[84:87], v[200:203], v[88:91], a[84:87]\n"
    "v_exp_f32 v15, v15\n"
    "v_mfma_f32_16x16x32_bf16 a[84:87], v[196:199], v[72:75], a[84:87]\n"
    "v_mfma_f32_16x16x32_bf16 a[68:71], v[188:191], v[72:75], a[68:71]\n"
    "v_mfma_f32_16x16x32_bf16 a[68:71], v[192:195], v[88:91], a[68:71]\n"
    "v_exp_f32 v16, v16\n"
    "v_mfma_f32_16x16x32_bf16 a[52:55], v[184:187], v[88:91], a[52:55]\n"
    "v_mfma_f32_16x16x32_bf16 a[52:55], v[180:183], v[72:75], a[52:55]\n"
    "v_mfma_f32_16x16x32_bf16 a[56:59], v[180:183], v[76:79], a[56:59]\n"
    "v_exp_f32 v17, v17\n"
    "v_mfma_f32_16x16x32_bf16 a[56:59], v[184:187], v[92:95], a[56:59]\n"
    "v_mfma_f32_16x16x32_bf16 a[72:75], v[192:195], v[92:95], a[72:75]\n"
    "v_mfma_f32_16x16x32_bf16 a[72:75], v[188:191], v[76:79], a[72:75]\n"
    "v_exp_f32 v18, v18\n"
    "v_mfma_f32_16x16x32_bf16 a[88:91], v[196:199], v[76:79], a[88:91]\n"
    "v_mfma_f32_16x16x32_bf16 a[88:91], v[200:203], v[92:95], a[88:91]\n"
    "v_mfma_f32_16x16x32_bf16 v[116:119], v[104:107], v[92:95], v[116:119]\n"
    "v_exp_f32 v19, v19\n"
    "v_mfma_f32_16x16x32_bf16 v[116:119], v[100:103], v[76:79], v[116:119]\n"
    "v_mfma_f32_16x16x32_bf16 v[120:123], v[100:103], v[80:83], v[120:123]\n"
    "v_mfma_f32_16x16x32_bf16 v[120:123], v[104:107], v[96:99], v[120:123]\n"
    "v_exp_f32 v20, v20\n"
    "v_mfma_f32_16x16x32_bf16 a[92:95], v[200:203], v[96:99], a[92:95]\n"
    "v_mfma_f32_16x16x32_bf16 a[92:95], v[196:199], v[80:83], a[92:95]\n"
    "v_exp_f32 v21, v21\n"
    "v_mfma_f32_16x16x32_bf16 a[76:79], v[188:191], v[80:83], a[76:79]\n"
    "v_mfma_f32_16x16x32_bf16 a[76:79], v[192:195], v[96:99], a[76:79]\n"
    "v_mfma_f32_16x16x32_bf16 a[60:63], v[184:187], v[96:99], a[60:63]\n"
    "v_exp_f32 v22, v22\n"
    "v_mfma_f32_16x16x32_bf16 a[60:63], v[180:183], v[80:83], a[60:63]\n"
    "s_waitcnt lgkmcnt(0)\n"
    "v_mfma_f32_16x16x32_bf16 a[96:99], v[156:159], v[68:71], a[96:99]\n"
    "ds_read_b128 v[180:183], v248 offset:9216\n"
    "v_mfma_f32_16x16x32_bf16 a[96:99], v[160:163], v[84:87], a[96:99]\n"
    "ds_read_b128 v[184:187], v248 offset:21504\n"
    "v_mfma_f32_16x16x32_bf16 a[112:115], v[168:171], v[84:87], a[112:115]\n"
    "ds_read_b128 v[188:191], v248 offset:10240\n"
    "v_mfma_f32_16x16x32_bf16 a[112:115], v[164:167], v[68:71], a[112:115]\n"
    "ds_read_b128 v[192:195], v248 offset:22528\n"
    "v_mfma_f32_16x16x32_bf16 a[128:131], v[172:175], v[68:71], a[128:131]\n"
    "buffer_load_dwordx2 v[236:237], v241, s[36:39], s81 offen offset:0 sc1\n"
    "v_mfma_f32_16x16x32_bf16 a[128:131], v[176:179], v[84:87], a[128:131]\n"
    "ds_read_b128 v[196:199], v248 offset:11264\n"
    "ds_read_b128 v[200:203], v248 offset:23552\n"
    "v_mfma_f32_16x16x32_bf16 a[132:135], v[176:179], v[88:91], a[132:135]\n"
    "v_exp_f32 v23, v23\n"
    "v_mfma_f32_16x16x32_bf16 a[132:135], v[172:175], v[72:75], a[132:135]\n"
    "v_exp_f32 v24, v24\n"
    "v_mfma_f32_16x16x32_bf16 a[116:119], v[164:167], v[72:75], a[116:119]\n"
    "v_exp_f32 v25, v25\n"
    "v_mfma_f32_16x16x32_bf16 a[116:119], v[168:171], v[88:91], a[116:119]\n"
    "v_exp_f32 v26, v26\n"
    "v_mfma_f32_16x16x32_bf16 a[100:103], v[160:163], v[88:91], a[100:103]\n"
    "v_mfma_f32_16x16x32_bf16 a[100:103], v[156:159], v[72:75], a[100:103]\n"
    "v_mfma_f32_16x16x32_bf16 a[104:107], v[156:159], v[76:79], a[104:107]\n"
    "v_exp_f32 v27, v27\n"
    "v_mfma_f32_16x16x32_bf16 a[104:107], v[160:163], v[92:95], a[104:107]\n"
    "v_mfma_f32_16x16x32_bf16 a[120:123], v[168:171], v[92:95], a[120:123]\n"
    "v_mfma_f32_16x16x32_bf16 a[120:123], v[164:167], v[76:79], a[120:123]\n"
    "v_exp_f32 v28, v28\n"
    "v_mfma_f32_16x16x32_bf16 a[136:139], v[172:175], v[76:79], a[136:139]\n"
    "v_mfma_f32_16x16x32_bf16 a[136:139], v[176:179], v[92:95], a[136:139]\n"
    "v_mfma_f32_16x16x32_bf16 a[140:143], v[176:179], v[96:99], a[140:143]\n"
    "v_exp_f32 v29, v29\n"
    "v_mfma_f32_16x16x32_bf16 a[140:143], v[172:175], v[80:83], a[140:143]\n"
    "v_mfma_f32_16x16x32_bf16 a[124:127], v[164:167], v[80:83], a[124:127]\n"
    "v_mfma_f32_16x16x32_bf16 a[124:127], v[168:171], v[96:99], a[124:127]\n"
    "v_exp_f32 v30, v30\n"
    "v_mfma_f32_16x16x32_bf16 a[108:111], v[160:163], v[96:99], a[108:111]\n"
    "v_mfma_f32_16x16x32_bf16 a[108:111], v[156:159], v[80:83], a[108:111]\n"
    "s_waitcnt lgkmcnt(0)\n"
    "v_exp_f32 v31, v31\n"
    "v_mfma_f32_16x16x32_bf16 a[144:147], v[180:183], v[68:71], a[144:147]\n"
    "ds_read_b128 v[156:159], v3 offset:12288\n"
    "v_mfma_f32_16x16x32_bf16 a[144:147], v[184:187], v[84:87], a[144:147]\n"
    "ds_read_b128 v[160:163], v3 offset:14336\n"
    "v_mfma_f32_16x16x32_bf16 a[160:163], v[192:195], v[84:87], a[160:163]\n"
    "ds_read_b128 v[164:167], v3 offset:16384\n"
    "v_mfma_f32_16x16x32_bf16 a[160:163], v[188:191], v[68:71], a[160:163]\n"
    "ds_read_b128 v[168:171], v3 offset:18432\n"
    "v_mfma_f32_16x16x32_bf16 a[176:179], v[196:199], v[68:71], a[176:179]\n"
    "ds_read_b128 v[172:175], v3 offset:20480\n"
    "v_mfma_f32_16x16x32_bf16 a[176:179], v[200:203], v[84:87], a[176:179]\n"
    "ds_read_b128 v[176:179], v3 offset:22528\n"
    "v_mfma_f32_16x16x32_bf16 a[180:183], v[200:203], v[88:91], a[180:183]\n"
    "ds_read_b128 v[100:103], v250 offset:896\n"
    "v_mfma_f32_16x16x32_bf16 a[180:183], v[196:199], v[72:75], a[180:183]\n"
    "v_exp_f32 v32, v32\n"
    "v_mfma_f32_16x16x32_bf16 a[164:167], v[188:191], v[72:75], a[164:167]\n"
    "v_exp_f32 v33, v33\n"
    "v_mfma_f32_16x16x32_bf16 a[164:167], v[192:195], v[88:91], a[164:167]\n"
    "v_exp_f32 v34, v34\n"
    "v_mfma_f32_16x16x32_bf16 a[148:151], v[184:187], v[88:91], a[148:151]\n"
    "v_mfma_f32_16x16x32_bf16 a[148:151], v[180:183], v[72:75], a[148:151]\n"
    "v_exp_f32 v35, v35\n"
    "v_mfma_f32_16x16x32_bf16 a[152:155], v[180:183], v[76:79], a[152:155]\n"
    "v_mfma_f32_16x16x32_bf16 a[152:155], v[184:187], v[92:95], a[152:155]\n"
    "v_mfma_f32_16x16x32_bf16 a[168:171], v[192:195], v[92:95], a[168:171]\n"
    "s_waitcnt vmcnt(10)\n"
    "ds_add_u32 v247, v246\n"
    "v_mfma_f32_16x16x32_bf16 a[168:171], v[188:191], v[76:79], a[168:171]\n"
    "v_mfma_f32_16x16x32_bf16 a[184:187], v[196:199], v[76:79], a[184:187]\n"
    "v_mfma_f32_16x16x32_bf16 a[184:187], v[200:203], v[92:95], a[184:187]\n"
    "v_mfma_f32_16x16x32_bf16 a[188:191], v[200:203], v[96:99], a[188:191]\n"
    "v_mfma_f32_16x16x32_bf16 a[188:191], v[196:199], v[80:83], a[188:191]\n"
    "v_mfma_f32_16x16x32_bf16 a[172:175], v[188:191], v[80:83], a[172:175]\n"
    "s_sub_u32 s74, s68, s65\n"
    "v_mfma_f32_16x16x32_bf16 a[172:175], v[192:195], v[96:99], a[172:175]\n"
    "s_add_u32 s66, s66, 1\n"
    "v_mov_b32 v2, v3\n"
    "v_add_u32 v3, s74, v252\n"
    "v_mfma_f32_16x16x32_bf16 a[156:159], v[184:187], v[96:99], a[156:159]\n"
    "s_waitcnt lgkmcnt(1)\n"
    "s_cmp_eq_u32 s66, 31\n"
    "v_mfma_f32_16x16x32_bf16 a[156:159], v[180:183], v[80:83], a[156:159]\n"
    "s_cbranch_scc1 .LtailP\n"
    ".LNV:\n"
    "v_mfma_f32_16x16x32_bf16 v[36:39], v[156:159], a[192:195], v[100:103]\n"
    "ds_read_b128 v[180:183], v2 offset:13312\n"
    "v_cvt_pk_bf16_f32 v68, v4, v5\n"
    "v_mfma_f32_16x16x32_bf16 v[36:39], v[160:163], a[196:199], v[36:39]\n"
    "ds_read_b128 v[184:187], v2 offset:15360\n"
    "v_mfma_f32_16x16x32_bf16 v[36:39], v[164:167], a[200:203], v[36:39]\n"
    "ds_read_b128 v[188:191], v2 offset:17408\n"
    "v_mfma_f32_16x16x32_bf16 v[36:39], v[168:171], a[204:207], v[36:39]\n"
    "ds_read_b128 v[192:195], v2 offset:19456\n"
    "v_cvt_pk_bf16_f32 v69, v6, v7\n"
    "v_mfma_f32_16x16x32_bf16 v[36:39], v[172:175], a[208:211], v[36:39]\n"
    "ds_read_b128 v[196:199], v2 offset:21504\n"
    "v_mfma_f32_16x16x32_bf16 v[36:39], v[176:179], a[212:215], v[36:39]\n"
    "ds_read_b128 v[200:203], v2 offset:23552\n"
    "v_mfma_f32_16x16x32_bf16 v[40:43], v[176:179], a[236:239], v[100:103]\n"
    "ds_read_b128 v[104:107], v250 offset:912\n"
    "v_mfma_f32_16x16x32_bf16 v[40:43], v[172:175], a[232:235], v[40:43]\n"
    "s_add_u32 s68, s68, 24576\n"
    "v_cvt_pk_bf16_f32 v72, v8, v9\n"
    "v_mfma_f32_16x16x32_bf16 v[40:43], v[168:171], a[228:231], v[40:43]\n"
    "s_cmp_ge_u32 s68, s69\n"
    "s_cselect_b32 s68, s65, s68\n"
    "v_mfma_f32_16x16x32_bf16 v[40:43], v[164:167], a[224:227], v[40:43]\n"
    "v_exp_f32 v36, v36\n"
    "v_mfma_f32_16x16x32_bf16 v[40:43], v[160:163], a[220:223], v[40:43]\n"
    "v_exp_f32 v37, v37\n"
    "v_mfma_f32_16x16x32_bf16 v[40:43], v[156:159], a[216:219], v[40:43]\n"
    "v_cvt_pk_bf16_f32 v73, v10, v11\n"
    "v_cvt_pk_bf16_f32 v84, v36, v37\n"
    "v_mfma_f32_16x16x32_bf16 v[44:47], v[156:159], a[240:243], v[100:103]\n"
    "v_mfma_f32_16x16x32_bf16 v[44:47], v[160:163], a[244:247], v[44:47]\n"
    "v_exp_f32 v38, v38\n"
    "v_mfma_f32_16x16x32_bf16 v[44:47], v[164:167], a[248:251], v[44:47]\n"
    "v_mfma_f32_16x16x32_bf16 v[44:47], v[168:171], a[252:255], v[44:47]\n"
    "v_cvt_pk_bf16_f32 v76, v12, v13\n"
    "v_mfma_f32_16x16x32_bf16 v[44:47], v[172:175], v[124:127], v[44:47]\n"
    "v_exp_f32 v39, v39\n"
    "v_mfma_f32_16x16x32_bf16 v[44:47], v[176:179], v[128:131], v[44:47]\n"
    "v_cvt_pk_bf16_f32 v85, v38, v39\n"
    "v_mfma_f32_16x16x32_bf16 v[48:51], v[176:179], v[152:155], v[100:103]\n"
    "ds_read_b32 v245, v247\n"
    "v_cvt_pk_bf16_f32 v77, v14, v15\n"
    "v_mfma_f32_16x16x32_bf16 v[48:51], v[172:175], v[148:151], v[48:51]\n"
    "s_add_u32 m0, s68, 0\n"
    "v_exp_f32 v40, v40\n"
    "v_mfma_f32_16x16x32_bf16 v[48:51], v[168:171], v[144:147], v[48:51]\n"
    "v_mfma_f32_16x16x32_bf16 v[48:51], v[164:167], v[140:143], v[48:51]\n"
    "v_mfma_f32_16x16x32_bf16 v[48:51], v[160:163], v[136:139], v[48:51]\n"
    "s_waitcnt vmcnt(0)\n"
    "v_cmp_eq_u64 s[54:55], v[236:237], v[238:239]\n"
    "v_cvt_pk_bf16_f32 v80, v16, v17\n"
    "v_mfma_f32_16x16x32_bf16 v[48:51], v[156:159], v[132:135], v[48:51]\n"
    "s_waitcnt lgkmcnt(1)\n"
    "v_exp_f32 v41, v41\n"
    "v_mfma_f32_16x16x32_bf16 v[52:55], v[180:183], a[192:195], v[104:107]\n"
    "s_waitcnt lgkmcnt(0)\n"
    "v_cmp_le_u32 s[56:57], s59, v245\n"
    "v_cvt_pk_bf16_f32 v88, v40, v41\n"
    "v_mfma_f32_16x16x32_bf16 v[52:55], v[184:187], a[196:199], v[52:55]\n"
    "v_exp_f32 v42, v42\n"
    "v_mfma_f32_16x16x32_bf16 v[52:55], v[188:191], a[200:203], v[52:55]\n"
    "s_and_b64 s[54:55], s[54:55], s[56:57]\n"
    "s_cmp_eq_u64 s[54:55], -1\n"
    "v_mfma_f32_16x16x32_bf16 v[52:55], v[192:195], a[204:207], v[52:55]\n"
    "s_cbranch_scc0 .Lpoll4\n"
    ".Lpoll4r:\n"
    "v_mfma_f32_16x16x32_bf16 v[52:55], v[196:199], a[208:211], v[52:55]\n"
    "buffer_load_dwordx4 v1, s[16:19], s67 offen offset:0 sc1 lds\n"
    "v_mfma_f32_16x16x32_bf16 v[52:55], v[200:203], a[212:215], v[52:55]\n"
    "ds_read_b128 v[156:159], v3 offset:0\n"
    "ds_read_b128 v[160:163], v3 offset:2048\n"
    "v_mfma_f32_16x16x32_bf16 v[56:59], v[200:203], a[236:239], v[104:107]\n"
    "ds_read_b128 v[164:167], v3 offset:4096\n"
    "ds_read_b128 v[168:171], v3 offset:6144\n"
    "v_mfma_f32_16x16x32_bf16 v[56:59], v[196:199], a[232:235], v[56:59]\n"
    "ds_read_b128 v[172:175], v3 offset:8192\n"
    "ds_read_b128 v[176:179], v3 offset:10240\n"
    "v_mfma_f32_16x16x32_bf16 v[56:59], v[192:195], a[228:231], v[56:59]\n"
    "buffer_load_dwordx4 v1, s[16:19], s67 offen offset:1024 sc1 lds\n"
    "v_mfma_f32_16x16x32_bf16 v[56:59], v[188:191], a[224:227], v[56:59]\n"
    "ds_read_b128 v[100:103], v250 offset:1536\n"
    "v_cvt_pk_bf16_f32 v81, v18, v19\n"
    "v_mfma_f32_16x16x32_bf16 v[56:59], v[184:187], a[220:223], v[56:59]\n"
    "v_cvt_pk_bf16_f32 v70, v20, v21\n"
    "v_cvt_pk_bf16_f32 v71, v22, v23\n"
    "v_mfma_f32_16x16x32_bf16 v[56:59], v[180:183], a[216:219], v[56:59]\n"
    "v_exp_f32 v43, v43\n"
    "v_mfma_f32_16x16x32_bf16 v[60:63], v[180:183], a[240:243], v[104:107]\n"
    "buffer_load_dwordx4 v1, s[16:19], s67 offen offset:2048 sc1 lds\n"
    "v_mfma_f32_16x16x32_bf16 v[60:63], v[184:187], a[244:247], v[60:63]\n"
    "v_cvt_pk_bf16_f32 v74, v24, v25\n"
    "v_cvt_pk_bf16_f32 v89, v42, v43\n"
    "v_mfma_f32_16x16x32_bf16 v[60:63], v[188:191], a[248:251], v[60:63]\n"
    "v_exp_f32 v44, v44\n"
    "v_mfma_f32_16x16x32_bf16 v[60:63], v[192:195], a[252:255], v[60:63]\n"
    "v_exp_f32 v45, v45\n"
    "v_mfma_f32_16x16x32_bf16 v[60:63], v[196:199], v[124:127], v[60:63]\n"
    "buffer_load_dwordx4 v1, s[16:19], s67 offen offset:3072 sc1 lds\n"
    "v_mfma_f32_16x16x32_bf16 v[60:63], v[200:203], v[128:131], v[60:63]\n"
    "s_add_u32 m0, s68, 4096\n"
    "v_cvt_pk_bf16_f32 v75, v26, v27\n"
    "v_cvt_pk_bf16_f32 v92, v44, v45\n"
    "v_mfma_f32_16x16x32_bf16 v[64:67], v[200:203], v[152:155], v[104:107]\n"
    "s_add_u32 s83, s67, 4096\n"
    "v_exp_f32 v46, v46\n"
    "v_mfma_f32_16x16x32_bf16 v[64:67], v[196:199], v[148:151], v[64:67]\n"
    "v_exp_f32 v47, v47\n"
    "v_mfma_f32_16x16x32_bf16 v[64:67], v[192:195], v[144:147], v[64:67]\n"
    "buffer_load_dwordx4 v1, s[16:19], s83 offen offset:0 sc1 lds\n"
    "v_mfma_f32_16x16x32_bf16 v[64:67], v[188:191], v[140:143], v[64:67]\n"
    "v_cvt_pk_bf16_f32 v78, v28, v29\n"
    "v_cvt_pk_bf16_f32 v93, v46, v47\n"
    "v_mfma_f32_16x16x32_bf16 v[64:67], v[184:187], v[136:139], v[64:67]\n"
    "v_exp_f32 v48, v48\n"
    "v_mfma_f32_16x16x32_bf16 v[64:67], v[180:183], v[132:135], v[64:67]\n"
    "s_waitcnt lgkmcnt(0)\n"
    "v_exp_f32 v49, v49\n"
    "v_mfma_f32_16x16x32_bf16 v[4:7], v[156:159], a[192:195], v[100:103]\n"
    "buffer_load_dwordx4 v1, s[16:19], s83 offen offset:1024 sc1 lds\n"
    "v_mfma_f32_16x16x32_bf16 v[4:7], v[160:163], a[196:199], v[4:7]\n"
    "s_add_u32 m0, s70, 0\n"
    "ds_read_b128 v[180:183], v3 offset:1024\n"
    "ds_read_b128 v[184:187], v3 offset:3072\n"
    "v_mfma_f32_16x16x32_bf16 v[4:7], v[164:167], a[200:203], v[4:7]\n"
    "s_add_u32 s83, s67, 24576\n"
    "ds_read_b128 v[188:191], v3 offset:5120\n"
    "v_cvt_pk_bf16_f32 v79, v30, v31\n"
    "v_mfma_f32_16x16x32_bf16 v[4:7], v[168:171], a[204:207], v[4:7]\n"
    "ds_read_b128 v[192:195], v3 offset:7168\n"
    "v_cvt_pk_bf16_f32 v96, v48, v49\n"
    "v_mfma_f32_16x16x32_bf16 v[4:7], v[172:175], a[208:211], v[4:7]\n"
    "buffer_load_dwordx4 v1, s[16:19], s83 offen offset:0 sc1 lds\n"
    "v_mfma_f32_16x16x32_bf16 v[4:7], v[176:179], a[212:215], v[4:7]\n"
    "ds_read_b128 v[196:199], v3 offset:9216\n"
    "ds_read_b128 v[200:203], v3 offset:11264\n"
    "v_mfma_f32_16x16x32_bf16 v[8:11], v[176:179], a[236:239], v[100:103]\n"
    "ds_read_b128 v[104:107], v250 offset:1552\n"
    "v_cvt_pk_bf16_f32 v82, v32, v33\n"
    "v_mfma_f32_16x16x32_bf16 v[8:11], v[172:175], a[232:235], v[8:11]\n"
    "v_exp_f32 v52, v52\n"
    "v_mfma_f32_16x16x32_bf16 v[8:11], v[168:171], a[228:231], v[8:11]\n"
    "buffer_load_dwordx4 v1, s[16:19], s83 offen offset:1024 sc1 lds\n"
    "v_mfma_f32_16x16x32_bf16 v[8:11], v[164:167], a[224:227], v[8:11]\n"
    "v_cvt_pk_bf16_f32 v83, v34, v35\n"
    "v_mfma_f32_16x16x32_bf16 v[8:11], v[160:163], a[220:223], v[8:11]\n"
    "v_exp_f32 v53, v53\n"
    "v_mfma_f32_16x16x32_bf16 v[8:11], v[156:159], a[216:219], v[8:11]\n"
    "v_exp_f32 v54, v54\n"
    "v_mfma_f32_16x16x32_bf16 v[12:15], v[156:159], a[240:243], v[100:103]\n"
    "buffer_load_dwordx4 v1, s[16:19], s83 offen offset:2048 sc1 lds\n"
    "v_mfma_f32_16x16x32_bf16 v[12:15], v[160:163], a[244:247], v[12:15]\n"
    "s_waitcnt vmcnt(10)\n"
    "s_add_u32 s76, s71, s60\n"
    "v_cvt_pk_bf16_f32 v86, v52, v53\n"
    "v_mfma_f32_16x16x32_bf16 v[12:15], v[164:167], a[248:251], v[12:15]\n"
    "s_add_u32 s77, s71, s61\n"
    "v_exp_f32 v55, v55\n"
    "v_mfma_f32_16x16x32_bf16 v[12:15], v[168:171], a[252:255], v[12:15]\n"
    "v_cvt_pk_bf16_f32 v87, v54, v55\n"
    "v_mfma_f32_16x16x32_bf16 v[12:15], v[172:175], v[124:127], v[12:15]\n"
    "buffer_load_dwordx4 v1, s[16:19], s83 offen offset:3072 sc1 lds\n"
    "v_mfma_f32_16x16x32_bf16 v[12:15], v[176:179], v[128:131], v[12:15]\n"
    "s_add_u32 m0, s70, 4096\n"
    "v_exp_f32 v56, v56\n"
    "v_mfma_f32_16x16x32_bf16 v[16:19], v[176:179], v[152:155], v[100:103]\n"
    "s_add_u32 s83, s67, 28672\n"
    "v_exp_f32 v50, v50\n"
    "v_mfma_f32_16x16x32_bf16 v[16:19], v[172:175], v[148:151], v[16:19]\n"
    "s_add_u32 s78, s71, s62\n"
    "v_exp_f32 v51, v51\n"
    "v_mfma_f32_16x16x32_bf16 v[16:19], v[168:171], v[144:147], v[16:19]\n"
    "buffer_load_dwordx4 v1, s[16:19], s83 offen offset:0 sc1 lds\n"
    "v_mfma_f32_16x16x32_bf16 v[16:19], v[164:167], v[140:143], v[16:19]\n"
    "v_exp_f32 v57, v57\n"
    "v_mfma_f32_16x16x32_bf16 v[16:19], v[160:163], v[136:139], v[16:19]\n"
    "v_cvt_pk_bf16_f32 v90, v56, v57\n"
    "v_cvt_pk_bf16_f32 v97, v50, v51\n"
    "v_mfma_f32_16x16x32_bf16 v[16:19], v[156:159], v[132:135], v[16:19]\n"
    "s_waitcnt lgkmcnt(0)\n"
    "v_exp_f32 v58, v58\n"
    "v_mfma_f32_16x16x32_bf16 v[20:23], v[180:183], a[192:195], v[104:107]\n"
    "buffer_load_dwordx4 v1, s[16:19], s83 offen offset:1024 sc1 lds\n"
    "v_mfma_f32_16x16x32_bf16 v[20:23], v[184:187], a[196:199], v[20:23]\n"
    "s_add_u32 s67, s67, 49152\n"
    "ds_read_b128 v[156:159], v249 offset:0\n"
    "ds_read_b128 v[160:163], v249 offset:12288\n"
    "v_mfma_f32_16x16x32_bf16 v[20:23], v[188:191], a[200:203], v[20:23]\n"
    "s_add_u32 s59, s59, 4\n"
    "ds_read_b128 v[164:167], v249 offset:1024\n"
    "v_cvt_f32_i32 v204, v204\n"
    "v_mfma_f32_16x16x32_bf16 v[20:23], v[192:195], a[204:207], v[20:23]\n"
    "s_add_u32 s81, s81, 128\n"
    "ds_read_b128 v[168:171], v249 offset:13312\n"
    "v_cvt_f32_i32 v208, v208\n"
    "v_mfma_f32_16x16x32_bf16 v[20:23], v[196:199], a[208:211], v[20:23]\n"
    "s_min_u32 s81, s81, 4096\n"
    "ds_read_b128 v[172:175], v249 offset:2048\n"
    "v_cvt_f32_i32 v205, v205\n"
    "v_mfma_f32_16x16x32_bf16 v[20:23], v[200:203], a[212:215], v[20:23]\n"
    "ds_read_b128 v[176:179], v249 offset:14336\n"
    "v_cvt_f32_i32 v209, v209\n"
    "v_mfma_f32_16x16x32_bf16 v[24:27], v[200:203], a[236:239], v[104:107]\n"
    "ds_read_b128 v[100:103], v251 offset:768\n"
    "v_cvt_f32_i32 v206, v206\n"
    "v_mfma_f32_16x16x32_bf16 v[24:27], v[196:199], a[232:235], v[24:27]\n"
    "v_exp_f32 v59, v59\n"
    "v_mfma_f32_16x16x32_bf16 v[24:27], v[192:195], a[228:231], v[24:27]\n"
    "v_cvt_pk_bf16_f32 v91, v58, v59\n"
    "v_cvt_f32_i32 v210, v210\n"
    "v_mfma_f32_16x16x32_bf16 v[24:27], v[188:191], a[224:227], v[24:27]\n"
    "v_exp_f32 v60, v60\n"
    "v_mfma_f32_16x16x32_bf16 v[24:27], v[184:187], a[220:223], v[24:27]\n"
    "v_exp_f32 v61, v61\n"
    "v_mfma_f32_16x16x32_bf16 v[24:27], v[180:183], a[216:219], v[24:27]\n"
    "v_exp_f32 v62, v62\n"
    "v_mfma_f32_16x16x32_bf16 v[28:31], v[180:183], a[240:243], v[104:107]\n"
    "v_cvt_pk_bf16_f32 v94, v60, v61\n"
    "v_cvt_pk_bf16_f32 v207, v204, v208\n"
    "v_mfma_f32_16x16x32_bf16 v[28:31], v[184:187], a[244:247], v[28:31]\n"
    "v_exp_f32 v63, v63\n"
    "v_mfma_f32_16x16x32_bf16 v[28:31], v[188:191], a[248:251], v[28:31]\n"
    "v_cvt_pk_bf16_f32 v95, v62, v63\n"
    "v_cvt_pk_bf16_f32 v211, v205, v209\n"
    "v_mfma_f32_16x16x32_bf16 v[28:31], v[192:195], a[252:255], v[28:31]\n"
    "v_cvt_pk_bf16_f32 v215, v206, v210\n"
    "v_mfma_f32_16x16x32_bf16 v[28:31], v[196:199], v[124:127], v[28:31]\n"
    "v_exp_f32 v64, v64\n"
    "v_mfma_f32_16x16x32_bf16 v[28:31], v[200:203], v[128:131], v[28:31]\n"
    "v_cvt_f32_i32 v212, v212\n"
    "v_mfma_f32_16x16x32_bf16 v[32:35], v[200:203], v[152:155], v[104:107]\n"
    "v_mfma_f32_16x16x32_bf16 v[32:35], v[196:199], v[148:151], v[32:35]\n"
    "v_exp_f32 v65, v65\n"
    "v_mfma_f32_16x16x32_bf16 v[32:35], v[192:195], v[144:147], v[32:35]\n"
    "v_cvt_pk_bf16_f32 v98, v64, v65\n"
    "v_cvt_f32_i32 v216, v216\n"
    "v_mfma_f32_16x16x32_bf16 v[32:35], v[188:191], v[140:143], v[32:35]\n"
    "v_cvt_f32_i32 v213, v213\n"
    "v_mfma_f32_16x16x32_bf16 v[32:35], v[184:187], v[136:139], v[32:35]\n"
    "v_exp_f32 v66, v66\n"
    "v_mfma_f32_16x16x32_bf16 v[32:35], v[180:183], v[132:135], v[32:35]\n"
    "s_waitcnt lgkmcnt(0)\n"
    "v_mfma_f32_16x16x32_bf16 a[0:3], v[156:159], v[68:71], a[0:3]\n"
    "ds_read_b128 v[180:183], v249 offset:3072\n"
    "v_cvt_f32_i32 v217, v217\n"
    "v_mfma_f32_16x16x32_bf16 a[0:3], v[160:163], v[84:87], a[0:3]\n"
    "ds_read_b128 v[184:187], v249 offset:15360\n"
    "v_mfma_f32_16x16x32_bf16 a[16:19], v[168:171], v[84:87], a[16:19]\n"
    "ds_read_b128 v[188:191], v249 offset:4096\n"
    "v_cvt_f32_i32 v214, v214\n"
    "v_mfma_f32_16x16x32_bf16 a[16:19], v[164:167], v[68:71], a[16:19]\n"
    "ds_read_b128 v[192:195], v249 offset:16384\n"
    "v_mfma_f32_16x16x32_bf16 a[32:35], v[172:175], v[68:71], a[32:35]\n"
    "ds_read_b128 v[196:199], v249 offset:5120\n"
    "v_cvt_f32_i32 v218, v218\n"
    "v_mfma_f32_16x16x32_bf16 a[32:35], v[176:179], v[84:87], a[32:35]\n"
    "ds_read_b128 v[200:203], v249 offset:17408\n"
    "v_mfma_f32_16x16x32_bf16 a[36:39], v[176:179], v[88:91], a[36:39]\n"
    "ds_read_b128 v[104:107], v251 offset:832\n"
    "v_mfma_f32_16x16x32_bf16 a[36:39], v[172:175], v[72:75], a[36:39]\n"
    "v_exp_f32 v67, v67\n"
    "v_mfma_f32_16x16x32_bf16 a[20:23], v[164:167], v[72:75], a[20:23]\n"
    "v_cvt_pk_bf16_f32 v99, v66, v67\n"
    "v_cvt_pk_bf16_f32 v206, v212, v216\n"
    "v_mfma_f32_16x16x32_bf16 a[20:23], v[168:171], v[88:91], a[20:23]\n"
    "v_exp_f32 v4, v4\n"
    "v_mfma_f32_16x16x32_bf16 a[4:7], v[160:163], v[88:91], a[4:7]\n"
    "v_exp_f32 v5, v5\n"
    "v_mfma_f32_16x16x32_bf16 a[4:7], v[156:159], v[72:75], a[4:7]\n"
    "v_exp_f32 v6, v6\n"
    "v_mfma_f32_16x16x32_bf16 a[8:11], v[156:159], v[76:79], a[8:11]\n"
    "v_exp_f32 v7, v7\n"
    "v_mfma_f32_16x16x32_bf16 a[8:11], v[160:163], v[92:95], a[8:11]\n"
    "v_cvt_pk_bf16_f32 v210, v213, v217\n"
    "v_cvt_pk_bf16_f32 v214, v214, v218\n"
    "v_mfma_f32_16x16x32_bf16 a[24:27], v[168:171], v[92:95], a[24:27]\n"
    "v_cvt_f32_i32 v220, v220\n"
    "v_mfma_f32_16x16x32_bf16 a[24:27], v[164:167], v[76:79], a[24:27]\n"
    "v_exp_f32 v8, v8\n"
    "v_mfma_f32_16x16x32_bf16 a[40:43], v[172:175], v[76:79], a[40:43]\n"
    "v_mfma_f32_16x16x32_bf16 a[40:43], v[176:179], v[92:95], a[40:43]\n"
    "v_cvt_f32_i32 v224, v224\n"
    "v_mfma_f32_16x16x32_bf16 a[44:47], v[176:179], v[96:99], a[44:47]\n"
    "v_exp_f32 v9, v9\n"
    "v_mfma_f32_16x16x32_bf16 a[44:47], v[172:175], v[80:83], a[44:47]\n"
    "v_cvt_f32_i32 v221, v221\n"
    "v_mfma_f32_16x16x32_bf16 a[28:31], v[164:167], v[80:83], a[28:31]\n"
    "v_exp_f32 v10, v10\n"
    "v_mfma_f32_16x16x32_bf16 a[28:31], v[168:171], v[96:99], a[28:31]\n"
    "v_cvt_f32_i32 v225, v225\n"
    "v_mfma_f32_16x16x32_bf16 a[12:15], v[160:163], v[96:99], a[12:15]\n"
    "v_mfma_f32_16x16x32_bf16 a[12:15], v[156:159], v[80:83], a[12:15]\n"
    "s_waitcnt lgkmcnt(0)\n"
    "v_exp_f32 v11, v11\n"
    "v_mfma_f32_16x16x32_bf16 a[48:51], v[180:183], v[68:71], a[48:51]\n"
    "ds_read_b128 v[156:159], v249 offset:6144\n"
    "v_cvt_f32_i32 v222, v222\n"
    "v_mfma_f32_16x16x32_bf16 a[48:51], v[184:187], v[84:87], a[48:51]\n"
    "ds_read_b128 v[160:163], v249 offset:18432\n"
    "v_mfma_f32_16x16x32_bf16 a[64:67], v[192:195], v[84:87], a[64:67]\n"
    "ds_read_b128 v[164:167], v249 offset:7168\n"
    "v_cvt_f32_i32 v226, v226\n"
    "v_mfma_f32_16x16x32_bf16 a[64:67], v[188:191], v[68:71], a[64:67]\n"
    "ds_read_b128 v[168:171], v249 offset:19456\n"
    "v_mfma_f32_16x16x32_bf16 a[80:83], v[196:199], v[68:71], a[80:83]\n"
    "ds_read_b128 v[172:175], v249 offset:8192\n"
    "v_mfma_f32_16x16x32_bf16 a[80:83], v[200:203], v[84:87], a[80:83]\n"
    "ds_read_b128 v[176:179], v249 offset:20480\n"
    "v_cvt_pk_bf16_f32 v205, v220, v224\n"
    "v_mfma_f32_16x16x32_bf16 v[108:111], v[104:107], v[84:87], v[108:111]\n"
    "v_exp_f32 v12, v12\n"
    "v_mfma_f32_16x16x32_bf16 v[108:111], v[100:103], v[68:71], v[108:111]\n"
    "v_exp_f32 v13, v13\n"
    "v_mfma_f32_16x16x32_bf16 v[112:115], v[100:103], v[72:75], v[112:115]\n"
    "v_exp_f32 v14, v14\n"
    "v_mfma_f32_16x16x32_bf16 v[112:115], v[104:107], v[88:91], v[112:115]\n"
    "v_cvt_pk_bf16_f32 v209, v221, v225\n"
    "v_cvt_pk_bf16_f32 v213, v222, v226\n"
    "v_mfma_f32_16x16x32_bf16 a[84:87], v[200:203], v[88:91], a[84:87]\n"
    "v_exp_f32 v15, v15\n"
    "v_mfma_f32_16x16x32_bf16 a[84:87], v[196:199], v[72:75], a[84:87]\n"
    "v_mfma_f32_16x16x32_bf16 a[68:71], v[188:191], v[72:75], a[68:71]\n"
    "v_cvt_f32_i32 v228, v228\n"
    "v_mfma_f32_16x16x32_bf16 a[68:71], v[192:195], v[88:91], a[68:71]\n"
    "v_exp_f32 v16, v16\n"
    "v_mfma_f32_16x16x32_bf16 a[52:55], v[184:187], v[88:91], a[52:55]\n"
    "v_cvt_f32_i32 v232, v232\n"
    "v_mfma_f32_16x16x32_bf16 a[52:55], v[180:183], v[72:75], a[52:55]\n"
    "v_mfma_f32_16x16x32_bf16 a[56:59], v[180:183], v[76:79], a[56:59]\n"
    "v_exp_f32 v17, v17\n"
    "v_mfma_f32_16x16x32_bf16 a[56:59], v[184:187], v[92:95], a[56:59]\n"
    "v_cvt_f32_i32 v229, v229\n"
    "v_mfma_f32_16x16x32_bf16 a[72:75], v[192:195], v[92:95], a[72:75]\n"
    "v_mfma_f32_16x16x32_bf16 a[72:75], v[188:191], v[76:79], a[72:75]\n"
    "v_exp_f32 v18, v18\n"
    "v_mfma_f32_16x16x32_bf16 a[88:91], v[196:199], v[76:79], a[88:91]\n"
    "v_cvt_f32_i32 v233, v233\n"
    "v_mfma_f32_16x16x32_bf16 a[88:91], v[200:203], v[92:95], a[88:91]\n"
    "v_cvt_f32_i32 v230, v230\n"
    "v_mfma_f32_16x16x32_bf16 v[116:119], v[104:107], v[92:95], v[116:119]\n"
    "v_exp_f32 v19, v19\n"
    "v_mfma_f32_16x16x32_bf16 v[116:119], v[100:103], v[76:79], v[116:119]\n"
    "v_mfma_f32_16x16x32_bf16 v[120:123], v[100:103], v[80:83], v[120:123]\n"
    "v_cvt_f32_i32 v234, v234\n"
    "v_mfma_f32_16x16x32_bf16 v[120:123], v[104:107], v[96:99], v[120:123]\n"
    "v_exp_f32 v20, v20\n"
    "v_mfma_f32_16x16x32_bf16 a[92:95], v[200:203], v[96:99], a[92:95]\n"
    "v_cvt_pk_bf16_f32 v204, v228, v232\n"
    "v_mfma_f32_16x16x32_bf16 a[92:95], v[196:199], v[80:83], a[92:95]\n"
    "v_exp_f32 v21, v21\n"
    "v_mfma_f32_16x16x32_bf16 a[76:79], v[188:191], v[80:83], a[76:79]\n"
    "v_mfma_f32_16x16x32_bf16 a[76:79], v[192:195], v[96:99], a[76:79]\n"
    "v_cvt_pk_bf16_f32 v208, v229, v233\n"
    "v_mfma_f32_16x16x32_bf16 a[60:63], v[184:187], v[96:99], a[60:63]\n"
    "v_exp_f32 v22, v22\n"
    "v_mfma_f32_16x16x32_bf16 a[60:63], v[180:183], v[80:83], a[60:63]\n"
    "s_waitcnt lgkmcnt(0)\n"
    "v_cvt_pk_bf16_f32 v212, v230, v234\n"
    "v_mfma_f32_16x16x32_bf16 a[96:99], v[156:159], v[68:71], a[96:99]\n"
    "ds_read_b128 v[180:183], v249 offset:9216\n"
    "v_mfma_f32_16x16x32_bf16 a[96:99], v[160:163], v[84:87], a[96:99]\n"
    "ds_read_b128 v[184:187], v249 offset:21504\n"
    "v_mfma_f32_16x16x32_bf16 a[112:115], v[168:171], v[84:87], a[112:115]\n"
    "ds_read_b128 v[188:191], v249 offset:10240\n"
    "v_mfma_f32_16x16x32_bf16 a[112:115], v[164:167], v[68:71], a[112:115]\n"
    "ds_read_b128 v[192:195], v249 offset:22528\n"
    "v_mfma_f32_16x16x32_bf16 a[128:131], v[172:175], v[68:71], a[128:131]\n"
    "buffer_load_dwordx2 v[236:237], v241, s[36:39], s81 offen offset:0 sc1\n"
    "v_mfma_f32_16x16x32_bf16 a[128:131], v[176:179], v[84:87], a[128:131]\n"
    "ds_read_b128 v[196:199], v249 offset:11264\n"
    "ds_read_b128 v[200:203], v249 offset:23552\n"
    "v_mfma_f32_16x16x32_bf16 a[132:135], v[176:179], v[88:91], a[132:135]\n"
    "v_exp_f32 v23, v23\n"
    "v_mfma_f32_16x16x32_bf16 a[132:135], v[172:175], v[72:75], a[132:135]\n"
    "v_exp_f32 v24, v24\n"
    "v_mfma_f32_16x16x32_bf16 a[116:119], v[164:167], v[72:75], a[116:119]\n"
    "buffer_store_dwordx4 v[204:207], v244, s[16:19], s76 offen offset:0 sc1\n"
    "v_mfma_f32_16x16x32_bf16 a[116:119], v[168:171], v[88:91], a[116:119]\n"
    "v_exp_f32 v25, v25\n"
    "v_mfma_f32_16x16x32_bf16 a[100:103], v[160:163], v[88:91], a[100:103]\n"
    "buffer_store_dwordx4 v[208:211], v244, s[16:19], s77 offen offset:0 sc1\n"
    "v_mfma_f32_16x16x32_bf16 a[100:103], v[156:159], v[72:75], a[100:103]\n"
    "v_exp_f32 v26, v26\n"
    "v_mfma_f32_16x16x32_bf16 a[104:107], v[156:159], v[76:79], a[104:107]\n"
    "buffer_store_dwordx4 v[212:215], v244, s[16:19], s78 offen offset:0 sc1\n"
    "v_mfma_f32_16x16x32_bf16 a[104:107], v[160:163], v[92:95], a[104:107]\n"
    "v_exp_f32 v27, v27\n"
    "v_mfma_f32_16x16x32_bf16 a[120:123], v[168:171], v[92:95], a[120:123]\n"
    "v_mfma_f32_16x16x32_bf16 a[120:123], v[164:167], v[76:79], a[120:123]\n"
    "v_exp_f32 v28, v28\n"
    "v_mfma_f32_16x16x32_bf16 a[136:139], v[172:175], v[76:79], a[136:139]\n"
    "v_mfma_f32_16x16x32_bf16 a[136:139], v[176:179], v[92:95], a[136:139]\n"
    "v_mfma_f32_16x16x32_bf16 a[140:143], v[176:179], v[96:99], a[140:143]\n"
    "v_exp_f32 v29, v29\n"
    "v_mfma_f32_16x16x32_bf16 a[140:143], v[172:175], v[80:83], a[140:143]\n"
    "v_mfma_f32_16x16x32_bf16 a[124:127], v[164:167], v[80:83], a[124:127]\n"
    "v_mfma_f32_16x16x32_bf16 a[124:127], v[168:171], v[96:99], a[124:127]\n"
    "v_exp_f32 v30, v30\n"
    "v_mfma_f32_16x16x32_bf16 a[108:111], v[160:163], v[96:99], a[108:111]\n"
    "v_mfma_f32_16x16x32_bf16 a[108:111], v[156:159], v[80:83], a[108:111]\n"
    "s_waitcnt lgkmcnt(0)\n"
    "v_exp_f32 v31, v31\n"
    "v_mfma_f32_16x16x32_bf16 a[144:147], v[180:183], v[68:71], a[144:147]\n"
    "ds_read_b128 v[156:159], v3 offset:12288\n"
    "v_mfma_f32_16x16x32_bf16 a[144:147], v[184:187], v[84:87], a[144:147]\n"
    "ds_read_b128 v[160:163], v3 offset:14336\n"
    "v_mfma_f32_16x16x32_bf16 a[160:163], v[192:195], v[84:87], a[160:163]\n"
    "ds_read_b128 v[164:167], v3 offset:16384\n"
    "v_mfma_f32_16x16x32_bf16 a[160:163], v[188:191], v[68:71], a[160:163]\n"
    "ds_read_b128 v[168:171], v3 offset:18432\n"
    "v_mfma_f32_16x16x32_bf16 a[176:179], v[196:199], v[68:71], a[176:179]\n"
    "ds_read_b128 v[172:175], v3 offset:20480\n"
    "v_mfma_f32_16x16x32_bf16 a[176:179], v[200:203], v[84:87], a[176:179]\n"
    "ds_read_b128 v[176:179], v3 offset:22528\n"
    "v_mfma_f32_16x16x32_bf16 a[180:183], v[200:203], v[88:91], a[180:183]\n"
    "ds_read_b128 v[100:103], v250 offset:1664\n"
    "v_mfma_f32_16x16x32_bf16 a[180:183], v[196:199], v[72:75], a[180:183]\n"
    "v_exp_f32 v32, v32\n"
    "v_mfma_f32_16x16x32_bf16 a[164:167], v[188:191], v[72:75], a[164:167]\n"
    "v_exp_f32 v33, v33\n"
    "v_mfma_f32_16x16x32_bf16 a[164:167], v[192:195], v[88:91], a[164:167]\n"
    "v_exp_f32 v34, v34\n"
    "v_mfma_f32_16x16x32_bf16 a[148:151], v[184:187], v[88:91], a[148:151]\n"
    "v_mfma_f32_16x16x32_bf16 a[148:151], v[180:183], v[72:75], a[148:151]\n"
    "v_exp_f32 v35, v35\n"
    "v_mfma_f32_16x16x32_bf16 a[152:155], v[180:183], v[76:79], a[152:155]\n"
    "v_mfma_f32_16x16x32_bf16 a[152:155], v[184:187], v[92:95], a[152:155]\n"
    "v_mfma_f32_16x16x32_bf16 a[168:171], v[192:195], v[92:95], a[168:171]\n"
    "s_waitcnt vmcnt(4)\n"
    "ds_add_u32 v247, v246\n"
    "v_mfma_f32_16x16x32_bf16 a[168:171], v[188:191], v[76:79], a[168:171]\n"
    "v_mfma_f32_16x16x32_bf16 a[184:187], v[196:199], v[76:79], a[184:187]\n"
    "v_mfma_f32_16x16x32_bf16 a[184:187], v[200:203], v[92:95], a[184:187]\n"
    "v_mfma_f32_16x16x32_bf16 a[188:191], v[200:203], v[96:99], a[188:191]\n"
    "v_mfma_f32_16x16x32_bf16 a[188:191], v[196:199], v[80:83], a[188:191]\n"
    "v_mfma_f32_16x16x32_bf16 a[172:175], v[188:191], v[80:83], a[172:175]\n"
    "s_sub_u32 s74, s68, s65\n"
    "v_add_u32 v250, 1536, v250\n"
    "v_add_u32 v251, 1536, v251\n"
    "v_mfma_f32_16x16x32_bf16 a[172:175], v[192:195], v[96:99], a[172:175]\n"
    "s_add_u32 s66, s66, 1\n"
    "v_mov_b32 v2, v3\n"
    "v_add_u32 v3, s74, v252\n"
    "v_mfma_f32_16x16x32_bf16 a[156:159], v[184:187], v[96:99], a[156:159]\n"
    "s_waitcnt lgkmcnt(1)\n"
    "s_cmp_lt_u32 s66, 31\n"
    "v_mfma_f32_16x16x32_bf16 a[156:159], v[180:183], v[80:83], a[156:159]\n"
    "s_cbranch_scc1 .LPV\n"
    "s_branch .Ltail\n"
    ".LtailP:\n"
    "v_mov_b32 v248, v249\n"
    "v_add_u32 v250, 768, v250\n"
    "v_add_u32 v251, 768, v251\n"
    ".Ltail:\n"
    "v_mfma_f32_16x16x32_bf16 v[36:39], v[156:159], a[192:195], v[100:103]\n"
    "ds_read_b128 v[180:183], v2 offset:13312\n"
    "v_cvt_pk_bf16_f32 v68, v4, v5\n"
    "v_mfma_f32_16x16x32_bf16 v[36:39], v[160:163], a[196:199], v[36:39]\n"
    "ds_read_b128 v[184:187], v2 offset:15360\n"
    "v_mfma_f32_16x16x32_bf16 v[36:39], v[164:167], a[200:203], v[36:39]\n"
    "ds_read_b128 v[188:191], v2 offset:17408\n"
    "v_mfma_f32_16x16x32_bf16 v[36:39], v[168:171], a[204:207], v[36:39]\n"
    "ds_read_b128 v[192:195], v2 offset:19456\n"
    "v_cvt_pk_bf16_f32 v69, v6, v7\n"
    "v_mfma_f32_16x16x32_bf16 v[36:39], v[172:175], a[208:211], v[36:39]\n"
    "ds_read_b128 v[196:199], v2 offset:21504\n"
    "v_mfma_f32_16x16x32_bf16 v[36:39], v[176:179], a[212:215], v[36:39]\n"
    "ds_read_b128 v[200:203], v2 offset:23552\n"
    "v_mfma_f32_16x16x32_bf16 v[40:43], v[176:179], a[236:239], v[100:103]\n"
    "ds_read_b128 v[104:107], v250 offset:144\n"
    "v_mfma_f32_16x16x32_bf16 v[40:43], v[172:175], a[232:235], v[40:43]\n"
    "v_cvt_pk_bf16_f32 v72, v8, v9\n"
    "v_mfma_f32_16x16x32_bf16 v[40:43], v[168:171], a[228:231], v[40:43]\n"
    "v_mfma_f32_16x16x32_bf16 v[40:43], v[164:167], a[224:227], v[40:43]\n"
    "s_nop 0\n"
    "v_exp_f32 v36, v36\n"
    "v_mfma_f32_16x16x32_bf16 v[40:43], v[160:163], a[220:223], v[40:43]\n"
    "v_exp_f32 v37, v37\n"
    "v_mfma_f32_16x16x32_bf16 v[40:43], v[156:159], a[216:219], v[40:43]\n"
    "v_cvt_pk_bf16_f32 v73, v10, v11\n"
    "v_cvt_pk_bf16_f32 v84, v36, v37\n"
    "v_mfma_f32_16x16x32_bf16 v[44:47], v[156:159], a[240:243], v[100:103]\n"
    "v_exp_f32 v38, v38\n"
    "v_mfma_f32_16x16x32_bf16 v[44:47], v[160:163], a[244:247], v[44:47]\n"
    "v_exp_f32 v39, v39\n"
    "v_mfma_f32_16x16x32_bf16 v[44:47], v[164:167], a[248:251], v[44:47]\n"
    "v_cvt_pk_bf16_f32 v85, v38, v39\n"
    "v_mfma_f32_16x16x32_bf16 v[44:47], v[168:171], a[252:255], v[44:47]\n"
    "v_cvt_pk_bf16_f32 v76, v12, v13\n"
    "v_mfma_f32_16x16x32_bf16 v[44:47], v[172:175], v[124:127], v[44:47]\n"
    "v_exp_f32 v40, v40\n"
    "v_mfma_f32_16x16x32_bf16 v[44:47], v[176:179], v[128:131], v[44:47]\n"
    "v_exp_f32 v41, v41\n"
    "v_mfma_f32_16x16x32_bf16 v[48:51], v[176:179], v[152:155], v[100:103]\n"
    "ds_read_b32 v245, v247\n"
    "v_cvt_pk_bf16_f32 v77, v14, v15\n"
    "v_mfma_f32_16x16x32_bf16 v[48:51], v[172:175], v[148:151], v[48:51]\n"
    "v_exp_f32 v42, v42\n"
    "v_mfma_f32_16x16x32_bf16 v[48:51], v[168:171], v[144:147], v[48:51]\n"
    "v_exp_f32 v43, v43\n"
    "v_mfma_f32_16x16x32_bf16 v[48:51], v[164:167], v[140:143], v[48:51]\n"
    "v_cvt_pk_bf16_f32 v88, v40, v41\n"
    "v_cvt_pk_bf16_f32 v89, v42, v43\n"
    "v_mfma_f32_16x16x32_bf16 v[48:51], v[160:163], v[136:139], v[48:51]\n"
    "v_cvt_pk_bf16_f32 v80, v16, v17\n"
    "v_mfma_f32_16x16x32_bf16 v[48:51], v[156:159], v[132:135], v[48:51]\n"
    "s_waitcnt lgkmcnt(1)\n"
    "v_exp_f32 v44, v44\n"
    "v_mfma_f32_16x16x32_bf16 v[52:55], v[180:183], a[192:195], v[104:107]\n"
    "s_waitcnt lgkmcnt(0)\n"
    "v_cmp_le_u32 s[56:57], s59, v245\n"
    "v_mfma_f32_16x16x32_bf16 v[52:55], v[184:187], a[196:199], v[52:55]\n"
    "v_exp_f32 v45, v45\n"
    "v_mfma_f32_16x16x32_bf16 v[52:55], v[188:191], a[200:203], v[52:55]\n"
    "s_cmp_eq_u64 s[56:57], -1\n"
    "v_mfma_f32_16x16x32_bf16 v[52:55], v[192:195], a[204:207], v[52:55]\n"
    "s_cbranch_scc0 .Lpoll5\n"
    ".Lpoll5r:\n"
    "v_mfma_f32_16x16x32_bf16 v[52:55], v[196:199], a[208:211], v[52:55]\n"
    "ds_read_b128 v[156:159], v248 offset:0\n"
    "ds_read_b128 v[160:163], v248 offset:12288\n"
    "v_mfma_f32_16x16x32_bf16 v[52:55], v[200:203], a[212:215], v[52:55]\n"
    "ds_read_b128 v[164:167], v248 offset:1024\n"
    "ds_read_b128 v[168:171], v248 offset:13312\n"
    "v_mfma_f32_16x16x32_bf16 v[56:59], v[200:203], a[236:239], v[104:107]\n"
    "ds_read_b128 v[172:175], v248 offset:2048\n"
    "ds_read_b128 v[176:179], v248 offset:14336\n"
    "v_mfma_f32_16x16x32_bf16 v[56:59], v[196:199], a[232:235], v[56:59]\n"
    "ds_read_b128 v[100:103], v251 offset:0\n"
    "v_cvt_pk_bf16_f32 v81, v18, v19\n"
    "v_mfma_f32_16x16x32_bf16 v[56:59], v[192:195], a[228:231], v[56:59]\n"
    "v_cvt_pk_bf16_f32 v70, v20, v21\n"
    "v_cvt_pk_bf16_f32 v92, v44, v45\n"
    "v_mfma_f32_16x16x32_bf16 v[56:59], v[188:191], a[224:227], v[56:59]\n"
    "v_cvt_pk_bf16_f32 v71, v22, v23\n"
    "v_mfma_f32_16x16x32_bf16 v[56:59], v[184:187], a[220:223], v[56:59]\n"
    "v_exp_f32 v52, v52\n"
    "v_mfma_f32_16x16x32_bf16 v[56:59], v[180:183], a[216:219], v[56:59]\n"
    "v_exp_f32 v53, v53\n"
    "v_mfma_f32_16x16x32_bf16 v[60:63], v[180:183], a[240:243], v[104:107]\n"
    "v_exp_f32 v54, v54\n"
    "v_mfma_f32_16x16x32_bf16 v[60:63], v[184:187], a[244:247], v[60:63]\n"
    "v_cvt_pk_bf16_f32 v74, v24, v25\n"
    "v_cvt_pk_bf16_f32 v86, v52, v53\n"
    "v_mfma_f32_16x16x32_bf16 v[60:63], v[188:191], a[248:251], v[60:63]\n"
    "v_exp_f32 v55, v55\n"
    "v_mfma_f32_16x16x32_bf16 v[60:63], v[192:195], a[252:255], v[60:63]\n"
    "v_cvt_pk_bf16_f32 v87, v54, v55\n"
    "v_mfma_f32_16x16x32_bf16 v[60:63], v[196:199], v[124:127], v[60:63]\n"
    "v_exp_f32 v56, v56\n"
    "v_mfma_f32_16x16x32_bf16 v[60:63], v[200:203], v[128:131], v[60:63]\n"
    "v_cvt_pk_bf16_f32 v75, v26, v27\n"
    "v_mfma_f32_16x16x32_bf16 v[64:67], v[200:203], v[152:155], v[104:107]\n"
    "v_exp_f32 v57, v57\n"
    "v_mfma_f32_16x16x32_bf16 v[64:67], v[196:199], v[148:151], v[64:67]\n"
    "v_exp_f32 v58, v58\n"
    "v_mfma_f32_16x16x32_bf16 v[64:67], v[192:195], v[144:147], v[64:67]\n"
    "v_exp_f32 v59, v59\n"
    "v_mfma_f32_16x16x32_bf16 v[64:67], v[188:191], v[140:143], v[64:67]\n"
    "v_cvt_pk_bf16_f32 v78, v28, v29\n"
    "v_cvt_pk_bf16_f32 v90, v56, v57\n"
    "v_mfma_f32_16x16x32_bf16 v[64:67], v[184:187], v[136:139], v[64:67]\n"
    "v_cvt_pk_bf16_f32 v91, v58, v59\n"
    "v_mfma_f32_16x16x32_bf16 v[64:67], v[180:183], v[132:135], v[64:67]\n"
    "v_exp_f32 v46, v46\n"
    "v_cvt_pk_bf16_f32 v79, v30, v31\n"
    "v_exp_f32 v47, v47\n"
    "v_exp_f32 v60, v60\n"
    "v_exp_f32 v61, v61\n"
    "v_cvt_pk_bf16_f32 v82, v32, v33\n"
    "v_cvt_pk_bf16_f32 v93, v46, v47\n"
    "v_exp_f32 v62, v62\n"
    "v_exp_f32 v63, v63\n"
    "v_cvt_pk_bf16_f32 v94, v60, v61\n"
    "v_cvt_pk_bf16_f32 v95, v62, v63\n"
    "v_cvt_pk_bf16_f32 v83, v34, v35\n"
    "v_exp_f32 v48, v48\n"
    "v_exp_f32 v49, v49\n"
    "v_exp_f32 v50, v50\n"
    "v_exp_f32 v51, v51\n"
    "v_exp_f32 v64, v64\n"
    "v_exp_f32 v65, v65\n"
    "v_exp_f32 v66, v66\n"
    "v_exp_f32 v67, v67\n"
    "v_cvt_pk_bf16_f32 v96, v48, v49\n"
    "v_cvt_pk_bf16_f32 v97, v50, v51\n"
    "v_cvt_pk_bf16_f32 v98, v64, v65\n"
    "v_cvt_pk_bf16_f32 v99, v66, v67\n"
    "s_waitcnt lgkmcnt(0)\n"
    "v_mfma_f32_16x16x32_bf16 a[0:3], v[156:159], v[68:71], a[0:3]\n"
    "ds_read_b128 v[180:183], v248 offset:3072\n"
    "v_mfma_f32_16x16x32_bf16 a[0:3], v[160:163], v[84:87], a[0:3]\n"
    "ds_read_b128 v[184:187], v248 offset:15360\n"
    "v_mfma_f32_16x16x32_bf16 a[16:19], v[168:171], v[84:87], a[16:19]\n"
    "ds_read_b128 v[188:191], v248 offset:4096\n"
    "v_mfma_f32_16x16x32_bf16 a[16:19], v[164:167], v[68:71], a[16:19]\n"
    "ds_read_b128 v[192:195], v248 offset:16384\n"
    "v_mfma_f32_16x16x32_bf16 a[32:35], v[172:175], v[68:71], a[32:35]\n"
    "ds_read_b128 v[196:199], v248 offset:5120\n"
    "v_mfma_f32_16x16x32_bf16 a[32:35], v[176:179], v[84:87], a[32:35]\n"
    "ds_read_b128 v[200:203], v248 offset:17408\n"
    "v_mfma_f32_16x16x32_bf16 a[36:39], v[176:179], v[88:91], a[36:39]\n"
    "ds_read_b128 v[104:107], v251 offset:64\n"
    "v_mfma_f32_16x16x32_bf16 a[36:39], v[172:175], v[72:75], a[36:39]\n"
    "v_mfma_f32_16x16x32_bf16 a[20:23], v[164:167], v[72:75], a[20:23]\n"
    "v_mfma_f32_16x16x32_bf16 a[20:23], v[168:171], v[88:91], a[20:23]\n"
    "v_mfma_f32_16x16x32_bf16 a[4:7], v[160:163], v[88:91], a[4:7]\n"
    "v_mfma_f32_16x16x32_bf16 a[4:7], v[156:159], v[72:75], a[4:7]\n"
    "v_mfma_f32_16x16x32_bf16 a[8:11], v[156:159], v[76:79], a[8:11]\n"
    "v_mfma_f32_16x16x32_bf16 a[8:11], v[160:163], v[92:95], a[8:11]\n"
    "v_mfma_f32_16x16x32_bf16 a[24:27], v[168:171], v[92:95], a[24:27]\n"
    "s_waitcnt vmcnt(0)\n"
    "v_mfma_f32_16x16x32_bf16 a[24:27], v[164:167], v[76:79], a[24:27]\n"
    "v_lshrrev_b32 v204, 4, v1\n"
    "v_mfma_f32_16x16x32_bf16 a[40:43], v[172:175], v[76:79], a[40:43]\n"
    "s_mul_i32 s72, s84, 25600\n"
    "s_mul_i32 s73, s84, 24576\n"
    "v_mfma_f32_16x16x32_bf16 a[40:43], v[176:179], v[92:95], a[40:43]\n"
    "v_and_b32 v206, 15, v204\n"
    "v_lshrrev_b32 v207, 4, v204\n"
    "v_mfma_f32_16x16x32_bf16 a[44:47], v[176:179], v[96:99], a[44:47]\n"
    "v_mul_u32_u24 v205, 400, v206\n"
    "v_mul_u32_u24 v207, 24, v207\n"
    "v_mfma_f32_16x16x32_bf16 a[44:47], v[172:175], v[80:83], a[44:47]\n"
    "v_add_u32 v205, v207, v205\n"
    "v_add_u32 v205, s72, v205\n"
    "v_mfma_f32_16x16x32_bf16 a[28:31], v[164:167], v[80:83], a[28:31]\n"
    "v_add_u32 v206, 0, v204\n"
    "v_mul_u32_u24 v207, 2731, v206\n"
    "v_mfma_f32_16x16x32_bf16 a[28:31], v[168:171], v[96:99], a[28:31]\n"
    "v_lshrrev_b32 v207, 16, v207\n"
    "v_lshlrev_b32 v206, 4, v206\n"
    "v_mfma_f32_16x16x32_bf16 a[12:15], v[160:163], v[96:99], a[12:15]\n"
    "v_lshl_add_u32 v209, v207, 4, v206\n"
    "v_add_u32 v209, s72, v209\n"
    "v_mfma_f32_16x16x32_bf16 a[12:15], v[156:159], v[80:83], a[12:15]\n"
    "s_waitcnt lgkmcnt(0)\n"
    "v_add_u32 v206, 64, v204\n"
    "v_mul_u32_u24 v207, 2731, v206\n"
    "v_mfma_f32_16x16x32_bf16 a[48:51], v[180:183], v[68:71], a[48:51]\n"
    "ds_read_b128 v[156:159], v248 offset:6144\n"
    "v_lshrrev_b32 v207, 16, v207\n"
    "v_mfma_f32_16x16x32_bf16 a[48:51], v[184:187], v[84:87], a[48:51]\n"
    "ds_read_b128 v[160:163], v248 offset:18432\n"
    "v_lshlrev_b32 v206, 4, v206\n"
    "v_mfma_f32_16x16x32_bf16 a[64:67], v[192:195], v[84:87], a[64:67]\n"
    "ds_read_b128 v[164:167], v248 offset:7168\n"
    "v_lshl_add_u32 v210, v207, 4, v206\n"
    "v_mfma_f32_16x16x32_bf16 a[64:67], v[188:191], v[68:71], a[64:67]\n"
    "ds_read_b128 v[168:171], v248 offset:19456\n"
    "v_add_u32 v210, s72, v210\n"
    "v_mfma_f32_16x16x32_bf16 a[80:83], v[196:199], v[68:71], a[80:83]\n"
    "ds_read_b128 v[172:175], v248 offset:8192\n"
    "v_add_u32 v206, 128, v204\n"
    "v_mfma_f32_16x16x32_bf16 a[80:83], v[200:203], v[84:87], a[80:83]\n"
    "ds_read_b128 v[176:179], v248 offset:20480\n"
    "v_mul_u32_u24 v207, 2731, v206\n"
    "v_mfma_f32_16x16x32_bf16 v[108:111], v[104:107], v[84:87], v[108:111]\n"
    "v_lshrrev_b32 v207, 16, v207\n"
    "v_lshlrev_b32 v206, 4, v206\n"
    "v_mfma_f32_16x16x32_bf16 v[108:111], v[100:103], v[68:71], v[108:111]\n"
    "v_lshl_add_u32 v211, v207, 4, v206\n"
    "v_add_u32 v211, s72, v211\n"
    "v_mfma_f32_16x16x32_bf16 v[112:115], v[100:103], v[72:75], v[112:115]\n"
    "v_lshlrev_b32 v208, 16, v1\n"
    "v_mfma_f32_16x16x32_bf16 v[112:115], v[104:107], v[88:91], v[112:115]\n"
    "buffer_store_dwordx2 v[238:239], v208, s[36:39], s82 offen offset:0 sc1\n"
    "v_mfma_f32_16x16x32_bf16 a[84:87], v[200:203], v[88:91], a[84:87]\n"
    "v_mfma_f32_16x16x32_bf16 a[84:87], v[196:199], v[72:75], a[84:87]\n"
    "v_mfma_f32_16x16x32_bf16 a[68:71], v[188:191], v[72:75], a[68:71]\n"
    "v_mfma_f32_16x16x32_bf16 a[68:71], v[192:195], v[88:91], a[68:71]\n"
    "v_mfma_f32_16x16x32_bf16 a[52:55], v[184:187], v[88:91], a[52:55]\n"
    "s_mov_b32 s74, 0x5b800000\n"
    "v_mov_b32 v212, 0\n"
    "v_add_f32 v108, v108, v109\n"
    "v_mfma_f32_16x16x32_bf16 a[52:55], v[180:183], v[72:75], a[52:55]\n"
    "v_add_f32 v112, v112, v113\n"
    "v_mov_b32 v111, v110\n"
    "v_mfma_f32_16x16x32_bf16 a[56:59], v[180:183], v[76:79], a[56:59]\n"
    "v_mov_b32 v115, v114\n"
    "v_mov_b32 v109, v108\n"
    "v_mfma_f32_16x16x32_bf16 a[56:59], v[184:187], v[92:95], a[56:59]\n"
    "v_mov_b32 v113, v112\n"
    "v_permlane32_swap_b32 v110, v111\n"
    "v_mfma_f32_16x16x32_bf16 a[72:75], v[192:195], v[92:95], a[72:75]\n"
    "v_permlane32_swap_b32 v114, v115\n"
    "v_permlane32_swap_b32 v108, v109\n"
    "v_mfma_f32_16x16x32_bf16 a[72:75], v[188:191], v[76:79], a[72:75]\n"
    "v_permlane32_swap_b32 v112, v113\n"
    "v_add_f32 v110, v110, v111\n"
    "v_mfma_f32_16x16x32_bf16 a[88:91], v[196:199], v[76:79], a[88:91]\n"
    "v_add_f32 v114, v114, v115\n"
    "v_add_f32 v108, v108, v109\n"
    "v_mfma_f32_16x16x32_bf16 a[88:91], v[200:203], v[92:95], a[88:91]\n"
    "v_add_f32 v112, v112, v113\n"
    "v_mov_b32 v111, v110\n"
    "v_mfma_f32_16x16x32_bf16 v[116:119], v[104:107], v[92:95], v[116:119]\n"
    "v_mov_b32 v115, v114\n"
    "v_mov_b32 v109, v108\n"
    "v_mfma_f32_16x16x32_bf16 v[116:119], v[100:103], v[76:79], v[116:119]\n"
    "v_mov_b32 v113, v112\n"
    "v_permlane16_swap_b32 v110, v111\n"
    "v_mfma_f32_16x16x32_bf16 v[120:123], v[100:103], v[80:83], v[120:123]\n"
    "v_permlane16_swap_b32 v114, v115\n"
    "v_permlane16_swap_b32 v108, v109\n"
    "v_mfma_f32_16x16x32_bf16 v[120:123], v[104:107], v[96:99], v[120:123]\n"
    "v_permlane16_swap_b32 v112, v113\n"
    "v_add_f32 v110, v110, v111\n"
    "v_mfma_f32_16x16x32_bf16 a[92:95], v[200:203], v[96:99], a[92:95]\n"
    "v_add_f32 v114, v114, v115\n"
    "v_add_f32 v108, v108, v109\n"
    "v_mfma_f32_16x16x32_bf16 a[92:95], v[196:199], v[80:83], a[92:95]\n"
    "v_add_f32 v112, v112, v113\n"
    "v_fma_f32 v212, |v110|, 4.0, v212\n"
    "v_mfma_f32_16x16x32_bf16 a[76:79], v[188:191], v[80:83], a[76:79]\n"
    "v_fma_f32 v212, v108, s74, v212\n"
    "v_fma_f32 v212, |v114|, 4.0, v212\n"
    "v_mfma_f32_16x16x32_bf16 a[76:79], v[192:195], v[96:99], a[76:79]\n"
    "v_fma_f32 v212, v112, s74, v212\n"
    "v_mfma_f32_16x16x32_bf16 a[60:63], v[184:187], v[96:99], a[60:63]\n"
    "v_rcp_f32 v108, v108\n"
    "v_mfma_f32_16x16x32_bf16 a[60:63], v[180:183], v[80:83], a[60:63]\n"
    "s_waitcnt lgkmcnt(0)\n"
    "v_rcp_f32 v112, v112\n"
    "v_mfma_f32_16x16x32_bf16 a[96:99], v[156:159], v[68:71], a[96:99]\n"
    "ds_read_b128 v[180:183], v248 offset:9216\n"
    "v_fma_f32 v212, v108, s74, v212\n"
    "v_mfma_f32_16x16x32_bf16 a[96:99], v[160:163], v[84:87], a[96:99]\n"
    "ds_read_b128 v[184:187], v248 offset:21504\n"
    "v_mul_f32 v110, -v110, v108\n"
    "v_mfma_f32_16x16x32_bf16 a[112:115], v[168:171], v[84:87], a[112:115]\n"
    "ds_read_b128 v[188:191], v248 offset:10240\n"
    "v_fma_f32 v212, v112, s74, v212\n"
    "v_mfma_f32_16x16x32_bf16 a[112:115], v[164:167], v[68:71], a[112:115]\n"
    "ds_read_b128 v[192:195], v248 offset:22528\n"
    "v_mul_f32 v114, -v114, v112\n"
    "v_mfma_f32_16x16x32_bf16 a[128:131], v[172:175], v[68:71], a[128:131]\n"
    "ds_read_b128 v[196:199], v248 offset:11264\n"
    "v_add_f32 v116, v116, v117\n"
    "v_mfma_f32_16x16x32_bf16 a[128:131], v[176:179], v[84:87], a[128:131]\n"
    "ds_read_b128 v[200:203], v248 offset:23552\n"
    "v_add_f32 v120, v120, v121\n"
    "v_mfma_f32_16x16x32_bf16 a[132:135], v[176:179], v[88:91], a[132:135]\n"
    "v_mov_b32 v119, v118\n"
    "v_mov_b32 v123, v122\n"
    "v_mfma_f32_16x16x32_bf16 a[132:135], v[172:175], v[72:75], a[132:135]\n"
    "v_mov_b32 v117, v116\n"
    "v_mov_b32 v121, v120\n"
    "v_mfma_f32_16x16x32_bf16 a[116:119], v[164:167], v[72:75], a[116:119]\n"
    "v_permlane32_swap_b32 v118, v119\n"
    "v_permlane32_swap_b32 v122, v123\n"
    "v_mfma_f32_16x16x32_bf16 a[116:119], v[168:171], v[88:91], a[116:119]\n"
    "v_permlane32_swap_b32 v116, v117\n"
    "v_permlane32_swap_b32 v120, v121\n"
    "v_mfma_f32_16x16x32_bf16 a[100:103], v[160:163], v[88:91], a[100:103]\n"
    "v_add_f32 v118, v118, v119\n"
    "v_add_f32 v122, v122, v123\n"
    "v_mfma_f32_16x16x32_bf16 a[100:103], v[156:159], v[72:75], a[100:103]\n"
    "v_add_f32 v116, v116, v117\n"
    "v_add_f32 v120, v120, v121\n"
    "v_mfma_f32_16x16x32_bf16 a[104:107], v[156:159], v[76:79], a[104:107]\n"
    "v_mov_b32 v119, v118\n"
    "v_mov_b32 v123, v122\n"
    "v_mfma_f32_16x16x32_bf16 a[104:107], v[160:163], v[92:95], a[104:107]\n"
    "v_mov_b32 v117, v116\n"
    "v_mov_b32 v121, v120\n"
    "v_mfma_f32_16x16x32_bf16 a[120:123], v[168:171], v[92:95], a[120:123]\n"
    "v_permlane16_swap_b32 v118, v119\n"
    "v_permlane16_swap_b32 v122, v123\n"
    "v_mfma_f32_16x16x32_bf16 a[120:123], v[164:167], v[76:79], a[120:123]\n"
    "v_permlane16_swap_b32 v116, v117\n"
    "v_permlane16_swap_b32 v120, v121\n"
    "v_mfma_f32_16x16x32_bf16 a[136:139], v[172:175], v[76:79], a[136:139]\n"
    "v_add_f32 v118, v118, v119\n"
    "v_add_f32 v122, v122, v123\n"
    "v_mfma_f32_16x16x32_bf16 a[136:139], v[176:179], v[92:95], a[136:139]\n"
    "v_add_f32 v116, v116, v117\n"
    "v_add_f32 v120, v120, v121\n"
    "v_mfma_f32_16x16x32_bf16 a[140:143], v[176:179], v[96:99], a[140:143]\n"
    "v_fma_f32 v212, |v118|, 4.0, v212\n"
    "v_fma_f32 v212, v116, s74, v212\n"
    "v_mfma_f32_16x16x32_bf16 a[140:143], v[172:175], v[80:83], a[140:143]\n"
    "v_fma_f32 v212, |v122|, 4.0, v212\n"
    "v_fma_f32 v212, v120, s74, v212\n"
    "v_mfma_f32_16x16x32_bf16 a[124:127], v[164:167], v[80:83], a[124:127]\n"
    "v_rcp_f32 v116, v116\n"
    "v_mfma_f32_16x16x32_bf16 a[124:127], v[168:171], v[96:99], a[124:127]\n"
    "v_rcp_f32 v120, v120\n"
    "v_mfma_f32_16x16x32_bf16 a[108:111], v[160:163], v[96:99], a[108:111]\n"
    "v_fma_f32 v212, v116, s74, v212\n"
    "v_mul_f32 v118, -v118, v116\n"
    "v_mfma_f32_16x16x32_bf16 a[108:111], v[156:159], v[80:83], a[108:111]\n"
    "s_waitcnt lgkmcnt(0)\n"
    "v_fma_f32 v212, v120, s74, v212\n"
    "v_mul_f32 v122, -v122, v120\n"
    "v_mfma_f32_16x16x32_bf16 a[144:147], v[180:183], v[68:71], a[144:147]\n"
    "s_mov_b32 s74, 0x7f800000\n"
    "v_cmp_nlt_f32 s[54:55], |v212|, s74\n"
    "v_mfma_f32_16x16x32_bf16 a[144:147], v[184:187], v[84:87], a[144:147]\n"
    "s_or_b32 %0, s54, s55\n"
    "v_mfma_f32_16x16x32_bf16 a[160:163], v[192:195], v[84:87], a[160:163]\n"
    "v_mfma_f32_16x16x32_bf16 a[160:163], v[188:191], v[68:71], a[160:163]\n"
    "v_mfma_f32_16x16x32_bf16 a[176:179], v[196:199], v[68:71], a[176:179]\n"
    "v_mfma_f32_16x16x32_bf16 a[176:179], v[200:203], v[84:87], a[176:179]\n"
    "v_mfma_f32_16x16x32_bf16 a[180:183], v[200:203], v[88:91], a[180:183]\n"
    "v_mfma_f32_16x16x32_bf16 a[180:183], v[196:199], v[72:75], a[180:183]\n"
    "v_mfma_f32_16x16x32_bf16 a[164:167], v[188:191], v[72:75], a[164:167]\n"
    "v_mfma_f32_16x16x32_bf16 a[164:167], v[192:195], v[88:91], a[164:167]\n"
    "v_mfma_f32_16x16x32_bf16 a[148:151], v[184:187], v[88:91], a[148:151]\n"
    "v_mfma_f32_16x16x32_bf16 a[148:151], v[180:183], v[72:75], a[148:151]\n"
    "v_mfma_f32_16x16x32_bf16 a[152:155], v[180:183], v[76:79], a[152:155]\n"
    "v_mfma_f32_16x16x32_bf16 a[152:155], v[184:187], v[92:95], a[152:155]\n"
    "v_mfma_f32_16x16x32_bf16 a[168:171], v[192:195], v[92:95], a[168:171]\n"
    "v_mfma_f32_16x16x32_bf16 a[168:171], v[188:191], v[76:79], a[168:171]\n"
    "v_mfma_f32_16x16x32_bf16 a[184:187], v[196:199], v[76:79], a[184:187]\n"
    "v_mfma_f32_16x16x32_bf16 a[184:187], v[200:203], v[92:95], a[184:187]\n"
    "v_mfma_f32_16x16x32_bf16 a[188:191], v[200:203], v[96:99], a[188:191]\n"
    "v_mfma_f32_16x16x32_bf16 a[188:191], v[196:199], v[80:83], a[188:191]\n"
    "v_mfma_f32_16x16x32_bf16 a[172:175], v[188:191], v[80:83], a[172:175]\n"
    "v_mfma_f32_16x16x32_bf16 a[172:175], v[192:195], v[96:99], a[172:175]\n"
    "v_mfma_f32_16x16x32_bf16 a[156:159], v[184:187], v[96:99], a[156:159]\n"
    "v_mfma_f32_16x16x32_bf16 a[156:159], v[180:183], v[80:83], a[156:159]\n"
    "s_waitcnt lgkmcnt(0)\n"
    "s_barrier\n"
    "v_accvgpr_read_b32 v4, a0\n"
    "v_accvgpr_read_b32 v7, a1\n"
    "v_accvgpr_read_b32 v10, a2\n"
    "v_accvgpr_read_b32 v13, a3\n"
    "v_accvgpr_read_b32 v5, a64\n"
    "v_accvgpr_read_b32 v8, a65\n"
    "v_accvgpr_read_b32 v11, a66\n"
    "v_accvgpr_read_b32 v14, a67\n"
    "v_accvgpr_read_b32 v6, a128\n"
    "v_accvgpr_read_b32 v9, a129\n"
    "v_accvgpr_read_b32 v12, a130\n"
    "v_accvgpr_read_b32 v15, a131\n"
    "v_pk_fma_f32 v[4:5], v[4:5], v[108:109], v[110:111] op_sel_hi:[1,0,0]\n"
    "v_pk_fma_f32 v[6:7], v[6:7], v[108:109], v[110:111] op_sel_hi:[1,0,0]\n"
    "v_pk_fma_f32 v[8:9], v[8:9], v[108:109], v[110:111] op_sel_hi:[1,0,0]\n"
    "v_pk_fma_f32 v[10:11], v[10:11], v[108:109], v[110:111] op_sel_hi:[1,0,0]\n"
    "v_pk_fma_f32 v[12:13], v[12:13], v[108:109], v[110:111] op_sel_hi:[1,0,0]\n"
    "v_pk_fma_f32 v[14:15], v[14:15], v[108:109], v[110:111] op_sel_hi:[1,0,0]\n"
    "v_cvt_pk_bf16_f32 v16, v4, v5\n"
    "v_cvt_pk_bf16_f32 v17, v6, v7\n"
    "v_cvt_pk_bf16_f32 v18, v8, v9\n"
    "v_cvt_pk_bf16_f32 v19, v10, v11\n"
    "v_cvt_pk_bf16_f32 v20, v12, v13\n"
    "v_cvt_pk_bf16_f32 v21, v14, v15\n"
    "ds_write_b64 v205, v[16:17] offset:0\n"
    "ds_write_b64 v205, v[18:19] offset:8\n"
    "ds_write_b64 v205, v[20:21] offset:16\n"
    "v_accvgpr_read_b32 v22, a16\n"
    "v_accvgpr_read_b32 v25, a17\n"
    "v_accvgpr_read_b32 v28, a18\n"
    "v_accvgpr_read_b32 v31, a19\n"
    "v_accvgpr_read_b32 v23, a80\n"
    "v_accvgpr_read_b32 v26, a81\n"
    "v_accvgpr_read_b32 v29, a82\n"
    "v_accvgpr_read_b32 v32, a83\n"
    "v_accvgpr_read_b32 v24, a144\n"
    "v_accvgpr_read_b32 v27, a145\n"
    "v_accvgpr_read_b32 v30, a146\n"
    "v_accvgpr_read_b32 v33, a147\n"
    "v_pk_fma_f32 v[22:23], v[22:23], v[108:109], v[110:111] op_sel_hi:[1,0,0]\n"
    "v_pk_fma_f32 v[24:25], v[24:25], v[108:109], v[110:111] op_sel_hi:[1,0,0]\n"
    "v_pk_fma_f32 v[26:27], v[26:27], v[108:109], v[110:111] op_sel_hi:[1,0,0]\n"
    "v_pk_fma_f32 v[28:29], v[28:29], v[108:109], v[110:111] op_sel_hi:[1,0,0]\n"
    "v_pk_fma_f32 v[30:31], v[30:31], v[108:109], v[110:111] op_sel_hi:[1,0,0]\n"
    "v_pk_fma_f32 v[32:33], v[32:33], v[108:109], v[110:111] op_sel_hi:[1,0,0]\n"
    "v_cvt_pk_bf16_f32 v34, v22, v23\n"
    "v_cvt_pk_bf16_f32 v35, v24, v25\n"
    "v_cvt_pk_bf16_f32 v36, v26, v27\n"
    "v_cvt_pk_bf16_f32 v37, v28, v29\n"
    "v_cvt_pk_bf16_f32 v38, v30, v31\n"
    "v_cvt_pk_bf16_f32 v39, v32, v33\n"
    "ds_write_b64 v205, v[34:35] offset:96\n"
    "ds_write_b64 v205, v[36:37] offset:104\n"
    "ds_write_b64 v205, v[38:39] offset:112\n"
    "v_accvgpr_read_b32 v40, a32\n"
    "v_accvgpr_read_b32 v43, a33\n"
    "v_accvgpr_read_b32 v46, a34\n"
    "v_accvgpr_read_b32 v49, a35\n"
    "v_accvgpr_read_b32 v41, a96\n"
    "v_accvgpr_read_b32 v44, a97\n"
    "v_accvgpr_read_b32 v47, a98\n"
    "v_accvgpr_read_b32 v50, a99\n"
    "v_accvgpr_read_b32 v42, a160\n"
    "v_accvgpr_read_b32 v45, a161\n"
    "v_accvgpr_read_b32 v48, a162\n"
    "v_accvgpr_read_b32 v51, a163\n"
    "v_pk_fma_f32 v[40:41], v[40:41], v[108:109], v[110:111] op_sel_hi:[1,0,0]\n"
    "v_pk_fma_f32 v[42:43], v[42:43], v[108:109], v[110:111] op_sel_hi:[1,0,0]\n"
    "v_pk_fma_f32 v[44:45], v[44:45], v[108:109], v[110:111] op_sel_hi:[1,0,0]\n"
    "v_pk_fma_f32 v[46:47], v[46:47], v[108:109], v[110:111] op_sel_hi:[1,0,0]\n"
    "v_pk_fma_f32 v[48:49], v[48:49], v[108:109], v[110:111] op_sel_hi:[1,0,0]\n"
    "v_pk_fma_f32 v[50:51], v[50:51], v[108:109], v[110:111] op_sel_hi:[1,0,0]\n"
    "v_cvt_pk_bf16_f32 v52, v40, v41\n"
    "v_cvt_pk_bf16_f32 v53, v42, v43\n"
    "v_cvt_pk_bf16_f32 v54, v44, v45\n"
    "v_cvt_pk_bf16_f32 v55, v46, v47\n"
    "v_cvt_pk_bf16_f32 v56, v48, v49\n"
    "v_cvt_pk_bf16_f32 v57, v50, v51\n"
    "ds_write_b64 v205, v[52:53] offset:192\n"
    "ds_write_b64 v205, v[54:55] offset:200\n"
    "ds_write_b64 v205, v[56:57] offset:208\n"
    "v_accvgpr_read_b32 v58, a48\n"
    "v_accvgpr_read_b32 v61, a49\n"
    "v_accvgpr_read_b32 v64, a50\n"
    "v_accvgpr_read_b32 v67, a51\n"
    "v_accvgpr_read_b32 v59, a112\n"
    "v_accvgpr_read_b32 v62, a113\n"
    "v_accvgpr_read_b32 v65, a114\n"
    "v_accvgpr_read_b32 v68, a115\n"
    "v_accvgpr_read_b32 v60, a176\n"
    "v_accvgpr_read_b32 v63, a177\n"
    "v_accvgpr_read_b32 v66, a178\n"
    "v_accvgpr_read_b32 v69, a179\n"
    "v_pk_fma_f32 v[58:59], v[58:59], v[108:109], v[110:111] op_sel_hi:[1,0,0]\n"
    "v_pk_fma_f32 v[60:61], v[60:61], v[108:109], v[110:111] op_sel_hi:[1,0,0]\n"
    "v_pk_fma_f32 v[62:63], v[62:63], v[108:109], v[110:111] op_sel_hi:[1,0,0]\n"
    "v_pk_fma_f32 v[64:65], v[64:65], v[108:109], v[110:111] op_sel_hi:[1,0,0]\n"
    "v_pk_fma_f32 v[66:67], v[66:67], v[108:109], v[110:111] op_sel_hi:[1,0,0]\n"
    "v_pk_fma_f32 v[68:69], v[68:69], v[108:109], v[110:111] op_sel_hi:[1,0,0]\n"
    "v_cvt_pk_bf16_f32 v70, v58, v59\n"
    "v_cvt_pk_bf16_f32 v71, v60, v61\n"
    "v_cvt_pk_bf16_f32 v72, v62, v63\n"
    "v_cvt_pk_bf16_f32 v73, v64, v65\n"
    "v_cvt_pk_bf16_f32 v74, v66, v67\n"
    "v_cvt_pk_bf16_f32 v75, v68, v69\n"
    "ds_write_b64 v205, v[70:71] offset:288\n"
    "ds_write_b64 v205, v[72:73] offset:296\n"
    "ds_write_b64 v205, v[74:75] offset:304\n"
    "ds_read_b128 v[124:127], v209 offset:0\n"
    "ds_read_b128 v[128:131], v210 offset:0\n"
    "ds_read_b128 v[132:135], v211 offset:0\n"
    "ds_read_b128 v[136:139], v209 offset:3200\n"
    "ds_read_b128 v[140:143], v210 offset:3200\n"
    "ds_read_b128 v[144:147], v211 offset:3200\n"
    "v_accvgpr_read_b32 v76, a4\n"
    "v_accvgpr_read_b32 v79, a5\n"
    "v_accvgpr_read_b32 v82, a6\n"
    "v_accvgpr_read_b32 v85, a7\n"
    "v_accvgpr_read_b32 v77, a68\n"
    "v_accvgpr_read_b32 v80, a69\n"
    "v_accvgpr_read_b32 v83, a70\n"
    "v_accvgpr_read_b32 v86, a71\n"
    "v_accvgpr_read_b32 v78, a132\n"
    "v_accvgpr_read_b32 v81, a133\n"
    "v_accvgpr_read_b32 v84, a134\n"
    "v_accvgpr_read_b32 v87, a135\n"
    "v_pk_fma_f32 v[76:77], v[76:77], v[112:113], v[114:115] op_sel_hi:[1,0,0]\n"
    "v_pk_fma_f32 v[78:79], v[78:79], v[112:113], v[114:115] op_sel_hi:[1,0,0]\n"
    "v_pk_fma_f32 v[80:81], v[80:81], v[112:113], v[114:115] op_sel_hi:[1,0,0]\n"
    "v_pk_fma_f32 v[82:83], v[82:83], v[112:113], v[114:115] op_sel_hi:[1,0,0]\n"
    "v_pk_fma_f32 v[84:85], v[84:85], v[112:113], v[114:115] op_sel_hi:[1,0,0]\n"
    "v_pk_fma_f32 v[86:87], v[86:87], v[112:113], v[114:115] op_sel_hi:[1,0,0]\n"
    "v_cvt_pk_bf16_f32 v88, v76, v77\n"
    "v_cvt_pk_bf16_f32 v89, v78, v79\n"
    "v_cvt_pk_bf16_f32 v90, v80, v81\n"
    "v_cvt_pk_bf16_f32 v91, v82, v83\n"
    "v_cvt_pk_bf16_f32 v92, v84, v85\n"
    "v_cvt_pk_bf16_f32 v93, v86, v87\n"
    "ds_write_b64 v205, v[88:89] offset:6400\n"
    "ds_write_b64 v205, v[90:91] offset:6408\n"
    "ds_write_b64 v205, v[92:93] offset:6416\n"
    "s_waitcnt lgkmcnt(8)\n"
    "buffer_store_dwordx4 v[124:127], v1, s[24:27], s73 offen offset:0 nt sc1\n"
    "s_waitcnt lgkmcnt(7)\n"
    "buffer_store_dwordx4 v[128:131], v1, s[24:27], s73 offen offset:1024 nt sc1\n"
    "s_waitcnt lgkmcnt(6)\n"
    "buffer_store_dwordx4 v[132:135], v1, s[24:27], s73 offen offset:2048 nt sc1\n"
    "s_waitcnt lgkmcnt(5)\n"
    "buffer_store_dwordx4 v[136:139], v1, s[24:27], s73 offen offset:3072 nt sc1\n"
    "s_add_u32 s73, s73, 4096\n"
    "s_waitcnt lgkmcnt(4)\n"
    "buffer_store_dwordx4 v[140:143], v1, s[24:27], s73 offen offset:0 nt sc1\n"
    "s_waitcnt lgkmcnt(3)\n"
    "buffer_store_dwordx4 v[144:147], v1, s[24:27], s73 offen offset:1024 nt sc1\n"
    "v_accvgpr_read_b32 v4, a20\n"
    "v_accvgpr_read_b32 v7, a21\n"
    "v_accvgpr_read_b32 v10, a22\n"
    "v_accvgpr_read_b32 v13, a23\n"
    "v_accvgpr_read_b32 v5, a84\n"
    "v_accvgpr_read_b32 v8, a85\n"
    "v_accvgpr_read_b32 v11, a86\n"
    "v_accvgpr_read_b32 v14, a87\n"
    "v_accvgpr_read_b32 v6, a148\n"
    "v_accvgpr_read_b32 v9, a149\n"
    "v_accvgpr_read_b32 v12, a150\n"
    "v_accvgpr_read_b32 v15, a151\n"
    "v_pk_fma_f32 v[4:5], v[4:5], v[112:113], v[114:115] op_sel_hi:[1,0,0]\n"
    "v_pk_fma_f32 v[6:7], v[6:7], v[112:113], v[114:115] op_sel_hi:[1,0,0]\n"
    "v_pk_fma_f32 v[8:9], v[8:9], v[112:113], v[114:115] op_sel_hi:[1,0,0]\n"
    "v_pk_fma_f32 v[10:11], v[10:11], v[112:113], v[114:115] op_sel_hi:[1,0,0]\n"
    "v_pk_fma_f32 v[12:13], v[12:13], v[112:113], v[114:115] op_sel_hi:[1,0,0]\n"
    "v_pk_fma_f32 v[14:15], v[14:15], v[112:113], v[114:115] op_sel_hi:[1,0,0]\n"
    "v_cvt_pk_bf16_f32 v16, v4, v5\n"
    "v_cvt_pk_bf16_f32 v17, v6, v7\n"
    "v_cvt_pk_bf16_f32 v18, v8, v9\n"
    "v_cvt_pk_bf16_f32 v19, v10, v11\n"
    "v_cvt_pk_bf16_f32 v20, v12, v13\n"
    "v_cvt_pk_bf16_f32 v21, v14, v15\n"
    "ds_write_b64 v205, v[16:17] offset:6496\n"
    "ds_write_b64 v205, v[18:19] offset:6504\n"
    "ds_write_b64 v205, v[20:21] offset:6512\n"
    "v_accvgpr_read_b32 v22, a36\n"
    "v_accvgpr_read_b32 v25, a37\n"
    "v_accvgpr_read_b32 v28, a38\n"
    "v_accvgpr_read_b32 v31, a39\n"
    "v_accvgpr_read_b32 v23, a100\n"
    "v_accvgpr_read_b32 v26, a101\n"
    "v_accvgpr_read_b32 v29, a102\n"
    "v_accvgpr_read_b32 v32, a103\n"
    "v_accvgpr_read_b32 v24, a164\n"
    "v_accvgpr_read_b32 v27, a165\n"
    "v_accvgpr_read_b32 v30, a166\n"
    "v_accvgpr_read_b32 v33, a167\n"
    "v_pk_fma_f32 v[22:23], v[22:23], v[112:113], v[114:115] op_sel_hi:[1,0,0]\n"
    "v_pk_fma_f32 v[24:25], v[24:25], v[112:113], v[114:115] op_sel_hi:[1,0,0]\n"
    "v_pk_fma_f32 v[26:27], v[26:27], v[112:113], v[114:115] op_sel_hi:[1,0,0]\n"
    "v_pk_fma_f32 v[28:29], v[28:29], v[112:113], v[114:115] op_sel_hi:[1,0,0]\n"
    "v_pk_fma_f32 v[30:31], v[30:31], v[112:113], v[114:115] op_sel_hi:[1,0,0]\n"
    "v_pk_fma_f32 v[32:33], v[32:33], v[112:113], v[114:115] op_sel_hi:[1,0,0]\n"
    "v_cvt_pk_bf16_f32 v34, v22, v23\n"
    "v_cvt_pk_bf16_f32 v35, v24, v25\n"
    "v_cvt_pk_bf16_f32 v36, v26, v27\n"
    "v_cvt_pk_bf16_f32 v37, v28, v29\n"
    "v_cvt_pk_bf16_f32 v38, v30, v31\n"
    "v_cvt_pk_bf16_f32 v39, v32, v33\n"
    "ds_write_b64 v205, v[34:35] offset:6592\n"
    "ds_write_b64 v205, v[36:37] offset:6600\n"
    "ds_write_b64 v205, v[38:39] offset:6608\n"
    "v_accvgpr_read_b32 v40, a52\n"
    "v_accvgpr_read_b32 v43, a53\n"
    "v_accvgpr_read_b32 v46, a54\n"
    "v_accvgpr_read_b32 v49, a55\n"
    "v_accvgpr_read_b32 v41, a116\n"
    "v_accvgpr_read_b32 v44, a117\n"
    "v_accvgpr_read_b32 v47, a118\n"
    "v_accvgpr_read_b32 v50, a119\n"
    "v_accvgpr_read_b32 v42, a180\n"
    "v_accvgpr_read_b32 v45, a181\n"
    "v_accvgpr_read_b32 v48, a182\n"
    "v_accvgpr_read_b32 v51, a183\n"
    "v_pk_fma_f32 v[40:41], v[40:41], v[112:113], v[114:115] op_sel_hi:[1,0,0]\n"
    "v_pk_fma_f32 v[42:43], v[42:43], v[112:113], v[114:115] op_sel_hi:[1,0,0]\n"
    "v_pk_fma_f32 v[44:45], v[44:45], v[112:113], v[114:115] op_sel_hi:[1,0,0]\n"
    "v_pk_fma_f32 v[46:47], v[46:47], v[112:113], v[114:115] op_sel_hi:[1,0,0]\n"
    "v_pk_fma_f32 v[48:49], v[48:49], v[112:113], v[114:115] op_sel_hi:[1,0,0]\n"
    "v_pk_fma_f32 v[50:51], v[50:51], v[112:113], v[114:115] op_sel_hi:[1,0,0]\n"
    "v_cvt_pk_bf16_f32 v52, v40, v41\n"
    "v_cvt_pk_bf16_f32 v53, v42, v43\n"
    "v_cvt_pk_bf16_f32 v54, v44, v45\n"
    "v_cvt_pk_bf16_f32 v55, v46, v47\n"
    "v_cvt_pk_bf16_f32 v56, v48, v49\n"
    "v_cvt_pk_bf16_f32 v57, v50, v51\n"
    "ds_write_b64 v205, v[52:53] offset:6688\n"
    "ds_write_b64 v205, v[54:55] offset:6696\n"
    "ds_write_b64 v205, v[56:57] offset:6704\n"
    "ds_read_b128 v[148:151], v209 offset:6400\n"
    "ds_read_b128 v[152:155], v210 offset:6400\n"
    "ds_read_b128 v[156:159], v211 offset:6400\n"
    "ds_read_b128 v[160:163], v209 offset:9600\n"
    "ds_read_b128 v[164:167], v210 offset:9600\n"
    "ds_read_b128 v[168:171], v211 offset:9600\n"
    "v_accvgpr_read_b32 v58, a8\n"
    "v_accvgpr_read_b32 v61, a9\n"
    "v_accvgpr_read_b32 v64, a10\n"
    "v_accvgpr_read_b32 v67, a11\n"
    "v_accvgpr_read_b32 v59, a72\n"
    "v_accvgpr_read_b32 v62, a73\n"
    "v_accvgpr_read_b32 v65, a74\n"
    "v_accvgpr_read_b32 v68, a75\n"
    "v_accvgpr_read_b32 v60, a136\n"
    "v_accvgpr_read_b32 v63, a137\n"
    "v_accvgpr_read_b32 v66, a138\n"
    "v_accvgpr_read_b32 v69, a139\n"
    "v_pk_fma_f32 v[58:59], v[58:59], v[116:117], v[118:119] op_sel_hi:[1,0,0]\n"
    "v_pk_fma_f32 v[60:61], v[60:61], v[116:117], v[118:119] op_sel_hi:[1,0,0]\n"
    "v_pk_fma_f32 v[62:63], v[62:63], v[116:117], v[118:119] op_sel_hi:[1,0,0]\n"
    "v_pk_fma_f32 v[64:65], v[64:65], v[116:117], v[118:119] op_sel_hi:[1,0,0]\n"
    "v_pk_fma_f32 v[66:67], v[66:67], v[116:117], v[118:119] op_sel_hi:[1,0,0]\n"
    "v_pk_fma_f32 v[68:69], v[68:69], v[116:117], v[118:119] op_sel_hi:[1,0,0]\n"
    "v_cvt_pk_bf16_f32 v70, v58, v59\n"
    "v_cvt_pk_bf16_f32 v71, v60, v61\n"
    "v_cvt_pk_bf16_f32 v72, v62, v63\n"
    "v_cvt_pk_bf16_f32 v73, v64, v65\n"
    "v_cvt_pk_bf16_f32 v74, v66, v67\n"
    "v_cvt_pk_bf16_f32 v75, v68, v69\n"
    "ds_write_b64 v205, v[70:71] offset:12800\n"
    "ds_write_b64 v205, v[72:73] offset:12808\n"
    "ds_write_b64 v205, v[74:75] offset:12816\n"
    "s_waitcnt lgkmcnt(8)\n"
    "buffer_store_dwordx4 v[148:151], v1, s[24:27], s73 offen offset:2048 nt sc1\n"
    "s_waitcnt lgkmcnt(7)\n"
    "buffer_store_dwordx4 v[152:155], v1, s[24:27], s73 offen offset:3072 nt sc1\n"
    "s_add_u32 s73, s73, 4096\n"
    "s_waitcnt lgkmcnt(6)\n"
    "buffer_store_dwordx4 v[156:159], v1, s[24:27], s73 offen offset:0 nt sc1\n"
    "s_waitcnt lgkmcnt(5)\n"
    "buffer_store_dwordx4 v[160:163], v1, s[24:27], s73 offen offset:1024 nt sc1\n"
    "s_waitcnt lgkmcnt(4)\n"
    "buffer_store_dwordx4 v[164:167], v1, s[24:27], s73 offen offset:2048 nt sc1\n"
    "s_waitcnt lgkmcnt(3)\n"
    "buffer_store_dwordx4 v[168:171], v1, s[24:27], s73 offen offset:3072 nt sc1\n"
    "v_accvgpr_read_b32 v76, a24\n"
    "v_accvgpr_read_b32 v79, a25\n"
    "v_accvgpr_read_b32 v82, a26\n"
    "v_accvgpr_read_b32 v85, a27\n"
    "v_accvgpr_read_b32 v77, a88\n"
    "v_accvgpr_read_b32 v80, a89\n"
    "v_accvgpr_read_b32 v83, a90\n"
    "v_accvgpr_read_b32 v86, a91\n"
    "v_accvgpr_read_b32 v78, a152\n"
    "v_accvgpr_read_b32 v81, a153\n"
    "v_accvgpr_read_b32 v84, a154\n"
    "v_accvgpr_read_b32 v87, a155\n"
    "v_pk_fma_f32 v[76:77], v[76:77], v[116:117], v[118:119] op_sel_hi:[1,0,0]\n"
    "v_pk_fma_f32 v[78:79], v[78:79], v[116:117], v[118:119] op_sel_hi:[1,0,0]\n"
    "v_pk_fma_f32 v[80:81], v[80:81], v[116:117], v[118:119] op_sel_hi:[1,0,0]\n"
    "v_pk_fma_f32 v[82:83], v[82:83], v[116:117], v[118:119] op_sel_hi:[1,0,0]\n"
    "v_pk_fma_f32 v[84:85], v[84:85], v[116:117], v[118:119] op_sel_hi:[1,0,0]\n"
    "v_pk_fma_f32 v[86:87], v[86:87], v[116:117], v[118:119] op_sel_hi:[1,0,0]\n"
    "v_cvt_pk_bf16_f32 v88, v76, v77\n"
    "v_cvt_pk_bf16_f32 v89, v78, v79\n"
    "v_cvt_pk_bf16_f32 v90, v80, v81\n"
    "v_cvt_pk_bf16_f32 v91, v82, v83\n"
    "v_cvt_pk_bf16_f32 v92, v84, v85\n"
    "v_cvt_pk_bf16_f32 v93, v86, v87\n"
    "ds_write_b64 v205, v[88:89] offset:12896\n"
    "ds_write_b64 v205, v[90:91] offset:12904\n"
    "ds_write_b64 v205, v[92:93] offset:12912\n"
    "v_accvgpr_read_b32 v4, a40\n"
    "v_accvgpr_read_b32 v7, a41\n"
    "v_accvgpr_read_b32 v10, a42\n"
    "v_accvgpr_read_b32 v13, a43\n"
    "v_accvgpr_read_b32 v5, a104\n"
    "v_accvgpr_read_b32 v8, a105\n"
    "v_accvgpr_read_b32 v11, a106\n"
    "v_accvgpr_read_b32 v14, a107\n"
    "v_accvgpr_read_b32 v6, a168\n"
    "v_accvgpr_read_b32 v9, a169\n"
    "v_accvgpr_read_b32 v12, a170\n"
    "v_accvgpr_read_b32 v15, a171\n"
    "v_pk_fma_f32 v[4:5], v[4:5], v[116:117], v[118:119] op_sel_hi:[1,0,0]\n"
    "v_pk_fma_f32 v[6:7], v[6:7], v[116:117], v[118:119] op_sel_hi:[1,0,0]\n"
    "v_pk_fma_f32 v[8:9], v[8:9], v[116:117], v[118:119] op_sel_hi:[1,0,0]\n"
    "v_pk_fma_f32 v[10:11], v[10:11], v[116:117], v[118:119] op_sel_hi:[1,0,0]\n"
    "v_pk_fma_f32 v[12:13], v[12:13], v[116:117], v[118:119] op_sel_hi:[1,0,0]\n"
    "v_pk_fma_f32 v[14:15], v[14:15], v[116:117], v[118:119] op_sel_hi:[1,0,0]\n"
    "v_cvt_pk_bf16_f32 v16, v4, v5\n"
    "v_cvt_pk_bf16_f32 v17, v6, v7\n"
    "v_cvt_pk_bf16_f32 v18, v8, v9\n"
    "v_cvt_pk_bf16_f32 v19, v10, v11\n"
    "v_cvt_pk_bf16_f32 v20, v12, v13\n"
    "v_cvt_pk_bf16_f32 v21, v14, v15\n"
    "ds_write_b64 v205, v[16:17] offset:12992\n"
    "ds_write_b64 v205, v[18:19] offset:13000\n"
    "ds_write_b64 v205, v[20:21] offset:13008\n"
    "v_accvgpr_read_b32 v22, a56\n"
    "v_accvgpr_read_b32 v25, a57\n"
    "v_accvgpr_read_b32 v28, a58\n"
    "v_accvgpr_read_b32 v31, a59\n"
    "v_accvgpr_read_b32 v23, a120\n"
    "v_accvgpr_read_b32 v26, a121\n"
    "v_accvgpr_read_b32 v29, a122\n"
    "v_accvgpr_read_b32 v32, a123\n"
    "v_accvgpr_read_b32 v24, a184\n"
    "v_accvgpr_read_b32 v27, a185\n"
    "v_accvgpr_read_b32 v30, a186\n"
    "v_accvgpr_read_b32 v33, a187\n"
    "v_pk_fma_f32 v[22:23], v[22:23], v[116:117], v[118:119] op_sel_hi:[1,0,0]\n"
    "v_pk_fma_f32 v[24:25], v[24:25], v[116:117], v[118:119] op_sel_hi:[1,0,0]\n"
    "v_pk_fma_f32 v[26:27], v[26:27], v[116:117], v[118:119] op_sel_hi:[1,0,0]\n"
    "v_pk_fma_f32 v[28:29], v[28:29], v[116:117], v[118:119] op_sel_hi:[1,0,0]\n"
    "v_pk_fma_f32 v[30:31], v[30:31], v[116:117], v[118:119] op_sel_hi:[1,0,0]\n"
    "v_pk_fma_f32 v[32:33], v[32:33], v[116:117], v[118:119] op_sel_hi:[1,0,0]\n"
    "v_cvt_pk_bf16_f32 v34, v22, v23\n"
    "v_cvt_pk_bf16_f32 v35, v24, v25\n"
    "v_cvt_pk_bf16_f32 v36, v26, v27\n"
    "v_cvt_pk_bf16_f32 v37, v28, v29\n"
    "v_cvt_pk_bf16_f32 v38, v30, v31\n"
    "v_cvt_pk_bf16_f32 v39, v32, v33\n"
    "ds_write_b64 v205, v[34:35] offset:13088\n"
    "ds_write_b64 v205, v[36:37] offset:13096\n"
    "ds_write_b64 v205, v[38:39] offset:13104\n"
    "ds_read_b128 v[172:175], v209 offset:12800\n"
    "ds_read_b128 v[176:179], v210 offset:12800\n"
    "ds_read_b128 v[180:183], v211 offset:12800\n"
    "ds_read_b128 v[184:187], v209 offset:16000\n"
    "ds_read_b128 v[188:191], v210 offset:16000\n"
    "ds_read_b128 v[192:195], v211 offset:16000\n"
    "v_accvgpr_read_b32 v40, a12\n"
    "v_accvgpr_read_b32 v43, a13\n"
    "v_accvgpr_read_b32 v46, a14\n"
    "v_accvgpr_read_b32 v49, a15\n"
    "v_accvgpr_read_b32 v41, a76\n"
    "v_accvgpr_read_b32 v44, a77\n"
    "v_accvgpr_read_b32 v47, a78\n"
    "v_accvgpr_read_b32 v50, a79\n"
    "v_accvgpr_read_b32 v42, a140\n"
    "v_accvgpr_read_b32 v45, a141\n"
    "v_accvgpr_read_b32 v48, a142\n"
    "v_accvgpr_read_b32 v51, a143\n"
    "v_pk_fma_f32 v[40:41], v[40:41], v[120:121], v[122:123] op_sel_hi:[1,0,0]\n"
    "v_pk_fma_f32 v[42:43], v[42:43], v[120:121], v[122:123] op_sel_hi:[1,0,0]\n"
    "v_pk_fma_f32 v[44:45], v[44:45], v[120:121], v[122:123] op_sel_hi:[1,0,0]\n"
    "v_pk_fma_f32 v[46:47], v[46:47], v[120:121], v[122:123] op_sel_hi:[1,0,0]\n"
    "v_pk_fma_f32 v[48:49], v[48:49], v[120:121], v[122:123] op_sel_hi:[1,0,0]\n"
    "v_pk_fma_f32 v[50:51], v[50:51], v[120:121], v[122:123] op_sel_hi:[1,0,0]\n"
    "v_cvt_pk_bf16_f32 v52, v40, v41\n"
    "v_cvt_pk_bf16_f32 v53, v42, v43\n"
    "v_cvt_pk_bf16_f32 v54, v44, v45\n"
    "v_cvt_pk_bf16_f32 v55, v46, v47\n"
    "v_cvt_pk_bf16_f32 v56, v48, v49\n"
    "v_cvt_pk_bf16_f32 v57, v50, v51\n"
    "ds_write_b64 v205, v[52:53] offset:19200\n"
    "ds_write_b64 v205, v[54:55] offset:19208\n"
    "ds_write_b64 v205, v[56:57] offset:19216\n"
    "s_add_u32 s73, s73, 4096\n"
    "s_waitcnt lgkmcnt(8)\n"
    "buffer_store_dwordx4 v[172:175], v1, s[24:27], s73 offen offset:0 nt sc1\n"
    "s_waitcnt lgkmcnt(7)\n"
    "buffer_store_dwordx4 v[176:179], v1, s[24:27], s73 offen offset:1024 nt sc1\n"
    "s_waitcnt lgkmcnt(6)\n"
    "buffer_store_dwordx4 v[180:183], v1, s[24:27], s73 offen offset:2048 nt sc1\n"
    "s_waitcnt lgkmcnt(5)\n"
    "buffer_store_dwordx4 v[184:187], v1, s[24:27], s73 offen offset:3072 nt sc1\n"
    "s_add_u32 s73, s73, 4096\n"
    "s_waitcnt lgkmcnt(4)\n"
    "buffer_store_dwordx4 v[188:191], v1, s[24:27], s73 offen offset:0 nt sc1\n"
    "s_waitcnt lgkmcnt(3)\n"
    "buffer_store_dwordx4 v[192:195], v1, s[24:27], s73 offen offset:1024 nt sc1\n"
    "v_accvgpr_read_b32 v58, a28\n"
    "v_accvgpr_read_b32 v61, a29\n"
    "v_accvgpr_read_b32 v64, a30\n"
    "v_accvgpr_read_b32 v67, a31\n"
    "v_accvgpr_read_b32 v59, a92\n"
    "v_accvgpr_read_b32 v62, a93\n"
    "v_accvgpr_read_b32 v65, a94\n"
    "v_accvgpr_read_b32 v68, a95\n"
    "v_accvgpr_read_b32 v60, a156\n"
    "v_accvgpr_read_b32 v63, a157\n"
    "v_accvgpr_read_b32 v66, a158\n"
    "v_accvgpr_read_b32 v69, a159\n"
    "v_pk_fma_f32 v[58:59], v[58:59], v[120:121], v[122:123] op_sel_hi:[1,0,0]\n"
    "v_pk_fma_f32 v[60:61], v[60:61], v[120:121], v[122:123] op_sel_hi:[1,0,0]\n"
    "v_pk_fma_f32 v[62:63], v[62:63], v[120:121], v[122:123] op_sel_hi:[1,0,0]\n"
    "v_pk_fma_f32 v[64:65], v[64:65], v[120:121], v[122:123] op_sel_hi:[1,0,0]\n"
    "v_pk_fma_f32 v[66:67], v[66:67], v[120:121], v[122:123] op_sel_hi:[1,0,0]\n"
    "v_pk_fma_f32 v[68:69], v[68:69], v[120:121], v[122:123] op_sel_hi:[1,0,0]\n"
    "v_cvt_pk_bf16_f32 v70, v58, v59\n"
    "v_cvt_pk_bf16_f32 v71, v60, v61\n"
    "v_cvt_pk_bf16_f32 v72, v62, v63\n"
    "v_cvt_pk_bf16_f32 v73, v64, v65\n"
    "v_cvt_pk_bf16_f32 v74, v66, v67\n"
    "v_cvt_pk_bf16_f32 v75, v68, v69\n"
    "ds_write_b64 v205, v[70:71] offset:19296\n"
    "ds_write_b64 v205, v[72:73] offset:19304\n"
    "ds_write_b64 v205, v[74:75] offset:19312\n"
    "v_accvgpr_read_b32 v76, a44\n"
    "v_accvgpr_read_b32 v79, a45\n"
    "v_accvgpr_read_b32 v82, a46\n"
    "v_accvgpr_read_b32 v85, a47\n"
    "v_accvgpr_read_b32 v77, a108\n"
    "v_accvgpr_read_b32 v80, a109\n"
    "v_accvgpr_read_b32 v83, a110\n"
    "v_accvgpr_read_b32 v86, a111\n"
    "v_accvgpr_read_b32 v78, a172\n"
    "v_accvgpr_read_b32 v81, a173\n"
    "v_accvgpr_read_b32 v84, a174\n"
    "v_accvgpr_read_b32 v87, a175\n"
    "v_pk_fma_f32 v[76:77], v[76:77], v[120:121], v[122:123] op_sel_hi:[1,0,0]\n"
    "v_pk_fma_f32 v[78:79], v[78:79], v[120:121], v[122:123] op_sel_hi:[1,0,0]\n"
    "v_pk_fma_f32 v[80:81], v[80:81], v[120:121], v[122:123] op_sel_hi:[1,0,0]\n"
    "v_pk_fma_f32 v[82:83], v[82:83], v[120:121], v[122:123] op_sel_hi:[1,0,0]\n"
    "v_pk_fma_f32 v[84:85], v[84:85], v[120:121], v[122:123] op_sel_hi:[1,0,0]\n"
    "v_pk_fma_f32 v[86:87], v[86:87], v[120:121], v[122:123] op_sel_hi:[1,0,0]\n"
    "v_cvt_pk_bf16_f32 v88, v76, v77\n"
    "v_cvt_pk_bf16_f32 v89, v78, v79\n"
    "v_cvt_pk_bf16_f32 v90, v80, v81\n"
    "v_cvt_pk_bf16_f32 v91, v82, v83\n"
    "v_cvt_pk_bf16_f32 v92, v84, v85\n"
    "v_cvt_pk_bf16_f32 v93, v86, v87\n"
    "ds_write_b64 v205, v[88:89] offset:19392\n"
    "ds_write_b64 v205, v[90:91] offset:19400\n"
    "ds_write_b64 v205, v[92:93] offset:19408\n"
    "v_accvgpr_read_b32 v4, a60\n"
    "v_accvgpr_read_b32 v7, a61\n"
    "v_accvgpr_read_b32 v10, a62\n"
    "v_accvgpr_read_b32 v13, a63\n"
    "v_accvgpr_read_b32 v5, a124\n"
    "v_accvgpr_read_b32 v8, a125\n"
    "v_accvgpr_read_b32 v11, a126\n"
    "v_accvgpr_read_b32 v14, a127\n"
    "v_accvgpr_read_b32 v6, a188\n"
    "v_accvgpr_read_b32 v9, a189\n"
    "v_accvgpr_read_b32 v12, a190\n"
    "v_accvgpr_read_b32 v15, a191\n"
    "v_pk_fma_f32 v[4:5], v[4:5], v[120:121], v[122:123] op_sel_hi:[1,0,0]\n"
    "v_pk_fma_f32 v[6:7], v[6:7], v[120:121], v[122:123] op_sel_hi:[1,0,0]\n"
    "v_pk_fma_f32 v[8:9], v[8:9], v[120:121], v[122:123] op_sel_hi:[1,0,0]\n"
    "v_pk_fma_f32 v[10:11], v[10:11], v[120:121], v[122:123] op_sel_hi:[1,0,0]\n"
    "v_pk_fma_f32 v[12:13], v[12:13], v[120:121], v[122:123] op_sel_hi:[1,0,0]\n"
    "v_pk_fma_f32 v[14:15], v[14:15], v[120:121], v[122:123] op_sel_hi:[1,0,0]\n"
    "v_cvt_pk_bf16_f32 v16, v4, v5\n"
    "v_cvt_pk_bf16_f32 v17, v6, v7\n"
    "v_cvt_pk_bf16_f32 v18, v8, v9\n"
    "v_cvt_pk_bf16_f32 v19, v10, v11\n"
    "v_cvt_pk_bf16_f32 v20, v12, v13\n"
    "v_cvt_pk_bf16_f32 v21, v14, v15\n"
    "ds_write_b64 v205, v[16:17] offset:19488\n"
    "ds_write_b64 v205, v[18:19] offset:19496\n"
    "ds_write_b64 v205, v[20:21] offset:19504\n"
    "ds_read_b128 v[124:127], v209 offset:19200\n"
    "ds_read_b128 v[128:131], v210 offset:19200\n"
    "ds_read_b128 v[132:135], v211 offset:19200\n"
    "ds_read_b128 v[136:139], v209 offset:22400\n"
    "ds_read_b128 v[140:143], v210 offset:22400\n"
    "ds_read_b128 v[144:147], v211 offset:22400\n"
    "s_waitcnt lgkmcnt(5)\n"
    "buffer_store_dwordx4 v[124:127], v1, s[24:27], s73 offen offset:2048 nt sc1\n"
    "s_waitcnt lgkmcnt(4)\n"
    "buffer_store_dwordx4 v[128:131], v1, s[24:27], s73 offen offset:3072 nt sc1\n"
    "s_add_u32 s73, s73, 4096\n"
    "s_waitcnt lgkmcnt(3)\n"
    "buffer_store_dwordx4 v[132:135], v1, s[24:27], s73 offen offset:0 nt sc1\n"
    "s_waitcnt lgkmcnt(2)\n"
    "buffer_store_dwordx4 v[136:139], v1, s[24:27], s73 offen offset:1024 nt sc1\n"
    "s_waitcnt lgkmcnt(1)\n"
    "buffer_store_dwordx4 v[140:143], v1, s[24:27], s73 offen offset:2048 nt sc1\n"
    "s_waitcnt lgkmcnt(0)\n"
    "buffer_store_dwordx4 v[144:147], v1, s[24:27], s73 offen offset:3072 nt sc1\n"
    "s_cmp_eq_u32 %0, 0\n"
    "s_cbranch_scc1 .Lfin\n"
    "s_waitcnt vmcnt(0)\n"
    ".Lfin:\n"
    "s_branch .Lend\n"
    ".LpollA:\n"
    "s_sleep 1\n"
    "buffer_load_dwordx2 v[236:237], v87, s[36:39], 0 offen offset:0 sc1\n"
    "s_waitcnt vmcnt(0)\n"
    "v_cmp_eq_u64 s[54:55], v[236:237], v[238:239]\n"
    "s_add_u32 s58, s58, 1\n"
    "s_cmp_eq_u64 s[54:55], -1\n"
    "s_cbranch_scc1 .LpollAr\n"
    "s_cmp_lt_u32 s58, 0x80000\n"
    "s_cbranch_scc1 .LpollA\n"
    "s_branch .LpollAr\n"
    ".Lpoll1:\n"
    "s_sleep 1\n"
    "v_cmp_eq_u64 s[54:55], v[236:237], v[238:239]\n"
    "s_nop 1\n"
    "s_cmp_eq_u64 s[54:55], -1\n"
    "s_cbranch_scc1 .Lpoll1l\n"
    "buffer_load_dwordx2 v[236:237], v241, s[36:39], s81 offen offset:0 sc1\n"
    "s_waitcnt vmcnt(0)\n"
    "v_cmp_eq_u64 s[54:55], v[236:237], v[238:239]\n"
    ".Lpoll1l:\n"
    "ds_read_b32 v245, v247\n"
    "s_waitcnt lgkmcnt(0)\n"
    "v_cmp_le_u32 s[56:57], s59, v245\n"
    "s_add_u32 s58, s58, 1\n"
    "s_nop 1\n"
    "s_and_b64 s[54:55], s[54:55], s[56:57]\n"
    "s_cmp_eq_u64 s[54:55], -1\n"
    "s_cbranch_scc1 .Lpoll1r\n"
    "s_cmp_lt_u32 s58, 0x80000\n"
    "s_cbranch_scc1 .Lpoll1\n"
    "s_branch .Lpoll1r\n"
    ".Lpoll2:\n"
    "s_sleep 1\n"
    "v_cmp_eq_u64 s[54:55], v[236:237], v[238:239]\n"
    "s_nop 1\n"
    "s_cmp_eq_u64 s[54:55], -1\n"
    "s_cbranch_scc1 .Lpoll2l\n"
    "buffer_load_dwordx2 v[236:237], v241, s[36:39], s81 offen offset:0 sc1\n"
    "s_waitcnt vmcnt(0)\n"
    "v_cmp_eq_u64 s[54:55], v[236:237], v[238:239]\n"
    ".Lpoll2l:\n"
    "ds_read_b32 v245, v247\n"
    "s_waitcnt lgkmcnt(0)\n"
    "v_cmp_le_u32 s[56:57], s59, v245\n"
    "s_add_u32 s58, s58, 1\n"
    "s_nop 1\n"
    "s_and_b64 s[54:55], s[54:55], s[56:57]\n"
    "s_cmp_eq_u64 s[54:55], -1\n"
    "s_cbranch_scc1 .Lpoll2r\n"
    "s_cmp_lt_u32 s58, 0x80000\n"
    "s_cbranch_scc1 .Lpoll2\n"
    "s_branch .Lpoll2r\n"
    ".Lpoll3:\n"
    "s_sleep 1\n"
    "v_cmp_eq_u64 s[54:55], v[236:237], v[238:239]\n"
    "s_nop 1\n"
    "s_cmp_eq_u64 s[54:55], -1\n"
    "s_cbranch_scc1 .Lpoll3l\n"
    "buffer_load_dwordx2 v[236:237], v241, s[36:39], s81 offen offset:0 sc1\n"
    "s_waitcnt vmcnt(0)\n"
    "v_cmp_eq_u64 s[54:55], v[236:237], v[238:239]\n"
    ".Lpoll3l:\n"
    "ds_read_b32 v245, v247\n"
    "s_waitcnt lgkmcnt(0)\n"
    "v_cmp_le_u32 s[56:57], s59, v245\n"
    "s_add_u32 s58, s58, 1\n"
    "s_nop 1\n"
    "s_and_b64 s[54:55], s[54:55], s[56:57]\n"
    "s_cmp_eq_u64 s[54:55], -1\n"
    "s_cbranch_scc1 .Lpoll3r\n"
    "s_cmp_lt_u32 s58, 0x80000\n"
    "s_cbranch_scc1 .Lpoll3\n"
    "s_branch .Lpoll3r\n"
    ".Lpoll4:\n"
    "s_sleep 1\n"
    "v_cmp_eq_u64 s[54:55], v[236:237], v[238:239]\n"
    "s_nop 1\n"
    "s_cmp_eq_u64 s[54:55], -1\n"
    "s_cbranch_scc1 .Lpoll4l\n"
    "buffer_load_dwordx2 v[236:237], v241, s[36:39], s81 offen offset:0 sc1\n"
    "s_waitcnt vmcnt(0)\n"
    "v_cmp_eq_u64 s[54:55], v[236:237], v[238:239]\n"
    ".Lpoll4l:\n"
    "ds_read_b32 v245, v247\n"
    "s_waitcnt lgkmcnt(0)\n"
    "v_cmp_le_u32 s[56:57], s59, v245\n"
    "s_add_u32 s58, s58, 1\n"
    "s_nop 1\n"
    "s_and_b64 s[54:55], s[54:55], s[56:57]\n"
    "s_cmp_eq_u64 s[54:55], -1\n"
    "s_cbranch_scc1 .Lpoll4r\n"
    "s_cmp_lt_u32 s58, 0x80000\n"
    "s_cbranch_scc1 .Lpoll4\n"
    "s_branch .Lpoll4r\n"
    ".Lpoll5:\n"
    "s_sleep 1\n"
    "ds_read_b32 v245, v247\n"
    "s_waitcnt lgkmcnt(0)\n"
    "v_cmp_le_u32 s[56:57], s59, v245\n"
    "s_add_u32 s58, s58, 1\n"
    "s_nop 1\n"
    "s_cmp_eq_u64 s[56:57], -1\n"
    "s_cbranch_scc1 .Lpoll5r\n"
    "s_cmp_lt_u32 s58, 0x80000\n"
    "s_cbranch_scc1 .Lpoll5\n"
    "s_branch .Lpoll5r\n"
    ".Lend:\n"
    : "=&s"(bad), "+s"(ka) : "s"(b), "v"(tid)
    : "memory", "vcc", "s16", "s17", "s18", "s19", "s20", "s21", "s22", "s23", "s24", "s25", "s26", "s27", "s28", "s29", "s30", "s31", "s36", "s37", "s38", "s39", "s40", "s41", "s42", "s43", "s44", "s45", "s46", "s47", "s48", "s49", "s50", "s51", "s52", "s53", "s54", "s55", "s56", "s57", "s58", "s59", "s60", "s61", "s62", "s63", "s64", "s65", "s66", "s67", "s68", "s69", "s70", "s71", "s72", "s73", "s74", "s75", "s76", "s77", "s78", "s79", "s80", "s81", "s82", "s83", "s84", "s85", "s86", "s87", "s88", "s89", "s90", "s91", "s92", "s93", "s94", "s95", "s96", "s97", "s98", "s99", "v1", "v2", "v3", "v4", "v5", "v6", "v7", "v8", "v9", "v10", "v11", "v12", "v13", "v14", "v15", "v16", "v17", "v18", "v19", "v20", "v21", "v22", "v23", "v24", "v25", "v26", "v27", "v28", "v29", "v30", "v31", "v32", "v33", "v34", "v35", "v36", "v37", "v38", "v39", "v40", "v41", "v42", "v43", "v44", "v45", "v46", "v47", "v48", "v49", "v50", "v51", "v52", "v53", "v54", "v55", "v56", "v57", "v58", "v59", "v60", "v61", "v62", "v63", "v64", "v65", "v66", "v67", "v68", "v69", "v70", "v71", "v72", "v73", "v74", "v75", "v76", "v77", "v78", "v79", "v80", "v81", "v82", "v83", "v84", "v85", "v86", "v87", "v88", "v89", "v90", "v91", "v92", "v93", "v94", "v95", "v96", "v97", "v98", "v99", "v100", "v101", "v102", "v103", "v104", "v105", "v106", "v107", "v108", "v109", "v110", "v111", "v112", "v113", "v114", "v115", "v116", "v117", "v118", "v119", "v120", "v121", "v122", "v123", "v124", "v125", "v126", "v127", "v128", "v129", "v130", "v131", "v132", "v133", "v134", "v135", "v136", "v137", "v138", "v139", "v140", "v141", "v142", "v143", "v144", "v145", "v146", "v147", "v148", "v149", "v150", "v151", "v152", "v153", "v154", "v155", "v156", "v157", "v158", "v159", "v160", "v161", "v162", "v163", "v164", "v165", "v166", "v167", "v168", "v169", "v170", "v171", "v172", "v173", "v174", "v175", "v176", "v177", "v178", "v179", "v180", "v181", "v182", "v183", "v184", "v185", "v186", "v187", "v188", "v189", "v190", "v191", "v192", "v193", "v194", "v195", "v196", "v197", "v198", "v199", "v200", "v201", "v202", "v203", "v204", "v205", "v206", "v207", "v208", "v209", "v210", "v211", "v212", "v213", "v214", "v215", "v216", "v217", "v218", "v219", "v220", "v221", "v222", "v223", "v224", "v225", "v226", "v227", "v228", "v229", "v230", "v231", "v232", "v233", "v234", "v235", "v236", "v237", "v238", "v239", "v240", "v241", "v242", "v243", "v244", "v245", "v246", "v247", "v248", "v249", "v250", "v251", "v252", "v253", "v254", "v255", "a0", "a1", "a2", "a3", "a4", "a5", "a6", "a7", "a8", "a9", "a10", "a11", "a12", "a13", "a14", "a15", "a16", "a17", "a18", "a19", "a20", "a21", "a22", "a23", "a24", "a25", "a26", "a27", "a28", "a29", "a30", "a31", "a32", "a33", "a34", "a35", "a36", "a37", "a38", "a39", "a40", "a41", "a42", "a43", "a44", "a45", "a46", "a47", "a48", "a49", "a50", "a51", "a52", "a53", "a54", "a55", "a56", "a57", "a58", "a59", "a60", "a61", "a62", "a63", "a64", "a65", "a66", "a67", "a68", "a69", "a70", "a71", "a72", "a73", "a74", "a75", "a76", "a77", "a78", "a79", "a80", "a81", "a82", "a83", "a84", "a85", "a86", "a87", "a88", "a89", "a90", "a91", "a92", "a93", "a94", "a95", "a96", "a97", "a98", "a99", "a100", "a101", "a102", "a103", "a104", "a105", "a106", "a107", "a108", "a109", "a110", "a111", "a112", "a113", "a114", "a115", "a116", "a117", "a118", "a119", "a120", "a121", "a122", "a123", "a124", "a125", "a126", "a127", "a128", "a129", "a130", "a131", "a132", "a133", "a134", "a135", "a136", "a137", "a138", "a139", "a140", "a141", "a142", "a143", "a144", "a145", "a146", "a147", "a148", "a149", "a150", "a151", "a152", "a153", "a154", "a155", "a156", "a157", "a158", "a159", "a160", "a161", "a162", "a163", "a164", "a165", "a166", "a167", "a168", "a169", "a170", "a171", "a172", "a173", "a174", "a175", "a176", "a177", "a178", "a179", "a180", "a181", "a182", "a183", "a184", "a185", "a186", "a187", "a188", "a189", "a190", "a191", "a192", "a193", "a194", "a195", "a196", "a197", "a198", "a199", "a200", "a201", "a202", "a203", "a204", "a205", "a206", "a207", "a208", "a209", "a210", "a211", "a212", "a213", "a214", "a215", "a216", "a217", "a218", "a219", "a220", "a221", "a222", "a223", "a224", "a225", "a226", "a227", "a228", "a229", "a230", "a231", "a232", "a233", "a234", "a235", "a236", "a237", "a238", "a239", "a240", "a241", "a242", "a243", "a244", "a245", "a246", "a247", "a248", "a249", "a250", "a251", "a252", "a253", "a254", "a255");
  if (bad) {
    slow_rows((const Args*)ka, (b & 7) * 4 + ((b >> 3) & 3), (b >> 5) * 256 + (tid & 192), tid & 63);
  }
}

extern "C" void kernel_launch(void* const* d_in, const int* in_sizes, int n_in,
                              void* d_out, int out_size, void* d_ws, size_t ws_size,
                              hipStream_t stream) {
  (void)hipFuncSetAttribute((const void*)attn_fwd, hipFuncAttributeMaxDynamicSharedMemorySize, SHM);
  attn_fwd<<<256, 256, SHM, stream>>>((const unsigned short*)d_in[0], (const int*)d_in[1], (const int*)d_in[2], (const unsigned short*)d_in[3], (const unsigned short*)d_in[4],
                                      (const unsigned short*)d_in[5], (const unsigned short*)d_in[6], (char*)d_ws, (unsigned short*)d_out);
}
```

```cpp
#include <hip/hip_runtime.h>

constexpr int D = 192, NH = 32, S = 2048;
constexpr int SHM = 155968;

struct Args { const unsigned short* q; const int* qk; const int* qv; const unsigned short* ksc; const unsigned short* kzr;
              const unsigned short* vsc; const unsigned short* vzr; char* ws; unsigned short* o; };

__device__ __forceinline__ float bf2f(unsigned short x) { return __uint_as_float((unsigned)x << 16); }
__device__ __forceinline__ float rbf(float x) { return (float)(__bf16)x; }

__device__ __forceinline__ void slow_rows(const Args* a, unsigned h, unsigned row0, unsigned lane) {
  const unsigned short* q = a->q + ((size_t)h * S + row0) * D;
  unsigned short* o = a->o + ((size_t)h * S + row0) * D;
  const int* qk = a->qk + (size_t)h * S * D; const int* qv = a->qv + (size_t)h * S * D;
  const unsigned short* ks = a->ksc + h * 32 * D; const unsigned short* kz = a->kzr + h * 32 * D;
  const unsigned short* vs = a->vsc + h * S; const unsigned short* vz = a->vzr + h * S;
#pragma unroll 1
  for (int r = 0; r < 64; ++r) {
    float m = -INFINITY, l = 0.f, a0 = 0.f, a1 = 0.f, a2 = 0.f;
#pragma unroll 1
    for (int t = 0; t < 32; ++t) {
      const int* kr = qk + (size_t)(t * 64 + lane) * D;
      float s = 0.f;
#pragma unroll 4
      for (int d = 0; d < D; ++d) s = fmaf(bf2f(q[r * D + d]), rbf(rbf(rbf((float)kr[d]) - bf2f(kz[t * D + d])) * bf2f(ks[t * D + d])), s);
      s *= 0.07216878364870322f;
      float mx = s;
      for (int x = 32; x; x >>= 1) mx = fmaxf(mx, __shfl_xor(mx, x));
      const float mn = fmaxf(m, mx), c = __expf(m - mn), p = __expf(s - mn);
      float ps = p;
      for (int x = 32; x; x >>= 1) ps += __shfl_xor(ps, x);
      l = l * c + ps; a0 *= c; a1 *= c; a2 *= c; m = mn;
#pragma unroll 2
      for (int k = 0; k < 64; ++k) {
        const float pk = __shfl(p, k), z = bf2f(vz[t * 64 + k]), sc = bf2f(vs[t * 64 + k]);
        const int* vr = qv + (size_t)(t * 64 + k) * D + lane;
        a0 = fmaf(pk, rbf(rbf(rbf((float)vr[0]) - z) * sc), a0); a1 = fmaf(pk, rbf(rbf(rbf((float)vr[64]) - z) * sc), a1); a2 = fmaf(pk, rbf(rbf(rbf((float)vr[128]) - z) * sc), a2);
      }
    }
    const float i = 1.f / l;
    o[r * D + lane] = __builtin_bit_cast(unsigned short, (__bf16)(a0 * i)); o[r * D + 64 + lane] = __builtin_bit_cast(unsigned short, (__bf16)(a1 * i));
    o[r * D + 128 + lane] = __builtin_bit_cast(unsigned short, (__bf16)(a2 * i));
  }
}

__global__ __launch_bounds__(256, 1) void attn_fwd(const unsigned short* q, const int* qk, const int* qv, const unsigned short* ksc, const unsigned short* kzr,
                                                   const unsigned short* vsc, const unsigned short* vzr, char* ws, unsigned short* o) {
  unsigned long long ka = (unsigned long long)__builtin_amdgcn_kernarg_segment_ptr();
  unsigned bad, b = blockIdx.x;
  const unsigned tid = threadIdx.x;
  asm volatile(
    "s_load_dwordx2 s[28:29], %1, 8\n"
    "s_load_dwordx2 s[40:41], %1, 16\n"
    "s_load_dwordx2 s[44:45], %1, 24\n"
    "s_load_dwordx2 s[88:89], %1, 32\n"
    "s_load_dwordx2 s[36:37], %1, 56\n"
    "s_load_dwordx2 s[92:93], %1, 40\n"
    "s_load_dwordx2 s[96:97], %1, 48\n"
    "s_load_dwordx2 s[20:21], %1, 0\n"
    "s_load_dwordx2 s[16:17], %1, 56\n"
    "s_load_dwordx2 s[24:25], %1, 64\n"
    "v_readfirstlane_b32 s84, %3\n"
    "v_and_b32 v156, 63, %3\n"
    "s_and_b32 s52, %2, 7\n"
    "s_bfe_u32 s72, %2, 0x20003\n"
    "s_lshr_b32 s53, %2, 5\n"
    "s_lshl_b32 s52, s52, 2\n"
    "s_lshr_b32 s84, s84, 6\n"
    "s_or_b32 s52, s52, s72\n"
    "v_lshlrev_b32 v1, 4, v156\n"
    "s_getpc_b64 s[54:55]\n"
    ".Lpc:\n"
    "s_lshl_b32 s72, s72, 14\n"
    "v_and_b32 v246, 127, %3\n"
    "v_lshl_add_u32 v246, v246, 7, s72\n"
    "v_min_u32 v246, .Lend-.Lpc-4, v246\n"
    "global_load_dword v246, v246, s[54:55]\n"
    "s_mov_b32 s30, 1572864\n"
    "s_mov_b32 s31, 0x20000\n"
    "s_mov_b32 s42, 1572864\n"
    "s_mov_b32 s43, 0x20000\n"
    "s_mov_b32 s46, 12288\n"
    "s_mov_b32 s47, 0x20000\n"
    "s_mov_b32 s90, 12288\n"
    "s_mov_b32 s91, 0x20000\n"
    "s_lshr_b32 s79, s84, 1\n"
    "s_and_b32 s59, s84, 1\n"
    "s_cmp_ge_u32 s53, 6\n"
    "s_cselect_b32 s83, 1, 0\n"
    "s_or_b32 s83, s83, s84\n"
    "s_sub_u32 s58, 2, s79\n"
    "s_mul_i32 s71, s58, 49152\n"
    "s_mul_i32 s72, s52, 1572864\n"
    "s_mul_i32 s73, s52, 12288\n"
    "v_lshrrev_b32 v157, 2, v156\n"
    "v_and_b32 v158, 3, v156\n"
    "s_bitcmp1_b32 s83, 0\n"
    "s_cbranch_scc1 .Lf2b\n"
    "v_and_b32 v159, 32, v156\n"
    "v_mul_u32_u24 v242, 376, v159\n"
    "v_and_b32 v159, 16, v156\n"
    "v_mul_u32_u24 v159, 88, v159\n"
    "v_lshl_add_u32 v242, v156, 3, v242\n"
    "v_add_u32 v242, v159, v242\n"
    "s_lshl_b32 s63, s53, 7\n"
    "v_mul_u32_u24 v160, 768, v157\n"
    "v_lshl_add_u32 v160, v158, 4, v160\n"
    "s_and_b32 s81, s58, 1\n"
    "s_mul_i32 s81, s81, 384\n"
    "s_lshl_b32 s75, s53, 6\n"
    "s_add_u32 s81, s81, s75\n"
    "s_add_u32 s75, s71, s63\n"
    "s_add_u32 s76, s75, 6144\n"
    "s_add_u32 s77, s75, 24576\n"
    "s_add_u32 s78, s75, 30720\n"
    "s_waitcnt lgkmcnt(0)\n"
    "s_add_u32 s44, s44, s73\n"
    "s_addc_u32 s45, s45, 0\n"
    "s_add_u32 s88, s88, s73\n"
    "s_addc_u32 s89, s89, 0\n"
    "buffer_load_dwordx4 v[164:167], v160, s[44:47], s81 offen offset:0\n"
    "buffer_load_dwordx4 v[168:171], v160, s[88:91], s81 offen offset:0\n"
    "s_add_u32 s28, s28, s72\n"
    "s_addc_u32 s29, s29, 0\n"
    "buffer_load_dwordx2 v[204:205], v242, s[28:31], s75 offen offset:0 nt\n"
    "buffer_load_dwordx2 v[206:207], v242, s[28:31], s75 offen offset:768 nt\n"
    "buffer_load_dwordx2 v[208:209], v242, s[28:31], s75 offen offset:3072 nt\n"
    "buffer_load_dwordx2 v[210:211], v242, s[28:31], s75 offen offset:3840 nt\n"
    "buffer_load_dwordx2 v[212:213], v242, s[28:31], s76 offen offset:0 nt\n"
    "buffer_load_dwordx2 v[214:215], v242, s[28:31], s76 offen offset:768 nt\n"
    "buffer_load_dwordx2 v[216:217], v242, s[28:31], s76 offen offset:3072 nt\n"
    "buffer_load_dwordx2 v[218:219], v242, s[28:31], s76 offen offset:3840 nt\n"
    "buffer_load_dwordx2 v[220:221], v242, s[28:31], s77 offen offset:0 nt\n"
    "buffer_load_dwordx2 v[222:223], v242, s[28:31], s77 offen offset:768 nt\n"
    "buffer_load_dwordx2 v[224:225], v242, s[28:31], s77 offen offset:3072 nt\n"
    "buffer_load_dwordx2 v[226:227], v242, s[28:31], s77 offen offset:3840 nt\n"
    "buffer_load_dwordx2 v[228:229], v242, s[28:31], s78 offen offset:0 nt\n"
    "buffer_load_dwordx2 v[230:231], v242, s[28:31], s78 offen offset:768 nt\n"
    "buffer_load_dwordx2 v[232:233], v242, s[28:31], s78 offen offset:3072 nt\n"
    "buffer_load_dwordx2 v[234:235], v242, s[28:31], s78 offen offset:3840 nt\n"
    "s_add_u32 s75, s71, s63\n"
    "s_add_u32 s75, s75, 98304\n"
    "s_add_u32 s76, s75, 6144\n"
    "s_add_u32 s77, s75, 24576\n"
    "s_add_u32 s78, s75, 30720\n"
    "buffer_load_dwordx2 v[36:37], v242, s[28:31], s75 offen offset:0 nt\n"
    "buffer_load_dwordx2 v[38:39], v242, s[28:31], s75 offen offset:768 nt\n"
    "buffer_load_dwordx2 v[40:41], v242, s[28:31], s75 offen offset:3072 nt\n"
    "buffer_load_dwordx2 v[42:43], v242, s[28:31], s75 offen offset:3840 nt\n"
    "buffer_load_dwordx2 v[44:45], v242, s[28:31], s76 offen offset:0 nt\n"
    "buffer_load_dwordx2 v[46:47], v242, s[28:31], s76 offen offset:768 nt\n"
    "v_lshrrev_b32 v162, 1, v156\n"
    "v_and_b32 v162, 16, v162\n"
    "v_bfe_u32 v159, v156, 4, 1\n"
    "v_or_b32 v162, v162, v159\n"
    "v_and_or_b32 v162, v157, 2, v162\n"
    "v_mul_u32_u24 v162, 768, v162\n"
    "v_and_b32 v163, 0x70, v1\n"
    "v_add_u32 v162, v162, v163\n"
    "s_branch .Lf2e\n"
    ".Lf2b:\n"
    "v_and_b32 v159, 48, v156\n"
    "v_lshlrev_b32 v242, 2, v159\n"
    "v_lshl_add_u32 v243, v158, 4, v242\n"
    "v_and_b32 v159, 12, v156\n"
    "v_mul_u32_u24 v242, 384, v159\n"
    "v_add_u32 v242, v243, v242\n"
    "v_add_u32 v243, v243, v159\n"
    "s_mul_i32 s63, s53, 6144\n"
    "s_sub_u32 s63, s63, 49152\n"
    "s_cmp_eq_u32 s59, 0\n"
    "s_cselect_b32 s63, 0x7f000000, s63\n"
    "s_add_u32 s75, s71, s63\n"
    "s_waitcnt lgkmcnt(0)\n"
    "s_add_u32 s40, s40, s72\n"
    "s_addc_u32 s41, s41, 0\n"
    "buffer_load_dwordx4 v[204:207], v242, s[40:43], s75 offen offset:0 nt\n"
    "buffer_load_dwordx4 v[208:211], v242, s[40:43], s75 offen offset:768 nt\n"
    "buffer_load_dwordx4 v[212:215], v242, s[40:43], s75 offen offset:256 nt\n"
    "buffer_load_dwordx4 v[216:219], v242, s[40:43], s75 offen offset:1024 nt\n"
    "buffer_load_dwordx4 v[220:223], v242, s[40:43], s75 offen offset:512 nt\n"
    "buffer_load_dwordx4 v[224:227], v242, s[40:43], s75 offen offset:1280 nt\n"
    "s_add_u32 s75, s71, s63\n"
    "s_add_u32 s75, s75, 98304\n"
    "buffer_load_dwordx4 v[36:39], v242, s[40:43], s75 offen offset:0 nt\n"
    "buffer_load_dwordx4 v[40:43], v242, s[40:43], s75 offen offset:768 nt\n"
    "buffer_load_dwordx4 v[44:47], v242, s[40:43], s75 offen offset:256 nt\n"
    "s_add_u32 s44, s44, s73\n"
    "s_addc_u32 s45, s45, 0\n"
    "s_add_u32 s88, s88, s73\n"
    "s_addc_u32 s89, s89, 0\n"
    "s_add_u32 s28, s28, s72\n"
    "s_addc_u32 s29, s29, 0\n"
    "v_lshrrev_b32 v162, 1, v156\n"
    "v_and_b32 v162, 16, v162\n"
    "v_bfe_u32 v159, v156, 4, 1\n"
    "v_or_b32 v162, v162, v159\n"
    "v_and_or_b32 v162, v157, 2, v162\n"
    "v_mul_u32_u24 v162, 768, v162\n"
    "v_and_b32 v163, 0x70, v1\n"
    "v_add_u32 v162, v162, v163\n"
    ".Lf2e:\n"
    "s_nop 4\n"
    "v_lshlrev_b32 v247, 2, v156\n"
    "v_add_u32 v247, 155648, v247\n"
    "v_mov_b32 v159, 0\n"
    "ds_write_b32 v247, v159\n"
    "v_mov_b32 v159, 0xff800000\n"
    "v_mov_b32 v160, 155904\n"
    "ds_write_b32 v160, v159\n"
    "s_mov_b32 s38, 4864\n"
    "s_mov_b32 s39, 0x20000\n"
    "s_mov_b32 s94, 4096\n"
    "s_mov_b32 s95, 0x20000\n"
    "s_mov_b32 s98, 4096\n"
    "s_mov_b32 s99, 0x20000\n"
    "s_mov_b32 s22, 98304\n"
    "s_mov_b32 s23, 0x20000\n"
    "s_mov_b32 s18, 1622016\n"
    "s_mov_b32 s19, 0x20000\n"
    "s_mov_b32 s26, 98304\n"
    "s_mov_b32 s27, 0x20000\n"
    "s_lshl_b32 s80, s58, 7\n"
    "s_add_u32 s80, s80, 256\n"
    "s_lshl_b32 s74, s52, 13\n"
    "s_mul_i32 s69, s59, 384\n"
    "s_mul_i32 s57, s79, 6144\n"
    "s_add_u32 s57, s57, s69\n"
    "s_add_u32 s66, s57, 24576\n"
    "s_add_u32 s36, s36, s74\n"
    "s_addc_u32 s37, s37, 0\n"
    "s_lshr_b32 s74, s74, 1\n"
    "s_add_u32 s92, s92, s74\n"
    "s_addc_u32 s93, s93, 0\n"
    "s_add_u32 s96, s96, s74\n"
    "s_addc_u32 s97, s97, 0\n"
    "v_lshlrev_b32 v128, 3, v156\n"
    "v_mov_b32 v129, 0\n"
    "v_lshlrev_b32 v130, 4, %3\n"
    "buffer_load_dwordx2 v[124:125], v128, s[44:47], 0 offen offset:0\n"
    "buffer_load_dwordx2 v[126:127], v128, s[88:91], 0 offen offset:0\n"
    "buffer_load_dwordx2 v[238:239], v129, s[36:39], 0 offen offset:0 sc1\n"
    "s_mul_i32 s74, s52, 786432\n"
    "s_mul_i32 s73, s53, 98304\n"
    "s_add_u32 s74, s74, s73\n"
    "s_add_u32 s20, s20, s74\n"
    "s_addc_u32 s21, s21, 0\n"
    "s_add_u32 s24, s24, s74\n"
    "s_addc_u32 s25, s25, 0\n"
    "s_mul_i32 s72, s52, 1626112\n"
    "s_add_u32 s72, s72, 262144\n"
    "s_add_u32 s16, s16, s72\n"
    "s_addc_u32 s17, s17, 0\n"
    "s_mul_i32 s65, s84, 6144\n"
    "s_mul_i32 s85, s84, 24576\n"
    "s_waitcnt lgkmcnt(0)\n"
    "s_barrier\n"
    "s_add_u32 s73, s65, 49152\n"
    "v_add_u32 v131, s73, v1\n"
    "s_add_u32 s73, s73, s69\n"
    "v_add_u32 v132, s73, v163\n"
    "v_and_b32 v159, 32, v156\n"
    "v_lshlrev_b32 v244, 3, v156\n"
    "v_xor_b32 v244, v244, v159\n"
    "v_lshl_add_u32 v244, v159, 3, v244\n"
    "s_bitcmp1_b32 s83, 0\n"
    "s_cbranch_scc1 .Lf5b\n"
    "s_lshl_b32 s60, s53, 11\n"
    "s_add_u32 s61, s60, 12288\n"
    "s_lshl_b32 s73, s58, 8\n"
    "s_add_u32 s73, s73, 122880\n"
    "v_and_b32 v243, 0xf0, v1\n"
    "v_add_u32 v243, s73, v243\n"
    "s_and_b32 s73, s73, 0x1fd00\n"
    "v_lshlrev_b32 v160, 9, v157\n"
    "v_lshl_add_u32 v160, v158, 6, v160\n"
    "v_add_u32 v160, s73, v160\n"
    "s_lshl_b32 s73, s79, 8\n"
    "s_mul_i32 s74, s59, 6144\n"
    "s_add_u32 s73, s73, s74\n"
    "v_add_u32 v163, s73, v244\n"
    "v_and_b32 v159, 32, v156\n"
    "v_lshlrev_b32 v244, 2, v156\n"
    "v_xor_b32 v244, v244, v159\n"
    "v_mad_u32_u24 v244, v159, 12, v244\n"
    "buffer_load_dwordx2 v[48:49], v242, s[28:31], s76 offen offset:3072 nt\n"
    "buffer_load_dwordx2 v[50:51], v242, s[28:31], s76 offen offset:3840 nt\n"
    "v_add_u32 v159, 0, v156\n"
    "v_mul_u32_u24 v159, 2731, v159\n"
    "v_lshrrev_b32 v159, 16, v159\n"
    "v_xor_b32 v159, v159, v156\n"
    "v_and_b32 v159, 7, v159\n"
    "v_and_b32 v88, 0x380, v1\n"
    "v_lshl_add_u32 v88, v159, 4, v88\n"
    "v_add_u32 v159, 64, v156\n"
    "v_mul_u32_u24 v159, 2731, v159\n"
    "v_lshrrev_b32 v159, 16, v159\n"
    "v_xor_b32 v159, v159, v156\n"
    "v_and_b32 v159, 7, v159\n"
    "v_and_b32 v89, 0x380, v1\n"
    "v_lshl_add_u32 v89, v159, 4, v89\n"
    "v_add_u32 v159, 128, v156\n"
    "v_mul_u32_u24 v159, 2731, v159\n"
    "v_lshrrev_b32 v159, 16, v159\n"
    "v_xor_b32 v159, v159, v156\n"
    "v_and_b32 v159, 7, v159\n"
    "v_and_b32 v90, 0x380, v1\n"
    "v_lshl_add_u32 v90, v159, 4, v90\n"
    "v_and_b32 v2, 7, v156\n"
    "v_lshrrev_b32 v144, 3, %3\n"
    "v_mul_u32_u24 v144, 768, v144\n"
    "v_lshl_add_u32 v144, v2, 3, v144\n"
    "v_add_u32 v144, 131072, v144\n"
    "v_mad_u32_u24 v86, v2, 24, v144\n"
    "v_and_b32 v84, 15, v156\n"
    "v_lshrrev_b32 v159, 4, v156\n"
    "v_xor_b32 v159, v159, v2\n"
    "v_mul_u32_u24 v84, 384, v84\n"
    "v_lshl_add_u32 v84, v159, 4, v84\n"
    "s_add_u32 s73, s65, 49152\n"
    "v_add_u32 v84, s73, v84\n"
    "v_xor_b32 v85, 64, v84\n"
    "v_subrev_u32 v154, 24576, v84\n"
    "v_subrev_u32 v155, 24576, v85\n"
    "s_add_u32 s67, s65, 98304\n"
    "s_add_u32 s68, s65, 24576\n"
    "s_add_u32 s69, s65, 73728\n"
    "s_mov_b32 s81, 256\n"
    "s_mul_i32 s74, s79, 24576\n"
    "s_add_u32 s70, s74, s65\n"
    "s_add_u32 s70, s70, 73728\n"
    "s_sub_u32 s64, s65, s74\n"
    "s_add_u32 s64, s64, 98304\n"
    "s_add_u32 s73, s74, 73728\n"
    "v_add_u32 v248, s73, v1\n"
    "buffer_load_dwordx2 v[52:53], v242, s[28:31], s77 offen offset:0 nt\n"
    "buffer_load_dwordx2 v[54:55], v242, s[28:31], s77 offen offset:768 nt\n"
    "s_sub_u32 s73, 98304, s74\n"
    "v_add_u32 v249, s73, v1\n"
    "s_or_b32 s73, s53, s84\n"
    "s_cmp_eq_u32 s73, 0\n"
    "s_cselect_b32 s82, 0, 0x7fff0000\n"
    "v_and_b32 v252, 12, v156\n"
    "v_bfe_u32 v159, v156, 1, 1\n"
    "v_or_b32 v252, v252, v159\n"
    "v_and_b32 v159, 1, v156\n"
    "v_lshl_or_b32 v252, v159, 1, v252\n"
    "v_lshlrev_b32 v252, 6, v252\n"
    "v_lshlrev_b32 v159, 2, v156\n"
    "v_and_b32 v159, 32, v159\n"
    "v_xor_b32 v159, v159, v156\n"
    "v_and_or_b32 v252, v159, 48, v252\n"
    "v_mov_b32 v2, v252\n"
    "v_add_u32 v3, 24576, v252\n"
    "v_and_b32 v87, 0x70, v1\n"
    "v_lshl_add_u32 v87, v87, 2, 64\n"
    "v_add_u32 v87, 64, v87\n"
    "v_and_b32 v241, 0xf0, v1\n"
    "v_lshrrev_b32 v241, 1, v241\n"
    "v_add_u32 v241, 640, v241\n"
    "v_lshrrev_b32 v91, 3, v156\n"
    "v_lshlrev_b32 v91, 24, v91\n"
    "v_lshl_add_u32 v91, v156, 3, v91\n"
    "s_lshl_b32 s73, s53, 4\n"
    "s_lshl_b32 s74, s59, 3\n"
    "s_add_u32 s73, s73, s74\n"
    "s_add_u32 s73, s73, 640\n"
    "v_mov_b32 v240, 0x7fff0000\n"
    "v_writelane_b32 v240, s73, 0\n"
    "v_and_b32 v159, 48, v156\n"
    "s_mul_i32 s73, s79, 768\n"
    "s_sub_u32 s73, 131072, s73\n"
    "v_lshlrev_b32 v250, 1, v159\n"
    "v_add_u32 v250, s73, v250\n"
    "v_and_b32 v251, 3, v156\n"
    "v_lshl_add_u32 v251, v251, 7, v159\n"
    "s_add_u32 s73, s73, 256\n"
    "v_add_u32 v251, s73, v251\n"
    "v_and_b32 v159, 64, v1\n"
    "v_add_u32 v251, v159, v251\n"
    "v_bfe_i32 v159, v156, 2, 1\n"
    "v_and_b32 v159, 0xaaaa, v159\n"
    "v_or_b32 v253, 0x4444, v159\n"
    "s_mov_b32 s58, 0\n"
    "s_mov_b32 s59, 8\n"
    "s_waitcnt vmcnt(29)\n"
    "v_lshlrev_b32 v180, 16, v164\n"
    "v_and_b32 v181, 0xffff0000, v164\n"
    "v_lshlrev_b32 v182, 16, v168\n"
    "v_and_b32 v183, 0xffff0000, v168\n"
    "v_lshlrev_b32 v184, 16, v165\n"
    "v_and_b32 v185, 0xffff0000, v165\n"
    "v_lshlrev_b32 v186, 16, v169\n"
    "v_and_b32 v187, 0xffff0000, v169\n"
    "v_lshlrev_b32 v188, 16, v166\n"
    "v_and_b32 v189, 0xffff0000, v166\n"
    "v_lshlrev_b32 v190, 16, v170\n"
    "v_and_b32 v191, 0xffff0000, v170\n"
    "v_lshlrev_b32 v192, 16, v167\n"
    "v_and_b32 v193, 0xffff0000, v167\n"
    "v_lshlrev_b32 v194, 16, v171\n"
    "v_and_b32 v195, 0xffff0000, v171\n"
    "v_mul_f32 v180, 0x3dd53b95, v180\n"
    "v_mul_f32 v181, 0x3dd53b95, v181\n"
    "v_mul_f32 v184, 0x3dd53b95, v184\n"
    "v_mul_f32 v185, 0x3dd53b95, v185\n"
    "v_mul_f32 v188, 0x3dd53b95, v188\n"
    "v_mul_f32 v189, 0x3dd53b95, v189\n"
    "v_mul_f32 v192, 0x3dd53b95, v192\n"
    "v_mul_f32 v193, 0x3dd53b95, v193\n"
    "v_mul_f32 v182, -v182, v180\n"
    "v_mul_f32 v183, -v183, v181\n"
    "v_mul_f32 v186, -v186, v184\n"
    "v_mul_f32 v187, -v187, v185\n"
    "v_mul_f32 v190, -v190, v188\n"
    "v_mul_f32 v191, -v191, v189\n"
    "v_mul_f32 v194, -v194, v192\n"
    "v_mul_f32 v195, -v195, v193\n"
    "ds_write_b128 v160, v[180:183] offset:0\n"
    "ds_write_b128 v160, v[184:187] offset:16\n"
    "ds_write_b128 v160, v[188:191] offset:32\n"
    "ds_write_b128 v160, v[192:195] offset:48\n"
    "buffer_load_dwordx2 v[56:57], v242, s[28:31], s77 offen offset:3072 nt\n"
    "buffer_load_dwordx2 v[58:59], v242, s[28:31], s77 offen offset:3840 nt\n"
    "buffer_load_dwordx4 v[136:139], v130, s[92:95], 0 offen offset:0\n"
    "buffer_load_dwordx4 v[140:143], v130, s[96:99], 0 offen offset:0\n"
    "v_accvgpr_write_b32 a0, 0\n"
    "v_accvgpr_write_b32 a1, 0\n"
    "v_accvgpr_write_b32 a2, 0\n"
    "v_accvgpr_write_b32 a3, 0\n"
    "v_accvgpr_write_b32 a4, 0\n"
    "v_accvgpr_write_b32 a5, 0\n"
    "v_accvgpr_write_b32 a6, 0\n"
    "v_accvgpr_write_b32 a7, 0\n"
    "v_accvgpr_write_b32 a8, 0\n"
    "v_accvgpr_write_b32 a9, 0\n"
    "v_accvgpr_write_b32 a10, 0\n"
    "v_accvgpr_write_b32 a11, 0\n"
    "v_accvgpr_write_b32 a12, 0\n"
    "v_accvgpr_write_b32 a13, 0\n"
    "v_accvgpr_write_b32 a14, 0\n"
    "v_accvgpr_write_b32 a15, 0\n"
    "v_accvgpr_write_b32 a16, 0\n"
    "v_accvgpr_write_b32 a17, 0\n"
    "v_accvgpr_write_b32 a18, 0\n"
    "v_accvgpr_write_b32 a19, 0\n"
    "v_accvgpr_write_b32 a20, 0\n"
    "v_accvgpr_write_b32 a21, 0\n"
    "v_accvgpr_write_b32 a22, 0\n"
    "v_accvgpr_write_b32 a23, 0\n"
    "v_accvgpr_write_b32 a24, 0\n"
    "v_accvgpr_write_b32 a25, 0\n"
    "v_accvgpr_write_b32 a26, 0\n"
    "v_accvgpr_write_b32 a27, 0\n"
    "v_accvgpr_write_b32 a28, 0\n"
    "v_accvgpr_write_b32 a29, 0\n"
    "v_accvgpr_write_b32 a30, 0\n"
    "v_accvgpr_write_b32 a31, 0\n"
    "v_accvgpr_write_b32 a32, 0\n"
    "v_accvgpr_write_b32 a33, 0\n"
    "v_accvgpr_write_b32 a34, 0\n"
    "v_accvgpr_write_b32 a35, 0\n"
    "v_accvgpr_write_b32 a36, 0\n"
    "v_accvgpr_write_b32 a37, 0\n"
    "v_accvgpr_write_b32 a38, 0\n"
    "v_accvgpr_write_b32 a39, 0\n"
    "v_accvgpr_write_b32 a40, 0\n"
    "v_accvgpr_write_b32 a41, 0\n"
    "v_accvgpr_write_b32 a42, 0\n"
    "v_accvgpr_write_b32 a43, 0\n"
    "v_accvgpr_write_b32 a44, 0\n"
    "v_accvgpr_write_b32 a45, 0\n"
    "v_accvgpr_write_b32 a46, 0\n"
    "v_accvgpr_write_b32 a47, 0\n"
    "v_accvgpr_write_b32 a48, 0\n"
    "v_accvgpr_write_b32 a49, 0\n"
    "v_accvgpr_write_b32 a50, 0\n"
    "v_accvgpr_write_b32 a51, 0\n"
    "v_accvgpr_write_b32 a52, 0\n"
    "v_accvgpr_write_b32 a53, 0\n"
    "v_accvgpr_write_b32 a54, 0\n"
    "v_accvgpr_write_b32 a55, 0\n"
    "v_accvgpr_write_b32 a56, 0\n"
    "v_accvgpr_write_b32 a57, 0\n"
    "v_accvgpr_write_b32 a58, 0\n"
    "v_accvgpr_write_b32 a59, 0\n"
    "s_waitcnt vmcnt(17)\n"
    "s_add_u32 s54, s71, s60\n"
    "s_add_u32 s55, s71, s61\n"
    "v_cvt_f32_i32 v204, v204\n"
    "v_cvt_f32_i32 v205, v205\n"
    "v_cvt_f32_i32 v206, v206\n"
    "v_cvt_f32_i32 v207, v207\n"
    "v_cvt_f32_i32 v208, v208\n"
    "v_cvt_f32_i32 v209, v209\n"
    "v_cvt_f32_i32 v210, v210\n"
    "v_cvt_f32_i32 v211, v211\n"
    "v_cvt_f32_i32 v212, v212\n"
    "v_cvt_f32_i32 v213, v213\n"
    "v_cvt_f32_i32 v214, v214\n"
    "v_cvt_f32_i32 v215, v215\n"
    "ds_read_b128 v[92:95], v243 offset:0\n"
    "v_cvt_f32_i32 v216, v216\n"
    "v_cvt_f32_i32 v217, v217\n"
    "v_cvt_f32_i32 v218, v218\n"
    "v_cvt_f32_i32 v219, v219\n"
    "v_cvt_f32_i32 v220, v220\n"
    "v_cvt_f32_i32 v221, v221\n"
    "v_cvt_f32_i32 v222, v222\n"
    "v_cvt_f32_i32 v223, v223\n"
    "v_cvt_f32_i32 v224, v224\n"
    "v_cvt_f32_i32 v225, v225\n"
    "v_cvt_f32_i32 v226, v226\n"
    "v_cvt_f32_i32 v227, v227\n"
    "s_waitcnt lgkmcnt(0)\n"
    "v_fma_f32 v204, v204, v92, v94\n"
    "v_fma_f32 v205, v205, v93, v95\n"
    "v_fma_f32 v206, v206, v92, v94\n"
    "v_fma_f32 v207, v207, v93, v95\n"
    "v_cvt_pk_bf16_f32 v204, v204, v205\n"
    "buffer_load_dwordx2 v[60:61], v242, s[28:31], s78 offen offset:0 nt\n"
    "buffer_load_dwordx2 v[62:63], v242, s[28:31], s78 offen offset:768 nt\n"
    "buffer_load_dwordx4 v[4:7], v162, s[28:31], s57 offen offset:0\n"
    "buffer_load_dwordx4 v[8:11], v162, s[28:31], s57 offen offset:3072\n"
    "v_cvt_pk_bf16_f32 v206, v206, v207\n"
    "v_cvt_f32_i32 v228, v228\n"
    "v_cvt_f32_i32 v229, v229\n"
    "v_cvt_f32_i32 v230, v230\n"
    "v_cvt_f32_i32 v231, v231\n"
    "buffer_store_dword v204, v244, s[16:19], s54 offen offset:0 sc1\n"
    "buffer_store_dword v206, v244, s[16:19], s54 offen offset:128 sc1\n"
    "v_fma_f32 v208, v208, v92, v94\n"
    "v_fma_f32 v209, v209, v93, v95\n"
    "v_fma_f32 v210, v210, v92, v94\n"
    "v_fma_f32 v211, v211, v93, v95\n"
    "v_cvt_pk_bf16_f32 v208, v208, v209\n"
    "v_cvt_pk_bf16_f32 v210, v210, v211\n"
    "v_cvt_f32_i32 v232, v232\n"
    "v_cvt_f32_i32 v233, v233\n"
    "v_cvt_f32_i32 v234, v234\n"
    "v_cvt_f32_i32 v235, v235\n"
    "buffer_store_dword v208, v244, s[16:19], s54 offen offset:1024 sc1\n"
    "buffer_store_dword v210, v244, s[16:19], s54 offen offset:1152 sc1\n"
    "v_fma_f32 v212, v212, v92, v94\n"
    "v_fma_f32 v213, v213, v93, v95\n"
    "v_fma_f32 v214, v214, v92, v94\n"
    "v_fma_f32 v215, v215, v93, v95\n"
    "v_cvt_pk_bf16_f32 v212, v212, v213\n"
    "v_cvt_pk_bf16_f32 v214, v214, v215\n"
    "buffer_store_dword v212, v244, s[16:19], s54 offen offset:256 sc1\n"
    "buffer_store_dword v214, v244, s[16:19], s54 offen offset:384 sc1\n"
    "v_fma_f32 v216, v216, v92, v94\n"
    "v_fma_f32 v217, v217, v93, v95\n"
    "v_fma_f32 v218, v218, v92, v94\n"
    "v_fma_f32 v219, v219, v93, v95\n"
    "v_cvt_pk_bf16_f32 v216, v216, v217\n"
    "v_cvt_pk_bf16_f32 v218, v218, v219\n"
    "buffer_load_dwordx2 v[64:65], v242, s[28:31], s78 offen offset:3072 nt\n"
    "buffer_load_dwordx2 v[66:67], v242, s[28:31], s78 offen offset:3840 nt\n"
    "buffer_load_dwordx4 v[12:15], v162, s[28:31], s57 offen offset:128\n"
    "buffer_store_dword v216, v244, s[16:19], s54 offen offset:1280 sc1\n"
    "buffer_store_dword v218, v244, s[16:19], s54 offen offset:1408 sc1\n"
    "v_fma_f32 v220, v220, v92, v94\n"
    "v_fma_f32 v221, v221, v93, v95\n"
    "v_fma_f32 v222, v222, v92, v94\n"
    "v_fma_f32 v223, v223, v93, v95\n"
    "v_cvt_pk_bf16_f32 v220, v220, v221\n"
    "v_cvt_pk_bf16_f32 v222, v222, v223\n"
    "buffer_store_dword v220, v244, s[16:19], s55 offen offset:0 sc1\n"
    "buffer_store_dword v222, v244, s[16:19], s55 offen offset:128 sc1\n"
    "v_fma_f32 v224, v224, v92, v94\n"
    "v_fma_f32 v225, v225, v93, v95\n"
    "v_fma_f32 v226, v226, v92, v94\n"
    "v_fma_f32 v227, v227, v93, v95\n"
    "v_cvt_pk_bf16_f32 v224, v224, v225\n"
    "v_cvt_pk_bf16_f32 v226, v226, v227\n"
    "buffer_store_dword v224, v244, s[16:19], s55 offen offset:1024 sc1\n"
    "buffer_store_dword v226, v244, s[16:19], s55 offen offset:1152 sc1\n"
    "v_fma_f32 v228, v228, v92, v94\n"
    "v_fma_f32 v229, v229, v93, v95\n"
    "v_fma_f32 v230, v230, v92, v94\n"
    "v_fma_f32 v231, v231, v93, v95\n"
    "v_cvt_pk_bf16_f32 v228, v228, v229\n"
    "v_cvt_pk_bf16_f32 v230, v230, v231\n"
    "buffer_store_dword v228, v244, s[16:19], s55 offen offset:256 sc1\n"
    "buffer_store_dword v230, v244, s[16:19], s55 offen offset:384 sc1\n"
    "v_fma_f32 v232, v232, v92, v94\n"
    "v_fma_f32 v233, v233, v93, v95\n"
    "v_fma_f32 v234, v234, v92, v94\n"
    "v_fma_f32 v235, v235, v93, v95\n"
    "v_cvt_pk_bf16_f32 v232, v232, v233\n"
    "v_cvt_pk_bf16_f32 v234, v234, v235\n"
    "buffer_store_dword v232, v244, s[16:19], s55 offen offset:1280 sc1\n"
    "buffer_store_dword v234, v244, s[16:19], s55 offen offset:1408 sc1\n"
    "v_add_u32 v243, 512, v243\n"
    "s_add_u32 s71, s71, 98304\n"
    "s_add_u32 m0, s65, 24576\n"
    "s_add_u32 s72, s85, 0\n"
    "buffer_load_dwordx4 v88, s[20:23], s72 offen offset:0 nt lds\n"
    "buffer_load_dwordx4 v[16:19], v162, s[28:31], s57 offen offset:3200\n"
    "buffer_load_dwordx4 v[20:23], v162, s[28:31], s57 offen offset:256\n"
    "s_waitcnt vmcnt(34)\n"
    "v_add_co_u32 v238, vcc, 0x7f4a7c15, v238\n"
    "v_mov_b32 v159, 0x9e3779b9\n"
    "s_nop 0\n"
    "v_addc_co_u32 v239, vcc, v159, v239, vcc\n"
    "v_mov_b32 v246, 1\n"
    "s_waitcnt vmcnt(35)\n"
    "v_lshlrev_b32 v180, 16, v124\n"
    "v_and_b32 v181, 0xffff0000, v124\n"
    "v_lshlrev_b32 v182, 16, v125\n"
    "v_and_b32 v183, 0xffff0000, v125\n"
    "v_lshlrev_b32 v184, 16, v126\n"
    "v_and_b32 v185, 0xffff0000, v126\n"
    "v_lshlrev_b32 v186, 16, v127\n"
    "v_and_b32 v187, 0xffff0000, v127\n"
    "v_mul_f32 v180, 0x3dd53b95, v180\n"
    "v_mul_f32 v181, 0x3dd53b95, v181\n"
    "v_mul_f32 v182, 0x3dd53b95, v182\n"
    "v_mul_f32 v183, 0x3dd53b95, v183\n"
    "v_mul_f32 v184, -v184, v180\n"
    "v_mul_f32 v185, -v185, v181\n"
    "v_mul_f32 v186, -v186, v182\n"
    "v_mul_f32 v187, -v187, v183\n"
    "ds_write_b128 v131, v[180:183] offset:0\n"
    "ds_write_b128 v131, v[184:187] offset:1024\n"
    "ds_read_b128 v[180:183], v132 offset:0\n"
    "ds_read_b128 v[184:187], v132 offset:1024\n"
    "ds_read_b128 v[188:191], v132 offset:128\n"
    "ds_read_b128 v[192:195], v132 offset:1152\n"
    "ds_read_b128 v[196:199], v132 offset:256\n"
    "ds_read_b128 v[200:203], v132 offset:1280\n"
    "v_accvgpr_write_b32 a60, 0\n"
    "v_accvgpr_write_b32 a61, 0\n"
    "v_accvgpr_write_b32 a62, 0\n"
    "v_accvgpr_write_b32 a63, 0\n"
    "v_accvgpr_write_b32 a64, 0\n"
    "v_accvgpr_write_b32 a65, 0\n"
    "v_accvgpr_write_b32 a66, 0\n"
    "v_accvgpr_write_b32 a67, 0\n"
    "v_accvgpr_write_b32 a68, 0\n"
    "v_accvgpr_write_b32 a69, 0\n"
    "s_add_u32 m0, s65, 25600\n"
    "s_add_u32 s72, s85, 1024\n"
    "buffer_load_dwordx4 v89, s[20:23], s72 offen offset:0 nt lds\n"
    "buffer_load_dwordx4 v[24:27], v162, s[28:31], s57 offen offset:3328\n"
    "v_accvgpr_write_b32 a70, 0\n"
    "v_accvgpr_write_b32 a71, 0\n"
    "v_accvgpr_write_b32 a72, 0\n"
    "v_accvgpr_write_b32 a73, 0\n"
    "v_accvgpr_write_b32 a74, 0\n"
    "v_accvgpr_write_b32 a75, 0\n"
    "v_accvgpr_write_b32 a76, 0\n"
    "v_accvgpr_write_b32 a77, 0\n"
    "v_accvgpr_write_b32 a78, 0\n"
    "v_accvgpr_write_b32 a79, 0\n"
    "v_accvgpr_write_b32 a80, 0\n"
    "v_accvgpr_write_b32 a81, 0\n"
    "v_accvgpr_write_b32 a82, 0\n"
    "v_accvgpr_write_b32 a83, 0\n"
    "v_accvgpr_write_b32 a84, 0\n"
    "v_accvgpr_write_b32 a85, 0\n"
    "v_accvgpr_write_b32 a86, 0\n"
    "v_accvgpr_write_b32 a87, 0\n"
    "v_accvgpr_write_b32 a88, 0\n"
    "v_accvgpr_write_b32 a89, 0\n"
    "v_accvgpr_write_b32 a90, 0\n"
    "v_accvgpr_write_b32 a91, 0\n"
    "v_accvgpr_write_b32 a92, 0\n"
    "v_accvgpr_write_b32 a93, 0\n"
    "v_accvgpr_write_b32 a94, 0\n"
    "v_accvgpr_write_b32 a95, 0\n"
    "v_accvgpr_write_b32 a96, 0\n"
    "v_accvgpr_write_b32 a97, 0\n"
    "v_accvgpr_write_b32 a98, 0\n"
    "v_accvgpr_write_b32 a99, 0\n"
    "v_accvgpr_write_b32 a100, 0\n"
    "v_accvgpr_write_b32 a101, 0\n"
    "v_accvgpr_write_b32 a102, 0\n"
    "v_accvgpr_write_b32 a103, 0\n"
    "v_accvgpr_write_b32 a104, 0\n"
    "v_accvgpr_write_b32 a105, 0\n"
    "v_accvgpr_write_b32 a106, 0\n"
    "v_accvgpr_write_b32 a107, 0\n"
    "v_accvgpr_write_b32 a108, 0\n"
    "v_accvgpr_write_b32 a109, 0\n"
    "v_accvgpr_write_b32 a110, 0\n"
    "s_add_u32 m0, s65, 26624\n"
    "s_add_u32 s72, s85, 2048\n"
    "buffer_load_dwordx4 v90, s[20:23], s72 offen offset:0 nt lds\n"
    "buffer_load_dwordx4 v[100:103], v162, s[28:31], s66 offen offset:0\n"
    "buffer_load_dwordx4 v[104:107], v162, s[28:31], s66 offen offset:3072\n"
    "v_accvgpr_write_b32 a111, 0\n"
    "v_accvgpr_write_b32 a112, 0\n"
    "v_accvgpr_write_b32 a113, 0\n"
    "v_accvgpr_write_b32 a114, 0\n"
    "v_accvgpr_write_b32 a115, 0\n"
    "v_accvgpr_write_b32 a116, 0\n"
    "v_accvgpr_write_b32 a117, 0\n"
    "v_accvgpr_write_b32 a118, 0\n"
    "v_accvgpr_write_b32 a119, 0\n"
    "v_accvgpr_write_b32 a120, 0\n"
    "v_accvgpr_write_b32 a121, 0\n"
    "v_accvgpr_write_b32 a122, 0\n"
    "v_accvgpr_write_b32 a123, 0\n"
    "v_accvgpr_write_b32 a124, 0\n"
    "v_accvgpr_write_b32 a125, 0\n"
    "v_accvgpr_write_b32 a126, 0\n"
    "v_accvgpr_write_b32 a127, 0\n"
    "v_accvgpr_write_b32 a128, 0\n"
    "v_accvgpr_write_b32 a129, 0\n"
    "v_accvgpr_write_b32 a130, 0\n"
    "v_accvgpr_write_b32 a131, 0\n"
    "v_accvgpr_write_b32 a132, 0\n"
    "v_accvgpr_write_b32 a133, 0\n"
    "v_accvgpr_write_b32 a134, 0\n"
    "v_accvgpr_write_b32 a135, 0\n"
    "v_accvgpr_write_b32 a136, 0\n"
    "v_accvgpr_write_b32 a137, 0\n"
    "v_accvgpr_write_b32 a138, 0\n"
    "v_accvgpr_write_b32 a139, 0\n"
    "v_accvgpr_write_b32 a140, 0\n"
    "v_accvgpr_write_b32 a141, 0\n"
    "v_accvgpr_write_b32 a142, 0\n"
    "v_accvgpr_write_b32 a143, 0\n"
    "v_accvgpr_write_b32 a144, 0\n"
    "v_accvgpr_write_b32 a145, 0\n"
    "v_accvgpr_write_b32 a146, 0\n"
    "v_accvgpr_write_b32 a147, 0\n"
    "v_accvgpr_write_b32 a148, 0\n"
    "v_accvgpr_write_b32 a149, 0\n"
    "v_accvgpr_write_b32 a150, 0\n"
    "s_add_u32 m0, s65, 27648\n"
    "s_add_u32 s72, s85, 3072\n"
    "buffer_load_dwordx4 v88, s[20:23], s72 offen offset:0 nt lds\n"
    "buffer_load_dwordx4 v[108:111], v162, s[28:31], s66 offen offset:128\n"
    "v_accvgpr_write_b32 a151, 0\n"
    "v_accvgpr_write_b32 a152, 0\n"
    "v_accvgpr_write_b32 a153, 0\n"
    "v_accvgpr_write_b32 a154, 0\n"
    "v_accvgpr_write_b32 a155, 0\n"
    "v_accvgpr_write_b32 a156, 0\n"
    "v_accvgpr_write_b32 a157, 0\n"
    "v_accvgpr_write_b32 a158, 0\n"
    "v_accvgpr_write_b32 a159, 0\n"
    "v_accvgpr_write_b32 a160, 0\n"
    "v_accvgpr_write_b32 a161, 0\n"
    "v_accvgpr_write_b32 a162, 0\n"
    "v_accvgpr_write_b32 a163, 0\n"
    "v_accvgpr_write_b32 a164, 0\n"
    "v_accvgpr_write_b32 a165, 0\n"
    "v_accvgpr_write_b32 a166, 0\n"
    "v_accvgpr_write_b32 a167, 0\n"
    "v_accvgpr_write_b32 a168, 0\n"
    "v_accvgpr_write_b32 a169, 0\n"
    "v_accvgpr_write_b32 a170, 0\n"
    "v_accvgpr_write_b32 a171, 0\n"
    "v_accvgpr_write_b32 a172, 0\n"
    "v_accvgpr_write_b32 a173, 0\n"
    "v_accvgpr_write_b32 a174, 0\n"
    "v_accvgpr_write_b32 a175, 0\n"
    "v_accvgpr_write_b32 a176, 0\n"
    "v_accvgpr_write_b32 a177, 0\n"
    "v_accvgpr_write_b32 a178, 0\n"
    "v_accvgpr_write_b32 a179, 0\n"
    "v_accvgpr_write_b32 a180, 0\n"
    "v_accvgpr_write_b32 a181, 0\n"
    "v_accvgpr_write_b32 a182, 0\n"
    "v_accvgpr_write_b32 a183, 0\n"
    "v_accvgpr_write_b32 a184, 0\n"
    "v_accvgpr_write_b32 a185, 0\n"
    "v_accvgpr_write_b32 a186, 0\n"
    "v_accvgpr_write_b32 a187, 0\n"
    "v_accvgpr_write_b32 a188, 0\n"
    "v_accvgpr_write_b32 a189, 0\n"
    "v_accvgpr_write_b32 a190, 0\n"
    "v_accvgpr_write_b32 a191, 0\n"
    "s_waitcnt lgkmcnt(0)\n"
    "s_waitcnt vmcnt(10)\n"
    "ds_add_rtn_u32 v245, v247, v246\n"
    "s_lshl_b32 s73, s53, 6\n"
    "s_add_u32 s73, s73, 128\n"
    "s_waitcnt lgkmcnt(0)\n"
    "v_readfirstlane_b32 s74, v245\n"
    "s_nop 0\n"
    "s_cmp_eq_u32 s74, 3\n"
    "s_cselect_b32 s73, s73, 0x7fff0000\n"
    "buffer_store_dwordx2 v[238:239], v91, s[36:39], s73 offen offset:0 sc1\n"
    "s_waitcnt vmcnt(22)\n"
    "s_add_u32 m0, s65, 28672\n"
    "s_add_u32 s72, s85, 4096\n"
    "buffer_load_dwordx4 v89, s[20:23], s72 offen offset:0 nt lds\n"
    "buffer_load_dwordx4 v[112:115], v162, s[28:31], s66 offen offset:3200\n"
    "buffer_load_dwordx4 v[116:119], v162, s[28:31], s66 offen offset:256\n"
    "s_add_u32 s54, s71, s60\n"
    "s_add_u32 s55, s71, s61\n"
    "v_cvt_f32_i32 v36, v36\n"
    "v_cvt_f32_i32 v37, v37\n"
    "v_cvt_f32_i32 v38, v38\n"
    "v_cvt_f32_i32 v39, v39\n"
    "v_cvt_f32_i32 v40, v40\n"
    "v_cvt_f32_i32 v41, v41\n"
    "v_cvt_f32_i32 v42, v42\n"
    "v_cvt_f32_i32 v43, v43\n"
    "v_cvt_f32_i32 v44, v44\n"
    "v_cvt_f32_i32 v45, v45\n"
    "v_cvt_f32_i32 v46, v46\n"
    "v_cvt_f32_i32 v47, v47\n"
    "ds_read_b128 v[92:95], v243 offset:0\n"
    "v_cvt_f32_i32 v48, v48\n"
    "v_cvt_f32_i32 v49, v49\n"
    "v_cvt_f32_i32 v50, v50\n"
    "v_cvt_f32_i32 v51, v51\n"
    "v_cvt_f32_i32 v52, v52\n"
    "v_cvt_f32_i32 v53, v53\n"
    "v_cvt_f32_i32 v54, v54\n"
    "v_cvt_f32_i32 v55, v55\n"
    "v_cvt_f32_i32 v56, v56\n"
    "v_cvt_f32_i32 v57, v57\n"
    "v_cvt_f32_i32 v58, v58\n"
    "v_cvt_f32_i32 v59, v59\n"
    "s_waitcnt lgkmcnt(0)\n"
    "v_fma_f32 v36, v36, v92, v94\n"
    "v_fma_f32 v37, v37, v93, v95\n"
    "v_fma_f32 v38, v38, v92, v94\n"
    "v_fma_f32 v39, v39, v93, v95\n"
    "v_cvt_pk_bf16_f32 v36, v36, v37\n"
    "v_cvt_pk_bf16_f32 v38, v38, v39\n"
    "v_cvt_f32_i32 v60, v60\n"
    "v_cvt_f32_i32 v61, v61\n"
    "v_cvt_f32_i32 v62, v62\n"
    "v_cvt_f32_i32 v63, v63\n"
    "buffer_store_dword v36, v244, s[16:19], s54 offen offset:0 sc1\n"
    "buffer_store_dword v38, v244, s[16:19], s54 offen offset:128 sc1\n"
    "v_fma_f32 v40, v40, v92, v94\n"
    "v_fma_f32 v41, v41, v93, v95\n"
    "v_fma_f32 v42, v42, v92, v94\n"
    "v_fma_f32 v43, v43, v93, v95\n"
    "v_cvt_pk_bf16_f32 v40, v40, v41\n"
    "v_cvt_pk_bf16_f32 v42, v42, v43\n"
    "v_cvt_f32_i32 v64, v64\n"
    "v_cvt_f32_i32 v65, v65\n"
    "v_cvt_f32_i32 v66, v66\n"
    "v_cvt_f32_i32 v67, v67\n"
    "s_add_u32 m0, s65, 29696\n"
    "s_add_u32 s72, s85, 5120\n"
    "buffer_load_dwordx4 v90, s[20:23], s72 offen offset:0 nt lds\n"
    "buffer_load_dwordx4 v[120:123], v162, s[28:31], s66 offen offset:3328\n"
    "buffer_store_dword v40, v244, s[16:19], s54 offen offset:1024 sc1\n"
    "buffer_store_dword v42, v244, s[16:19], s54 offen offset:1152 sc1\n"
    "v_fma_f32 v44, v44, v92, v94\n"
    "v_fma_f32 v45, v45, v93, v95\n"
    "v_fma_f32 v46, v46, v92, v94\n"
    "v_fma_f32 v47, v47, v93, v95\n"
    "v_cvt_pk_bf16_f32 v44, v44, v45\n"
    "v_cvt_pk_bf16_f32 v46, v46, v47\n"
    "buffer_store_dword v44, v244, s[16:19], s54 offen offset:256 sc1\n"
    "buffer_store_dword v46, v244, s[16:19], s54 offen offset:384 sc1\n"
    "v_fma_f32 v48, v48, v92, v94\n"
    "v_fma_f32 v49, v49, v93, v95\n"
    "v_fma_f32 v50, v50, v92, v94\n"
    "v_fma_f32 v51, v51, v93, v95\n"
    "v_cvt_pk_bf16_f32 v48, v48, v49\n"
    "v_cvt_pk_bf16_f32 v50, v50, v51\n"
    "buffer_store_dword v48, v244, s[16:19], s54 offen offset:1280 sc1\n"
    "buffer_store_dword v50, v244, s[16:19], s54 offen offset:1408 sc1\n"
    "v_fma_f32 v52, v52, v92, v94\n"
    "v_fma_f32 v53, v53, v93, v95\n"
    "v_fma_f32 v54, v54, v92, v94\n"
    "v_fma_f32 v55, v55, v93, v95\n"
    "v_cvt_pk_bf16_f32 v52, v52, v53\n"
    "v_cvt_pk_bf16_f32 v54, v54, v55\n"
    "buffer_store_dword v52, v244, s[16:19], s55 offen offset:0 sc1\n"
    "buffer_store_dword v54, v244, s[16:19], s55 offen offset:128 sc1\n"
    "v_fma_f32 v56, v56, v92, v94\n"
    "v_fma_f32 v57, v57, v93, v95\n"
    "v_fma_f32 v58, v58, v92, v94\n"
    "v_fma_f32 v59, v59, v93, v95\n"
    "v_cvt_pk_bf16_f32 v56, v56, v57\n"
    "v_cvt_pk_bf16_f32 v58, v58, v59\n"
    "buffer_store_dword v56, v244, s[16:19], s55 offen offset:1024 sc1\n"
    "buffer_store_dword v58, v244, s[16:19], s55 offen offset:1152 sc1\n"
    "v_fma_f32 v60, v60, v92, v94\n"
    "v_fma_f32 v61, v61, v93, v95\n"
    "v_fma_f32 v62, v62, v92, v94\n"
    "v_fma_f32 v63, v63, v93, v95\n"
    "v_cvt_pk_bf16_f32 v60, v60, v61\n"
    "v_cvt_pk_bf16_f32 v62, v62, v63\n"
    "buffer_store_dword v60, v244, s[16:19], s55 offen offset:256 sc1\n"
    "buffer_store_dword v62, v244, s[16:19], s55 offen offset:384 sc1\n"
    "v_fma_f32 v64, v64, v92, v94\n"
    "v_fma_f32 v65, v65, v93, v95\n"
    "v_fma_f32 v66, v66, v92, v94\n"
    "v_fma_f32 v67, v67, v93, v95\n"
    "v_cvt_pk_bf16_f32 v64, v64, v65\n"
    "v_cvt_pk_bf16_f32 v66, v66, v67\n"
    "buffer_store_dword v64, v244, s[16:19], s55 offen offset:1280 sc1\n"
    "buffer_store_dword v66, v244, s[16:19], s55 offen offset:1408 sc1\n"
    "v_add_u32 v243, 512, v243\n"
    "s_add_u32 m0, s65, 73728\n"
    "s_add_u32 s72, s85, 6144\n"
    "buffer_load_dwordx4 v88, s[20:23], s72 offen offset:0 nt lds\n"
    "s_waitcnt vmcnt(56)\n"
    "v_lshlrev_b32 v204, 16, v136\n"
    "v_and_b32 v205, 0xffff0000, v136\n"
    "v_lshlrev_b32 v206, 16, v137\n"
    "v_and_b32 v207, 0xffff0000, v137\n"
    "v_lshlrev_b32 v208, 16, v138\n"
    "v_and_b32 v209, 0xffff0000, v138\n"
    "v_lshlrev_b32 v210, 16, v139\n"
    "v_and_b32 v211, 0xffff0000, v139\n"
    "v_log_f32 v68, v204\n"
    "v_log_f32 v69, v205\n"
    "v_log_f32 v70, v206\n"
    "v_log_f32 v71, v207\n"
    "v_log_f32 v72, v208\n"
    "v_log_f32 v73, v209\n"
    "v_log_f32 v74, v210\n"
    "v_log_f32 v75, v211\n"
    "v_rcp_f32 v204, v204\n"
    "v_rcp_f32 v205, v205\n"
    "v_rcp_f32 v206, v206\n"
    "v_rcp_f32 v207, v207\n"
    "v_rcp_f32 v208, v208\n"
    "v_rcp_f32 v209, v209\n"
    "v_rcp_f32 v210, v210\n"
    "v_rcp_f32 v211, v211\n"
    "s_mov_b32 s73, 0x41a00000\n"
    "v_mov_b32 v228, 0x7fc00000\n"
    "v_cmp_le_f32 vcc, |v68|, s73\n"
    "s_nop 1\n"
    "v_cndmask_b32 v68, v228, v68, vcc\n"
    "v_cmp_le_f32 vcc, |v69|, s73\n"
    "s_nop 1\n"
    "v_cndmask_b32 v69, v228, v69, vcc\n"
    "v_cmp_le_f32 vcc, |v70|, s73\n"
    "s_nop 1\n"
    "v_cndmask_b32 v70, v228, v70, vcc\n"
    "v_cmp_le_f32 vcc, |v71|, s73\n"
    "s_nop 1\n"
    "v_cndmask_b32 v71, v228, v71, vcc\n"
    "s_add_u32 m0, s65, 74752\n"
    "s_add_u32 s72, s85, 7168\n"
    "buffer_load_dwordx4 v89, s[20:23], s72 offen offset:0 nt lds\n"
    "v_cmp_le_f32 vcc, |v72|, s73\n"
    "s_nop 1\n"
    "v_cndmask_b32 v72, v228, v72, vcc\n"
    "v_cmp_le_f32 vcc, |v73|, s73\n"
    "s_nop 1\n"
    "v_cndmask_b32 v73, v228, v73, vcc\n"
    "v_cmp_le_f32 vcc, |v74|, s73\n"
    "s_nop 1\n"
    "v_cndmask_b32 v74, v228, v74, vcc\n"
    "v_cmp_le_f32 vcc, |v75|, s73\n"
    "s_nop 1\n"
    "v_cndmask_b32 v75, v228, v75, vcc\n"
    "v_cvt_pk_bf16_f32 v220, v204, v205\n"
    "v_cvt_pk_bf16_f32 v221, v206, v207\n"
    "v_cvt_pk_bf16_f32 v222, v208, v209\n"
    "v_cvt_pk_bf16_f32 v223, v210, v211\n"
    "v_lshlrev_b32 v212, 16, v220\n"
    "v_and_b32 v213, 0xffff0000, v220\n"
    "v_lshlrev_b32 v214, 16, v221\n"
    "v_and_b32 v215, 0xffff0000, v221\n"
    "v_lshlrev_b32 v216, 16, v222\n"
    "v_and_b32 v217, 0xffff0000, v222\n"
    "v_lshlrev_b32 v218, 16, v223\n"
    "v_and_b32 v219, 0xffff0000, v223\n"
    "v_sub_f32 v204, v204, v212\n"
    "v_sub_f32 v205, v205, v213\n"
    "v_sub_f32 v206, v206, v214\n"
    "v_sub_f32 v207, v207, v215\n"
    "v_sub_f32 v208, v208, v216\n"
    "v_sub_f32 v209, v209, v217\n"
    "v_sub_f32 v210, v210, v218\n"
    "v_sub_f32 v211, v211, v219\n"
    "v_cvt_pk_bf16_f32 v224, v204, v205\n"
    "v_cvt_pk_bf16_f32 v225, v206, v207\n"
    "v_cvt_pk_bf16_f32 v226, v208, v209\n"
    "v_cvt_pk_bf16_f32 v227, v210, v211\n"
    "v_mov_b32 v204, 0\n"
    "v_mov_b32 v205, 0\n"
    "v_mov_b32 v206, 0\n"
    "s_add_u32 m0, s65, 75776\n"
    "s_add_u32 s72, s85, 8192\n"
    "buffer_load_dwordx4 v90, s[20:23], s72 offen offset:0 nt lds\n"
    "v_mov_b32 v207, 0\n"
    "v_swap_b32 v221, v222\n"
    "v_swap_b32 v225, v226\n"
    "v_swap_b32 v141, v142\n"
    "ds_write2_b64 v144, v[220:221], v[222:223] offset0:32 offset1:40\n"
    "ds_write2_b64 v144, v[224:225], v[226:227] offset0:48 offset1:56\n"
    "ds_write2_b64 v144, v[140:141], v[142:143] offset0:64 offset1:72\n"
    "ds_write2_b64 v144, v[204:205], v[206:207] offset0:80 offset1:88\n"
    "s_waitcnt vmcnt(30)\n"
    "v_cvt_f32_i32 v4, v4\n"
    "v_cvt_f32_i32 v5, v5\n"
    "v_cvt_f32_i32 v6, v6\n"
    "v_cvt_f32_i32 v7, v7\n"
    "v_cvt_f32_i32 v8, v8\n"
    "v_cvt_f32_i32 v9, v9\n"
    "v_cvt_f32_i32 v10, v10\n"
    "v_cvt_f32_i32 v11, v11\n"
    "v_cvt_f32_i32 v12, v12\n"
    "v_cvt_f32_i32 v13, v13\n"
    "v_cvt_f32_i32 v14, v14\n"
    "v_cvt_f32_i32 v15, v15\n"
    "v_cvt_f32_i32 v16, v16\n"
    "v_cvt_f32_i32 v17, v17\n"
    "v_cvt_f32_i32 v18, v18\n"
    "v_cvt_f32_i32 v19, v19\n"
    "v_cvt_f32_i32 v20, v20\n"
    "v_cvt_f32_i32 v21, v21\n"
    "v_cvt_f32_i32 v22, v22\n"
    "v_cvt_f32_i32 v23, v23\n"
    "v_cvt_f32_i32 v24, v24\n"
    "v_cvt_f32_i32 v25, v25\n"
    "v_cvt_f32_i32 v26, v26\n"
    "v_cvt_f32_i32 v27, v27\n"
    "v_fma_f32 v4, v4, v180, v184\n"
    "v_fma_f32 v5, v5, v181, v185\n"
    "v_fma_f32 v6, v6, v182, v186\n"
    "s_add_u32 m0, s65, 76800\n"
    "s_add_u32 s72, s85, 9216\n"
    "buffer_load_dwordx4 v88, s[20:23], s72 offen offset:0 nt lds\n"
    "v_fma_f32 v7, v7, v183, v187\n"
    "v_fma_f32 v8, v8, v180, v184\n"
    "v_fma_f32 v9, v9, v181, v185\n"
    "v_fma_f32 v10, v10, v182, v186\n"
    "v_fma_f32 v11, v11, v183, v187\n"
    "v_cvt_pk_bf16_f32 v4, v4, v5\n"
    "v_cvt_pk_bf16_f32 v5, v6, v7\n"
    "v_cvt_pk_bf16_f32 v6, v8, v9\n"
    "v_cvt_pk_bf16_f32 v7, v10, v11\n"
    "ds_write_b64 v163, v[4:5] offset:0\n"
    "ds_write_b64 v163, v[6:7] offset:1024\n"
    "v_fma_f32 v12, v12, v188, v192\n"
    "v_fma_f32 v13, v13, v189, v193\n"
    "v_fma_f32 v14, v14, v190, v194\n"
    "v_fma_f32 v15, v15, v191, v195\n"
    "v_fma_f32 v16, v16, v188, v192\n"
    "v_fma_f32 v17, v17, v189, v193\n"
    "v_fma_f32 v18, v18, v190, v194\n"
    "v_fma_f32 v19, v19, v191, v195\n"
    "v_cvt_pk_bf16_f32 v12, v12, v13\n"
    "v_cvt_pk_bf16_f32 v13, v14, v15\n"
    "v_cvt_pk_bf16_f32 v14, v16, v17\n"
    "v_cvt_pk_bf16_f32 v15, v18, v19\n"
    "ds_write_b64 v163, v[12:13] offset:2048\n"
    "ds_write_b64 v163, v[14:15] offset:3072\n"
    "v_fma_f32 v20, v20, v196, v200\n"
    "v_fma_f32 v21, v21, v197, v201\n"
    "v_fma_f32 v22, v22, v198, v202\n"
    "v_fma_f32 v23, v23, v199, v203\n"
    "v_fma_f32 v24, v24, v196, v200\n"
    "v_fma_f32 v25, v25, v197, v201\n"
    "v_fma_f32 v26, v26, v198, v202\n"
    "v_fma_f32 v27, v27, v199, v203\n"
    "v_cvt_pk_bf16_f32 v20, v20, v21\n"
    "v_cvt_pk_bf16_f32 v21, v22, v23\n"
    "s_add_u32 m0, s65, 77824\n"
    "s_add_u32 s72, s85, 10240\n"
    "buffer_load_dwordx4 v89, s[20:23], s72 offen offset:0 nt lds\n"
    "v_cvt_pk_bf16_f32 v22, v24, v25\n"
    "v_cvt_pk_bf16_f32 v23, v26, v27\n"
    "ds_write_b64 v163, v[20:21] offset:4096\n"
    "ds_write_b64 v163, v[22:23] offset:5120\n"
    "s_waitcnt vmcnt(19)\n"
    "v_cvt_f32_i32 v100, v100\n"
    "v_cvt_f32_i32 v101, v101\n"
    "v_cvt_f32_i32 v102, v102\n"
    "v_cvt_f32_i32 v103, v103\n"
    "v_cvt_f32_i32 v104, v104\n"
    "v_cvt_f32_i32 v105, v105\n"
    "v_cvt_f32_i32 v106, v106\n"
    "v_cvt_f32_i32 v107, v107\n"
    "v_cvt_f32_i32 v108, v108\n"
    "v_cvt_f32_i32 v109, v109\n"
    "v_cvt_f32_i32 v110, v110\n"
    "v_cvt_f32_i32 v111, v111\n"
    "v_cvt_f32_i32 v112, v112\n"
    "v_cvt_f32_i32 v113, v113\n"
    "v_cvt_f32_i32 v114, v114\n"
    "v_cvt_f32_i32 v115, v115\n"
    "v_cvt_f32_i32 v116, v116\n"
    "v_cvt_f32_i32 v117, v117\n"
    "v_cvt_f32_i32 v118, v118\n"
    "v_cvt_f32_i32 v119, v119\n"
    "v_cvt_f32_i32 v120, v120\n"
    "v_cvt_f32_i32 v121, v121\n"
    "v_cvt_f32_i32 v122, v122\n"
    "v_cvt_f32_i32 v123, v123\n"
    "v_fma_f32 v100, v100, v180, v184\n"
    "v_fma_f32 v101, v101, v181, v185\n"
    "v_fma_f32 v102, v102, v182, v186\n"
    "v_fma_f32 v103, v103, v183, v187\n"
    "v_fma_f32 v104, v104, v180, v184\n"
    "v_fma_f32 v105, v105, v181, v185\n"
    "s_add_u32 m0, s65, 78848\n"
    "s_add_u32 s72, s85, 11264\n"
    "buffer_load_dwordx4 v90, s[20:23], s72 offen offset:0 nt lds\n"
    "v_fma_f32 v106, v106, v182, v186\n"
    "v_fma_f32 v107, v107, v183, v187\n"
    "v_cvt_pk_bf16_f32 v100, v100, v101\n"
    "v_cvt_pk_bf16_f32 v101, v102, v103\n"
    "v_cvt_pk_bf16_f32 v102, v104, v105\n"
    "v_cvt_pk_bf16_f32 v103, v106, v107\n"
    "ds_write_b64 v163, v[100:101] offset:12288\n"
    "ds_write_b64 v163, v[102:103] offset:13312\n"
    "v_fma_f32 v108, v108, v188, v192\n"
    "v_fma_f32 v109, v109, v189, v193\n"
    "v_fma_f32 v110, v110, v190, v194\n"
    "v_fma_f32 v111, v111, v191, v195\n"
    "v_fma_f32 v112, v112, v188, v192\n"
    "v_fma_f32 v113, v113, v189, v193\n"
    "v_fma_f32 v114, v114, v190, v194\n"
    "v_fma_f32 v115, v115, v191, v195\n"
    "v_cvt_pk_bf16_f32 v108, v108, v109\n"
    "v_cvt_pk_bf16_f32 v109, v110, v111\n"
    "v_cvt_pk_bf16_f32 v110, v112, v113\n"
    "v_cvt_pk_bf16_f32 v111, v114, v115\n"
    "ds_write_b64 v163, v[108:109] offset:14336\n"
    "ds_write_b64 v163, v[110:111] offset:15360\n"
    "v_fma_f32 v116, v116, v196, v200\n"
    "v_fma_f32 v117, v117, v197, v201\n"
    "v_fma_f32 v118, v118, v198, v202\n"
    "v_fma_f32 v119, v119, v199, v203\n"
    "v_fma_f32 v120, v120, v196, v200\n"
    "v_fma_f32 v121, v121, v197, v201\n"
    "v_fma_f32 v122, v122, v198, v202\n"
    "v_fma_f32 v123, v123, v199, v203\n"
    "v_cvt_pk_bf16_f32 v116, v116, v117\n"
    "v_cvt_pk_bf16_f32 v117, v118, v119\n"
    "v_cvt_pk_bf16_f32 v118, v120, v121\n"
    "v_cvt_pk_bf16_f32 v119, v122, v123\n"
    "ds_write_b64 v163, v[116:117] offset:16384\n"
    "ds_write_b64 v163, v[118:119] offset:17408\n"
    "s_waitcnt vmcnt(21)\n"
    "ds_read_b128 a[192:195], v154 offset:0\n"
    "ds_read_b128 a[196:199], v155 offset:0\n"
    "ds_read_b128 a[200:203], v154 offset:128\n"
    "ds_read_b128 a[204:207], v155 offset:128\n"
    "ds_read_b128 a[208:211], v154 offset:256\n"
    "ds_read_b128 a[212:215], v155 offset:256\n"
    "s_waitcnt lgkmcnt(0)\n"
    "s_barrier\n"
    "ds_read_b128 v[156:159], v2 offset:0\n"
    "ds_read_b128 v[160:163], v2 offset:2048\n"
    "ds_read_b128 v[164:167], v2 offset:4096\n"
    "ds_read_b128 v[168:171], v2 offset:6144\n"
    "ds_read_b128 v[172:175], v2 offset:8192\n"
    "ds_read_b128 v[176:179], v2 offset:10240\n"
    "ds_read_b128 v[180:183], v2 offset:1024\n"
    "ds_read_b128 v[184:187], v2 offset:3072\n"
    "ds_read_b128 v[188:191], v2 offset:5120\n"
    "ds_read_b128 v[192:195], v2 offset:7168\n"
    "ds_read_b128 v[196:199], v2 offset:9216\n"
    "ds_read_b128 v[200:203], v2 offset:11264\n"
    "s_add_u32 m0, s65, 49152\n"
    "s_add_u32 s72, s85, 12288\n"
    "buffer_load_dwordx4 v88, s[20:23], s72 offen offset:0 nt lds\n"
    "v_mov_b32 v108, 0\n"
    "v_mov_b32 v109, 0\n"
    "v_mov_b32 v110, 0\n"
    "v_mov_b32 v111, 0\n"
    "v_mov_b32 v112, 0\n"
    "v_mov_b32 v113, 0\n"
    "v_mov_b32 v114, 0\n"
    "v_mov_b32 v115, 0\n"
    "v_mov_b32 v116, 0\n"
    "v_mov_b32 v117, 0\n"
    "v_mov_b32 v118, 0\n"
    "v_mov_b32 v119, 0\n"
    "v_mov_b32 v120, 0\n"
    "v_mov_b32 v121, 0\n"
    "v_mov_b32 v122, 0\n"
    "v_mov_b32 v123, 0\n"
    "s_waitcnt lgkmcnt(11)\n"
    "v_mfma_f32_16x16x32_bf16 v[4:7], v[156:159], a[192:195], 0\n"
    "s_waitcnt lgkmcnt(10)\n"
    "v_mfma_f32_16x16x32_bf16 v[4:7], v[160:163], a[196:199], v[4:7]\n"
    "s_waitcnt lgkmcnt(9)\n"
    "v_mfma_f32_16x16x32_bf16 v[4:7], v[164:167], a[200:203], v[4:7]\n"
    "s_waitcnt lgkmcnt(8)\n"
    "v_mfma_f32_16x16x32_bf16 v[4:7], v[168:171], a[204:207], v[4:7]\n"
    "s_waitcnt lgkmcnt(7)\n"
    "v_mfma_f32_16x16x32_bf16 v[4:7], v[172:175], a[208:211], v[4:7]\n"
    "s_waitcnt lgkmcnt(6)\n"
    "v_mfma_f32_16x16x32_bf16 v[4:7], v[176:179], a[212:215], v[4:7]\n"
    "s_waitcnt lgkmcnt(5)\n"
    "v_mfma_f32_16x16x32_bf16 v[20:23], v[180:183], a[192:195], 0\n"
    "s_waitcnt lgkmcnt(4)\n"
    "v_mfma_f32_16x16x32_bf16 v[20:23], v[184:187], a[196:199], v[20:23]\n"
    "s_waitcnt lgkmcnt(3)\n"
    "v_mfma_f32_16x16x32_bf16 v[20:23], v[188:191], a[200:203], v[20:23]\n"
    "s_waitcnt lgkmcnt(2)\n"
    "v_mfma_f32_16x16x32_bf16 v[20:23], v[192:195], a[204:207], v[20:23]\n"
    "s_waitcnt lgkmcnt(1)\n"
    "v_mfma_f32_16x16x32_bf16 v[20:23], v[196:199], a[208:211], v[20:23]\n"
    "s_waitcnt lgkmcnt(0)\n"
    "v_mfma_f32_16x16x32_bf16 v[20:23], v[200:203], a[212:215], v[20:23]\n"
    "s_add_u32 m0, s65, 50176\n"
    "s_add_u32 s72, s85, 13312\n"
    "buffer_load_dwordx4 v89, s[20:23], s72 offen offset:0 nt lds\n"
    "v_mov_b32 v77, 155904\n"
    "v_max3_f32 v76, v4, v5, v6\n"
    "s_nop 2\n"
    "v_max3_f32 v76, v76, v7, v20\n"
    "v_max3_f32 v76, v76, v21, v22\n"
    "v_max_f32 v76, v76, v23\n"
    "v_mov_b32 v78, v76\n"
    "s_nop 1\n"
    "v_permlane32_swap_b32 v76, v78\n"
    "v_max_f32 v76, v76, v78\n"
    "v_mov_b32 v78, v76\n"
    "s_nop 1\n"
    "v_permlane16_swap_b32 v76, v78\n"
    "v_max_f32 v76, v76, v78\n"
    "ds_max_f32 v77, v76\n"
    "s_add_u32 m0, s65, 51200\n"
    "s_add_u32 s72, s85, 14336\n"
    "buffer_load_dwordx4 v90, s[20:23], s72 offen offset:0 nt lds\n"
    "buffer_load_dwordx2 v[236:237], v87, s[36:39], 0 offen offset:0 sc1\n"
    "s_waitcnt lgkmcnt(0)\n"
    "s_barrier\n"
    "ds_read_b32 v76, v77\n"
    "s_waitcnt vmcnt(10)\n"
    "buffer_store_dwordx2 v[238:239], v240, s[36:39], s80 offen offset:0 sc1\n"
    "s_lshl_b32 s73, s79, 8\n"
    "s_add_u32 s80, s80, s73\n"
    "s_mul_i32 s73, s79, 98304\n"
    "s_add_u32 s71, s71, s73\n"
    "s_waitcnt lgkmcnt(0)\n"
    "v_sub_f32 v68, v68, v76\n"
    "v_sub_f32 v69, v69, v76\n"
    "v_sub_f32 v70, v70, v76\n"
    "v_sub_f32 v71, v71, v76\n"
    "v_sub_f32 v72, v72, v76\n"
    "v_sub_f32 v73, v73, v76\n"
    "v_sub_f32 v74, v74, v76\n"
    "v_sub_f32 v75, v75, v76\n"
    "ds_write_b128 v86, v[68:71] offset:0\n"
    "ds_write_b128 v86, v[72:75] offset:16\n"
    "s_mul_i32 s73, s79, 768\n"
    "v_add_u32 v77, s73, v250\n"
    "s_add_u32 m0, s65, 52224\n"
    "s_add_u32 s72, s85, 15360\n"
    "buffer_load_dwordx4 v88, s[20:23], s72 offen offset:0 nt lds\n"
    "s_waitcnt lgkmcnt(0)\n"
    "s_barrier\n"
    "ds_read_b128 v[100:103], v77 offset:0\n"
    "ds_read_b128 v[104:107], v77 offset:16\n"
    "s_add_u32 m0, s65, 53248\n"
    "s_add_u32 s72, s85, 16384\n"
    "buffer_load_dwordx4 v89, s[20:23], s72 offen offset:0 nt lds\n"
    "s_waitcnt lgkmcnt(1)\n"
    "v_add_f32 v4, v4, v100\n"
    "v_add_f32 v5, v5, v101\n"
    "v_add_f32 v6, v6, v102\n"
    "v_add_f32 v7, v7, v103\n"
    "s_waitcnt lgkmcnt(0)\n"
    "v_add_f32 v20, v20, v104\n"
    "v_add_f32 v21, v21, v105\n"
    "v_add_f32 v22, v22, v106\n"
    "v_add_f32 v23, v23, v107\n"
    "v_exp_f32 v4, v4\n"
    "v_exp_f32 v5, v5\n"
    "v_exp_f32 v6, v6\n"
    "v_exp_f32 v7, v7\n"
    "v_exp_f32 v20, v20\n"
    "v_exp_f32 v21, v21\n"
    "v_exp_f32 v22, v22\n"
    "v_exp_f32 v23, v23\n"
    "s_add_u32 m0, s65, 54272\n"
    "s_add_u32 s72, s85, 17408\n"
    "buffer_load_dwordx4 v90, s[20:23], s72 offen offset:0 nt lds\n"
    "s_waitcnt vmcnt(8)\n"
    "ds_read_b128 a[216:219], v84 offset:24576\n"
    "ds_read_b128 a[220:223], v85 offset:24576\n"
    "ds_read_b128 a[224:227], v84 offset:24704\n"
    "ds_read_b128 a[228:231], v85 offset:24704\n"
    "ds_read_b128 a[232:235], v84 offset:24832\n"
    "ds_read_b128 a[236:239], v85 offset:24832\n"
    "s_waitcnt lgkmcnt(0)\n"
    "v_mfma_f32_16x16x32_bf16 v[8:11], v[176:179], a[236:239], v[100:103]\n"
    "v_mfma_f32_16x16x32_bf16 v[8:11], v[172:175], a[232:235], v[8:11]\n"
    "v_mfma_f32_16x16x32_bf16 v[8:11], v[168:171], a[228:231], v[8:11]\n"
    "v_mfma_f32_16x16x32_bf16 v[8:11], v[164:167], a[224:227], v[8:11]\n"
    "v_mfma_f32_16x16x32_bf16 v[8:11], v[160:163], a[220:223], v[8:11]\n"
    "v_mfma_f32_16x16x32_bf16 v[8:11], v[156:159], a[216:219], v[8:11]\n"
    "v_mfma_f32_16x16x32_bf16 v[24:27], v[200:203], a[236:239], v[104:107]\n"
    "v_mfma_f32_16x16x32_bf16 v[24:27], v[196:199], a[232:235], v[24:27]\n"
    "v_mfma_f32_16x16x32_bf16 v[24:27], v[192:195], a[228:231], v[24:27]\n"
    "v_mfma_f32_16x16x32_bf16 v[24:27], v[188:191], a[224:227], v[24:27]\n"
    "v_mfma_f32_16x16x32_bf16 v[24:27], v[184:187], a[220:223], v[24:27]\n"
    "v_mfma_f32_16x16x32_bf16 v[24:27], v[180:183], a[216:219], v[24:27]\n"
    "s_add_u32 m0, s65, 98304\n"
    "s_add_u32 s72, s85, 18432\n"
    "buffer_load_dwordx4 v88, s[20:23], s72 offen offset:0 nt lds\n"
    "v_exp_f32 v8, v8\n"
    "v_exp_f32 v9, v9\n"
    "v_exp_f32 v10, v10\n"
    "v_exp_f32 v11, v11\n"
    "s_nop 0\n"
    "v_exp_f32 v24, v24\n"
    "v_exp_f32 v25, v25\n"
    "v_exp_f32 v26, v26\n"
    "v_exp_f32 v27, v27\n"
    "s_add_u32 m0, s65, 99328\n"
    "s_add_u32 s72, s85, 19456\n"
    "buffer_load_dwordx4 v89, s[20:23], s72 offen offset:0 nt lds\n"
    "s_add_u32 m0, s65, 100352\n"
    "s_add_u32 s72, s85, 20480\n"
    "buffer_load_dwordx4 v90, s[20:23], s72 offen offset:0 nt lds\n"
    "s_waitcnt vmcnt(7)\n"
    "v_cmp_eq_u64 s[54:55], v[236:237], v[238:239]\n"
    "s_nop 0\n"
    "s_cmp_eq_u64 s[54:55], -1\n"
    "s_cbranch_scc0 .LpollAK\n"
    ".LpollAKr:\n"
    "s_add_u32 m0, s65, 101376\n"
    "s_add_u32 s72, s85, 21504\n"
    "buffer_load_dwordx4 v88, s[20:23], s72 offen offset:0 nt lds\n"
    "s_add_u32 m0, s65, 24576\n"
    "s_add_u32 s72, s65, 49152\n"
    "buffer_load_dwordx4 v1, s[16:19], s72 offen offset:0 sc1 lds\n"
    "s_add_u32 m0, s65, 25600\n"
    "s_add_u32 s72, s65, 50176\n"
    "buffer_load_dwordx4 v1, s[16:19], s72 offen offset:0 sc1 lds\n"
    "s_add_u32 m0, s65, 102400\n"
    "s_add_u32 s72, s85, 22528\n"
    "buffer_load_dwordx4 v89, s[20:23], s72 offen offset:0 nt lds\n"
    "s_add_u32 m0, s65, 26624\n"
    "s_add_u32 s72, s65, 51200\n"
    "buffer_load_dwordx4 v1, s[16:19], s72 offen offset:0 sc1 lds\n"
    "s_add_u32 m0, s65, 27648\n"
    "s_add_u32 s72, s65, 52224\n"
    "buffer_load_dwordx4 v1, s[16:19], s72 offen offset:0 sc1 lds\n"
    "s_add_u32 m0, s65, 103424\n"
    "s_add_u32 s72, s85, 23552\n"
    "buffer_load_dwordx4 v90, s[20:23], s72 offen offset:0 nt lds\n"
    "s_add_u32 m0, s65, 28672\n"
    "s_add_u32 s72, s65, 53248\n"
    "buffer_load_dwordx4 v1, s[16:19], s72 offen offset:0 sc1 lds\n"
    "s_add_u32 m0, s65, 29696\n"
    "s_add_u32 s72, s65, 54272\n"
    "buffer_load_dwordx4 v1, s[16:19], s72 offen offset:0 sc1 lds\n"
    "s_add_u32 m0, s65, 73728\n"
    "s_add_u32 s72, s65, 73728\n"
    "buffer_load_dwordx4 v1, s[16:19], s72 offen offset:0 sc1 lds\n"
    "s_add_u32 m0, s65, 74752\n"
    "s_add_u32 s72, s65, 74752\n"
    "buffer_load_dwordx4 v1, s[16:19], s72 offen offset:0 sc1 lds\n"
    "s_add_u32 m0, s65, 75776\n"
    "s_add_u32 s72, s65, 75776\n"
    "buffer_load_dwordx4 v1, s[16:19], s72 offen offset:0 sc1 lds\n"
    "s_add_u32 m0, s65, 76800\n"
    "s_add_u32 s72, s65, 76800\n"
    "buffer_load_dwordx4 v1, s[16:19], s72 offen offset:0 sc1 lds\n"
    "s_add_u32 m0, s65, 77824\n"
    "s_add_u32 s72, s65, 77824\n"
    "buffer_load_dwordx4 v1, s[16:19], s72 offen offset:0 sc1 lds\n"
    "s_add_u32 m0, s65, 78848\n"
    "s_add_u32 s72, s65, 78848\n"
    "buffer_load_dwordx4 v1, s[16:19], s72 offen offset:0 sc1 lds\n"
    "s_bitcmp1_b32 s83, 1\n"
    "s_cbranch_scc1 .Lf3b\n"
    "s_waitcnt vmcnt(18)\n"
    "ds_read_b128 a[240:243], v84 offset:0\n"
    "ds_read_b128 a[244:247], v85 offset:0\n"
    "ds_read_b128 a[248:251], v84 offset:128\n"
    "ds_read_b128 a[252:255], v85 offset:128\n"
    "ds_read_b128 v[124:127], v84 offset:256\n"
    "ds_read_b128 v[128:131], v85 offset:256\n"
    "s_waitcnt lgkmcnt(5)\n"
    "v_mfma_f32_16x16x32_bf16 v[12:15], v[156:159], a[240:243], v[100:103]\n"
    "s_waitcnt lgkmcnt(4)\n"
    "v_mfma_f32_16x16x32_bf16 v[12:15], v[160:163], a[244:247], v[12:15]\n"
    "s_waitcnt lgkmcnt(3)\n"
    "v_mfma_f32_16x16x32_bf16 v[12:15], v[164:167], a[248:251], v[12:15]\n"
    "s_waitcnt lgkmcnt(2)\n"
    "v_mfma_f32_16x16x32_bf16 v[12:15], v[168:171], a[252:255], v[12:15]\n"
    "s_waitcnt lgkmcnt(1)\n"
    "v_mfma_f32_16x16x32_bf16 v[12:15], v[172:175], v[124:127], v[12:15]\n"
    "s_waitcnt lgkmcnt(0)\n"
    "v_mfma_f32_16x16x32_bf16 v[12:15], v[176:179], v[128:131], v[12:15]\n"
    "v_mfma_f32_16x16x32_bf16 v[28:31], v[180:183], a[240:243], v[104:107]\n"
    "v_mfma_f32_16x16x32_bf16 v[28:31], v[184:187], a[244:247], v[28:31]\n"
    "v_mfma_f32_16x16x32_bf16 v[28:31], v[188:191], a[248:251], v[28:31]\n"
    "v_mfma_f32_16x16x32_bf16 v[28:31], v[192:195], a[252:255], v[28:31]\n"
    "v_mfma_f32_16x16x32_bf16 v[28:31], v[196:199], v[124:127], v[28:31]\n"
    "v_mfma_f32_16x16x32_bf16 v[28:31], v[200:203], v[128:131], v[28:31]\n"
    "s_nop 1\n"
    "v_exp_f32 v12, v12\n"
    "v_exp_f32 v13, v13\n"
    "v_exp_f32 v14, v14\n"
    "v_exp_f32 v15, v15\n"
    "s_nop 1\n"
    "v_exp_f32 v28, v28\n"
    "v_exp_f32 v29, v29\n"
    "v_exp_f32 v30, v30\n"
    "v_exp_f32 v31, v31\n"
    "s_waitcnt vmcnt(8)\n"
    "ds_read_b128 v[132:135], v84 offset:49152\n"
    "ds_read_b128 v[136:139], v85 offset:49152\n"
    "ds_read_b128 v[140:143], v84 offset:49280\n"
    "ds_read_b128 v[144:147], v85 offset:49280\n"
    "ds_read_b128 v[148:151], v84 offset:49408\n"
    "ds_read_b128 v[152:155], v85 offset:49408\n"
    "s_waitcnt lgkmcnt(0)\n"
    "v_mfma_f32_16x16x32_bf16 v[16:19], v[176:179], v[152:155], v[100:103]\n"
    "v_mfma_f32_16x16x32_bf16 v[16:19], v[172:175], v[148:151], v[16:19]\n"
    "v_mfma_f32_16x16x32_bf16 v[16:19], v[168:171], v[144:147], v[16:19]\n"
    "v_mfma_f32_16x16x32_bf16 v[16:19], v[164:167], v[140:143], v[16:19]\n"
    "v_mfma_f32_16x16x32_bf16 v[16:19], v[160:163], v[136:139], v[16:19]\n"
    "v_mfma_f32_16x16x32_bf16 v[16:19], v[156:159], v[132:135], v[16:19]\n"
    "v_mfma_f32_16x16x32_bf16 v[32:35], v[200:203], v[152:155], v[104:107]\n"
    "v_mfma_f32_16x16x32_bf16 v[32:35], v[196:199], v[148:151], v[32:35]\n"
    "v_mfma_f32_16x16x32_bf16 v[32:35], v[192:195], v[144:147], v[32:35]\n"
    "v_mfma_f32_16x16x32_bf16 v[32:35], v[188:191], v[140:143], v[32:35]\n"
    "v_mfma_f32_16x16x32_bf16 v[32:35], v[184:187], v[136:139], v[32:35]\n"
    "v_mfma_f32_16x16x32_bf16 v[32:35], v[180:183], v[132:135], v[32:35]\n"
    "ds_read_b128 v[156:159], v2 offset:12288\n"
    "ds_read_b128 v[160:163], v2 offset:14336\n"
    "ds_read_b128 v[164:167], v2 offset:16384\n"
    "ds_read_b128 v[168:171], v2 offset:18432\n"
    "ds_read_b128 v[172:175], v2 offset:20480\n"
    "ds_read_b128 v[176:179], v2 offset:22528\n"
    "ds_read_b128 v[100:103], v77 offset:128\n"
    "v_exp_f32 v16, v16\n"
    "v_exp_f32 v17, v17\n"
    "v_exp_f32 v18, v18\n"
    "v_exp_f32 v19, v19\n"
    "v_exp_f32 v32, v32\n"
    "v_exp_f32 v33, v33\n"
    "v_exp_f32 v34, v34\n"
    "v_exp_f32 v35, v35\n"
    "s_mov_b32 s66, 0\n"
    "s_waitcnt lgkmcnt(0)\n"
    "s_waitcnt vmcnt(0)\n"
    "ds_add_u32 v247, v246\n"
    "s_nop 7\n"
    "s_branch .LPK\n"
    "s_branch .Lf3e\n"
    ".Lf3b:\n"
    "s_add_u32 s75, s71, s63\n"
    "s_add_u32 s76, s75, 6144\n"
    "s_add_u32 s77, s75, 24576\n"
    "s_add_u32 s78, s75, 30720\n"
    "buffer_load_dwordx2 v[204:205], v242, s[28:31], s75 offen offset:0 nt\n"
    "buffer_load_dwordx2 v[206:207], v242, s[28:31], s75 offen offset:768 nt\n"
    "buffer_load_dwordx2 v[208:209], v242, s[28:31], s75 offen offset:3072 nt\n"
    "buffer_load_dwordx2 v[210:211], v242, s[28:31], s75 offen offset:3840 nt\n"
    "buffer_load_dwordx2 v[212:213], v242, s[28:31], s76 offen offset:0 nt\n"
    "buffer_load_dwordx2 v[214:215], v242, s[28:31], s76 offen offset:768 nt\n"
    "buffer_load_dwordx2 v[216:217], v242, s[28:31], s76 offen offset:3072 nt\n"
    "buffer_load_dwordx2 v[218:219], v242, s[28:31], s76 offen offset:3840 nt\n"
    "buffer_load_dwordx2 v[220:221], v242, s[28:31], s77 offen offset:0 nt\n"
    "buffer_load_dwordx2 v[222:223], v242, s[28:31], s77 offen offset:768 nt\n"
    "buffer_load_dwordx2 v[224:225], v242, s[28:31], s77 offen offset:3072 nt\n"
    "buffer_load_dwordx2 v[226:227], v242, s[28:31], s77 offen offset:3840 nt\n"
    "buffer_load_dwordx2 v[228:229], v242, s[28:31], s78 offen offset:0 nt\n"
    "buffer_load_dwordx2 v[230:231], v242, s[28:31], s78 offen offset:768 nt\n"
    "buffer_load_dwordx2 v[232:233], v242, s[28:31], s78 offen offset:3072 nt\n"
    "buffer_load_dwordx2 v[234:235], v242, s[28:31], s78 offen offset:3840 nt\n"
    "s_waitcnt vmcnt(34)\n"
    "ds_read_b128 a[240:243], v84 offset:0\n"
    "ds_read_b128 a[244:247], v85 offset:0\n"
    "ds_read_b128 a[248:251], v84 offset:128\n"
    "ds_read_b128 a[252:255], v85 offset:128\n"
    "ds_read_b128 v[124:127], v84 offset:256\n"
    "ds_read_b128 v[128:131], v85 offset:256\n"
    "s_waitcnt lgkmcnt(5)\n"
    "v_mfma_f32_16x16x32_bf16 v[12:15], v[156:159], a[240:243], v[100:103]\n"
    "s_waitcnt lgkmcnt(4)\n"
    "v_mfma_f32_16x16x32_bf16 v[12:15], v[160:163], a[244:247], v[12:15]\n"
    "s_waitcnt lgkmcnt(3)\n"
    "v_mfma_f32_16x16x32_bf16 v[12:15], v[164:167], a[248:251], v[12:15]\n"
    "s_waitcnt lgkmcnt(2)\n"
    "v_mfma_f32_16x16x32_bf16 v[12:15], v[168:171], a[252:255], v[12:15]\n"
    "s_waitcnt lgkmcnt(1)\n"
    "v_mfma_f32_16x16x32_bf16 v[12:15], v[172:175], v[124:127], v[12:15]\n"
    "s_waitcnt lgkmcnt(0)\n"
    "v_mfma_f32_16x16x32_bf16 v[12:15], v[176:179], v[128:131], v[12:15]\n"
    "v_mfma_f32_16x16x32_bf16 v[28:31], v[180:183], a[240:243], v[104:107]\n"
    "v_mfma_f32_16x16x32_bf16 v[28:31], v[184:187], a[244:247], v[28:31]\n"
    "v_mfma_f32_16x16x32_bf16 v[28:31], v[188:191], a[248:251], v[28:31]\n"
    "v_mfma_f32_16x16x32_bf16 v[28:31], v[192:195], a[252:255], v[28:31]\n"
    "v_mfma_f32_16x16x32_bf16 v[28:31], v[196:199], v[124:127], v[28:31]\n"
    "v_mfma_f32_16x16x32_bf16 v[28:31], v[200:203], v[128:131], v[28:31]\n"
    "s_nop 1\n"
    "v_exp_f32 v12, v12\n"
    "v_exp_f32 v13, v13\n"
    "v_exp_f32 v14, v14\n"
    "v_exp_f32 v15, v15\n"
    "s_nop 1\n"
    "v_exp_f32 v28, v28\n"
    "v_exp_f32 v29, v29\n"
    "v_exp_f32 v30, v30\n"
    "v_exp_f32 v31, v31\n"
    "s_waitcnt vmcnt(24)\n"
    "ds_read_b128 v[132:135], v84 offset:49152\n"
    "ds_read_b128 v[136:139], v85 offset:49152\n"
    "ds_read_b128 v[140:143], v84 offset:49280\n"
    "ds_read_b128 v[144:147], v85 offset:49280\n"
    "ds_read_b128 v[148:151], v84 offset:49408\n"
    "ds_read_b128 v[152:155], v85 offset:49408\n"
    "s_waitcnt lgkmcnt(0)\n"
    "v_mfma_f32_16x16x32_bf16 v[16:19], v[176:179], v[152:155], v[100:103]\n"
    "v_mfma_f32_16x16x32_bf16 v[16:19], v[172:175], v[148:151], v[16:19]\n"
    "v_mfma_f32_16x16x32_bf16 v[16:19], v[168:171], v[144:147], v[16:19]\n"
    "v_mfma_f32_16x16x32_bf16 v[16:19], v[164:167], v[140:143], v[16:19]\n"
    "v_mfma_f32_16x16x32_bf16 v[16:19], v[160:163], v[136:139], v[16:19]\n"
    "v_mfma_f32_16x16x32_bf16 v[16:19], v[156:159], v[132:135], v[16:19]\n"
    "v_mfma_f32_16x16x32_bf16 v[32:35], v[200:203], v[152:155], v[104:107]\n"
    "v_mfma_f32_16x16x32_bf16 v[32:35], v[196:199], v[148:151], v[32:35]\n"
    "v_mfma_f32_16x16x32_bf16 v[32:35], v[192:195], v[144:147], v[32:35]\n"
    "v_mfma_f32_16x16x32_bf16 v[32:35], v[188:191], v[140:143], v[32:35]\n"
    "v_mfma_f32_16x16x32_bf16 v[32:35], v[184:187], v[136:139], v[32:35]\n"
    "v_mfma_f32_16x16x32_bf16 v[32:35], v[180:183], v[132:135], v[32:35]\n"
    "ds_read_b128 v[156:159], v2 offset:12288\n"
    "ds_read_b128 v[160:163], v2 offset:14336\n"
    "ds_read_b128 v[164:167], v2 offset:16384\n"
    "ds_read_b128 v[168:171], v2 offset:18432\n"
    "ds_read_b128 v[172:175], v2 offset:20480\n"
    "ds_read_b128 v[176:179], v2 offset:22528\n"
    "ds_read_b128 v[100:103], v77 offset:128\n"
    "v_exp_f32 v16, v16\n"
    "v_exp_f32 v17, v17\n"
    "v_exp_f32 v18, v18\n"
    "v_exp_f32 v19, v19\n"
    "v_exp_f32 v32, v32\n"
    "v_exp_f32 v33, v33\n"
    "v_exp_f32 v34, v34\n"
    "v_exp_f32 v35, v35\n"
    "s_mov_b32 s66, 0\n"
    "s_waitcnt lgkmcnt(0)\n"
    "s_waitcnt vmcnt(16)\n"
    "ds_add_u32 v247, v246\n"
    "s_nop 7\n"
    "s_branch .LNK\n"
    ".Lf3e:\n"
    "s_nop 4\n"
    "s_branch .Lf5e\n"
    ".Lf5b:\n"
    "s_lshl_b32 s73, s79, 8\n"
    "s_mul_i32 s74, s59, 6144\n"
    "s_add_u32 s73, s73, s74\n"
    "v_add_u32 v163, s73, v244\n"
    "s_lshr_b32 s73, s53, 2\n"
    "s_mul_i32 s73, s73, 12288\n"
    "s_and_b32 s74, s53, 3\n"
    "s_lshl_b32 s74, s74, 8\n"
    "s_add_u32 s73, s73, s74\n"
    "s_cmp_eq_u32 s59, 0\n"
    "s_cselect_b32 s73, 0x7f000000, s73\n"
    "s_add_u32 s60, s73, 24576\n"
    "s_add_u32 s61, s73, 28672\n"
    "s_add_u32 s62, s73, 32768\n"
    "v_mov_b32 v244, v243\n"
    "buffer_load_dwordx4 v[48:51], v242, s[40:43], s75 offen offset:1024 nt\n"
    "v_add_u32 v159, 0, v156\n"
    "v_mul_u32_u24 v159, 2731, v159\n"
    "v_lshrrev_b32 v159, 16, v159\n"
    "v_xor_b32 v159, v159, v156\n"
    "v_and_b32 v159, 7, v159\n"
    "v_and_b32 v88, 0x380, v1\n"
    "v_lshl_add_u32 v88, v159, 4, v88\n"
    "v_add_u32 v159, 64, v156\n"
    "v_mul_u32_u24 v159, 2731, v159\n"
    "v_lshrrev_b32 v159, 16, v159\n"
    "v_xor_b32 v159, v159, v156\n"
    "v_and_b32 v159, 7, v159\n"
    "v_and_b32 v89, 0x380, v1\n"
    "v_lshl_add_u32 v89, v159, 4, v89\n"
    "v_add_u32 v159, 128, v156\n"
    "v_mul_u32_u24 v159, 2731, v159\n"
    "v_lshrrev_b32 v159, 16, v159\n"
    "v_xor_b32 v159, v159, v156\n"
    "v_and_b32 v159, 7, v159\n"
    "v_and_b32 v90, 0x380, v1\n"
    "v_lshl_add_u32 v90, v159, 4, v90\n"
    "v_and_b32 v2, 7, v156\n"
    "v_lshrrev_b32 v144, 3, %3\n"
    "v_mul_u32_u24 v144, 768, v144\n"
    "v_lshl_add_u32 v144, v2, 3, v144\n"
    "v_add_u32 v144, 131072, v144\n"
    "v_mad_u32_u24 v86, v2, 24, v144\n"
    "v_and_b32 v84, 15, v156\n"
    "v_lshrrev_b32 v159, 4, v156\n"
    "v_xor_b32 v159, v159, v2\n"
    "v_mul_u32_u24 v84, 384, v84\n"
    "v_lshl_add_u32 v84, v159, 4, v84\n"
    "s_add_u32 s73, s65, 49152\n"
    "v_add_u32 v84, s73, v84\n"
    "v_xor_b32 v85, 64, v84\n"
    "v_subrev_u32 v154, 24576, v84\n"
    "v_subrev_u32 v155, 24576, v85\n"
    "s_add_u32 s67, s65, 98304\n"
    "s_add_u32 s68, s65, 24576\n"
    "s_add_u32 s69, s65, 73728\n"
    "s_mov_b32 s81, 256\n"
    "s_mul_i32 s74, s79, 24576\n"
    "s_add_u32 s70, s74, s65\n"
    "s_add_u32 s70, s70, 73728\n"
    "s_sub_u32 s64, s65, s74\n"
    "s_add_u32 s64, s64, 98304\n"
    "s_add_u32 s73, s74, 73728\n"
    "v_add_u32 v248, s73, v1\n"
    "buffer_load_dwordx4 v[52:55], v242, s[40:43], s75 offen offset:512 nt\n"
    "s_sub_u32 s73, 98304, s74\n"
    "v_add_u32 v249, s73, v1\n"
    "s_or_b32 s73, s53, s84\n"
    "s_cmp_eq_u32 s73, 0\n"
    "s_cselect_b32 s82, 0, 0x7fff0000\n"
    "v_and_b32 v252, 12, v156\n"
    "v_bfe_u32 v159, v156, 1, 1\n"
    "v_or_b32 v252, v252, v159\n"
    "v_and_b32 v159, 1, v156\n"
    "v_lshl_or_b32 v252, v159, 1, v252\n"
    "v_lshlrev_b32 v252, 6, v252\n"
    "v_lshlrev_b32 v159, 2, v156\n"
    "v_and_b32 v159, 32, v159\n"
    "v_xor_b32 v159, v159, v156\n"
    "v_and_or_b32 v252, v159, 48, v252\n"
    "v_mov_b32 v2, v252\n"
    "v_add_u32 v3, 24576, v252\n"
    "v_and_b32 v87, 0x70, v1\n"
    "v_lshl_add_u32 v87, v87, 2, 64\n"
    "v_add_u32 v87, 64, v87\n"
    "v_and_b32 v241, 0xf0, v1\n"
    "v_lshrrev_b32 v241, 1, v241\n"
    "v_add_u32 v241, 640, v241\n"
    "v_lshrrev_b32 v91, 3, v156\n"
    "v_lshlrev_b32 v91, 24, v91\n"
    "v_lshl_add_u32 v91, v156, 3, v91\n"
    "s_lshl_b32 s73, s53, 4\n"
    "s_lshl_b32 s74, s59, 3\n"
    "s_add_u32 s73, s73, s74\n"
    "s_add_u32 s73, s73, 640\n"
    "v_mov_b32 v240, 0x7fff0000\n"
    "v_writelane_b32 v240, s73, 0\n"
    "v_and_b32 v159, 48, v156\n"
    "s_mul_i32 s73, s79, 768\n"
    "s_sub_u32 s73, 131072, s73\n"
    "v_lshlrev_b32 v250, 1, v159\n"
    "v_add_u32 v250, s73, v250\n"
    "v_and_b32 v251, 3, v156\n"
    "v_lshl_add_u32 v251, v251, 7, v159\n"
    "s_add_u32 s73, s73, 256\n"
    "v_add_u32 v251, s73, v251\n"
    "v_and_b32 v159, 64, v1\n"
    "v_add_u32 v251, v159, v251\n"
    "v_bfe_i32 v159, v156, 2, 1\n"
    "v_and_b32 v159, 0xaaaa, v159\n"
    "v_or_b32 v253, 0x4444, v159\n"
    "s_mov_b32 s58, 0\n"
    "s_mov_b32 s59, 8\n"
    "buffer_load_dwordx4 v[56:59], v242, s[40:43], s75 offen offset:1280 nt\n"
    "buffer_load_dwordx4 v[136:139], v130, s[92:95], 0 offen offset:0\n"
    "buffer_load_dwordx4 v[140:143], v130, s[96:99], 0 offen offset:0\n"
    "v_accvgpr_write_b32 a0, 0\n"
    "v_accvgpr_write_b32 a1, 0\n"
    "v_accvgpr_write_b32 a2, 0\n"
    "v_accvgpr_write_b32 a3, 0\n"
    "v_accvgpr_write_b32 a4, 0\n"
    "v_accvgpr_write_b32 a5, 0\n"
    "v_accvgpr_write_b32 a6, 0\n"
    "v_accvgpr_write_b32 a7, 0\n"
    "v_accvgpr_write_b32 a8, 0\n"
    "v_accvgpr_write_b32 a9, 0\n"
    "v_accvgpr_write_b32 a10, 0\n"
    "v_accvgpr_write_b32 a11, 0\n"
    "v_accvgpr_write_b32 a12, 0\n"
    "v_accvgpr_write_b32 a13, 0\n"
    "v_accvgpr_write_b32 a14, 0\n"
    "v_accvgpr_write_b32 a15, 0\n"
    "v_accvgpr_write_b32 a16, 0\n"
    "v_accvgpr_write_b32 a17, 0\n"
    "v_accvgpr_write_b32 a18, 0\n"
    "v_accvgpr_write_b32 a19, 0\n"
    "v_accvgpr_write_b32 a20, 0\n"
    "v_accvgpr_write_b32 a21, 0\n"
    "v_accvgpr_write_b32 a22, 0\n"
    "v_accvgpr_write_b32 a23, 0\n"
    "v_accvgpr_write_b32 a24, 0\n"
    "v_accvgpr_write_b32 a25, 0\n"
    "v_accvgpr_write_b32 a26, 0\n"
    "v_accvgpr_write_b32 a27, 0\n"
    "v_accvgpr_write_b32 a28, 0\n"
    "v_accvgpr_write_b32 a29, 0\n"
    "v_accvgpr_write_b32 a30, 0\n"
    "v_accvgpr_write_b32 a31, 0\n"
    "v_accvgpr_write_b32 a32, 0\n"
    "v_accvgpr_write_b32 a33, 0\n"
    "v_accvgpr_write_b32 a34, 0\n"
    "v_accvgpr_write_b32 a35, 0\n"
    "v_accvgpr_write_b32 a36, 0\n"
    "v_accvgpr_write_b32 a37, 0\n"
    "v_accvgpr_write_b32 a38, 0\n"
    "v_accvgpr_write_b32 a39, 0\n"
    "v_accvgpr_write_b32 a40, 0\n"
    "v_accvgpr_write_b32 a41, 0\n"
    "v_accvgpr_write_b32 a42, 0\n"
    "v_accvgpr_write_b32 a43, 0\n"
    "v_accvgpr_write_b32 a44, 0\n"
    "v_accvgpr_write_b32 a45, 0\n"
    "v_accvgpr_write_b32 a46, 0\n"
    "v_accvgpr_write_b32 a47, 0\n"
    "v_accvgpr_write_b32 a48, 0\n"
    "v_accvgpr_write_b32 a49, 0\n"
    "v_accvgpr_write_b32 a50, 0\n"
    "v_accvgpr_write_b32 a51, 0\n"
    "v_accvgpr_write_b32 a52, 0\n"
    "v_accvgpr_write_b32 a53, 0\n"
    "v_accvgpr_write_b32 a54, 0\n"
    "v_accvgpr_write_b32 a55, 0\n"
    "v_accvgpr_write_b32 a56, 0\n"
    "v_accvgpr_write_b32 a57, 0\n"
    "v_accvgpr_write_b32 a58, 0\n"
    "v_accvgpr_write_b32 a59, 0\n"
    "s_waitcnt vmcnt(11)\n"
    "s_add_u32 s54, s71, s60\n"
    "s_add_u32 s55, s71, s61\n"
    "s_add_u32 s56, s71, s62\n"
    "v_cvt_f32_i32 v204, v204\n"
    "v_cvt_f32_i32 v208, v208\n"
    "v_cvt_f32_i32 v205, v205\n"
    "v_cvt_f32_i32 v209, v209\n"
    "v_cvt_f32_i32 v206, v206\n"
    "v_cvt_f32_i32 v210, v210\n"
    "v_cvt_f32_i32 v207, v207\n"
    "v_cvt_f32_i32 v211, v211\n"
    "v_cvt_pk_bf16_f32 v204, v204, v208\n"
    "v_cvt_pk_bf16_f32 v205, v205, v209\n"
    "v_cvt_pk_bf16_f32 v206, v206, v210\n"
    "v_cvt_pk_bf16_f32 v207, v207, v211\n"
    "buffer_store_dword v204, v244, s[16:19], s54 offen offset:0 sc1\n"
    "buffer_store_dword v205, v244, s[16:19], s54 offen offset:1024 sc1\n"
    "buffer_load_dwordx4 v[4:7], v162, s[28:31], s57 offen offset:0\n"
    "buffer_load_dwordx4 v[8:11], v162, s[28:31], s57 offen offset:3072\n"
    "buffer_store_dword v206, v244, s[16:19], s54 offen offset:2048 sc1\n"
    "buffer_store_dword v207, v244, s[16:19], s54 offen offset:3072 sc1\n"
    "v_cvt_f32_i32 v212, v212\n"
    "v_cvt_f32_i32 v216, v216\n"
    "v_cvt_f32_i32 v213, v213\n"
    "v_cvt_f32_i32 v217, v217\n"
    "v_cvt_f32_i32 v214, v214\n"
    "v_cvt_f32_i32 v218, v218\n"
    "v_cvt_f32_i32 v215, v215\n"
    "v_cvt_f32_i32 v219, v219\n"
    "v_cvt_pk_bf16_f32 v212, v212, v216\n"
    "v_cvt_pk_bf16_f32 v213, v213, v217\n"
    "v_cvt_pk_bf16_f32 v214, v214, v218\n"
    "v_cvt_pk_bf16_f32 v215, v215, v219\n"
    "buffer_store_dword v212, v244, s[16:19], s55 offen offset:0 sc1\n"
    "buffer_store_dword v213, v244, s[16:19], s55 offen offset:1024 sc1\n"
    "buffer_store_dword v214, v244, s[16:19], s55 offen offset:2048 sc1\n"
    "buffer_load_dwordx4 v[12:15], v162, s[28:31], s57 offen offset:128\n"
    "buffer_store_dword v215, v244, s[16:19], s55 offen offset:3072 sc1\n"
    "v_cvt_f32_i32 v220, v220\n"
    "v_cvt_f32_i32 v224, v224\n"
    "v_cvt_f32_i32 v221, v221\n"
    "v_cvt_f32_i32 v225, v225\n"
    "v_cvt_f32_i32 v222, v222\n"
    "v_cvt_f32_i32 v226, v226\n"
    "v_cvt_f32_i32 v223, v223\n"
    "v_cvt_f32_i32 v227, v227\n"
    "v_cvt_pk_bf16_f32 v220, v220, v224\n"
    "v_cvt_pk_bf16_f32 v221, v221, v225\n"
    "v_cvt_pk_bf16_f32 v222, v222, v226\n"
    "v_cvt_pk_bf16_f32 v223, v223, v227\n"
    "buffer_store_dword v220, v244, s[16:19], s56 offen offset:0 sc1\n"
    "buffer_store_dword v221, v244, s[16:19], s56 offen offset:1024 sc1\n"
    "buffer_store_dword v222, v244, s[16:19], s56 offen offset:2048 sc1\n"
    "buffer_store_dword v223, v244, s[16:19], s56 offen offset:3072 sc1\n"
    "s_add_u32 s71, s71, 98304\n"
    "s_add_u32 m0, s65, 24576\n"
    "s_add_u32 s72, s85, 0\n"
    "buffer_load_dwordx4 v88, s[20:23], s72 offen offset:0 nt lds\n"
    "buffer_load_dwordx4 v[16:19], v162, s[28:31], s57 offen offset:3200\n"
    "buffer_load_dwordx4 v[20:23], v162, s[28:31], s57 offen offset:256\n"
    "s_waitcnt vmcnt(23)\n"
    "v_add_co_u32 v238, vcc, 0x7f4a7c15, v238\n"
    "v_mov_b32 v159, 0x9e3779b9\n"
    "s_nop 0\n"
    "v_addc_co_u32 v239, vcc, v159, v239, vcc\n"
    "v_mov_b32 v246, 1\n"
    "s_waitcnt vmcnt(24)\n"
    "v_lshlrev_b32 v180, 16, v124\n"
    "v_and_b32 v181, 0xffff0000, v124\n"
    "v_lshlrev_b32 v182, 16, v125\n"
    "v_and_b32 v183, 0xffff0000, v125\n"
    "v_lshlrev_b32 v184, 16, v126\n"
    "v_and_b32 v185, 0xffff0000, v126\n"
    "v_lshlrev_b32 v186, 16, v127\n"
    "v_and_b32 v187, 0xffff0000, v127\n"
    "v_mul_f32 v180, 0x3dd53b95, v180\n"
    "v_mul_f32 v181, 0x3dd53b95, v181\n"
    "v_mul_f32 v182, 0x3dd53b95, v182\n"
    "v_mul_f32 v183, 0x3dd53b95, v183\n"
    "v_mul_f32 v184, -v184, v180\n"
    "v_mul_f32 v185, -v185, v181\n"
    "v_mul_f32 v186, -v186, v182\n"
    "v_mul_f32 v187, -v187, v183\n"
    "ds_write_b128 v131, v[180:183] offset:0\n"
    "ds_write_b128 v131, v[184:187] offset:1024\n"
    "ds_read_b128 v[180:183], v132 offset:0\n"
    "ds_read_b128 v[184:187], v132 offset:1024\n"
    "ds_read_b128 v[188:191], v132 offset:128\n"
    "ds_read_b128 v[192:195], v132 offset:1152\n"
    "ds_read_b128 v[196:199], v132 offset:256\n"
    "ds_read_b128 v[200:203], v132 offset:1280\n"
    "v_accvgpr_write_b32 a60, 0\n"
    "v_accvgpr_write_b32 a61, 0\n"
    "v_accvgpr_write_b32 a62, 0\n"
    "v_accvgpr_write_b32 a63, 0\n"
    "v_accvgpr_write_b32 a64, 0\n"
    "v_accvgpr_write_b32 a65, 0\n"
    "v_accvgpr_write_b32 a66, 0\n"
    "v_accvgpr_write_b32 a67, 0\n"
    "v_accvgpr_write_b32 a68, 0\n"
    "v_accvgpr_write_b32 a69, 0\n"
    "s_add_u32 m0, s65, 25600\n"
    "s_add_u32 s72, s85, 1024\n"
    "buffer_load_dwordx4 v89, s[20:23], s72 offen offset:0 nt lds\n"
    "buffer_load_dwordx4 v[24:27], v162, s[28:31], s57 offen offset:3328\n"
    "v_accvgpr_write_b32 a70, 0\n"
    "v_accvgpr_write_b32 a71, 0\n"
    "v_accvgpr_write_b32 a72, 0\n"
    "v_accvgpr_write_b32 a73, 0\n"
    "v_accvgpr_write_b32 a74, 0\n"
    "v_accvgpr_write_b32 a75, 0\n"
    "v_accvgpr_write_b32 a76, 0\n"
    "v_accvgpr_write_b32 a77, 0\n"
    "v_accvgpr_write_b32 a78, 0\n"
    "v_accvgpr_write_b32 a79, 0\n"
    "v_accvgpr_write_b32 a80, 0\n"
    "v_accvgpr_write_b32 a81, 0\n"
    "v_accvgpr_write_b32 a82, 0\n"
    "v_accvgpr_write_b32 a83, 0\n"
    "v_accvgpr_write_b32 a84, 0\n"
    "v_accvgpr_write_b32 a85, 0\n"
    "v_accvgpr_write_b32 a86, 0\n"
    "v_accvgpr_write_b32 a87, 0\n"
    "v_accvgpr_write_b32 a88, 0\n"
    "v_accvgpr_write_b32 a89, 0\n"
    "v_accvgpr_write_b32 a90, 0\n"
    "v_accvgpr_write_b32 a91, 0\n"
    "v_accvgpr_write_b32 a92, 0\n"
    "v_accvgpr_write_b32 a93, 0\n"
    "v_accvgpr_write_b32 a94, 0\n"
    "v_accvgpr_write_b32 a95, 0\n"
    "v_accvgpr_write_b32 a96, 0\n"
    "v_accvgpr_write_b32 a97, 0\n"
    "v_accvgpr_write_b32 a98, 0\n"
    "v_accvgpr_write_b32 a99, 0\n"
    "v_accvgpr_write_b32 a100, 0\n"
    "v_accvgpr_write_b32 a101, 0\n"
    "v_accvgpr_write_b32 a102, 0\n"
    "v_accvgpr_write_b32 a103, 0\n"
    "v_accvgpr_write_b32 a104, 0\n"
    "v_accvgpr_write_b32 a105, 0\n"
    "v_accvgpr_write_b32 a106, 0\n"
    "v_accvgpr_write_b32 a107, 0\n"
    "v_accvgpr_write_b32 a108, 0\n"
    "v_accvgpr_write_b32 a109, 0\n"
    "v_accvgpr_write_b32 a110, 0\n"
    "s_add_u32 m0, s65, 26624\n"
    "s_add_u32 s72, s85, 2048\n"
    "buffer_load_dwordx4 v90, s[20:23], s72 offen offset:0 nt lds\n"
    "buffer_load_dwordx4 v[100:103], v162, s[28:31], s66 offen offset:0\n"
    "buffer_load_dwordx4 v[104:107], v162, s[28:31], s66 offen offset:3072\n"
    "v_accvgpr_write_b32 a111, 0\n"
    "v_accvgpr_write_b32 a112, 0\n"
    "v_accvgpr_write_b32 a113, 0\n"
    "v_accvgpr_write_b32 a114, 0\n"
    "v_accvgpr_write_b32 a115, 0\n"
    "v_accvgpr_write_b32 a116, 0\n"
    "v_accvgpr_write_b32 a117, 0\n"
    "v_accvgpr_write_b32 a118, 0\n"
    "v_accvgpr_write_b32 a119, 0\n"
    "v_accvgpr_write_b32 a120, 0\n"
    "v_accvgpr_write_b32 a121, 0\n"
    "v_accvgpr_write_b32 a122, 0\n"
    "v_accvgpr_write_b32 a123, 0\n"
    "v_accvgpr_write_b32 a124, 0\n"
    "v_accvgpr_write_b32 a125, 0\n"
    "v_accvgpr_write_b32 a126, 0\n"
    "v_accvgpr_write_b32 a127, 0\n"
    "v_accvgpr_write_b32 a128, 0\n"
    "v_accvgpr_write_b32 a129, 0\n"
    "v_accvgpr_write_b32 a130, 0\n"
    "v_accvgpr_write_b32 a131, 0\n"
    "v_accvgpr_write_b32 a132, 0\n"
    "v_accvgpr_write_b32 a133, 0\n"
    "v_accvgpr_write_b32 a134, 0\n"
    "v_accvgpr_write_b32 a135, 0\n"
    "v_accvgpr_write_b32 a136, 0\n"
    "v_accvgpr_write_b32 a137, 0\n"
    "v_accvgpr_write_b32 a138, 0\n"
    "v_accvgpr_write_b32 a139, 0\n"
    "v_accvgpr_write_b32 a140, 0\n"
    "v_accvgpr_write_b32 a141, 0\n"
    "v_accvgpr_write_b32 a142, 0\n"
    "v_accvgpr_write_b32 a143, 0\n"
    "v_accvgpr_write_b32 a144, 0\n"
    "v_accvgpr_write_b32 a145, 0\n"
    "v_accvgpr_write_b32 a146, 0\n"
    "v_accvgpr_write_b32 a147, 0\n"
    "v_accvgpr_write_b32 a148, 0\n"
    "v_accvgpr_write_b32 a149, 0\n"
    "v_accvgpr_write_b32 a150, 0\n"
    "s_add_u32 m0, s65, 27648\n"
    "s_add_u32 s72, s85, 3072\n"
    "buffer_load_dwordx4 v88, s[20:23], s72 offen offset:0 nt lds\n"
    "buffer_load_dwordx4 v[108:111], v162, s[28:31], s66 offen offset:128\n"
    "v_accvgpr_write_b32 a151, 0\n"
    "v_accvgpr_write_b32 a152, 0\n"
    "v_accvgpr_write_b32 a153, 0\n"
    "v_accvgpr_write_b32 a154, 0\n"
    "v_accvgpr_write_b32 a155, 0\n"
    "v_accvgpr_write_b32 a156, 0\n"
    "v_accvgpr_write_b32 a157, 0\n"
    "v_accvgpr_write_b32 a158, 0\n"
    "v_accvgpr_write_b32 a159, 0\n"
    "v_accvgpr_write_b32 a160, 0\n"
    "v_accvgpr_write_b32 a161, 0\n"
    "v_accvgpr_write_b32 a162, 0\n"
    "v_accvgpr_write_b32 a163, 0\n"
    "v_accvgpr_write_b32 a164, 0\n"
    "v_accvgpr_write_b32 a165, 0\n"
    "v_accvgpr_write_b32 a166, 0\n"
    "v_accvgpr_write_b32 a167, 0\n"
    "v_accvgpr_write_b32 a168, 0\n"
    "v_accvgpr_write_b32 a169, 0\n"
    "v_accvgpr_write_b32 a170, 0\n"
    "v_accvgpr_write_b32 a171, 0\n"
    "v_accvgpr_write_b32 a172, 0\n"
    "v_accvgpr_write_b32 a173, 0\n"
    "v_accvgpr_write_b32 a174, 0\n"
    "v_accvgpr_write_b32 a175, 0\n"
    "v_accvgpr_write_b32 a176, 0\n"
    "v_accvgpr_write_b32 a177, 0\n"
    "v_accvgpr_write_b32 a178, 0\n"
    "v_accvgpr_write_b32 a179, 0\n"
    "v_accvgpr_write_b32 a180, 0\n"
    "v_accvgpr_write_b32 a181, 0\n"
    "v_accvgpr_write_b32 a182, 0\n"
    "v_accvgpr_write_b32 a183, 0\n"
    "v_accvgpr_write_b32 a184, 0\n"
    "v_accvgpr_write_b32 a185, 0\n"
    "v_accvgpr_write_b32 a186, 0\n"
    "v_accvgpr_write_b32 a187, 0\n"
    "v_accvgpr_write_b32 a188, 0\n"
    "v_accvgpr_write_b32 a189, 0\n"
    "v_accvgpr_write_b32 a190, 0\n"
    "v_accvgpr_write_b32 a191, 0\n"
    "s_waitcnt lgkmcnt(0)\n"
    "s_waitcnt vmcnt(10)\n"
    "ds_add_rtn_u32 v245, v247, v246\n"
    "s_lshl_b32 s73, s53, 6\n"
    "s_add_u32 s73, s73, 128\n"
    "s_waitcnt lgkmcnt(0)\n"
    "v_readfirstlane_b32 s74, v245\n"
    "s_nop 0\n"
    "s_cmp_eq_u32 s74, 3\n"
    "s_cselect_b32 s73, s73, 0x7fff0000\n"
    "buffer_store_dwordx2 v[238:239], v91, s[36:39], s73 offen offset:0 sc1\n"
    "s_waitcnt vmcnt(28)\n"
    "s_add_u32 m0, s65, 28672\n"
    "s_add_u32 s72, s85, 4096\n"
    "buffer_load_dwordx4 v89, s[20:23], s72 offen offset:0 nt lds\n"
    "buffer_load_dwordx4 v[112:115], v162, s[28:31], s66 offen offset:3200\n"
    "buffer_load_dwordx4 v[116:119], v162, s[28:31], s66 offen offset:256\n"
    "s_add_u32 s54, s71, s60\n"
    "s_add_u32 s55, s71, s61\n"
    "s_add_u32 s56, s71, s62\n"
    "v_cvt_f32_i32 v36, v36\n"
    "v_cvt_f32_i32 v40, v40\n"
    "v_cvt_f32_i32 v37, v37\n"
    "v_cvt_f32_i32 v41, v41\n"
    "v_cvt_f32_i32 v38, v38\n"
    "v_cvt_f32_i32 v42, v42\n"
    "v_cvt_f32_i32 v39, v39\n"
    "v_cvt_f32_i32 v43, v43\n"
    "v_cvt_pk_bf16_f32 v36, v36, v40\n"
    "v_cvt_pk_bf16_f32 v37, v37, v41\n"
    "v_cvt_pk_bf16_f32 v38, v38, v42\n"
    "v_cvt_pk_bf16_f32 v39, v39, v43\n"
    "buffer_store_dword v36, v244, s[16:19], s54 offen offset:0 sc1\n"
    "buffer_store_dword v37, v244, s[16:19], s54 offen offset:1024 sc1\n"
    "buffer_store_dword v38, v244, s[16:19], s54 offen offset:2048 sc1\n"
    "buffer_store_dword v39, v244, s[16:19], s54 offen offset:3072 sc1\n"
    "v_cvt_f32_i32 v44, v44\n"
    "v_cvt_f32_i32 v48, v48\n"
    "v_cvt_f32_i32 v45, v45\n"
    "v_cvt_f32_i32 v49, v49\n"
    "v_cvt_f32_i32 v46, v46\n"
    "v_cvt_f32_i32 v50, v50\n"
    "s_add_u32 m0, s65, 29696\n"
    "s_add_u32 s72, s85, 5120\n"
    "buffer_load_dwordx4 v90, s[20:23], s72 offen offset:0 nt lds\n"
    "buffer_load_dwordx4 v[120:123], v162, s[28:31], s66 offen offset:3328\n"
    "v_cvt_f32_i32 v47, v47\n"
    "v_cvt_f32_i32 v51, v51\n"
    "v_cvt_pk_bf16_f32 v44, v44, v48\n"
    "v_cvt_pk_bf16_f32 v45, v45, v49\n"
    "v_cvt_pk_bf16_f32 v46, v46, v50\n"
    "v_cvt_pk_bf16_f32 v47, v47, v51\n"
    "buffer_store_dword v44, v244, s[16:19], s55 offen offset:0 sc1\n"
    "buffer_store_dword v45, v244, s[16:19], s55 offen offset:1024 sc1\n"
    "buffer_store_dword v46, v244, s[16:19], s55 offen offset:2048 sc1\n"
    "buffer_store_dword v47, v244, s[16:19], s55 offen offset:3072 sc1\n"
    "v_cvt_f32_i32 v52, v52\n"
    "v_cvt_f32_i32 v56, v56\n"
    "v_cvt_f32_i32 v53, v53\n"
    "v_cvt_f32_i32 v57, v57\n"
    "v_cvt_f32_i32 v54, v54\n"
    "v_cvt_f32_i32 v58, v58\n"
    "v_cvt_f32_i32 v55, v55\n"
    "v_cvt_f32_i32 v59, v59\n"
    "v_cvt_pk_bf16_f32 v52, v52, v56\n"
    "v_cvt_pk_bf16_f32 v53, v53, v57\n"
    "v_cvt_pk_bf16_f32 v54, v54, v58\n"
    "v_cvt_pk_bf16_f32 v55, v55, v59\n"
    "buffer_store_dword v52, v244, s[16:19], s56 offen offset:0 sc1\n"
    "buffer_store_dword v53, v244, s[16:19], s56 offen offset:1024 sc1\n"
    "buffer_store_dword v54, v244, s[16:19], s56 offen offset:2048 sc1\n"
    "buffer_store_dword v55, v244, s[16:19], s56 offen offset:3072 sc1\n"
    "s_add_u32 m0, s65, 73728\n"
    "s_add_u32 s72, s85, 6144\n"
    "buffer_load_dwordx4 v88, s[20:23], s72 offen offset:0 nt lds\n"
    "s_waitcnt vmcnt(44)\n"
    "v_lshlrev_b32 v204, 16, v136\n"
    "v_and_b32 v205, 0xffff0000, v136\n"
    "v_lshlrev_b32 v206, 16, v137\n"
    "v_and_b32 v207, 0xffff0000, v137\n"
    "v_lshlrev_b32 v208, 16, v138\n"
    "v_and_b32 v209, 0xffff0000, v138\n"
    "v_lshlrev_b32 v210, 16, v139\n"
    "v_and_b32 v211, 0xffff0000, v139\n"
    "v_log_f32 v68, v204\n"
    "v_log_f32 v69, v205\n"
    "v_log_f32 v70, v206\n"
    "v_log_f32 v71, v207\n"
    "v_log_f32 v72, v208\n"
    "v_log_f32 v73, v209\n"
    "v_log_f32 v74, v210\n"
    "v_log_f32 v75, v211\n"
    "v_rcp_f32 v204, v204\n"
    "v_rcp_f32 v205, v205\n"
    "v_rcp_f32 v206, v206\n"
    "v_rcp_f32 v207, v207\n"
    "v_rcp_f32 v208, v208\n"
    "v_rcp_f32 v209, v209\n"
    "v_rcp_f32 v210, v210\n"
    "v_rcp_f32 v211, v211\n"
    "s_mov_b32 s73, 0x41a00000\n"
    "v_mov_b32 v228, 0x7fc00000\n"
    "v_cmp_le_f32 vcc, |v68|, s73\n"
    "s_nop 1\n"
    "v_cndmask_b32 v68, v228, v68, vcc\n"
    "v_cmp_le_f32 vcc, |v69|, s73\n"
    "s_nop 1\n"
    "v_cndmask_b32 v69, v228, v69, vcc\n"
    "v_cmp_le_f32 vcc, |v70|, s73\n"
    "s_nop 1\n"
    "v_cndmask_b32 v70, v228, v70, vcc\n"
    "v_cmp_le_f32 vcc, |v71|, s73\n"
    "s_nop 1\n"
    "v_cndmask_b32 v71, v228, v71, vcc\n"
    "s_add_u32 m0, s65, 74752\n"
    "s_add_u32 s72, s85, 7168\n"
    "buffer_load_dwordx4 v89, s[20:23], s72 offen offset:0 nt lds\n"
    "v_cmp_le_f32 vcc, |v72|, s73\n"
    "s_nop 1\n"
    "v_cndmask_b32 v72, v228, v72, vcc\n"
    "v_cmp_le_f32 vcc, |v73|, s73\n"
    "s_nop 1\n"
    "v_cndmask_b32 v73, v228, v73, vcc\n"
    "v_cmp_le_f32 vcc, |v74|, s73\n"
    "s_nop 1\n"
    "v_cndmask_b32 v74, v228, v74, vcc\n"
    "v_cmp_le_f32 vcc, |v75|, s73\n"
    "s_nop 1\n"
    "v_cndmask_b32 v75, v228, v75, vcc\n"
    "v_cvt_pk_bf16_f32 v220, v204, v205\n"
    "v_cvt_pk_bf16_f32 v221, v206, v207\n"
    "v_cvt_pk_bf16_f32 v222, v208, v209\n"
    "v_cvt_pk_bf16_f32 v223, v210, v211\n"
    "v_lshlrev_b32 v212, 16, v220\n"
    "v_and_b32 v213, 0xffff0000, v220\n"
    "v_lshlrev_b32 v214, 16, v221\n"
    "v_and_b32 v215, 0xffff0000, v221\n"
    "v_lshlrev_b32 v216, 16, v222\n"
    "v_and_b32 v217, 0xffff0000, v222\n"
    "v_lshlrev_b32 v218, 16, v223\n"
    "v_and_b32 v219, 0xffff0000, v223\n"
    "v_sub_f32 v204, v204, v212\n"
    "v_sub_f32 v205, v205, v213\n"
    "v_sub_f32 v206, v206, v214\n"
    "v_sub_f32 v207, v207, v215\n"
    "v_sub_f32 v208, v208, v216\n"
    "v_sub_f32 v209, v209, v217\n"
    "v_sub_f32 v210, v210, v218\n"
    "v_sub_f32 v211, v211, v219\n"
    "v_cvt_pk_bf16_f32 v224, v204, v205\n"
    "v_cvt_pk_bf16_f32 v225, v206, v207\n"
    "v_cvt_pk_bf16_f32 v226, v208, v209\n"
    "v_cvt_pk_bf16_f32 v227, v210, v211\n"
    "v_mov_b32 v204, 0\n"
    "v_mov_b32 v205, 0\n"
    "v_mov_b32 v206, 0\n"
    "s_add_u32 m0, s65, 75776\n"
    "s_add_u32 s72, s85, 8192\n"
    "buffer_load_dwordx4 v90, s[20:23], s72 offen offset:0 nt lds\n"
    "v_mov_b32 v207, 0\n"
    "v_swap_b32 v221, v222\n"
    "v_swap_b32 v225, v226\n"
    "v_swap_b32 v141, v142\n"
    "ds_write2_b64 v144, v[220:221], v[222:223] offset0:32 offset1:40\n"
    "ds_write2_b64 v144, v[224:225], v[226:227] offset0:48 offset1:56\n"
    "ds_write2_b64 v144, v[140:141], v[142:143] offset0:64 offset1:72\n"
    "ds_write2_b64 v144, v[204:205], v[206:207] offset0:80 offset1:88\n"
    "s_waitcnt vmcnt(26)\n"
    "v_cvt_f32_i32 v4, v4\n"
    "v_cvt_f32_i32 v5, v5\n"
    "v_cvt_f32_i32 v6, v6\n"
    "v_cvt_f32_i32 v7, v7\n"
    "v_cvt_f32_i32 v8, v8\n"
    "v_cvt_f32_i32 v9, v9\n"
    "v_cvt_f32_i32 v10, v10\n"
    "v_cvt_f32_i32 v11, v11\n"
    "v_cvt_f32_i32 v12, v12\n"
    "v_cvt_f32_i32 v13, v13\n"
    "v_cvt_f32_i32 v14, v14\n"
    "v_cvt_f32_i32 v15, v15\n"
    "v_cvt_f32_i32 v16, v16\n"
    "v_cvt_f32_i32 v17, v17\n"
    "v_cvt_f32_i32 v18, v18\n"
    "v_cvt_f32_i32 v19, v19\n"
    "v_cvt_f32_i32 v20, v20\n"
    "v_cvt_f32_i32 v21, v21\n"
    "v_cvt_f32_i32 v22, v22\n"
    "v_cvt_f32_i32 v23, v23\n"
    "v_cvt_f32_i32 v24, v24\n"
    "v_cvt_f32_i32 v25, v25\n"
    "v_cvt_f32_i32 v26, v26\n"
    "v_cvt_f32_i32 v27, v27\n"
    "v_fma_f32 v4, v4, v180, v184\n"
    "v_fma_f32 v5, v5, v181, v185\n"
    "v_fma_f32 v6, v6, v182, v186\n"
    "s_add_u32 m0, s65, 76800\n"
    "s_add_u32 s72, s85, 9216\n"
    "buffer_load_dwordx4 v88, s[20:23], s72 offen offset:0 nt lds\n"
    "v_fma_f32 v7, v7, v183, v187\n"
    "v_fma_f32 v8, v8, v180, v184\n"
    "v_fma_f32 v9, v9, v181, v185\n"
    "v_fma_f32 v10, v10, v182, v186\n"
    "v_fma_f32 v11, v11, v183, v187\n"
    "v_cvt_pk_bf16_f32 v4, v4, v5\n"
    "v_cvt_pk_bf16_f32 v5, v6, v7\n"
    "v_cvt_pk_bf16_f32 v6, v8, v9\n"
    "v_cvt_pk_bf16_f32 v7, v10, v11\n"
    "ds_write_b64 v163, v[4:5] offset:0\n"
    "ds_write_b64 v163, v[6:7] offset:1024\n"
    "v_fma_f32 v12, v12, v188, v192\n"
    "v_fma_f32 v13, v13, v189, v193\n"
    "v_fma_f32 v14, v14, v190, v194\n"
    "v_fma_f32 v15, v15, v191, v195\n"
    "v_fma_f32 v16, v16, v188, v192\n"
    "v_fma_f32 v17, v17, v189, v193\n"
    "v_fma_f32 v18, v18, v190, v194\n"
    "v_fma_f32 v19, v19, v191, v195\n"
    "v_cvt_pk_bf16_f32 v12, v12, v13\n"
    "v_cvt_pk_bf16_f32 v13, v14, v15\n"
    "v_cvt_pk_bf16_f32 v14, v16, v17\n"
    "v_cvt_pk_bf16_f32 v15, v18, v19\n"
    "ds_write_b64 v163, v[12:13] offset:2048\n"
    "ds_write_b64 v163, v[14:15] offset:3072\n"
    "v_fma_f32 v20, v20, v196, v200\n"
    "v_fma_f32 v21, v21, v197, v201\n"
    "v_fma_f32 v22, v22, v198, v202\n"
    "v_fma_f32 v23, v23, v199, v203\n"
    "v_fma_f32 v24, v24, v196, v200\n"
    "v_fma_f32 v25, v25, v197, v201\n"
    "v_fma_f32 v26, v26, v198, v202\n"
    "v_fma_f32 v27, v27, v199, v203\n"
    "v_cvt_pk_bf16_f32 v20, v20, v21\n"
    "v_cvt_pk_bf16_f32 v21, v22, v23\n"
    "s_add_u32 m0, s65, 77824\n"
    "s_add_u32 s72, s85, 10240\n"
    "buffer_load_dwordx4 v89, s[20:23], s72 offen offset:0 nt lds\n"
    "v_cvt_pk_bf16_f32 v22, v24, v25\n"
    "v_cvt_pk_bf16_f32 v23, v26, v27\n"
    "ds_write_b64 v163, v[20:21] offset:4096\n"
    "ds_write_b64 v163, v[22:23] offset:5120\n"
    "s_waitcnt vmcnt(13)\n"
    "v_cvt_f32_i32 v100, v100\n"
    "v_cvt_f32_i32 v101, v101\n"
    "v_cvt_f32_i32 v102, v102\n"
    "v_cvt_f32_i32 v103, v103\n"
    "v_cvt_f32_i32 v104, v104\n"
    "v_cvt_f32_i32 v105, v105\n"
    "v_cvt_f32_i32 v106, v106\n"
    "v_cvt_f32_i32 v107, v107\n"
    "v_cvt_f32_i32 v108, v108\n"
    "v_cvt_f32_i32 v109, v109\n"
    "v_cvt_f32_i32 v110, v110\n"
    "v_cvt_f32_i32 v111, v111\n"
    "v_cvt_f32_i32 v112, v112\n"
    "v_cvt_f32_i32 v113, v113\n"
    "v_cvt_f32_i32 v114, v114\n"
    "v_cvt_f32_i32 v115, v115\n"
    "v_cvt_f32_i32 v116, v116\n"
    "v_cvt_f32_i32 v117, v117\n"
    "v_cvt_f32_i32 v118, v118\n"
    "v_cvt_f32_i32 v119, v119\n"
    "v_cvt_f32_i32 v120, v120\n"
    "v_cvt_f32_i32 v121, v121\n"
    "v_cvt_f32_i32 v122, v122\n"
    "v_cvt_f32_i32 v123, v123\n"
    "v_fma_f32 v100, v100, v180, v184\n"
    "v_fma_f32 v101, v101, v181, v185\n"
    "v_fma_f32 v102, v102, v182, v186\n"
    "v_fma_f32 v103, v103, v183, v187\n"
    "v_fma_f32 v104, v104, v180, v184\n"
    "v_fma_f32 v105, v105, v181, v185\n"
    "s_add_u32 m0, s65, 78848\n"
    "s_add_u32 s72, s85, 11264\n"
    "buffer_load_dwordx4 v90, s[20:23], s72 offen offset:0 nt lds\n"
    "v_fma_f32 v106, v106, v182, v186\n"
    "v_fma_f32 v107, v107, v183, v187\n"
    "v_cvt_pk_bf16_f32 v100, v100, v101\n"
    "v_cvt_pk_bf16_f32 v101, v102, v103\n"
    "v_cvt_pk_bf16_f32 v102, v104, v105\n"
    "v_cvt_pk_bf16_f32 v103, v106, v107\n"
    "ds_write_b64 v163, v[100:101] offset:12288\n"
    "ds_write_b64 v163, v[102:103] offset:13312\n"
    "v_fma_f32 v108, v108, v188, v192\n"
    "v_fma_f32 v109, v109, v189, v193\n"
    "v_fma_f32 v110, v110, v190, v194\n"
    "v_fma_f32 v111, v111, v191, v195\n"
    "v_fma_f32 v112, v112, v188, v192\n"
    "v_fma_f32 v113, v113, v189, v193\n"
    "v_fma_f32 v114, v114, v190, v194\n"
    "v_fma_f32 v115, v115, v191, v195\n"
    "v_cvt_pk_bf16_f32 v108, v108, v109\n"
    "v_cvt_pk_bf16_f32 v109, v110, v111\n"
    "v_cvt_pk_bf16_f32 v110, v112, v113\n"
    "v_cvt_pk_bf16_f32 v111, v114, v115\n"
    "ds_write_b64 v163, v[108:109] offset:14336\n"
    "ds_write_b64 v163, v[110:111] offset:15360\n"
    "v_fma_f32 v116, v116, v196, v200\n"
    "v_fma_f32 v117, v117, v197, v201\n"
    "v_fma_f32 v118, v118, v198, v202\n"
    "v_fma_f32 v119, v119, v199, v203\n"
    "v_fma_f32 v120, v120, v196, v200\n"
    "v_fma_f32 v121, v121, v197, v201\n"
    "v_fma_f32 v122, v122, v198, v202\n"
    "v_fma_f32 v123, v123, v199, v203\n"
    "v_cvt_pk_bf16_f32 v116, v116, v117\n"
    "v_cvt_pk_bf16_f32 v117, v118, v119\n"
    "v_cvt_pk_bf16_f32 v118, v120, v121\n"
    "v_cvt_pk_bf16_f32 v119, v122, v123\n"
    "ds_write_b64 v163, v[116:117] offset:16384\n"
    "ds_write_b64 v163, v[118:119] offset:17408\n"
    "s_waitcnt vmcnt(15)\n"
    "ds_read_b128 a[192:195], v154 offset:0\n"
    "ds_read_b128 a[196:199], v155 offset:0\n"
    "ds_read_b128 a[200:203], v154 offset:128\n"
    "ds_read_b128 a[204:207], v155 offset:128\n"
    "ds_read_b128 a[208:211], v154 offset:256\n"
    "ds_read_b128 a[212:215], v155 offset:256\n"
    "s_waitcnt lgkmcnt(0)\n"
    "s_barrier\n"
    "ds_read_b128 v[156:159], v2 offset:0\n"
    "ds_read_b128 v[160:163], v2 offset:2048\n"
    "ds_read_b128 v[164:167], v2 offset:4096\n"
    "ds_read_b128 v[168:171], v2 offset:6144\n"
    "ds_read_b128 v[172:175], v2 offset:8192\n"
    "ds_read_b128 v[176:179], v2 offset:10240\n"
    "ds_read_b128 v[180:183], v2 offset:1024\n"
    "ds_read_b128 v[184:187], v2 offset:3072\n"
    "ds_read_b128 v[188:191], v2 offset:5120\n"
    "ds_read_b128 v[192:195], v2 offset:7168\n"
    "ds_read_b128 v[196:199], v2 offset:9216\n"
    "ds_read_b128 v[200:203], v2 offset:11264\n"
    "s_add_u32 m0, s65, 49152\n"
    "s_add_u32 s72, s85, 12288\n"
    "buffer_load_dwordx4 v88, s[20:23], s72 offen offset:0 nt lds\n"
    "v_mov_b32 v108, 0\n"
    "v_mov_b32 v109, 0\n"
    "v_mov_b32 v110, 0\n"
    "v_mov_b32 v111, 0\n"
    "v_mov_b32 v112, 0\n"
    "v_mov_b32 v113, 0\n"
    "v_mov_b32 v114, 0\n"
    "v_mov_b32 v115, 0\n"
    "v_mov_b32 v116, 0\n"
    "v_mov_b32 v117, 0\n"
    "v_mov_b32 v118, 0\n"
    "v_mov_b32 v119, 0\n"
    "v_mov_b32 v120, 0\n"
    "v_mov_b32 v121, 0\n"
    "v_mov_b32 v122, 0\n"
    "v_mov_b32 v123, 0\n"
    "s_waitcnt lgkmcnt(11)\n"
    "v_mfma_f32_16x16x32_bf16 v[4:7], v[156:159], a[192:195], 0\n"
    "s_waitcnt lgkmcnt(10)\n"
    "v_mfma_f32_16x16x32_bf16 v[4:7], v[160:163], a[196:199], v[4:7]\n"
    "s_waitcnt lgkmcnt(9)\n"
    "v_mfma_f32_16x16x32_bf16 v[4:7], v[164:167], a[200:203], v[4:7]\n"
    "s_waitcnt lgkmcnt(8)\n"
    "v_mfma_f32_16x16x32_bf16 v[4:7], v[168:171], a[204:207], v[4:7]\n"
    "s_waitcnt lgkmcnt(7)\n"
    "v_mfma_f32_16x16x32_bf16 v[4:7], v[172:175], a[208:211], v[4:7]\n"
    "s_waitcnt lgkmcnt(6)\n"
    "v_mfma_f32_16x16x32_bf16 v[4:7], v[176:179], a[212:215], v[4:7]\n"
    "s_waitcnt lgkmcnt(5)\n"
    "v_mfma_f32_16x16x32_bf16 v[20:23], v[180:183], a[192:195], 0\n"
    "s_waitcnt lgkmcnt(4)\n"
    "v_mfma_f32_16x16x32_bf16 v[20:23], v[184:187], a[196:199], v[20:23]\n"
    "s_waitcnt lgkmcnt(3)\n"
    "v_mfma_f32_16x16x32_bf16 v[20:23], v[188:191], a[200:203], v[20:23]\n"
    "s_waitcnt lgkmcnt(2)\n"
    "v_mfma_f32_16x16x32_bf16 v[20:23], v[192:195], a[204:207], v[20:23]\n"
    "s_waitcnt lgkmcnt(1)\n"
    "v_mfma_f32_16x16x32_bf16 v[20:23], v[196:199], a[208:211], v[20:23]\n"
    "s_waitcnt lgkmcnt(0)\n"
    "v_mfma_f32_16x16x32_bf16 v[20:23], v[200:203], a[212:215], v[20:23]\n"
    "s_add_u32 m0, s65, 50176\n"
    "s_add_u32 s72, s85, 13312\n"
    "buffer_load_dwordx4 v89, s[20:23], s72 offen offset:0 nt lds\n"
    "v_mov_b32 v77, 155904\n"
    "v_max3_f32 v76, v4, v5, v6\n"
    "s_nop 2\n"
    "v_max3_f32 v76, v76, v7, v20\n"
    "v_max3_f32 v76, v76, v21, v22\n"
    "v_max_f32 v76, v76, v23\n"
    "v_mov_b32 v78, v76\n"
    "s_nop 1\n"
    "v_permlane32_swap_b32 v76, v78\n"
    "v_max_f32 v76, v76, v78\n"
    "v_mov_b32 v78, v76\n"
    "s_nop 1\n"
    "v_permlane16_swap_b32 v76, v78\n"
    "v_max_f32 v76, v76, v78\n"
    "ds_max_f32 v77, v76\n"
    "s_add_u32 m0, s65, 51200\n"
    "s_add_u32 s72, s85, 14336\n"
    "buffer_load_dwordx4 v90, s[20:23], s72 offen offset:0 nt lds\n"
    "buffer_load_dwordx2 v[236:237], v87, s[36:39], 0 offen offset:0 sc1\n"
    "s_waitcnt lgkmcnt(0)\n"
    "s_barrier\n"
    "ds_read_b32 v76, v77\n"
    "s_waitcnt vmcnt(10)\n"
    "buffer_store_dwordx2 v[238:239], v240, s[36:39], s80 offen offset:0 sc1\n"
    "s_lshl_b32 s73, s79, 8\n"
    "s_add_u32 s80, s80, s73\n"
    "s_mul_i32 s73, s79, 98304\n"
    "s_add_u32 s71, s71, s73\n"
    "s_waitcnt lgkmcnt(0)\n"
    "v_sub_f32 v68, v68, v76\n"
    "v_sub_f32 v69, v69, v76\n"
    "v_sub_f32 v70, v70, v76\n"
    "v_sub_f32 v71, v71, v76\n"
    "v_sub_f32 v72, v72, v76\n"
    "v_sub_f32 v73, v73, v76\n"
    "v_sub_f32 v74, v74, v76\n"
    "v_sub_f32 v75, v75, v76\n"
    "ds_write_b128 v86, v[68:71] offset:0\n"
    "ds_write_b128 v86, v[72:75] offset:16\n"
    "s_mul_i32 s73, s79, 768\n"
    "v_add_u32 v77, s73, v250\n"
    "s_add_u32 m0, s65, 52224\n"
    "s_add_u32 s72, s85, 15360\n"
    "buffer_load_dwordx4 v88, s[20:23], s72 offen offset:0 nt lds\n"
    "s_waitcnt lgkmcnt(0)\n"
    "s_barrier\n"
    "ds_read_b128 v[100:103], v77 offset:0\n"
    "ds_read_b128 v[104:107], v77 offset:16\n"
    "s_add_u32 m0, s65, 53248\n"
    "s_add_u32 s72, s85, 16384\n"
    "buffer_load_dwordx4 v89, s[20:23], s72 offen offset:0 nt lds\n"
    "s_waitcnt lgkmcnt(1)\n"
    "v_add_f32 v4, v4, v100\n"
    "v_add_f32 v5, v5, v101\n"
    "v_add_f32 v6, v6, v102\n"
    "v_add_f32 v7, v7, v103\n"
    "s_waitcnt lgkmcnt(0)\n"
    "v_add_f32 v20, v20, v104\n"
    "v_add_f32 v21, v21, v105\n"
    "v_add_f32 v22, v22, v106\n"
    "v_add_f32 v23, v23, v107\n"
    "v_exp_f32 v4, v4\n"
    "v_exp_f32 v5, v5\n"
    "v_exp_f32 v6, v6\n"
    "v_exp_f32 v7, v7\n"
    "v_exp_f32 v20, v20\n"
    "v_exp_f32 v21, v21\n"
    "v_exp_f32 v22, v22\n"
    "v_exp_f32 v23, v23\n"
    "s_add_u32 m0, s65, 54272\n"
    "s_add_u32 s72, s85, 17408\n"
    "buffer_load_dwordx4 v90, s[20:23], s72 offen offset:0 nt lds\n"
    "s_waitcnt vmcnt(8)\n"
    "ds_read_b128 a[216:219], v84 offset:24576\n"
    "ds_read_b128 a[220:223], v85 offset:24576\n"
    "ds_read_b128 a[224:227], v84 offset:24704\n"
    "ds_read_b128 a[228:231], v85 offset:24704\n"
    "ds_read_b128 a[232:235], v84 offset:24832\n"
    "ds_read_b128 a[236:239], v85 offset:24832\n"
    "s_waitcnt lgkmcnt(0)\n"
    "v_mfma_f32_16x16x32_bf16 v[8:11], v[176:179], a[236:239], v[100:103]\n"
    "v_mfma_f32_16x16x32_bf16 v[8:11], v[172:175], a[232:235], v[8:11]\n"
    "v_mfma_f32_16x16x32_bf16 v[8:11], v[168:171], a[228:231], v[8:11]\n"
    "v_mfma_f32_16x16x32_bf16 v[8:11], v[164:167], a[224:227], v[8:11]\n"
    "v_mfma_f32_16x16x32_bf16 v[8:11], v[160:163], a[220:223], v[8:11]\n"
    "v_mfma_f32_16x16x32_bf16 v[8:11], v[156:159], a[216:219], v[8:11]\n"
    "v_mfma_f32_16x16x32_bf16 v[24:27], v[200:203], a[236:239], v[104:107]\n"
    "v_mfma_f32_16x16x32_bf16 v[24:27], v[196:199], a[232:235], v[24:27]\n"
    "v_mfma_f32_16x16x32_bf16 v[24:27], v[192:195], a[228:231], v[24:27]\n"
    "v_mfma_f32_16x16x32_bf16 v[24:27], v[188:191], a[224:227], v[24:27]\n"
    "v_mfma_f32_16x16x32_bf16 v[24:27], v[184:187], a[220:223], v[24:27]\n"
    "v_mfma_f32_16x16x32_bf16 v[24:27], v[180:183], a[216:219], v[24:27]\n"
    "s_add_u32 m0, s65, 98304\n"
    "s_add_u32 s72, s85, 18432\n"
    "buffer_load_dwordx4 v88, s[20:23], s72 offen offset:0 nt lds\n"
    "v_exp_f32 v8, v8\n"
    "v_exp_f32 v9, v9\n"
    "v_exp_f32 v10, v10\n"
    "v_exp_f32 v11, v11\n"
    "s_nop 0\n"
    "v_exp_f32 v24, v24\n"
    "v_exp_f32 v25, v25\n"
    "v_exp_f32 v26, v26\n"
    "v_exp_f32 v27, v27\n"
    "s_add_u32 m0, s65, 99328\n"
    "s_add_u32 s72, s85, 19456\n"
    "buffer_load_dwordx4 v89, s[20:23], s72 offen offset:0 nt lds\n"
    "s_add_u32 m0, s65, 100352\n"
    "s_add_u32 s72, s85, 20480\n"
    "buffer_load_dwordx4 v90, s[20:23], s72 offen offset:0 nt lds\n"
    "s_waitcnt vmcnt(7)\n"
    "v_cmp_eq_u64 s[54:55], v[236:237], v[238:239]\n"
    "s_nop 0\n"
    "s_cmp_eq_u64 s[54:55], -1\n"
    "s_cbranch_scc0 .LpollAV\n"
    ".LpollAVr:\n"
    "s_add_u32 m0, s65, 101376\n"
    "s_add_u32 s72, s85, 21504\n"
    "buffer_load_dwordx4 v88, s[20:23], s72 offen offset:0 nt lds\n"
    "s_add_u32 m0, s65, 24576\n"
    "s_add_u32 s72, s65, 49152\n"
    "buffer_load_dwordx4 v1, s[16:19], s72 offen offset:0 sc1 lds\n"
    "s_add_u32 m0, s65, 25600\n"
    "s_add_u32 s72, s65, 50176\n"
    "buffer_load_dwordx4 v1, s[16:19], s72 offen offset:0 sc1 lds\n"
    "s_add_u32 m0, s65, 102400\n"
    "s_add_u32 s72, s85, 22528\n"
    "buffer_load_dwordx4 v89, s[20:23], s72 offen offset:0 nt lds\n"
    "s_add_u32 m0, s65, 26624\n"
    "s_add_u32 s72, s65, 51200\n"
    "buffer_load_dwordx4 v1, s[16:19], s72 offen offset:0 sc1 lds\n"
    "s_add_u32 m0, s65, 27648\n"
    "s_add_u32 s72, s65, 52224\n"
    "buffer_load_dwordx4 v1, s[16:19], s72 offen offset:0 sc1 lds\n"
    "s_add_u32 m0, s65, 103424\n"
    "s_add_u32 s72, s85, 23552\n"
    "buffer_load_dwordx4 v90, s[20:23], s72 offen offset:0 nt lds\n"
    "s_add_u32 m0, s65, 28672\n"
    "s_add_u32 s72, s65, 53248\n"
    "buffer_load_dwordx4 v1, s[16:19], s72 offen offset:0 sc1 lds\n"
    "s_add_u32 m0, s65, 29696\n"
    "s_add_u32 s72, s65, 54272\n"
    "buffer_load_dwordx4 v1, s[16:19], s72 offen offset:0 sc1 lds\n"
    "s_add_u32 m0, s65, 73728\n"
    "s_add_u32 s72, s65, 73728\n"
    "buffer_load_dwordx4 v1, s[16:19], s72 offen offset:0 sc1 lds\n"
    "s_add_u32 m0, s65, 74752\n"
    "s_add_u32 s72, s65, 74752\n"
    "buffer_load_dwordx4 v1, s[16:19], s72 offen offset:0 sc1 lds\n"
    "s_add_u32 m0, s65, 75776\n"
    "s_add_u32 s72, s65, 75776\n"
    "buffer_load_dwordx4 v1, s[16:19], s72 offen offset:0 sc1 lds\n"
    "s_add_u32 m0, s65, 76800\n"
    "s_add_u32 s72, s65, 76800\n"
    "buffer_load_dwordx4 v1, s[16:19], s72 offen offset:0 sc1 lds\n"
    "s_add_u32 m0, s65, 77824\n"
    "s_add_u32 s72, s65, 77824\n"
    "buffer_load_dwordx4 v1, s[16:19], s72 offen offset:0 sc1 lds\n"
    "s_add_u32 m0, s65, 78848\n"
    "s_add_u32 s72, s65, 78848\n"
    "buffer_load_dwordx4 v1, s[16:19], s72 offen offset:0 sc1 lds\n"
    "s_bitcmp1_b32 s83, 1\n"
    "s_cbranch_scc1 .Lf4b\n"
    "s_waitcnt vmcnt(18)\n"
    "ds_read_b128 a[240:243], v84 offset:0\n"
    "ds_read_b128 a[244:247], v85 offset:0\n"
    "ds_read_b128 a[248:251], v84 offset:128\n"
    "ds_read_b128 a[252:255], v85 offset:128\n"
    "ds_read_b128 v[124:127], v84 offset:256\n"
    "ds_read_b128 v[128:131], v85 offset:256\n"
    "s_waitcnt lgkmcnt(5)\n"
    "v_mfma_f32_16x16x32_bf16 v[12:15], v[156:159], a[240:243], v[100:103]\n"
    "s_waitcnt lgkmcnt(4)\n"
    "v_mfma_f32_16x16x32_bf16 v[12:15], v[160:163], a[244:247], v[12:15]\n"
    "s_waitcnt lgkmcnt(3)\n"
    "v_mfma_f32_16x16x32_bf16 v[12:15], v[164:167], a[248:251], v[12:15]\n"
    "s_waitcnt lgkmcnt(2)\n"
    "v_mfma_f32_16x16x32_bf16 v[12:15], v[168:171], a[252:255], v[12:15]\n"
    "s_waitcnt lgkmcnt(1)\n"
    "v_mfma_f32_16x16x32_bf16 v[12:15], v[172:175], v[124:127], v[12:15]\n"
    "s_waitcnt lgkmcnt(0)\n"
    "v_mfma_f32_16x16x32_bf16 v[12:15], v[176:179], v[128:131], v[12:15]\n"
    "v_mfma_f32_16x16x32_bf16 v[28:31], v[180:183], a[240:243], v[104:107]\n"
    "v_mfma_f32_16x16x32_bf16 v[28:31], v[184:187], a[244:247], v[28:31]\n"
    "v_mfma_f32_16x16x32_bf16 v[28:31], v[188:191], a[248:251], v[28:31]\n"
    "v_mfma_f32_16x16x32_bf16 v[28:31], v[192:195], a[252:255], v[28:31]\n"
    "v_mfma_f32_16x16x32_bf16 v[28:31], v[196:199], v[124:127], v[28:31]\n"
    "v_mfma_f32_16x16x32_bf16 v[28:31], v[200:203], v[128:131], v[28:31]\n"
    "s_nop 1\n"
    "v_exp_f32 v12, v12\n"
    "v_exp_f32 v13, v13\n"
    "v_exp_f32 v14, v14\n"
    "v_exp_f32 v15, v15\n"
    "s_nop 1\n"
    "v_exp_f32 v28, v28\n"
    "v_exp_f32 v29, v29\n"
    "v_exp_f32 v30, v30\n"
    "v_exp_f32 v31, v31\n"
    "s_waitcnt vmcnt(8)\n"
    "ds_read_b128 v[132:135], v84 offset:49152\n"
    "ds_read_b128 v[136:139], v85 offset:49152\n"
    "ds_read_b128 v[140:143], v84 offset:49280\n"
    "ds_read_b128 v[144:147], v85 offset:49280\n"
    "ds_read_b128 v[148:151], v84 offset:49408\n"
    "ds_read_b128 v[152:155], v85 offset:49408\n"
    "s_waitcnt lgkmcnt(0)\n"
    "v_mfma_f32_16x16x32_bf16 v[16:19], v[176:179], v[152:155], v[100:103]\n"
    "v_mfma_f32_16x16x32_bf16 v[16:19], v[172:175], v[148:151], v[16:19]\n"
    "v_mfma_f32_16x16x32_bf16 v[16:19], v[168:171], v[144:147], v[16:19]\n"
    "v_mfma_f32_16x16x32_bf16 v[16:19], v[164:167], v[140:143], v[16:19]\n"
    "v_mfma_f32_16x16x32_bf16 v[16:19], v[160:163], v[136:139], v[16:19]\n"
    "v_mfma_f32_16x16x32_bf16 v[16:19], v[156:159], v[132:135], v[16:19]\n"
    "v_mfma_f32_16x16x32_bf16 v[32:35], v[200:203], v[152:155], v[104:107]\n"
    "v_mfma_f32_16x16x32_bf16 v[32:35], v[196:199], v[148:151], v[32:35]\n"
    "v_mfma_f32_16x16x32_bf16 v[32:35], v[192:195], v[144:147], v[32:35]\n"
    "v_mfma_f32_16x16x32_bf16 v[32:35], v[188:191], v[140:143], v[32:35]\n"
    "v_mfma_f32_16x16x32_bf16 v[32:35], v[184:187], v[136:139], v[32:35]\n"
    "v_mfma_f32_16x16x32_bf16 v[32:35], v[180:183], v[132:135], v[32:35]\n"
    "ds_read_b128 v[156:159], v2 offset:12288\n"
    "ds_read_b128 v[160:163], v2 offset:14336\n"
    "ds_read_b128 v[164:167], v2 offset:16384\n"
    "ds_read_b128 v[168:171], v2 offset:18432\n"
    "ds_read_b128 v[172:175], v2 offset:20480\n"
    "ds_read_b128 v[176:179], v2 offset:22528\n"
    "ds_read_b128 v[100:103], v77 offset:128\n"
    "v_exp_f32 v16, v16\n"
    "v_exp_f32 v17, v17\n"
    "v_exp_f32 v18, v18\n"
    "v_exp_f32 v19, v19\n"
    "v_exp_f32 v32, v32\n"
    "v_exp_f32 v33, v33\n"
    "v_exp_f32 v34, v34\n"
    "v_exp_f32 v35, v35\n"
    "s_mov_b32 s66, 0\n"
    "s_waitcnt lgkmcnt(0)\n"
    "s_waitcnt vmcnt(0)\n"
    "ds_add_u32 v247, v246\n"
    "s_nop 7\n"
    "s_branch .LPV\n"
    "s_branch .Lf4e\n"
    ".Lf4b:\n"
    "s_add_u32 s75, s71, s63\n"
    "buffer_load_dwordx4 v[204:207], v242, s[40:43], s75 offen offset:0 nt\n"
    "buffer_load_dwordx4 v[208:211], v242, s[40:43], s75 offen offset:768 nt\n"
    "buffer_load_dwordx4 v[212:215], v242, s[40:43], s75 offen offset:256 nt\n"
    "buffer_load_dwordx4 v[216:219], v242, s[40:43], s75 offen offset:1024 nt\n"
    "buffer_load_dwordx4 v[220:223], v242, s[40:43], s75 offen offset:512 nt\n"
    "buffer_load_dwordx4 v[224:227], v242, s[40:43], s75 offen offset:1280 nt\n"
    "s_waitcnt vmcnt(24)\n"
    "ds_read_b128 a[240:243], v84 offset:0\n"
    "ds_read_b128 a[244:247], v85 offset:0\n"
    "ds_read_b128 a[248:251], v84 offset:128\n"
    "ds_read_b128 a[252:255], v85 offset:128\n"
    "ds_read_b128 v[124:127], v84 offset:256\n"
    "ds_read_b128 v[128:131], v85 offset:256\n"
    "s_waitcnt lgkmcnt(5)\n"
    "v_mfma_f32_16x16x32_bf16 v[12:15], v[156:159], a[240:243], v[100:103]\n"
    "s_waitcnt lgkmcnt(4)\n"
    "v_mfma_f32_16x16x32_bf16 v[12:15], v[160:163], a[244:247], v[12:15]\n"
    "s_waitcnt lgkmcnt(3)\n"
    "v_mfma_f32_16x16x32_bf16 v[12:15], v[164:167], a[248:251], v[12:15]\n"
    "s_waitcnt lgkmcnt(2)\n"
    "v_mfma_f32_16x16x32_bf16 v[12:15], v[168:171], a[252:255], v[12:15]\n"
    "s_waitcnt lgkmcnt(1)\n"
    "v_mfma_f32_16x16x32_bf16 v[12:15], v[172:175], v[124:127], v[12:15]\n"
    "s_waitcnt lgkmcnt(0)\n"
    "v_mfma_f32_16x16x32_bf16 v[12:15], v[176:179], v[128:131], v[12:15]\n"
    "v_mfma_f32_16x16x32_bf16 v[28:31], v[180:183], a[240:243], v[104:107]\n"
    "v_mfma_f32_16x16x32_bf16 v[28:31], v[184:187], a[244:247], v[28:31]\n"
    "v_mfma_f32_16x16x32_bf16 v[28:31], v[188:191], a[248:251], v[28:31]\n"
    "v_mfma_f32_16x16x32_bf16 v[28:31], v[192:195], a[252:255], v[28:31]\n"
    "v_mfma_f32_16x16x32_bf16 v[28:31], v[196:199], v[124:127], v[28:31]\n"
    "v_mfma_f32_16x16x32_bf16 v[28:31], v[200:203], v[128:131], v[28:31]\n"
    "s_nop 1\n"
    "v_exp_f32 v12, v12\n"
    "v_exp_f32 v13, v13\n"
    "v_exp_f32 v14, v14\n"
    "v_exp_f32 v15, v15\n"
    "s_nop 1\n"
    "v_exp_f32 v28, v28\n"
    "v_exp_f32 v29, v29\n"
    "v_exp_f32 v30, v30\n"
    "v_exp_f32 v31, v31\n"
    "s_waitcnt vmcnt(14)\n"
    "ds_read_b128 v[132:135], v84 offset:49152\n"
    "ds_read_b128 v[136:139], v85 offset:49152\n"
    "ds_read_b128 v[140:143], v84 offset:49280\n"
    "ds_read_b128 v[144:147], v85 offset:49280\n"
    "ds_read_b128 v[148:151], v84 offset:49408\n"
    "ds_read_b128 v[152:155], v85 offset:49408\n"
    "s_waitcnt lgkmcnt(0)\n"
    "v_mfma_f32_16x16x32_bf16 v[16:19], v[176:179], v[152:155], v[100:103]\n"
    "v_mfma_f32_16x16x32_bf16 v[16:19], v[172:175], v[148:151], v[16:19]\n"
    "v_mfma_f32_16x16x32_bf16 v[16:19], v[168:171], v[144:147], v[16:19]\n"
    "v_mfma_f32_16x16x32_bf16 v[16:19], v[164:167], v[140:143], v[16:19]\n"
    "v_mfma_f32_16x16x32_bf16 v[16:19], v[160:163], v[136:139], v[16:19]\n"
    "v_mfma_f32_16x16x32_bf16 v[16:19], v[156:159], v[132:135], v[16:19]\n"
    "v_mfma_f32_16x16x32_bf16 v[32:35], v[200:203], v[152:155], v[104:107]\n"
    "v_mfma_f32_16x16x32_bf16 v[32:35], v[196:199], v[148:151], v[32:35]\n"
    "v_mfma_f32_16x16x32_bf16 v[32:35], v[192:195], v[144:147], v[32:35]\n"
    "v_mfma_f32_16x16x32_bf16 v[32:35], v[188:191], v[140:143], v[32:35]\n"
    "v_mfma_f32_16x16x32_bf16 v[32:35], v[184:187], v[136:139], v[32:35]\n"
    "v_mfma_f32_16x16x32_bf16 v[32:35], v[180:183], v[132:135], v[32:35]\n"
    "ds_read_b128 v[156:159], v2 offset:12288\n"
    "ds_read_b128 v[160:163], v2 offset:14336\n"
    "ds_read_b128 v[164:167], v2 offset:16384\n"
    "ds_read_b128 v[168:171], v2 offset:18432\n"
    "ds_read_b128 v[172:175], v2 offset:20480\n"
    "ds_read_b128 v[176:179], v2 offset:22528\n"
    "ds_read_b128 v[100:103], v77 offset:128\n"
    "v_exp_f32 v16, v16\n"
    "v_exp_f32 v17, v17\n"
    "v_exp_f32 v18, v18\n"
    "v_exp_f32 v19, v19\n"
    "v_exp_f32 v32, v32\n"
    "v_exp_f32 v33, v33\n"
    "v_exp_f32 v34, v34\n"
    "v_exp_f32 v35, v35\n"
    "s_mov_b32 s66, 0\n"
    "s_waitcnt lgkmcnt(0)\n"
    "s_waitcnt vmcnt(6)\n"
    "ds_add_u32 v247, v246\n"
    "s_nop 7\n"
    "s_branch .LNV\n"
    ".Lf4e:\n"
    "s_nop 4\n"
    ".Lf5e:\n"
    "s_nop 4\n"
    ".p2align 6\n"
    ".LPK:\n"
    "v_mfma_f32_16x16x32_bf16 v[36:39], v[156:159], a[192:195], v[100:103]\n"
    "s_add_u32 s71, s71, 98304\n"
    "ds_read_b128 v[180:183], v2 offset:13312\n"
    "v_cvt_pk_bf16_f32 v68, v4, v5\n"
    "v_mfma_f32_16x16x32_bf16 v[36:39], v[160:163], a[196:199], v[36:39]\n"
    "s_add_u32 s75, s71, s63\n"
    "ds_read_b128 v[184:187], v2 offset:15360\n"
    "v_mfma_f32_16x16x32_bf16 v[36:39], v[164:167], a[200:203], v[36:39]\n"
    "s_add_u32 s76, s75, 6144\n"
    "ds_read_b128 v[188:191], v2 offset:17408\n"
    "v_mfma_f32_16x16x32_bf16 v[36:39], v[168:171], a[204:207], v[36:39]\n"
    "s_add_u32 s77, s75, 24576\n"
    "ds_read_b128 v[192:195], v2 offset:19456\n"
    "v_cvt_pk_bf16_f32 v69, v6, v7\n"
    "v_mfma_f32_16x16x32_bf16 v[36:39], v[172:175], a[208:211], v[36:39]\n"
    "s_add_u32 s78, s75, 30720\n"
    "ds_read_b128 v[196:199], v2 offset:21504\n"
    "v_mfma_f32_16x16x32_bf16 v[36:39], v[176:179], a[212:215], v[36:39]\n"
    "ds_read_b128 v[200:203], v2 offset:23552\n"
    "v_mfma_f32_16x16x32_bf16 v[40:43], v[176:179], a[236:239], v[100:103]\n"
    "ds_read_b128 v[104:107], v250 offset:144\n"
    "v_mfma_f32_16x16x32_bf16 v[40:43], v[172:175], a[232:235], v[40:43]\n"
    "s_add_u32 s68, s68, 24576\n"
    "v_cvt_pk_bf16_f32 v76, v8, v9\n"
    "v_mfma_f32_16x16x32_bf16 v[40:43], v[168:171], a[228:231], v[40:43]\n"
    "s_cmp_ge_u32 s68, s69\n"
    "s_cselect_b32 s68, s65, s68\n"
    "v_mfma_f32_16x16x32_bf16 v[40:43], v[164:167], a[224:227], v[40:43]\n"
    "v_exp_f32 v36, v36\n"
    "v_mfma_f32_16x16x32_bf16 v[40:43], v[160:163], a[220:223], v[40:43]\n"
    "v_exp_f32 v37, v37\n"
    "v_mfma_f32_16x16x32_bf16 v[40:43], v[156:159], a[216:219], v[40:43]\n"
    "v_cvt_pk_bf16_f32 v77, v10, v11\n"
    "v_cvt_pk_bf16_f32 v72, v36, v37\n"
    "v_mfma_f32_16x16x32_bf16 v[44:47], v[156:159], a[240:243], v[100:103]\n"
    "v_mfma_f32_16x16x32_bf16 v[44:47], v[160:163], a[244:247], v[44:47]\n"
    "v_exp_f32 v38, v38\n"
    "v_mfma_f32_16x16x32_bf16 v[44:47], v[164:167], a[248:251], v[44:47]\n"
    "v_mfma_f32_16x16x32_bf16 v[44:47], v[168:171], a[252:255], v[44:47]\n"
    "v_cvt_pk_bf16_f32 v84, v12, v13\n"
    "v_mfma_f32_16x16x32_bf16 v[44:47], v[172:175], v[124:127], v[44:47]\n"
    "v_exp_f32 v39, v39\n"
    "v_mfma_f32_16x16x32_bf16 v[44:47], v[176:179], v[128:131], v[44:47]\n"
    "v_cvt_pk_bf16_f32 v73, v38, v39\n"
    "v_mfma_f32_16x16x32_bf16 v[48:51], v[176:179], v[152:155], v[100:103]\n"
    "ds_read_b32 v245, v247\n"
    "v_cvt_pk_bf16_f32 v85, v14, v15\n"
    "v_mfma_f32_16x16x32_bf16 v[48:51], v[172:175], v[148:151], v[48:51]\n"
    "s_add_u32 m0, s68, 0\n"
    "v_exp_f32 v40, v40\n"
    "v_mfma_f32_16x16x32_bf16 v[48:51], v[168:171], v[144:147], v[48:51]\n"
    "v_mfma_f32_16x16x32_bf16 v[48:51], v[164:167], v[140:143], v[48:51]\n"
    "v_mfma_f32_16x16x32_bf16 v[48:51], v[160:163], v[136:139], v[48:51]\n"
    "s_waitcnt vmcnt(10)\n"
    "v_cmp_eq_u64 s[54:55], v[236:237], v[238:239]\n"
    "v_cvt_pk_bf16_f32 v92, v16, v17\n"
    "v_mfma_f32_16x16x32_bf16 v[48:51], v[156:159], v[132:135], v[48:51]\n"
    "s_waitcnt lgkmcnt(1)\n"
    "v_exp_f32 v41, v41\n"
    "v_mfma_f32_16x16x32_bf16 v[52:55], v[180:183], a[192:195], v[104:107]\n"
    "s_waitcnt lgkmcnt(0)\n"
    "v_cmp_le_u32 s[56:57], s59, v245\n"
    "v_cvt_pk_bf16_f32 v80, v40, v41\n"
    "v_mfma_f32_16x16x32_bf16 v[52:55], v[184:187], a[196:199], v[52:55]\n"
    "v_exp_f32 v42, v42\n"
    "v_mfma_f32_16x16x32_bf16 v[52:55], v[188:191], a[200:203], v[52:55]\n"
    "s_and_b64 s[54:55], s[54:55], s[56:57]\n"
    "s_cmp_eq_u64 s[54:55], -1\n"
    "v_mfma_f32_16x16x32_bf16 v[52:55], v[192:195], a[204:207], v[52:55]\n"
    "s_cbranch_scc0 .Lpoll1\n"
    ".Lpoll1r:\n"
    "v_mfma_f32_16x16x32_bf16 v[52:55], v[196:199], a[208:211], v[52:55]\n"
    "buffer_load_dwordx4 v1, s[16:19], s67 offen offset:0 sc1 lds\n"
    "v_mfma_f32_16x16x32_bf16 v[52:55], v[200:203], a[212:215], v[52:55]\n"
    "ds_read_b128 v[156:159], v3 offset:0\n"
    "ds_read_b128 v[160:163], v3 offset:2048\n"
    "v_mfma_f32_16x16x32_bf16 v[56:59], v[200:203], a[236:239], v[104:107]\n"
    "ds_read_b128 v[164:167], v3 offset:4096\n"
    "ds_read_b128 v[168:171], v3 offset:6144\n"
    "v_mfma_f32_16x16x32_bf16 v[56:59], v[196:199], a[232:235], v[56:59]\n"
    "ds_read_b128 v[172:175], v3 offset:8192\n"
    "ds_read_b128 v[176:179], v3 offset:10240\n"
    "v_mfma_f32_16x16x32_bf16 v[56:59], v[192:195], a[228:231], v[56:59]\n"
    "buffer_load_dwordx4 v1, s[16:19], s67 offen offset:1024 sc1 lds\n"
    "v_mfma_f32_16x16x32_bf16 v[56:59], v[188:191], a[224:227], v[56:59]\n"
    "ds_read_b128 v[100:103], v250 offset:768\n"
    "v_cvt_pk_bf16_f32 v93, v18, v19\n"
    "v_mfma_f32_16x16x32_bf16 v[56:59], v[184:187], a[220:223], v[56:59]\n"
    "v_cvt_pk_bf16_f32 v70, v20, v21\n"
    "v_cvt_pk_bf16_f32 v71, v22, v23\n"
    "v_mfma_f32_16x16x32_bf16 v[56:59], v[180:183], a[216:219], v[56:59]\n"
    "v_exp_f32 v43, v43\n"
    "v_mfma_f32_16x16x32_bf16 v[60:63], v[180:183], a[240:243], v[104:107]\n"
    "buffer_load_dwordx4 v1, s[16:19], s67 offen offset:2048 sc1 lds\n"
    "v_mfma_f32_16x16x32_bf16 v[60:63], v[184:187], a[244:247], v[60:63]\n"
    "v_cvt_pk_bf16_f32 v78, v24, v25\n"
    "v_cvt_pk_bf16_f32 v81, v42, v43\n"
    "v_mfma_f32_16x16x32_bf16 v[60:63], v[188:191], a[248:251], v[60:63]\n"
    "v_exp_f32 v44, v44\n"
    "v_mfma_f32_16x16x32_bf16 v[60:63], v[192:195], a[252:255], v[60:63]\n"
    "v_exp_f32 v45, v45\n"
    "v_mfma_f32_16x16x32_bf16 v[60:63], v[196:199], v[124:127], v[60:63]\n"
    "buffer_load_dwordx4 v1, s[16:19], s67 offen offset:3072 sc1 lds\n"
    "v_mfma_f32_16x16x32_bf16 v[60:63], v[200:203], v[128:131], v[60:63]\n"
    "s_add_u32 m0, s68, 4096\n"
    "v_cvt_pk_bf16_f32 v79, v26, v27\n"
    "v_cvt_pk_bf16_f32 v88, v44, v45\n"
    "v_mfma_f32_16x16x32_bf16 v[64:67], v[200:203], v[152:155], v[104:107]\n"
    "s_add_u32 s83, s67, 4096\n"
    "v_exp_f32 v46, v46\n"
    "v_mfma_f32_16x16x32_bf16 v[64:67], v[196:199], v[148:151], v[64:67]\n"
    "v_exp_f32 v47, v47\n"
    "v_mfma_f32_16x16x32_bf16 v[64:67], v[192:195], v[144:147], v[64:67]\n"
    "buffer_load_dwordx4 v1, s[16:19], s83 offen offset:0 sc1 lds\n"
    "v_mfma_f32_16x16x32_bf16 v[64:67], v[188:191], v[140:143], v[64:67]\n"
    "v_cvt_pk_bf16_f32 v86, v28, v29\n"
    "v_cvt_pk_bf16_f32 v89, v46, v47\n"
    "v_mfma_f32_16x16x32_bf16 v[64:67], v[184:187], v[136:139], v[64:67]\n"
    "v_exp_f32 v48, v48\n"
    "v_mfma_f32_16x16x32_bf16 v[64:67], v[180:183], v[132:135], v[64:67]\n"
    "s_waitcnt lgkmcnt(0)\n"
    "v_exp_f32 v49, v49\n"
    "v_mfma_f32_16x16x32_bf16 v[4:7], v[156:159], a[192:195], v[100:103]\n"
    "buffer_load_dwordx4 v1, s[16:19], s83 offen offset:1024 sc1 lds\n"
    "v_mfma_f32_16x16x32_bf16 v[4:7], v[160:163], a[196:199], v[4:7]\n"
    "s_add_u32 m0, s64, 0\n"
    "ds_read_b128 v[180:183], v3 offset:1024\n"
    "ds_read_b128 v[184:187], v3 offset:3072\n"
    "v_mfma_f32_16x16x32_bf16 v[4:7], v[164:167], a[200:203], v[4:7]\n"
    "s_add_u32 s83, s67, 24576\n"
    "ds_read_b128 v[188:191], v3 offset:5120\n"
    "v_cvt_pk_bf16_f32 v87, v30, v31\n"
    "v_mfma_f32_16x16x32_bf16 v[4:7], v[168:171], a[204:207], v[4:7]\n"
    "ds_read_b128 v[192:195], v3 offset:7168\n"
    "v_cvt_pk_bf16_f32 v96, v48, v49\n"
    "v_mfma_f32_16x16x32_bf16 v[4:7], v[172:175], a[208:211], v[4:7]\n"
    "buffer_load_dwordx4 v1, s[16:19], s83 offen offset:0 sc1 lds\n"
    "v_mfma_f32_16x16x32_bf16 v[4:7], v[176:179], a[212:215], v[4:7]\n"
    "ds_read_b128 v[196:199], v3 offset:9216\n"
    "ds_read_b128 v[200:203], v3 offset:11264\n"
    "v_mfma_f32_16x16x32_bf16 v[8:11], v[176:179], a[236:239], v[100:103]\n"
    "ds_read_b128 v[104:107], v250 offset:784\n"
    "v_cvt_pk_bf16_f32 v94, v32, v33\n"
    "v_mfma_f32_16x16x32_bf16 v[8:11], v[172:175], a[232:235], v[8:11]\n"
    "v_exp_f32 v52, v52\n"
    "v_mfma_f32_16x16x32_bf16 v[8:11], v[168:171], a[228:231], v[8:11]\n"
    "buffer_load_dwordx4 v1, s[16:19], s83 offen offset:1024 sc1 lds\n"
    "v_mfma_f32_16x16x32_bf16 v[8:11], v[164:167], a[224:227], v[8:11]\n"
    "v_cvt_pk_bf16_f32 v95, v34, v35\n"
    "v_mfma_f32_16x16x32_bf16 v[8:11], v[160:163], a[220:223], v[8:11]\n"
    "v_exp_f32 v53, v53\n"
    "v_mfma_f32_16x16x32_bf16 v[8:11], v[156:159], a[216:219], v[8:11]\n"
    "v_exp_f32 v54, v54\n"
    "v_mfma_f32_16x16x32_bf16 v[12:15], v[156:159], a[240:243], v[100:103]\n"
    "buffer_load_dwordx4 v1, s[16:19], s83 offen offset:2048 sc1 lds\n"
    "v_mfma_f32_16x16x32_bf16 v[12:15], v[160:163], a[244:247], v[12:15]\n"
    "v_cvt_pk_bf16_f32 v74, v52, v53\n"
    "v_mfma_f32_16x16x32_bf16 v[12:15], v[164:167], a[248:251], v[12:15]\n"
    "v_exp_f32 v55, v55\n"
    "v_mfma_f32_16x16x32_bf16 v[12:15], v[168:171], a[252:255], v[12:15]\n"
    "v_cvt_pk_bf16_f32 v75, v54, v55\n"
    "v_mfma_f32_16x16x32_bf16 v[12:15], v[172:175], v[124:127], v[12:15]\n"
    "buffer_load_dwordx4 v1, s[16:19], s83 offen offset:3072 sc1 lds\n"
    "v_mfma_f32_16x16x32_bf16 v[12:15], v[176:179], v[128:131], v[12:15]\n"
    "s_add_u32 m0, s64, 4096\n"
    "v_exp_f32 v56, v56\n"
    "v_mfma_f32_16x16x32_bf16 v[16:19], v[176:179], v[152:155], v[100:103]\n"
    "s_add_u32 s83, s67, 28672\n"
    "v_exp_f32 v50, v50\n"
    "v_mfma_f32_16x16x32_bf16 v[16:19], v[172:175], v[148:151], v[16:19]\n"
    "v_exp_f32 v51, v51\n"
    "v_mfma_f32_16x16x32_bf16 v[16:19], v[168:171], v[144:147], v[16:19]\n"
    "buffer_load_dwordx4 v1, s[16:19], s83 offen offset:0 sc1 lds\n"
    "v_mfma_f32_16x16x32_bf16 v[16:19], v[164:167], v[140:143], v[16:19]\n"
    "v_exp_f32 v57, v57\n"
    "v_mfma_f32_16x16x32_bf16 v[16:19], v[160:163], v[136:139], v[16:19]\n"
    "v_cvt_pk_bf16_f32 v82, v56, v57\n"
    "v_cvt_pk_bf16_f32 v97, v50, v51\n"
    "v_mfma_f32_16x16x32_bf16 v[16:19], v[156:159], v[132:135], v[16:19]\n"
    "s_waitcnt lgkmcnt(0)\n"
    "v_exp_f32 v58, v58\n"
    "v_mfma_f32_16x16x32_bf16 v[20:23], v[180:183], a[192:195], v[104:107]\n"
    "buffer_load_dwordx4 v1, s[16:19], s83 offen offset:1024 sc1 lds\n"
    "v_mfma_f32_16x16x32_bf16 v[20:23], v[184:187], a[196:199], v[20:23]\n"
    "s_add_u32 s67, s67, 49152\n"
    "ds_read_b128 v[156:159], v248 offset:0\n"
    "ds_read_b128 v[160:163], v248 offset:12288\n"
    "v_mfma_f32_16x16x32_bf16 v[20:23], v[188:191], a[200:203], v[20:23]\n"
    "s_add_u32 s59, s59, 4\n"
    "ds_read_b128 v[164:167], v248 offset:1024\n"
    "v_mfma_f32_16x16x32_bf16 v[20:23], v[192:195], a[204:207], v[20:23]\n"
    "s_add_u32 s81, s81, 128\n"
    "s_waitcnt vmcnt(12)\n"
    "ds_read_b128 v[168:171], v248 offset:13312\n"
    "v_mfma_f32_16x16x32_bf16 v[20:23], v[196:199], a[208:211], v[20:23]\n"
    "s_min_u32 s81, s81, 4096\n"
    "ds_read_b128 v[172:175], v248 offset:2048\n"
    "v_mfma_f32_16x16x32_bf16 v[20:23], v[200:203], a[212:215], v[20:23]\n"
    "buffer_store_dwordx2 v[238:239], v240, s[36:39], s80 offen offset:0 sc1\n"
    "v_mfma_f32_16x16x32_bf16 v[24:27], v[200:203], a[236:239], v[104:107]\n"
    "s_add_u32 s80, s80, 256\n"
    "ds_read_b128 v[176:179], v248 offset:14336\n"
    "v_mfma_f32_16x16x32_bf16 v[24:27], v[196:199], a[232:235], v[24:27]\n"
    "v_exp_f32 v59, v59\n"
    "v_mfma_f32_16x16x32_bf16 v[24:27], v[192:195], a[228:231], v[24:27]\n"
    "buffer_load_dwordx2 v[204:205], v242, s[28:31], s75 offen offset:0 nt\n"
    "v_mfma_f32_16x16x32_bf16 v[24:27], v[188:191], a[224:227], v[24:27]\n"
    "v_cvt_pk_bf16_f32 v83, v58, v59\n"
    "v_mfma_f32_16x16x32_bf16 v[24:27], v[184:187], a[220:223], v[24:27]\n"
    "buffer_load_dwordx2 v[206:207], v242, s[28:31], s75 offen offset:768 nt\n"
    "v_mfma_f32_16x16x32_bf16 v[24:27], v[180:183], a[216:219], v[24:27]\n"
    "v_exp_f32 v60, v60\n"
    "v_mfma_f32_16x16x32_bf16 v[28:31], v[180:183], a[240:243], v[104:107]\n"
    "buffer_load_dwordx2 v[208:209], v242, s[28:31], s75 offen offset:3072 nt\n"
    "v_mfma_f32_16x16x32_bf16 v[28:31], v[184:187], a[244:247], v[28:31]\n"
    "v_exp_f32 v61, v61\n"
    "v_mfma_f32_16x16x32_bf16 v[28:31], v[188:191], a[248:251], v[28:31]\n"
    "buffer_load_dwordx2 v[210:211], v242, s[28:31], s75 offen offset:3840 nt\n"
    "v_mfma_f32_16x16x32_bf16 v[28:31], v[192:195], a[252:255], v[28:31]\n"
    "v_exp_f32 v62, v62\n"
    "v_mfma_f32_16x16x32_bf16 v[28:31], v[196:199], v[124:127], v[28:31]\n"
    "buffer_load_dwordx2 v[212:213], v242, s[28:31], s76 offen offset:0 nt\n"
    "v_mfma_f32_16x16x32_bf16 v[28:31], v[200:203], v[128:131], v[28:31]\n"
    "v_exp_f32 v63, v63\n"
    "v_mfma_f32_16x16x32_bf16 v[32:35], v[200:203], v[152:155], v[104:107]\n"
    "v_cvt_pk_bf16_f32 v90, v60, v61\n"
    "v_cvt_pk_bf16_f32 v91, v62, v63\n"
    "v_mfma_f32_16x16x32_bf16 v[32:35], v[196:199], v[148:151], v[32:35]\n"
    "buffer_load_dwordx2 v[214:215], v242, s[28:31], s76 offen offset:768 nt\n"
    "v_mfma_f32_16x16x32_bf16 v[32:35], v[192:195], v[144:147], v[32:35]\n"
    "v_exp_f32 v64, v64\n"
    "v_mfma_f32_16x16x32_bf16 v[32:35], v[188:191], v[140:143], v[32:35]\n"
    "buffer_load_dwordx2 v[216:217], v242, s[28:31], s76 offen offset:3072 nt\n"
    "v_mfma_f32_16x16x32_bf16 v[32:35], v[184:187], v[136:139], v[32:35]\n"
    "v_exp_f32 v65, v65\n"
    "v_mfma_f32_16x16x32_bf16 v[32:35], v[180:183], v[132:135], v[32:35]\n"
    "s_waitcnt lgkmcnt(0)\n"
    "v_exp_f32 v66, v66\n"
    "v_mfma_f32_16x16x32_bf16 a[0:3], v[68:71], v[156:159], a[0:3]\n"
    "ds_read_b128 v[180:183], v248 offset:3072\n"
    "v_cvt_pk_bf16_f32 v98, v64, v65\n"
    "v_mfma_f32_16x16x32_bf16 a[0:3], v[72:75], v[160:163], a[0:3]\n"
    "ds_read_b128 v[184:187], v248 offset:15360\n"
    "v_mfma_f32_16x16x32_bf16 a[16:19], v[72:75], v[168:171], a[16:19]\n"
    "ds_read_b128 v[188:191], v248 offset:4096\n"
    "v_mfma_f32_16x16x32_bf16 a[16:19], v[68:71], v[164:167], a[16:19]\n"
    "ds_read_b128 v[192:195], v248 offset:16384\n"
    "v_mfma_f32_16x16x32_bf16 a[32:35], v[68:71], v[172:175], a[32:35]\n"
    "ds_read_b128 v[196:199], v248 offset:5120\n"
    "v_mfma_f32_16x16x32_bf16 a[32:35], v[72:75], v[176:179], a[32:35]\n"
    "ds_read_b128 v[200:203], v248 offset:17408\n"
    "v_mfma_f32_16x16x32_bf16 a[36:39], v[80:83], v[176:179], a[36:39]\n"
    "ds_read_b128 v[104:107], v251 offset:0\n"
    "v_mfma_f32_16x16x32_bf16 a[36:39], v[76:79], v[172:175], a[36:39]\n"
    "v_exp_f32 v67, v67\n"
    "v_mfma_f32_16x16x32_bf16 a[20:23], v[76:79], v[164:167], a[20:23]\n"
    "v_cvt_pk_bf16_f32 v99, v66, v67\n"
    "v_mfma_f32_16x16x32_bf16 a[20:23], v[80:83], v[168:171], a[20:23]\n"
    "buffer_load_dwordx2 v[218:219], v242, s[28:31], s76 offen offset:3840 nt\n"
    "v_mfma_f32_16x16x32_bf16 a[4:7], v[80:83], v[160:163], a[4:7]\n"
    "v_exp_f32 v4, v4\n"
    "v_mfma_f32_16x16x32_bf16 a[4:7], v[76:79], v[156:159], a[4:7]\n"
    "buffer_load_dwordx2 v[220:221], v242, s[28:31], s77 offen offset:0 nt\n"
    "v_mfma_f32_16x16x32_bf16 a[8:11], v[84:87], v[156:159], a[8:11]\n"
    "v_exp_f32 v5, v5\n"
    "v_mfma_f32_16x16x32_bf16 a[8:11], v[88:91], v[160:163], a[8:11]\n"
    "buffer_load_dwordx2 v[222:223], v242, s[28:31], s77 offen offset:768 nt\n"
    "v_mfma_f32_16x16x32_bf16 a[24:27], v[88:91], v[168:171], a[24:27]\n"
    "v_exp_f32 v6, v6\n"
    "v_mfma_f32_16x16x32_bf16 a[24:27], v[84:87], v[164:167], a[24:27]\n"
    "buffer_load_dwordx2 v[224:225], v242, s[28:31], s77 offen offset:3072 nt\n"
    "v_mfma_f32_16x16x32_bf16 a[40:43], v[84:87], v[172:175], a[40:43]\n"
    "v_exp_f32 v7, v7\n"
    "v_mfma_f32_16x16x32_bf16 a[40:43], v[88:91], v[176:179], a[40:43]\n"
    "buffer_load_dwordx2 v[226:227], v242, s[28:31], s77 offen offset:3840 nt\n"
    "v_mfma_f32_16x16x32_bf16 a[44:47], v[96:99], v[176:179], a[44:47]\n"
    "v_exp_f32 v8, v8\n"
    "v_mfma_f32_16x16x32_bf16 a[44:47], v[92:95], v[172:175], a[44:47]\n"
    "buffer_load_dwordx2 v[228:229], v242, s[28:31], s78 offen offset:0 nt\n"
    "v_mfma_f32_16x16x32_bf16 a[28:31], v[92:95], v[164:167], a[28:31]\n"
    "v_exp_f32 v9, v9\n"
    "v_mfma_f32_16x16x32_bf16 a[28:31], v[96:99], v[168:171], a[28:31]\n"
    "buffer_load_dwordx2 v[230:231], v242, s[28:31], s78 offen offset:768 nt\n"
    "v_mfma_f32_16x16x32_bf16 a[12:15], v[96:99], v[160:163], a[12:15]\n"
    "v_exp_f32 v10, v10\n"
    "v_mfma_f32_16x16x32_bf16 a[12:15], v[92:95], v[156:159], a[12:15]\n"
    "s_waitcnt lgkmcnt(0)\n"
    "v_exp_f32 v11, v11\n"
    "v_mfma_f32_16x16x32_bf16 a[48:51], v[68:71], v[180:183], a[48:51]\n"
    "ds_read_b128 v[156:159], v248 offset:6144\n"
    "v_mfma_f32_16x16x32_bf16 a[48:51], v[72:75], v[184:187], a[48:51]\n"
    "ds_read_b128 v[160:163], v248 offset:18432\n"
    "v_mfma_f32_16x16x32_bf16 a[64:67], v[72:75], v[192:195], a[64:67]\n"
    "ds_read_b128 v[164:167], v248 offset:7168\n"
    "v_mfma_f32_16x16x32_bf16 a[64:67], v[68:71], v[188:191], a[64:67]\n"
    "ds_read_b128 v[168:171], v248 offset:19456\n"
    "v_mfma_f32_16x16x32_bf16 a[80:83], v[68:71], v[196:199], a[80:83]\n"
    "ds_read_b128 v[172:175], v248 offset:8192\n"
    "v_mfma_f32_16x16x32_bf16 a[80:83], v[72:75], v[200:203], a[80:83]\n"
    "ds_read_b128 v[176:179], v248 offset:20480\n"
    "v_smfmac_f32_16x16x64_bf16 v[108:111], v[104:107], v[68:75], v253\n"
    "v_smfmac_f32_16x16x64_bf16 v[112:115], v[104:107], v[76:83], v253\n"
    "v_mfma_f32_16x16x32_bf16 a[84:87], v[80:83], v[200:203], a[84:87]\n"
    "buffer_load_dwordx2 v[232:233], v242, s[28:31], s78 offen offset:3072 nt\n"
    "v_mfma_f32_16x16x32_bf16 a[84:87], v[76:79], v[196:199], a[84:87]\n"
    "v_exp_f32 v12, v12\n"
    "v_mfma_f32_16x16x32_bf16 a[68:71], v[76:79], v[188:191], a[68:71]\n"
    "buffer_load_dwordx2 v[234:235], v242, s[28:31], s78 offen offset:3840 nt\n"
    "v_mfma_f32_16x16x32_bf16 a[68:71], v[80:83], v[192:195], a[68:71]\n"
    "v_exp_f32 v13, v13\n"
    "v_mfma_f32_16x16x32_bf16 a[52:55], v[80:83], v[184:187], a[52:55]\n"
    "v_exp_f32 v14, v14\n"
    "v_mfma_f32_16x16x32_bf16 a[52:55], v[76:79], v[180:183], a[52:55]\n"
    "v_exp_f32 v15, v15\n"
    "v_mfma_f32_16x16x32_bf16 a[56:59], v[84:87], v[180:183], a[56:59]\n"
    "v_exp_f32 v16, v16\n"
    "v_mfma_f32_16x16x32_bf16 a[56:59], v[88:91], v[184:187], a[56:59]\n"
    "v_exp_f32 v17, v17\n"
    "v_mfma_f32_16x16x32_bf16 a[72:75], v[88:91], v[192:195], a[72:75]\n"
    "v_mfma_f32_16x16x32_bf16 a[72:75], v[84:87], v[188:191], a[72:75]\n"
    "v_exp_f32 v18, v18\n"
    "v_mfma_f32_16x16x32_bf16 a[88:91], v[84:87], v[196:199], a[88:91]\n"
    "v_mfma_f32_16x16x32_bf16 a[88:91], v[88:91], v[200:203], a[88:91]\n"
    "v_smfmac_f32_16x16x64_bf16 v[116:119], v[104:107], v[84:91], v253\n"
    "v_smfmac_f32_16x16x64_bf16 v[120:123], v[104:107], v[92:99], v253\n"
    "v_mfma_f32_16x16x32_bf16 a[92:95], v[96:99], v[200:203], a[92:95]\n"
    "v_exp_f32 v19, v19\n"
    "v_mfma_f32_16x16x32_bf16 a[92:95], v[92:95], v[196:199], a[92:95]\n"
    "v_exp_f32 v20, v20\n"
    "v_mfma_f32_16x16x32_bf16 a[76:79], v[92:95], v[188:191], a[76:79]\n"
    "v_mfma_f32_16x16x32_bf16 a[76:79], v[96:99], v[192:195], a[76:79]\n"
    "v_exp_f32 v21, v21\n"
    "v_mfma_f32_16x16x32_bf16 a[60:63], v[96:99], v[184:187], a[60:63]\n"
    "v_mfma_f32_16x16x32_bf16 a[60:63], v[92:95], v[180:183], a[60:63]\n"
    "s_waitcnt lgkmcnt(0)\n"
    "v_mfma_f32_16x16x32_bf16 a[96:99], v[68:71], v[156:159], a[96:99]\n"
    "ds_read_b128 v[180:183], v248 offset:9216\n"
    "v_mfma_f32_16x16x32_bf16 a[96:99], v[72:75], v[160:163], a[96:99]\n"
    "ds_read_b128 v[184:187], v248 offset:21504\n"
    "v_mfma_f32_16x16x32_bf16 a[112:115], v[72:75], v[168:171], a[112:115]\n"
    "ds_read_b128 v[188:191], v248 offset:10240\n"
    "v_mfma_f32_16x16x32_bf16 a[112:115], v[68:71], v[164:167], a[112:115]\n"
    "ds_read_b128 v[192:195], v248 offset:22528\n"
    "v_mfma_f32_16x16x32_bf16 a[128:131], v[68:71], v[172:175], a[128:131]\n"
    "buffer_load_dwordx2 v[236:237], v241, s[36:39], s81 offen offset:0 sc1\n"
    "v_mfma_f32_16x16x32_bf16 a[128:131], v[72:75], v[176:179], a[128:131]\n"
    "ds_read_b128 v[196:199], v248 offset:11264\n"
    "ds_read_b128 v[200:203], v248 offset:23552\n"
    "v_mfma_f32_16x16x32_bf16 a[132:135], v[80:83], v[176:179], a[132:135]\n"
    "v_exp_f32 v22, v22\n"
    "v_mfma_f32_16x16x32_bf16 a[132:135], v[76:79], v[172:175], a[132:135]\n"
    "v_exp_f32 v23, v23\n"
    "v_mfma_f32_16x16x32_bf16 a[116:119], v[76:79], v[164:167], a[116:119]\n"
    "v_exp_f32 v24, v24\n"
    "v_mfma_f32_16x16x32_bf16 a[116:119], v[80:83], v[168:171], a[116:119]\n"
    "v_exp_f32 v25, v25\n"
    "v_mfma_f32_16x16x32_bf16 a[100:103], v[80:83], v[160:163], a[100:103]\n"
    "v_mfma_f32_16x16x32_bf16 a[100:103], v[76:79], v[156:159], a[100:103]\n"
    "v_exp_f32 v26, v26\n"
    "v_mfma_f32_16x16x32_bf16 a[104:107], v[84:87], v[156:159], a[104:107]\n"
    "v_mfma_f32_16x16x32_bf16 a[104:107], v[88:91], v[160:163], a[104:107]\n"
    "v_exp_f32 v27, v27\n"
    "v_mfma_f32_16x16x32_bf16 a[120:123], v[88:91], v[168:171], a[120:123]\n"
    "v_mfma_f32_16x16x32_bf16 a[120:123], v[84:87], v[164:167], a[120:123]\n"
    "v_mfma_f32_16x16x32_bf16 a[136:139], v[84:87], v[172:175], a[136:139]\n"
    "v_exp_f32 v28, v28\n"
    "v_mfma_f32_16x16x32_bf16 a[136:139], v[88:91], v[176:179], a[136:139]\n"
    "v_mfma_f32_16x16x32_bf16 a[140:143], v[96:99], v[176:179], a[140:143]\n"
    "v_mfma_f32_16x16x32_bf16 a[140:143], v[92:95], v[172:175], a[140:143]\n"
    "v_exp_f32 v29, v29\n"
    "v_mfma_f32_16x16x32_bf16 a[124:127], v[92:95], v[164:167], a[124:127]\n"
    "v_mfma_f32_16x16x32_bf16 a[124:127], v[96:99], v[168:171], a[124:127]\n"
    "v_exp_f32 v30, v30\n"
    "v_mfma_f32_16x16x32_bf16 a[108:111], v[96:99], v[160:163], a[108:111]\n"
    "v_mfma_f32_16x16x32_bf16 a[108:111], v[92:95], v[156:159], a[108:111]\n"
    "s_waitcnt lgkmcnt(0)\n"
    "v_mfma_f32_16x16x32_bf16 a[144:147], v[68:71], v[180:183], a[144:147]\n"
    "ds_read_b128 v[156:159], v3 offset:12288\n"
    "v_mfma_f32_16x16x32_bf16 a[144:147], v[72:75], v[184:187], a[144:147]\n"
    "ds_read_b128 v[160:163], v3 offset:14336\n"
    "v_mfma_f32_16x16x32_bf16 a[160:163], v[72:75], v[192:195], a[160:163]\n"
    "ds_read_b128 v[164:167], v3 offset:16384\n"
    "v_mfma_f32_16x16x32_bf16 a[160:163], v[68:71], v[188:191], a[160:163]\n"
    "ds_read_b128 v[168:171], v3 offset:18432\n"
    "v_mfma_f32_16x16x32_bf16 a[176:179], v[68:71], v[196:199], a[176:179]\n"
    "ds_read_b128 v[172:175], v3 offset:20480\n"
    "v_mfma_f32_16x16x32_bf16 a[176:179], v[72:75], v[200:203], a[176:179]\n"
    "ds_read_b128 v[176:179], v3 offset:22528\n"
    "v_mfma_f32_16x16x32_bf16 a[180:183], v[80:83], v[200:203], a[180:183]\n"
    "ds_read_b128 v[100:103], v250 offset:896\n"
    "v_mfma_f32_16x16x32_bf16 a[180:183], v[76:79], v[196:199], a[180:183]\n"
    "v_exp_f32 v31, v31\n"
    "v_mfma_f32_16x16x32_bf16 a[164:167], v[76:79], v[188:191], a[164:167]\n"
    "v_exp_f32 v32, v32\n"
    "v_mfma_f32_16x16x32_bf16 a[164:167], v[80:83], v[192:195], a[164:167]\n"
    "v_exp_f32 v33, v33\n"
    "v_mfma_f32_16x16x32_bf16 a[148:151], v[80:83], v[184:187], a[148:151]\n"
    "v_exp_f32 v34, v34\n"
    "v_mfma_f32_16x16x32_bf16 a[148:151], v[76:79], v[180:183], a[148:151]\n"
    "v_exp_f32 v35, v35\n"
    "v_mfma_f32_16x16x32_bf16 a[152:155], v[84:87], v[180:183], a[152:155]\n"
    "v_mfma_f32_16x16x32_bf16 a[152:155], v[88:91], v[184:187], a[152:155]\n"
    "v_mfma_f32_16x16x32_bf16 a[168:171], v[88:91], v[192:195], a[168:171]\n"
    "s_waitcnt vmcnt(18)\n"
    "ds_add_u32 v247, v246\n"
    "v_mfma_f32_16x16x32_bf16 a[168:171], v[84:87], v[188:191], a[168:171]\n"
    "v_mfma_f32_16x16x32_bf16 a[184:187], v[84:87], v[196:199], a[184:187]\n"
    "v_mfma_f32_16x16x32_bf16 a[184:187], v[88:91], v[200:203], a[184:187]\n"
    "v_mfma_f32_16x16x32_bf16 a[188:191], v[96:99], v[200:203], a[188:191]\n"
    "v_mfma_f32_16x16x32_bf16 a[188:191], v[92:95], v[196:199], a[188:191]\n"
    "v_mfma_f32_16x16x32_bf16 a[172:175], v[92:95], v[188:191], a[172:175]\n"
    "s_sub_u32 s74, s68, s65\n"
    "v_mfma_f32_16x16x32_bf16 a[172:175], v[96:99], v[192:195], a[172:175]\n"
    "s_add_u32 s66, s66, 1\n"
    "v_mov_b32 v2, v3\n"
    "v_add_u32 v3, s74, v252\n"
    "v_mfma_f32_16x16x32_bf16 a[156:159], v[96:99], v[184:187], a[156:159]\n"
    "s_waitcnt lgkmcnt(1)\n"
    "s_cmp_eq_u32 s66, 31\n"
    "v_mfma_f32_16x16x32_bf16 a[156:159], v[92:95], v[180:183], a[156:159]\n"
    "s_cbranch_scc1 .LtailP\n"
    ".LNK:\n"
    "v_mfma_f32_16x16x32_bf16 v[36:39], v[156:159], a[192:195], v[100:103]\n"
    "ds_read_b128 v[180:183], v2 offset:13312\n"
    "v_cvt_pk_bf16_f32 v68, v4, v5\n"
    "v_mfma_f32_16x16x32_bf16 v[36:39], v[160:163], a[196:199], v[36:39]\n"
    "ds_read_b128 v[184:187], v2 offset:15360\n"
    "v_cvt_pk_bf16_f32 v69, v6, v7\n"
    "v_mfma_f32_16x16x32_bf16 v[36:39], v[164:167], a[200:203], v[36:39]\n"
    "ds_read_b128 v[188:191], v2 offset:17408\n"
    "v_cvt_pk_bf16_f32 v76, v8, v9\n"
    "v_mfma_f32_16x16x32_bf16 v[36:39], v[168:171], a[204:207], v[36:39]\n"
    "ds_read_b128 v[192:195], v2 offset:19456\n"
    "v_cvt_pk_bf16_f32 v77, v10, v11\n"
    "v_mfma_f32_16x16x32_bf16 v[36:39], v[172:175], a[208:211], v[36:39]\n"
    "ds_read_b128 v[196:199], v2 offset:21504\n"
    "v_cvt_pk_bf16_f32 v84, v12, v13\n"
    "v_mfma_f32_16x16x32_bf16 v[36:39], v[176:179], a[212:215], v[36:39]\n"
    "ds_read_b128 v[200:203], v2 offset:23552\n"
    "v_cvt_pk_bf16_f32 v85, v14, v15\n"
    "v_mfma_f32_16x16x32_bf16 v[40:43], v[176:179], a[236:239], v[100:103]\n"
    "ds_read_b128 v[104:107], v250 offset:912\n"
    "v_cvt_pk_bf16_f32 v92, v16, v17\n"
    "v_mfma_f32_16x16x32_bf16 v[40:43], v[172:175], a[232:235], v[40:43]\n"
    "s_add_u32 s68, s68, 24576\n"
    "v_cvt_pk_bf16_f32 v93, v18, v19\n"
    "v_cvt_pk_bf16_f32 v70, v20, v21\n"
    "v_mfma_f32_16x16x32_bf16 v[40:43], v[168:171], a[228:231], v[40:43]\n"
    "s_cmp_ge_u32 s68, s69\n"
    "s_cselect_b32 s68, s65, s68\n"
    "v_cvt_pk_bf16_f32 v71, v22, v23\n"
    "v_mfma_f32_16x16x32_bf16 v[40:43], v[164:167], a[224:227], v[40:43]\n"
    "v_cvt_pk_bf16_f32 v78, v24, v25\n"
    "v_cvt_pk_bf16_f32 v79, v26, v27\n"
    "v_mfma_f32_16x16x32_bf16 v[40:43], v[160:163], a[220:223], v[40:43]\n"
    "v_cvt_pk_bf16_f32 v86, v28, v29\n"
    "v_cvt_pk_bf16_f32 v87, v30, v31\n"
    "v_mfma_f32_16x16x32_bf16 v[40:43], v[156:159], a[216:219], v[40:43]\n"
    "v_cvt_pk_bf16_f32 v94, v32, v33\n"
    "v_cvt_pk_bf16_f32 v95, v34, v35\n"
    "v_mfma_f32_16x16x32_bf16 v[44:47], v[156:159], a[240:243], v[100:103]\n"
    "v_exp_f32 v36, v36\n"
    "v_mfma_f32_16x16x32_bf16 v[44:47], v[160:163], a[244:247], v[44:47]\n"
    "v_exp_f32 v37, v37\n"
    "v_mfma_f32_16x16x32_bf16 v[44:47], v[164:167], a[248:251], v[44:47]\n"
    "v_exp_f32 v38, v38\n"
    "v_mfma_f32_16x16x32_bf16 v[44:47], v[168:171], a[252:255], v[44:47]\n"
    "v_cvt_pk_bf16_f32 v72, v36, v37\n"
    "v_mfma_f32_16x16x32_bf16 v[44:47], v[172:175], v[124:127], v[44:47]\n"
    "v_exp_f32 v39, v39\n"
    "v_mfma_f32_16x16x32_bf16 v[44:47], v[176:179], v[128:131], v[44:47]\n"
    "v_cvt_pk_bf16_f32 v73, v38, v39\n"
    "v_mfma_f32_16x16x32_bf16 v[48:51], v[176:179], v[152:155], v[100:103]\n"
    "ds_read_b32 v245, v247\n"
    "v_mfma_f32_16x16x32_bf16 v[48:51], v[172:175], v[148:151], v[48:51]\n"
    "s_add_u32 m0, s68, 0\n"
    "v_exp_f32 v40, v40\n"
    "v_mfma_f32_16x16x32_bf16 v[48:51], v[168:171], v[144:147], v[48:51]\n"
    "v_mfma_f32_16x16x32_bf16 v[48:51], v[164:167], v[140:143], v[48:51]\n"
    "v_mfma_f32_16x16x32_bf16 v[48:51], v[160:163], v[136:139], v[48:51]\n"
    "s_waitcnt vmcnt(0)\n"
    "v_cmp_eq_u64 s[54:55], v[236:237], v[238:239]\n"
    "v_mfma_f32_16x16x32_bf16 v[48:51], v[156:159], v[132:135], v[48:51]\n"
    "s_waitcnt lgkmcnt(1)\n"
    "v_exp_f32 v41, v41\n"
    "v_mfma_f32_16x16x32_bf16 v[52:55], v[180:183], a[192:195], v[104:107]\n"
    "s_waitcnt lgkmcnt(0)\n"
    "s_waitcnt vmcnt(1)\n"
    "v_cmp_le_u32 s[56:57], s59, v245\n"
    "v_mfma_f32_16x16x32_bf16 v[52:55], v[184:187], a[196:199], v[52:55]\n"
    "s_add_u32 s76, s71, s60\n"
    "v_exp_f32 v42, v42\n"
    "v_mfma_f32_16x16x32_bf16 v[52:55], v[188:191], a[200:203], v[52:55]\n"
    "s_and_b64 s[54:55], s[54:55], s[56:57]\n"
    "s_cmp_eq_u64 s[54:55], -1\n"
    "v_mfma_f32_16x16x32_bf16 v[52:55], v[192:195], a[204:207], v[52:55]\n"
    "s_cbranch_scc0 .Lpoll2\n"
    ".Lpoll2r:\n"
    "v_mfma_f32_16x16x32_bf16 v[52:55], v[196:199], a[208:211], v[52:55]\n"
    "buffer_load_dwordx4 v1, s[16:19], s67 offen offset:0 sc1 lds\n"
    "v_mfma_f32_16x16x32_bf16 v[52:55], v[200:203], a[212:215], v[52:55]\n"
    "s_add_u32 s77, s71, s61\n"
    "ds_read_b128 v[156:159], v3 offset:0\n"
    "ds_read_b128 v[160:163], v3 offset:2048\n"
    "v_mfma_f32_16x16x32_bf16 v[56:59], v[200:203], a[236:239], v[104:107]\n"
    "ds_read_b128 v[164:167], v3 offset:4096\n"
    "ds_read_b128 v[168:171], v3 offset:6144\n"
    "v_mfma_f32_16x16x32_bf16 v[56:59], v[196:199], a[232:235], v[56:59]\n"
    "ds_read_b128 v[172:175], v3 offset:8192\n"
    "ds_read_b128 v[176:179], v3 offset:10240\n"
    "v_mfma_f32_16x16x32_bf16 v[56:59], v[192:195], a[228:231], v[56:59]\n"
    "buffer_load_dwordx4 v1, s[16:19], s67 offen offset:1024 sc1 lds\n"
    "v_mfma_f32_16x16x32_bf16 v[56:59], v[188:191], a[224:227], v[56:59]\n"
    "ds_read_b128 v[100:103], v250 offset:1536\n"
    "v_cvt_pk_bf16_f32 v80, v40, v41\n"
    "v_mfma_f32_16x16x32_bf16 v[56:59], v[184:187], a[220:223], v[56:59]\n"
    "v_exp_f32 v43, v43\n"
    "v_mfma_f32_16x16x32_bf16 v[56:59], v[180:183], a[216:219], v[56:59]\n"
    "v_cvt_pk_bf16_f32 v81, v42, v43\n"
    "v_cvt_f32_i32 v204, v204\n"
    "v_mfma_f32_16x16x32_bf16 v[60:63], v[180:183], a[240:243], v[104:107]\n"
    "buffer_load_dwordx4 v1, s[16:19], s67 offen offset:2048 sc1 lds\n"
    "v_mfma_f32_16x16x32_bf16 v[60:63], v[184:187], a[244:247], v[60:63]\n"
    "v_exp_f32 v44, v44\n"
    "v_mfma_f32_16x16x32_bf16 v[60:63], v[188:191], a[248:251], v[60:63]\n"
    "v_exp_f32 v45, v45\n"
    "v_mfma_f32_16x16x32_bf16 v[60:63], v[192:195], a[252:255], v[60:63]\n"
    "v_exp_f32 v46, v46\n"
    "v_mfma_f32_16x16x32_bf16 v[60:63], v[196:199], v[124:127], v[60:63]\n"
    "buffer_load_dwordx4 v1, s[16:19], s67 offen offset:3072 sc1 lds\n"
    "v_mfma_f32_16x16x32_bf16 v[60:63], v[200:203], v[128:131], v[60:63]\n"
    "s_add_u32 m0, s68, 4096\n"
    "v_exp_f32 v47, v47\n"
    "v_mfma_f32_16x16x32_bf16 v[64:67], v[200:203], v[152:155], v[104:107]\n"
    "s_add_u32 s83, s67, 4096\n"
    "v_cvt_pk_bf16_f32 v88, v44, v45\n"
    "v_cvt_pk_bf16_f32 v89, v46, v47\n"
    "v_mfma_f32_16x16x32_bf16 v[64:67], v[196:199], v[148:151], v[64:67]\n"
    "v_exp_f32 v48, v48\n"
    "v_mfma_f32_16x16x32_bf16 v[64:67], v[192:195], v[144:147], v[64:67]\n"
    "buffer_load_dwordx4 v1, s[16:19], s83 offen offset:0 sc1 lds\n"
    "v_mfma_f32_16x16x32_bf16 v[64:67], v[188:191], v[140:143], v[64:67]\n"
    "v_exp_f32 v49, v49\n"
    "v_mfma_f32_16x16x32_bf16 v[64:67], v[184:187], v[136:139], v[64:67]\n"
    "v_cvt_pk_bf16_f32 v96, v48, v49\n"
    "v_cvt_f32_i32 v205, v205\n"
    "v_mfma_f32_16x16x32_bf16 v[64:67], v[180:183], v[132:135], v[64:67]\n"
    "s_waitcnt lgkmcnt(0)\n"
    "v_cvt_f32_i32 v206, v206\n"
    "v_cvt_f32_i32 v207, v207\n"
    "v_mfma_f32_16x16x32_bf16 v[4:7], v[156:159], a[192:195], v[100:103]\n"
    "buffer_load_dwordx4 v1, s[16:19], s83 offen offset:1024 sc1 lds\n"
    "v_mfma_f32_16x16x32_bf16 v[4:7], v[160:163], a[196:199], v[4:7]\n"
    "s_add_u32 m0, s70, 0\n"
    "ds_read_b128 v[180:183], v3 offset:1024\n"
    "ds_read_b128 v[184:187], v3 offset:3072\n"
    "v_mfma_f32_16x16x32_bf16 v[4:7], v[164:167], a[200:203], v[4:7]\n"
    "s_add_u32 s83, s67, 24576\n"
    "ds_read_b128 v[188:191], v3 offset:5120\n"
    "v_cvt_f32_i32 v208, v208\n"
    "v_mfma_f32_16x16x32_bf16 v[4:7], v[168:171], a[204:207], v[4:7]\n"
    "ds_read_b128 v[192:195], v3 offset:7168\n"
    "v_cvt_f32_i32 v209, v209\n"
    "v_mfma_f32_16x16x32_bf16 v[4:7], v[172:175], a[208:211], v[4:7]\n"
    "buffer_load_dwordx4 v1, s[16:19], s83 offen offset:0 sc1 lds\n"
    "v_mfma_f32_16x16x32_bf16 v[4:7], v[176:179], a[212:215], v[4:7]\n"
    "ds_read_b128 v[196:199], v3 offset:9216\n"
    "ds_read_b128 v[200:203], v3 offset:11264\n"
    "v_mfma_f32_16x16x32_bf16 v[8:11], v[176:179], a[236:239], v[100:103]\n"
    "ds_read_b128 v[104:107], v250 offset:1552\n"
    "v_cvt_f32_i32 v210, v210\n"
    "v_mfma_f32_16x16x32_bf16 v[8:11], v[172:175], a[232:235], v[8:11]\n"
    "v_exp_f32 v52, v52\n"
    "v_mfma_f32_16x16x32_bf16 v[8:11], v[168:171], a[228:231], v[8:11]\n"
    "buffer_load_dwordx4 v1, s[16:19], s83 offen offset:1024 sc1 lds\n"
    "v_mfma_f32_16x16x32_bf16 v[8:11], v[164:167], a[224:227], v[8:11]\n"
    "v_exp_f32 v53, v53\n"
    "v_mfma_f32_16x16x32_bf16 v[8:11], v[160:163], a[220:223], v[8:11]\n"
    "v_cvt_pk_bf16_f32 v74, v52, v53\n"
    "v_cvt_f32_i32 v211, v211\n"
    "v_mfma_f32_16x16x32_bf16 v[8:11], v[156:159], a[216:219], v[8:11]\n"
    "v_exp_f32 v54, v54\n"
    "v_mfma_f32_16x16x32_bf16 v[12:15], v[156:159], a[240:243], v[100:103]\n"
    "buffer_load_dwordx4 v1, s[16:19], s83 offen offset:2048 sc1 lds\n"
    "v_mfma_f32_16x16x32_bf16 v[12:15], v[160:163], a[244:247], v[12:15]\n"
    "v_exp_f32 v50, v50\n"
    "v_mfma_f32_16x16x32_bf16 v[12:15], v[164:167], a[248:251], v[12:15]\n"
    "v_exp_f32 v55, v55\n"
    "v_mfma_f32_16x16x32_bf16 v[12:15], v[168:171], a[252:255], v[12:15]\n"
    "v_cvt_pk_bf16_f32 v75, v54, v55\n"
    "v_cvt_f32_i32 v212, v212\n"
    "v_mfma_f32_16x16x32_bf16 v[12:15], v[172:175], v[124:127], v[12:15]\n"
    "buffer_load_dwordx4 v1, s[16:19], s83 offen offset:3072 sc1 lds\n"
    "v_mfma_f32_16x16x32_bf16 v[12:15], v[176:179], v[128:131], v[12:15]\n"
    "s_add_u32 m0, s70, 4096\n"
    "v_exp_f32 v56, v56\n"
    "v_mfma_f32_16x16x32_bf16 v[16:19], v[176:179], v[152:155], v[100:103]\n"
    "s_add_u32 s83, s67, 28672\n"
    "v_exp_f32 v51, v51\n"
    "v_mfma_f32_16x16x32_bf16 v[16:19], v[172:175], v[148:151], v[16:19]\n"
    "v_cvt_pk_bf16_f32 v97, v50, v51\n"
    "v_cvt_f32_i32 v213, v213\n"
    "v_mfma_f32_16x16x32_bf16 v[16:19], v[168:171], v[144:147], v[16:19]\n"
    "buffer_load_dwordx4 v1, s[16:19], s83 offen offset:0 sc1 lds\n"
    "v_mfma_f32_16x16x32_bf16 v[16:19], v[164:167], v[140:143], v[16:19]\n"
    "v_exp_f32 v57, v57\n"
    "v_mfma_f32_16x16x32_bf16 v[16:19], v[160:163], v[136:139], v[16:19]\n"
    "v_cvt_pk_bf16_f32 v82, v56, v57\n"
    "v_cvt_f32_i32 v214, v214\n"
    "v_mfma_f32_16x16x32_bf16 v[16:19], v[156:159], v[132:135], v[16:19]\n"
    "s_waitcnt lgkmcnt(0)\n"
    "v_exp_f32 v58, v58\n"
    "v_mfma_f32_16x16x32_bf16 v[20:23], v[180:183], a[192:195], v[104:107]\n"
    "buffer_load_dwordx4 v1, s[16:19], s83 offen offset:1024 sc1 lds\n"
    "v_mfma_f32_16x16x32_bf16 v[20:23], v[184:187], a[196:199], v[20:23]\n"
    "s_add_u32 s67, s67, 49152\n"
    "ds_read_b128 v[156:159], v249 offset:0\n"
    "ds_read_b128 v[160:163], v249 offset:12288\n"
    "v_mfma_f32_16x16x32_bf16 v[20:23], v[188:191], a[200:203], v[20:23]\n"
    "s_add_u32 s59, s59, 4\n"
    "ds_read_b128 v[164:167], v249 offset:1024\n"
    "v_cvt_f32_i32 v215, v215\n"
    "v_mfma_f32_16x16x32_bf16 v[20:23], v[192:195], a[204:207], v[20:23]\n"
    "s_add_u32 s81, s81, 128\n"
    "ds_read_b128 v[168:171], v249 offset:13312\n"
    "ds_read_b128 v[36:39], v243 offset:0\n"
    "v_mfma_f32_16x16x32_bf16 v[20:23], v[196:199], a[208:211], v[20:23]\n"
    "s_min_u32 s81, s81, 4096\n"
    "ds_read_b128 v[172:175], v249 offset:2048\n"
    "v_add_u32 v243, 512, v243\n"
    "v_mfma_f32_16x16x32_bf16 v[20:23], v[200:203], a[212:215], v[20:23]\n"
    "ds_read_b128 v[176:179], v249 offset:14336\n"
    "v_cvt_f32_i32 v216, v216\n"
    "v_mfma_f32_16x16x32_bf16 v[24:27], v[200:203], a[236:239], v[104:107]\n"
    "v_exp_f32 v59, v59\n"
    "v_mfma_f32_16x16x32_bf16 v[24:27], v[196:199], a[232:235], v[24:27]\n"
    "v_cvt_pk_bf16_f32 v83, v58, v59\n"
    "v_cvt_f32_i32 v217, v217\n"
    "v_mfma_f32_16x16x32_bf16 v[24:27], v[192:195], a[228:231], v[24:27]\n"
    "v_exp_f32 v60, v60\n"
    "v_mfma_f32_16x16x32_bf16 v[24:27], v[188:191], a[224:227], v[24:27]\n"
    "v_exp_f32 v61, v61\n"
    "v_mfma_f32_16x16x32_bf16 v[24:27], v[184:187], a[220:223], v[24:27]\n"
    "v_exp_f32 v62, v62\n"
    "v_mfma_f32_16x16x32_bf16 v[24:27], v[180:183], a[216:219], v[24:27]\n"
    "v_cvt_pk_bf16_f32 v90, v60, v61\n"
    "v_cvt_f32_i32 v218, v218\n"
    "v_mfma_f32_16x16x32_bf16 v[28:31], v[180:183], a[240:243], v[104:107]\n"
    "v_cvt_f32_i32 v219, v219\n"
    "v_cvt_f32_i32 v220, v220\n"
    "v_mfma_f32_16x16x32_bf16 v[28:31], v[184:187], a[244:247], v[28:31]\n"
    "v_exp_f32 v63, v63\n"
    "v_mfma_f32_16x16x32_bf16 v[28:31], v[188:191], a[248:251], v[28:31]\n"
    "v_cvt_pk_bf16_f32 v91, v62, v63\n"
    "v_cvt_f32_i32 v221, v221\n"
    "v_mfma_f32_16x16x32_bf16 v[28:31], v[192:195], a[252:255], v[28:31]\n"
    "v_cvt_f32_i32 v222, v222\n"
    "v_cvt_f32_i32 v223, v223\n"
    "v_mfma_f32_16x16x32_bf16 v[28:31], v[196:199], v[124:127], v[28:31]\n"
    "v_exp_f32 v64, v64\n"
    "v_mfma_f32_16x16x32_bf16 v[28:31], v[200:203], v[128:131], v[28:31]\n"
    "v_cvt_f32_i32 v224, v224\n"
    "v_cvt_f32_i32 v225, v225\n"
    "v_mfma_f32_16x16x32_bf16 v[32:35], v[200:203], v[152:155], v[104:107]\n"
    "v_cvt_f32_i32 v226, v226\n"
    "v_cvt_f32_i32 v227, v227\n"
    "v_mfma_f32_16x16x32_bf16 v[32:35], v[196:199], v[148:151], v[32:35]\n"
    "v_exp_f32 v65, v65\n"
    "v_mfma_f32_16x16x32_bf16 v[32:35], v[192:195], v[144:147], v[32:35]\n"
    "s_waitcnt lgkmcnt(2)\n"
    "v_cvt_pk_bf16_f32 v98, v64, v65\n"
    "v_fma_f32 v204, v204, v36, v38\n"
    "v_mfma_f32_16x16x32_bf16 v[32:35], v[188:191], v[140:143], v[32:35]\n"
    "v_fma_f32 v205, v205, v37, v39\n"
    "v_fma_f32 v206, v206, v36, v38\n"
    "v_mfma_f32_16x16x32_bf16 v[32:35], v[184:187], v[136:139], v[32:35]\n"
    "v_exp_f32 v66, v66\n"
    "v_mfma_f32_16x16x32_bf16 v[32:35], v[180:183], v[132:135], v[32:35]\n"
    "s_waitcnt lgkmcnt(0)\n"
    "v_fma_f32 v207, v207, v37, v39\n"
    "v_cvt_pk_bf16_f32 v204, v204, v205\n"
    "v_mfma_f32_16x16x32_bf16 a[0:3], v[68:71], v[156:159], a[0:3]\n"
    "ds_read_b128 v[180:183], v249 offset:3072\n"
    "v_cvt_pk_bf16_f32 v206, v206, v207\n"
    "v_mfma_f32_16x16x32_bf16 a[0:3], v[72:75], v[160:163], a[0:3]\n"
    "ds_read_b128 v[184:187], v249 offset:15360\n"
    "v_cvt_f32_i32 v228, v228\n"
    "v_mfma_f32_16x16x32_bf16 a[16:19], v[72:75], v[168:171], a[16:19]\n"
    "ds_read_b128 v[188:191], v249 offset:4096\n"
    "v_cvt_f32_i32 v229, v229\n"
    "v_mfma_f32_16x16x32_bf16 a[16:19], v[68:71], v[164:167], a[16:19]\n"
    "ds_read_b128 v[192:195], v249 offset:16384\n"
    "v_cvt_f32_i32 v230, v230\n"
    "v_mfma_f32_16x16x32_bf16 a[32:35], v[68:71], v[172:175], a[32:35]\n"
    "ds_read_b128 v[196:199], v249 offset:5120\n"
    "v_cvt_f32_i32 v231, v231\n"
    "v_mfma_f32_16x16x32_bf16 a[32:35], v[72:75], v[176:179], a[32:35]\n"
    "ds_read_b128 v[200:203], v249 offset:17408\n"
    "v_fma_f32 v208, v208, v36, v38\n"
    "v_mfma_f32_16x16x32_bf16 a[36:39], v[80:83], v[176:179], a[36:39]\n"
    "ds_read_b128 v[104:107], v251 offset:768\n"
    "v_fma_f32 v209, v209, v37, v39\n"
    "v_mfma_f32_16x16x32_bf16 a[36:39], v[76:79], v[172:175], a[36:39]\n"
    "v_exp_f32 v67, v67\n"
    "v_mfma_f32_16x16x32_bf16 a[20:23], v[76:79], v[164:167], a[20:23]\n"
    "v_cvt_pk_bf16_f32 v99, v66, v67\n"
    "v_fma_f32 v210, v210, v36, v38\n"
    "v_mfma_f32_16x16x32_bf16 a[20:23], v[80:83], v[168:171], a[20:23]\n"
    "buffer_store_dword v204, v244, s[16:19], s76 offen offset:0 sc1\n"
    "v_mfma_f32_16x16x32_bf16 a[4:7], v[80:83], v[160:163], a[4:7]\n"
    "v_fma_f32 v211, v211, v37, v39\n"
    "v_cvt_pk_bf16_f32 v208, v208, v209\n"
    "v_mfma_f32_16x16x32_bf16 a[4:7], v[76:79], v[156:159], a[4:7]\n"
    "buffer_store_dword v206, v244, s[16:19], s76 offen offset:128 sc1\n"
    "v_mfma_f32_16x16x32_bf16 a[8:11], v[84:87], v[156:159], a[8:11]\n"
    "v_cvt_pk_bf16_f32 v210, v210, v211\n"
    "v_cvt_f32_i32 v232, v232\n"
    "v_mfma_f32_16x16x32_bf16 a[8:11], v[88:91], v[160:163], a[8:11]\n"
    "buffer_store_dword v208, v244, s[16:19], s76 offen offset:1024 sc1\n"
    "v_mfma_f32_16x16x32_bf16 a[24:27], v[88:91], v[168:171], a[24:27]\n"
    "v_exp_f32 v4, v4\n"
    "v_mfma_f32_16x16x32_bf16 a[24:27], v[84:87], v[164:167], a[24:27]\n"
    "buffer_store_dword v210, v244, s[16:19], s76 offen offset:1152 sc1\n"
    "v_mfma_f32_16x16x32_bf16 a[40:43], v[84:87], v[172:175], a[40:43]\n"
    "v_cvt_f32_i32 v233, v233\n"
    "v_cvt_f32_i32 v234, v234\n"
    "v_mfma_f32_16x16x32_bf16 a[40:43], v[88:91], v[176:179], a[40:43]\n"
    "v_exp_f32 v5, v5\n"
    "v_mfma_f32_16x16x32_bf16 a[44:47], v[96:99], v[176:179], a[44:47]\n"
    "v_cvt_f32_i32 v235, v235\n"
    "v_fma_f32 v212, v212, v36, v38\n"
    "v_mfma_f32_16x16x32_bf16 a[44:47], v[92:95], v[172:175], a[44:47]\n"
    "v_exp_f32 v6, v6\n"
    "v_mfma_f32_16x16x32_bf16 a[28:31], v[92:95], v[164:167], a[28:31]\n"
    "v_fma_f32 v213, v213, v37, v39\n"
    "v_fma_f32 v214, v214, v36, v38\n"
    "v_mfma_f32_16x16x32_bf16 a[28:31], v[96:99], v[168:171], a[28:31]\n"
    "v_exp_f32 v7, v7\n"
    "v_mfma_f32_16x16x32_bf16 a[12:15], v[96:99], v[160:163], a[12:15]\n"
    "v_exp_f32 v8, v8\n"
    "v_mfma_f32_16x16x32_bf16 a[12:15], v[92:95], v[156:159], a[12:15]\n"
    "s_waitcnt lgkmcnt(0)\n"
    "v_fma_f32 v215, v215, v37, v39\n"
    "v_cvt_pk_bf16_f32 v212, v212, v213\n"
    "v_mfma_f32_16x16x32_bf16 a[48:51], v[68:71], v[180:183], a[48:51]\n"
    "buffer_store_dword v212, v244, s[16:19], s76 offen offset:256 sc1\n"
    "v_mfma_f32_16x16x32_bf16 a[48:51], v[72:75], v[184:187], a[48:51]\n"
    "ds_read_b128 v[156:159], v249 offset:6144\n"
    "ds_read_b128 v[160:163], v249 offset:18432\n"
    "v_mfma_f32_16x16x32_bf16 a[64:67], v[72:75], v[192:195], a[64:67]\n"
    "ds_read_b128 v[164:167], v249 offset:7168\n"
    "v_cvt_pk_bf16_f32 v214, v214, v215\n"
    "v_mfma_f32_16x16x32_bf16 a[64:67], v[68:71], v[188:191], a[64:67]\n"
    "buffer_store_dword v214, v244, s[16:19], s76 offen offset:384 sc1\n"
    "v_mfma_f32_16x16x32_bf16 a[80:83], v[68:71], v[196:199], a[80:83]\n"
    "ds_read_b128 v[168:171], v249 offset:19456\n"
    "ds_read_b128 v[172:175], v249 offset:8192\n"
    "v_mfma_f32_16x16x32_bf16 a[80:83], v[72:75], v[200:203], a[80:83]\n"
    "ds_read_b128 v[176:179], v249 offset:20480\n"
    "v_fma_f32 v216, v216, v36, v38\n"
    "v_smfmac_f32_16x16x64_bf16 v[108:111], v[104:107], v[68:75], v253\n"
    "v_smfmac_f32_16x16x64_bf16 v[112:115], v[104:107], v[76:83], v253\n"
    "v_mfma_f32_16x16x32_bf16 a[84:87], v[80:83], v[200:203], a[84:87]\n"
    "v_exp_f32 v9, v9\n"
    "v_mfma_f32_16x16x32_bf16 a[84:87], v[76:79], v[196:199], a[84:87]\n"
    "v_exp_f32 v10, v10\n"
    "v_mfma_f32_16x16x32_bf16 a[68:71], v[76:79], v[188:191], a[68:71]\n"
    "v_fma_f32 v217, v217, v37, v39\n"
    "v_fma_f32 v218, v218, v36, v38\n"
    "v_mfma_f32_16x16x32_bf16 a[68:71], v[80:83], v[192:195], a[68:71]\n"
    "v_exp_f32 v11, v11\n"
    "v_mfma_f32_16x16x32_bf16 a[52:55], v[80:83], v[184:187], a[52:55]\n"
    "v_fma_f32 v219, v219, v37, v39\n"
    "v_cvt_pk_bf16_f32 v216, v216, v217\n"
    "v_mfma_f32_16x16x32_bf16 a[52:55], v[76:79], v[180:183], a[52:55]\n"
    "v_exp_f32 v12, v12\n"
    "v_mfma_f32_16x16x32_bf16 a[56:59], v[84:87], v[180:183], a[56:59]\n"
    "v_cvt_pk_bf16_f32 v218, v218, v219\n"
    "v_fma_f32 v220, v220, v36, v38\n"
    "v_mfma_f32_16x16x32_bf16 a[56:59], v[88:91], v[184:187], a[56:59]\n"
    "v_exp_f32 v13, v13\n"
    "v_mfma_f32_16x16x32_bf16 a[72:75], v[88:91], v[192:195], a[72:75]\n"
    "v_exp_f32 v14, v14\n"
    "v_mfma_f32_16x16x32_bf16 a[72:75], v[84:87], v[188:191], a[72:75]\n"
    "v_fma_f32 v221, v221, v37, v39\n"
    "v_fma_f32 v222, v222, v36, v38\n"
    "v_mfma_f32_16x16x32_bf16 a[88:91], v[84:87], v[196:199], a[88:91]\n"
    "v_exp_f32 v15, v15\n"
    "v_mfma_f32_16x16x32_bf16 a[88:91], v[88:91], v[200:203], a[88:91]\n"
    "v_fma_f32 v223, v223, v37, v39\n"
    "v_cvt_pk_bf16_f32 v220, v220, v221\n"
    "v_smfmac_f32_16x16x64_bf16 v[116:119], v[104:107], v[84:91], v253\n"
    "v_smfmac_f32_16x16x64_bf16 v[120:123], v[104:107], v[92:99], v253\n"
    "v_mfma_f32_16x16x32_bf16 a[92:95], v[96:99], v[200:203], a[92:95]\n"
    "v_exp_f32 v16, v16\n"
    "v_mfma_f32_16x16x32_bf16 a[92:95], v[92:95], v[196:199], a[92:95]\n"
    "v_cvt_pk_bf16_f32 v222, v222, v223\n"
    "v_fma_f32 v224, v224, v36, v38\n"
    "v_mfma_f32_16x16x32_bf16 a[76:79], v[92:95], v[188:191], a[76:79]\n"
    "v_exp_f32 v17, v17\n"
    "v_mfma_f32_16x16x32_bf16 a[76:79], v[96:99], v[192:195], a[76:79]\n"
    "v_fma_f32 v225, v225, v37, v39\n"
    "v_fma_f32 v226, v226, v36, v38\n"
    "v_mfma_f32_16x16x32_bf16 a[60:63], v[96:99], v[184:187], a[60:63]\n"
    "v_exp_f32 v18, v18\n"
    "v_mfma_f32_16x16x32_bf16 a[60:63], v[92:95], v[180:183], a[60:63]\n"
    "s_waitcnt lgkmcnt(0)\n"
    "v_exp_f32 v19, v19\n"
    "v_mfma_f32_16x16x32_bf16 a[96:99], v[68:71], v[156:159], a[96:99]\n"
    "ds_read_b128 v[180:183], v249 offset:9216\n"
    "v_fma_f32 v227, v227, v37, v39\n"
    "v_mfma_f32_16x16x32_bf16 a[96:99], v[72:75], v[160:163], a[96:99]\n"
    "ds_read_b128 v[184:187], v249 offset:21504\n"
    "v_cvt_pk_bf16_f32 v224, v224, v225\n"
    "v_mfma_f32_16x16x32_bf16 a[112:115], v[72:75], v[168:171], a[112:115]\n"
    "ds_read_b128 v[188:191], v249 offset:10240\n"
    "v_cvt_pk_bf16_f32 v226, v226, v227\n"
    "v_mfma_f32_16x16x32_bf16 a[112:115], v[68:71], v[164:167], a[112:115]\n"
    "ds_read_b128 v[192:195], v249 offset:22528\n"
    "v_fma_f32 v228, v228, v36, v38\n"
    "v_mfma_f32_16x16x32_bf16 a[128:131], v[68:71], v[172:175], a[128:131]\n"
    "buffer_load_dwordx2 v[236:237], v241, s[36:39], s81 offen offset:0 sc1\n"
    "v_mfma_f32_16x16x32_bf16 a[128:131], v[72:75], v[176:179], a[128:131]\n"
    "ds_read_b128 v[196:199], v249 offset:11264\n"
    "ds_read_b128 v[200:203], v249 offset:23552\n"
    "v_mfma_f32_16x16x32_bf16 a[132:135], v[80:83], v[176:179], a[132:135]\n"
    "buffer_store_dword v216, v244, s[16:19], s76 offen offset:1280 sc1\n"
    "v_mfma_f32_16x16x32_bf16 a[132:135], v[76:79], v[172:175], a[132:135]\n"
    "v_exp_f32 v20, v20\n"
    "v_mfma_f32_16x16x32_bf16 a[116:119], v[76:79], v[164:167], a[116:119]\n"
    "buffer_store_dword v218, v244, s[16:19], s76 offen offset:1408 sc1\n"
    "v_mfma_f32_16x16x32_bf16 a[116:119], v[80:83], v[168:171], a[116:119]\n"
    "v_exp_f32 v21, v21\n"
    "v_mfma_f32_16x16x32_bf16 a[100:103], v[80:83], v[160:163], a[100:103]\n"
    "buffer_store_dword v220, v244, s[16:19], s77 offen offset:0 sc1\n"
    "v_mfma_f32_16x16x32_bf16 a[100:103], v[76:79], v[156:159], a[100:103]\n"
    "v_fma_f32 v229, v229, v37, v39\n"
    "v_fma_f32 v230, v230, v36, v38\n"
    "v_mfma_f32_16x16x32_bf16 a[104:107], v[84:87], v[156:159], a[104:107]\n"
    "buffer_store_dword v222, v244, s[16:19], s77 offen offset:128 sc1\n"
    "v_mfma_f32_16x16x32_bf16 a[104:107], v[88:91], v[160:163], a[104:107]\n"
    "v_exp_f32 v22, v22\n"
    "v_mfma_f32_16x16x32_bf16 a[120:123], v[88:91], v[168:171], a[120:123]\n"
    "buffer_store_dword v224, v244, s[16:19], s77 offen offset:1024 sc1\n"
    "v_mfma_f32_16x16x32_bf16 a[120:123], v[84:87], v[164:167], a[120:123]\n"
    "v_fma_f32 v231, v231, v37, v39\n"
    "v_cvt_pk_bf16_f32 v228, v228, v229\n"
    "v_mfma_f32_16x16x32_bf16 a[136:139], v[84:87], v[172:175], a[136:139]\n"
    "buffer_store_dword v226, v244, s[16:19], s77 offen offset:1152 sc1\n"
    "v_mfma_f32_16x16x32_bf16 a[136:139], v[88:91], v[176:179], a[136:139]\n"
    "v_exp_f32 v23, v23\n"
    "v_mfma_f32_16x16x32_bf16 a[140:143], v[96:99], v[176:179], a[140:143]\n"
    "buffer_store_dword v228, v244, s[16:19], s77 offen offset:256 sc1\n"
    "v_mfma_f32_16x16x32_bf16 a[140:143], v[92:95], v[172:175], a[140:143]\n"
    "v_exp_f32 v24, v24\n"
    "v_mfma_f32_16x16x32_bf16 a[124:127], v[92:95], v[164:167], a[124:127]\n"
    "v_cvt_pk_bf16_f32 v230, v230, v231\n"
    "v_fma_f32 v232, v232, v36, v38\n"
    "v_mfma_f32_16x16x32_bf16 a[124:127], v[96:99], v[168:171], a[124:127]\n"
    "buffer_store_dword v230, v244, s[16:19], s77 offen offset:384 sc1\n"
    "v_mfma_f32_16x16x32_bf16 a[108:111], v[96:99], v[160:163], a[108:111]\n"
    "v_exp_f32 v25, v25\n"
    "v_mfma_f32_16x16x32_bf16 a[108:111], v[92:95], v[156:159], a[108:111]\n"
    "s_waitcnt lgkmcnt(0)\n"
    "v_fma_f32 v233, v233, v37, v39\n"
    "v_fma_f32 v234, v234, v36, v38\n"
    "v_mfma_f32_16x16x32_bf16 a[144:147], v[68:71], v[180:183], a[144:147]\n"
    "ds_read_b128 v[156:159], v3 offset:12288\n"
    "v_fma_f32 v235, v235, v37, v39\n"
    "v_mfma_f32_16x16x32_bf16 a[144:147], v[72:75], v[184:187], a[144:147]\n"
    "ds_read_b128 v[160:163], v3 offset:14336\n"
    "v_cvt_pk_bf16_f32 v232, v232, v233\n"
    "v_mfma_f32_16x16x32_bf16 a[160:163], v[72:75], v[192:195], a[160:163]\n"
    "ds_read_b128 v[164:167], v3 offset:16384\n"
    "v_cvt_pk_bf16_f32 v234, v234, v235\n"
    "v_mfma_f32_16x16x32_bf16 a[160:163], v[68:71], v[188:191], a[160:163]\n"
    "ds_read_b128 v[168:171], v3 offset:18432\n"
    "v_mfma_f32_16x16x32_bf16 a[176:179], v[68:71], v[196:199], a[176:179]\n"
    "ds_read_b128 v[172:175], v3 offset:20480\n"
    "v_mfma_f32_16x16x32_bf16 a[176:179], v[72:75], v[200:203], a[176:179]\n"
    "ds_read_b128 v[176:179], v3 offset:22528\n"
    "v_mfma_f32_16x16x32_bf16 a[180:183], v[80:83], v[200:203], a[180:183]\n"
    "ds_read_b128 v[100:103], v250 offset:1664\n"
    "v_mfma_f32_16x16x32_bf16 a[180:183], v[76:79], v[196:199], a[180:183]\n"
    "buffer_store_dword v232, v244, s[16:19], s77 offen offset:1280 sc1\n"
    "v_mfma_f32_16x16x32_bf16 a[164:167], v[76:79], v[188:191], a[164:167]\n"
    "v_exp_f32 v26, v26\n"
    "v_mfma_f32_16x16x32_bf16 a[164:167], v[80:83], v[192:195], a[164:167]\n"
    "buffer_store_dword v234, v244, s[16:19], s77 offen offset:1408 sc1\n"
    "v_mfma_f32_16x16x32_bf16 a[148:151], v[80:83], v[184:187], a[148:151]\n"
    "v_exp_f32 v27, v27\n"
    "v_mfma_f32_16x16x32_bf16 a[148:151], v[76:79], v[180:183], a[148:151]\n"
    "v_exp_f32 v28, v28\n"
    "v_mfma_f32_16x16x32_bf16 a[152:155], v[84:87], v[180:183], a[152:155]\n"
    "v_exp_f32 v29, v29\n"
    "v_mfma_f32_16x16x32_bf16 a[152:155], v[88:91], v[184:187], a[152:155]\n"
    "v_exp_f32 v30, v30\n"
    "v_mfma_f32_16x16x32_bf16 a[168:171], v[88:91], v[192:195], a[168:171]\n"
    "s_waitcnt vmcnt(17)\n"
    "ds_add_u32 v247, v246\n"
    "v_mfma_f32_16x16x32_bf16 a[168:171], v[84:87], v[188:191], a[168:171]\n"
    "v_exp_f32 v31, v31\n"
    "v_mfma_f32_16x16x32_bf16 a[184:187], v[84:87], v[196:199], a[184:187]\n"
    "v_exp_f32 v32, v32\n"
    "v_mfma_f32_16x16x32_bf16 a[184:187], v[88:91], v[200:203], a[184:187]\n"
    "v_exp_f32 v33, v33\n"
    "v_mfma_f32_16x16x32_bf16 a[188:191], v[96:99], v[200:203], a[188:191]\n"
    "v_exp_f32 v34, v34\n"
    "v_mfma_f32_16x16x32_bf16 a[188:191], v[92:95], v[196:199], a[188:191]\n"
    "v_exp_f32 v35, v35\n"
    "v_mfma_f32_16x16x32_bf16 a[172:175], v[92:95], v[188:191], a[172:175]\n"
    "s_sub_u32 s74, s68, s65\n"
    "v_add_u32 v250, 1536, v250\n"
    "v_add_u32 v251, 1536, v251\n"
    "v_mfma_f32_16x16x32_bf16 a[172:175], v[96:99], v[192:195], a[172:175]\n"
    "s_add_u32 s66, s66, 1\n"
    "v_mov_b32 v2, v3\n"
    "v_add_u32 v3, s74, v252\n"
    "v_mfma_f32_16x16x32_bf16 a[156:159], v[96:99], v[184:187], a[156:159]\n"
    "s_waitcnt lgkmcnt(1)\n"
    "s_cmp_lt_u32 s66, 31\n"
    "v_mfma_f32_16x16x32_bf16 a[156:159], v[92:95], v[180:183], a[156:159]\n"
    "s_cbranch_scc1 .LPK\n"
    "s_branch .Ltail\n"
    ".LPV:\n"
    "v_mfma_f32_16x16x32_bf16 v[36:39], v[156:159], a[192:195], v[100:103]\n"
    "s_add_u32 s71, s71, 98304\n"
    "ds_read_b128 v[180:183], v2 offset:13312\n"
    "v_cvt_pk_bf16_f32 v68, v4, v5\n"
    "v_mfma_f32_16x16x32_bf16 v[36:39], v[160:163], a[196:199], v[36:39]\n"
    "s_add_u32 s75, s71, s63\n"
    "ds_read_b128 v[184:187], v2 offset:15360\n"
    "v_mfma_f32_16x16x32_bf16 v[36:39], v[164:167], a[200:203], v[36:39]\n"
    "ds_read_b128 v[188:191], v2 offset:17408\n"
    "v_mfma_f32_16x16x32_bf16 v[36:39], v[168:171], a[204:207], v[36:39]\n"
    "ds_read_b128 v[192:195], v2 offset:19456\n"
    "v_cvt_pk_bf16_f32 v69, v6, v7\n"
    "v_mfma_f32_16x16x32_bf16 v[36:39], v[172:175], a[208:211], v[36:39]\n"
    "ds_read_b128 v[196:199], v2 offset:21504\n"
    "v_mfma_f32_16x16x32_bf16 v[36:39], v[176:179], a[212:215], v[36:39]\n"
    "ds_read_b128 v[200:203], v2 offset:23552\n"
    "v_mfma_f32_16x16x32_bf16 v[40:43], v[176:179], a[236:239], v[100:103]\n"
    "ds_read_b128 v[104:107], v250 offset:144\n"
    "v_mfma_f32_16x16x32_bf16 v[40:43], v[172:175], a[232:235], v[40:43]\n"
    "s_add_u32 s68, s68, 24576\n"
    "v_cvt_pk_bf16_f32 v76, v8, v9\n"
    "v_mfma_f32_16x16x32_bf16 v[40:43], v[168:171], a[228:231], v[40:43]\n"
    "s_cmp_ge_u32 s68, s69\n"
    "s_cselect_b32 s68, s65, s68\n"
    "v_mfma_f32_16x16x32_bf16 v[40:43], v[164:167], a[224:227], v[40:43]\n"
    "v_exp_f32 v36, v36\n"
    "v_mfma_f32_16x16x32_bf16 v[40:43], v[160:163], a[220:223], v[40:43]\n"
    "v_exp_f32 v37, v37\n"
    "v_mfma_f32_16x16x32_bf16 v[40:43], v[156:159], a[216:219], v[40:43]\n"
    "v_cvt_pk_bf16_f32 v77, v10, v11\n"
    "v_cvt_pk_bf16_f32 v72, v36, v37\n"
    "v_mfma_f32_16x16x32_bf16 v[44:47], v[156:159], a[240:243], v[100:103]\n"
    "v_mfma_f32_16x16x32_bf16 v[44:47], v[160:163], a[244:247], v[44:47]\n"
    "v_exp_f32 v38, v38\n"
    "v_mfma_f32_16x16x32_bf16 v[44:47], v[164:167], a[248:251], v[44:47]\n"
    "v_mfma_f32_16x16x32_bf16 v[44:47], v[168:171], a[252:255], v[44:47]\n"
    "v_cvt_pk_bf16_f32 v84, v12, v13\n"
    "v_mfma_f32_16x16x32_bf16 v[44:47], v[172:175], v[124:127], v[44:47]\n"
    "v_exp_f32 v39, v39\n"
    "v_mfma_f32_16x16x32_bf16 v[44:47], v[176:179], v[128:131], v[44:47]\n"
    "v_cvt_pk_bf16_f32 v73, v38, v39\n"
    "v_mfma_f32_16x16x32_bf16 v[48:51], v[176:179], v[152:155], v[100:103]\n"
    "ds_read_b32 v245, v247\n"
    "v_cvt_pk_bf16_f32 v85, v14, v15\n"
    "v_mfma_f32_16x16x32_bf16 v[48:51], v[172:175], v[148:151], v[48:51]\n"
    "s_add_u32 m0, s68, 0\n"
    "v_exp_f32 v40, v40\n"
    "v_mfma_f32_16x16x32_bf16 v[48:51], v[168:171], v[144:147], v[48:51]\n"
    "v_mfma_f32_16x16x32_bf16 v[48:51], v[164:167], v[140:143], v[48:51]\n"
    "v_mfma_f32_16x16x32_bf16 v[48:51], v[160:163], v[136:139], v[48:51]\n"
    "s_waitcnt vmcnt(12)\n"
    "v_cmp_eq_u64 s[54:55], v[236:237], v[238:239]\n"
    "v_cvt_pk_bf16_f32 v92, v16, v17\n"
    "v_mfma_f32_16x16x32_bf16 v[48:51], v[156:159], v[132:135], v[48:51]\n"
    "s_waitcnt lgkmcnt(1)\n"
    "v_exp_f32 v41, v41\n"
    "v_mfma_f32_16x16x32_bf16 v[52:55], v[180:183], a[192:195], v[104:107]\n"
    "s_waitcnt lgkmcnt(0)\n"
    "v_cmp_le_u32 s[56:57], s59, v245\n"
    "v_cvt_pk_bf16_f32 v80, v40, v41\n"
    "v_mfma_f32_16x16x32_bf16 v[52:55], v[184:187], a[196:199], v[52:55]\n"
    "v_exp_f32 v42, v42\n"
    "v_mfma_f32_16x16x32_bf16 v[52:55], v[188:191], a[200:203], v[52:55]\n"
    "s_and_b64 s[54:55], s[54:55], s[56:57]\n"
    "s_cmp_eq_u64 s[54:55], -1\n"
    "v_mfma_f32_16x16x32_bf16 v[52:55], v[192:195], a[204:207], v[52:55]\n"
    "s_cbranch_scc0 .Lpoll3\n"
    ".Lpoll3r:\n"
    "v_mfma_f32_16x16x32_bf16 v[52:55], v[196:199], a[208:211], v[52:55]\n"
    "buffer_load_dwordx4 v1, s[16:19], s67 offen offset:0 sc1 lds\n"
    "v_mfma_f32_16x16x32_bf16 v[52:55], v[200:203], a[212:215], v[52:55]\n"
    "ds_read_b128 v[156:159], v3 offset:0\n"
    "ds_read_b128 v[160:163], v3 offset:2048\n"
    "v_mfma_f32_16x16x32_bf16 v[56:59], v[200:203], a[236:239], v[104:107]\n"
    "ds_read_b128 v[164:167], v3 offset:4096\n"
    "ds_read_b128 v[168:171], v3 offset:6144\n"
    "v_mfma_f32_16x16x32_bf16 v[56:59], v[196:199], a[232:235], v[56:59]\n"
    "ds_read_b128 v[172:175], v3 offset:8192\n"
    "ds_read_b128 v[176:179], v3 offset:10240\n"
    "v_mfma_f32_16x16x32_bf16 v[56:59], v[192:195], a[228:231], v[56:59]\n"
    "buffer_load_dwordx4 v1, s[16:19], s67 offen offset:1024 sc1 lds\n"
    "v_mfma_f32_16x16x32_bf16 v[56:59], v[188:191], a[224:227], v[56:59]\n"
    "ds_read_b128 v[100:103], v250 offset:768\n"
    "v_cvt_pk_bf16_f32 v93, v18, v19\n"
    "v_mfma_f32_16x16x32_bf16 v[56:59], v[184:187], a[220:223], v[56:59]\n"
    "v_cvt_pk_bf16_f32 v70, v20, v21\n"
    "v_cvt_pk_bf16_f32 v71, v22, v23\n"
    "v_mfma_f32_16x16x32_bf16 v[56:59], v[180:183], a[216:219], v[56:59]\n"
    "v_exp_f32 v43, v43\n"
    "v_mfma_f32_16x16x32_bf16 v[60:63], v[180:183], a[240:243], v[104:107]\n"
    "buffer_load_dwordx4 v1, s[16:19], s67 offen offset:2048 sc1 lds\n"
    "v_mfma_f32_16x16x32_bf16 v[60:63], v[184:187], a[244:247], v[60:63]\n"
    "v_cvt_pk_bf16_f32 v78, v24, v25\n"
    "v_cvt_pk_bf16_f32 v81, v42, v43\n"
    "v_mfma_f32_16x16x32_bf16 v[60:63], v[188:191], a[248:251], v[60:63]\n"
    "v_exp_f32 v44, v44\n"
    "v_mfma_f32_16x16x32_bf16 v[60:63], v[192:195], a[252:255], v[60:63]\n"
    "v_exp_f32 v45, v45\n"
    "v_mfma_f32_16x16x32_bf16 v[60:63], v[196:199], v[124:127], v[60:63]\n"
    "buffer_load_dwordx4 v1, s[16:19], s67 offen offset:3072 sc1 lds\n"
    "v_mfma_f32_16x16x32_bf16 v[60:63], v[200:203], v[128:131], v[60:63]\n"
    "s_add_u32 m0, s68, 4096\n"
    "v_cvt_pk_bf16_f32 v79, v26, v27\n"
    "v_cvt_pk_bf16_f32 v88, v44, v45\n"
    "v_mfma_f32_16x16x32_bf16 v[64:67], v[200:203], v[152:155], v[104:107]\n"
    "s_add_u32 s83, s67, 4096\n"
    "v_exp_f32 v46, v46\n"
    "v_mfma_f32_16x16x32_bf16 v[64:67], v[196:199], v[148:151], v[64:67]\n"
    "v_exp_f32 v47, v47\n"
    "v_mfma_f32_16x16x32_bf16 v[64:67], v[192:195], v[144:147], v[64:67]\n"
    "buffer_load_dwordx4 v1, s[16:19], s83 offen offset:0 sc1 lds\n"
    "v_mfma_f32_16x16x32_bf16 v[64:67], v[188:191], v[140:143], v[64:67]\n"
    "v_cvt_pk_bf16_f32 v86, v28, v29\n"
    "v_cvt_pk_bf16_f32 v89, v46, v47\n"
    "v_mfma_f32_16x16x32_bf16 v[64:67], v[184:187], v[136:139], v[64:67]\n"
    "v_exp_f32 v48, v48\n"
    "v_mfma_f32_16x16x32_bf16 v[64:67], v[180:183], v[132:135], v[64:67]\n"
    "s_waitcnt lgkmcnt(0)\n"
    "v_exp_f32 v49, v49\n"
    "v_mfma_f32_16x16x32_bf16 v[4:7], v[156:159], a[192:195], v[100:103]\n"
    "buffer_load_dwordx4 v1, s[16:19], s83 offen offset:1024 sc1 lds\n"
    "v_mfma_f32_16x16x32_bf16 v[4:7], v[160:163], a[196:199], v[4:7]\n"
    "s_add_u32 m0, s64, 0\n"
    "ds_read_b128 v[180:183], v3 offset:1024\n"
    "ds_read_b128 v[184:187], v3 offset:3072\n"
    "v_mfma_f32_16x16x32_bf16 v[4:7], v[164:167], a[200:203], v[4:7]\n"
    "s_add_u32 s83, s67, 24576\n"
    "ds_read_b128 v[188:191], v3 offset:5120\n"
    "v_cvt_pk_bf16_f32 v87, v30, v31\n"
    "v_mfma_f32_16x16x32_bf16 v[4:7], v[168:171], a[204:207], v[4:7]\n"
    "ds_read_b128 v[192:195], v3 offset:7168\n"
    "v_cvt_pk_bf16_f32 v96, v48, v49\n"
    "v_mfma_f32_16x16x32_bf16 v[4:7], v[172:175], a[208:211], v[4:7]\n"
    "buffer_load_dwordx4 v1, s[16:19], s83 offen offset:0 sc1 lds\n"
    "v_mfma_f32_16x16x32_bf16 v[4:7], v[176:179], a[212:215], v[4:7]\n"
    "ds_read_b128 v[196:199], v3 offset:9216\n"
    "ds_read_b128 v[200:203], v3 offset:11264\n"
    "v_mfma_f32_16x16x32_bf16 v[8:11], v[176:179], a[236:239], v[100:103]\n"
    "ds_read_b128 v[104:107], v250 offset:784\n"
    "v_cvt_pk_bf16_f32 v94, v32, v33\n"
    "v_mfma_f32_16x16x32_bf16 v[8:11], v[172:175], a[232:235], v[8:11]\n"
    "v_exp_f32 v52, v52\n"
    "v_mfma_f32_16x16x32_bf16 v[8:11], v[168:171], a[228:231], v[8:11]\n"
    "buffer_load_dwordx4 v1, s[16:19], s83 offen offset:1024 sc1 lds\n"
    "v_mfma_f32_16x16x32_bf16 v[8:11], v[164:167], a[224:227], v[8:11]\n"
    "v_cvt_pk_bf16_f32 v95, v34, v35\n"
    "v_mfma_f32_16x16x32_bf16 v[8:11], v[160:163], a[220:223], v[8:11]\n"
    "v_exp_f32 v53, v53\n"
    "v_mfma_f32_16x16x32_bf16 v[8:11], v[156:159], a[216:219], v[8:11]\n"
    "v_exp_f32 v54, v54\n"
    "v_mfma_f32_16x16x32_bf16 v[12:15], v[156:159], a[240:243], v[100:103]\n"
    "buffer_load_dwordx4 v1, s[16:19], s83 offen offset:2048 sc1 lds\n"
    "v_mfma_f32_16x16x32_bf16 v[12:15], v[160:163], a[244:247], v[12:15]\n"
    "v_cvt_pk_bf16_f32 v74, v52, v53\n"
    "v_mfma_f32_16x16x32_bf16 v[12:15], v[164:167], a[248:251], v[12:15]\n"
    "v_exp_f32 v55, v55\n"
    "v_mfma_f32_16x16x32_bf16 v[12:15], v[168:171], a[252:255], v[12:15]\n"
    "v_cvt_pk_bf16_f32 v75, v54, v55\n"
    "v_mfma_f32_16x16x32_bf16 v[12:15], v[172:175], v[124:127], v[12:15]\n"
    "buffer_load_dwordx4 v1, s[16:19], s83 offen offset:3072 sc1 lds\n"
    "v_mfma_f32_16x16x32_bf16 v[12:15], v[176:179], v[128:131], v[12:15]\n"
    "s_add_u32 m0, s64, 4096\n"
    "v_exp_f32 v56, v56\n"
    "v_mfma_f32_16x16x32_bf16 v[16:19], v[176:179], v[152:155], v[100:103]\n"
    "s_add_u32 s83, s67, 28672\n"
    "v_exp_f32 v50, v50\n"
    "v_mfma_f32_16x16x32_bf16 v[16:19], v[172:175], v[148:151], v[16:19]\n"
    "v_exp_f32 v51, v51\n"
    "v_mfma_f32_16x16x32_bf16 v[16:19], v[168:171], v[144:147], v[16:19]\n"
    "buffer_load_dwordx4 v1, s[16:19], s83 offen offset:0 sc1 lds\n"
    "v_mfma_f32_16x16x32_bf16 v[16:19], v[164:167], v[140:143], v[16:19]\n"
    "v_exp_f32 v57, v57\n"
    "v_mfma_f32_16x16x32_bf16 v[16:19], v[160:163], v[136:139], v[16:19]\n"
    "v_cvt_pk_bf16_f32 v82, v56, v57\n"
    "v_cvt_pk_bf16_f32 v97, v50, v51\n"
    "v_mfma_f32_16x16x32_bf16 v[16:19], v[156:159], v[132:135], v[16:19]\n"
    "s_waitcnt lgkmcnt(0)\n"
    "v_exp_f32 v58, v58\n"
    "v_mfma_f32_16x16x32_bf16 v[20:23], v[180:183], a[192:195], v[104:107]\n"
    "buffer_load_dwordx4 v1, s[16:19], s83 offen offset:1024 sc1 lds\n"
    "v_mfma_f32_16x16x32_bf16 v[20:23], v[184:187], a[196:199], v[20:23]\n"
    "s_add_u32 s67, s67, 49152\n"
    "ds_read_b128 v[156:159], v248 offset:0\n"
    "ds_read_b128 v[160:163], v248 offset:12288\n"
    "v_mfma_f32_16x16x32_bf16 v[20:23], v[188:191], a[200:203], v[20:23]\n"
    "s_add_u32 s59, s59, 4\n"
    "ds_read_b128 v[164:167], v248 offset:1024\n"
    "v_mfma_f32_16x16x32_bf16 v[20:23], v[192:195], a[204:207], v[20:23]\n"
    "s_add_u32 s81, s81, 128\n"
    "s_waitcnt vmcnt(12)\n"
    "ds_read_b128 v[168:171], v248 offset:13312\n"
    "v_mfma_f32_16x16x32_bf16 v[20:23], v[196:199], a[208:211], v[20:23]\n"
    "s_min_u32 s81, s81, 4096\n"
    "ds_read_b128 v[172:175], v248 offset:2048\n"
    "v_mfma_f32_16x16x32_bf16 v[20:23], v[200:203], a[212:215], v[20:23]\n"
    "buffer_store_dwordx2 v[238:239], v240, s[36:39], s80 offen offset:0 sc1\n"
    "v_mfma_f32_16x16x32_bf16 v[24:27], v[200:203], a[236:239], v[104:107]\n"
    "s_add_u32 s80, s80, 256\n"
    "ds_read_b128 v[176:179], v248 offset:14336\n"
    "v_mfma_f32_16x16x32_bf16 v[24:27], v[196:199], a[232:235], v[24:27]\n"
    "v_exp_f32 v59, v59\n"
    "v_mfma_f32_16x16x32_bf16 v[24:27], v[192:195], a[228:231], v[24:27]\n"
    "buffer_load_dwordx4 v[204:207], v242, s[40:43], s75 offen offset:0 nt\n"
    "v_mfma_f32_16x16x32_bf16 v[24:27], v[188:191], a[224:227], v[24:27]\n"
    "v_cvt_pk_bf16_f32 v83, v58, v59\n"
    "v_mfma_f32_16x16x32_bf16 v[24:27], v[184:187], a[220:223], v[24:27]\n"
    "buffer_load_dwordx4 v[208:211], v242, s[40:43], s75 offen offset:768 nt\n"
    "v_mfma_f32_16x16x32_bf16 v[24:27], v[180:183], a[216:219], v[24:27]\n"
    "v_exp_f32 v60, v60\n"
    "v_mfma_f32_16x16x32_bf16 v[28:31], v[180:183], a[240:243], v[104:107]\n"
    "buffer_load_dwordx4 v[212:215], v242, s[40:43], s75 offen offset:256 nt\n"
    "v_mfma_f32_16x16x32_bf16 v[28:31], v[184:187], a[244:247], v[28:31]\n"
    "v_exp_f32 v61, v61\n"
    "v_mfma_f32_16x16x32_bf16 v[28:31], v[188:191], a[248:251], v[28:31]\n"
    "buffer_load_dwordx4 v[216:219], v242, s[40:43], s75 offen offset:1024 nt\n"
    "v_mfma_f32_16x16x32_bf16 v[28:31], v[192:195], a[252:255], v[28:31]\n"
    "v_exp_f32 v62, v62\n"
    "v_mfma_f32_16x16x32_bf16 v[28:31], v[196:199], v[124:127], v[28:31]\n"
    "buffer_load_dwordx4 v[220:223], v242, s[40:43], s75 offen offset:512 nt\n"
    "v_mfma_f32_16x16x32_bf16 v[28:31], v[200:203], v[128:131], v[28:31]\n"
    "v_exp_f32 v63, v63\n"
    "v_mfma_f32_16x16x32_bf16 v[32:35], v[200:203], v[152:155], v[104:107]\n"
    "v_cvt_pk_bf16_f32 v90, v60, v61\n"
    "v_cvt_pk_bf16_f32 v91, v62, v63\n"
    "v_mfma_f32_16x16x32_bf16 v[32:35], v[196:199], v[148:151], v[32:35]\n"
    "buffer_load_dwordx4 v[224:227], v242, s[40:43], s75 offen offset:1280 nt\n"
    "v_mfma_f32_16x16x32_bf16 v[32:35], v[192:195], v[144:147], v[32:35]\n"
    "v_exp_f32 v64, v64\n"
    "v_mfma_f32_16x16x32_bf16 v[32:35], v[188:191], v[140:143], v[32:35]\n"
    "v_exp_f32 v65, v65\n"
    "v_mfma_f32_16x16x32_bf16 v[32:35], v[184:187], v[136:139], v[32:35]\n"
    "v_exp_f32 v66, v66\n"
    "v_mfma_f32_16x16x32_bf16 v[32:35], v[180:183], v[132:135], v[32:35]\n"
    "s_waitcnt lgkmcnt(0)\n"
    "v_cvt_pk_bf16_f32 v98, v64, v65\n"
    "v_mfma_f32_16x16x32_bf16 a[0:3], v[68:71], v[156:159], a[0:3]\n"
    "ds_read_b128 v[180:183], v248 offset:3072\n"
    "v_mfma_f32_16x16x32_bf16 a[0:3], v[72:75], v[160:163], a[0:3]\n"
    "ds_read_b128 v[184:187], v248 offset:15360\n"
    "v_mfma_f32_16x16x32_bf16 a[16:19], v[72:75], v[168:171], a[16:19]\n"
    "ds_read_b128 v[188:191], v248 offset:4096\n"
    "v_mfma_f32_16x16x32_bf16 a[16:19], v[68:71], v[164:167], a[16:19]\n"
    "ds_read_b128 v[192:195], v248 offset:16384\n"
    "v_mfma_f32_16x16x32_bf16 a[32:35], v[68:71], v[172:175], a[32:35]\n"
    "ds_read_b128 v[196:199], v248 offset:5120\n"
    "v_mfma_f32_16x16x32_bf16 a[32:35], v[72:75], v[176:179], a[32:35]\n"
    "ds_read_b128 v[200:203], v248 offset:17408\n"
    "v_mfma_f32_16x16x32_bf16 a[36:39], v[80:83], v[176:179], a[36:39]\n"
    "ds_read_b128 v[104:107], v251 offset:0\n"
    "v_mfma_f32_16x16x32_bf16 a[36:39], v[76:79], v[172:175], a[36:39]\n"
    "v_exp_f32 v67, v67\n"
    "v_mfma_f32_16x16x32_bf16 a[20:23], v[76:79], v[164:167], a[20:23]\n"
    "v_cvt_pk_bf16_f32 v99, v66, v67\n"
    "v_mfma_f32_16x16x32_bf16 a[20:23], v[80:83], v[168:171], a[20:23]\n"
    "v_exp_f32 v4, v4\n"
    "v_mfma_f32_16x16x32_bf16 a[4:7], v[80:83], v[160:163], a[4:7]\n"
    "v_exp_f32 v5, v5\n"
    "v_mfma_f32_16x16x32_bf16 a[4:7], v[76:79], v[156:159], a[4:7]\n"
    "v_exp_f32 v6, v6\n"
    "v_mfma_f32_16x16x32_bf16 a[8:11], v[84:87], v[156:159], a[8:11]\n"
    "v_exp_f32 v7, v7\n"
    "v_mfma_f32_16x16x32_bf16 a[8:11], v[88:91], v[160:163], a[8:11]\n"
    "v_mfma_f32_16x16x32_bf16 a[24:27], v[88:91], v[168:171], a[24:27]\n"
    "v_exp_f32 v8, v8\n"
    "v_mfma_f32_16x16x32_bf16 a[24:27], v[84:87], v[164:167], a[24:27]\n"
    "v_mfma_f32_16x16x32_bf16 a[40:43], v[84:87], v[172:175], a[40:43]\n"
    "v_mfma_f32_16x16x32_bf16 a[40:43], v[88:91], v[176:179], a[40:43]\n"
    "v_exp_f32 v9, v9\n"
    "v_mfma_f32_16x16x32_bf16 a[44:47], v[96:99], v[176:179], a[44:47]\n"
    "v_mfma_f32_16x16x32_bf16 a[44:47], v[92:95], v[172:175], a[44:47]\n"
    "v_mfma_f32_16x16x32_bf16 a[28:31], v[92:95], v[164:167], a[28:31]\n"
    "v_exp_f32 v10, v10\n"
    "v_mfma_f32_16x16x32_bf16 a[28:31], v[96:99], v[168:171], a[28:31]\n"
    "v_mfma_f32_16x16x32_bf16 a[12:15], v[96:99], v[160:163], a[12:15]\n"
    "v_exp_f32 v11, v11\n"
    "v_mfma_f32_16x16x32_bf16 a[12:15], v[92:95], v[156:159], a[12:15]\n"
    "s_waitcnt lgkmcnt(0)\n"
    "v_mfma_f32_16x16x32_bf16 a[48:51], v[68:71], v[180:183], a[48:51]\n"
    "ds_read_b128 v[156:159], v248 offset:6144\n"
    "v_mfma_f32_16x16x32_bf16 a[48:51], v[72:75], v[184:187], a[48:51]\n"
    "ds_read_b128 v[160:163], v248 offset:18432\n"
    "v_mfma_f32_16x16x32_bf16 a[64:67], v[72:75], v[192:195], a[64:67]\n"
    "ds_read_b128 v[164:167], v248 offset:7168\n"
    "v_mfma_f32_16x16x32_bf16 a[64:67], v[68:71], v[188:191], a[64:67]\n"
    "ds_read_b128 v[168:171], v248 offset:19456\n"
    "v_mfma_f32_16x16x32_bf16 a[80:83], v[68:71], v[196:199], a[80:83]\n"
    "ds_read_b128 v[172:175], v248 offset:8192\n"
    "v_mfma_f32_16x16x32_bf16 a[80:83], v[72:75], v[200:203], a[80:83]\n"
    "ds_read_b128 v[176:179], v248 offset:20480\n"
    "v_smfmac_f32_16x16x64_bf16 v[108:111], v[104:107], v[68:75], v253\n"
    "v_smfmac_f32_16x16x64_bf16 v[112:115], v[104:107], v[76:83], v253\n"
    "v_mfma_f32_16x16x32_bf16 a[84:87], v[80:83], v[200:203], a[84:87]\n"
    "v_exp_f32 v12, v12\n"
    "v_mfma_f32_16x16x32_bf16 a[84:87], v[76:79], v[196:199], a[84:87]\n"
    "v_exp_f32 v13, v13\n"
    "v_mfma_f32_16x16x32_bf16 a[68:71], v[76:79], v[188:191], a[68:71]\n"
    "v_exp_f32 v14, v14\n"
    "v_mfma_f32_16x16x32_bf16 a[68:71], v[80:83], v[192:195], a[68:71]\n"
    "v_exp_f32 v15, v15\n"
    "v_mfma_f32_16x16x32_bf16 a[52:55], v[80:83], v[184:187], a[52:55]\n"
    "v_exp_f32 v16, v16\n"
    "v_mfma_f32_16x16x32_bf16 a[52:55], v[76:79], v[180:183], a[52:55]\n"
    "v_mfma_f32_16x16x32_bf16 a[56:59], v[84:87], v[180:183], a[56:59]\n"
    "v_exp_f32 v17, v17\n"
    "v_mfma_f32_16x16x32_bf16 a[56:59], v[88:91], v[184:187], a[56:59]\n"
    "v_mfma_f32_16x16x32_bf16 a[72:75], v[88:91], v[192:195], a[72:75]\n"
    "v_mfma_f32_16x16x32_bf16 a[72:75], v[84:87], v[188:191], a[72:75]\n"
    "v_exp_f32 v18, v18\n"
    "v_mfma_f32_16x16x32_bf16 a[88:91], v[84:87], v[196:199], a[88:91]\n"
    "v_mfma_f32_16x16x32_bf16 a[88:91], v[88:91], v[200:203], a[88:91]\n"
    "v_smfmac_f32_16x16x64_bf16 v[116:119], v[104:107], v[84:91], v253\n"
    "v_smfmac_f32_16x16x64_bf16 v[120:123], v[104:107], v[92:99], v253\n"
    "v_mfma_f32_16x16x32_bf16 a[92:95], v[96:99], v[200:203], a[92:95]\n"
    "v_exp_f32 v19, v19\n"
    "v_mfma_f32_16x16x32_bf16 a[92:95], v[92:95], v[196:199], a[92:95]\n"
    "v_exp_f32 v20, v20\n"
    "v_mfma_f32_16x16x32_bf16 a[76:79], v[92:95], v[188:191], a[76:79]\n"
    "v_mfma_f32_16x16x32_bf16 a[76:79], v[96:99], v[192:195], a[76:79]\n"
    "v_exp_f32 v21, v21\n"
    "v_mfma_f32_16x16x32_bf16 a[60:63], v[96:99], v[184:187], a[60:63]\n"
    "v_mfma_f32_16x16x32_bf16 a[60:63], v[92:95], v[180:183], a[60:63]\n"
    "s_waitcnt lgkmcnt(0)\n"
    "v_mfma_f32_16x16x32_bf16 a[96:99], v[68:71], v[156:159], a[96:99]\n"
    "ds_read_b128 v[180:183], v248 offset:9216\n"
    "v_mfma_f32_16x16x32_bf16 a[96:99], v[72:75], v[160:163], a[96:99]\n"
    "ds_read_b128 v[184:187], v248 offset:21504\n"
    "v_mfma_f32_16x16x32_bf16 a[112:115], v[72:75], v[168:171], a[112:115]\n"
    "ds_read_b128 v[188:191], v248 offset:10240\n"
    "v_mfma_f32_16x16x32_bf16 a[112:115], v[68:71], v[164:167], a[112:115]\n"
    "ds_read_b128 v[192:195], v248 offset:22528\n"
    "v_mfma_f32_16x16x32_bf16 a[128:131], v[68:71], v[172:175], a[128:131]\n"
    "buffer_load_dwordx2 v[236:237], v241, s[36:39], s81 offen offset:0 sc1\n"
    "v_mfma_f32_16x16x32_bf16 a[128:131], v[72:75], v[176:179], a[128:131]\n"
    "ds_read_b128 v[196:199], v248 offset:11264\n"
    "ds_read_b128 v[200:203], v248 offset:23552\n"
    "v_mfma_f32_16x16x32_bf16 a[132:135], v[80:83], v[176:179], a[132:135]\n"
    "v_exp_f32 v22, v22\n"
    "v_mfma_f32_16x16x32_bf16 a[132:135], v[76:79], v[172:175], a[132:135]\n"
    "v_exp_f32 v23, v23\n"
    "v_mfma_f32_16x16x32_bf16 a[116:119], v[76:79], v[164:167], a[116:119]\n"
    "v_exp_f32 v24, v24\n"
    "v_mfma_f32_16x16x32_bf16 a[116:119], v[80:83], v[168:171], a[116:119]\n"
    "v_exp_f32 v25, v25\n"
    "v_mfma_f32_16x16x32_bf16 a[100:103], v[80:83], v[160:163], a[100:103]\n"
    "v_mfma_f32_16x16x32_bf16 a[100:103], v[76:79], v[156:159], a[100:103]\n"
    "v_exp_f32 v26, v26\n"
    "v_mfma_f32_16x16x32_bf16 a[104:107], v[84:87], v[156:159], a[104:107]\n"
    "v_mfma_f32_16x16x32_bf16 a[104:107], v[88:91], v[160:163], a[104:107]\n"
    "v_exp_f32 v27, v27\n"
    "v_mfma_f32_16x16x32_bf16 a[120:123], v[88:91], v[168:171], a[120:123]\n"
    "v_mfma_f32_16x16x32_bf16 a[120:123], v[84:87], v[164:167], a[120:123]\n"
    "v_mfma_f32_16x16x32_bf16 a[136:139], v[84:87], v[172:175], a[136:139]\n"
    "v_exp_f32 v28, v28\n"
    "v_mfma_f32_16x16x32_bf16 a[136:139], v[88:91], v[176:179], a[136:139]\n"
    "v_mfma_f32_16x16x32_bf16 a[140:143], v[96:99], v[176:179], a[140:143]\n"
    "v_mfma_f32_16x16x32_bf16 a[140:143], v[92:95], v[172:175], a[140:143]\n"
    "v_exp_f32 v29, v29\n"
    "v_mfma_f32_16x16x32_bf16 a[124:127], v[92:95], v[164:167], a[124:127]\n"
    "v_mfma_f32_16x16x32_bf16 a[124:127], v[96:99], v[168:171], a[124:127]\n"
    "v_exp_f32 v30, v30\n"
    "v_mfma_f32_16x16x32_bf16 a[108:111], v[96:99], v[160:163], a[108:111]\n"
    "v_mfma_f32_16x16x32_bf16 a[108:111], v[92:95], v[156:159], a[108:111]\n"
    "s_waitcnt lgkmcnt(0)\n"
    "v_mfma_f32_16x16x32_bf16 a[144:147], v[68:71], v[180:183], a[144:147]\n"
    "ds_read_b128 v[156:159], v3 offset:12288\n"
    "v_mfma_f32_16x16x32_bf16 a[144:147], v[72:75], v[184:187], a[144:147]\n"
    "ds_read_b128 v[160:163], v3 offset:14336\n"
    "v_mfma_f32_16x16x32_bf16 a[160:163], v[72:75], v[192:195], a[160:163]\n"
    "ds_read_b128 v[164:167], v3 offset:16384\n"
    "v_mfma_f32_16x16x32_bf16 a[160:163], v[68:71], v[188:191], a[160:163]\n"
    "ds_read_b128 v[168:171], v3 offset:18432\n"
    "v_mfma_f32_16x16x32_bf16 a[176:179], v[68:71], v[196:199], a[176:179]\n"
    "ds_read_b128 v[172:175], v3 offset:20480\n"
    "v_mfma_f32_16x16x32_bf16 a[176:179], v[72:75], v[200:203], a[176:179]\n"
    "ds_read_b128 v[176:179], v3 offset:22528\n"
    "v_mfma_f32_16x16x32_bf16 a[180:183], v[80:83], v[200:203], a[180:183]\n"
    "ds_read_b128 v[100:103], v250 offset:896\n"
    "v_mfma_f32_16x16x32_bf16 a[180:183], v[76:79], v[196:199], a[180:183]\n"
    "v_exp_f32 v31, v31\n"
    "v_mfma_f32_16x16x32_bf16 a[164:167], v[76:79], v[188:191], a[164:167]\n"
    "v_exp_f32 v32, v32\n"
    "v_mfma_f32_16x16x32_bf16 a[164:167], v[80:83], v[192:195], a[164:167]\n"
    "v_exp_f32 v33, v33\n"
    "v_mfma_f32_16x16x32_bf16 a[148:151], v[80:83], v[184:187], a[148:151]\n"
    "v_exp_f32 v34, v34\n"
    "v_mfma_f32_16x16x32_bf16 a[148:151], v[76:79], v[180:183], a[148:151]\n"
    "v_exp_f32 v35, v35\n"
    "v_mfma_f32_16x16x32_bf16 a[152:155], v[84:87], v[180:183], a[152:155]\n"
    "v_mfma_f32_16x16x32_bf16 a[152:155], v[88:91], v[184:187], a[152:155]\n"
    "v_mfma_f32_16x16x32_bf16 a[168:171], v[88:91], v[192:195], a[168:171]\n"
    "s_waitcnt vmcnt(8)\n"
    "ds_add_u32 v247, v246\n"
    "v_mfma_f32_16x16x32_bf16 a[168:171], v[84:87], v[188:191], a[168:171]\n"
    "v_mfma_f32_16x16x32_bf16 a[184:187], v[84:87], v[196:199], a[184:187]\n"
    "v_mfma_f32_16x16x32_bf16 a[184:187], v[88:91], v[200:203], a[184:187]\n"
    "v_mfma_f32_16x16x32_bf16 a[188:191], v[96:99], v[200:203], a[188:191]\n"
    "v_mfma_f32_16x16x32_bf16 a[188:191], v[92:95], v[196:199], a[188:191]\n"
    "v_mfma_f32_16x16x32_bf16 a[172:175], v[92:95], v[188:191], a[172:175]\n"
    "s_sub_u32 s74, s68, s65\n"
    "v_mfma_f32_16x16x32_bf16 a[172:175], v[96:99], v[192:195], a[172:175]\n"
    "s_add_u32 s66, s66, 1\n"
    "v_mov_b32 v2, v3\n"
    "v_add_u32 v3, s74, v252\n"
    "v_mfma_f32_16x16x32_bf16 a[156:159], v[96:99], v[184:187], a[156:159]\n"
    "s_waitcnt lgkmcnt(1)\n"
    "s_cmp_eq_u32 s66, 31\n"
    "v_mfma_f32_16x16x32_bf16 a[156:159], v[92:95], v[180:183], a[156:159]\n"
    "s_cbranch_scc1 .LtailP\n"
    ".LNV:\n"
    "v_mfma_f32_16x16x32_bf16 v[36:39], v[156:159], a[192:195], v[100:103]\n"
    "ds_read_b128 v[180:183], v2 offset:13312\n"
    "v_cvt_pk_bf16_f32 v68, v4, v5\n"
    "v_mfma_f32_16x16x32_bf16 v[36:39], v[160:163], a[196:199], v[36:39]\n"
    "ds_read_b128 v[184:187], v2 offset:15360\n"
    "v_mfma_f32_16x16x32_bf16 v[36:39], v[164:167], a[200:203], v[36:39]\n"
    "ds_read_b128 v[188:191], v2 offset:17408\n"
    "v_mfma_f32_16x16x32_bf16 v[36:39], v[168:171], a[204:207], v[36:39]\n"
    "ds_read_b128 v[192:195], v2 offset:19456\n"
    "v_cvt_pk_bf16_f32 v69, v6, v7\n"
    "v_mfma_f32_16x16x32_bf16 v[36:39], v[172:175], a[208:211], v[36:39]\n"
    "ds_read_b128 v[196:199], v2 offset:21504\n"
    "v_mfma_f32_16x16x32_bf16 v[36:39], v[176:179], a[212:215], v[36:39]\n"
    "ds_read_b128 v[200:203], v2 offset:23552\n"
    "v_mfma_f32_16x16x32_bf16 v[40:43], v[176:179], a[236:239], v[100:103]\n"
    "ds_read_b128 v[104:107], v250 offset:912\n"
    "v_mfma_f32_16x16x32_bf16 v[40:43], v[172:175], a[232:235], v[40:43]\n"
    "s_add_u32 s68, s68, 24576\n"
    "v_cvt_pk_bf16_f32 v76, v8, v9\n"
    "v_mfma_f32_16x16x32_bf16 v[40:43], v[168:171], a[228:231], v[40:43]\n"
    "s_cmp_ge_u32 s68, s69\n"
    "s_cselect_b32 s68, s65, s68\n"
    "v_mfma_f32_16x16x32_bf16 v[40:43], v[164:167], a[224:227], v[40:43]\n"
    "v_exp_f32 v36, v36\n"
    "v_mfma_f32_16x16x32_bf16 v[40:43], v[160:163], a[220:223], v[40:43]\n"
    "v_exp_f32 v37, v37\n"
    "v_mfma_f32_16x16x32_bf16 v[40:43], v[156:159], a[216:219], v[40:43]\n"
    "v_cvt_pk_bf16_f32 v77, v10, v11\n"
    "v_cvt_pk_bf16_f32 v72, v36, v37\n"
    "v_mfma_f32_16x16x32_bf16 v[44:47], v[156:159], a[240:243], v[100:103]\n"
    "v_mfma_f32_16x16x32_bf16 v[44:47], v[160:163], a[244:247], v[44:47]\n"
    "v_exp_f32 v38, v38\n"
    "v_mfma_f32_16x16x32_bf16 v[44:47], v[164:167], a[248:251], v[44:47]\n"
    "v_mfma_f32_16x16x32_bf16 v[44:47], v[168:171], a[252:255], v[44:47]\n"
    "v_cvt_pk_bf16_f32 v84, v12, v13\n"
    "v_mfma_f32_16x16x32_bf16 v[44:47], v[172:175], v[124:127], v[44:47]\n"
    "v_exp_f32 v39, v39\n"
    "v_mfma_f32_16x16x32_bf16 v[44:47], v[176:179], v[128:131], v[44:47]\n"
    "v_cvt_pk_bf16_f32 v73, v38, v39\n"
    "v_mfma_f32_16x16x32_bf16 v[48:51], v[176:179], v[152:155], v[100:103]\n"
    "ds_read_b32 v245, v247\n"
    "v_cvt_pk_bf16_f32 v85, v14, v15\n"
    "v_mfma_f32_16x16x32_bf16 v[48:51], v[172:175], v[148:151], v[48:51]\n"
    "s_add_u32 m0, s68, 0\n"
    "v_exp_f32 v40, v40\n"
    "v_mfma_f32_16x16x32_bf16 v[48:51], v[168:171], v[144:147], v[48:51]\n"
    "v_mfma_f32_16x16x32_bf16 v[48:51], v[164:167], v[140:143], v[48:51]\n"
    "v_mfma_f32_16x16x32_bf16 v[48:51], v[160:163], v[136:139], v[48:51]\n"
    "s_waitcnt vmcnt(0)\n"
    "v_cmp_eq_u64 s[54:55], v[236:237], v[238:239]\n"
    "v_cvt_pk_bf16_f32 v92, v16, v17\n"
    "v_mfma_f32_16x16x32_bf16 v[48:51], v[156:159], v[132:135], v[48:51]\n"
    "s_waitcnt lgkmcnt(1)\n"
    "v_exp_f32 v41, v41\n"
    "v_mfma_f32_16x16x32_bf16 v[52:55], v[180:183], a[192:195], v[104:107]\n"
    "s_waitcnt lgkmcnt(0)\n"
    "s_waitcnt vmcnt(1)\n"
    "v_cmp_le_u32 s[56:57], s59, v245\n"
    "v_mfma_f32_16x16x32_bf16 v[52:55], v[184:187], a[196:199], v[52:55]\n"
    "s_add_u32 s76, s71, s60\n"
    "v_exp_f32 v42, v42\n"
    "v_mfma_f32_16x16x32_bf16 v[52:55], v[188:191], a[200:203], v[52:55]\n"
    "s_and_b64 s[54:55], s[54:55], s[56:57]\n"
    "s_cmp_eq_u64 s[54:55], -1\n"
    "v_mfma_f32_16x16x32_bf16 v[52:55], v[192:195], a[204:207], v[52:55]\n"
    "s_cbranch_scc0 .Lpoll4\n"
    ".Lpoll4r:\n"
    "v_mfma_f32_16x16x32_bf16 v[52:55], v[196:199], a[208:211], v[52:55]\n"
    "buffer_load_dwordx4 v1, s[16:19], s67 offen offset:0 sc1 lds\n"
    "v_mfma_f32_16x16x32_bf16 v[52:55], v[200:203], a[212:215], v[52:55]\n"
    "s_add_u32 s77, s71, s61\n"
    "ds_read_b128 v[156:159], v3 offset:0\n"
    "ds_read_b128 v[160:163], v3 offset:2048\n"
    "v_mfma_f32_16x16x32_bf16 v[56:59], v[200:203], a[236:239], v[104:107]\n"
    "s_add_u32 s78, s71, s62\n"
    "ds_read_b128 v[164:167], v3 offset:4096\n"
    "ds_read_b128 v[168:171], v3 offset:6144\n"
    "v_mfma_f32_16x16x32_bf16 v[56:59], v[196:199], a[232:235], v[56:59]\n"
    "ds_read_b128 v[172:175], v3 offset:8192\n"
    "ds_read_b128 v[176:179], v3 offset:10240\n"
    "v_mfma_f32_16x16x32_bf16 v[56:59], v[192:195], a[228:231], v[56:59]\n"
    "buffer_load_dwordx4 v1, s[16:19], s67 offen offset:1024 sc1 lds\n"
    "v_mfma_f32_16x16x32_bf16 v[56:59], v[188:191], a[224:227], v[56:59]\n"
    "ds_read_b128 v[100:103], v250 offset:1536\n"
    "v_cvt_pk_bf16_f32 v93, v18, v19\n"
    "v_mfma_f32_16x16x32_bf16 v[56:59], v[184:187], a[220:223], v[56:59]\n"
    "v_cvt_pk_bf16_f32 v70, v20, v21\n"
    "v_cvt_pk_bf16_f32 v71, v22, v23\n"
    "v_mfma_f32_16x16x32_bf16 v[56:59], v[180:183], a[216:219], v[56:59]\n"
    "v_exp_f32 v43, v43\n"
    "v_mfma_f32_16x16x32_bf16 v[60:63], v[180:183], a[240:243], v[104:107]\n"
    "buffer_load_dwordx4 v1, s[16:19], s67 offen offset:2048 sc1 lds\n"
    "v_mfma_f32_16x16x32_bf16 v[60:63], v[184:187], a[244:247], v[60:63]\n"
    "v_cvt_pk_bf16_f32 v78, v24, v25\n"
    "v_cvt_pk_bf16_f32 v80, v40, v41\n"
    "v_mfma_f32_16x16x32_bf16 v[60:63], v[188:191], a[248:251], v[60:63]\n"
    "v_cvt_pk_bf16_f32 v81, v42, v43\n"
    "v_cvt_f32_i32 v204, v204\n"
    "v_mfma_f32_16x16x32_bf16 v[60:63], v[192:195], a[252:255], v[60:63]\n"
    "v_exp_f32 v44, v44\n"
    "v_mfma_f32_16x16x32_bf16 v[60:63], v[196:199], v[124:127], v[60:63]\n"
    "buffer_load_dwordx4 v1, s[16:19], s67 offen offset:3072 sc1 lds\n"
    "v_mfma_f32_16x16x32_bf16 v[60:63], v[200:203], v[128:131], v[60:63]\n"
    "s_add_u32 m0, s68, 4096\n"
    "v_cvt_pk_bf16_f32 v79, v26, v27\n"
    "v_cvt_f32_i32 v208, v208\n"
    "v_mfma_f32_16x16x32_bf16 v[64:67], v[200:203], v[152:155], v[104:107]\n"
    "s_add_u32 s83, s67, 4096\n"
    "v_exp_f32 v45, v45\n"
    "v_mfma_f32_16x16x32_bf16 v[64:67], v[196:199], v[148:151], v[64:67]\n"
    "v_exp_f32 v46, v46\n"
    "v_mfma_f32_16x16x32_bf16 v[64:67], v[192:195], v[144:147], v[64:67]\n"
    "buffer_load_dwordx4 v1, s[16:19], s83 offen offset:0 sc1 lds\n"
    "v_mfma_f32_16x16x32_bf16 v[64:67], v[188:191], v[140:143], v[64:67]\n"
    "v_cvt_pk_bf16_f32 v86, v28, v29\n"
    "v_cvt_pk_bf16_f32 v88, v44, v45\n"
    "v_mfma_f32_16x16x32_bf16 v[64:67], v[184:187], v[136:139], v[64:67]\n"
    "v_exp_f32 v47, v47\n"
    "v_mfma_f32_16x16x32_bf16 v[64:67], v[180:183], v[132:135], v[64:67]\n"
    "s_waitcnt lgkmcnt(0)\n"
    "v_cvt_pk_bf16_f32 v89, v46, v47\n"
    "v_cvt_f32_i32 v205, v205\n"
    "v_mfma_f32_16x16x32_bf16 v[4:7], v[156:159], a[192:195], v[100:103]\n"
    "buffer_load_dwordx4 v1, s[16:19], s83 offen offset:1024 sc1 lds\n"
    "v_mfma_f32_16x16x32_bf16 v[4:7], v[160:163], a[196:199], v[4:7]\n"
    "s_add_u32 m0, s70, 0\n"
    "ds_read_b128 v[180:183], v3 offset:1024\n"
    "ds_read_b128 v[184:187], v3 offset:3072\n"
    "v_mfma_f32_16x16x32_bf16 v[4:7], v[164:167], a[200:203], v[4:7]\n"
    "s_add_u32 s83, s67, 24576\n"
    "ds_read_b128 v[188:191], v3 offset:5120\n"
    "v_cvt_pk_bf16_f32 v87, v30, v31\n"
    "v_mfma_f32_16x16x32_bf16 v[4:7], v[168:171], a[204:207], v[4:7]\n"
    "ds_read_b128 v[192:195], v3 offset:7168\n"
    "v_cvt_f32_i32 v209, v209\n"
    "v_mfma_f32_16x16x32_bf16 v[4:7], v[172:175], a[208:211], v[4:7]\n"
    "buffer_load_dwordx4 v1, s[16:19], s83 offen offset:0 sc1 lds\n"
    "v_mfma_f32_16x16x32_bf16 v[4:7], v[176:179], a[212:215], v[4:7]\n"
    "ds_read_b128 v[196:199], v3 offset:9216\n"
    "ds_read_b128 v[200:203], v3 offset:11264\n"
    "v_mfma_f32_16x16x32_bf16 v[8:11], v[176:179], a[236:239], v[100:103]\n"
    "ds_read_b128 v[104:107], v250 offset:1552\n"
    "v_cvt_pk_bf16_f32 v94, v32, v33\n"
    "v_mfma_f32_16x16x32_bf16 v[8:11], v[172:175], a[232:235], v[8:11]\n"
    "v_exp_f32 v52, v52\n"
    "v_mfma_f32_16x16x32_bf16 v[8:11], v[168:171], a[228:231], v[8:11]\n"
    "buffer_load_dwordx4 v1, s[16:19], s83 offen offset:1024 sc1 lds\n"
    "v_mfma_f32_16x16x32_bf16 v[8:11], v[164:167], a[224:227], v[8:11]\n"
    "v_cvt_pk_bf16_f32 v95, v34, v35\n"
    "v_cvt_f32_i32 v206, v206\n"
    "v_mfma_f32_16x16x32_bf16 v[8:11], v[160:163], a[220:223], v[8:11]\n"
    "v_exp_f32 v53, v53\n"
    "v_mfma_f32_16x16x32_bf16 v[8:11], v[156:159], a[216:219], v[8:11]\n"
    "v_exp_f32 v54, v54\n"
    "v_mfma_f32_16x16x32_bf16 v[12:15], v[156:159], a[240:243], v[100:103]\n"
    "buffer_load_dwordx4 v1, s[16:19], s83 offen offset:2048 sc1 lds\n"
    "v_mfma_f32_16x16x32_bf16 v[12:15], v[160:163], a[244:247], v[12:15]\n"
    "v_cvt_pk_bf16_f32 v74, v52, v53\n"
    "v_cvt_f32_i32 v210, v210\n"
    "v_mfma_f32_16x16x32_bf16 v[12:15], v[164:167], a[248:251], v[12:15]\n"
    "v_exp_f32 v55, v55\n"
    "v_mfma_f32_16x16x32_bf16 v[12:15], v[168:171], a[252:255], v[12:15]\n"
    "v_cvt_pk_bf16_f32 v75, v54, v55\n"
    "v_cvt_f32_i32 v207, v207\n"
    "v_mfma_f32_16x16x32_bf16 v[12:15], v[172:175], v[124:127], v[12:15]\n"
    "buffer_load_dwordx4 v1, s[16:19], s83 offen offset:3072 sc1 lds\n"
    "v_mfma_f32_16x16x32_bf16 v[12:15], v[176:179], v[128:131], v[12:15]\n"
    "s_add_u32 m0, s70, 4096\n"
    "v_exp_f32 v56, v56\n"
    "v_mfma_f32_16x16x32_bf16 v[16:19], v[176:179], v[152:155], v[100:103]\n"
    "s_add_u32 s83, s67, 28672\n"
    "v_exp_f32 v48, v48\n"
    "v_mfma_f32_16x16x32_bf16 v[16:19], v[172:175], v[148:151], v[16:19]\n"
    "v_exp_f32 v49, v49\n"
    "v_mfma_f32_16x16x32_bf16 v[16:19], v[168:171], v[144:147], v[16:19]\n"
    "buffer_load_dwordx4 v1, s[16:19], s83 offen offset:0 sc1 lds\n"
    "v_mfma_f32_16x16x32_bf16 v[16:19], v[164:167], v[140:143], v[16:19]\n"
    "v_exp_f32 v57, v57\n"
    "v_mfma_f32_16x16x32_bf16 v[16:19], v[160:163], v[136:139], v[16:19]\n"
    "v_cvt_pk_bf16_f32 v82, v56, v57\n"
    "v_cvt_pk_bf16_f32 v96, v48, v49\n"
    "v_mfma_f32_16x16x32_bf16 v[16:19], v[156:159], v[132:135], v[16:19]\n"
    "s_waitcnt lgkmcnt(0)\n"
    "v_exp_f32 v58, v58\n"
    "v_mfma_f32_16x16x32_bf16 v[20:23], v[180:183], a[192:195], v[104:107]\n"
    "buffer_load_dwordx4 v1, s[16:19], s83 offen offset:1024 sc1 lds\n"
    "v_mfma_f32_16x16x32_bf16 v[20:23], v[184:187], a[196:199], v[20:23]\n"
    "s_add_u32 s67, s67, 49152\n"
    "ds_read_b128 v[156:159], v249 offset:0\n"
    "ds_read_b128 v[160:163], v249 offset:12288\n"
    "v_mfma_f32_16x16x32_bf16 v[20:23], v[188:191], a[200:203], v[20:23]\n"
    "s_add_u32 s59, s59, 4\n"
    "ds_read_b128 v[164:167], v249 offset:1024\n"
    "v_cvt_f32_i32 v211, v211\n"
    "v_mfma_f32_16x16x32_bf16 v[20:23], v[192:195], a[204:207], v[20:23]\n"
    "s_add_u32 s81, s81, 128\n"
    "ds_read_b128 v[168:171], v249 offset:13312\n"
    "v_cvt_pk_bf16_f32 v204, v204, v208\n"
    "v_mfma_f32_16x16x32_bf16 v[20:23], v[196:199], a[208:211], v[20:23]\n"
    "s_min_u32 s81, s81, 4096\n"
    "ds_read_b128 v[172:175], v249 offset:2048\n"
    "v_cvt_pk_bf16_f32 v205, v205, v209\n"
    "v_mfma_f32_16x16x32_bf16 v[20:23], v[200:203], a[212:215], v[20:23]\n"
    "ds_read_b128 v[176:179], v249 offset:14336\n"
    "v_cvt_pk_bf16_f32 v206, v206, v210\n"
    "v_mfma_f32_16x16x32_bf16 v[24:27], v[200:203], a[236:239], v[104:107]\n"
    "v_exp_f32 v59, v59\n"
    "v_mfma_f32_16x16x32_bf16 v[24:27], v[196:199], a[232:235], v[24:27]\n"
    "v_cvt_pk_bf16_f32 v83, v58, v59\n"
    "v_cvt_pk_bf16_f32 v207, v207, v211\n"
    "v_mfma_f32_16x16x32_bf16 v[24:27], v[192:195], a[228:231], v[24:27]\n"
    "v_exp_f32 v60, v60\n"
    "v_mfma_f32_16x16x32_bf16 v[24:27], v[188:191], a[224:227], v[24:27]\n"
    "v_exp_f32 v61, v61\n"
    "v_mfma_f32_16x16x32_bf16 v[24:27], v[184:187], a[220:223], v[24:27]\n"
    "v_exp_f32 v62, v62\n"
    "v_mfma_f32_16x16x32_bf16 v[24:27], v[180:183], a[216:219], v[24:27]\n"
    "v_cvt_pk_bf16_f32 v90, v60, v61\n"
    "v_cvt_f32_i32 v212, v212\n"
    "v_mfma_f32_16x16x32_bf16 v[28:31], v[180:183], a[240:243], v[104:107]\n"
    "v_exp_f32 v50, v50\n"
    "v_mfma_f32_16x16x32_bf16 v[28:31], v[184:187], a[244:247], v[28:31]\n"
    "v_exp_f32 v63, v63\n"
    "v_mfma_f32_16x16x32_bf16 v[28:31], v[188:191], a[248:251], v[28:31]\n"
    "v_cvt_pk_bf16_f32 v91, v62, v63\n"
    "v_cvt_f32_i32 v216, v216\n"
    "v_mfma_f32_16x16x32_bf16 v[28:31], v[192:195], a[252:255], v[28:31]\n"
    "v_exp_f32 v51, v51\n"
    "v_mfma_f32_16x16x32_bf16 v[28:31], v[196:199], v[124:127], v[28:31]\n"
    "v_exp_f32 v64, v64\n"
    "v_mfma_f32_16x16x32_bf16 v[28:31], v[200:203], v[128:131], v[28:31]\n"
    "v_cvt_pk_bf16_f32 v97, v50, v51\n"
    "v_cvt_f32_i32 v213, v213\n"
    "v_mfma_f32_16x16x32_bf16 v[32:35], v[200:203], v[152:155], v[104:107]\n"
    "v_cvt_f32_i32 v217, v217\n"
    "v_cvt_f32_i32 v214, v214\n"
    "v_mfma_f32_16x16x32_bf16 v[32:35], v[196:199], v[148:151], v[32:35]\n"
    "v_exp_f32 v65, v65\n"
    "v_mfma_f32_16x16x32_bf16 v[32:35], v[192:195], v[144:147], v[32:35]\n"
    "v_cvt_pk_bf16_f32 v98, v64, v65\n"
    "v_cvt_f32_i32 v218, v218\n"
    "v_mfma_f32_16x16x32_bf16 v[32:35], v[188:191], v[140:143], v[32:35]\n"
    "v_cvt_f32_i32 v215, v215\n"
    "v_cvt_f32_i32 v219, v219\n"
    "v_mfma_f32_16x16x32_bf16 v[32:35], v[184:187], v[136:139], v[32:35]\n"
    "v_exp_f32 v66, v66\n"
    "v_mfma_f32_16x16x32_bf16 v[32:35], v[180:183], v[132:135], v[32:35]\n"
    "s_waitcnt lgkmcnt(0)\n"
    "v_mfma_f32_16x16x32_bf16 a[0:3], v[68:71], v[156:159], a[0:3]\n"
    "ds_read_b128 v[180:183], v249 offset:3072\n"
    "v_mfma_f32_16x16x32_bf16 a[0:3], v[72:75], v[160:163], a[0:3]\n"
    "ds_read_b128 v[184:187], v249 offset:15360\n"
    "v_cvt_pk_bf16_f32 v212, v212, v216\n"
    "v_mfma_f32_16x16x32_bf16 a[16:19], v[72:75], v[168:171], a[16:19]\n"
    "ds_read_b128 v[188:191], v249 offset:4096\n"
    "v_mfma_f32_16x16x32_bf16 a[16:19], v[68:71], v[164:167], a[16:19]\n"
    "ds_read_b128 v[192:195], v249 offset:16384\n"
    "v_mfma_f32_16x16x32_bf16 a[32:35], v[68:71], v[172:175], a[32:35]\n"
    "ds_read_b128 v[196:199], v249 offset:5120\n"
    "v_cvt_pk_bf16_f32 v213, v213, v217\n"
    "v_mfma_f32_16x16x32_bf16 a[32:35], v[72:75], v[176:179], a[32:35]\n"
    "ds_read_b128 v[200:203], v249 offset:17408\n"
    "v_mfma_f32_16x16x32_bf16 a[36:39], v[80:83], v[176:179], a[36:39]\n"
    "ds_read_b128 v[104:107], v251 offset:768\n"
    "v_mfma_f32_16x16x32_bf16 a[36:39], v[76:79], v[172:175], a[36:39]\n"
    "v_exp_f32 v67, v67\n"
    "v_mfma_f32_16x16x32_bf16 a[20:23], v[76:79], v[164:167], a[20:23]\n"
    "v_cvt_pk_bf16_f32 v99, v66, v67\n"
    "v_cvt_pk_bf16_f32 v214, v214, v218\n"
    "v_mfma_f32_16x16x32_bf16 a[20:23], v[80:83], v[168:171], a[20:23]\n"
    "v_exp_f32 v4, v4\n"
    "v_mfma_f32_16x16x32_bf16 a[4:7], v[80:83], v[160:163], a[4:7]\n"
    "v_exp_f32 v5, v5\n"
    "v_mfma_f32_16x16x32_bf16 a[4:7], v[76:79], v[156:159], a[4:7]\n"
    "v_exp_f32 v6, v6\n"
    "v_mfma_f32_16x16x32_bf16 a[8:11], v[84:87], v[156:159], a[8:11]\n"
    "v_exp_f32 v7, v7\n"
    "v_mfma_f32_16x16x32_bf16 a[8:11], v[88:91], v[160:163], a[8:11]\n"
    "v_cvt_pk_bf16_f32 v215, v215, v219\n"
    "v_cvt_f32_i32 v220, v220\n"
    "v_mfma_f32_16x16x32_bf16 a[24:27], v[88:91], v[168:171], a[24:27]\n"
    "v_exp_f32 v8, v8\n"
    "v_mfma_f32_16x16x32_bf16 a[24:27], v[84:87], v[164:167], a[24:27]\n"
    "v_mfma_f32_16x16x32_bf16 a[40:43], v[84:87], v[172:175], a[40:43]\n"
    "v_mfma_f32_16x16x32_bf16 a[40:43], v[88:91], v[176:179], a[40:43]\n"
    "v_exp_f32 v9, v9\n"
    "v_mfma_f32_16x16x32_bf16 a[44:47], v[96:99], v[176:179], a[44:47]\n"
    "v_cvt_f32_i32 v224, v224\n"
    "v_mfma_f32_16x16x32_bf16 a[44:47], v[92:95], v[172:175], a[44:47]\n"
    "v_mfma_f32_16x16x32_bf16 a[28:31], v[92:95], v[164:167], a[28:31]\n"
    "v_exp_f32 v10, v10\n"
    "v_mfma_f32_16x16x32_bf16 a[28:31], v[96:99], v[168:171], a[28:31]\n"
    "v_cvt_f32_i32 v221, v221\n"
    "v_mfma_f32_16x16x32_bf16 a[12:15], v[96:99], v[160:163], a[12:15]\n"
    "v_exp_f32 v11, v11\n"
    "v_mfma_f32_16x16x32_bf16 a[12:15], v[92:95], v[156:159], a[12:15]\n"
    "s_waitcnt lgkmcnt(0)\n"
    "v_cvt_f32_i32 v225, v225\n"
    "v_mfma_f32_16x16x32_bf16 a[48:51], v[68:71], v[180:183], a[48:51]\n"
    "ds_read_b128 v[156:159], v249 offset:6144\n"
    "v_mfma_f32_16x16x32_bf16 a[48:51], v[72:75], v[184:187], a[48:51]\n"
    "ds_read_b128 v[160:163], v249 offset:18432\n"
    "v_mfma_f32_16x16x32_bf16 a[64:67], v[72:75], v[192:195], a[64:67]\n"
    "ds_read_b128 v[164:167], v249 offset:7168\n"
    "v_cvt_f32_i32 v222, v222\n"
    "v_mfma_f32_16x16x32_bf16 a[64:67], v[68:71], v[188:191], a[64:67]\n"
    "ds_read_b128 v[168:171], v249 offset:19456\n"
    "v_mfma_f32_16x16x32_bf16 a[80:83], v[68:71], v[196:199], a[80:83]\n"
    "ds_read_b128 v[172:175], v249 offset:8192\n"
    "v_mfma_f32_16x16x32_bf16 a[80:83], v[72:75], v[200:203], a[80:83]\n"
    "ds_read_b128 v[176:179], v249 offset:20480\n"
    "v_cvt_f32_i32 v226, v226\n"
    "v_smfmac_f32_16x16x64_bf16 v[108:111], v[104:107], v[68:75], v253\n"
    "v_smfmac_f32_16x16x64_bf16 v[112:115], v[104:107], v[76:83], v253\n"
    "v_mfma_f32_16x16x32_bf16 a[84:87], v[80:83], v[200:203], a[84:87]\n"
    "v_exp_f32 v12, v12\n"
    "v_mfma_f32_16x16x32_bf16 a[84:87], v[76:79], v[196:199], a[84:87]\n"
    "v_exp_f32 v13, v13\n"
    "v_mfma_f32_16x16x32_bf16 a[68:71], v[76:79], v[188:191], a[68:71]\n"
    "v_exp_f32 v14, v14\n"
    "v_mfma_f32_16x16x32_bf16 a[68:71], v[80:83], v[192:195], a[68:71]\n"
    "v_exp_f32 v15, v15\n"
    "v_mfma_f32_16x16x32_bf16 a[52:55], v[80:83], v[184:187], a[52:55]\n"
    "v_cvt_f32_i32 v223, v223\n"
    "v_cvt_f32_i32 v227, v227\n"
    "v_mfma_f32_16x16x32_bf16 a[52:55], v[76:79], v[180:183], a[52:55]\n"
    "v_exp_f32 v16, v16\n"
    "v_mfma_f32_16x16x32_bf16 a[56:59], v[84:87], v[180:183], a[56:59]\n"
    "v_exp_f32 v17, v17\n"
    "v_mfma_f32_16x16x32_bf16 a[56:59], v[88:91], v[184:187], a[56:59]\n"
    "v_cvt_pk_bf16_f32 v220, v220, v224\n"
    "v_mfma_f32_16x16x32_bf16 a[72:75], v[88:91], v[192:195], a[72:75]\n"
    "v_mfma_f32_16x16x32_bf16 a[72:75], v[84:87], v[188:191], a[72:75]\n"
    "v_exp_f32 v18, v18\n"
    "v_mfma_f32_16x16x32_bf16 a[88:91], v[84:87], v[196:199], a[88:91]\n"
    "v_cvt_pk_bf16_f32 v221, v221, v225\n"
    "v_mfma_f32_16x16x32_bf16 a[88:91], v[88:91], v[200:203], a[88:91]\n"
    "v_smfmac_f32_16x16x64_bf16 v[116:119], v[104:107], v[84:91], v253\n"
    "v_smfmac_f32_16x16x64_bf16 v[120:123], v[104:107], v[92:99], v253\n"
    "v_mfma_f32_16x16x32_bf16 a[92:95], v[96:99], v[200:203], a[92:95]\n"
    "v_exp_f32 v19, v19\n"
    "v_mfma_f32_16x16x32_bf16 a[92:95], v[92:95], v[196:199], a[92:95]\n"
    "v_exp_f32 v20, v20\n"
    "v_mfma_f32_16x16x32_bf16 a[76:79], v[92:95], v[188:191], a[76:79]\n"
    "v_cvt_pk_bf16_f32 v222, v222, v226\n"
    "v_cvt_pk_bf16_f32 v223, v223, v227\n"
    "v_mfma_f32_16x16x32_bf16 a[76:79], v[96:99], v[192:195], a[76:79]\n"
    "v_exp_f32 v21, v21\n"
    "v_mfma_f32_16x16x32_bf16 a[60:63], v[96:99], v[184:187], a[60:63]\n"
    "v_mfma_f32_16x16x32_bf16 a[60:63], v[92:95], v[180:183], a[60:63]\n"
    "s_waitcnt lgkmcnt(0)\n"
    "v_mfma_f32_16x16x32_bf16 a[96:99], v[68:71], v[156:159], a[96:99]\n"
    "ds_read_b128 v[180:183], v249 offset:9216\n"
    "v_mfma_f32_16x16x32_bf16 a[96:99], v[72:75], v[160:163], a[96:99]\n"
    "ds_read_b128 v[184:187], v249 offset:21504\n"
    "v_mfma_f32_16x16x32_bf16 a[112:115], v[72:75], v[168:171], a[112:115]\n"
    "ds_read_b128 v[188:191], v249 offset:10240\n"
    "v_mfma_f32_16x16x32_bf16 a[112:115], v[68:71], v[164:167], a[112:115]\n"
    "ds_read_b128 v[192:195], v249 offset:22528\n"
    "v_mfma_f32_16x16x32_bf16 a[128:131], v[68:71], v[172:175], a[128:131]\n"
    "buffer_load_dwordx2 v[236:237], v241, s[36:39], s81 offen offset:0 sc1\n"
    "v_mfma_f32_16x16x32_bf16 a[128:131], v[72:75], v[176:179], a[128:131]\n"
    "ds_read_b128 v[196:199], v249 offset:11264\n"
    "ds_read_b128 v[200:203], v249 offset:23552\n"
    "v_mfma_f32_16x16x32_bf16 a[132:135], v[80:83], v[176:179], a[132:135]\n"
    "buffer_store_dword v204, v244, s[16:19], s76 offen offset:0 sc1\n"
    "v_mfma_f32_16x16x32_bf16 a[132:135], v[76:79], v[172:175], a[132:135]\n"
    "v_exp_f32 v22, v22\n"
    "v_mfma_f32_16x16x32_bf16 a[116:119], v[76:79], v[164:167], a[116:119]\n"
    "buffer_store_dword v205, v244, s[16:19], s76 offen offset:1024 sc1\n"
    "v_mfma_f32_16x16x32_bf16 a[116:119], v[80:83], v[168:171], a[116:119]\n"
    "v_exp_f32 v23, v23\n"
    "v_mfma_f32_16x16x32_bf16 a[100:103], v[80:83], v[160:163], a[100:103]\n"
    "buffer_store_dword v206, v244, s[16:19], s76 offen offset:2048 sc1\n"
    "v_mfma_f32_16x16x32_bf16 a[100:103], v[76:79], v[156:159], a[100:103]\n"
    "v_exp_f32 v24, v24\n"
    "v_mfma_f32_16x16x32_bf16 a[104:107], v[84:87], v[156:159], a[104:107]\n"
    "buffer_store_dword v207, v244, s[16:19], s76 offen offset:3072 sc1\n"
    "v_mfma_f32_16x16x32_bf16 a[104:107], v[88:91], v[160:163], a[104:107]\n"
    "v_exp_f32 v25, v25\n"
    "v_mfma_f32_16x16x32_bf16 a[120:123], v[88:91], v[168:171], a[120:123]\n"
    "buffer_store_dword v212, v244, s[16:19], s77 offen offset:0 sc1\n"
    "v_mfma_f32_16x16x32_bf16 a[120:123], v[84:87], v[164:167], a[120:123]\n"
    "v_exp_f32 v26, v26\n"
    "v_mfma_f32_16x16x32_bf16 a[136:139], v[84:87], v[172:175], a[136:139]\n"
    "buffer_store_dword v213, v244, s[16:19], s77 offen offset:1024 sc1\n"
    "v_mfma_f32_16x16x32_bf16 a[136:139], v[88:91], v[176:179], a[136:139]\n"
    "v_exp_f32 v27, v27\n"
    "v_mfma_f32_16x16x32_bf16 a[140:143], v[96:99], v[176:179], a[140:143]\n"
    "buffer_store_dword v214, v244, s[16:19], s77 offen offset:2048 sc1\n"
    "v_mfma_f32_16x16x32_bf16 a[140:143], v[92:95], v[172:175], a[140:143]\n"
    "v_exp_f32 v28, v28\n"
    "v_mfma_f32_16x16x32_bf16 a[124:127], v[92:95], v[164:167], a[124:127]\n"
    "buffer_store_dword v215, v244, s[16:19], s77 offen offset:3072 sc1\n"
    "v_mfma_f32_16x16x32_bf16 a[124:127], v[96:99], v[168:171], a[124:127]\n"
    "v_exp_f32 v29, v29\n"
    "v_mfma_f32_16x16x32_bf16 a[108:111], v[96:99], v[160:163], a[108:111]\n"
    "buffer_store_dword v220, v244, s[16:19], s78 offen offset:0 sc1\n"
    "v_mfma_f32_16x16x32_bf16 a[108:111], v[92:95], v[156:159], a[108:111]\n"
    "s_waitcnt lgkmcnt(0)\n"
    "v_exp_f32 v30, v30\n"
    "v_mfma_f32_16x16x32_bf16 a[144:147], v[68:71], v[180:183], a[144:147]\n"
    "ds_read_b128 v[156:159], v3 offset:12288\n"
    "v_mfma_f32_16x16x32_bf16 a[144:147], v[72:75], v[184:187], a[144:147]\n"
    "ds_read_b128 v[160:163], v3 offset:14336\n"
    "v_mfma_f32_16x16x32_bf16 a[160:163], v[72:75], v[192:195], a[160:163]\n"
    "ds_read_b128 v[164:167], v3 offset:16384\n"
    "v_mfma_f32_16x16x32_bf16 a[160:163], v[68:71], v[188:191], a[160:163]\n"
    "ds_read_b128 v[168:171], v3 offset:18432\n"
    "v_mfma_f32_16x16x32_bf16 a[176:179], v[68:71], v[196:199], a[176:179]\n"
    "ds_read_b128 v[172:175], v3 offset:20480\n"
    "v_mfma_f32_16x16x32_bf16 a[176:179], v[72:75], v[200:203], a[176:179]\n"
    "ds_read_b128 v[176:179], v3 offset:22528\n"
    "v_mfma_f32_16x16x32_bf16 a[180:183], v[80:83], v[200:203], a[180:183]\n"
    "ds_read_b128 v[100:103], v250 offset:1664\n"
    "v_mfma_f32_16x16x32_bf16 a[180:183], v[76:79], v[196:199], a[180:183]\n"
    "buffer_store_dword v221, v244, s[16:19], s78 offen offset:1024 sc1\n"
    "v_mfma_f32_16x16x32_bf16 a[164:167], v[76:79], v[188:191], a[164:167]\n"
    "v_exp_f32 v31, v31\n"
    "v_mfma_f32_16x16x32_bf16 a[164:167], v[80:83], v[192:195], a[164:167]\n"
    "buffer_store_dword v222, v244, s[16:19], s78 offen offset:2048 sc1\n"
    "v_mfma_f32_16x16x32_bf16 a[148:151], v[80:83], v[184:187], a[148:151]\n"
    "v_exp_f32 v32, v32\n"
    "v_mfma_f32_16x16x32_bf16 a[148:151], v[76:79], v[180:183], a[148:151]\n"
    "buffer_store_dword v223, v244, s[16:19], s78 offen offset:3072 sc1\n"
    "v_mfma_f32_16x16x32_bf16 a[152:155], v[84:87], v[180:183], a[152:155]\n"
    "v_exp_f32 v33, v33\n"
    "v_mfma_f32_16x16x32_bf16 a[152:155], v[88:91], v[184:187], a[152:155]\n"
    "v_exp_f32 v34, v34\n"
    "v_mfma_f32_16x16x32_bf16 a[168:171], v[88:91], v[192:195], a[168:171]\n"
    "s_waitcnt vmcnt(13)\n"
    "ds_add_u32 v247, v246\n"
    "v_mfma_f32_16x16x32_bf16 a[168:171], v[84:87], v[188:191], a[168:171]\n"
    "v_exp_f32 v35, v35\n"
    "v_mfma_f32_16x16x32_bf16 a[184:187], v[84:87], v[196:199], a[184:187]\n"
    "v_mfma_f32_16x16x32_bf16 a[184:187], v[88:91], v[200:203], a[184:187]\n"
    "v_mfma_f32_16x16x32_bf16 a[188:191], v[96:99], v[200:203], a[188:191]\n"
    "v_mfma_f32_16x16x32_bf16 a[188:191], v[92:95], v[196:199], a[188:191]\n"
    "v_mfma_f32_16x16x32_bf16 a[172:175], v[92:95], v[188:191], a[172:175]\n"
    "s_sub_u32 s74, s68, s65\n"
    "v_add_u32 v250, 1536, v250\n"
    "v_add_u32 v251, 1536, v251\n"
    "v_mfma_f32_16x16x32_bf16 a[172:175], v[96:99], v[192:195], a[172:175]\n"
    "s_add_u32 s66, s66, 1\n"
    "v_mov_b32 v2, v3\n"
    "v_add_u32 v3, s74, v252\n"
    "v_mfma_f32_16x16x32_bf16 a[156:159], v[96:99], v[184:187], a[156:159]\n"
    "s_waitcnt lgkmcnt(1)\n"
    "s_cmp_lt_u32 s66, 31\n"
    "v_mfma_f32_16x16x32_bf16 a[156:159], v[92:95], v[180:183], a[156:159]\n"
    "s_cbranch_scc1 .LPV\n"
    "s_branch .Ltail\n"
    ".LtailP:\n"
    "v_mov_b32 v248, v249\n"
    "v_add_u32 v250, 768, v250\n"
    "v_add_u32 v251, 768, v251\n"
    ".Ltail:\n"
    "v_mfma_f32_16x16x32_bf16 v[36:39], v[156:159], a[192:195], v[100:103]\n"
    "s_lshl_b32 s72, s84, 9\n"
    "ds_read_b128 v[180:183], v2 offset:13312\n"
    "ds_read_b128 v[104:107], v250 offset:144\n"
    "v_mfma_f32_16x16x32_bf16 v[36:39], v[160:163], a[196:199], v[36:39]\n"
    "s_add_u32 s72, s72, 122880\n"
    "ds_read_b128 v[184:187], v2 offset:15360\n"
    "ds_read_b128 v[188:191], v2 offset:17408\n"
    "v_mfma_f32_16x16x32_bf16 v[36:39], v[164:167], a[200:203], v[36:39]\n"
    "s_mul_i32 s75, s84, 24576\n"
    "ds_read_b128 v[192:195], v2 offset:19456\n"
    "ds_read_b128 v[196:199], v2 offset:21504\n"
    "v_mfma_f32_16x16x32_bf16 v[36:39], v[168:171], a[204:207], v[36:39]\n"
    "s_add_u32 s76, s75, 6144\n"
    "ds_read_b128 v[200:203], v2 offset:23552\n"
    "v_cvt_pk_bf16_f32 v68, v4, v5\n"
    "v_mfma_f32_16x16x32_bf16 v[36:39], v[172:175], a[208:211], v[36:39]\n"
    "s_add_u32 s77, s76, 6144\n"
    "v_cvt_pk_bf16_f32 v69, v6, v7\n"
    "v_cvt_pk_bf16_f32 v84, v12, v13\n"
    "v_mfma_f32_16x16x32_bf16 v[36:39], v[176:179], a[212:215], v[36:39]\n"
    "s_add_u32 s78, s77, 6144\n"
    "v_cvt_pk_bf16_f32 v85, v14, v15\n"
    "v_cvt_pk_bf16_f32 v70, v20, v21\n"
    "v_mfma_f32_16x16x32_bf16 v[40:43], v[176:179], a[236:239], v[100:103]\n"
    "s_mov_b32 s74, 0x5b800000\n"
    "v_cvt_pk_bf16_f32 v71, v22, v23\n"
    "v_cvt_pk_bf16_f32 v86, v28, v29\n"
    "v_mfma_f32_16x16x32_bf16 v[40:43], v[172:175], a[232:235], v[40:43]\n"
    "s_mov_b32 s73, 0x7f800000\n"
    "v_cvt_pk_bf16_f32 v87, v30, v31\n"
    "ds_read_b128 v[244:247], v251 offset:0\n"
    "v_mfma_f32_16x16x32_bf16 v[40:43], v[168:171], a[228:231], v[40:43]\n"
    "s_waitcnt lgkmcnt(5)\n"
    "v_cvt_pk_bf16_f32 v76, v8, v9\n"
    "v_cvt_pk_bf16_f32 v77, v10, v11\n"
    "v_mfma_f32_16x16x32_bf16 v[40:43], v[164:167], a[224:227], v[40:43]\n"
    "s_waitcnt lgkmcnt(3)\n"
    "v_cvt_pk_bf16_f32 v92, v16, v17\n"
    "v_cvt_pk_bf16_f32 v93, v18, v19\n"
    "v_mfma_f32_16x16x32_bf16 v[40:43], v[160:163], a[220:223], v[40:43]\n"
    "s_waitcnt lgkmcnt(1)\n"
    "v_cvt_pk_bf16_f32 v78, v24, v25\n"
    "v_cvt_pk_bf16_f32 v79, v26, v27\n"
    "v_mfma_f32_16x16x32_bf16 v[40:43], v[156:159], a[216:219], v[40:43]\n"
    "v_cvt_pk_bf16_f32 v94, v32, v33\n"
    "v_cvt_pk_bf16_f32 v95, v34, v35\n"
    "v_mfma_f32_16x16x32_bf16 v[52:55], v[180:183], a[192:195], v[104:107]\n"
    "v_exp_f32 v36, v36\n"
    "v_mfma_f32_16x16x32_bf16 v[52:55], v[184:187], a[196:199], v[52:55]\n"
    "v_exp_f32 v37, v37\n"
    "v_mfma_f32_16x16x32_bf16 v[52:55], v[188:191], a[200:203], v[52:55]\n"
    "v_exp_f32 v38, v38\n"
    "v_mfma_f32_16x16x32_bf16 v[52:55], v[192:195], a[204:207], v[52:55]\n"
    "s_waitcnt lgkmcnt(0)\n"
    "v_exp_f32 v39, v39\n"
    "v_mfma_f32_16x16x32_bf16 v[52:55], v[196:199], a[208:211], v[52:55]\n"
    "v_cvt_pk_bf16_f32 v72, v36, v37\n"
    "v_cvt_pk_bf16_f32 v73, v38, v39\n"
    "v_mfma_f32_16x16x32_bf16 v[52:55], v[200:203], a[212:215], v[52:55]\n"
    "s_barrier\n"
    "ds_read_b128 v[4:7], v248 offset:0\n"
    "ds_read_b128 v[12:15], v248 offset:1024\n"
    "v_mfma_f32_16x16x32_bf16 v[56:59], v[200:203], a[236:239], v[104:107]\n"
    "ds_read_b128 v[20:23], v248 offset:2048\n"
    "ds_read_b128 v[28:31], v248 offset:3072\n"
    "v_mfma_f32_16x16x32_bf16 v[56:59], v[196:199], a[232:235], v[56:59]\n"
    "ds_read_b128 v[8:11], v248 offset:12288\n"
    "ds_read_b128 v[16:19], v248 offset:13312\n"
    "v_mfma_f32_16x16x32_bf16 v[56:59], v[192:195], a[228:231], v[56:59]\n"
    "ds_read_b128 v[24:27], v248 offset:14336\n"
    "ds_read_b128 v[32:35], v248 offset:15360\n"
    "v_mfma_f32_16x16x32_bf16 v[56:59], v[188:191], a[224:227], v[56:59]\n"
    "s_waitcnt vmcnt(0)\n"
    "v_exp_f32 v52, v52\n"
    "v_mfma_f32_16x16x32_bf16 v[56:59], v[184:187], a[220:223], v[56:59]\n"
    "v_exp_f32 v53, v53\n"
    "v_mfma_f32_16x16x32_bf16 v[56:59], v[180:183], a[216:219], v[56:59]\n"
    "v_exp_f32 v54, v54\n"
    "v_exp_f32 v55, v55\n"
    "v_cvt_pk_bf16_f32 v74, v52, v53\n"
    "v_cvt_pk_bf16_f32 v75, v54, v55\n"
    "s_waitcnt lgkmcnt(7)\n"
    "v_mfma_f32_16x16x32_bf16 a[0:3], v[68:71], v[4:7], a[0:3]\n"
    "s_waitcnt lgkmcnt(4)\n"
    "v_bfe_u32 v204, v1, 2, 6\n"
    "v_lshrrev_b32 v208, 4, v1\n"
    "v_mfma_f32_16x16x32_bf16 a[16:19], v[68:71], v[12:15], a[16:19]\n"
    "s_waitcnt lgkmcnt(2)\n"
    "v_add_u32 v206, s72, v204\n"
    "v_and_b32 v208, 48, v208\n"
    "v_mfma_f32_16x16x32_bf16 a[32:35], v[68:71], v[20:23], a[32:35]\n"
    "s_waitcnt lgkmcnt(0)\n"
    "v_add_u32 v207, s72, v208\n"
    "v_exp_f32 v40, v40\n"
    "v_mfma_f32_16x16x32_bf16 a[48:51], v[68:71], v[28:31], a[48:51]\n"
    "v_exp_f32 v41, v41\n"
    "v_smfmac_f32_16x16x64_bf16 v[108:111], v[244:247], v[68:75], v253\n"
    "s_nop 0\n"
    "s_nop 0\n"
    "v_exp_f32 v42, v42\n"
    "v_mfma_f32_16x16x32_bf16 a[0:3], v[72:75], v[8:11], a[0:3]\n"
    "s_nop 0\n"
    "v_exp_f32 v43, v43\n"
    "v_mfma_f32_16x16x32_bf16 a[16:19], v[72:75], v[16:19], a[16:19]\n"
    "s_nop 0\n"
    "v_add_f32 v108, v108, v109\n"
    "v_mov_b32 v109, v108\n"
    "v_mov_b32 v111, v110\n"
    "v_mfma_f32_16x16x32_bf16 a[32:35], v[72:75], v[24:27], a[32:35]\n"
    "v_permlane16_swap_b32 v108, v109\n"
    "v_add_f32 v108, v108, v109\n"
    "v_rcp_f32 v109, v108\n"
    "v_permlane16_swap_b32 v110, v111\n"
    "v_add_f32 v110, v110, v111\n"
    "v_mul_f32 v111, -v110, v109\n"
    "ds_write2_b32 v206, v109, v111 offset0:0 offset1:16\n"
    "ds_read_b128 v[36:39], v207 offset:0\n"
    "ds_read_b128 v[52:55], v207 offset:64\n"
    "v_exp_f32 v56, v56\n"
    "v_exp_f32 v57, v57\n"
    "v_exp_f32 v58, v58\n"
    "v_exp_f32 v59, v59\n"
    "v_mfma_f32_16x16x32_bf16 a[48:51], v[72:75], v[32:35], a[48:51]\n"
    "v_cvt_pk_bf16_f32 v80, v40, v41\n"
    "v_cvt_pk_bf16_f32 v81, v42, v43\n"
    "v_cvt_pk_bf16_f32 v82, v56, v57\n"
    "v_cvt_pk_bf16_f32 v83, v58, v59\n"
    "v_accvgpr_read_b32 v216, a0\n"
    "v_accvgpr_read_b32 v217, a16\n"
    "v_smfmac_f32_16x16x64_bf16 v[112:115], v[244:247], v[76:83], v253\n"
    "v_accvgpr_read_b32 v218, a32\n"
    "v_mfma_f32_16x16x32_bf16 v[44:47], v[156:159], a[240:243], v[100:103]\n"
    "v_accvgpr_read_b32 v219, a48\n"
    "v_mul_u32_u24 v205, 96, v208\n"
    "v_mfma_f32_16x16x32_bf16 v[44:47], v[160:163], a[244:247], v[44:47]\n"
    "s_waitcnt lgkmcnt(1)\n"
    "v_lshl_add_u32 v205, v204, 1, v205\n"
    "v_accvgpr_read_b32 v220, a1\n"
    "v_mfma_f32_16x16x32_bf16 v[44:47], v[164:167], a[248:251], v[44:47]\n"
    "s_waitcnt lgkmcnt(0)\n"
    "v_accvgpr_read_b32 v221, a17\n"
    "v_accvgpr_read_b32 v222, a33\n"
    "v_mfma_f32_16x16x32_bf16 v[44:47], v[168:171], a[252:255], v[44:47]\n"
    "v_accvgpr_read_b32 v223, a49\n"
    "v_add_f32 v112, v112, v113\n"
    "v_mfma_f32_16x16x32_bf16 v[44:47], v[172:175], v[124:127], v[44:47]\n"
    "v_accvgpr_read_b32 v224, a2\n"
    "v_accvgpr_read_b32 v225, a18\n"
    "v_mov_b32 v113, v112\n"
    "v_accvgpr_read_b32 v226, a34\n"
    "v_accvgpr_read_b32 v227, a50\n"
    "v_pk_fma_f32 v[216:217], v[216:217], v[36:37], v[52:53] op_sel:[0,0,0] op_sel_hi:[1,0,0]\n"
    "v_pk_fma_f32 v[218:219], v[218:219], v[36:37], v[52:53] op_sel:[0,0,0] op_sel_hi:[1,0,0]\n"
    "v_cvt_pk_bf16_f32 v216, v216, v217\n"
    "v_cvt_pk_bf16_f32 v217, v218, v219\n"
    "v_mfma_f32_16x16x32_bf16 v[44:47], v[176:179], v[128:131], v[44:47]\n"
    "v_mov_b32 v115, v114\n"
    "v_permlane16_swap_b32 v112, v113\n"
    "v_mfma_f32_16x16x32_bf16 a[52:55], v[76:79], v[28:31], a[52:55]\n"
    "v_add_f32 v112, v112, v113\n"
    "buffer_store_dwordx2 v[216:217], v205, s[24:27], s75 offen offset:0 nt sc1\n"
    "v_mfma_f32_16x16x32_bf16 a[52:55], v[80:83], v[32:35], a[52:55]\n"
    "v_rcp_f32 v113, v112\n"
    "v_mfma_f32_16x16x32_bf16 a[36:39], v[80:83], v[24:27], a[36:39]\n"
    "v_permlane16_swap_b32 v114, v115\n"
    "v_add_f32 v114, v114, v115\n"
    "v_mfma_f32_16x16x32_bf16 a[36:39], v[76:79], v[20:23], a[36:39]\n"
    "v_mul_f32 v115, -v114, v113\n"
    "ds_write2_b32 v206, v113, v115 offset0:32 offset1:48\n"
    "ds_read_b128 v[40:43], v207 offset:128\n"
    "ds_read_b128 v[56:59], v207 offset:192\n"
    "v_pk_fma_f32 v[220:221], v[220:221], v[36:37], v[52:53] op_sel:[0,1,1] op_sel_hi:[1,1,1]\n"
    "v_pk_fma_f32 v[222:223], v[222:223], v[36:37], v[52:53] op_sel:[0,1,1] op_sel_hi:[1,1,1]\n"
    "v_cvt_pk_bf16_f32 v220, v220, v221\n"
    "v_cvt_pk_bf16_f32 v221, v222, v223\n"
    "v_mfma_f32_16x16x32_bf16 a[20:23], v[76:79], v[12:15], a[20:23]\n"
    "buffer_store_dwordx2 v[220:221], v205, s[24:27], s75 offen offset:384 nt sc1\n"
    "v_mfma_f32_16x16x32_bf16 a[20:23], v[80:83], v[16:19], a[20:23]\n"
    "v_accvgpr_read_b32 v228, a3\n"
    "v_accvgpr_read_b32 v229, a19\n"
    "v_mfma_f32_16x16x32_bf16 a[4:7], v[80:83], v[8:11], a[4:7]\n"
    "v_accvgpr_read_b32 v230, a35\n"
    "v_accvgpr_read_b32 v231, a51\n"
    "v_mfma_f32_16x16x32_bf16 a[4:7], v[76:79], v[4:7], a[4:7]\n"
    "v_accvgpr_read_b32 v234, a36\n"
    "v_accvgpr_read_b32 v235, a52\n"
    "v_mfma_f32_16x16x32_bf16 v[60:63], v[180:183], a[240:243], v[104:107]\n"
    "v_accvgpr_read_b32 v233, a20\n"
    "v_accvgpr_read_b32 v217, a21\n"
    "v_accvgpr_read_b32 v218, a37\n"
    "v_accvgpr_read_b32 v219, a53\n"
    "v_exp_f32 v44, v44\n"
    "v_pk_fma_f32 v[224:225], v[224:225], v[38:39], v[54:55] op_sel:[0,0,0] op_sel_hi:[1,0,0]\n"
    "v_pk_fma_f32 v[226:227], v[226:227], v[38:39], v[54:55] op_sel:[0,0,0] op_sel_hi:[1,0,0]\n"
    "v_cvt_pk_bf16_f32 v224, v224, v225\n"
    "v_cvt_pk_bf16_f32 v225, v226, v227\n"
    "v_mfma_f32_16x16x32_bf16 v[60:63], v[184:187], a[244:247], v[60:63]\n"
    "s_waitcnt lgkmcnt(0)\n"
    "buffer_store_dwordx2 v[224:225], v205, s[24:27], s75 offen offset:768 nt sc1\n"
    "v_mfma_f32_16x16x32_bf16 v[60:63], v[188:191], a[248:251], v[60:63]\n"
    "v_accvgpr_read_b32 v232, a4\n"
    "v_accvgpr_read_b32 v216, a5\n"
    "v_mfma_f32_16x16x32_bf16 v[60:63], v[192:195], a[252:255], v[60:63]\n"
    "v_exp_f32 v45, v45\n"
    "v_mfma_f32_16x16x32_bf16 v[60:63], v[196:199], v[124:127], v[60:63]\n"
    "v_exp_f32 v46, v46\n"
    "v_mfma_f32_16x16x32_bf16 v[60:63], v[200:203], v[128:131], v[60:63]\n"
    "v_exp_f32 v47, v47\n"
    "v_cvt_pk_bf16_f32 v88, v44, v45\n"
    "v_cvt_pk_bf16_f32 v89, v46, v47\n"
    "v_accvgpr_read_b32 v220, a6\n"
    "v_pk_fma_f32 v[228:229], v[228:229], v[38:39], v[54:55] op_sel:[0,1,1] op_sel_hi:[1,1,1]\n"
    "v_pk_fma_f32 v[230:231], v[230:231], v[38:39], v[54:55] op_sel:[0,1,1] op_sel_hi:[1,1,1]\n"
    "v_cvt_pk_bf16_f32 v228, v228, v229\n"
    "v_cvt_pk_bf16_f32 v229, v230, v231\n"
    "v_mfma_f32_16x16x32_bf16 v[48:51], v[176:179], v[152:155], v[100:103]\n"
    "buffer_store_dwordx2 v[228:229], v205, s[24:27], s75 offen offset:1152 nt sc1\n"
    "v_mfma_f32_16x16x32_bf16 v[48:51], v[172:175], v[148:151], v[48:51]\n"
    "v_exp_f32 v60, v60\n"
    "v_mfma_f32_16x16x32_bf16 v[48:51], v[168:171], v[144:147], v[48:51]\n"
    "v_exp_f32 v61, v61\n"
    "v_mfma_f32_16x16x32_bf16 v[48:51], v[164:167], v[140:143], v[48:51]\n"
    "v_exp_f32 v62, v62\n"
    "v_mfma_f32_16x16x32_bf16 v[48:51], v[160:163], v[136:139], v[48:51]\n"
    "v_exp_f32 v63, v63\n"
    "v_cvt_pk_bf16_f32 v90, v60, v61\n"
    "v_cvt_pk_bf16_f32 v91, v62, v63\n"
    "v_accvgpr_read_b32 v221, a22\n"
    "v_pk_fma_f32 v[232:233], v[232:233], v[40:41], v[56:57] op_sel:[0,0,0] op_sel_hi:[1,0,0]\n"
    "v_pk_fma_f32 v[234:235], v[234:235], v[40:41], v[56:57] op_sel:[0,0,0] op_sel_hi:[1,0,0]\n"
    "v_cvt_pk_bf16_f32 v232, v232, v233\n"
    "v_cvt_pk_bf16_f32 v233, v234, v235\n"
    "v_mfma_f32_16x16x32_bf16 v[48:51], v[156:159], v[132:135], v[48:51]\n"
    "buffer_store_dwordx2 v[232:233], v205, s[24:27], s76 offen offset:0 nt sc1\n"
    "v_mfma_f32_16x16x32_bf16 v[64:67], v[200:203], v[152:155], v[104:107]\n"
    "v_accvgpr_read_b32 v222, a38\n"
    "v_accvgpr_read_b32 v223, a54\n"
    "v_mfma_f32_16x16x32_bf16 v[64:67], v[196:199], v[148:151], v[64:67]\n"
    "v_accvgpr_read_b32 v224, a7\n"
    "v_accvgpr_read_b32 v225, a23\n"
    "v_mfma_f32_16x16x32_bf16 v[64:67], v[192:195], v[144:147], v[64:67]\n"
    "v_accvgpr_read_b32 v226, a39\n"
    "v_accvgpr_read_b32 v227, a55\n"
    "v_mfma_f32_16x16x32_bf16 v[64:67], v[188:191], v[140:143], v[64:67]\n"
    "v_exp_f32 v48, v48\n"
    "v_exp_f32 v49, v49\n"
    "v_exp_f32 v50, v50\n"
    "v_pk_fma_f32 v[216:217], v[216:217], v[40:41], v[56:57] op_sel:[0,1,1] op_sel_hi:[1,1,1]\n"
    "v_pk_fma_f32 v[218:219], v[218:219], v[40:41], v[56:57] op_sel:[0,1,1] op_sel_hi:[1,1,1]\n"
    "v_cvt_pk_bf16_f32 v216, v216, v217\n"
    "v_cvt_pk_bf16_f32 v217, v218, v219\n"
    "v_mfma_f32_16x16x32_bf16 v[64:67], v[184:187], v[136:139], v[64:67]\n"
    "buffer_store_dwordx2 v[216:217], v205, s[24:27], s76 offen offset:384 nt sc1\n"
    "v_mfma_f32_16x16x32_bf16 v[64:67], v[180:183], v[132:135], v[64:67]\n"
    "v_exp_f32 v51, v51\n"
    "v_smfmac_f32_16x16x64_bf16 v[116:119], v[244:247], v[84:91], v253\n"
    "s_nop 0\n"
    "s_nop 0\n"
    "v_cvt_pk_bf16_f32 v96, v48, v49\n"
    "v_cvt_pk_bf16_f32 v97, v50, v51\n"
    "ds_read_b128 v[156:159], v248 offset:4096\n"
    "ds_read_b128 v[160:163], v248 offset:16384\n"
    "v_pk_fma_f32 v[220:221], v[220:221], v[42:43], v[58:59] op_sel:[0,0,0] op_sel_hi:[1,0,0]\n"
    "v_pk_fma_f32 v[222:223], v[222:223], v[42:43], v[58:59] op_sel:[0,0,0] op_sel_hi:[1,0,0]\n"
    "v_add_f32 v116, v116, v117\n"
    "v_mov_b32 v117, v116\n"
    "v_cvt_pk_bf16_f32 v220, v220, v221\n"
    "v_cvt_pk_bf16_f32 v221, v222, v223\n"
    "v_mov_b32 v119, v118\n"
    "v_permlane16_swap_b32 v116, v117\n"
    "buffer_store_dwordx2 v[220:221], v205, s[24:27], s76 offen offset:768 nt sc1\n"
    "v_add_f32 v116, v116, v117\n"
    "v_mfma_f32_16x16x32_bf16 a[8:11], v[84:87], v[4:7], a[8:11]\n"
    "v_rcp_f32 v117, v116\n"
    "v_permlane16_swap_b32 v118, v119\n"
    "v_add_f32 v118, v118, v119\n"
    "v_mul_f32 v119, -v118, v117\n"
    "v_mfma_f32_16x16x32_bf16 a[8:11], v[88:91], v[8:11], a[8:11]\n"
    "ds_write2_b32 v206, v117, v119 offset0:64 offset1:80\n"
    "ds_read_b128 v[44:47], v207 offset:256\n"
    "ds_read_b128 v[60:63], v207 offset:320\n"
    "v_mfma_f32_16x16x32_bf16 a[24:27], v[88:91], v[16:19], a[24:27]\n"
    "s_waitcnt lgkmcnt(3)\n"
    "v_exp_f32 v64, v64\n"
    "v_mfma_f32_16x16x32_bf16 a[24:27], v[84:87], v[12:15], a[24:27]\n"
    "v_exp_f32 v65, v65\n"
    "v_mfma_f32_16x16x32_bf16 a[40:43], v[84:87], v[20:23], a[40:43]\n"
    "v_accvgpr_read_b32 v228, a8\n"
    "v_accvgpr_read_b32 v232, a9\n"
    "v_exp_f32 v66, v66\n"
    "v_exp_f32 v67, v67\n"
    "v_pk_fma_f32 v[224:225], v[224:225], v[42:43], v[58:59] op_sel:[0,1,1] op_sel_hi:[1,1,1]\n"
    "v_pk_fma_f32 v[226:227], v[226:227], v[42:43], v[58:59] op_sel:[0,1,1] op_sel_hi:[1,1,1]\n"
    "v_cvt_pk_bf16_f32 v224, v224, v225\n"
    "v_cvt_pk_bf16_f32 v225, v226, v227\n"
    "buffer_store_dwordx2 v[224:225], v205, s[24:27], s76 offen offset:1152 nt sc1\n"
    "v_mfma_f32_16x16x32_bf16 a[40:43], v[88:91], v[24:27], a[40:43]\n"
    "v_accvgpr_read_b32 v229, a24\n"
    "v_accvgpr_read_b32 v233, a25\n"
    "v_mfma_f32_16x16x32_bf16 a[56:59], v[88:91], v[32:35], a[56:59]\n"
    "s_waitcnt lgkmcnt(0)\n"
    "v_cvt_pk_bf16_f32 v98, v64, v65\n"
    "v_cvt_pk_bf16_f32 v99, v66, v67\n"
    "v_mfma_f32_16x16x32_bf16 a[56:59], v[84:87], v[28:31], a[56:59]\n"
    "s_nop 0\n"
    "v_accvgpr_read_b32 v230, a40\n"
    "v_accvgpr_read_b32 v234, a41\n"
    "v_accvgpr_read_b32 v216, a10\n"
    "v_accvgpr_read_b32 v217, a26\n"
    "v_accvgpr_read_b32 v218, a42\n"
    "v_pk_fma_f32 v[228:229], v[228:229], v[44:45], v[60:61] op_sel:[0,0,0] op_sel_hi:[1,0,0]\n"
    "v_cvt_pk_bf16_f32 v228, v228, v229\n"
    "v_accvgpr_read_b32 v231, a56\n"
    "v_pk_fma_f32 v[230:231], v[230:231], v[44:45], v[60:61] op_sel:[0,0,0] op_sel_hi:[1,0,0]\n"
    "v_cvt_pk_bf16_f32 v229, v230, v231\n"
    "buffer_store_dwordx2 v[228:229], v205, s[24:27], s77 offen offset:0 nt sc1\n"
    "v_accvgpr_read_b32 v235, a57\n"
    "v_pk_fma_f32 v[232:233], v[232:233], v[44:45], v[60:61] op_sel:[0,1,1] op_sel_hi:[1,1,1]\n"
    "v_pk_fma_f32 v[234:235], v[234:235], v[44:45], v[60:61] op_sel:[0,1,1] op_sel_hi:[1,1,1]\n"
    "v_cvt_pk_bf16_f32 v232, v232, v233\n"
    "v_cvt_pk_bf16_f32 v233, v234, v235\n"
    "buffer_store_dwordx2 v[232:233], v205, s[24:27], s77 offen offset:384 nt sc1\n"
    "v_smfmac_f32_16x16x64_bf16 v[120:123], v[244:247], v[92:99], v253\n"
    "s_nop 0\n"
    "s_nop 0\n"
    "v_accvgpr_read_b32 v219, a58\n"
    "v_accvgpr_read_b32 v220, a11\n"
    "v_accvgpr_read_b32 v221, a27\n"
    "v_accvgpr_read_b32 v222, a43\n"
    "v_pk_fma_f32 v[216:217], v[216:217], v[46:47], v[62:63] op_sel:[0,0,0] op_sel_hi:[1,0,0]\n"
    "v_pk_fma_f32 v[218:219], v[218:219], v[46:47], v[62:63] op_sel:[0,0,0] op_sel_hi:[1,0,0]\n"
    "v_add_f32 v120, v120, v121\n"
    "v_mov_b32 v121, v120\n"
    "v_cvt_pk_bf16_f32 v216, v216, v217\n"
    "v_cvt_pk_bf16_f32 v217, v218, v219\n"
    "v_mov_b32 v123, v122\n"
    "v_permlane16_swap_b32 v120, v121\n"
    "v_mfma_f32_16x16x32_bf16 a[60:63], v[92:95], v[28:31], a[60:63]\n"
    "buffer_store_dwordx2 v[216:217], v205, s[24:27], s77 offen offset:768 nt sc1\n"
    "v_add_f32 v120, v120, v121\n"
    "v_rcp_f32 v121, v120\n"
    "v_permlane16_swap_b32 v122, v123\n"
    "v_add_f32 v122, v122, v123\n"
    "v_mfma_f32_16x16x32_bf16 a[60:63], v[96:99], v[32:35], a[60:63]\n"
    "v_mul_f32 v123, -v122, v121\n"
    "ds_write2_b32 v206, v121, v123 offset0:96 offset1:112\n"
    "ds_read_b128 v[48:51], v207 offset:384\n"
    "ds_read_b128 v[64:67], v207 offset:448\n"
    "v_mfma_f32_16x16x32_bf16 a[44:47], v[96:99], v[24:27], a[44:47]\n"
    "v_accvgpr_read_b32 v223, a59\n"
    "ds_read_b128 v[168:171], v248 offset:17408\n"
    "v_mfma_f32_16x16x32_bf16 a[44:47], v[92:95], v[20:23], a[44:47]\n"
    "v_accvgpr_read_b32 v227, a60\n"
    "v_accvgpr_read_b32 v231, a61\n"
    "v_mfma_f32_16x16x32_bf16 a[28:31], v[92:95], v[12:15], a[28:31]\n"
    "ds_read_b128 v[164:167], v248 offset:5120\n"
    "ds_read_b128 v[172:175], v248 offset:6144\n"
    "ds_read_b128 v[176:179], v248 offset:18432\n"
    "ds_read_b128 v[184:187], v248 offset:19456\n"
    "v_accvgpr_read_b32 v235, a62\n"
    "v_pk_fma_f32 v[220:221], v[220:221], v[46:47], v[62:63] op_sel:[0,1,1] op_sel_hi:[1,1,1]\n"
    "v_pk_fma_f32 v[222:223], v[222:223], v[46:47], v[62:63] op_sel:[0,1,1] op_sel_hi:[1,1,1]\n"
    "v_cvt_pk_bf16_f32 v220, v220, v221\n"
    "v_cvt_pk_bf16_f32 v221, v222, v223\n"
    "v_mfma_f32_16x16x32_bf16 a[28:31], v[96:99], v[16:19], a[28:31]\n"
    "buffer_store_dwordx2 v[220:221], v205, s[24:27], s77 offen offset:1152 nt sc1\n"
    "v_mfma_f32_16x16x32_bf16 a[12:15], v[96:99], v[8:11], a[12:15]\n"
    "s_waitcnt lgkmcnt(6)\n"
    "v_accvgpr_read_b32 v226, a44\n"
    "v_accvgpr_read_b32 v230, a45\n"
    "v_mfma_f32_16x16x32_bf16 a[12:15], v[92:95], v[4:7], a[12:15]\n"
    "s_waitcnt lgkmcnt(5)\n"
    "v_accvgpr_read_b32 v234, a46\n"
    "v_accvgpr_read_b32 v225, a28\n"
    "v_accvgpr_read_b32 v229, a29\n"
    "v_accvgpr_read_b32 v233, a30\n"
    "ds_read_b128 v[180:183], v248 offset:7168\n"
    "v_pk_fma_f32 v[226:227], v[226:227], v[48:49], v[64:65] op_sel:[0,0,0] op_sel_hi:[1,0,0]\n"
    "v_pk_fma_f32 v[230:231], v[230:231], v[48:49], v[64:65] op_sel:[0,1,1] op_sel_hi:[1,1,1]\n"
    "v_accvgpr_read_b32 v224, a12\n"
    "v_pk_fma_f32 v[224:225], v[224:225], v[48:49], v[64:65] op_sel:[0,0,0] op_sel_hi:[1,0,0]\n"
    "v_cvt_pk_bf16_f32 v224, v224, v225\n"
    "v_cvt_pk_bf16_f32 v225, v226, v227\n"
    "buffer_store_dwordx2 v[224:225], v205, s[24:27], s78 offen offset:0 nt sc1\n"
    "v_accvgpr_read_b32 v228, a13\n"
    "v_pk_fma_f32 v[228:229], v[228:229], v[48:49], v[64:65] op_sel:[0,1,1] op_sel_hi:[1,1,1]\n"
    "v_cvt_pk_bf16_f32 v228, v228, v229\n"
    "v_cvt_pk_bf16_f32 v229, v230, v231\n"
    "buffer_store_dwordx2 v[228:229], v205, s[24:27], s78 offen offset:384 nt sc1\n"
    "v_accvgpr_read_b32 v232, a14\n"
    "v_pk_fma_f32 v[232:233], v[232:233], v[50:51], v[66:67] op_sel:[0,0,0] op_sel_hi:[1,0,0]\n"
    "v_pk_fma_f32 v[234:235], v[234:235], v[50:51], v[66:67] op_sel:[0,0,0] op_sel_hi:[1,0,0]\n"
    "v_cvt_pk_bf16_f32 v232, v232, v233\n"
    "v_cvt_pk_bf16_f32 v233, v234, v235\n"
    "buffer_store_dwordx2 v[232:233], v205, s[24:27], s78 offen offset:768 nt sc1\n"
    "v_mfma_f32_16x16x32_bf16 a[64:67], v[68:71], v[156:159], a[64:67]\n"
    "s_waitcnt lgkmcnt(1)\n"
    "v_accvgpr_read_b32 v216, a15\n"
    "v_accvgpr_read_b32 v217, a31\n"
    "v_mfma_f32_16x16x32_bf16 a[64:67], v[72:75], v[160:163], a[64:67]\n"
    "s_waitcnt lgkmcnt(0)\n"
    "v_accvgpr_read_b32 v218, a47\n"
    "v_accvgpr_read_b32 v219, a63\n"
    "v_mfma_f32_16x16x32_bf16 a[80:83], v[72:75], v[168:171], a[80:83]\n"
    "ds_read_b128 v[188:191], v248 offset:8192\n"
    "ds_read_b128 v[192:195], v248 offset:20480\n"
    "v_mfma_f32_16x16x32_bf16 a[80:83], v[68:71], v[164:167], a[80:83]\n"
    "ds_read_b128 v[200:203], v248 offset:21504\n"
    "v_accvgpr_read_b32 v220, a64\n"
    "v_mfma_f32_16x16x32_bf16 a[96:99], v[68:71], v[172:175], a[96:99]\n"
    "v_accvgpr_read_b32 v224, a65\n"
    "v_accvgpr_read_b32 v228, a66\n"
    "v_accvgpr_read_b32 v232, a67\n"
    "ds_read_b128 v[196:199], v248 offset:9216\n"
    "ds_read_b128 v[124:127], v248 offset:10240\n"
    "v_pk_fma_f32 v[216:217], v[216:217], v[50:51], v[66:67] op_sel:[0,1,1] op_sel_hi:[1,1,1]\n"
    "v_pk_fma_f32 v[218:219], v[218:219], v[50:51], v[66:67] op_sel:[0,1,1] op_sel_hi:[1,1,1]\n"
    "v_cvt_pk_bf16_f32 v216, v216, v217\n"
    "v_cvt_pk_bf16_f32 v217, v218, v219\n"
    "buffer_store_dwordx2 v[216:217], v205, s[24:27], s78 offen offset:1152 nt sc1\n"
    "v_mfma_f32_16x16x32_bf16 a[96:99], v[72:75], v[176:179], a[96:99]\n"
    "v_accvgpr_read_b32 v221, a80\n"
    "v_accvgpr_read_b32 v225, a81\n"
    "v_mfma_f32_16x16x32_bf16 a[112:115], v[72:75], v[184:187], a[112:115]\n"
    "v_accvgpr_read_b32 v229, a82\n"
    "v_accvgpr_read_b32 v233, a83\n"
    "v_mfma_f32_16x16x32_bf16 a[112:115], v[68:71], v[180:183], a[112:115]\n"
    "s_waitcnt lgkmcnt(3)\n"
    "ds_read_b128 v[128:131], v248 offset:22528\n"
    "v_accvgpr_read_b32 v222, a96\n"
    "v_accvgpr_read_b32 v226, a97\n"
    "v_accvgpr_read_b32 v230, a98\n"
    "v_accvgpr_read_b32 v234, a99\n"
    "v_pk_fma_f32 v[220:221], v[220:221], v[36:37], v[52:53] op_sel:[0,0,0] op_sel_hi:[1,0,0]\n"
    "v_cvt_pk_bf16_f32 v220, v220, v221\n"
    "v_accvgpr_read_b32 v223, a112\n"
    "v_pk_fma_f32 v[222:223], v[222:223], v[36:37], v[52:53] op_sel:[0,0,0] op_sel_hi:[1,0,0]\n"
    "v_cvt_pk_bf16_f32 v221, v222, v223\n"
    "buffer_store_dwordx2 v[220:221], v205, s[24:27], s75 offen offset:128 nt sc1\n"
    "v_accvgpr_read_b32 v227, a113\n"
    "v_pk_fma_f32 v[224:225], v[224:225], v[36:37], v[52:53] op_sel:[0,1,1] op_sel_hi:[1,1,1]\n"
    "v_pk_fma_f32 v[226:227], v[226:227], v[36:37], v[52:53] op_sel:[0,1,1] op_sel_hi:[1,1,1]\n"
    "v_cvt_pk_bf16_f32 v224, v224, v225\n"
    "v_cvt_pk_bf16_f32 v225, v226, v227\n"
    "buffer_store_dwordx2 v[224:225], v205, s[24:27], s75 offen offset:512 nt sc1\n"
    "v_accvgpr_read_b32 v231, a114\n"
    "v_pk_fma_f32 v[228:229], v[228:229], v[38:39], v[54:55] op_sel:[0,0,0] op_sel_hi:[1,0,0]\n"
    "v_pk_fma_f32 v[230:231], v[230:231], v[38:39], v[54:55] op_sel:[0,0,0] op_sel_hi:[1,0,0]\n"
    "v_cvt_pk_bf16_f32 v228, v228, v229\n"
    "v_cvt_pk_bf16_f32 v229, v230, v231\n"
    "v_mfma_f32_16x16x32_bf16 a[116:119], v[76:79], v[180:183], a[116:119]\n"
    "s_waitcnt lgkmcnt(0)\n"
    "buffer_store_dwordx2 v[228:229], v205, s[24:27], s75 offen offset:896 nt sc1\n"
    "v_mfma_f32_16x16x32_bf16 a[116:119], v[80:83], v[184:187], a[116:119]\n"
    "v_accvgpr_read_b32 v235, a115\n"
    "ds_read_b128 v[136:139], v248 offset:23552\n"
    "v_mfma_f32_16x16x32_bf16 a[100:103], v[80:83], v[176:179], a[100:103]\n"
    "ds_read_b128 v[132:135], v248 offset:11264\n"
    "v_mov_b32 v212, 0\n"
    "v_mfma_f32_16x16x32_bf16 a[100:103], v[76:79], v[172:175], a[100:103]\n"
    "v_fma_f32 v212, |v110|, 4.0, v212\n"
    "v_fma_f32 v212, v108, s74, v212\n"
    "v_mfma_f32_16x16x32_bf16 a[84:87], v[76:79], v[164:167], a[84:87]\n"
    "v_accvgpr_read_b32 v219, a116\n"
    "v_accvgpr_read_b32 v223, a117\n"
    "v_accvgpr_read_b32 v227, a118\n"
    "v_accvgpr_read_b32 v231, a119\n"
    "v_fma_f32 v212, v109, s74, v212\n"
    "v_pk_fma_f32 v[232:233], v[232:233], v[38:39], v[54:55] op_sel:[0,1,1] op_sel_hi:[1,1,1]\n"
    "v_pk_fma_f32 v[234:235], v[234:235], v[38:39], v[54:55] op_sel:[0,1,1] op_sel_hi:[1,1,1]\n"
    "v_cvt_pk_bf16_f32 v232, v232, v233\n"
    "v_cvt_pk_bf16_f32 v233, v234, v235\n"
    "v_mfma_f32_16x16x32_bf16 a[84:87], v[80:83], v[168:171], a[84:87]\n"
    "buffer_store_dwordx2 v[232:233], v205, s[24:27], s75 offen offset:1280 nt sc1\n"
    "v_mfma_f32_16x16x32_bf16 a[68:71], v[80:83], v[160:163], a[68:71]\n"
    "v_accvgpr_read_b32 v218, a100\n"
    "v_accvgpr_read_b32 v222, a101\n"
    "v_mfma_f32_16x16x32_bf16 a[68:71], v[76:79], v[156:159], a[68:71]\n"
    "s_waitcnt lgkmcnt(0)\n"
    "v_accvgpr_read_b32 v226, a102\n"
    "v_accvgpr_read_b32 v230, a103\n"
    "v_accvgpr_read_b32 v217, a84\n"
    "v_accvgpr_read_b32 v221, a85\n"
    "v_accvgpr_read_b32 v225, a86\n"
    "v_pk_fma_f32 v[218:219], v[218:219], v[40:41], v[56:57] op_sel:[0,0,0] op_sel_hi:[1,0,0]\n"
    "v_pk_fma_f32 v[222:223], v[222:223], v[40:41], v[56:57] op_sel:[0,1,1] op_sel_hi:[1,1,1]\n"
    "v_accvgpr_read_b32 v216, a68\n"
    "v_pk_fma_f32 v[216:217], v[216:217], v[40:41], v[56:57] op_sel:[0,0,0] op_sel_hi:[1,0,0]\n"
    "v_cvt_pk_bf16_f32 v216, v216, v217\n"
    "v_cvt_pk_bf16_f32 v217, v218, v219\n"
    "buffer_store_dwordx2 v[216:217], v205, s[24:27], s76 offen offset:128 nt sc1\n"
    "v_accvgpr_read_b32 v220, a69\n"
    "v_pk_fma_f32 v[220:221], v[220:221], v[40:41], v[56:57] op_sel:[0,1,1] op_sel_hi:[1,1,1]\n"
    "v_cvt_pk_bf16_f32 v220, v220, v221\n"
    "v_cvt_pk_bf16_f32 v221, v222, v223\n"
    "buffer_store_dwordx2 v[220:221], v205, s[24:27], s76 offen offset:512 nt sc1\n"
    "v_accvgpr_read_b32 v224, a70\n"
    "v_pk_fma_f32 v[224:225], v[224:225], v[42:43], v[58:59] op_sel:[0,0,0] op_sel_hi:[1,0,0]\n"
    "v_pk_fma_f32 v[226:227], v[226:227], v[42:43], v[58:59] op_sel:[0,0,0] op_sel_hi:[1,0,0]\n"
    "v_cvt_pk_bf16_f32 v224, v224, v225\n"
    "v_cvt_pk_bf16_f32 v225, v226, v227\n"
    "buffer_store_dwordx2 v[224:225], v205, s[24:27], s76 offen offset:896 nt sc1\n"
    "v_mfma_f32_16x16x32_bf16 a[72:75], v[84:87], v[156:159], a[72:75]\n"
    "v_accvgpr_read_b32 v228, a71\n"
    "v_accvgpr_read_b32 v229, a87\n"
    "v_mfma_f32_16x16x32_bf16 a[72:75], v[88:91], v[160:163], a[72:75]\n"
    "v_fma_f32 v212, |v114|, 4.0, v212\n"
    "v_fma_f32 v212, v112, s74, v212\n"
    "v_mfma_f32_16x16x32_bf16 a[88:91], v[88:91], v[168:171], a[88:91]\n"
    "v_fma_f32 v212, v113, s74, v212\n"
    "v_fma_f32 v212, |v118|, 4.0, v212\n"
    "v_mfma_f32_16x16x32_bf16 a[88:91], v[84:87], v[164:167], a[88:91]\n"
    "v_fma_f32 v212, v116, s74, v212\n"
    "v_fma_f32 v212, v117, s74, v212\n"
    "v_mfma_f32_16x16x32_bf16 a[104:107], v[84:87], v[172:175], a[104:107]\n"
    "v_accvgpr_read_b32 v232, a72\n"
    "v_accvgpr_read_b32 v216, a73\n"
    "v_accvgpr_read_b32 v220, a74\n"
    "v_accvgpr_read_b32 v224, a75\n"
    "v_fma_f32 v212, |v122|, 4.0, v212\n"
    "v_pk_fma_f32 v[228:229], v[228:229], v[42:43], v[58:59] op_sel:[0,1,1] op_sel_hi:[1,1,1]\n"
    "v_pk_fma_f32 v[230:231], v[230:231], v[42:43], v[58:59] op_sel:[0,1,1] op_sel_hi:[1,1,1]\n"
    "v_cvt_pk_bf16_f32 v228, v228, v229\n"
    "v_cvt_pk_bf16_f32 v229, v230, v231\n"
    "buffer_store_dwordx2 v[228:229], v205, s[24:27], s76 offen offset:1280 nt sc1\n"
    "v_mfma_f32_16x16x32_bf16 a[104:107], v[88:91], v[176:179], a[104:107]\n"
    "v_accvgpr_read_b32 v233, a88\n"
    "v_accvgpr_read_b32 v217, a89\n"
    "v_mfma_f32_16x16x32_bf16 a[120:123], v[88:91], v[184:187], a[120:123]\n"
    "v_accvgpr_read_b32 v221, a90\n"
    "v_accvgpr_read_b32 v225, a91\n"
    "v_mfma_f32_16x16x32_bf16 a[120:123], v[84:87], v[180:183], a[120:123]\n"
    "s_nop 0\n"
    "v_fma_f32 v212, v120, s74, v212\n"
    "v_accvgpr_read_b32 v234, a104\n"
    "v_accvgpr_read_b32 v218, a105\n"
    "v_accvgpr_read_b32 v222, a106\n"
    "v_accvgpr_read_b32 v226, a107\n"
    "v_pk_fma_f32 v[232:233], v[232:233], v[44:45], v[60:61] op_sel:[0,0,0] op_sel_hi:[1,0,0]\n"
    "v_cvt_pk_bf16_f32 v232, v232, v233\n"
    "v_accvgpr_read_b32 v235, a120\n"
    "v_pk_fma_f32 v[234:235], v[234:235], v[44:45], v[60:61] op_sel:[0,0,0] op_sel_hi:[1,0,0]\n"
    "v_cvt_pk_bf16_f32 v233, v234, v235\n"
    "buffer_store_dwordx2 v[232:233], v205, s[24:27], s77 offen offset:128 nt sc1\n"
    "v_accvgpr_read_b32 v219, a121\n"
    "v_pk_fma_f32 v[216:217], v[216:217], v[44:45], v[60:61] op_sel:[0,1,1] op_sel_hi:[1,1,1]\n"
    "v_pk_fma_f32 v[218:219], v[218:219], v[44:45], v[60:61] op_sel:[0,1,1] op_sel_hi:[1,1,1]\n"
    "v_cvt_pk_bf16_f32 v216, v216, v217\n"
    "v_cvt_pk_bf16_f32 v217, v218, v219\n"
    "buffer_store_dwordx2 v[216:217], v205, s[24:27], s77 offen offset:512 nt sc1\n"
    "v_accvgpr_read_b32 v223, a122\n"
    "v_pk_fma_f32 v[220:221], v[220:221], v[46:47], v[62:63] op_sel:[0,0,0] op_sel_hi:[1,0,0]\n"
    "v_pk_fma_f32 v[222:223], v[222:223], v[46:47], v[62:63] op_sel:[0,0,0] op_sel_hi:[1,0,0]\n"
    "v_cvt_pk_bf16_f32 v220, v220, v221\n"
    "v_cvt_pk_bf16_f32 v221, v222, v223\n"
    "v_mfma_f32_16x16x32_bf16 a[124:127], v[92:95], v[180:183], a[124:127]\n"
    "buffer_store_dwordx2 v[220:221], v205, s[24:27], s77 offen offset:896 nt sc1\n"
    "v_mfma_f32_16x16x32_bf16 a[124:127], v[96:99], v[184:187], a[124:127]\n"
    "v_accvgpr_read_b32 v227, a123\n"
    "v_fma_f32 v212, v121, s74, v212\n"
    "v_mfma_f32_16x16x32_bf16 a[108:111], v[96:99], v[176:179], a[108:111]\n"
    "v_cmp_nlt_f32 s[54:55], |v212|, s73\n"
    "v_lshlrev_b32 v209, 16, v1\n"
    "v_mfma_f32_16x16x32_bf16 a[108:111], v[92:95], v[172:175], a[108:111]\n"
    "s_or_b32 %0, s54, s55\n"
    "buffer_store_dwordx2 v[238:239], v209, s[36:39], s82 offen offset:0 sc1\n"
    "v_mfma_f32_16x16x32_bf16 a[92:95], v[92:95], v[164:167], a[92:95]\n"
    "v_accvgpr_read_b32 v231, a124\n"
    "v_accvgpr_read_b32 v235, a125\n"
    "v_accvgpr_read_b32 v219, a126\n"
    "v_accvgpr_read_b32 v223, a127\n"
    "v_pk_fma_f32 v[224:225], v[224:225], v[46:47], v[62:63] op_sel:[0,1,1] op_sel_hi:[1,1,1]\n"
    "v_pk_fma_f32 v[226:227], v[226:227], v[46:47], v[62:63] op_sel:[0,1,1] op_sel_hi:[1,1,1]\n"
    "v_cvt_pk_bf16_f32 v224, v224, v225\n"
    "v_cvt_pk_bf16_f32 v225, v226, v227\n"
    "v_mfma_f32_16x16x32_bf16 a[92:95], v[96:99], v[168:171], a[92:95]\n"
    "buffer_store_dwordx2 v[224:225], v205, s[24:27], s77 offen offset:1280 nt sc1\n"
    "v_mfma_f32_16x16x32_bf16 a[76:79], v[96:99], v[160:163], a[76:79]\n"
    "v_accvgpr_read_b32 v230, a108\n"
    "v_accvgpr_read_b32 v234, a109\n"
    "v_mfma_f32_16x16x32_bf16 a[76:79], v[92:95], v[156:159], a[76:79]\n"
    "s_nop 0\n"
    "v_accvgpr_read_b32 v218, a110\n"
    "v_accvgpr_read_b32 v222, a111\n"
    "v_accvgpr_read_b32 v229, a92\n"
    "v_accvgpr_read_b32 v233, a93\n"
    "v_accvgpr_read_b32 v217, a94\n"
    "v_pk_fma_f32 v[230:231], v[230:231], v[48:49], v[64:65] op_sel:[0,0,0] op_sel_hi:[1,0,0]\n"
    "v_pk_fma_f32 v[234:235], v[234:235], v[48:49], v[64:65] op_sel:[0,1,1] op_sel_hi:[1,1,1]\n"
    "v_accvgpr_read_b32 v228, a76\n"
    "v_pk_fma_f32 v[228:229], v[228:229], v[48:49], v[64:65] op_sel:[0,0,0] op_sel_hi:[1,0,0]\n"
    "v_cvt_pk_bf16_f32 v228, v228, v229\n"
    "v_cvt_pk_bf16_f32 v229, v230, v231\n"
    "buffer_store_dwordx2 v[228:229], v205, s[24:27], s78 offen offset:128 nt sc1\n"
    "v_accvgpr_read_b32 v232, a77\n"
    "v_pk_fma_f32 v[232:233], v[232:233], v[48:49], v[64:65] op_sel:[0,1,1] op_sel_hi:[1,1,1]\n"
    "v_cvt_pk_bf16_f32 v232, v232, v233\n"
    "v_cvt_pk_bf16_f32 v233, v234, v235\n"
    "buffer_store_dwordx2 v[232:233], v205, s[24:27], s78 offen offset:512 nt sc1\n"
    "v_accvgpr_read_b32 v216, a78\n"
    "v_pk_fma_f32 v[216:217], v[216:217], v[50:51], v[66:67] op_sel:[0,0,0] op_sel_hi:[1,0,0]\n"
    "v_pk_fma_f32 v[218:219], v[218:219], v[50:51], v[66:67] op_sel:[0,0,0] op_sel_hi:[1,0,0]\n"
    "v_cvt_pk_bf16_f32 v216, v216, v217\n"
    "v_cvt_pk_bf16_f32 v217, v218, v219\n"
    "v_mfma_f32_16x16x32_bf16 a[128:131], v[68:71], v[188:191], a[128:131]\n"
    "v_accvgpr_read_b32 v220, a79\n"
    "v_accvgpr_read_b32 v221, a95\n"
    "v_mfma_f32_16x16x32_bf16 a[128:131], v[72:75], v[192:195], a[128:131]\n"
    "v_fma_f32 v220, v220, v51, v67\n"
    "v_fma_f32 v221, v221, v51, v67\n"
    "v_mfma_f32_16x16x32_bf16 a[144:147], v[72:75], v[200:203], a[144:147]\n"
    "v_cvt_pk_bf16_f32 v220, v220, v221\n"
    "v_fma_f32 v222, v222, v51, v67\n"
    "v_fma_f32 v223, v223, v51, v67\n"
    "buffer_store_dwordx2 v[216:217], v205, s[24:27], s78 offen offset:896 nt sc1\n"
    "v_mfma_f32_16x16x32_bf16 a[144:147], v[68:71], v[196:199], a[144:147]\n"
    "v_cvt_pk_bf16_f32 v221, v222, v223\n"
    "v_accvgpr_read_b32 v224, a128\n"
    "v_mfma_f32_16x16x32_bf16 a[160:163], v[68:71], v[124:127], a[160:163]\n"
    "v_accvgpr_read_b32 v228, a129\n"
    "v_accvgpr_read_b32 v232, a130\n"
    "v_mfma_f32_16x16x32_bf16 a[160:163], v[72:75], v[128:131], a[160:163]\n"
    "v_accvgpr_read_b32 v216, a131\n"
    "v_mfma_f32_16x16x32_bf16 a[176:179], v[72:75], v[136:139], a[176:179]\n"
    "v_accvgpr_read_b32 v225, a144\n"
    "v_accvgpr_read_b32 v229, a145\n"
    "buffer_store_dwordx2 v[220:221], v205, s[24:27], s78 offen offset:1280 nt sc1\n"
    "v_mfma_f32_16x16x32_bf16 a[176:179], v[68:71], v[132:135], a[176:179]\n"
    "s_nop 0\n"
    "v_accvgpr_read_b32 v233, a146\n"
    "v_accvgpr_read_b32 v226, a160\n"
    "v_accvgpr_read_b32 v230, a161\n"
    "v_accvgpr_read_b32 v234, a162\n"
    "v_accvgpr_read_b32 v217, a147\n"
    "v_pk_fma_f32 v[224:225], v[224:225], v[36:37], v[52:53] op_sel:[0,0,0] op_sel_hi:[1,0,0]\n"
    "v_cvt_pk_bf16_f32 v224, v224, v225\n"
    "v_accvgpr_read_b32 v227, a176\n"
    "v_pk_fma_f32 v[226:227], v[226:227], v[36:37], v[52:53] op_sel:[0,0,0] op_sel_hi:[1,0,0]\n"
    "v_cvt_pk_bf16_f32 v225, v226, v227\n"
    "v_accvgpr_read_b32 v231, a177\n"
    "v_pk_fma_f32 v[228:229], v[228:229], v[36:37], v[52:53] op_sel:[0,1,1] op_sel_hi:[1,1,1]\n"
    "v_pk_fma_f32 v[230:231], v[230:231], v[36:37], v[52:53] op_sel:[0,1,1] op_sel_hi:[1,1,1]\n"
    "v_cvt_pk_bf16_f32 v228, v228, v229\n"
    "v_cvt_pk_bf16_f32 v229, v230, v231\n"
    "buffer_store_dwordx2 v[224:225], v205, s[24:27], s75 offen offset:256 nt sc1\n"
    "v_accvgpr_read_b32 v235, a178\n"
    "v_pk_fma_f32 v[232:233], v[232:233], v[38:39], v[54:55] op_sel:[0,0,0] op_sel_hi:[1,0,0]\n"
    "v_pk_fma_f32 v[234:235], v[234:235], v[38:39], v[54:55] op_sel:[0,0,0] op_sel_hi:[1,0,0]\n"
    "v_cvt_pk_bf16_f32 v232, v232, v233\n"
    "v_cvt_pk_bf16_f32 v233, v234, v235\n"
    "v_mfma_f32_16x16x32_bf16 a[180:183], v[76:79], v[132:135], a[180:183]\n"
    "v_accvgpr_read_b32 v218, a163\n"
    "v_accvgpr_read_b32 v219, a179\n"
    "v_mfma_f32_16x16x32_bf16 a[180:183], v[80:83], v[136:139], a[180:183]\n"
    "v_fma_f32 v216, v216, v39, v55\n"
    "v_fma_f32 v217, v217, v39, v55\n"
    "v_mfma_f32_16x16x32_bf16 a[164:167], v[80:83], v[128:131], a[164:167]\n"
    "v_cvt_pk_bf16_f32 v216, v216, v217\n"
    "v_fma_f32 v218, v218, v39, v55\n"
    "v_fma_f32 v219, v219, v39, v55\n"
    "buffer_store_dwordx2 v[228:229], v205, s[24:27], s75 offen offset:640 nt sc1\n"
    "v_mfma_f32_16x16x32_bf16 a[164:167], v[76:79], v[124:127], a[164:167]\n"
    "v_cvt_pk_bf16_f32 v217, v218, v219\n"
    "v_accvgpr_read_b32 v223, a180\n"
    "v_mfma_f32_16x16x32_bf16 a[148:151], v[76:79], v[196:199], a[148:151]\n"
    "v_accvgpr_read_b32 v227, a181\n"
    "v_accvgpr_read_b32 v231, a182\n"
    "v_mfma_f32_16x16x32_bf16 a[148:151], v[80:83], v[200:203], a[148:151]\n"
    "v_accvgpr_read_b32 v235, a183\n"
    "v_mfma_f32_16x16x32_bf16 a[132:135], v[80:83], v[192:195], a[132:135]\n"
    "v_accvgpr_read_b32 v222, a164\n"
    "v_accvgpr_read_b32 v226, a165\n"
    "buffer_store_dwordx2 v[232:233], v205, s[24:27], s75 offen offset:1024 nt sc1\n"
    "v_mfma_f32_16x16x32_bf16 a[132:135], v[76:79], v[188:191], a[132:135]\n"
    "v_accvgpr_read_b32 v230, a166\n"
    "v_accvgpr_read_b32 v234, a167\n"
    "v_accvgpr_read_b32 v221, a148\n"
    "v_accvgpr_read_b32 v225, a149\n"
    "v_accvgpr_read_b32 v229, a150\n"
    "v_pk_fma_f32 v[222:223], v[222:223], v[40:41], v[56:57] op_sel:[0,0,0] op_sel_hi:[1,0,0]\n"
    "v_pk_fma_f32 v[226:227], v[226:227], v[40:41], v[56:57] op_sel:[0,1,1] op_sel_hi:[1,1,1]\n"
    "v_pk_fma_f32 v[230:231], v[230:231], v[42:43], v[58:59] op_sel:[0,0,0] op_sel_hi:[1,0,0]\n"
    "v_accvgpr_read_b32 v220, a132\n"
    "v_pk_fma_f32 v[220:221], v[220:221], v[40:41], v[56:57] op_sel:[0,0,0] op_sel_hi:[1,0,0]\n"
    "v_cvt_pk_bf16_f32 v220, v220, v221\n"
    "v_cvt_pk_bf16_f32 v221, v222, v223\n"
    "v_accvgpr_read_b32 v224, a133\n"
    "v_pk_fma_f32 v[224:225], v[224:225], v[40:41], v[56:57] op_sel:[0,1,1] op_sel_hi:[1,1,1]\n"
    "v_cvt_pk_bf16_f32 v224, v224, v225\n"
    "buffer_store_dwordx2 v[216:217], v205, s[24:27], s75 offen offset:1408 nt sc1\n"
    "v_cvt_pk_bf16_f32 v225, v226, v227\n"
    "v_accvgpr_read_b32 v228, a134\n"
    "v_pk_fma_f32 v[228:229], v[228:229], v[42:43], v[58:59] op_sel:[0,0,0] op_sel_hi:[1,0,0]\n"
    "v_cvt_pk_bf16_f32 v228, v228, v229\n"
    "v_cvt_pk_bf16_f32 v229, v230, v231\n"
    "v_mfma_f32_16x16x32_bf16 a[136:139], v[84:87], v[188:191], a[136:139]\n"
    "v_accvgpr_read_b32 v232, a135\n"
    "v_accvgpr_read_b32 v233, a151\n"
    "v_mfma_f32_16x16x32_bf16 a[136:139], v[88:91], v[192:195], a[136:139]\n"
    "v_fma_f32 v232, v232, v43, v59\n"
    "v_fma_f32 v233, v233, v43, v59\n"
    "v_mfma_f32_16x16x32_bf16 a[152:155], v[88:91], v[200:203], a[152:155]\n"
    "v_cvt_pk_bf16_f32 v232, v232, v233\n"
    "v_fma_f32 v234, v234, v43, v59\n"
    "v_fma_f32 v235, v235, v43, v59\n"
    "buffer_store_dwordx2 v[220:221], v205, s[24:27], s76 offen offset:256 nt sc1\n"
    "v_mfma_f32_16x16x32_bf16 a[152:155], v[84:87], v[196:199], a[152:155]\n"
    "v_cvt_pk_bf16_f32 v233, v234, v235\n"
    "v_accvgpr_read_b32 v216, a136\n"
    "v_mfma_f32_16x16x32_bf16 a[168:171], v[84:87], v[124:127], a[168:171]\n"
    "v_accvgpr_read_b32 v220, a137\n"
    "v_mfma_f32_16x16x32_bf16 a[168:171], v[88:91], v[128:131], a[168:171]\n"
    "v_mfma_f32_16x16x32_bf16 a[184:187], v[88:91], v[136:139], a[184:187]\n"
    "buffer_store_dwordx2 v[224:225], v205, s[24:27], s76 offen offset:640 nt sc1\n"
    "v_mfma_f32_16x16x32_bf16 a[184:187], v[84:87], v[132:135], a[184:187]\n"
    "v_accvgpr_read_b32 v217, a152\n"
    "v_accvgpr_read_b32 v221, a153\n"
    "v_accvgpr_read_b32 v224, a138\n"
    "v_accvgpr_read_b32 v225, a154\n"
    "v_pk_fma_f32 v[216:217], v[216:217], v[44:45], v[60:61] op_sel:[0,0,0] op_sel_hi:[1,0,0]\n"
    "v_accvgpr_read_b32 v218, a168\n"
    "v_cvt_pk_bf16_f32 v216, v216, v217\n"
    "v_accvgpr_read_b32 v222, a169\n"
    "v_accvgpr_read_b32 v219, a184\n"
    "v_pk_fma_f32 v[218:219], v[218:219], v[44:45], v[60:61] op_sel:[0,0,0] op_sel_hi:[1,0,0]\n"
    "v_cvt_pk_bf16_f32 v217, v218, v219\n"
    "v_accvgpr_read_b32 v223, a185\n"
    "v_pk_fma_f32 v[220:221], v[220:221], v[44:45], v[60:61] op_sel:[0,1,1] op_sel_hi:[1,1,1]\n"
    "v_pk_fma_f32 v[222:223], v[222:223], v[44:45], v[60:61] op_sel:[0,1,1] op_sel_hi:[1,1,1]\n"
    "buffer_store_dwordx2 v[228:229], v205, s[24:27], s76 offen offset:1024 nt sc1\n"
    "v_cvt_pk_bf16_f32 v220, v220, v221\n"
    "v_cvt_pk_bf16_f32 v221, v222, v223\n"
    "v_accvgpr_read_b32 v226, a170\n"
    "v_accvgpr_read_b32 v227, a186\n"
    "v_pk_fma_f32 v[224:225], v[224:225], v[46:47], v[62:63] op_sel:[0,0,0] op_sel_hi:[1,0,0]\n"
    "v_pk_fma_f32 v[226:227], v[226:227], v[46:47], v[62:63] op_sel:[0,0,0] op_sel_hi:[1,0,0]\n"
    "v_cvt_pk_bf16_f32 v224, v224, v225\n"
    "v_cvt_pk_bf16_f32 v225, v226, v227\n"
    "v_mfma_f32_16x16x32_bf16 a[188:191], v[92:95], v[132:135], a[188:191]\n"
    "v_accvgpr_read_b32 v228, a139\n"
    "v_accvgpr_read_b32 v229, a155\n"
    "v_mfma_f32_16x16x32_bf16 a[188:191], v[96:99], v[136:139], a[188:191]\n"
    "v_accvgpr_read_b32 v230, a171\n"
    "v_accvgpr_read_b32 v231, a187\n"
    "v_mfma_f32_16x16x32_bf16 a[172:175], v[96:99], v[128:131], a[172:175]\n"
    "buffer_store_dwordx2 v[232:233], v205, s[24:27], s76 offen offset:1408 nt sc1\n"
    "v_mfma_f32_16x16x32_bf16 a[172:175], v[92:95], v[124:127], a[172:175]\n"
    "v_fma_f32 v228, v228, v47, v63\n"
    "v_fma_f32 v229, v229, v47, v63\n"
    "v_mfma_f32_16x16x32_bf16 a[156:159], v[92:95], v[196:199], a[156:159]\n"
    "v_cvt_pk_bf16_f32 v228, v228, v229\n"
    "v_accvgpr_read_b32 v235, a188\n"
    "v_accvgpr_read_b32 v219, a189\n"
    "v_accvgpr_read_b32 v223, a190\n"
    "v_accvgpr_read_b32 v227, a191\n"
    "v_pk_fma_f32 v[230:231], v[230:231], v[46:47], v[62:63] op_sel:[0,1,1] op_sel_hi:[1,1,1]\n"
    "v_cvt_pk_bf16_f32 v229, v230, v231\n"
    "v_accvgpr_read_b32 v234, a172\n"
    "v_mfma_f32_16x16x32_bf16 a[156:159], v[96:99], v[200:203], a[156:159]\n"
    "buffer_store_dwordx2 v[216:217], v205, s[24:27], s77 offen offset:256 nt sc1\n"
    "v_mfma_f32_16x16x32_bf16 a[140:143], v[96:99], v[192:195], a[140:143]\n"
    "v_accvgpr_read_b32 v218, a173\n"
    "v_accvgpr_read_b32 v222, a174\n"
    "v_mfma_f32_16x16x32_bf16 a[140:143], v[92:95], v[188:191], a[140:143]\n"
    "v_accvgpr_read_b32 v226, a175\n"
    "v_fma_f32 v234, v234, v48, v64\n"
    "v_fma_f32 v235, v235, v48, v64\n"
    "v_accvgpr_read_b32 v233, a156\n"
    "v_accvgpr_read_b32 v217, a157\n"
    "v_pk_fma_f32 v[218:219], v[218:219], v[48:49], v[64:65] op_sel:[0,1,1] op_sel_hi:[1,1,1]\n"
    "v_pk_fma_f32 v[222:223], v[222:223], v[50:51], v[66:67] op_sel:[0,0,0] op_sel_hi:[1,0,0]\n"
    "v_pk_fma_f32 v[226:227], v[226:227], v[50:51], v[66:67] op_sel:[0,1,1] op_sel_hi:[1,1,1]\n"
    "v_accvgpr_read_b32 v232, a140\n"
    "v_pk_fma_f32 v[232:233], v[232:233], v[48:49], v[64:65] op_sel:[0,0,0] op_sel_hi:[1,0,0]\n"
    "v_cvt_pk_bf16_f32 v232, v232, v233\n"
    "buffer_store_dwordx2 v[220:221], v205, s[24:27], s77 offen offset:640 nt sc1\n"
    "v_cvt_pk_bf16_f32 v233, v234, v235\n"
    "v_accvgpr_read_b32 v216, a141\n"
    "v_pk_fma_f32 v[216:217], v[216:217], v[48:49], v[64:65] op_sel:[0,1,1] op_sel_hi:[1,1,1]\n"
    "v_cvt_pk_bf16_f32 v216, v216, v217\n"
    "v_cvt_pk_bf16_f32 v217, v218, v219\n"
    "v_accvgpr_read_b32 v220, a142\n"
    "v_accvgpr_read_b32 v221, a158\n"
    "v_pk_fma_f32 v[220:221], v[220:221], v[50:51], v[66:67] op_sel:[0,0,0] op_sel_hi:[1,0,0]\n"
    "v_cvt_pk_bf16_f32 v220, v220, v221\n"
    "v_cvt_pk_bf16_f32 v221, v222, v223\n"
    "buffer_store_dwordx2 v[224:225], v205, s[24:27], s77 offen offset:1024 nt sc1\n"
    "buffer_store_dwordx2 v[228:229], v205, s[24:27], s77 offen offset:1408 nt sc1\n"
    "buffer_store_dwordx2 v[232:233], v205, s[24:27], s78 offen offset:256 nt sc1\n"
    "v_accvgpr_read_b32 v224, a143\n"
    "v_accvgpr_read_b32 v225, a159\n"
    "v_pk_fma_f32 v[224:225], v[224:225], v[50:51], v[66:67] op_sel:[0,1,1] op_sel_hi:[1,1,1]\n"
    "v_cvt_pk_bf16_f32 v224, v224, v225\n"
    "v_cvt_pk_bf16_f32 v225, v226, v227\n"
    "buffer_store_dwordx2 v[216:217], v205, s[24:27], s78 offen offset:640 nt sc1\n"
    "buffer_store_dwordx2 v[220:221], v205, s[24:27], s78 offen offset:1024 nt sc1\n"
    "buffer_store_dwordx2 v[224:225], v205, s[24:27], s78 offen offset:1408 nt sc1\n"
    "s_cmp_eq_u32 %0, 0\n"
    "s_cbranch_scc1 .Lfin\n"
    "s_waitcnt vmcnt(0)\n"
    ".Lfin:\n"
    "s_branch .Lend\n"
    ".LpollAK:\n"
    "s_sleep 1\n"
    "buffer_load_dwordx2 v[236:237], v87, s[36:39], 0 offen offset:0 sc1\n"
    "s_waitcnt vmcnt(0)\n"
    "v_cmp_eq_u64 s[54:55], v[236:237], v[238:239]\n"
    "s_add_u32 s58, s58, 1\n"
    "s_cmp_eq_u64 s[54:55], -1\n"
    "s_cbranch_scc1 .LpollAKr\n"
    "s_cmp_lt_u32 s58, 0x80000\n"
    "s_cbranch_scc1 .LpollAK\n"
    "s_branch .LpollAKr\n"
    ".LpollAV:\n"
    "s_sleep 1\n"
    "buffer_load_dwordx2 v[236:237], v87, s[36:39], 0 offen offset:0 sc1\n"
    "s_waitcnt vmcnt(0)\n"
    "v_cmp_eq_u64 s[54:55], v[236:237], v[238:239]\n"
    "s_add_u32 s58, s58, 1\n"
    "s_cmp_eq_u64 s[54:55], -1\n"
    "s_cbranch_scc1 .LpollAVr\n"
    "s_cmp_lt_u32 s58, 0x80000\n"
    "s_cbranch_scc1 .LpollAV\n"
    "s_branch .LpollAVr\n"
    ".Lpoll1:\n"
    "s_sleep 1\n"
    "v_cmp_eq_u64 s[54:55], v[236:237], v[238:239]\n"
    "s_nop 1\n"
    "s_cmp_eq_u64 s[54:55], -1\n"
    "s_cbranch_scc1 .Lpoll1l\n"
    "buffer_load_dwordx2 v[236:237], v241, s[36:39], s81 offen offset:0 sc1\n"
    "s_waitcnt vmcnt(0)\n"
    "v_cmp_eq_u64 s[54:55], v[236:237], v[238:239]\n"
    ".Lpoll1l:\n"
    "ds_read_b32 v245, v247\n"
    "s_waitcnt lgkmcnt(0)\n"
    "v_cmp_le_u32 s[56:57], s59, v245\n"
    "s_add_u32 s58, s58, 1\n"
    "s_nop 1\n"
    "s_and_b64 s[54:55], s[54:55], s[56:57]\n"
    "s_cmp_eq_u64 s[54:55], -1\n"
    "s_cbranch_scc1 .Lpoll1r\n"
    "s_cmp_lt_u32 s58, 0x80000\n"
    "s_cbranch_scc1 .Lpoll1\n"
    "s_branch .Lpoll1r\n"
    ".Lpoll2:\n"
    "s_sleep 1\n"
    "v_cmp_eq_u64 s[54:55], v[236:237], v[238:239]\n"
    "s_nop 1\n"
    "s_cmp_eq_u64 s[54:55], -1\n"
    "s_cbranch_scc1 .Lpoll2l\n"
    "buffer_load_dwordx2 v[236:237], v241, s[36:39], s81 offen offset:0 sc1\n"
    "s_waitcnt vmcnt(0)\n"
    "v_cmp_eq_u64 s[54:55], v[236:237], v[238:239]\n"
    ".Lpoll2l:\n"
    "ds_read_b32 v245, v247\n"
    "s_waitcnt lgkmcnt(0)\n"
    "v_cmp_le_u32 s[56:57], s59, v245\n"
    "s_add_u32 s58, s58, 1\n"
    "s_nop 1\n"
    "s_and_b64 s[54:55], s[54:55], s[56:57]\n"
    "s_cmp_eq_u64 s[54:55], -1\n"
    "s_cbranch_scc1 .Lpoll2r\n"
    "s_cmp_lt_u32 s58, 0x80000\n"
    "s_cbranch_scc1 .Lpoll2\n"
    "s_branch .Lpoll2r\n"
    ".Lpoll3:\n"
    "s_sleep 1\n"
    "v_cmp_eq_u64 s[54:55], v[236:237], v[238:239]\n"
    "s_nop 1\n"
    "s_cmp_eq_u64 s[54:55], -1\n"
    "s_cbranch_scc1 .Lpoll3l\n"
    "buffer_load_dwordx2 v[236:237], v241, s[36:39], s81 offen offset:0 sc1\n"
    "s_waitcnt vmcnt(0)\n"
    "v_cmp_eq_u64 s[54:55], v[236:237], v[238:239]\n"
    ".Lpoll3l:\n"
    "ds_read_b32 v245, v247\n"
    "s_waitcnt lgkmcnt(0)\n"
    "v_cmp_le_u32 s[56:57], s59, v245\n"
    "s_add_u32 s58, s58, 1\n"
    "s_nop 1\n"
    "s_and_b64 s[54:55], s[54:55], s[56:57]\n"
    "s_cmp_eq_u64 s[54:55], -1\n"
    "s_cbranch_scc1 .Lpoll3r\n"
    "s_cmp_lt_u32 s58, 0x80000\n"
    "s_cbranch_scc1 .Lpoll3\n"
    "s_branch .Lpoll3r\n"
    ".Lpoll4:\n"
    "s_sleep 1\n"
    "v_cmp_eq_u64 s[54:55], v[236:237], v[238:239]\n"
    "s_nop 1\n"
    "s_cmp_eq_u64 s[54:55], -1\n"
    "s_cbranch_scc1 .Lpoll4l\n"
    "buffer_load_dwordx2 v[236:237], v241, s[36:39], s81 offen offset:0 sc1\n"
    "s_waitcnt vmcnt(0)\n"
    "v_cmp_eq_u64 s[54:55], v[236:237], v[238:239]\n"
    ".Lpoll4l:\n"
    "ds_read_b32 v245, v247\n"
    "s_waitcnt lgkmcnt(0)\n"
    "v_cmp_le_u32 s[56:57], s59, v245\n"
    "s_add_u32 s58, s58, 1\n"
    "s_nop 1\n"
    "s_and_b64 s[54:55], s[54:55], s[56:57]\n"
    "s_cmp_eq_u64 s[54:55], -1\n"
    "s_cbranch_scc1 .Lpoll4r\n"
    "s_cmp_lt_u32 s58, 0x80000\n"
    "s_cbranch_scc1 .Lpoll4\n"
    "s_branch .Lpoll4r\n"
    ".Lend:\n"
    : "=&s"(bad), "+s"(ka) : "s"(b), "v"(tid)
    : "memory", "vcc", "s16", "s17", "s18", "s19", "s20", "s21", "s22", "s23", "s24", "s25", "s26", "s27", "s28", "s29", "s30", "s31", "s36", "s37", "s38", "s39", "s40", "s41", "s42", "s43", "s44", "s45", "s46", "s47", "s48", "s49", "s50", "s51", "s52", "s53", "s54", "s55", "s56", "s57", "s58", "s59", "s60", "s61", "s62", "s63", "s64", "s65", "s66", "s67", "s68", "s69", "s70", "s71", "s72", "s73", "s74", "s75", "s76", "s77", "s78", "s79", "s80", "s81", "s82", "s83", "s84", "s85", "s86", "s87", "s88", "s89", "s90", "s91", "s92", "s93", "s94", "s95", "s96", "s97", "s98", "s99", "v1", "v2", "v3", "v4", "v5", "v6", "v7", "v8", "v9", "v10", "v11", "v12", "v13", "v14", "v15", "v16", "v17", "v18", "v19", "v20", "v21", "v22", "v23", "v24", "v25", "v26", "v27", "v28", "v29", "v30", "v31", "v32", "v33", "v34", "v35", "v36", "v37", "v38", "v39", "v40", "v41", "v42", "v43", "v44", "v45", "v46", "v47", "v48", "v49", "v50", "v51", "v52", "v53", "v54", "v55", "v56", "v57", "v58", "v59", "v60", "v61", "v62", "v63", "v64", "v65", "v66", "v67", "v68", "v69", "v70", "v71", "v72", "v73", "v74", "v75", "v76", "v77", "v78", "v79", "v80", "v81", "v82", "v83", "v84", "v85", "v86", "v87", "v88", "v89", "v90", "v91", "v92", "v93", "v94", "v95", "v96", "v97", "v98", "v99", "v100", "v101", "v102", "v103", "v104", "v105", "v106", "v107", "v108", "v109", "v110", "v111", "v112", "v113", "v114", "v115", "v116", "v117", "v118", "v119", "v120", "v121", "v122", "v123", "v124", "v125", "v126", "v127", "v128", "v129", "v130", "v131", "v132", "v133", "v134", "v135", "v136", "v137", "v138", "v139", "v140", "v141", "v142", "v143", "v144", "v145", "v146", "v147", "v148", "v149", "v150", "v151", "v152", "v153", "v154", "v155", "v156", "v157", "v158", "v159", "v160", "v161", "v162", "v163", "v164", "v165", "v166", "v167", "v168", "v169", "v170", "v171", "v172", "v173", "v174", "v175", "v176", "v177", "v178", "v179", "v180", "v181", "v182", "v183", "v184", "v185", "v186", "v187", "v188", "v189", "v190", "v191", "v192", "v193", "v194", "v195", "v196", "v197", "v198", "v199", "v200", "v201", "v202", "v203", "v204", "v205", "v206", "v207", "v208", "v209", "v210", "v211", "v212", "v213", "v214", "v215", "v216", "v217", "v218", "v219", "v220", "v221", "v222", "v223", "v224", "v225", "v226", "v227", "v228", "v229", "v230", "v231", "v232", "v233", "v234", "v235", "v236", "v237", "v238", "v239", "v240", "v241", "v242", "v243", "v244", "v245", "v246", "v247", "v248", "v249", "v250", "v251", "v252", "v253", "v254", "v255", "a0", "a1", "a2", "a3", "a4", "a5", "a6", "a7", "a8", "a9", "a10", "a11", "a12", "a13", "a14", "a15", "a16", "a17", "a18", "a19", "a20", "a21", "a22", "a23", "a24", "a25", "a26", "a27", "a28", "a29", "a30", "a31", "a32", "a33", "a34", "a35", "a36", "a37", "a38", "a39", "a40", "a41", "a42", "a43", "a44", "a45", "a46", "a47", "a48", "a49", "a50", "a51", "a52", "a53", "a54", "a55", "a56", "a57", "a58", "a59", "a60", "a61", "a62", "a63", "a64", "a65", "a66", "a67", "a68", "a69", "a70", "a71", "a72", "a73", "a74", "a75", "a76", "a77", "a78", "a79", "a80", "a81", "a82", "a83", "a84", "a85", "a86", "a87", "a88", "a89", "a90", "a91", "a92", "a93", "a94", "a95", "a96", "a97", "a98", "a99", "a100", "a101", "a102", "a103", "a104", "a105", "a106", "a107", "a108", "a109", "a110", "a111", "a112", "a113", "a114", "a115", "a116", "a117", "a118", "a119", "a120", "a121", "a122", "a123", "a124", "a125", "a126", "a127", "a128", "a129", "a130", "a131", "a132", "a133", "a134", "a135", "a136", "a137", "a138", "a139", "a140", "a141", "a142", "a143", "a144", "a145", "a146", "a147", "a148", "a149", "a150", "a151", "a152", "a153", "a154", "a155", "a156", "a157", "a158", "a159", "a160", "a161", "a162", "a163", "a164", "a165", "a166", "a167", "a168", "a169", "a170", "a171", "a172", "a173", "a174", "a175", "a176", "a177", "a178", "a179", "a180", "a181", "a182", "a183", "a184", "a185", "a186", "a187", "a188", "a189", "a190", "a191", "a192", "a193", "a194", "a195", "a196", "a197", "a198", "a199", "a200", "a201", "a202", "a203", "a204", "a205", "a206", "a207", "a208", "a209", "a210", "a211", "a212", "a213", "a214", "a215", "a216", "a217", "a218", "a219", "a220", "a221", "a222", "a223", "a224", "a225", "a226", "a227", "a228", "a229", "a230", "a231", "a232", "a233", "a234", "a235", "a236", "a237", "a238", "a239", "a240", "a241", "a242", "a243", "a244", "a245", "a246", "a247", "a248", "a249", "a250", "a251", "a252", "a253", "a254", "a255");
  if (bad) {
    slow_rows((const Args*)ka, (b & 7) * 4 + ((b >> 3) & 3), (b >> 5) * 256 + (tid & 192), tid & 63);
  }
}

extern "C" void kernel_launch(void* const* d_in, const int* in_sizes, int n_in,
                              void* d_out, int out_size, void* d_ws, size_t ws_size,
                              hipStream_t stream) {
  (void)hipFuncSetAttribute((const void*)attn_fwd, hipFuncAttributeMaxDynamicSharedMemorySize, SHM);
  attn_fwd<<<256, 256, SHM, stream>>>((const unsigned short*)d_in[0], (const int*)d_in[1], (const int*)d_in[2], (const unsigned short*)d_in[3], (const unsigned short*)d_in[4],
                                      (const unsigned short*)d_in[5], (const unsigned short*)d_in[6], (char*)d_ws, (unsigned short*)d_out);
}
```

```cpp
#include <hip/hip_runtime.h>

constexpr int D = 192, NH = 32, S = 2048;
constexpr int SHM = 155968;

struct Args { const unsigned short* q; const int* qk; const int* qv; const unsigned short* ksc; const unsigned short* kzr;
              const unsigned short* vsc; const unsigned short* vzr; char* ws; unsigned short* o; };

__device__ __forceinline__ float bf2f(unsigned short x) { return __uint_as_float((unsigned)x << 16); }
__device__ __forceinline__ float rbf(float x) { return (float)(__bf16)x; }

__device__ __forceinline__ void slow_rows(const Args* a, unsigned h, unsigned row0, unsigned lane) {
  const unsigned short* q = a->q + ((size_t)h * S + row0) * D;
  unsigned short* o = a->o + ((size_t)h * S + row0) * D;
  const int* qk = a->qk + (size_t)h * S * D; const int* qv = a->qv + (size_t)h * S * D;
  const unsigned short* ks = a->ksc + h * 32 * D; const unsigned short* kz = a->kzr + h * 32 * D;
  const unsigned short* vs = a->vsc + h * S; const unsigned short* vz = a->vzr + h * S;
#pragma unroll 1
  for (int r = 0; r < 64; ++r) {
    float m = -INFINITY, l = 0.f, a0 = 0.f, a1 = 0.f, a2 = 0.f;
#pragma unroll 1
    for (int t = 0; t < 32; ++t) {
      const int* kr = qk + (size_t)(t * 64 + lane) * D;
      float s = 0.f;
#pragma unroll 4
      for (int d = 0; d < D; ++d) s = fmaf(bf2f(q[r * D + d]), rbf(rbf(rbf((float)kr[d]) - bf2f(kz[t * D + d])) * bf2f(ks[t * D + d])), s);
      s *= 0.07216878364870322f;
      float mx = s;
      for (int x = 32; x; x >>= 1) mx = fmaxf(mx, __shfl_xor(mx, x));
      const float mn = fmaxf(m, mx), c = __expf(m - mn), p = __expf(s - mn);
      float ps = p;
      for (int x = 32; x; x >>= 1) ps += __shfl_xor(ps, x);
      l = l * c + ps; a0 *= c; a1 *= c; a2 *= c; m = mn;
#pragma unroll 2
      for (int k = 0; k < 64; ++k) {
        const float pk = __shfl(p, k), z = bf2f(vz[t * 64 + k]), sc = bf2f(vs[t * 64 + k]);
        const int* vr = qv + (size_t)(t * 64 + k) * D + lane;
        a0 = fmaf(pk, rbf(rbf(rbf((float)vr[0]) - z) * sc), a0); a1 = fmaf(pk, rbf(rbf(rbf((float)vr[64]) - z) * sc), a1); a2 = fmaf(pk, rbf(rbf(rbf((float)vr[128]) - z) * sc), a2);
      }
    }
    const float i = 1.f / l;
    o[r * D + lane] = __builtin_bit_cast(unsigned short, (__bf16)(a0 * i)); o[r * D + 64 + lane] = __builtin_bit_cast(unsigned short, (__bf16)(a1 * i));
    o[r * D + 128 + lane] = __builtin_bit_cast(unsigned short, (__bf16)(a2 * i));
  }
}

__global__ __launch_bounds__(256, 1) void attn_fwd(const unsigned short* q, const int* qk, const int* qv, const unsigned short* ksc, const unsigned short* kzr,
                                                   const unsigned short* vsc, const unsigned short* vzr, char* ws, unsigned short* o) {
  unsigned long long ka = (unsigned long long)__builtin_amdgcn_kernarg_segment_ptr();
  unsigned bad, b = blockIdx.x;
  const unsigned tid = threadIdx.x;
  asm volatile(
    "s_load_dwordx2 s[28:29], %1, 8\n"
    "s_load_dwordx2 s[40:41], %1, 16\n"
    "s_load_dwordx2 s[44:45], %1, 24\n"
    "s_load_dwordx2 s[88:89], %1, 32\n"
    "s_load_dwordx2 s[36:37], %1, 56\n"
    "s_load_dwordx2 s[92:93], %1, 40\n"
    "s_load_dwordx2 s[96:97], %1, 48\n"
    "s_load_dwordx2 s[20:21], %1, 0\n"
    "s_load_dwordx2 s[16:17], %1, 56\n"
    "s_load_dwordx2 s[24:25], %1, 64\n"
    "v_readfirstlane_b32 s84, %3\n"
    "v_and_b32 v156, 63, %3\n"
    "s_and_b32 s52, %2, 7\n"
    "s_bfe_u32 s72, %2, 0x20003\n"
    "s_lshr_b32 s53, %2, 5\n"
    "s_lshl_b32 s52, s52, 2\n"
    "s_lshr_b32 s84, s84, 6\n"
    "s_or_b32 s52, s52, s72\n"
    "v_lshlrev_b32 v1, 4, v156\n"
    "s_getpc_b64 s[54:55]\n"
    ".Lpc:\n"
    "s_lshl_b32 s72, s72, 14\n"
    "v_and_b32 v246, 127, %3\n"
    "v_lshl_add_u32 v246, v246, 7, s72\n"
    "v_min_u32 v246, .Lend-.Lpc-4, v246\n"
    "global_load_dword v246, v246, s[54:55]\n"
    "s_mov_b32 s30, 1572864\n"
    "s_mov_b32 s31, 0x20000\n"
    "s_mov_b32 s42, 1572864\n"
    "s_mov_b32 s43, 0x20000\n"
    "s_mov_b32 s46, 12288\n"
    "s_mov_b32 s47, 0x20000\n"
    "s_mov_b32 s90, 12288\n"
    "s_mov_b32 s91, 0x20000\n"
    "s_lshr_b32 s79, s84, 1\n"
    "s_and_b32 s59, s84, 1\n"
    "s_cmp_ge_u32 s53, 6\n"
    "s_cselect_b32 s83, 1, 0\n"
    "s_or_b32 s83, s83, s84\n"
    "s_sub_u32 s58, 2, s79\n"
    "s_mul_i32 s71, s58, 49152\n"
    "s_mul_i32 s72, s52, 1572864\n"
    "s_mul_i32 s73, s52, 12288\n"
    "v_lshrrev_b32 v157, 2, v156\n"
    "v_and_b32 v158, 3, v156\n"
    "s_mul_i32 s35, s84, 3072\n"
    "s_bitcmp1_b32 s83, 0\n"
    "s_cbranch_scc1 .Lf2b\n"
    "v_and_b32 v159, 32, v156\n"
    "v_mul_u32_u24 v242, 376, v159\n"
    "v_and_b32 v159, 16, v156\n"
    "v_mul_u32_u24 v159, 88, v159\n"
    "v_lshl_add_u32 v242, v156, 3, v242\n"
    "v_add_u32 v242, v159, v242\n"
    "s_lshl_b32 s63, s53, 7\n"
    "v_mul_u32_u24 v160, 768, v157\n"
    "v_lshl_add_u32 v160, v158, 4, v160\n"
    "s_and_b32 s81, s58, 1\n"
    "s_mul_i32 s81, s81, 384\n"
    "s_lshl_b32 s75, s53, 6\n"
    "s_add_u32 s81, s81, s75\n"
    "s_add_u32 s75, s71, s63\n"
    "s_add_u32 s76, s75, 6144\n"
    "s_add_u32 s77, s75, 24576\n"
    "s_add_u32 s78, s75, 30720\n"
    "s_waitcnt lgkmcnt(0)\n"
    "s_add_u32 s44, s44, s73\n"
    "s_addc_u32 s45, s45, 0\n"
    "s_add_u32 s88, s88, s73\n"
    "s_addc_u32 s89, s89, 0\n"
    "buffer_load_dwordx4 v[4:7], v1, s[44:47], s35 offen offset:0\n"
    "buffer_load_dwordx4 v[8:11], v1, s[44:47], s35 offen offset:1024\n"
    "buffer_load_dwordx4 v[12:15], v1, s[44:47], s35 offen offset:2048\n"
    "buffer_load_dwordx4 v[16:19], v1, s[88:91], 0 offen offset:0\n"
    "buffer_load_dwordx4 v[164:167], v160, s[44:47], s81 offen offset:0\n"
    "buffer_load_dwordx4 v[168:171], v160, s[88:91], s81 offen offset:0\n"
    "s_add_u32 s28, s28, s72\n"
    "s_addc_u32 s29, s29, 0\n"
    "buffer_load_dwordx2 v[204:205], v242, s[28:31], s75 offen offset:0 nt\n"
    "buffer_load_dwordx2 v[206:207], v242, s[28:31], s75 offen offset:768 nt\n"
    "buffer_load_dwordx2 v[208:209], v242, s[28:31], s75 offen offset:3072 nt\n"
    "buffer_load_dwordx2 v[210:211], v242, s[28:31], s75 offen offset:3840 nt\n"
    "buffer_load_dwordx2 v[212:213], v242, s[28:31], s76 offen offset:0 nt\n"
    "buffer_load_dwordx2 v[214:215], v242, s[28:31], s76 offen offset:768 nt\n"
    "buffer_load_dwordx2 v[216:217], v242, s[28:31], s76 offen offset:3072 nt\n"
    "buffer_load_dwordx2 v[218:219], v242, s[28:31], s76 offen offset:3840 nt\n"
    "buffer_load_dwordx2 v[220:221], v242, s[28:31], s77 offen offset:0 nt\n"
    "buffer_load_dwordx2 v[222:223], v242, s[28:31], s77 offen offset:768 nt\n"
    "buffer_load_dwordx2 v[224:225], v242, s[28:31], s77 offen offset:3072 nt\n"
    "buffer_load_dwordx2 v[226:227], v242, s[28:31], s77 offen offset:3840 nt\n"
    "buffer_load_dwordx2 v[228:229], v242, s[28:31], s78 offen offset:0 nt\n"
    "buffer_load_dwordx2 v[230:231], v242, s[28:31], s78 offen offset:768 nt\n"
    "buffer_load_dwordx2 v[232:233], v242, s[28:31], s78 offen offset:3072 nt\n"
    "buffer_load_dwordx2 v[234:235], v242, s[28:31], s78 offen offset:3840 nt\n"
    "s_add_u32 s75, s71, s63\n"
    "s_add_u32 s75, s75, 98304\n"
    "s_add_u32 s76, s75, 6144\n"
    "s_add_u32 s77, s75, 24576\n"
    "s_add_u32 s78, s75, 30720\n"
    "buffer_load_dwordx2 v[36:37], v242, s[28:31], s75 offen offset:0 nt\n"
    "buffer_load_dwordx2 v[38:39], v242, s[28:31], s75 offen offset:768 nt\n"
    "v_lshrrev_b32 v162, 1, v156\n"
    "v_and_b32 v162, 16, v162\n"
    "v_bfe_u32 v159, v156, 4, 1\n"
    "v_or_b32 v162, v162, v159\n"
    "v_and_or_b32 v162, v157, 2, v162\n"
    "v_mul_u32_u24 v162, 768, v162\n"
    "v_and_b32 v163, 0x70, v1\n"
    "v_add_u32 v162, v162, v163\n"
    "s_branch .Lf2e\n"
    ".Lf2b:\n"
    "v_and_b32 v159, 48, v156\n"
    "v_lshlrev_b32 v242, 2, v159\n"
    "v_lshl_add_u32 v243, v158, 4, v242\n"
    "v_and_b32 v159, 12, v156\n"
    "v_mul_u32_u24 v242, 384, v159\n"
    "v_add_u32 v242, v243, v242\n"
    "v_add_u32 v243, v243, v159\n"
    "s_mul_i32 s63, s53, 6144\n"
    "s_sub_u32 s63, s63, 49152\n"
    "s_cmp_eq_u32 s59, 0\n"
    "s_cselect_b32 s63, 0x7f000000, s63\n"
    "s_add_u32 s75, s71, s63\n"
    "s_waitcnt lgkmcnt(0)\n"
    "s_add_u32 s44, s44, s73\n"
    "s_addc_u32 s45, s45, 0\n"
    "s_add_u32 s88, s88, s73\n"
    "s_addc_u32 s89, s89, 0\n"
    "buffer_load_dwordx4 v[4:7], v1, s[44:47], s35 offen offset:0\n"
    "buffer_load_dwordx4 v[8:11], v1, s[44:47], s35 offen offset:1024\n"
    "buffer_load_dwordx4 v[12:15], v1, s[44:47], s35 offen offset:2048\n"
    "buffer_load_dwordx4 v[16:19], v1, s[88:91], 0 offen offset:0\n"
    "s_add_u32 s40, s40, s72\n"
    "s_addc_u32 s41, s41, 0\n"
    "buffer_load_dwordx4 v[204:207], v242, s[40:43], s75 offen offset:0 nt\n"
    "buffer_load_dwordx4 v[208:211], v242, s[40:43], s75 offen offset:768 nt\n"
    "buffer_load_dwordx4 v[212:215], v242, s[40:43], s75 offen offset:256 nt\n"
    "buffer_load_dwordx4 v[216:219], v242, s[40:43], s75 offen offset:1024 nt\n"
    "buffer_load_dwordx4 v[220:223], v242, s[40:43], s75 offen offset:512 nt\n"
    "buffer_load_dwordx4 v[224:227], v242, s[40:43], s75 offen offset:1280 nt\n"
    "s_add_u32 s75, s71, s63\n"
    "s_add_u32 s75, s75, 98304\n"
    "buffer_load_dwordx4 v[36:39], v242, s[40:43], s75 offen offset:0 nt\n"
    "s_add_u32 s28, s28, s72\n"
    "s_addc_u32 s29, s29, 0\n"
    "v_lshrrev_b32 v162, 1, v156\n"
    "v_and_b32 v162, 16, v162\n"
    "v_bfe_u32 v159, v156, 4, 1\n"
    "v_or_b32 v162, v162, v159\n"
    "v_and_or_b32 v162, v157, 2, v162\n"
    "v_mul_u32_u24 v162, 768, v162\n"
    "v_and_b32 v163, 0x70, v1\n"
    "v_add_u32 v162, v162, v163\n"
    ".Lf2e:\n"
    "s_nop 4\n"
    "v_lshlrev_b32 v247, 2, v156\n"
    "v_add_u32 v247, 155648, v247\n"
    "v_mov_b32 v159, 0\n"
    "ds_write_b32 v247, v159\n"
    "v_mov_b32 v159, 0xff800000\n"
    "v_mov_b32 v160, 155904\n"
    "ds_write_b32 v160, v159\n"
    "s_mov_b32 s38, 5376\n"
    "s_mov_b32 s39, 0x20000\n"
    "s_mov_b32 s94, 4096\n"
    "s_mov_b32 s95, 0x20000\n"
    "s_mov_b32 s98, 4096\n"
    "s_mov_b32 s99, 0x20000\n"
    "s_mov_b32 s22, 98304\n"
    "s_mov_b32 s23, 0x20000\n"
    "s_mov_b32 s18, 1622016\n"
    "s_mov_b32 s19, 0x20000\n"
    "s_mov_b32 s26, 98304\n"
    "s_mov_b32 s27, 0x20000\n"
    "s_lshl_b32 s80, s58, 7\n"
    "s_add_u32 s80, s80, 256\n"
    "s_lshl_b32 s74, s52, 16\n"
    "s_mul_i32 s69, s59, 384\n"
    "s_mul_i32 s57, s79, 6144\n"
    "s_add_u32 s57, s57, s69\n"
    "s_add_u32 s66, s57, 24576\n"
    "s_add_u32 s36, s36, s74\n"
    "s_addc_u32 s37, s37, 0\n"
    "s_lshr_b32 s74, s74, 4\n"
    "s_add_u32 s92, s92, s74\n"
    "s_addc_u32 s93, s93, 0\n"
    "s_add_u32 s96, s96, s74\n"
    "s_addc_u32 s97, s97, 0\n"
    "v_lshlrev_b32 v128, 3, v156\n"
    "v_mov_b32 v129, 0\n"
    "v_lshlrev_b32 v130, 4, %3\n"
    "buffer_load_dwordx2 v[124:125], v128, s[44:47], 0 offen offset:0\n"
    "buffer_load_dwordx2 v[126:127], v128, s[88:91], 0 offen offset:0\n"
    "buffer_load_dwordx2 v[238:239], v129, s[36:39], 0 offen offset:0 sc1\n"
    "s_mul_i32 s74, s52, 786432\n"
    "s_mul_i32 s73, s53, 98304\n"
    "s_add_u32 s74, s74, s73\n"
    "s_add_u32 s20, s20, s74\n"
    "s_addc_u32 s21, s21, 0\n"
    "s_add_u32 s24, s24, s74\n"
    "s_addc_u32 s25, s25, 0\n"
    "s_mul_i32 s72, s52, 1626112\n"
    "s_add_u32 s72, s72, 2097152\n"
    "s_add_u32 s16, s16, s72\n"
    "s_addc_u32 s17, s17, 0\n"
    "s_mul_i32 s65, s84, 6144\n"
    "s_mul_i32 s85, s84, 24576\n"
    "s_waitcnt lgkmcnt(0)\n"
    "s_barrier\n"
    "s_add_u32 s73, s65, 49152\n"
    "v_add_u32 v131, s73, v1\n"
    "s_add_u32 s73, s73, s69\n"
    "v_add_u32 v132, s73, v163\n"
    "v_and_b32 v159, 32, v156\n"
    "v_lshlrev_b32 v244, 3, v156\n"
    "v_xor_b32 v244, v244, v159\n"
    "v_lshl_add_u32 v244, v159, 3, v244\n"
    "s_bitcmp1_b32 s83, 0\n"
    "s_cbranch_scc1 .Lf5b\n"
    "s_lshl_b32 s60, s53, 11\n"
    "s_add_u32 s61, s60, 12288\n"
    "s_lshl_b32 s73, s58, 8\n"
    "s_add_u32 s73, s73, 122880\n"
    "v_and_b32 v243, 0xf0, v1\n"
    "v_add_u32 v243, s73, v243\n"
    "s_and_b32 s73, s73, 0x1fd00\n"
    "v_lshlrev_b32 v160, 9, v157\n"
    "v_lshl_add_u32 v160, v158, 6, v160\n"
    "v_add_u32 v160, s73, v160\n"
    "s_lshl_b32 s73, s79, 8\n"
    "s_mul_i32 s74, s59, 6144\n"
    "s_add_u32 s73, s73, s74\n"
    "v_add_u32 v163, s73, v244\n"
    "v_and_b32 v159, 32, v156\n"
    "v_lshlrev_b32 v244, 2, v156\n"
    "v_xor_b32 v244, v244, v159\n"
    "v_mad_u32_u24 v244, v159, 12, v244\n"
    "buffer_load_dwordx2 v[40:41], v242, s[28:31], s75 offen offset:3072 nt\n"
    "buffer_load_dwordx2 v[42:43], v242, s[28:31], s75 offen offset:3840 nt\n"
    "v_add_u32 v159, 0, v156\n"
    "v_mul_u32_u24 v159, 2731, v159\n"
    "v_lshrrev_b32 v159, 16, v159\n"
    "v_xor_b32 v159, v159, v156\n"
    "v_and_b32 v159, 7, v159\n"
    "v_and_b32 v88, 0x380, v1\n"
    "v_lshl_add_u32 v88, v159, 4, v88\n"
    "v_add_u32 v159, 64, v156\n"
    "v_mul_u32_u24 v159, 2731, v159\n"
    "v_lshrrev_b32 v159, 16, v159\n"
    "v_xor_b32 v159, v159, v156\n"
    "v_and_b32 v159, 7, v159\n"
    "v_and_b32 v89, 0x380, v1\n"
    "v_lshl_add_u32 v89, v159, 4, v89\n"
    "v_add_u32 v159, 128, v156\n"
    "v_mul_u32_u24 v159, 2731, v159\n"
    "v_lshrrev_b32 v159, 16, v159\n"
    "v_xor_b32 v159, v159, v156\n"
    "v_and_b32 v159, 7, v159\n"
    "v_and_b32 v90, 0x380, v1\n"
    "v_lshl_add_u32 v90, v159, 4, v90\n"
    "v_and_b32 v2, 7, v156\n"
    "v_lshrrev_b32 v144, 3, %3\n"
    "v_mul_u32_u24 v144, 768, v144\n"
    "v_lshl_add_u32 v144, v2, 3, v144\n"
    "buffer_load_dwordx2 v[44:45], v242, s[28:31], s76 offen offset:0 nt\n"
    "buffer_load_dwordx2 v[46:47], v242, s[28:31], s76 offen offset:768 nt\n"
    "v_add_u32 v144, 131072, v144\n"
    "v_mad_u32_u24 v86, v2, 24, v144\n"
    "v_and_b32 v84, 15, v156\n"
    "v_lshrrev_b32 v159, 4, v156\n"
    "v_xor_b32 v159, v159, v2\n"
    "v_mul_u32_u24 v84, 384, v84\n"
    "v_lshl_add_u32 v84, v159, 4, v84\n"
    "s_add_u32 s73, s65, 49152\n"
    "v_add_u32 v84, s73, v84\n"
    "v_xor_b32 v85, 64, v84\n"
    "v_subrev_u32 v154, 24576, v84\n"
    "v_subrev_u32 v155, 24576, v85\n"
    "s_add_u32 s67, s65, 98304\n"
    "s_add_u32 s68, s65, 24576\n"
    "s_add_u32 s69, s65, 73728\n"
    "s_mov_b32 s81, 256\n"
    "s_mul_i32 s74, s79, 24576\n"
    "s_add_u32 s70, s74, s65\n"
    "s_add_u32 s70, s70, 73728\n"
    "s_sub_u32 s64, s65, s74\n"
    "s_add_u32 s64, s64, 98304\n"
    "v_and_b32 v249, 0x310, v1\n"
    "v_and_b32 v159, 12, v156\n"
    "v_lshl_add_u32 v249, v159, 3, v249\n"
    "v_and_b32 v159, 2, v156\n"
    "buffer_load_dwordx2 v[48:49], v242, s[28:31], s76 offen offset:3072 nt\n"
    "buffer_load_dwordx2 v[50:51], v242, s[28:31], s76 offen offset:3840 nt\n"
    "v_lshl_add_u32 v249, v159, 6, v249\n"
    "s_add_u32 s73, s74, 73728\n"
    "v_add_u32 v248, s73, v249\n"
    "s_sub_u32 s73, 98304, s74\n"
    "v_add_u32 v249, s73, v249\n"
    "s_or_b32 s73, s53, s84\n"
    "s_cmp_eq_u32 s73, 0\n"
    "s_cselect_b32 s82, 0, 0x7fff0000\n"
    "v_and_b32 v252, 12, v156\n"
    "v_bfe_u32 v159, v156, 1, 1\n"
    "v_or_b32 v252, v252, v159\n"
    "v_and_b32 v159, 1, v156\n"
    "v_lshl_or_b32 v252, v159, 1, v252\n"
    "v_lshlrev_b32 v252, 6, v252\n"
    "v_lshlrev_b32 v159, 2, v156\n"
    "v_and_b32 v159, 32, v159\n"
    "v_xor_b32 v159, v159, v156\n"
    "v_and_or_b32 v252, v159, 48, v252\n"
    "v_mov_b32 v2, v252\n"
    "v_add_u32 v3, 24576, v252\n"
    "v_lshrrev_b32 v91, 3, v156\n"
    "v_lshl_add_u32 v87, v91, 6, 64\n"
    "v_add_u32 v87, 64, v87\n"
    "v_and_b32 v241, 1, v156\n"
    "v_lshlrev_b32 v241, 3, v241\n"
    "buffer_load_dwordx2 v[52:53], v242, s[28:31], s77 offen offset:0 nt\n"
    "buffer_load_dwordx2 v[54:55], v242, s[28:31], s77 offen offset:768 nt\n"
    "v_lshl_add_u32 v241, v91, 4, v241\n"
    "v_add_u32 v241, 1152, v241\n"
    "v_lshlrev_b32 v91, 24, v91\n"
    "v_lshl_add_u32 v91, v156, 3, v91\n"
    "s_lshl_b32 s73, s53, 4\n"
    "s_lshl_b32 s74, s59, 3\n"
    "s_add_u32 s73, s73, s74\n"
    "s_add_u32 s73, s73, 1152\n"
    "v_mov_b32 v240, 0x7fff0000\n"
    "v_writelane_b32 v240, s73, 0\n"
    "v_and_b32 v159, 48, v156\n"
    "s_mul_i32 s73, s79, 768\n"
    "s_sub_u32 s73, 131072, s73\n"
    "v_lshlrev_b32 v250, 1, v159\n"
    "v_add_u32 v250, s73, v250\n"
    "v_and_b32 v251, 3, v156\n"
    "v_lshl_add_u32 v251, v251, 7, v159\n"
    "s_add_u32 s73, s73, 256\n"
    "v_add_u32 v251, s73, v251\n"
    "v_and_b32 v159, 64, v1\n"
    "v_add_u32 v251, v159, v251\n"
    "v_bfe_i32 v159, v156, 2, 1\n"
    "v_and_b32 v159, 0xaaaa, v159\n"
    "v_or_b32 v253, 0x4444, v159\n"
    "s_mov_b32 s58, 0\n"
    "s_mov_b32 s59, 8\n"
    "s_waitcnt vmcnt(31)\n"
    "v_pk_min_i16 v4, v4, v10\n"
    "v_pk_min_i16 v5, v5, v11\n"
    "v_pk_min_i16 v6, v6, v12\n"
    "v_pk_min_i16 v7, v7, v13\n"
    "v_pk_min_i16 v8, v8, v14\n"
    "v_pk_min_i16 v9, v9, v15\n"
    "v_pk_min_i16 v16, v16, v18\n"
    "v_pk_min_i16 v17, v17, v19\n"
    "v_pk_min_i16 v4, v4, v7\n"
    "v_pk_min_i16 v5, v5, v8\n"
    "v_pk_min_i16 v6, v6, v9\n"
    "v_pk_min_i16 v16, v16, v17\n"
    "v_pk_min_i16 v4, v4, v5\n"
    "v_lshlrev_b32 v17, 16, v16\n"
    "v_pk_min_i16 v4, v4, v6\n"
    "v_min_i32 v16, v16, v17\n"
    "v_lshlrev_b32 v5, 16, v4\n"
    "v_mov_b32 v7, 155920\n"
    "v_min_i32 v4, v4, v5\n"
    "v_lshl_add_u32 v6, s84, 2, v7\n"
    "v_min_i32_dpp v16, v16, v16 row_shr:1\n"
    "v_min_i32_dpp v4, v4, v4 row_shr:1\n"
    "s_nop 0\n"
    "v_min_i32_dpp v16, v16, v16 row_shr:2\n"
    "v_min_i32_dpp v4, v4, v4 row_shr:2\n"
    "s_nop 0\n"
    "v_min_i32_dpp v16, v16, v16 row_shr:4\n"
    "v_min_i32_dpp v4, v4, v4 row_shr:4\n"
    "s_nop 0\n"
    "v_min_i32_dpp v16, v16, v16 row_shr:8\n"
    "v_min_i32_dpp v4, v4, v4 row_shr:8\n"
    "s_nop 0\n"
    "v_min_i32_dpp v16, v16, v16 row_bcast:15 row_mask:0xa\n"
    "v_min_i32_dpp v4, v4, v4 row_bcast:15 row_mask:0xa\n"
    "s_nop 0\n"
    "v_min_i32_dpp v16, v16, v16 row_bcast:31 row_mask:0xc\n"
    "v_min_i32_dpp v4, v4, v4 row_bcast:31 row_mask:0xc\n"
    "v_readlane_b32 s35, v16, 63\n"
    "v_readlane_b32 s73, v4, 63\n"
    "s_max_i32 s35, s35, 0\n"
    "s_and_b32 s35, s35, 0xffff0000\n"
    "v_mov_b32 v4, s73\n"
    "ds_write_b32 v6, v4\n"
    "s_waitcnt vmcnt(29)\n"
    "v_lshlrev_b32 v180, 16, v164\n"
    "v_and_b32 v181, 0xffff0000, v164\n"
    "v_lshlrev_b32 v182, 16, v168\n"
    "v_and_b32 v183, 0xffff0000, v168\n"
    "v_lshlrev_b32 v184, 16, v165\n"
    "v_and_b32 v185, 0xffff0000, v165\n"
    "v_lshlrev_b32 v186, 16, v169\n"
    "v_and_b32 v187, 0xffff0000, v169\n"
    "v_lshlrev_b32 v188, 16, v166\n"
    "v_and_b32 v189, 0xffff0000, v166\n"
    "v_lshlrev_b32 v190, 16, v170\n"
    "v_and_b32 v191, 0xffff0000, v170\n"
    "v_lshlrev_b32 v192, 16, v167\n"
    "v_and_b32 v193, 0xffff0000, v167\n"
    "v_lshlrev_b32 v194, 16, v171\n"
    "v_and_b32 v195, 0xffff0000, v171\n"
    "v_mul_f32 v180, 0x3dd53b95, v180\n"
    "v_mul_f32 v181, 0x3dd53b95, v181\n"
    "v_mul_f32 v184, 0x3dd53b95, v184\n"
    "v_mul_f32 v185, 0x3dd53b95, v185\n"
    "v_mul_f32 v188, 0x3dd53b95, v188\n"
    "v_mul_f32 v189, 0x3dd53b95, v189\n"
    "v_mul_f32 v192, 0x3dd53b95, v192\n"
    "v_mul_f32 v193, 0x3dd53b95, v193\n"
    "v_mul_f32 v182, -v182, v180\n"
    "v_mul_f32 v183, -v183, v181\n"
    "v_mul_f32 v186, -v186, v184\n"
    "v_mul_f32 v187, -v187, v185\n"
    "v_mul_f32 v190, -v190, v188\n"
    "v_mul_f32 v191, -v191, v189\n"
    "v_mul_f32 v194, -v194, v192\n"
    "v_mul_f32 v195, -v195, v193\n"
    "ds_write_b128 v160, v[180:183] offset:0\n"
    "ds_write_b128 v160, v[184:187] offset:16\n"
    "ds_write_b128 v160, v[188:191] offset:32\n"
    "ds_write_b128 v160, v[192:195] offset:48\n"
    "s_waitcnt lgkmcnt(0)\n"
    "s_barrier\n"
    "ds_read_b128 v[8:11], v7 offset:0\n"
    "buffer_load_dwordx2 v[56:57], v242, s[28:31], s77 offen offset:3072 nt\n"
    "buffer_load_dwordx2 v[58:59], v242, s[28:31], s77 offen offset:3840 nt\n"
    "buffer_load_dwordx4 v[136:139], v130, s[92:95], 0 offen offset:0\n"
    "buffer_load_dwordx4 v[140:143], v130, s[96:99], 0 offen offset:0\n"
    "v_accvgpr_write_b32 a0, 0\n"
    "v_accvgpr_write_b32 a1, 0\n"
    "v_accvgpr_write_b32 a2, 0\n"
    "v_accvgpr_write_b32 a3, 0\n"
    "v_accvgpr_write_b32 a4, 0\n"
    "v_accvgpr_write_b32 a5, 0\n"
    "v_accvgpr_write_b32 a6, 0\n"
    "v_accvgpr_write_b32 a7, 0\n"
    "v_accvgpr_write_b32 a8, 0\n"
    "v_accvgpr_write_b32 a9, 0\n"
    "v_accvgpr_write_b32 a10, 0\n"
    "v_accvgpr_write_b32 a11, 0\n"
    "v_accvgpr_write_b32 a12, 0\n"
    "v_accvgpr_write_b32 a13, 0\n"
    "v_accvgpr_write_b32 a14, 0\n"
    "v_accvgpr_write_b32 a15, 0\n"
    "v_accvgpr_write_b32 a16, 0\n"
    "v_accvgpr_write_b32 a17, 0\n"
    "v_accvgpr_write_b32 a18, 0\n"
    "v_accvgpr_write_b32 a19, 0\n"
    "v_accvgpr_write_b32 a20, 0\n"
    "v_accvgpr_write_b32 a21, 0\n"
    "v_accvgpr_write_b32 a22, 0\n"
    "v_accvgpr_write_b32 a23, 0\n"
    "v_accvgpr_write_b32 a24, 0\n"
    "v_accvgpr_write_b32 a25, 0\n"
    "v_accvgpr_write_b32 a26, 0\n"
    "v_accvgpr_write_b32 a27, 0\n"
    "v_accvgpr_write_b32 a28, 0\n"
    "v_accvgpr_write_b32 a29, 0\n"
    "v_accvgpr_write_b32 a30, 0\n"
    "v_accvgpr_write_b32 a31, 0\n"
    "v_accvgpr_write_b32 a32, 0\n"
    "v_accvgpr_write_b32 a33, 0\n"
    "v_accvgpr_write_b32 a34, 0\n"
    "v_accvgpr_write_b32 a35, 0\n"
    "v_accvgpr_write_b32 a36, 0\n"
    "v_accvgpr_write_b32 a37, 0\n"
    "v_accvgpr_write_b32 a38, 0\n"
    "v_accvgpr_write_b32 a39, 0\n"
    "v_accvgpr_write_b32 a40, 0\n"
    "v_accvgpr_write_b32 a41, 0\n"
    "v_accvgpr_write_b32 a42, 0\n"
    "v_accvgpr_write_b32 a43, 0\n"
    "v_accvgpr_write_b32 a44, 0\n"
    "v_accvgpr_write_b32 a45, 0\n"
    "v_accvgpr_write_b32 a46, 0\n"
    "v_accvgpr_write_b32 a47, 0\n"
    "v_accvgpr_write_b32 a48, 0\n"
    "v_accvgpr_write_b32 a49, 0\n"
    "v_accvgpr_write_b32 a50, 0\n"
    "v_accvgpr_write_b32 a51, 0\n"
    "v_accvgpr_write_b32 a52, 0\n"
    "v_accvgpr_write_b32 a53, 0\n"
    "v_accvgpr_write_b32 a54, 0\n"
    "v_accvgpr_write_b32 a55, 0\n"
    "v_accvgpr_write_b32 a56, 0\n"
    "v_accvgpr_write_b32 a57, 0\n"
    "v_accvgpr_write_b32 a58, 0\n"
    "v_accvgpr_write_b32 a59, 0\n"
    "s_waitcnt lgkmcnt(0)\n"
    "v_min3_i32 v4, v8, v9, v10\n"
    "v_min_i32 v4, v4, v11\n"
    "v_max_i32 v4, 0, v4\n"
    "v_and_b32 v4, 0xffff0000, v4\n"
    "v_mul_f32 v4, s35, v4\n"
    "v_mul_f32 v4, 0x3e7537b8, v4\n"
    "s_nop 0\n"
    "v_readfirstlane_b32 s35, v4\n"
    "s_waitcnt vmcnt(17)\n"
    "s_add_u32 s54, s71, s60\n"
    "s_add_u32 s55, s71, s61\n"
    "v_cvt_f32_i32 v204, v204\n"
    "v_cvt_f32_i32 v205, v205\n"
    "v_cvt_f32_i32 v206, v206\n"
    "v_cvt_f32_i32 v207, v207\n"
    "v_cvt_f32_i32 v208, v208\n"
    "v_cvt_f32_i32 v209, v209\n"
    "v_cvt_f32_i32 v210, v210\n"
    "v_cvt_f32_i32 v211, v211\n"
    "v_cvt_f32_i32 v212, v212\n"
    "v_cvt_f32_i32 v213, v213\n"
    "v_cvt_f32_i32 v214, v214\n"
    "v_cvt_f32_i32 v215, v215\n"
    "ds_read_b128 v[92:95], v243 offset:0\n"
    "v_cvt_f32_i32 v216, v216\n"
    "v_cvt_f32_i32 v217, v217\n"
    "v_cvt_f32_i32 v218, v218\n"
    "v_cvt_f32_i32 v219, v219\n"
    "v_cvt_f32_i32 v220, v220\n"
    "v_cvt_f32_i32 v221, v221\n"
    "v_cvt_f32_i32 v222, v222\n"
    "v_cvt_f32_i32 v223, v223\n"
    "v_cvt_f32_i32 v224, v224\n"
    "v_cvt_f32_i32 v225, v225\n"
    "v_cvt_f32_i32 v226, v226\n"
    "v_cvt_f32_i32 v227, v227\n"
    "s_waitcnt lgkmcnt(0)\n"
    "v_add_f32 v94, s35, v94\n"
    "v_add_f32 v95, s35, v95\n"
    "v_fma_f32 v204, v204, v92, v94\n"
    "v_fma_f32 v205, v205, v93, v95\n"
    "v_fma_f32 v206, v206, v92, v94\n"
    "v_fma_f32 v207, v207, v93, v95\n"
    "buffer_load_dwordx2 v[60:61], v242, s[28:31], s78 offen offset:0 nt\n"
    "buffer_load_dwordx2 v[62:63], v242, s[28:31], s78 offen offset:768 nt\n"
    "buffer_load_dwordx4 v[4:7], v162, s[28:31], s57 offen offset:0\n"
    "buffer_load_dwordx4 v[8:11], v162, s[28:31], s57 offen offset:3072\n"
    "v_cvt_pk_bf16_f32 v204, v204, v205\n"
    "v_cvt_pk_bf16_f32 v206, v206, v207\n"
    "v_cvt_f32_i32 v228, v228\n"
    "v_cvt_f32_i32 v229, v229\n"
    "v_cvt_f32_i32 v230, v230\n"
    "v_cvt_f32_i32 v231, v231\n"
    "buffer_store_dword v204, v244, s[16:19], s54 offen offset:0 sc1\n"
    "buffer_store_dword v206, v244, s[16:19], s54 offen offset:128 sc1\n"
    "v_fma_f32 v208, v208, v92, v94\n"
    "v_fma_f32 v209, v209, v93, v95\n"
    "v_fma_f32 v210, v210, v92, v94\n"
    "v_fma_f32 v211, v211, v93, v95\n"
    "v_cvt_pk_bf16_f32 v208, v208, v209\n"
    "v_cvt_pk_bf16_f32 v210, v210, v211\n"
    "v_cvt_f32_i32 v232, v232\n"
    "v_cvt_f32_i32 v233, v233\n"
    "v_cvt_f32_i32 v234, v234\n"
    "v_cvt_f32_i32 v235, v235\n"
    "buffer_store_dword v208, v244, s[16:19], s54 offen offset:1024 sc1\n"
    "buffer_store_dword v210, v244, s[16:19], s54 offen offset:1152 sc1\n"
    "v_fma_f32 v212, v212, v92, v94\n"
    "v_fma_f32 v213, v213, v93, v95\n"
    "v_fma_f32 v214, v214, v92, v94\n"
    "v_fma_f32 v215, v215, v93, v95\n"
    "v_cvt_pk_bf16_f32 v212, v212, v213\n"
    "v_cvt_pk_bf16_f32 v214, v214, v215\n"
    "buffer_store_dword v212, v244, s[16:19], s54 offen offset:256 sc1\n"
    "buffer_store_dword v214, v244, s[16:19], s54 offen offset:384 sc1\n"
    "v_fma_f32 v216, v216, v92, v94\n"
    "v_fma_f32 v217, v217, v93, v95\n"
    "v_fma_f32 v218, v218, v92, v94\n"
    "v_fma_f32 v219, v219, v93, v95\n"
    "v_cvt_pk_bf16_f32 v216, v216, v217\n"
    "v_cvt_pk_bf16_f32 v218, v218, v219\n"
    "buffer_load_dwordx2 v[64:65], v242, s[28:31], s78 offen offset:3072 nt\n"
    "buffer_load_dwordx2 v[66:67], v242, s[28:31], s78 offen offset:3840 nt\n"
    "buffer_load_dwordx4 v[12:15], v162, s[28:31], s57 offen offset:128\n"
    "buffer_store_dword v216, v244, s[16:19], s54 offen offset:1280 sc1\n"
    "buffer_store_dword v218, v244, s[16:19], s54 offen offset:1408 sc1\n"
    "v_fma_f32 v220, v220, v92, v94\n"
    "v_fma_f32 v221, v221, v93, v95\n"
    "v_fma_f32 v222, v222, v92, v94\n"
    "v_fma_f32 v223, v223, v93, v95\n"
    "v_cvt_pk_bf16_f32 v220, v220, v221\n"
    "v_cvt_pk_bf16_f32 v222, v222, v223\n"
    "buffer_store_dword v220, v244, s[16:19], s55 offen offset:0 sc1\n"
    "buffer_store_dword v222, v244, s[16:19], s55 offen offset:128 sc1\n"
    "v_fma_f32 v224, v224, v92, v94\n"
    "v_fma_f32 v225, v225, v93, v95\n"
    "v_fma_f32 v226, v226, v92, v94\n"
    "v_fma_f32 v227, v227, v93, v95\n"
    "v_cvt_pk_bf16_f32 v224, v224, v225\n"
    "v_cvt_pk_bf16_f32 v226, v226, v227\n"
    "buffer_store_dword v224, v244, s[16:19], s55 offen offset:1024 sc1\n"
    "buffer_store_dword v226, v244, s[16:19], s55 offen offset:1152 sc1\n"
    "v_fma_f32 v228, v228, v92, v94\n"
    "v_fma_f32 v229, v229, v93, v95\n"
    "v_fma_f32 v230, v230, v92, v94\n"
    "v_fma_f32 v231, v231, v93, v95\n"
    "v_cvt_pk_bf16_f32 v228, v228, v229\n"
    "v_cvt_pk_bf16_f32 v230, v230, v231\n"
    "buffer_store_dword v228, v244, s[16:19], s55 offen offset:256 sc1\n"
    "buffer_store_dword v230, v244, s[16:19], s55 offen offset:384 sc1\n"
    "v_fma_f32 v232, v232, v92, v94\n"
    "v_fma_f32 v233, v233, v93, v95\n"
    "v_fma_f32 v234, v234, v92, v94\n"
    "v_fma_f32 v235, v235, v93, v95\n"
    "v_cvt_pk_bf16_f32 v232, v232, v233\n"
    "v_cvt_pk_bf16_f32 v234, v234, v235\n"
    "buffer_store_dword v232, v244, s[16:19], s55 offen offset:1280 sc1\n"
    "buffer_store_dword v234, v244, s[16:19], s55 offen offset:1408 sc1\n"
    "v_add_u32 v243, 512, v243\n"
    "s_add_u32 s71, s71, 98304\n"
    "s_add_u32 m0, s65, 24576\n"
    "s_add_u32 s72, s85, 0\n"
    "buffer_load_dwordx4 v88, s[20:23], s72 offen offset:0 nt lds\n"
    "buffer_load_dwordx4 v[16:19], v162, s[28:31], s57 offen offset:3200\n"
    "buffer_load_dwordx4 v[20:23], v162, s[28:31], s57 offen offset:256\n"
    "s_waitcnt vmcnt(38)\n"
    "v_add_co_u32 v238, vcc, 0x7f4a7c15, v238\n"
    "v_mov_b32 v159, 0x9e3779b9\n"
    "s_nop 0\n"
    "v_addc_co_u32 v239, vcc, v159, v239, vcc\n"
    "v_mov_b32 v246, 1\n"
    "s_waitcnt vmcnt(39)\n"
    "v_lshlrev_b32 v180, 16, v124\n"
    "v_and_b32 v181, 0xffff0000, v124\n"
    "v_lshlrev_b32 v182, 16, v125\n"
    "v_and_b32 v183, 0xffff0000, v125\n"
    "v_lshlrev_b32 v184, 16, v126\n"
    "v_and_b32 v185, 0xffff0000, v126\n"
    "v_lshlrev_b32 v186, 16, v127\n"
    "v_and_b32 v187, 0xffff0000, v127\n"
    "v_mul_f32 v180, 0x3dd53b95, v180\n"
    "v_mul_f32 v181, 0x3dd53b95, v181\n"
    "v_mul_f32 v182, 0x3dd53b95, v182\n"
    "v_mul_f32 v183, 0x3dd53b95, v183\n"
    "v_mul_f32 v184, -v184, v180\n"
    "v_mul_f32 v185, -v185, v181\n"
    "v_mul_f32 v186, -v186, v182\n"
    "v_mul_f32 v187, -v187, v183\n"
    "v_add_f32 v184, s35, v184\n"
    "v_add_f32 v185, s35, v185\n"
    "v_add_f32 v186, s35, v186\n"
    "v_add_f32 v187, s35, v187\n"
    "ds_write_b128 v131, v[180:183] offset:0\n"
    "ds_write_b128 v131, v[184:187] offset:1024\n"
    "ds_read_b128 v[180:183], v132 offset:0\n"
    "ds_read_b128 v[184:187], v132 offset:1024\n"
    "ds_read_b128 v[188:191], v132 offset:128\n"
    "ds_read_b128 v[192:195], v132 offset:1152\n"
    "ds_read_b128 v[196:199], v132 offset:256\n"
    "ds_read_b128 v[200:203], v132 offset:1280\n"
    "v_accvgpr_write_b32 a60, 0\n"
    "v_accvgpr_write_b32 a61, 0\n"
    "v_accvgpr_write_b32 a62, 0\n"
    "v_accvgpr_write_b32 a63, 0\n"
    "v_accvgpr_write_b32 a64, 0\n"
    "v_accvgpr_write_b32 a65, 0\n"
    "v_accvgpr_write_b32 a66, 0\n"
    "s_add_u32 m0, s65, 25600\n"
    "s_add_u32 s72, s85, 1024\n"
    "buffer_load_dwordx4 v89, s[20:23], s72 offen offset:0 nt lds\n"
    "buffer_load_dwordx4 v[24:27], v162, s[28:31], s57 offen offset:3328\n"
    "v_accvgpr_write_b32 a67, 0\n"
    "v_accvgpr_write_b32 a68, 0\n"
    "v_accvgpr_write_b32 a69, 0\n"
    "v_accvgpr_write_b32 a70, 0\n"
    "v_accvgpr_write_b32 a71, 0\n"
    "v_accvgpr_write_b32 a72, 0\n"
    "v_accvgpr_write_b32 a73, 0\n"
    "v_accvgpr_write_b32 a74, 0\n"
    "v_accvgpr_write_b32 a75, 0\n"
    "v_accvgpr_write_b32 a76, 0\n"
    "v_accvgpr_write_b32 a77, 0\n"
    "v_accvgpr_write_b32 a78, 0\n"
    "v_accvgpr_write_b32 a79, 0\n"
    "v_accvgpr_write_b32 a80, 0\n"
    "v_accvgpr_write_b32 a81, 0\n"
    "v_accvgpr_write_b32 a82, 0\n"
    "v_accvgpr_write_b32 a83, 0\n"
    "v_accvgpr_write_b32 a84, 0\n"
    "v_accvgpr_write_b32 a85, 0\n"
    "v_accvgpr_write_b32 a86, 0\n"
    "v_accvgpr_write_b32 a87, 0\n"
    "v_accvgpr_write_b32 a88, 0\n"
    "v_accvgpr_write_b32 a89, 0\n"
    "v_accvgpr_write_b32 a90, 0\n"
    "v_accvgpr_write_b32 a91, 0\n"
    "v_accvgpr_write_b32 a92, 0\n"
    "v_accvgpr_write_b32 a93, 0\n"
    "v_accvgpr_write_b32 a94, 0\n"
    "v_accvgpr_write_b32 a95, 0\n"
    "v_accvgpr_write_b32 a96, 0\n"
    "v_accvgpr_write_b32 a97, 0\n"
    "v_accvgpr_write_b32 a98, 0\n"
    "v_accvgpr_write_b32 a99, 0\n"
    "v_accvgpr_write_b32 a100, 0\n"
    "v_accvgpr_write_b32 a101, 0\n"
    "v_accvgpr_write_b32 a102, 0\n"
    "v_accvgpr_write_b32 a103, 0\n"
    "v_accvgpr_write_b32 a104, 0\n"
    "v_accvgpr_write_b32 a105, 0\n"
    "v_accvgpr_write_b32 a106, 0\n"
    "v_accvgpr_write_b32 a107, 0\n"
    "v_accvgpr_write_b32 a108, 0\n"
    "s_add_u32 m0, s65, 26624\n"
    "s_add_u32 s72, s85, 2048\n"
    "buffer_load_dwordx4 v90, s[20:23], s72 offen offset:0 nt lds\n"
    "buffer_load_dwordx4 v[100:103], v162, s[28:31], s66 offen offset:0\n"
    "buffer_load_dwordx4 v[104:107], v162, s[28:31], s66 offen offset:3072\n"
    "v_accvgpr_write_b32 a109, 0\n"
    "v_accvgpr_write_b32 a110, 0\n"
    "v_accvgpr_write_b32 a111, 0\n"
    "v_accvgpr_write_b32 a112, 0\n"
    "v_accvgpr_write_b32 a113, 0\n"
    "v_accvgpr_write_b32 a114, 0\n"
    "v_accvgpr_write_b32 a115, 0\n"
    "v_accvgpr_write_b32 a116, 0\n"
    "v_accvgpr_write_b32 a117, 0\n"
    "v_accvgpr_write_b32 a118, 0\n"
    "v_accvgpr_write_b32 a119, 0\n"
    "v_accvgpr_write_b32 a120, 0\n"
    "v_accvgpr_write_b32 a121, 0\n"
    "v_accvgpr_write_b32 a122, 0\n"
    "v_accvgpr_write_b32 a123, 0\n"
    "v_accvgpr_write_b32 a124, 0\n"
    "v_accvgpr_write_b32 a125, 0\n"
    "v_accvgpr_write_b32 a126, 0\n"
    "v_accvgpr_write_b32 a127, 0\n"
    "v_accvgpr_write_b32 a128, 0\n"
    "v_accvgpr_write_b32 a129, 0\n"
    "v_accvgpr_write_b32 a130, 0\n"
    "v_accvgpr_write_b32 a131, 0\n"
    "v_accvgpr_write_b32 a132, 0\n"
    "v_accvgpr_write_b32 a133, 0\n"
    "v_accvgpr_write_b32 a134, 0\n"
    "v_accvgpr_write_b32 a135, 0\n"
    "v_accvgpr_write_b32 a136, 0\n"
    "v_accvgpr_write_b32 a137, 0\n"
    "v_accvgpr_write_b32 a138, 0\n"
    "v_accvgpr_write_b32 a139, 0\n"
    "v_accvgpr_write_b32 a140, 0\n"
    "v_accvgpr_write_b32 a141, 0\n"
    "v_accvgpr_write_b32 a142, 0\n"
    "v_accvgpr_write_b32 a143, 0\n"
    "v_accvgpr_write_b32 a144, 0\n"
    "v_accvgpr_write_b32 a145, 0\n"
    "v_accvgpr_write_b32 a146, 0\n"
    "v_accvgpr_write_b32 a147, 0\n"
    "v_accvgpr_write_b32 a148, 0\n"
    "v_accvgpr_write_b32 a149, 0\n"
    "s_add_u32 m0, s65, 27648\n"
    "s_add_u32 s72, s85, 3072\n"
    "buffer_load_dwordx4 v88, s[20:23], s72 offen offset:0 nt lds\n"
    "buffer_load_dwordx4 v[108:111], v162, s[28:31], s66 offen offset:128\n"
    "v_accvgpr_write_b32 a150, 0\n"
    "v_accvgpr_write_b32 a151, 0\n"
    "v_accvgpr_write_b32 a152, 0\n"
    "v_accvgpr_write_b32 a153, 0\n"
    "v_accvgpr_write_b32 a154, 0\n"
    "v_accvgpr_write_b32 a155, 0\n"
    "v_accvgpr_write_b32 a156, 0\n"
    "v_accvgpr_write_b32 a157, 0\n"
    "v_accvgpr_write_b32 a158, 0\n"
    "v_accvgpr_write_b32 a159, 0\n"
    "v_accvgpr_write_b32 a160, 0\n"
    "v_accvgpr_write_b32 a161, 0\n"
    "v_accvgpr_write_b32 a162, 0\n"
    "v_accvgpr_write_b32 a163, 0\n"
    "v_accvgpr_write_b32 a164, 0\n"
    "v_accvgpr_write_b32 a165, 0\n"
    "v_accvgpr_write_b32 a166, 0\n"
    "v_accvgpr_write_b32 a167, 0\n"
    "v_accvgpr_write_b32 a168, 0\n"
    "v_accvgpr_write_b32 a169, 0\n"
    "v_accvgpr_write_b32 a170, 0\n"
    "v_accvgpr_write_b32 a171, 0\n"
    "v_accvgpr_write_b32 a172, 0\n"
    "v_accvgpr_write_b32 a173, 0\n"
    "v_accvgpr_write_b32 a174, 0\n"
    "v_accvgpr_write_b32 a175, 0\n"
    "v_accvgpr_write_b32 a176, 0\n"
    "v_accvgpr_write_b32 a177, 0\n"
    "v_accvgpr_write_b32 a178, 0\n"
    "v_accvgpr_write_b32 a179, 0\n"
    "v_accvgpr_write_b32 a180, 0\n"
    "v_accvgpr_write_b32 a181, 0\n"
    "v_accvgpr_write_b32 a182, 0\n"
    "v_accvgpr_write_b32 a183, 0\n"
    "v_accvgpr_write_b32 a184, 0\n"
    "v_accvgpr_write_b32 a185, 0\n"
    "v_accvgpr_write_b32 a186, 0\n"
    "v_accvgpr_write_b32 a187, 0\n"
    "v_accvgpr_write_b32 a188, 0\n"
    "v_accvgpr_write_b32 a189, 0\n"
    "v_accvgpr_write_b32 a190, 0\n"
    "v_accvgpr_write_b32 a191, 0\n"
    "s_waitcnt lgkmcnt(0)\n"
    "s_waitcnt vmcnt(10)\n"
    "ds_add_rtn_u32 v245, v247, v246\n"
    "s_lshl_b32 s73, s53, 6\n"
    "s_add_u32 s73, s73, 128\n"
    "s_waitcnt lgkmcnt(0)\n"
    "v_readfirstlane_b32 s74, v245\n"
    "s_nop 0\n"
    "s_cmp_eq_u32 s74, 3\n"
    "s_cselect_b32 s73, s73, 0x7fff0000\n"
    "buffer_store_dwordx2 v[238:239], v91, s[36:39], s73 offen offset:0 sc1\n"
    "s_waitcnt vmcnt(22)\n"
    "s_add_u32 m0, s65, 28672\n"
    "s_add_u32 s72, s85, 4096\n"
    "buffer_load_dwordx4 v89, s[20:23], s72 offen offset:0 nt lds\n"
    "buffer_load_dwordx4 v[112:115], v162, s[28:31], s66 offen offset:3200\n"
    "buffer_load_dwordx4 v[116:119], v162, s[28:31], s66 offen offset:256\n"
    "s_add_u32 s54, s71, s60\n"
    "s_add_u32 s55, s71, s61\n"
    "v_cvt_f32_i32 v36, v36\n"
    "v_cvt_f32_i32 v37, v37\n"
    "v_cvt_f32_i32 v38, v38\n"
    "v_cvt_f32_i32 v39, v39\n"
    "v_cvt_f32_i32 v40, v40\n"
    "v_cvt_f32_i32 v41, v41\n"
    "v_cvt_f32_i32 v42, v42\n"
    "v_cvt_f32_i32 v43, v43\n"
    "v_cvt_f32_i32 v44, v44\n"
    "v_cvt_f32_i32 v45, v45\n"
    "v_cvt_f32_i32 v46, v46\n"
    "v_cvt_f32_i32 v47, v47\n"
    "ds_read_b128 v[92:95], v243 offset:0\n"
    "v_cvt_f32_i32 v48, v48\n"
    "v_cvt_f32_i32 v49, v49\n"
    "v_cvt_f32_i32 v50, v50\n"
    "v_cvt_f32_i32 v51, v51\n"
    "v_cvt_f32_i32 v52, v52\n"
    "v_cvt_f32_i32 v53, v53\n"
    "v_cvt_f32_i32 v54, v54\n"
    "v_cvt_f32_i32 v55, v55\n"
    "v_cvt_f32_i32 v56, v56\n"
    "v_cvt_f32_i32 v57, v57\n"
    "v_cvt_f32_i32 v58, v58\n"
    "v_cvt_f32_i32 v59, v59\n"
    "s_waitcnt lgkmcnt(0)\n"
    "v_add_f32 v94, s35, v94\n"
    "v_add_f32 v95, s35, v95\n"
    "v_fma_f32 v36, v36, v92, v94\n"
    "v_fma_f32 v37, v37, v93, v95\n"
    "v_fma_f32 v38, v38, v92, v94\n"
    "v_fma_f32 v39, v39, v93, v95\n"
    "v_cvt_pk_bf16_f32 v36, v36, v37\n"
    "v_cvt_pk_bf16_f32 v38, v38, v39\n"
    "v_cvt_f32_i32 v60, v60\n"
    "v_cvt_f32_i32 v61, v61\n"
    "v_cvt_f32_i32 v62, v62\n"
    "v_cvt_f32_i32 v63, v63\n"
    "buffer_store_dword v36, v244, s[16:19], s54 offen offset:0 sc1\n"
    "buffer_store_dword v38, v244, s[16:19], s54 offen offset:128 sc1\n"
    "v_fma_f32 v40, v40, v92, v94\n"
    "v_fma_f32 v41, v41, v93, v95\n"
    "v_fma_f32 v42, v42, v92, v94\n"
    "v_fma_f32 v43, v43, v93, v95\n"
    "v_cvt_pk_bf16_f32 v40, v40, v41\n"
    "v_cvt_pk_bf16_f32 v42, v42, v43\n"
    "v_cvt_f32_i32 v64, v64\n"
    "v_cvt_f32_i32 v65, v65\n"
    "v_cvt_f32_i32 v66, v66\n"
    "s_add_u32 m0, s65, 29696\n"
    "s_add_u32 s72, s85, 5120\n"
    "buffer_load_dwordx4 v90, s[20:23], s72 offen offset:0 nt lds\n"
    "buffer_load_dwordx4 v[120:123], v162, s[28:31], s66 offen offset:3328\n"
    "v_cvt_f32_i32 v67, v67\n"
    "buffer_store_dword v40, v244, s[16:19], s54 offen offset:1024 sc1\n"
    "buffer_store_dword v42, v244, s[16:19], s54 offen offset:1152 sc1\n"
    "v_fma_f32 v44, v44, v92, v94\n"
    "v_fma_f32 v45, v45, v93, v95\n"
    "v_fma_f32 v46, v46, v92, v94\n"
    "v_fma_f32 v47, v47, v93, v95\n"
    "v_cvt_pk_bf16_f32 v44, v44, v45\n"
    "v_cvt_pk_bf16_f32 v46, v46, v47\n"
    "buffer_store_dword v44, v244, s[16:19], s54 offen offset:256 sc1\n"
    "buffer_store_dword v46, v244, s[16:19], s54 offen offset:384 sc1\n"
    "v_fma_f32 v48, v48, v92, v94\n"
    "v_fma_f32 v49, v49, v93, v95\n"
    "v_fma_f32 v50, v50, v92, v94\n"
    "v_fma_f32 v51, v51, v93, v95\n"
    "v_cvt_pk_bf16_f32 v48, v48, v49\n"
    "v_cvt_pk_bf16_f32 v50, v50, v51\n"
    "buffer_store_dword v48, v244, s[16:19], s54 offen offset:1280 sc1\n"
    "buffer_store_dword v50, v244, s[16:19], s54 offen offset:1408 sc1\n"
    "v_fma_f32 v52, v52, v92, v94\n"
    "v_fma_f32 v53, v53, v93, v95\n"
    "v_fma_f32 v54, v54, v92, v94\n"
    "v_fma_f32 v55, v55, v93, v95\n"
    "v_cvt_pk_bf16_f32 v52, v52, v53\n"
    "v_cvt_pk_bf16_f32 v54, v54, v55\n"
    "buffer_store_dword v52, v244, s[16:19], s55 offen offset:0 sc1\n"
    "buffer_store_dword v54, v244, s[16:19], s55 offen offset:128 sc1\n"
    "v_fma_f32 v56, v56, v92, v94\n"
    "v_fma_f32 v57, v57, v93, v95\n"
    "v_fma_f32 v58, v58, v92, v94\n"
    "v_fma_f32 v59, v59, v93, v95\n"
    "v_cvt_pk_bf16_f32 v56, v56, v57\n"
    "v_cvt_pk_bf16_f32 v58, v58, v59\n"
    "buffer_store_dword v56, v244, s[16:19], s55 offen offset:1024 sc1\n"
    "buffer_store_dword v58, v244, s[16:19], s55 offen offset:1152 sc1\n"
    "v_fma_f32 v60, v60, v92, v94\n"
    "v_fma_f32 v61, v61, v93, v95\n"
    "v_fma_f32 v62, v62, v92, v94\n"
    "v_fma_f32 v63, v63, v93, v95\n"
    "v_cvt_pk_bf16_f32 v60, v60, v61\n"
    "v_cvt_pk_bf16_f32 v62, v62, v63\n"
    "buffer_store_dword v60, v244, s[16:19], s55 offen offset:256 sc1\n"
    "buffer_store_dword v62, v244, s[16:19], s55 offen offset:384 sc1\n"
    "v_fma_f32 v64, v64, v92, v94\n"
    "v_fma_f32 v65, v65, v93, v95\n"
    "v_fma_f32 v66, v66, v92, v94\n"
    "v_fma_f32 v67, v67, v93, v95\n"
    "v_cvt_pk_bf16_f32 v64, v64, v65\n"
    "v_cvt_pk_bf16_f32 v66, v66, v67\n"
    "buffer_store_dword v64, v244, s[16:19], s55 offen offset:1280 sc1\n"
    "buffer_store_dword v66, v244, s[16:19], s55 offen offset:1408 sc1\n"
    "v_add_u32 v243, 512, v243\n"
    "s_add_u32 m0, s65, 73728\n"
    "s_add_u32 s72, s85, 6144\n"
    "buffer_load_dwordx4 v88, s[20:23], s72 offen offset:0 nt lds\n"
    "s_waitcnt vmcnt(56)\n"
    "v_lshlrev_b32 v204, 16, v136\n"
    "v_and_b32 v205, 0xffff0000, v136\n"
    "v_lshlrev_b32 v206, 16, v137\n"
    "v_and_b32 v207, 0xffff0000, v137\n"
    "v_lshlrev_b32 v208, 16, v138\n"
    "v_and_b32 v209, 0xffff0000, v138\n"
    "v_lshlrev_b32 v210, 16, v139\n"
    "v_and_b32 v211, 0xffff0000, v139\n"
    "v_log_f32 v68, v204\n"
    "v_log_f32 v69, v205\n"
    "v_log_f32 v70, v206\n"
    "v_log_f32 v71, v207\n"
    "v_log_f32 v72, v208\n"
    "v_log_f32 v73, v209\n"
    "v_log_f32 v74, v210\n"
    "v_log_f32 v75, v211\n"
    "v_rcp_f32 v204, v204\n"
    "v_rcp_f32 v205, v205\n"
    "v_rcp_f32 v206, v206\n"
    "v_rcp_f32 v207, v207\n"
    "v_rcp_f32 v208, v208\n"
    "v_rcp_f32 v209, v209\n"
    "v_rcp_f32 v210, v210\n"
    "v_rcp_f32 v211, v211\n"
    "s_mov_b32 s73, 0x41a00000\n"
    "v_mov_b32 v228, 0x7fc00000\n"
    "v_cmp_le_f32 vcc, |v68|, s73\n"
    "s_nop 1\n"
    "v_cndmask_b32 v68, v228, v68, vcc\n"
    "v_cmp_le_f32 vcc, |v69|, s73\n"
    "s_nop 1\n"
    "v_cndmask_b32 v69, v228, v69, vcc\n"
    "v_cmp_le_f32 vcc, |v70|, s73\n"
    "s_nop 1\n"
    "v_cndmask_b32 v70, v228, v70, vcc\n"
    "v_cmp_le_f32 vcc, |v71|, s73\n"
    "s_nop 1\n"
    "v_cndmask_b32 v71, v228, v71, vcc\n"
    "s_add_u32 m0, s65, 74752\n"
    "s_add_u32 s72, s85, 7168\n"
    "buffer_load_dwordx4 v89, s[20:23], s72 offen offset:0 nt lds\n"
    "v_cmp_le_f32 vcc, |v72|, s73\n"
    "s_nop 1\n"
    "v_cndmask_b32 v72, v228, v72, vcc\n"
    "v_cmp_le_f32 vcc, |v73|, s73\n"
    "s_nop 1\n"
    "v_cndmask_b32 v73, v228, v73, vcc\n"
    "v_cmp_le_f32 vcc, |v74|, s73\n"
    "s_nop 1\n"
    "v_cndmask_b32 v74, v228, v74, vcc\n"
    "v_cmp_le_f32 vcc, |v75|, s73\n"
    "s_nop 1\n"
    "v_cndmask_b32 v75, v228, v75, vcc\n"
    "v_cvt_pk_bf16_f32 v220, v204, v205\n"
    "v_cvt_pk_bf16_f32 v221, v206, v207\n"
    "v_cvt_pk_bf16_f32 v222, v208, v209\n"
    "v_cvt_pk_bf16_f32 v223, v210, v211\n"
    "v_lshlrev_b32 v212, 16, v220\n"
    "v_and_b32 v213, 0xffff0000, v220\n"
    "v_lshlrev_b32 v214, 16, v221\n"
    "v_and_b32 v215, 0xffff0000, v221\n"
    "v_lshlrev_b32 v216, 16, v222\n"
    "v_and_b32 v217, 0xffff0000, v222\n"
    "v_lshlrev_b32 v218, 16, v223\n"
    "v_and_b32 v219, 0xffff0000, v223\n"
    "v_sub_f32 v204, v204, v212\n"
    "v_sub_f32 v205, v205, v213\n"
    "v_sub_f32 v206, v206, v214\n"
    "v_sub_f32 v207, v207, v215\n"
    "v_sub_f32 v208, v208, v216\n"
    "v_sub_f32 v209, v209, v217\n"
    "v_sub_f32 v210, v210, v218\n"
    "v_sub_f32 v211, v211, v219\n"
    "v_cvt_pk_bf16_f32 v224, v204, v205\n"
    "v_cvt_pk_bf16_f32 v225, v206, v207\n"
    "v_cvt_pk_bf16_f32 v226, v208, v209\n"
    "v_cvt_pk_bf16_f32 v227, v210, v211\n"
    "v_mov_b32 v204, 0\n"
    "v_mov_b32 v205, 0\n"
    "v_mov_b32 v206, 0\n"
    "s_add_u32 m0, s65, 75776\n"
    "s_add_u32 s72, s85, 8192\n"
    "buffer_load_dwordx4 v90, s[20:23], s72 offen offset:0 nt lds\n"
    "v_mov_b32 v207, 0\n"
    "v_swap_b32 v221, v222\n"
    "v_swap_b32 v225, v226\n"
    "v_swap_b32 v141, v142\n"
    "ds_write2_b64 v144, v[220:221], v[222:223] offset0:32 offset1:40\n"
    "ds_write2_b64 v144, v[224:225], v[226:227] offset0:48 offset1:56\n"
    "ds_write2_b64 v144, v[140:141], v[142:143] offset0:64 offset1:72\n"
    "ds_write2_b64 v144, v[204:205], v[206:207] offset0:80 offset1:88\n"
    "s_waitcnt vmcnt(30)\n"
    "v_cvt_f32_i32 v4, v4\n"
    "v_cvt_f32_i32 v5, v5\n"
    "v_cvt_f32_i32 v6, v6\n"
    "v_cvt_f32_i32 v7, v7\n"
    "v_cvt_f32_i32 v8, v8\n"
    "v_cvt_f32_i32 v9, v9\n"
    "v_cvt_f32_i32 v10, v10\n"
    "v_cvt_f32_i32 v11, v11\n"
    "v_cvt_f32_i32 v12, v12\n"
    "v_cvt_f32_i32 v13, v13\n"
    "v_cvt_f32_i32 v14, v14\n"
    "v_cvt_f32_i32 v15, v15\n"
    "v_cvt_f32_i32 v16, v16\n"
    "v_cvt_f32_i32 v17, v17\n"
    "v_cvt_f32_i32 v18, v18\n"
    "v_cvt_f32_i32 v19, v19\n"
    "v_cvt_f32_i32 v20, v20\n"
    "v_cvt_f32_i32 v21, v21\n"
    "v_cvt_f32_i32 v22, v22\n"
    "v_cvt_f32_i32 v23, v23\n"
    "v_cvt_f32_i32 v24, v24\n"
    "v_cvt_f32_i32 v25, v25\n"
    "v_cvt_f32_i32 v26, v26\n"
    "v_cvt_f32_i32 v27, v27\n"
    "v_fma_f32 v4, v4, v180, v184\n"
    "v_fma_f32 v5, v5, v181, v185\n"
    "v_fma_f32 v6, v6, v182, v186\n"
    "s_add_u32 m0, s65, 76800\n"
    "s_add_u32 s72, s85, 9216\n"
    "buffer_load_dwordx4 v88, s[20:23], s72 offen offset:0 nt lds\n"
    "v_fma_f32 v7, v7, v183, v187\n"
    "v_fma_f32 v8, v8, v180, v184\n"
    "v_fma_f32 v9, v9, v181, v185\n"
    "v_fma_f32 v10, v10, v182, v186\n"
    "v_fma_f32 v11, v11, v183, v187\n"
    "v_cvt_pk_bf16_f32 v4, v4, v5\n"
    "v_cvt_pk_bf16_f32 v5, v6, v7\n"
    "v_cvt_pk_bf16_f32 v6, v8, v9\n"
    "v_cvt_pk_bf16_f32 v7, v10, v11\n"
    "ds_write_b64 v163, v[4:5] offset:0\n"
    "ds_write_b64 v163, v[6:7] offset:1024\n"
    "v_fma_f32 v12, v12, v188, v192\n"
    "v_fma_f32 v13, v13, v189, v193\n"
    "v_fma_f32 v14, v14, v190, v194\n"
    "v_fma_f32 v15, v15, v191, v195\n"
    "v_fma_f32 v16, v16, v188, v192\n"
    "v_fma_f32 v17, v17, v189, v193\n"
    "v_fma_f32 v18, v18, v190, v194\n"
    "v_fma_f32 v19, v19, v191, v195\n"
    "v_cvt_pk_bf16_f32 v12, v12, v13\n"
    "v_cvt_pk_bf16_f32 v13, v14, v15\n"
    "v_cvt_pk_bf16_f32 v14, v16, v17\n"
    "v_cvt_pk_bf16_f32 v15, v18, v19\n"
    "ds_write_b64 v163, v[12:13] offset:2048\n"
    "ds_write_b64 v163, v[14:15] offset:3072\n"
    "v_fma_f32 v20, v20, v196, v200\n"
    "v_fma_f32 v21, v21, v197, v201\n"
    "v_fma_f32 v22, v22, v198, v202\n"
    "v_fma_f32 v23, v23, v199, v203\n"
    "v_fma_f32 v24, v24, v196, v200\n"
    "v_fma_f32 v25, v25, v197, v201\n"
    "v_fma_f32 v26, v26, v198, v202\n"
    "v_fma_f32 v27, v27, v199, v203\n"
    "v_cvt_pk_bf16_f32 v20, v20, v21\n"
    "v_cvt_pk_bf16_f32 v21, v22, v23\n"
    "s_add_u32 m0, s65, 77824\n"
    "s_add_u32 s72, s85, 10240\n"
    "buffer_load_dwordx4 v89, s[20:23], s72 offen offset:0 nt lds\n"
    "v_cvt_pk_bf16_f32 v22, v24, v25\n"
    "v_cvt_pk_bf16_f32 v23, v26, v27\n"
    "ds_write_b64 v163, v[20:21] offset:4096\n"
    "ds_write_b64 v163, v[22:23] offset:5120\n"
    "s_waitcnt vmcnt(19)\n"
    "v_cvt_f32_i32 v100, v100\n"
    "v_cvt_f32_i32 v101, v101\n"
    "v_cvt_f32_i32 v102, v102\n"
    "v_cvt_f32_i32 v103, v103\n"
    "v_cvt_f32_i32 v104, v104\n"
    "v_cvt_f32_i32 v105, v105\n"
    "v_cvt_f32_i32 v106, v106\n"
    "v_cvt_f32_i32 v107, v107\n"
    "v_cvt_f32_i32 v108, v108\n"
    "v_cvt_f32_i32 v109, v109\n"
    "v_cvt_f32_i32 v110, v110\n"
    "v_cvt_f32_i32 v111, v111\n"
    "v_cvt_f32_i32 v112, v112\n"
    "v_cvt_f32_i32 v113, v113\n"
    "v_cvt_f32_i32 v114, v114\n"
    "v_cvt_f32_i32 v115, v115\n"
    "v_cvt_f32_i32 v116, v116\n"
    "v_cvt_f32_i32 v117, v117\n"
    "v_cvt_f32_i32 v118, v118\n"
    "v_cvt_f32_i32 v119, v119\n"
    "v_cvt_f32_i32 v120, v120\n"
    "v_cvt_f32_i32 v121, v121\n"
    "v_cvt_f32_i32 v122, v122\n"
    "v_cvt_f32_i32 v123, v123\n"
    "v_fma_f32 v100, v100, v180, v184\n"
    "v_fma_f32 v101, v101, v181, v185\n"
    "v_fma_f32 v102, v102, v182, v186\n"
    "v_fma_f32 v103, v103, v183, v187\n"
    "v_fma_f32 v104, v104, v180, v184\n"
    "v_fma_f32 v105, v105, v181, v185\n"
    "s_add_u32 m0, s65, 78848\n"
    "s_add_u32 s72, s85, 11264\n"
    "buffer_load_dwordx4 v90, s[20:23], s72 offen offset:0 nt lds\n"
    "v_fma_f32 v106, v106, v182, v186\n"
    "v_fma_f32 v107, v107, v183, v187\n"
    "v_cvt_pk_bf16_f32 v100, v100, v101\n"
    "v_cvt_pk_bf16_f32 v101, v102, v103\n"
    "v_cvt_pk_bf16_f32 v102, v104, v105\n"
    "v_cvt_pk_bf16_f32 v103, v106, v107\n"
    "ds_write_b64 v163, v[100:101] offset:12288\n"
    "ds_write_b64 v163, v[102:103] offset:13312\n"
    "v_fma_f32 v108, v108, v188, v192\n"
    "v_fma_f32 v109, v109, v189, v193\n"
    "v_fma_f32 v110, v110, v190, v194\n"
    "v_fma_f32 v111, v111, v191, v195\n"
    "v_fma_f32 v112, v112, v188, v192\n"
    "v_fma_f32 v113, v113, v189, v193\n"
    "v_fma_f32 v114, v114, v190, v194\n"
    "v_fma_f32 v115, v115, v191, v195\n"
    "v_cvt_pk_bf16_f32 v108, v108, v109\n"
    "v_cvt_pk_bf16_f32 v109, v110, v111\n"
    "v_cvt_pk_bf16_f32 v110, v112, v113\n"
    "v_cvt_pk_bf16_f32 v111, v114, v115\n"
    "ds_write_b64 v163, v[108:109] offset:14336\n"
    "ds_write_b64 v163, v[110:111] offset:15360\n"
    "v_fma_f32 v116, v116, v196, v200\n"
    "v_fma_f32 v117, v117, v197, v201\n"
    "v_fma_f32 v118, v118, v198, v202\n"
    "v_fma_f32 v119, v119, v199, v203\n"
    "v_fma_f32 v120, v120, v196, v200\n"
    "v_fma_f32 v121, v121, v197, v201\n"
    "v_fma_f32 v122, v122, v198, v202\n"
    "v_fma_f32 v123, v123, v199, v203\n"
    "v_cvt_pk_bf16_f32 v116, v116, v117\n"
    "v_cvt_pk_bf16_f32 v117, v118, v119\n"
    "v_cvt_pk_bf16_f32 v118, v120, v121\n"
    "v_cvt_pk_bf16_f32 v119, v122, v123\n"
    "ds_write_b64 v163, v[116:117] offset:16384\n"
    "ds_write_b64 v163, v[118:119] offset:17408\n"
    "s_waitcnt vmcnt(21)\n"
    "ds_read_b128 a[192:195], v154 offset:0\n"
    "ds_read_b128 a[196:199], v155 offset:0\n"
    "ds_read_b128 a[200:203], v154 offset:128\n"
    "ds_read_b128 a[204:207], v155 offset:128\n"
    "ds_read_b128 a[208:211], v154 offset:256\n"
    "ds_read_b128 a[212:215], v155 offset:256\n"
    "s_waitcnt lgkmcnt(0)\n"
    "s_barrier\n"
    "ds_read_b128 v[156:159], v2 offset:0\n"
    "ds_read_b128 v[160:163], v2 offset:2048\n"
    "ds_read_b128 v[164:167], v2 offset:4096\n"
    "ds_read_b128 v[168:171], v2 offset:6144\n"
    "ds_read_b128 v[172:175], v2 offset:8192\n"
    "ds_read_b128 v[176:179], v2 offset:10240\n"
    "ds_read_b128 v[180:183], v2 offset:1024\n"
    "ds_read_b128 v[184:187], v2 offset:3072\n"
    "ds_read_b128 v[188:191], v2 offset:5120\n"
    "ds_read_b128 v[192:195], v2 offset:7168\n"
    "ds_read_b128 v[196:199], v2 offset:9216\n"
    "ds_read_b128 v[200:203], v2 offset:11264\n"
    "s_add_u32 m0, s65, 49152\n"
    "s_add_u32 s72, s85, 12288\n"
    "buffer_load_dwordx4 v88, s[20:23], s72 offen offset:0 nt lds\n"
    "v_mov_b32 v108, 0\n"
    "v_mov_b32 v109, 0\n"
    "v_mov_b32 v110, 0\n"
    "v_mov_b32 v111, 0\n"
    "v_mov_b32 v112, 0\n"
    "v_mov_b32 v113, 0\n"
    "v_mov_b32 v114, 0\n"
    "v_mov_b32 v115, 0\n"
    "v_mov_b32 v116, 0\n"
    "v_mov_b32 v117, 0\n"
    "v_mov_b32 v118, 0\n"
    "v_mov_b32 v119, 0\n"
    "v_mov_b32 v120, 0\n"
    "v_mov_b32 v121, 0\n"
    "v_mov_b32 v122, 0\n"
    "v_mov_b32 v123, 0\n"
    "s_waitcnt lgkmcnt(11)\n"
    "v_mfma_f32_16x16x32_bf16 v[4:7], v[156:159], a[192:195], 0\n"
    "s_waitcnt lgkmcnt(10)\n"
    "v_mfma_f32_16x16x32_bf16 v[4:7], v[160:163], a[196:199], v[4:7]\n"
    "s_waitcnt lgkmcnt(9)\n"
    "v_mfma_f32_16x16x32_bf16 v[4:7], v[164:167], a[200:203], v[4:7]\n"
    "s_waitcnt lgkmcnt(8)\n"
    "v_mfma_f32_16x16x32_bf16 v[4:7], v[168:171], a[204:207], v[4:7]\n"
    "s_waitcnt lgkmcnt(7)\n"
    "v_mfma_f32_16x16x32_bf16 v[4:7], v[172:175], a[208:211], v[4:7]\n"
    "s_waitcnt lgkmcnt(6)\n"
    "v_mfma_f32_16x16x32_bf16 v[4:7], v[176:179], a[212:215], v[4:7]\n"
    "s_waitcnt lgkmcnt(5)\n"
    "v_mfma_f32_16x16x32_bf16 v[20:23], v[180:183], a[192:195], 0\n"
    "s_waitcnt lgkmcnt(4)\n"
    "v_mfma_f32_16x16x32_bf16 v[20:23], v[184:187], a[196:199], v[20:23]\n"
    "s_waitcnt lgkmcnt(3)\n"
    "v_mfma_f32_16x16x32_bf16 v[20:23], v[188:191], a[200:203], v[20:23]\n"
    "s_waitcnt lgkmcnt(2)\n"
    "v_mfma_f32_16x16x32_bf16 v[20:23], v[192:195], a[204:207], v[20:23]\n"
    "s_waitcnt lgkmcnt(1)\n"
    "v_mfma_f32_16x16x32_bf16 v[20:23], v[196:199], a[208:211], v[20:23]\n"
    "s_waitcnt lgkmcnt(0)\n"
    "v_mfma_f32_16x16x32_bf16 v[20:23], v[200:203], a[212:215], v[20:23]\n"
    "s_add_u32 m0, s65, 50176\n"
    "s_add_u32 s72, s85, 13312\n"
    "buffer_load_dwordx4 v89, s[20:23], s72 offen offset:0 nt lds\n"
    "v_mov_b32 v77, 155904\n"
    "v_max3_f32 v76, v4, v5, v6\n"
    "s_nop 2\n"
    "v_max3_f32 v76, v76, v7, v20\n"
    "v_max3_f32 v76, v76, v21, v22\n"
    "v_max_f32 v76, v76, v23\n"
    "v_mov_b32 v78, v76\n"
    "s_nop 1\n"
    "v_permlane32_swap_b32 v76, v78\n"
    "v_max_f32 v76, v76, v78\n"
    "v_mov_b32 v78, v76\n"
    "s_nop 1\n"
    "v_permlane16_swap_b32 v76, v78\n"
    "v_max_f32 v76, v76, v78\n"
    "ds_max_f32 v77, v76\n"
    "s_add_u32 m0, s65, 51200\n"
    "s_add_u32 s72, s85, 14336\n"
    "buffer_load_dwordx4 v90, s[20:23], s72 offen offset:0 nt lds\n"
    "buffer_load_dwordx2 v[236:237], v87, s[36:39], 0 offen offset:0 sc1\n"
    "s_waitcnt lgkmcnt(0)\n"
    "s_barrier\n"
    "ds_read_b32 v76, v77\n"
    "s_waitcnt vmcnt(10)\n"
    "buffer_store_dwordx2 v[238:239], v240, s[36:39], s80 offen offset:0 sc1\n"
    "s_lshl_b32 s73, s79, 8\n"
    "s_add_u32 s80, s80, s73\n"
    "s_mul_i32 s73, s79, 98304\n"
    "s_add_u32 s71, s71, s73\n"
    "s_waitcnt lgkmcnt(0)\n"
    "v_add_f32 v76, 0xc1800000, v76\n"
    "v_sub_f32 v68, v68, v76\n"
    "v_sub_f32 v69, v69, v76\n"
    "v_sub_f32 v70, v70, v76\n"
    "v_sub_f32 v71, v71, v76\n"
    "v_sub_f32 v72, v72, v76\n"
    "v_sub_f32 v73, v73, v76\n"
    "v_sub_f32 v74, v74, v76\n"
    "v_sub_f32 v75, v75, v76\n"
    "ds_write_b128 v86, v[68:71] offset:0\n"
    "ds_write_b128 v86, v[72:75] offset:16\n"
    "s_mul_i32 s73, s79, 768\n"
    "v_add_u32 v77, s73, v250\n"
    "s_add_u32 m0, s65, 52224\n"
    "s_add_u32 s72, s85, 15360\n"
    "buffer_load_dwordx4 v88, s[20:23], s72 offen offset:0 nt lds\n"
    "s_waitcnt lgkmcnt(0)\n"
    "s_barrier\n"
    "ds_read_b128 v[100:103], v77 offset:0\n"
    "ds_read_b128 v[104:107], v77 offset:16\n"
    "s_add_u32 m0, s65, 53248\n"
    "s_add_u32 s72, s85, 16384\n"
    "buffer_load_dwordx4 v89, s[20:23], s72 offen offset:0 nt lds\n"
    "s_waitcnt lgkmcnt(1)\n"
    "v_add_f32 v4, v4, v100\n"
    "v_add_f32 v5, v5, v101\n"
    "v_add_f32 v6, v6, v102\n"
    "v_add_f32 v7, v7, v103\n"
    "s_waitcnt lgkmcnt(0)\n"
    "v_add_f32 v20, v20, v104\n"
    "v_add_f32 v21, v21, v105\n"
    "v_add_f32 v22, v22, v106\n"
    "v_add_f32 v23, v23, v107\n"
    "v_exp_f32 v4, v4\n"
    "v_exp_f32 v5, v5\n"
    "v_exp_f32 v6, v6\n"
    "v_exp_f32 v7, v7\n"
    "v_exp_f32 v20, v20\n"
    "v_exp_f32 v21, v21\n"
    "v_exp_f32 v22, v22\n"
    "v_exp_f32 v23, v23\n"
    "s_add_u32 m0, s65, 54272\n"
    "s_add_u32 s72, s85, 17408\n"
    "buffer_load_dwordx4 v90, s[20:23], s72 offen offset:0 nt lds\n"
    "s_waitcnt vmcnt(8)\n"
    "ds_read_b128 a[216:219], v84 offset:24576\n"
    "ds_read_b128 a[220:223], v85 offset:24576\n"
    "ds_read_b128 a[224:227], v84 offset:24704\n"
    "ds_read_b128 a[228:231], v85 offset:24704\n"
    "ds_read_b128 a[232:235], v84 offset:24832\n"
    "ds_read_b128 a[236:239], v85 offset:24832\n"
    "s_waitcnt lgkmcnt(0)\n"
    "v_mfma_f32_16x16x32_bf16 v[8:11], v[176:179], a[236:239], v[100:103]\n"
    "v_mfma_f32_16x16x32_bf16 v[8:11], v[172:175], a[232:235], v[8:11]\n"
    "v_mfma_f32_16x16x32_bf16 v[8:11], v[168:171], a[228:231], v[8:11]\n"
    "v_mfma_f32_16x16x32_bf16 v[8:11], v[164:167], a[224:227], v[8:11]\n"
    "v_mfma_f32_16x16x32_bf16 v[8:11], v[160:163], a[220:223], v[8:11]\n"
    "v_mfma_f32_16x16x32_bf16 v[8:11], v[156:159], a[216:219], v[8:11]\n"
    "v_mfma_f32_16x16x32_bf16 v[24:27], v[200:203], a[236:239], v[104:107]\n"
    "v_mfma_f32_16x16x32_bf16 v[24:27], v[196:199], a[232:235], v[24:27]\n"
    "v_mfma_f32_16x16x32_bf16 v[24:27], v[192:195], a[228:231], v[24:27]\n"
    "v_mfma_f32_16x16x32_bf16 v[24:27], v[188:191], a[224:227], v[24:27]\n"
    "v_mfma_f32_16x16x32_bf16 v[24:27], v[184:187], a[220:223], v[24:27]\n"
    "v_mfma_f32_16x16x32_bf16 v[24:27], v[180:183], a[216:219], v[24:27]\n"
    "s_add_u32 m0, s65, 98304\n"
    "s_add_u32 s72, s85, 18432\n"
    "buffer_load_dwordx4 v88, s[20:23], s72 offen offset:0 nt lds\n"
    "v_exp_f32 v8, v8\n"
    "v_exp_f32 v9, v9\n"
    "v_exp_f32 v10, v10\n"
    "v_exp_f32 v11, v11\n"
    "s_nop 0\n"
    "v_exp_f32 v24, v24\n"
    "v_exp_f32 v25, v25\n"
    "v_exp_f32 v26, v26\n"
    "v_exp_f32 v27, v27\n"
    "s_add_u32 m0, s65, 99328\n"
    "s_add_u32 s72, s85, 19456\n"
    "buffer_load_dwordx4 v89, s[20:23], s72 offen offset:0 nt lds\n"
    "s_add_u32 m0, s65, 100352\n"
    "s_add_u32 s72, s85, 20480\n"
    "buffer_load_dwordx4 v90, s[20:23], s72 offen offset:0 nt lds\n"
    "s_waitcnt vmcnt(7)\n"
    "v_cmp_eq_u64 s[54:55], v[236:237], v[238:239]\n"
    "s_nop 0\n"
    "s_cmp_eq_u64 s[54:55], -1\n"
    "s_cbranch_scc0 .LpollAK\n"
    ".LpollAKr:\n"
    "s_add_u32 m0, s65, 101376\n"
    "s_add_u32 s72, s85, 21504\n"
    "buffer_load_dwordx4 v88, s[20:23], s72 offen offset:0 nt lds\n"
    "s_add_u32 m0, s65, 24576\n"
    "s_add_u32 s72, s65, 49152\n"
    "buffer_load_dwordx4 v1, s[16:19], s72 offen offset:0 sc1 lds\n"
    "s_add_u32 m0, s65, 25600\n"
    "s_add_u32 s72, s65, 50176\n"
    "buffer_load_dwordx4 v1, s[16:19], s72 offen offset:0 sc1 lds\n"
    "s_add_u32 m0, s65, 102400\n"
    "s_add_u32 s72, s85, 22528\n"
    "buffer_load_dwordx4 v89, s[20:23], s72 offen offset:0 nt lds\n"
    "s_add_u32 m0, s65, 26624\n"
    "s_add_u32 s72, s65, 51200\n"
    "buffer_load_dwordx4 v1, s[16:19], s72 offen offset:0 sc1 lds\n"
    "s_add_u32 m0, s65, 27648\n"
    "s_add_u32 s72, s65, 52224\n"
    "buffer_load_dwordx4 v1, s[16:19], s72 offen offset:0 sc1 lds\n"
    "s_add_u32 m0, s65, 103424\n"
    "s_add_u32 s72, s85, 23552\n"
    "buffer_load_dwordx4 v90, s[20:23], s72 offen offset:0 nt lds\n"
    "s_add_u32 m0, s65, 28672\n"
    "s_add_u32 s72, s65, 53248\n"
    "buffer_load_dwordx4 v1, s[16:19], s72 offen offset:0 sc1 lds\n"
    "s_add_u32 m0, s65, 29696\n"
    "s_add_u32 s72, s65, 54272\n"
    "buffer_load_dwordx4 v1, s[16:19], s72 offen offset:0 sc1 lds\n"
    "s_add_u32 m0, s65, 73728\n"
    "s_add_u32 s72, s65, 73728\n"
    "buffer_load_dwordx4 v1, s[16:19], s72 offen offset:0 sc1 lds\n"
    "s_add_u32 m0, s65, 74752\n"
    "s_add_u32 s72, s65, 74752\n"
    "buffer_load_dwordx4 v1, s[16:19], s72 offen offset:0 sc1 lds\n"
    "s_add_u32 m0, s65, 75776\n"
    "s_add_u32 s72, s65, 75776\n"
    "buffer_load_dwordx4 v1, s[16:19], s72 offen offset:0 sc1 lds\n"
    "s_add_u32 m0, s65, 76800\n"
    "s_add_u32 s72, s65, 76800\n"
    "buffer_load_dwordx4 v1, s[16:19], s72 offen offset:0 sc1 lds\n"
    "s_add_u32 m0, s65, 77824\n"
    "s_add_u32 s72, s65, 77824\n"
    "buffer_load_dwordx4 v1, s[16:19], s72 offen offset:0 sc1 lds\n"
    "s_add_u32 m0, s65, 78848\n"
    "s_add_u32 s72, s65, 78848\n"
    "buffer_load_dwordx4 v1, s[16:19], s72 offen offset:0 sc1 lds\n"
    "s_bitcmp1_b32 s83, 1\n"
    "s_cbranch_scc1 .Lf3b\n"
    "s_waitcnt vmcnt(18)\n"
    "ds_read_b128 a[240:243], v84 offset:0\n"
    "ds_read_b128 a[244:247], v85 offset:0\n"
    "ds_read_b128 a[248:251], v84 offset:128\n"
    "ds_read_b128 a[252:255], v85 offset:128\n"
    "ds_read_b128 v[124:127], v84 offset:256\n"
    "ds_read_b128 v[128:131], v85 offset:256\n"
    "s_waitcnt lgkmcnt(5)\n"
    "v_mfma_f32_16x16x32_bf16 v[12:15], v[156:159], a[240:243], v[100:103]\n"
    "s_waitcnt lgkmcnt(4)\n"
    "v_mfma_f32_16x16x32_bf16 v[12:15], v[160:163], a[244:247], v[12:15]\n"
    "s_waitcnt lgkmcnt(3)\n"
    "v_mfma_f32_16x16x32_bf16 v[12:15], v[164:167], a[248:251], v[12:15]\n"
    "s_waitcnt lgkmcnt(2)\n"
    "v_mfma_f32_16x16x32_bf16 v[12:15], v[168:171], a[252:255], v[12:15]\n"
    "s_waitcnt lgkmcnt(1)\n"
    "v_mfma_f32_16x16x32_bf16 v[12:15], v[172:175], v[124:127], v[12:15]\n"
    "s_waitcnt lgkmcnt(0)\n"
    "v_mfma_f32_16x16x32_bf16 v[12:15], v[176:179], v[128:131], v[12:15]\n"
    "v_mfma_f32_16x16x32_bf16 v[28:31], v[180:183], a[240:243], v[104:107]\n"
    "v_mfma_f32_16x16x32_bf16 v[28:31], v[184:187], a[244:247], v[28:31]\n"
    "v_mfma_f32_16x16x32_bf16 v[28:31], v[188:191], a[248:251], v[28:31]\n"
    "v_mfma_f32_16x16x32_bf16 v[28:31], v[192:195], a[252:255], v[28:31]\n"
    "v_mfma_f32_16x16x32_bf16 v[28:31], v[196:199], v[124:127], v[28:31]\n"
    "v_mfma_f32_16x16x32_bf16 v[28:31], v[200:203], v[128:131], v[28:31]\n"
    "s_nop 1\n"
    "v_exp_f32 v12, v12\n"
    "v_exp_f32 v13, v13\n"
    "v_exp_f32 v14, v14\n"
    "v_exp_f32 v15, v15\n"
    "s_nop 1\n"
    "v_exp_f32 v28, v28\n"
    "v_exp_f32 v29, v29\n"
    "v_exp_f32 v30, v30\n"
    "v_exp_f32 v31, v31\n"
    "s_waitcnt vmcnt(8)\n"
    "ds_read_b128 v[132:135], v84 offset:49152\n"
    "ds_read_b128 v[136:139], v85 offset:49152\n"
    "ds_read_b128 v[140:143], v84 offset:49280\n"
    "ds_read_b128 v[144:147], v85 offset:49280\n"
    "ds_read_b128 v[148:151], v84 offset:49408\n"
    "ds_read_b128 v[152:155], v85 offset:49408\n"
    "s_waitcnt lgkmcnt(0)\n"
    "v_mfma_f32_16x16x32_bf16 v[16:19], v[176:179], v[152:155], v[100:103]\n"
    "v_mfma_f32_16x16x32_bf16 v[16:19], v[172:175], v[148:151], v[16:19]\n"
    "v_mfma_f32_16x16x32_bf16 v[16:19], v[168:171], v[144:147], v[16:19]\n"
    "v_mfma_f32_16x16x32_bf16 v[16:19], v[164:167], v[140:143], v[16:19]\n"
    "v_mfma_f32_16x16x32_bf16 v[16:19], v[160:163], v[136:139], v[16:19]\n"
    "v_mfma_f32_16x16x32_bf16 v[16:19], v[156:159], v[132:135], v[16:19]\n"
    "v_mfma_f32_16x16x32_bf16 v[32:35], v[200:203], v[152:155], v[104:107]\n"
    "v_mfma_f32_16x16x32_bf16 v[32:35], v[196:199], v[148:151], v[32:35]\n"
    "v_mfma_f32_16x16x32_bf16 v[32:35], v[192:195], v[144:147], v[32:35]\n"
    "v_mfma_f32_16x16x32_bf16 v[32:35], v[188:191], v[140:143], v[32:35]\n"
    "v_mfma_f32_16x16x32_bf16 v[32:35], v[184:187], v[136:139], v[32:35]\n"
    "v_mfma_f32_16x16x32_bf16 v[32:35], v[180:183], v[132:135], v[32:35]\n"
    "ds_read_b128 v[156:159], v2 offset:12288\n"
    "ds_read_b128 v[160:163], v2 offset:14336\n"
    "ds_read_b128 v[164:167], v2 offset:16384\n"
    "ds_read_b128 v[168:171], v2 offset:18432\n"
    "ds_read_b128 v[172:175], v2 offset:20480\n"
    "ds_read_b128 v[176:179], v2 offset:22528\n"
    "ds_read_b128 v[100:103], v77 offset:128\n"
    "v_exp_f32 v16, v16\n"
    "v_exp_f32 v17, v17\n"
    "v_exp_f32 v18, v18\n"
    "v_exp_f32 v19, v19\n"
    "v_exp_f32 v32, v32\n"
    "v_exp_f32 v33, v33\n"
    "v_exp_f32 v34, v34\n"
    "v_exp_f32 v35, v35\n"
    "s_mov_b32 s66, 0\n"
    "s_waitcnt lgkmcnt(0)\n"
    "s_waitcnt vmcnt(0)\n"
    "ds_add_u32 v247, v246\n"
    "s_nop 7\n"
    "s_branch .LPK\n"
    "s_branch .Lf3e\n"
    ".Lf3b:\n"
    "s_add_u32 s75, s71, s63\n"
    "s_add_u32 s76, s75, 6144\n"
    "s_add_u32 s77, s75, 24576\n"
    "s_add_u32 s78, s75, 30720\n"
    "buffer_load_dwordx2 v[204:205], v242, s[28:31], s75 offen offset:0 nt\n"
    "buffer_load_dwordx2 v[206:207], v242, s[28:31], s75 offen offset:768 nt\n"
    "buffer_load_dwordx2 v[208:209], v242, s[28:31], s75 offen offset:3072 nt\n"
    "buffer_load_dwordx2 v[210:211], v242, s[28:31], s75 offen offset:3840 nt\n"
    "buffer_load_dwordx2 v[212:213], v242, s[28:31], s76 offen offset:0 nt\n"
    "buffer_load_dwordx2 v[214:215], v242, s[28:31], s76 offen offset:768 nt\n"
    "buffer_load_dwordx2 v[216:217], v242, s[28:31], s76 offen offset:3072 nt\n"
    "buffer_load_dwordx2 v[218:219], v242, s[28:31], s76 offen offset:3840 nt\n"
    "buffer_load_dwordx2 v[220:221], v242, s[28:31], s77 offen offset:0 nt\n"
    "buffer_load_dwordx2 v[222:223], v242, s[28:31], s77 offen offset:768 nt\n"
    "buffer_load_dwordx2 v[224:225], v242, s[28:31], s77 offen offset:3072 nt\n"
    "buffer_load_dwordx2 v[226:227], v242, s[28:31], s77 offen offset:3840 nt\n"
    "buffer_load_dwordx2 v[228:229], v242, s[28:31], s78 offen offset:0 nt\n"
    "buffer_load_dwordx2 v[230:231], v242, s[28:31], s78 offen offset:768 nt\n"
    "buffer_load_dwordx2 v[232:233], v242, s[28:31], s78 offen offset:3072 nt\n"
    "buffer_load_dwordx2 v[234:235], v242, s[28:31], s78 offen offset:3840 nt\n"
    "s_waitcnt vmcnt(34)\n"
    "ds_read_b128 a[240:243], v84 offset:0\n"
    "ds_read_b128 a[244:247], v85 offset:0\n"
    "ds_read_b128 a[248:251], v84 offset:128\n"
    "ds_read_b128 a[252:255], v85 offset:128\n"
    "ds_read_b128 v[124:127], v84 offset:256\n"
    "ds_read_b128 v[128:131], v85 offset:256\n"
    "s_waitcnt lgkmcnt(5)\n"
    "v_mfma_f32_16x16x32_bf16 v[12:15], v[156:159], a[240:243], v[100:103]\n"
    "s_waitcnt lgkmcnt(4)\n"
    "v_mfma_f32_16x16x32_bf16 v[12:15], v[160:163], a[244:247], v[12:15]\n"
    "s_waitcnt lgkmcnt(3)\n"
    "v_mfma_f32_16x16x32_bf16 v[12:15], v[164:167], a[248:251], v[12:15]\n"
    "s_waitcnt lgkmcnt(2)\n"
    "v_mfma_f32_16x16x32_bf16 v[12:15], v[168:171], a[252:255], v[12:15]\n"
    "s_waitcnt lgkmcnt(1)\n"
    "v_mfma_f32_16x16x32_bf16 v[12:15], v[172:175], v[124:127], v[12:15]\n"
    "s_waitcnt lgkmcnt(0)\n"
    "v_mfma_f32_16x16x32_bf16 v[12:15], v[176:179], v[128:131], v[12:15]\n"
    "v_mfma_f32_16x16x32_bf16 v[28:31], v[180:183], a[240:243], v[104:107]\n"
    "v_mfma_f32_16x16x32_bf16 v[28:31], v[184:187], a[244:247], v[28:31]\n"
    "v_mfma_f32_16x16x32_bf16 v[28:31], v[188:191], a[248:251], v[28:31]\n"
    "v_mfma_f32_16x16x32_bf16 v[28:31], v[192:195], a[252:255], v[28:31]\n"
    "v_mfma_f32_16x16x32_bf16 v[28:31], v[196:199], v[124:127], v[28:31]\n"
    "v_mfma_f32_16x16x32_bf16 v[28:31], v[200:203], v[128:131], v[28:31]\n"
    "s_nop 1\n"
    "v_exp_f32 v12, v12\n"
    "v_exp_f32 v13, v13\n"
    "v_exp_f32 v14, v14\n"
    "v_exp_f32 v15, v15\n"
    "s_nop 1\n"
    "v_exp_f32 v28, v28\n"
    "v_exp_f32 v29, v29\n"
    "v_exp_f32 v30, v30\n"
    "v_exp_f32 v31, v31\n"
    "s_waitcnt vmcnt(24)\n"
    "ds_read_b128 v[132:135], v84 offset:49152\n"
    "ds_read_b128 v[136:139], v85 offset:49152\n"
    "ds_read_b128 v[140:143], v84 offset:49280\n"
    "ds_read_b128 v[144:147], v85 offset:49280\n"
    "ds_read_b128 v[148:151], v84 offset:49408\n"
    "ds_read_b128 v[152:155], v85 offset:49408\n"
    "s_waitcnt lgkmcnt(0)\n"
    "v_mfma_f32_16x16x32_bf16 v[16:19], v[176:179], v[152:155], v[100:103]\n"
    "v_mfma_f32_16x16x32_bf16 v[16:19], v[172:175], v[148:151], v[16:19]\n"
    "v_mfma_f32_16x16x32_bf16 v[16:19], v[168:171], v[144:147], v[16:19]\n"
    "v_mfma_f32_16x16x32_bf16 v[16:19], v[164:167], v[140:143], v[16:19]\n"
    "v_mfma_f32_16x16x32_bf16 v[16:19], v[160:163], v[136:139], v[16:19]\n"
    "v_mfma_f32_16x16x32_bf16 v[16:19], v[156:159], v[132:135], v[16:19]\n"
    "v_mfma_f32_16x16x32_bf16 v[32:35], v[200:203], v[152:155], v[104:107]\n"
    "v_mfma_f32_16x16x32_bf16 v[32:35], v[196:199], v[148:151], v[32:35]\n"
    "v_mfma_f32_16x16x32_bf16 v[32:35], v[192:195], v[144:147], v[32:35]\n"
    "v_mfma_f32_16x16x32_bf16 v[32:35], v[188:191], v[140:143], v[32:35]\n"
    "v_mfma_f32_16x16x32_bf16 v[32:35], v[184:187], v[136:139], v[32:35]\n"
    "v_mfma_f32_16x16x32_bf16 v[32:35], v[180:183], v[132:135], v[32:35]\n"
    "ds_read_b128 v[156:159], v2 offset:12288\n"
    "ds_read_b128 v[160:163], v2 offset:14336\n"
    "ds_read_b128 v[164:167], v2 offset:16384\n"
    "ds_read_b128 v[168:171], v2 offset:18432\n"
    "ds_read_b128 v[172:175], v2 offset:20480\n"
    "ds_read_b128 v[176:179], v2 offset:22528\n"
    "ds_read_b128 v[100:103], v77 offset:128\n"
    "v_exp_f32 v16, v16\n"
    "v_exp_f32 v17, v17\n"
    "v_exp_f32 v18, v18\n"
    "v_exp_f32 v19, v19\n"
    "v_exp_f32 v32, v32\n"
    "v_exp_f32 v33, v33\n"
    "v_exp_f32 v34, v34\n"
    "v_exp_f32 v35, v35\n"
    "s_mov_b32 s66, 0\n"
    "s_waitcnt lgkmcnt(0)\n"
    "s_waitcnt vmcnt(16)\n"
    "ds_add_u32 v247, v246\n"
    "s_nop 7\n"
    "s_branch .LNK\n"
    ".Lf3e:\n"
    "s_nop 4\n"
    "s_branch .Lf5e\n"
    ".Lf5b:\n"
    "s_lshl_b32 s73, s79, 8\n"
    "s_mul_i32 s74, s59, 6144\n"
    "s_add_u32 s73, s73, s74\n"
    "v_add_u32 v163, s73, v244\n"
    "s_lshr_b32 s73, s53, 2\n"
    "s_mul_i32 s73, s73, 12288\n"
    "s_and_b32 s74, s53, 3\n"
    "s_lshl_b32 s74, s74, 8\n"
    "s_add_u32 s73, s73, s74\n"
    "s_cmp_eq_u32 s59, 0\n"
    "s_cselect_b32 s73, 0x7f000000, s73\n"
    "s_add_u32 s60, s73, 24576\n"
    "s_add_u32 s61, s73, 28672\n"
    "s_add_u32 s62, s73, 32768\n"
    "v_mov_b32 v244, v243\n"
    "buffer_load_dwordx4 v[40:43], v242, s[40:43], s75 offen offset:768 nt\n"
    "v_add_u32 v159, 0, v156\n"
    "v_mul_u32_u24 v159, 2731, v159\n"
    "v_lshrrev_b32 v159, 16, v159\n"
    "v_xor_b32 v159, v159, v156\n"
    "v_and_b32 v159, 7, v159\n"
    "v_and_b32 v88, 0x380, v1\n"
    "v_lshl_add_u32 v88, v159, 4, v88\n"
    "v_add_u32 v159, 64, v156\n"
    "v_mul_u32_u24 v159, 2731, v159\n"
    "v_lshrrev_b32 v159, 16, v159\n"
    "v_xor_b32 v159, v159, v156\n"
    "v_and_b32 v159, 7, v159\n"
    "v_and_b32 v89, 0x380, v1\n"
    "v_lshl_add_u32 v89, v159, 4, v89\n"
    "v_add_u32 v159, 128, v156\n"
    "v_mul_u32_u24 v159, 2731, v159\n"
    "v_lshrrev_b32 v159, 16, v159\n"
    "v_xor_b32 v159, v159, v156\n"
    "v_and_b32 v159, 7, v159\n"
    "v_and_b32 v90, 0x380, v1\n"
    "v_lshl_add_u32 v90, v159, 4, v90\n"
    "v_and_b32 v2, 7, v156\n"
    "v_lshrrev_b32 v144, 3, %3\n"
    "v_mul_u32_u24 v144, 768, v144\n"
    "v_lshl_add_u32 v144, v2, 3, v144\n"
    "buffer_load_dwordx4 v[44:47], v242, s[40:43], s75 offen offset:256 nt\n"
    "v_add_u32 v144, 131072, v144\n"
    "v_mad_u32_u24 v86, v2, 24, v144\n"
    "v_and_b32 v84, 15, v156\n"
    "v_lshrrev_b32 v159, 4, v156\n"
    "v_xor_b32 v159, v159, v2\n"
    "v_mul_u32_u24 v84, 384, v84\n"
    "v_lshl_add_u32 v84, v159, 4, v84\n"
    "s_add_u32 s73, s65, 49152\n"
    "v_add_u32 v84, s73, v84\n"
    "v_xor_b32 v85, 64, v84\n"
    "v_subrev_u32 v154, 24576, v84\n"
    "v_subrev_u32 v155, 24576, v85\n"
    "s_add_u32 s67, s65, 98304\n"
    "s_add_u32 s68, s65, 24576\n"
    "s_add_u32 s69, s65, 73728\n"
    "s_mov_b32 s81, 256\n"
    "s_mul_i32 s74, s79, 24576\n"
    "s_add_u32 s70, s74, s65\n"
    "s_add_u32 s70, s70, 73728\n"
    "s_sub_u32 s64, s65, s74\n"
    "s_add_u32 s64, s64, 98304\n"
    "v_and_b32 v249, 0x310, v1\n"
    "v_and_b32 v159, 12, v156\n"
    "v_lshl_add_u32 v249, v159, 3, v249\n"
    "v_and_b32 v159, 2, v156\n"
    "buffer_load_dwordx4 v[48:51], v242, s[40:43], s75 offen offset:1024 nt\n"
    "v_lshl_add_u32 v249, v159, 6, v249\n"
    "s_add_u32 s73, s74, 73728\n"
    "v_add_u32 v248, s73, v249\n"
    "s_sub_u32 s73, 98304, s74\n"
    "v_add_u32 v249, s73, v249\n"
    "s_or_b32 s73, s53, s84\n"
    "s_cmp_eq_u32 s73, 0\n"
    "s_cselect_b32 s82, 0, 0x7fff0000\n"
    "v_and_b32 v252, 12, v156\n"
    "v_bfe_u32 v159, v156, 1, 1\n"
    "v_or_b32 v252, v252, v159\n"
    "v_and_b32 v159, 1, v156\n"
    "v_lshl_or_b32 v252, v159, 1, v252\n"
    "v_lshlrev_b32 v252, 6, v252\n"
    "v_lshlrev_b32 v159, 2, v156\n"
    "v_and_b32 v159, 32, v159\n"
    "v_xor_b32 v159, v159, v156\n"
    "v_and_or_b32 v252, v159, 48, v252\n"
    "v_mov_b32 v2, v252\n"
    "v_add_u32 v3, 24576, v252\n"
    "v_lshrrev_b32 v91, 3, v156\n"
    "v_lshl_add_u32 v87, v91, 6, 64\n"
    "v_add_u32 v87, 64, v87\n"
    "v_and_b32 v241, 1, v156\n"
    "v_lshlrev_b32 v241, 3, v241\n"
    "buffer_load_dwordx4 v[52:55], v242, s[40:43], s75 offen offset:512 nt\n"
    "v_lshl_add_u32 v241, v91, 4, v241\n"
    "v_add_u32 v241, 1152, v241\n"
    "v_lshlrev_b32 v91, 24, v91\n"
    "v_lshl_add_u32 v91, v156, 3, v91\n"
    "s_lshl_b32 s73, s53, 4\n"
    "s_lshl_b32 s74, s59, 3\n"
    "s_add_u32 s73, s73, s74\n"
    "s_add_u32 s73, s73, 1152\n"
    "v_mov_b32 v240, 0x7fff0000\n"
    "v_writelane_b32 v240, s73, 0\n"
    "v_and_b32 v159, 48, v156\n"
    "s_mul_i32 s73, s79, 768\n"
    "s_sub_u32 s73, 131072, s73\n"
    "v_lshlrev_b32 v250, 1, v159\n"
    "v_add_u32 v250, s73, v250\n"
    "v_and_b32 v251, 3, v156\n"
    "v_lshl_add_u32 v251, v251, 7, v159\n"
    "s_add_u32 s73, s73, 256\n"
    "v_add_u32 v251, s73, v251\n"
    "v_and_b32 v159, 64, v1\n"
    "v_add_u32 v251, v159, v251\n"
    "v_bfe_i32 v159, v156, 2, 1\n"
    "v_and_b32 v159, 0xaaaa, v159\n"
    "v_or_b32 v253, 0x4444, v159\n"
    "s_mov_b32 s58, 0\n"
    "s_mov_b32 s59, 8\n"
    "s_waitcnt vmcnt(14)\n"
    "v_pk_min_i16 v4, v4, v10\n"
    "v_pk_min_i16 v5, v5, v11\n"
    "v_pk_min_i16 v6, v6, v12\n"
    "v_pk_min_i16 v7, v7, v13\n"
    "v_pk_min_i16 v8, v8, v14\n"
    "v_pk_min_i16 v9, v9, v15\n"
    "v_pk_min_i16 v16, v16, v18\n"
    "v_pk_min_i16 v17, v17, v19\n"
    "v_pk_min_i16 v4, v4, v7\n"
    "v_pk_min_i16 v5, v5, v8\n"
    "v_pk_min_i16 v6, v6, v9\n"
    "v_pk_min_i16 v16, v16, v17\n"
    "v_pk_min_i16 v4, v4, v5\n"
    "v_lshlrev_b32 v17, 16, v16\n"
    "v_pk_min_i16 v4, v4, v6\n"
    "v_min_i32 v16, v16, v17\n"
    "v_lshlrev_b32 v5, 16, v4\n"
    "v_mov_b32 v7, 155920\n"
    "v_min_i32 v4, v4, v5\n"
    "v_lshl_add_u32 v6, s84, 2, v7\n"
    "v_min_i32_dpp v16, v16, v16 row_shr:1\n"
    "v_min_i32_dpp v4, v4, v4 row_shr:1\n"
    "s_nop 0\n"
    "v_min_i32_dpp v16, v16, v16 row_shr:2\n"
    "v_min_i32_dpp v4, v4, v4 row_shr:2\n"
    "s_nop 0\n"
    "v_min_i32_dpp v16, v16, v16 row_shr:4\n"
    "v_min_i32_dpp v4, v4, v4 row_shr:4\n"
    "s_nop 0\n"
    "v_min_i32_dpp v16, v16, v16 row_shr:8\n"
    "v_min_i32_dpp v4, v4, v4 row_shr:8\n"
    "s_nop 0\n"
    "v_min_i32_dpp v16, v16, v16 row_bcast:15 row_mask:0xa\n"
    "v_min_i32_dpp v4, v4, v4 row_bcast:15 row_mask:0xa\n"
    "s_nop 0\n"
    "v_min_i32_dpp v16, v16, v16 row_bcast:31 row_mask:0xc\n"
    "v_min_i32_dpp v4, v4, v4 row_bcast:31 row_mask:0xc\n"
    "v_readlane_b32 s35, v16, 63\n"
    "v_readlane_b32 s73, v4, 63\n"
    "s_max_i32 s35, s35, 0\n"
    "s_and_b32 s35, s35, 0xffff0000\n"
    "v_mov_b32 v4, s73\n"
    "ds_write_b32 v6, v4\n"
    "s_waitcnt lgkmcnt(0)\n"
    "s_barrier\n"
    "ds_read_b128 v[8:11], v7 offset:0\n"
    "buffer_load_dwordx4 v[56:59], v242, s[40:43], s75 offen offset:1280 nt\n"
    "buffer_load_dwordx4 v[136:139], v130, s[92:95], 0 offen offset:0\n"
    "buffer_load_dwordx4 v[140:143], v130, s[96:99], 0 offen offset:0\n"
    "v_accvgpr_write_b32 a0, 0\n"
    "v_accvgpr_write_b32 a1, 0\n"
    "v_accvgpr_write_b32 a2, 0\n"
    "v_accvgpr_write_b32 a3, 0\n"
    "v_accvgpr_write_b32 a4, 0\n"
    "v_accvgpr_write_b32 a5, 0\n"
    "v_accvgpr_write_b32 a6, 0\n"
    "v_accvgpr_write_b32 a7, 0\n"
    "v_accvgpr_write_b32 a8, 0\n"
    "v_accvgpr_write_b32 a9, 0\n"
    "v_accvgpr_write_b32 a10, 0\n"
    "v_accvgpr_write_b32 a11, 0\n"
    "v_accvgpr_write_b32 a12, 0\n"
    "v_accvgpr_write_b32 a13, 0\n"
    "v_accvgpr_write_b32 a14, 0\n"
    "v_accvgpr_write_b32 a15, 0\n"
    "v_accvgpr_write_b32 a16, 0\n"
    "v_accvgpr_write_b32 a17, 0\n"
    "v_accvgpr_write_b32 a18, 0\n"
    "v_accvgpr_write_b32 a19, 0\n"
    "v_accvgpr_write_b32 a20, 0\n"
    "v_accvgpr_write_b32 a21, 0\n"
    "v_accvgpr_write_b32 a22, 0\n"
    "v_accvgpr_write_b32 a23, 0\n"
    "v_accvgpr_write_b32 a24, 0\n"
    "v_accvgpr_write_b32 a25, 0\n"
    "v_accvgpr_write_b32 a26, 0\n"
    "v_accvgpr_write_b32 a27, 0\n"
    "v_accvgpr_write_b32 a28, 0\n"
    "v_accvgpr_write_b32 a29, 0\n"
    "v_accvgpr_write_b32 a30, 0\n"
    "v_accvgpr_write_b32 a31, 0\n"
    "v_accvgpr_write_b32 a32, 0\n"
    "v_accvgpr_write_b32 a33, 0\n"
    "v_accvgpr_write_b32 a34, 0\n"
    "v_accvgpr_write_b32 a35, 0\n"
    "v_accvgpr_write_b32 a36, 0\n"
    "v_accvgpr_write_b32 a37, 0\n"
    "v_accvgpr_write_b32 a38, 0\n"
    "v_accvgpr_write_b32 a39, 0\n"
    "v_accvgpr_write_b32 a40, 0\n"
    "v_accvgpr_write_b32 a41, 0\n"
    "v_accvgpr_write_b32 a42, 0\n"
    "v_accvgpr_write_b32 a43, 0\n"
    "v_accvgpr_write_b32 a44, 0\n"
    "v_accvgpr_write_b32 a45, 0\n"
    "v_accvgpr_write_b32 a46, 0\n"
    "v_accvgpr_write_b32 a47, 0\n"
    "v_accvgpr_write_b32 a48, 0\n"
    "v_accvgpr_write_b32 a49, 0\n"
    "v_accvgpr_write_b32 a50, 0\n"
    "v_accvgpr_write_b32 a51, 0\n"
    "v_accvgpr_write_b32 a52, 0\n"
    "v_accvgpr_write_b32 a53, 0\n"
    "v_accvgpr_write_b32 a54, 0\n"
    "v_accvgpr_write_b32 a55, 0\n"
    "v_accvgpr_write_b32 a56, 0\n"
    "v_accvgpr_write_b32 a57, 0\n"
    "v_accvgpr_write_b32 a58, 0\n"
    "v_accvgpr_write_b32 a59, 0\n"
    "s_waitcnt lgkmcnt(0)\n"
    "v_min3_i32 v4, v8, v9, v10\n"
    "v_min_i32 v4, v4, v11\n"
    "v_max_i32 v4, 0, v4\n"
    "v_and_b32 v4, 0xffff0000, v4\n"
    "v_mul_f32 v4, s35, v4\n"
    "v_mul_f32 v4, 0x3e7537b8, v4\n"
    "s_nop 0\n"
    "v_readfirstlane_b32 s35, v4\n"
    "s_waitcnt vmcnt(11)\n"
    "s_add_u32 s54, s71, s60\n"
    "s_add_u32 s55, s71, s61\n"
    "s_add_u32 s56, s71, s62\n"
    "v_cvt_f32_i32 v204, v204\n"
    "v_cvt_f32_i32 v208, v208\n"
    "v_cvt_f32_i32 v205, v205\n"
    "v_cvt_f32_i32 v209, v209\n"
    "v_cvt_f32_i32 v206, v206\n"
    "v_cvt_f32_i32 v210, v210\n"
    "v_cvt_f32_i32 v207, v207\n"
    "v_cvt_f32_i32 v211, v211\n"
    "v_cvt_pk_bf16_f32 v204, v204, v208\n"
    "v_cvt_pk_bf16_f32 v205, v205, v209\n"
    "v_cvt_pk_bf16_f32 v206, v206, v210\n"
    "v_cvt_pk_bf16_f32 v207, v207, v211\n"
    "buffer_store_dword v204, v244, s[16:19], s54 offen offset:0 sc1\n"
    "buffer_store_dword v205, v244, s[16:19], s54 offen offset:1024 sc1\n"
    "buffer_load_dwordx4 v[4:7], v162, s[28:31], s57 offen offset:0\n"
    "buffer_load_dwordx4 v[8:11], v162, s[28:31], s57 offen offset:3072\n"
    "buffer_store_dword v206, v244, s[16:19], s54 offen offset:2048 sc1\n"
    "buffer_store_dword v207, v244, s[16:19], s54 offen offset:3072 sc1\n"
    "v_cvt_f32_i32 v212, v212\n"
    "v_cvt_f32_i32 v216, v216\n"
    "v_cvt_f32_i32 v213, v213\n"
    "v_cvt_f32_i32 v217, v217\n"
    "v_cvt_f32_i32 v214, v214\n"
    "v_cvt_f32_i32 v218, v218\n"
    "v_cvt_f32_i32 v215, v215\n"
    "v_cvt_f32_i32 v219, v219\n"
    "v_cvt_pk_bf16_f32 v212, v212, v216\n"
    "v_cvt_pk_bf16_f32 v213, v213, v217\n"
    "v_cvt_pk_bf16_f32 v214, v214, v218\n"
    "v_cvt_pk_bf16_f32 v215, v215, v219\n"
    "buffer_store_dword v212, v244, s[16:19], s55 offen offset:0 sc1\n"
    "buffer_store_dword v213, v244, s[16:19], s55 offen offset:1024 sc1\n"
    "buffer_store_dword v214, v244, s[16:19], s55 offen offset:2048 sc1\n"
    "buffer_load_dwordx4 v[12:15], v162, s[28:31], s57 offen offset:128\n"
    "buffer_store_dword v215, v244, s[16:19], s55 offen offset:3072 sc1\n"
    "v_cvt_f32_i32 v220, v220\n"
    "v_cvt_f32_i32 v224, v224\n"
    "v_cvt_f32_i32 v221, v221\n"
    "v_cvt_f32_i32 v225, v225\n"
    "v_cvt_f32_i32 v222, v222\n"
    "v_cvt_f32_i32 v226, v226\n"
    "v_cvt_f32_i32 v223, v223\n"
    "v_cvt_f32_i32 v227, v227\n"
    "v_cvt_pk_bf16_f32 v220, v220, v224\n"
    "v_cvt_pk_bf16_f32 v221, v221, v225\n"
    "v_cvt_pk_bf16_f32 v222, v222, v226\n"
    "v_cvt_pk_bf16_f32 v223, v223, v227\n"
    "buffer_store_dword v220, v244, s[16:19], s56 offen offset:0 sc1\n"
    "buffer_store_dword v221, v244, s[16:19], s56 offen offset:1024 sc1\n"
    "buffer_store_dword v222, v244, s[16:19], s56 offen offset:2048 sc1\n"
    "buffer_store_dword v223, v244, s[16:19], s56 offen offset:3072 sc1\n"
    "s_add_u32 s71, s71, 98304\n"
    "s_add_u32 m0, s65, 24576\n"
    "s_add_u32 s72, s85, 0\n"
    "buffer_load_dwordx4 v88, s[20:23], s72 offen offset:0 nt lds\n"
    "buffer_load_dwordx4 v[16:19], v162, s[28:31], s57 offen offset:3200\n"
    "buffer_load_dwordx4 v[20:23], v162, s[28:31], s57 offen offset:256\n"
    "s_waitcnt vmcnt(25)\n"
    "v_add_co_u32 v238, vcc, 0x7f4a7c15, v238\n"
    "v_mov_b32 v159, 0x9e3779b9\n"
    "s_nop 0\n"
    "v_addc_co_u32 v239, vcc, v159, v239, vcc\n"
    "v_mov_b32 v246, 1\n"
    "s_waitcnt vmcnt(26)\n"
    "v_lshlrev_b32 v180, 16, v124\n"
    "v_and_b32 v181, 0xffff0000, v124\n"
    "v_lshlrev_b32 v182, 16, v125\n"
    "v_and_b32 v183, 0xffff0000, v125\n"
    "v_lshlrev_b32 v184, 16, v126\n"
    "v_and_b32 v185, 0xffff0000, v126\n"
    "v_lshlrev_b32 v186, 16, v127\n"
    "v_and_b32 v187, 0xffff0000, v127\n"
    "v_mul_f32 v180, 0x3dd53b95, v180\n"
    "v_mul_f32 v181, 0x3dd53b95, v181\n"
    "v_mul_f32 v182, 0x3dd53b95, v182\n"
    "v_mul_f32 v183, 0x3dd53b95, v183\n"
    "v_mul_f32 v184, -v184, v180\n"
    "v_mul_f32 v185, -v185, v181\n"
    "v_mul_f32 v186, -v186, v182\n"
    "v_mul_f32 v187, -v187, v183\n"
    "v_add_f32 v184, s35, v184\n"
    "v_add_f32 v185, s35, v185\n"
    "v_add_f32 v186, s35, v186\n"
    "v_add_f32 v187, s35, v187\n"
    "ds_write_b128 v131, v[180:183] offset:0\n"
    "ds_write_b128 v131, v[184:187] offset:1024\n"
    "ds_read_b128 v[180:183], v132 offset:0\n"
    "ds_read_b128 v[184:187], v132 offset:1024\n"
    "ds_read_b128 v[188:191], v132 offset:128\n"
    "ds_read_b128 v[192:195], v132 offset:1152\n"
    "ds_read_b128 v[196:199], v132 offset:256\n"
    "ds_read_b128 v[200:203], v132 offset:1280\n"
    "v_accvgpr_write_b32 a60, 0\n"
    "v_accvgpr_write_b32 a61, 0\n"
    "v_accvgpr_write_b32 a62, 0\n"
    "v_accvgpr_write_b32 a63, 0\n"
    "v_accvgpr_write_b32 a64, 0\n"
    "v_accvgpr_write_b32 a65, 0\n"
    "v_accvgpr_write_b32 a66, 0\n"
    "s_add_u32 m0, s65, 25600\n"
    "s_add_u32 s72, s85, 1024\n"
    "buffer_load_dwordx4 v89, s[20:23], s72 offen offset:0 nt lds\n"
    "buffer_load_dwordx4 v[24:27], v162, s[28:31], s57 offen offset:3328\n"
    "v_accvgpr_write_b32 a67, 0\n"
    "v_accvgpr_write_b32 a68, 0\n"
    "v_accvgpr_write_b32 a69, 0\n"
    "v_accvgpr_write_b32 a70, 0\n"
    "v_accvgpr_write_b32 a71, 0\n"
    "v_accvgpr_write_b32 a72, 0\n"
    "v_accvgpr_write_b32 a73, 0\n"
    "v_accvgpr_write_b32 a74, 0\n"
    "v_accvgpr_write_b32 a75, 0\n"
    "v_accvgpr_write_b32 a76, 0\n"
    "v_accvgpr_write_b32 a77, 0\n"
    "v_accvgpr_write_b32 a78, 0\n"
    "v_accvgpr_write_b32 a79, 0\n"
    "v_accvgpr_write_b32 a80, 0\n"
    "v_accvgpr_write_b32 a81, 0\n"
    "v_accvgpr_write_b32 a82, 0\n"
    "v_accvgpr_write_b32 a83, 0\n"
    "v_accvgpr_write_b32 a84, 0\n"
    "v_accvgpr_write_b32 a85, 0\n"
    "v_accvgpr_write_b32 a86, 0\n"
    "v_accvgpr_write_b32 a87, 0\n"
    "v_accvgpr_write_b32 a88, 0\n"
    "v_accvgpr_write_b32 a89, 0\n"
    "v_accvgpr_write_b32 a90, 0\n"
    "v_accvgpr_write_b32 a91, 0\n"
    "v_accvgpr_write_b32 a92, 0\n"
    "v_accvgpr_write_b32 a93, 0\n"
    "v_accvgpr_write_b32 a94, 0\n"
    "v_accvgpr_write_b32 a95, 0\n"
    "v_accvgpr_write_b32 a96, 0\n"
    "v_accvgpr_write_b32 a97, 0\n"
    "v_accvgpr_write_b32 a98, 0\n"
    "v_accvgpr_write_b32 a99, 0\n"
    "v_accvgpr_write_b32 a100, 0\n"
    "v_accvgpr_write_b32 a101, 0\n"
    "v_accvgpr_write_b32 a102, 0\n"
    "v_accvgpr_write_b32 a103, 0\n"
    "v_accvgpr_write_b32 a104, 0\n"
    "v_accvgpr_write_b32 a105, 0\n"
    "v_accvgpr_write_b32 a106, 0\n"
    "v_accvgpr_write_b32 a107, 0\n"
    "v_accvgpr_write_b32 a108, 0\n"
    "s_add_u32 m0, s65, 26624\n"
    "s_add_u32 s72, s85, 2048\n"
    "buffer_load_dwordx4 v90, s[20:23], s72 offen offset:0 nt lds\n"
    "buffer_load_dwordx4 v[100:103], v162, s[28:31], s66 offen offset:0\n"
    "buffer_load_dwordx4 v[104:107], v162, s[28:31], s66 offen offset:3072\n"
    "v_accvgpr_write_b32 a109, 0\n"
    "v_accvgpr_write_b32 a110, 0\n"
    "v_accvgpr_write_b32 a111, 0\n"
    "v_accvgpr_write_b32 a112, 0\n"
    "v_accvgpr_write_b32 a113, 0\n"
    "v_accvgpr_write_b32 a114, 0\n"
    "v_accvgpr_write_b32 a115, 0\n"
    "v_accvgpr_write_b32 a116, 0\n"
    "v_accvgpr_write_b32 a117, 0\n"
    "v_accvgpr_write_b32 a118, 0\n"
    "v_accvgpr_write_b32 a119, 0\n"
    "v_accvgpr_write_b32 a120, 0\n"
    "v_accvgpr_write_b32 a121, 0\n"
    "v_accvgpr_write_b32 a122, 0\n"
    "v_accvgpr_write_b32 a123, 0\n"
    "v_accvgpr_write_b32 a124, 0\n"
    "v_accvgpr_write_b32 a125, 0\n"
    "v_accvgpr_write_b32 a126, 0\n"
    "v_accvgpr_write_b32 a127, 0\n"
    "v_accvgpr_write_b32 a128, 0\n"
    "v_accvgpr_write_b32 a129, 0\n"
    "v_accvgpr_write_b32 a130, 0\n"
    "v_accvgpr_write_b32 a131, 0\n"
    "v_accvgpr_write_b32 a132, 0\n"
    "v_accvgpr_write_b32 a133, 0\n"
    "v_accvgpr_write_b32 a134, 0\n"
    "v_accvgpr_write_b32 a135, 0\n"
    "v_accvgpr_write_b32 a136, 0\n"
    "v_accvgpr_write_b32 a137, 0\n"
    "v_accvgpr_write_b32 a138, 0\n"
    "v_accvgpr_write_b32 a139, 0\n"
    "v_accvgpr_write_b32 a140, 0\n"
    "v_accvgpr_write_b32 a141, 0\n"
    "v_accvgpr_write_b32 a142, 0\n"
    "v_accvgpr_write_b32 a143, 0\n"
    "v_accvgpr_write_b32 a144, 0\n"
    "v_accvgpr_write_b32 a145, 0\n"
    "v_accvgpr_write_b32 a146, 0\n"
    "v_accvgpr_write_b32 a147, 0\n"
    "v_accvgpr_write_b32 a148, 0\n"
    "v_accvgpr_write_b32 a149, 0\n"
    "s_add_u32 m0, s65, 27648\n"
    "s_add_u32 s72, s85, 3072\n"
    "buffer_load_dwordx4 v88, s[20:23], s72 offen offset:0 nt lds\n"
    "buffer_load_dwordx4 v[108:111], v162, s[28:31], s66 offen offset:128\n"
    "v_accvgpr_write_b32 a150, 0\n"
    "v_accvgpr_write_b32 a151, 0\n"
    "v_accvgpr_write_b32 a152, 0\n"
    "v_accvgpr_write_b32 a153, 0\n"
    "v_accvgpr_write_b32 a154, 0\n"
    "v_accvgpr_write_b32 a155, 0\n"
    "v_accvgpr_write_b32 a156, 0\n"
    "v_accvgpr_write_b32 a157, 0\n"
    "v_accvgpr_write_b32 a158, 0\n"
    "v_accvgpr_write_b32 a159, 0\n"
    "v_accvgpr_write_b32 a160, 0\n"
    "v_accvgpr_write_b32 a161, 0\n"
    "v_accvgpr_write_b32 a162, 0\n"
    "v_accvgpr_write_b32 a163, 0\n"
    "v_accvgpr_write_b32 a164, 0\n"
    "v_accvgpr_write_b32 a165, 0\n"
    "v_accvgpr_write_b32 a166, 0\n"
    "v_accvgpr_write_b32 a167, 0\n"
    "v_accvgpr_write_b32 a168, 0\n"
    "v_accvgpr_write_b32 a169, 0\n"
    "v_accvgpr_write_b32 a170, 0\n"
    "v_accvgpr_write_b32 a171, 0\n"
    "v_accvgpr_write_b32 a172, 0\n"
    "v_accvgpr_write_b32 a173, 0\n"
    "v_accvgpr_write_b32 a174, 0\n"
    "v_accvgpr_write_b32 a175, 0\n"
    "v_accvgpr_write_b32 a176, 0\n"
    "v_accvgpr_write_b32 a177, 0\n"
    "v_accvgpr_write_b32 a178, 0\n"
    "v_accvgpr_write_b32 a179, 0\n"
    "v_accvgpr_write_b32 a180, 0\n"
    "v_accvgpr_write_b32 a181, 0\n"
    "v_accvgpr_write_b32 a182, 0\n"
    "v_accvgpr_write_b32 a183, 0\n"
    "v_accvgpr_write_b32 a184, 0\n"
    "v_accvgpr_write_b32 a185, 0\n"
    "v_accvgpr_write_b32 a186, 0\n"
    "v_accvgpr_write_b32 a187, 0\n"
    "v_accvgpr_write_b32 a188, 0\n"
    "v_accvgpr_write_b32 a189, 0\n"
    "v_accvgpr_write_b32 a190, 0\n"
    "v_accvgpr_write_b32 a191, 0\n"
    "s_waitcnt lgkmcnt(0)\n"
    "s_waitcnt vmcnt(10)\n"
    "ds_add_rtn_u32 v245, v247, v246\n"
    "s_lshl_b32 s73, s53, 6\n"
    "s_add_u32 s73, s73, 128\n"
    "s_waitcnt lgkmcnt(0)\n"
    "v_readfirstlane_b32 s74, v245\n"
    "s_nop 0\n"
    "s_cmp_eq_u32 s74, 3\n"
    "s_cselect_b32 s73, s73, 0x7fff0000\n"
    "buffer_store_dwordx2 v[238:239], v91, s[36:39], s73 offen offset:0 sc1\n"
    "s_waitcnt vmcnt(28)\n"
    "s_add_u32 m0, s65, 28672\n"
    "s_add_u32 s72, s85, 4096\n"
    "buffer_load_dwordx4 v89, s[20:23], s72 offen offset:0 nt lds\n"
    "buffer_load_dwordx4 v[112:115], v162, s[28:31], s66 offen offset:3200\n"
    "buffer_load_dwordx4 v[116:119], v162, s[28:31], s66 offen offset:256\n"
    "s_add_u32 s54, s71, s60\n"
    "s_add_u32 s55, s71, s61\n"
    "s_add_u32 s56, s71, s62\n"
    "v_cvt_f32_i32 v36, v36\n"
    "v_cvt_f32_i32 v40, v40\n"
    "v_cvt_f32_i32 v37, v37\n"
    "v_cvt_f32_i32 v41, v41\n"
    "v_cvt_f32_i32 v38, v38\n"
    "v_cvt_f32_i32 v42, v42\n"
    "v_cvt_f32_i32 v39, v39\n"
    "v_cvt_f32_i32 v43, v43\n"
    "v_cvt_pk_bf16_f32 v36, v36, v40\n"
    "v_cvt_pk_bf16_f32 v37, v37, v41\n"
    "v_cvt_pk_bf16_f32 v38, v38, v42\n"
    "v_cvt_pk_bf16_f32 v39, v39, v43\n"
    "buffer_store_dword v36, v244, s[16:19], s54 offen offset:0 sc1\n"
    "buffer_store_dword v37, v244, s[16:19], s54 offen offset:1024 sc1\n"
    "buffer_store_dword v38, v244, s[16:19], s54 offen offset:2048 sc1\n"
    "buffer_store_dword v39, v244, s[16:19], s54 offen offset:3072 sc1\n"
    "v_cvt_f32_i32 v44, v44\n"
    "v_cvt_f32_i32 v48, v48\n"
    "v_cvt_f32_i32 v45, v45\n"
    "v_cvt_f32_i32 v49, v49\n"
    "v_cvt_f32_i32 v46, v46\n"
    "v_cvt_f32_i32 v50, v50\n"
    "s_add_u32 m0, s65, 29696\n"
    "s_add_u32 s72, s85, 5120\n"
    "buffer_load_dwordx4 v90, s[20:23], s72 offen offset:0 nt lds\n"
    "buffer_load_dwordx4 v[120:123], v162, s[28:31], s66 offen offset:3328\n"
    "v_cvt_f32_i32 v47, v47\n"
    "v_cvt_f32_i32 v51, v51\n"
    "v_cvt_pk_bf16_f32 v44, v44, v48\n"
    "v_cvt_pk_bf16_f32 v45, v45, v49\n"
    "v_cvt_pk_bf16_f32 v46, v46, v50\n"
    "v_cvt_pk_bf16_f32 v47, v47, v51\n"
    "buffer_store_dword v44, v244, s[16:19], s55 offen offset:0 sc1\n"
    "buffer_store_dword v45, v244, s[16:19], s55 offen offset:1024 sc1\n"
    "buffer_store_dword v46, v244, s[16:19], s55 offen offset:2048 sc1\n"
    "buffer_store_dword v47, v244, s[16:19], s55 offen offset:3072 sc1\n"
    "v_cvt_f32_i32 v52, v52\n"
    "v_cvt_f32_i32 v56, v56\n"
    "v_cvt_f32_i32 v53, v53\n"
    "v_cvt_f32_i32 v57, v57\n"
    "v_cvt_f32_i32 v54, v54\n"
    "v_cvt_f32_i32 v58, v58\n"
    "v_cvt_f32_i32 v55, v55\n"
    "v_cvt_f32_i32 v59, v59\n"
    "v_cvt_pk_bf16_f32 v52, v52, v56\n"
    "v_cvt_pk_bf16_f32 v53, v53, v57\n"
    "v_cvt_pk_bf16_f32 v54, v54, v58\n"
    "v_cvt_pk_bf16_f32 v55, v55, v59\n"
    "buffer_store_dword v52, v244, s[16:19], s56 offen offset:0 sc1\n"
    "buffer_store_dword v53, v244, s[16:19], s56 offen offset:1024 sc1\n"
    "buffer_store_dword v54, v244, s[16:19], s56 offen offset:2048 sc1\n"
    "buffer_store_dword v55, v244, s[16:19], s56 offen offset:3072 sc1\n"
    "s_add_u32 m0, s65, 73728\n"
    "s_add_u32 s72, s85, 6144\n"
    "buffer_load_dwordx4 v88, s[20:23], s72 offen offset:0 nt lds\n"
    "s_waitcnt vmcnt(44)\n"
    "v_lshlrev_b32 v204, 16, v136\n"
    "v_and_b32 v205, 0xffff0000, v136\n"
    "v_lshlrev_b32 v206, 16, v137\n"
    "v_and_b32 v207, 0xffff0000, v137\n"
    "v_lshlrev_b32 v208, 16, v138\n"
    "v_and_b32 v209, 0xffff0000, v138\n"
    "v_lshlrev_b32 v210, 16, v139\n"
    "v_and_b32 v211, 0xffff0000, v139\n"
    "v_log_f32 v68, v204\n"
    "v_log_f32 v69, v205\n"
    "v_log_f32 v70, v206\n"
    "v_log_f32 v71, v207\n"
    "v_log_f32 v72, v208\n"
    "v_log_f32 v73, v209\n"
    "v_log_f32 v74, v210\n"
    "v_log_f32 v75, v211\n"
    "v_rcp_f32 v204, v204\n"
    "v_rcp_f32 v205, v205\n"
    "v_rcp_f32 v206, v206\n"
    "v_rcp_f32 v207, v207\n"
    "v_rcp_f32 v208, v208\n"
    "v_rcp_f32 v209, v209\n"
    "v_rcp_f32 v210, v210\n"
    "v_rcp_f32 v211, v211\n"
    "s_mov_b32 s73, 0x41a00000\n"
    "v_mov_b32 v228, 0x7fc00000\n"
    "v_cmp_le_f32 vcc, |v68|, s73\n"
    "s_nop 1\n"
    "v_cndmask_b32 v68, v228, v68, vcc\n"
    "v_cmp_le_f32 vcc, |v69|, s73\n"
    "s_nop 1\n"
    "v_cndmask_b32 v69, v228, v69, vcc\n"
    "v_cmp_le_f32 vcc, |v70|, s73\n"
    "s_nop 1\n"
    "v_cndmask_b32 v70, v228, v70, vcc\n"
    "v_cmp_le_f32 vcc, |v71|, s73\n"
    "s_nop 1\n"
    "v_cndmask_b32 v71, v228, v71, vcc\n"
    "s_add_u32 m0, s65, 74752\n"
    "s_add_u32 s72, s85, 7168\n"
    "buffer_load_dwordx4 v89, s[20:23], s72 offen offset:0 nt lds\n"
    "v_cmp_le_f32 vcc, |v72|, s73\n"
    "s_nop 1\n"
    "v_cndmask_b32 v72, v228, v72, vcc\n"
    "v_cmp_le_f32 vcc, |v73|, s73\n"
    "s_nop 1\n"
    "v_cndmask_b32 v73, v228, v73, vcc\n"
    "v_cmp_le_f32 vcc, |v74|, s73\n"
    "s_nop 1\n"
    "v_cndmask_b32 v74, v228, v74, vcc\n"
    "v_cmp_le_f32 vcc, |v75|, s73\n"
    "s_nop 1\n"
    "v_cndmask_b32 v75, v228, v75, vcc\n"
    "v_cvt_pk_bf16_f32 v220, v204, v205\n"
    "v_cvt_pk_bf16_f32 v221, v206, v207\n"
    "v_cvt_pk_bf16_f32 v222, v208, v209\n"
    "v_cvt_pk_bf16_f32 v223, v210, v211\n"
    "v_lshlrev_b32 v212, 16, v220\n"
    "v_and_b32 v213, 0xffff0000, v220\n"
    "v_lshlrev_b32 v214, 16, v221\n"
    "v_and_b32 v215, 0xffff0000, v221\n"
    "v_lshlrev_b32 v216, 16, v222\n"
    "v_and_b32 v217, 0xffff0000, v222\n"
    "v_lshlrev_b32 v218, 16, v223\n"
    "v_and_b32 v219, 0xffff0000, v223\n"
    "v_sub_f32 v204, v204, v212\n"
    "v_sub_f32 v205, v205, v213\n"
    "v_sub_f32 v206, v206, v214\n"
    "v_sub_f32 v207, v207, v215\n"
    "v_sub_f32 v208, v208, v216\n"
    "v_sub_f32 v209, v209, v217\n"
    "v_sub_f32 v210, v210, v218\n"
    "v_sub_f32 v211, v211, v219\n"
    "v_cvt_pk_bf16_f32 v224, v204, v205\n"
    "v_cvt_pk_bf16_f32 v225, v206, v207\n"
    "v_cvt_pk_bf16_f32 v226, v208, v209\n"
    "v_cvt_pk_bf16_f32 v227, v210, v211\n"
    "v_mov_b32 v204, 0\n"
    "v_mov_b32 v205, 0\n"
    "v_mov_b32 v206, 0\n"
    "s_add_u32 m0, s65, 75776\n"
    "s_add_u32 s72, s85, 8192\n"
    "buffer_load_dwordx4 v90, s[20:23], s72 offen offset:0 nt lds\n"
    "v_mov_b32 v207, 0\n"
    "v_swap_b32 v221, v222\n"
    "v_swap_b32 v225, v226\n"
    "v_swap_b32 v141, v142\n"
    "ds_write2_b64 v144, v[220:221], v[222:223] offset0:32 offset1:40\n"
    "ds_write2_b64 v144, v[224:225], v[226:227] offset0:48 offset1:56\n"
    "ds_write2_b64 v144, v[140:141], v[142:143] offset0:64 offset1:72\n"
    "ds_write2_b64 v144, v[204:205], v[206:207] offset0:80 offset1:88\n"
    "s_waitcnt vmcnt(26)\n"
    "v_cvt_f32_i32 v4, v4\n"
    "v_cvt_f32_i32 v5, v5\n"
    "v_cvt_f32_i32 v6, v6\n"
    "v_cvt_f32_i32 v7, v7\n"
    "v_cvt_f32_i32 v8, v8\n"
    "v_cvt_f32_i32 v9, v9\n"
    "v_cvt_f32_i32 v10, v10\n"
    "v_cvt_f32_i32 v11, v11\n"
    "v_cvt_f32_i32 v12, v12\n"
    "v_cvt_f32_i32 v13, v13\n"
    "v_cvt_f32_i32 v14, v14\n"
    "v_cvt_f32_i32 v15, v15\n"
    "v_cvt_f32_i32 v16, v16\n"
    "v_cvt_f32_i32 v17, v17\n"
    "v_cvt_f32_i32 v18, v18\n"
    "v_cvt_f32_i32 v19, v19\n"
    "v_cvt_f32_i32 v20, v20\n"
    "v_cvt_f32_i32 v21, v21\n"
    "v_cvt_f32_i32 v22, v22\n"
    "v_cvt_f32_i32 v23, v23\n"
    "v_cvt_f32_i32 v24, v24\n"
    "v_cvt_f32_i32 v25, v25\n"
    "v_cvt_f32_i32 v26, v26\n"
    "v_cvt_f32_i32 v27, v27\n"
    "v_fma_f32 v4, v4, v180, v184\n"
    "v_fma_f32 v5, v5, v181, v185\n"
    "v_fma_f32 v6, v6, v182, v186\n"
    "s_add_u32 m0, s65, 76800\n"
    "s_add_u32 s72, s85, 9216\n"
    "buffer_load_dwordx4 v88, s[20:23], s72 offen offset:0 nt lds\n"
    "v_fma_f32 v7, v7, v183, v187\n"
    "v_fma_f32 v8, v8, v180, v184\n"
    "v_fma_f32 v9, v9, v181, v185\n"
    "v_fma_f32 v10, v10, v182, v186\n"
    "v_fma_f32 v11, v11, v183, v187\n"
    "v_cvt_pk_bf16_f32 v4, v4, v5\n"
    "v_cvt_pk_bf16_f32 v5, v6, v7\n"
    "v_cvt_pk_bf16_f32 v6, v8, v9\n"
    "v_cvt_pk_bf16_f32 v7, v10, v11\n"
    "ds_write_b64 v163, v[4:5] offset:0\n"
    "ds_write_b64 v163, v[6:7] offset:1024\n"
    "v_fma_f32 v12, v12, v188, v192\n"
    "v_fma_f32 v13, v13, v189, v193\n"
    "v_fma_f32 v14, v14, v190, v194\n"
    "v_fma_f32 v15, v15, v191, v195\n"
    "v_fma_f32 v16, v16, v188, v192\n"
    "v_fma_f32 v17, v17, v189, v193\n"
    "v_fma_f32 v18, v18, v190, v194\n"
    "v_fma_f32 v19, v19, v191, v195\n"
    "v_cvt_pk_bf16_f32 v12, v12, v13\n"
    "v_cvt_pk_bf16_f32 v13, v14, v15\n"
    "v_cvt_pk_bf16_f32 v14, v16, v17\n"
    "v_cvt_pk_bf16_f32 v15, v18, v19\n"
    "ds_write_b64 v163, v[12:13] offset:2048\n"
    "ds_write_b64 v163, v[14:15] offset:3072\n"
    "v_fma_f32 v20, v20, v196, v200\n"
    "v_fma_f32 v21, v21, v197, v201\n"
    "v_fma_f32 v22, v22, v198, v202\n"
    "v_fma_f32 v23, v23, v199, v203\n"
    "v_fma_f32 v24, v24, v196, v200\n"
    "v_fma_f32 v25, v25, v197, v201\n"
    "v_fma_f32 v26, v26, v198, v202\n"
    "v_fma_f32 v27, v27, v199, v203\n"
    "v_cvt_pk_bf16_f32 v20, v20, v21\n"
    "v_cvt_pk_bf16_f32 v21, v22, v23\n"
    "s_add_u32 m0, s65, 77824\n"
    "s_add_u32 s72, s85, 10240\n"
    "buffer_load_dwordx4 v89, s[20:23], s72 offen offset:0 nt lds\n"
    "v_cvt_pk_bf16_f32 v22, v24, v25\n"
    "v_cvt_pk_bf16_f32 v23, v26, v27\n"
    "ds_write_b64 v163, v[20:21] offset:4096\n"
    "ds_write_b64 v163, v[22:23] offset:5120\n"
    "s_waitcnt vmcnt(13)\n"
    "v_cvt_f32_i32 v100, v100\n"
    "v_cvt_f32_i32 v101, v101\n"
    "v_cvt_f32_i32 v102, v102\n"
    "v_cvt_f32_i32 v103, v103\n"
    "v_cvt_f32_i32 v104, v104\n"
    "v_cvt_f32_i32 v105, v105\n"
    "v_cvt_f32_i32 v106, v106\n"
    "v_cvt_f32_i32 v107, v107\n"
    "v_cvt_f32_i32 v108, v108\n"
    "v_cvt_f32_i32 v109, v109\n"
    "v_cvt_f32_i32 v110, v110\n"
    "v_cvt_f32_i32 v111, v111\n"
    "v_cvt_f32_i32 v112, v112\n"
    "v_cvt_f32_i32 v113, v113\n"
    "v_cvt_f32_i32 v114, v114\n"
    "v_cvt_f32_i32 v115, v115\n"
    "v_cvt_f32_i32 v116, v116\n"
    "v_cvt_f32_i32 v117, v117\n"
    "v_cvt_f32_i32 v118, v118\n"
    "v_cvt_f32_i32 v119, v119\n"
    "v_cvt_f32_i32 v120, v120\n"
    "v_cvt_f32_i32 v121, v121\n"
    "v_cvt_f32_i32 v122, v122\n"
    "v_cvt_f32_i32 v123, v123\n"
    "v_fma_f32 v100, v100, v180, v184\n"
    "v_fma_f32 v101, v101, v181, v185\n"
    "v_fma_f32 v102, v102, v182, v186\n"
    "v_fma_f32 v103, v103, v183, v187\n"
    "v_fma_f32 v104, v104, v180, v184\n"
    "v_fma_f32 v105, v105, v181, v185\n"
    "s_add_u32 m0, s65, 78848\n"
    "s_add_u32 s72, s85, 11264\n"
    "buffer_load_dwordx4 v90, s[20:23], s72 offen offset:0 nt lds\n"
    "v_fma_f32 v106, v106, v182, v186\n"
    "v_fma_f32 v107, v107, v183, v187\n"
    "v_cvt_pk_bf16_f32 v100, v100, v101\n"
    "v_cvt_pk_bf16_f32 v101, v102, v103\n"
    "v_cvt_pk_bf16_f32 v102, v104, v105\n"
    "v_cvt_pk_bf16_f32 v103, v106, v107\n"
    "ds_write_b64 v163, v[100:101] offset:12288\n"
    "ds_write_b64 v163, v[102:103] offset:13312\n"
    "v_fma_f32 v108, v108, v188, v192\n"
    "v_fma_f32 v109, v109, v189, v193\n"
    "v_fma_f32 v110, v110, v190, v194\n"
    "v_fma_f32 v111, v111, v191, v195\n"
    "v_fma_f32 v112, v112, v188, v192\n"
    "v_fma_f32 v113, v113, v189, v193\n"
    "v_fma_f32 v114, v114, v190, v194\n"
    "v_fma_f32 v115, v115, v191, v195\n"
    "v_cvt_pk_bf16_f32 v108, v108, v109\n"
    "v_cvt_pk_bf16_f32 v109, v110, v111\n"
    "v_cvt_pk_bf16_f32 v110, v112, v113\n"
    "v_cvt_pk_bf16_f32 v111, v114, v115\n"
    "ds_write_b64 v163, v[108:109] offset:14336\n"
    "ds_write_b64 v163, v[110:111] offset:15360\n"
    "v_fma_f32 v116, v116, v196, v200\n"
    "v_fma_f32 v117, v117, v197, v201\n"
    "v_fma_f32 v118, v118, v198, v202\n"
    "v_fma_f32 v119, v119, v199, v203\n"
    "v_fma_f32 v120, v120, v196, v200\n"
    "v_fma_f32 v121, v121, v197, v201\n"
    "v_fma_f32 v122, v122, v198, v202\n"
    "v_fma_f32 v123, v123, v199, v203\n"
    "v_cvt_pk_bf16_f32 v116, v116, v117\n"
    "v_cvt_pk_bf16_f32 v117, v118, v119\n"
    "v_cvt_pk_bf16_f32 v118, v120, v121\n"
    "v_cvt_pk_bf16_f32 v119, v122, v123\n"
    "ds_write_b64 v163, v[116:117] offset:16384\n"
    "ds_write_b64 v163, v[118:119] offset:17408\n"
    "s_waitcnt vmcnt(15)\n"
    "ds_read_b128 a[192:195], v154 offset:0\n"
    "ds_read_b128 a[196:199], v155 offset:0\n"
    "ds_read_b128 a[200:203], v154 offset:128\n"
    "ds_read_b128 a[204:207], v155 offset:128\n"
    "ds_read_b128 a[208:211], v154 offset:256\n"
    "ds_read_b128 a[212:215], v155 offset:256\n"
    "s_waitcnt lgkmcnt(0)\n"
    "s_barrier\n"
    "ds_read_b128 v[156:159], v2 offset:0\n"
    "ds_read_b128 v[160:163], v2 offset:2048\n"
    "ds_read_b128 v[164:167], v2 offset:4096\n"
    "ds_read_b128 v[168:171], v2 offset:6144\n"
    "ds_read_b128 v[172:175], v2 offset:8192\n"
    "ds_read_b128 v[176:179], v2 offset:10240\n"
    "ds_read_b128 v[180:183], v2 offset:1024\n"
    "ds_read_b128 v[184:187], v2 offset:3072\n"
    "ds_read_b128 v[188:191], v2 offset:5120\n"
    "ds_read_b128 v[192:195], v2 offset:7168\n"
    "ds_read_b128 v[196:199], v2 offset:9216\n"
    "ds_read_b128 v[200:203], v2 offset:11264\n"
    "s_add_u32 m0, s65, 49152\n"
    "s_add_u32 s72, s85, 12288\n"
    "buffer_load_dwordx4 v88, s[20:23], s72 offen offset:0 nt lds\n"
    "v_mov_b32 v108, 0\n"
    "v_mov_b32 v109, 0\n"
    "v_mov_b32 v110, 0\n"
    "v_mov_b32 v111, 0\n"
    "v_mov_b32 v112, 0\n"
    "v_mov_b32 v113, 0\n"
    "v_mov_b32 v114, 0\n"
    "v_mov_b32 v115, 0\n"
    "v_mov_b32 v116, 0\n"
    "v_mov_b32 v117, 0\n"
    "v_mov_b32 v118, 0\n"
    "v_mov_b32 v119, 0\n"
    "v_mov_b32 v120, 0\n"
    "v_mov_b32 v121, 0\n"
    "v_mov_b32 v122, 0\n"
    "v_mov_b32 v123, 0\n"
    "s_waitcnt lgkmcnt(11)\n"
    "v_mfma_f32_16x16x32_bf16 v[4:7], v[156:159], a[192:195], 0\n"
    "s_waitcnt lgkmcnt(10)\n"
    "v_mfma_f32_16x16x32_bf16 v[4:7], v[160:163], a[196:199], v[4:7]\n"
    "s_waitcnt lgkmcnt(9)\n"
    "v_mfma_f32_16x16x32_bf16 v[4:7], v[164:167], a[200:203], v[4:7]\n"
    "s_waitcnt lgkmcnt(8)\n"
    "v_mfma_f32_16x16x32_bf16 v[4:7], v[168:171], a[204:207], v[4:7]\n"
    "s_waitcnt lgkmcnt(7)\n"
    "v_mfma_f32_16x16x32_bf16 v[4:7], v[172:175], a[208:211], v[4:7]\n"
    "s_waitcnt lgkmcnt(6)\n"
    "v_mfma_f32_16x16x32_bf16 v[4:7], v[176:179], a[212:215], v[4:7]\n"
    "s_waitcnt lgkmcnt(5)\n"
    "v_mfma_f32_16x16x32_bf16 v[20:23], v[180:183], a[192:195], 0\n"
    "s_waitcnt lgkmcnt(4)\n"
    "v_mfma_f32_16x16x32_bf16 v[20:23], v[184:187], a[196:199], v[20:23]\n"
    "s_waitcnt lgkmcnt(3)\n"
    "v_mfma_f32_16x16x32_bf16 v[20:23], v[188:191], a[200:203], v[20:23]\n"
    "s_waitcnt lgkmcnt(2)\n"
    "v_mfma_f32_16x16x32_bf16 v[20:23], v[192:195], a[204:207], v[20:23]\n"
    "s_waitcnt lgkmcnt(1)\n"
    "v_mfma_f32_16x16x32_bf16 v[20:23], v[196:199], a[208:211], v[20:23]\n"
    "s_waitcnt lgkmcnt(0)\n"
    "v_mfma_f32_16x16x32_bf16 v[20:23], v[200:203], a[212:215], v[20:23]\n"
    "s_add_u32 m0, s65, 50176\n"
    "s_add_u32 s72, s85, 13312\n"
    "buffer_load_dwordx4 v89, s[20:23], s72 offen offset:0 nt lds\n"
    "v_mov_b32 v77, 155904\n"
    "v_max3_f32 v76, v4, v5, v6\n"
    "s_nop 2\n"
    "v_max3_f32 v76, v76, v7, v20\n"
    "v_max3_f32 v76, v76, v21, v22\n"
    "v_max_f32 v76, v76, v23\n"
    "v_mov_b32 v78, v76\n"
    "s_nop 1\n"
    "v_permlane32_swap_b32 v76, v78\n"
    "v_max_f32 v76, v76, v78\n"
    "v_mov_b32 v78, v76\n"
    "s_nop 1\n"
    "v_permlane16_swap_b32 v76, v78\n"
    "v_max_f32 v76, v76, v78\n"
    "ds_max_f32 v77, v76\n"
    "s_add_u32 m0, s65, 51200\n"
    "s_add_u32 s72, s85, 14336\n"
    "buffer_load_dwordx4 v90, s[20:23], s72 offen offset:0 nt lds\n"
    "buffer_load_dwordx2 v[236:237], v87, s[36:39], 0 offen offset:0 sc1\n"
    "s_waitcnt lgkmcnt(0)\n"
    "s_barrier\n"
    "ds_read_b32 v76, v77\n"
    "s_waitcnt vmcnt(10)\n"
    "buffer_store_dwordx2 v[238:239], v240, s[36:39], s80 offen offset:0 sc1\n"
    "s_lshl_b32 s73, s79, 8\n"
    "s_add_u32 s80, s80, s73\n"
    "s_mul_i32 s73, s79, 98304\n"
    "s_add_u32 s71, s71, s73\n"
    "s_waitcnt lgkmcnt(0)\n"
    "v_add_f32 v76, 0xc1800000, v76\n"
    "v_sub_f32 v68, v68, v76\n"
    "v_sub_f32 v69, v69, v76\n"
    "v_sub_f32 v70, v70, v76\n"
    "v_sub_f32 v71, v71, v76\n"
    "v_sub_f32 v72, v72, v76\n"
    "v_sub_f32 v73, v73, v76\n"
    "v_sub_f32 v74, v74, v76\n"
    "v_sub_f32 v75, v75, v76\n"
    "ds_write_b128 v86, v[68:71] offset:0\n"
    "ds_write_b128 v86, v[72:75] offset:16\n"
    "s_mul_i32 s73, s79, 768\n"
    "v_add_u32 v77, s73, v250\n"
    "s_add_u32 m0, s65, 52224\n"
    "s_add_u32 s72, s85, 15360\n"
    "buffer_load_dwordx4 v88, s[20:23], s72 offen offset:0 nt lds\n"
    "s_waitcnt lgkmcnt(0)\n"
    "s_barrier\n"
    "ds_read_b128 v[100:103], v77 offset:0\n"
    "ds_read_b128 v[104:107], v77 offset:16\n"
    "s_add_u32 m0, s65, 53248\n"
    "s_add_u32 s72, s85, 16384\n"
    "buffer_load_dwordx4 v89, s[20:23], s72 offen offset:0 nt lds\n"
    "s_waitcnt lgkmcnt(1)\n"
    "v_add_f32 v4, v4, v100\n"
    "v_add_f32 v5, v5, v101\n"
    "v_add_f32 v6, v6, v102\n"
    "v_add_f32 v7, v7, v103\n"
    "s_waitcnt lgkmcnt(0)\n"
    "v_add_f32 v20, v20, v104\n"
    "v_add_f32 v21, v21, v105\n"
    "v_add_f32 v22, v22, v106\n"
    "v_add_f32 v23, v23, v107\n"
    "v_exp_f32 v4, v4\n"
    "v_exp_f32 v5, v5\n"
    "v_exp_f32 v6, v6\n"
    "v_exp_f32 v7, v7\n"
    "v_exp_f32 v20, v20\n"
    "v_exp_f32 v21, v21\n"
    "v_exp_f32 v22, v22\n"
    "v_exp_f32 v23, v23\n"
    "s_add_u32 m0, s65, 54272\n"
    "s_add_u32 s72, s85, 17408\n"
    "buffer_load_dwordx4 v90, s[20:23], s72 offen offset:0 nt lds\n"
    "s_waitcnt vmcnt(8)\n"
    "ds_read_b128 a[216:219], v84 offset:24576\n"
    "ds_read_b128 a[220:223], v85 offset:24576\n"
    "ds_read_b128 a[224:227], v84 offset:24704\n"
    "ds_read_b128 a[228:231], v85 offset:24704\n"
    "ds_read_b128 a[232:235], v84 offset:24832\n"
    "ds_read_b128 a[236:239], v85 offset:24832\n"
    "s_waitcnt lgkmcnt(0)\n"
    "v_mfma_f32_16x16x32_bf16 v[8:11], v[176:179], a[236:239], v[100:103]\n"
    "v_mfma_f32_16x16x32_bf16 v[8:11], v[172:175], a[232:235], v[8:11]\n"
    "v_mfma_f32_16x16x32_bf16 v[8:11], v[168:171], a[228:231], v[8:11]\n"
    "v_mfma_f32_16x16x32_bf16 v[8:11], v[164:167], a[224:227], v[8:11]\n"
    "v_mfma_f32_16x16x32_bf16 v[8:11], v[160:163], a[220:223], v[8:11]\n"
    "v_mfma_f32_16x16x32_bf16 v[8:11], v[156:159], a[216:219], v[8:11]\n"
    "v_mfma_f32_16x16x32_bf16 v[24:27], v[200:203], a[236:239], v[104:107]\n"
    "v_mfma_f32_16x16x32_bf16 v[24:27], v[196:199], a[232:235], v[24:27]\n"
    "v_mfma_f32_16x16x32_bf16 v[24:27], v[192:195], a[228:231], v[24:27]\n"
    "v_mfma_f32_16x16x32_bf16 v[24:27], v[188:191], a[224:227], v[24:27]\n"
    "v_mfma_f32_16x16x32_bf16 v[24:27], v[184:187], a[220:223], v[24:27]\n"
    "v_mfma_f32_16x16x32_bf16 v[24:27], v[180:183], a[216:219], v[24:27]\n"
    "s_add_u32 m0, s65, 98304\n"
    "s_add_u32 s72, s85, 18432\n"
    "buffer_load_dwordx4 v88, s[20:23], s72 offen offset:0 nt lds\n"
    "v_exp_f32 v8, v8\n"
    "v_exp_f32 v9, v9\n"
    "v_exp_f32 v10, v10\n"
    "v_exp_f32 v11, v11\n"
    "s_nop 0\n"
    "v_exp_f32 v24, v24\n"
    "v_exp_f32 v25, v25\n"
    "v_exp_f32 v26, v26\n"
    "v_exp_f32 v27, v27\n"
    "s_add_u32 m0, s65, 99328\n"
    "s_add_u32 s72, s85, 19456\n"
    "buffer_load_dwordx4 v89, s[20:23], s72 offen offset:0 nt lds\n"
    "s_add_u32 m0, s65, 100352\n"
    "s_add_u32 s72, s85, 20480\n"
    "buffer_load_dwordx4 v90, s[20:23], s72 offen offset:0 nt lds\n"
    "s_waitcnt vmcnt(7)\n"
    "v_cmp_eq_u64 s[54:55], v[236:237], v[238:239]\n"
    "s_nop 0\n"
    "s_cmp_eq_u64 s[54:55], -1\n"
    "s_cbranch_scc0 .LpollAV\n"
    ".LpollAVr:\n"
    "s_add_u32 m0, s65, 101376\n"
    "s_add_u32 s72, s85, 21504\n"
    "buffer_load_dwordx4 v88, s[20:23], s72 offen offset:0 nt lds\n"
    "s_add_u32 m0, s65, 24576\n"
    "s_add_u32 s72, s65, 49152\n"
    "buffer_load_dwordx4 v1, s[16:19], s72 offen offset:0 sc1 lds\n"
    "s_add_u32 m0, s65, 25600\n"
    "s_add_u32 s72, s65, 50176\n"
    "buffer_load_dwordx4 v1, s[16:19], s72 offen offset:0 sc1 lds\n"
    "s_add_u32 m0, s65, 102400\n"
    "s_add_u32 s72, s85, 22528\n"
    "buffer_load_dwordx4 v89, s[20:23], s72 offen offset:0 nt lds\n"
    "s_add_u32 m0, s65, 26624\n"
    "s_add_u32 s72, s65, 51200\n"
    "buffer_load_dwordx4 v1, s[16:19], s72 offen offset:0 sc1 lds\n"
    "s_add_u32 m0, s65, 27648\n"
    "s_add_u32 s72, s65, 52224\n"
    "buffer_load_dwordx4 v1, s[16:19], s72 offen offset:0 sc1 lds\n"
    "s_add_u32 m0, s65, 103424\n"
    "s_add_u32 s72, s85, 23552\n"
    "buffer_load_dwordx4 v90, s[20:23], s72 offen offset:0 nt lds\n"
    "s_add_u32 m0, s65, 28672\n"
    "s_add_u32 s72, s65, 53248\n"
    "buffer_load_dwordx4 v1, s[16:19], s72 offen offset:0 sc1 lds\n"
    "s_add_u32 m0, s65, 29696\n"
    "s_add_u32 s72, s65, 54272\n"
    "buffer_load_dwordx4 v1, s[16:19], s72 offen offset:0 sc1 lds\n"
    "s_add_u32 m0, s65, 73728\n"
    "s_add_u32 s72, s65, 73728\n"
    "buffer_load_dwordx4 v1, s[16:19], s72 offen offset:0 sc1 lds\n"
    "s_add_u32 m0, s65, 74752\n"
    "s_add_u32 s72, s65, 74752\n"
    "buffer_load_dwordx4 v1, s[16:19], s72 offen offset:0 sc1 lds\n"
    "s_add_u32 m0, s65, 75776\n"
    "s_add_u32 s72, s65, 75776\n"
    "buffer_load_dwordx4 v1, s[16:19], s72 offen offset:0 sc1 lds\n"
    "s_add_u32 m0, s65, 76800\n"
    "s_add_u32 s72, s65, 76800\n"
    "buffer_load_dwordx4 v1, s[16:19], s72 offen offset:0 sc1 lds\n"
    "s_add_u32 m0, s65, 77824\n"
    "s_add_u32 s72, s65, 77824\n"
    "buffer_load_dwordx4 v1, s[16:19], s72 offen offset:0 sc1 lds\n"
    "s_add_u32 m0, s65, 78848\n"
    "s_add_u32 s72, s65, 78848\n"
    "buffer_load_dwordx4 v1, s[16:19], s72 offen offset:0 sc1 lds\n"
    "s_bitcmp1_b32 s83, 1\n"
    "s_cbranch_scc1 .Lf4b\n"
    "s_waitcnt vmcnt(18)\n"
    "ds_read_b128 a[240:243], v84 offset:0\n"
    "ds_read_b128 a[244:247], v85 offset:0\n"
    "ds_read_b128 a[248:251], v84 offset:128\n"
    "ds_read_b128 a[252:255], v85 offset:128\n"
    "ds_read_b128 v[124:127], v84 offset:256\n"
    "ds_read_b128 v[128:131], v85 offset:256\n"
    "s_waitcnt lgkmcnt(5)\n"
    "v_mfma_f32_16x16x32_bf16 v[12:15], v[156:159], a[240:243], v[100:103]\n"
    "s_waitcnt lgkmcnt(4)\n"
    "v_mfma_f32_16x16x32_bf16 v[12:15], v[160:163], a[244:247], v[12:15]\n"
    "s_waitcnt lgkmcnt(3)\n"
    "v_mfma_f32_16x16x32_bf16 v[12:15], v[164:167], a[248:251], v[12:15]\n"
    "s_waitcnt lgkmcnt(2)\n"
    "v_mfma_f32_16x16x32_bf16 v[12:15], v[168:171], a[252:255], v[12:15]\n"
    "s_waitcnt lgkmcnt(1)\n"
    "v_mfma_f32_16x16x32_bf16 v[12:15], v[172:175], v[124:127], v[12:15]\n"
    "s_waitcnt lgkmcnt(0)\n"
    "v_mfma_f32_16x16x32_bf16 v[12:15], v[176:179], v[128:131], v[12:15]\n"
    "v_mfma_f32_16x16x32_bf16 v[28:31], v[180:183], a[240:243], v[104:107]\n"
    "v_mfma_f32_16x16x32_bf16 v[28:31], v[184:187], a[244:247], v[28:31]\n"
    "v_mfma_f32_16x16x32_bf16 v[28:31], v[188:191], a[248:251], v[28:31]\n"
    "v_mfma_f32_16x16x32_bf16 v[28:31], v[192:195], a[252:255], v[28:31]\n"
    "v_mfma_f32_16x16x32_bf16 v[28:31], v[196:199], v[124:127], v[28:31]\n"
    "v_mfma_f32_16x16x32_bf16 v[28:31], v[200:203], v[128:131], v[28:31]\n"
    "s_nop 1\n"
    "v_exp_f32 v12, v12\n"
    "v_exp_f32 v13, v13\n"
    "v_exp_f32 v14, v14\n"
    "v_exp_f32 v15, v15\n"
    "s_nop 1\n"
    "v_exp_f32 v28, v28\n"
    "v_exp_f32 v29, v29\n"
    "v_exp_f32 v30, v30\n"
    "v_exp_f32 v31, v31\n"
    "s_waitcnt vmcnt(8)\n"
    "ds_read_b128 v[132:135], v84 offset:49152\n"
    "ds_read_b128 v[136:139], v85 offset:49152\n"
    "ds_read_b128 v[140:143], v84 offset:49280\n"
    "ds_read_b128 v[144:147], v85 offset:49280\n"
    "ds_read_b128 v[148:151], v84 offset:49408\n"
    "ds_read_b128 v[152:155], v85 offset:49408\n"
    "s_waitcnt lgkmcnt(0)\n"
    "v_mfma_f32_16x16x32_bf16 v[16:19], v[176:179], v[152:155], v[100:103]\n"
    "v_mfma_f32_16x16x32_bf16 v[16:19], v[172:175], v[148:151], v[16:19]\n"
    "v_mfma_f32_16x16x32_bf16 v[16:19], v[168:171], v[144:147], v[16:19]\n"
    "v_mfma_f32_16x16x32_bf16 v[16:19], v[164:167], v[140:143], v[16:19]\n"
    "v_mfma_f32_16x16x32_bf16 v[16:19], v[160:163], v[136:139], v[16:19]\n"
    "v_mfma_f32_16x16x32_bf16 v[16:19], v[156:159], v[132:135], v[16:19]\n"
    "v_mfma_f32_16x16x32_bf16 v[32:35], v[200:203], v[152:155], v[104:107]\n"
    "v_mfma_f32_16x16x32_bf16 v[32:35], v[196:199], v[148:151], v[32:35]\n"
    "v_mfma_f32_16x16x32_bf16 v[32:35], v[192:195], v[144:147], v[32:35]\n"
    "v_mfma_f32_16x16x32_bf16 v[32:35], v[188:191], v[140:143], v[32:35]\n"
    "v_mfma_f32_16x16x32_bf16 v[32:35], v[184:187], v[136:139], v[32:35]\n"
    "v_mfma_f32_16x16x32_bf16 v[32:35], v[180:183], v[132:135], v[32:35]\n"
    "ds_read_b128 v[156:159], v2 offset:12288\n"
    "ds_read_b128 v[160:163], v2 offset:14336\n"
    "ds_read_b128 v[164:167], v2 offset:16384\n"
    "ds_read_b128 v[168:171], v2 offset:18432\n"
    "ds_read_b128 v[172:175], v2 offset:20480\n"
    "ds_read_b128 v[176:179], v2 offset:22528\n"
    "ds_read_b128 v[100:103], v77 offset:128\n"
    "v_exp_f32 v16, v16\n"
    "v_exp_f32 v17, v17\n"
    "v_exp_f32 v18, v18\n"
    "v_exp_f32 v19, v19\n"
    "v_exp_f32 v32, v32\n"
    "v_exp_f32 v33, v33\n"
    "v_exp_f32 v34, v34\n"
    "v_exp_f32 v35, v35\n"
    "s_mov_b32 s66, 0\n"
    "s_waitcnt lgkmcnt(0)\n"
    "s_waitcnt vmcnt(0)\n"
    "ds_add_u32 v247, v246\n"
    "s_nop 7\n"
    "s_branch .LPV\n"
    "s_branch .Lf4e\n"
    ".Lf4b:\n"
    "s_add_u32 s75, s71, s63\n"
    "buffer_load_dwordx4 v[204:207], v242, s[40:43], s75 offen offset:0 nt\n"
    "buffer_load_dwordx4 v[208:211], v242, s[40:43], s75 offen offset:768 nt\n"
    "buffer_load_dwordx4 v[212:215], v242, s[40:43], s75 offen offset:256 nt\n"
    "buffer_load_dwordx4 v[216:219], v242, s[40:43], s75 offen offset:1024 nt\n"
    "buffer_load_dwordx4 v[220:223], v242, s[40:43], s75 offen offset:512 nt\n"
    "buffer_load_dwordx4 v[224:227], v242, s[40:43], s75 offen offset:1280 nt\n"
    "s_waitcnt vmcnt(24)\n"
    "ds_read_b128 a[240:243], v84 offset:0\n"
    "ds_read_b128 a[244:247], v85 offset:0\n"
    "ds_read_b128 a[248:251], v84 offset:128\n"
    "ds_read_b128 a[252:255], v85 offset:128\n"
    "ds_read_b128 v[124:127], v84 offset:256\n"
    "ds_read_b128 v[128:131], v85 offset:256\n"
    "s_waitcnt lgkmcnt(5)\n"
    "v_mfma_f32_16x16x32_bf16 v[12:15], v[156:159], a[240:243], v[100:103]\n"
    "s_waitcnt lgkmcnt(4)\n"
    "v_mfma_f32_16x16x32_bf16 v[12:15], v[160:163], a[244:247], v[12:15]\n"
    "s_waitcnt lgkmcnt(3)\n"
    "v_mfma_f32_16x16x32_bf16 v[12:15], v[164:167], a[248:251], v[12:15]\n"
    "s_waitcnt lgkmcnt(2)\n"
    "v_mfma_f32_16x16x32_bf16 v[12:15], v[168:171], a[252:255], v[12:15]\n"
    "s_waitcnt lgkmcnt(1)\n"
    "v_mfma_f32_16x16x32_bf16 v[12:15], v[172:175], v[124:127], v[12:15]\n"
    "s_waitcnt lgkmcnt(0)\n"
    "v_mfma_f32_16x16x32_bf16 v[12:15], v[176:179], v[128:131], v[12:15]\n"
    "v_mfma_f32_16x16x32_bf16 v[28:31], v[180:183], a[240:243], v[104:107]\n"
    "v_mfma_f32_16x16x32_bf16 v[28:31], v[184:187], a[244:247], v[28:31]\n"
    "v_mfma_f32_16x16x32_bf16 v[28:31], v[188:191], a[248:251], v[28:31]\n"
    "v_mfma_f32_16x16x32_bf16 v[28:31], v[192:195], a[252:255], v[28:31]\n"
    "v_mfma_f32_16x16x32_bf16 v[28:31], v[196:199], v[124:127], v[28:31]\n"
    "v_mfma_f32_16x16x32_bf16 v[28:31], v[200:203], v[128:131], v[28:31]\n"
    "s_nop 1\n"
    "v_exp_f32 v12, v12\n"
    "v_exp_f32 v13, v13\n"
    "v_exp_f32 v14, v14\n"
    "v_exp_f32 v15, v15\n"
    "s_nop 1\n"
    "v_exp_f32 v28, v28\n"
    "v_exp_f32 v29, v29\n"
    "v_exp_f32 v30, v30\n"
    "v_exp_f32 v31, v31\n"
    "s_waitcnt vmcnt(14)\n"
    "ds_read_b128 v[132:135], v84 offset:49152\n"
    "ds_read_b128 v[136:139], v85 offset:49152\n"
    "ds_read_b128 v[140:143], v84 offset:49280\n"
    "ds_read_b128 v[144:147], v85 offset:49280\n"
    "ds_read_b128 v[148:151], v84 offset:49408\n"
    "ds_read_b128 v[152:155], v85 offset:49408\n"
    "s_waitcnt lgkmcnt(0)\n"
    "v_mfma_f32_16x16x32_bf16 v[16:19], v[176:179], v[152:155], v[100:103]\n"
    "v_mfma_f32_16x16x32_bf16 v[16:19], v[172:175], v[148:151], v[16:19]\n"
    "v_mfma_f32_16x16x32_bf16 v[16:19], v[168:171], v[144:147], v[16:19]\n"
    "v_mfma_f32_16x16x32_bf16 v[16:19], v[164:167], v[140:143], v[16:19]\n"
    "v_mfma_f32_16x16x32_bf16 v[16:19], v[160:163], v[136:139], v[16:19]\n"
    "v_mfma_f32_16x16x32_bf16 v[16:19], v[156:159], v[132:135], v[16:19]\n"
    "v_mfma_f32_16x16x32_bf16 v[32:35], v[200:203], v[152:155], v[104:107]\n"
    "v_mfma_f32_16x16x32_bf16 v[32:35], v[196:199], v[148:151], v[32:35]\n"
    "v_mfma_f32_16x16x32_bf16 v[32:35], v[192:195], v[144:147], v[32:35]\n"
    "v_mfma_f32_16x16x32_bf16 v[32:35], v[188:191], v[140:143], v[32:35]\n"
    "v_mfma_f32_16x16x32_bf16 v[32:35], v[184:187], v[136:139], v[32:35]\n"
    "v_mfma_f32_16x16x32_bf16 v[32:35], v[180:183], v[132:135], v[32:35]\n"
    "ds_read_b128 v[156:159], v2 offset:12288\n"
    "ds_read_b128 v[160:163], v2 offset:14336\n"
    "ds_read_b128 v[164:167], v2 offset:16384\n"
    "ds_read_b128 v[168:171], v2 offset:18432\n"
    "ds_read_b128 v[172:175], v2 offset:20480\n"
    "ds_read_b128 v[176:179], v2 offset:22528\n"
    "ds_read_b128 v[100:103], v77 offset:128\n"
    "v_exp_f32 v16, v16\n"
    "v_exp_f32 v17, v17\n"
    "v_exp_f32 v18, v18\n"
    "v_exp_f32 v19, v19\n"
    "v_exp_f32 v32, v32\n"
    "v_exp_f32 v33, v33\n"
    "v_exp_f32 v34, v34\n"
    "v_exp_f32 v35, v35\n"
    "s_mov_b32 s66, 0\n"
    "s_waitcnt lgkmcnt(0)\n"
    "s_waitcnt vmcnt(6)\n"
    "ds_add_u32 v247, v246\n"
    "s_nop 7\n"
    "s_branch .LNV\n"
    ".Lf4e:\n"
    "s_nop 4\n"
    ".Lf5e:\n"
    "s_nop 4\n"
    ".p2align 6\n"
    ".LPK:\n"
    "v_mfma_f32_16x16x32_bf16 v[36:39], v[156:159], a[192:195], v[100:103]\n"
    "s_add_u32 s71, s71, 98304\n"
    "ds_read_b128 v[180:183], v2 offset:13312\n"
    "v_cvt_pk_bf16_f32 v68, v4, v5\n"
    "v_mfma_f32_16x16x32_bf16 v[36:39], v[160:163], a[196:199], v[36:39]\n"
    "s_add_u32 s75, s71, s63\n"
    "ds_read_b128 v[184:187], v2 offset:15360\n"
    "v_mfma_f32_16x16x32_bf16 v[36:39], v[164:167], a[200:203], v[36:39]\n"
    "s_add_u32 s76, s75, 6144\n"
    "ds_read_b128 v[188:191], v2 offset:17408\n"
    "v_mfma_f32_16x16x32_bf16 v[36:39], v[168:171], a[204:207], v[36:39]\n"
    "s_add_u32 s77, s75, 24576\n"
    "ds_read_b128 v[192:195], v2 offset:19456\n"
    "v_cvt_pk_bf16_f32 v69, v6, v7\n"
    "v_mfma_f32_16x16x32_bf16 v[36:39], v[172:175], a[208:211], v[36:39]\n"
    "s_add_u32 s78, s75, 30720\n"
    "ds_read_b128 v[196:199], v2 offset:21504\n"
    "v_mfma_f32_16x16x32_bf16 v[36:39], v[176:179], a[212:215], v[36:39]\n"
    "ds_read_b128 v[200:203], v2 offset:23552\n"
    "v_mfma_f32_16x16x32_bf16 v[40:43], v[176:179], a[236:239], v[100:103]\n"
    "ds_read_b128 v[104:107], v250 offset:144\n"
    "v_mfma_f32_16x16x32_bf16 v[40:43], v[172:175], a[232:235], v[40:43]\n"
    "s_add_u32 s68, s68, 24576\n"
    "v_cvt_pk_bf16_f32 v76, v8, v9\n"
    "v_mfma_f32_16x16x32_bf16 v[40:43], v[168:171], a[228:231], v[40:43]\n"
    "s_cmp_ge_u32 s68, s69\n"
    "s_cselect_b32 s68, s65, s68\n"
    "v_mfma_f32_16x16x32_bf16 v[40:43], v[164:167], a[224:227], v[40:43]\n"
    "v_exp_f32 v36, v36\n"
    "v_mfma_f32_16x16x32_bf16 v[40:43], v[160:163], a[220:223], v[40:43]\n"
    "v_exp_f32 v37, v37\n"
    "v_mfma_f32_16x16x32_bf16 v[40:43], v[156:159], a[216:219], v[40:43]\n"
    "v_cvt_pk_bf16_f32 v77, v10, v11\n"
    "v_cvt_pk_bf16_f32 v72, v36, v37\n"
    "v_mfma_f32_16x16x32_bf16 v[44:47], v[156:159], a[240:243], v[100:103]\n"
    "v_mfma_f32_16x16x32_bf16 v[44:47], v[160:163], a[244:247], v[44:47]\n"
    "v_exp_f32 v38, v38\n"
    "v_mfma_f32_16x16x32_bf16 v[44:47], v[164:167], a[248:251], v[44:47]\n"
    "v_mfma_f32_16x16x32_bf16 v[44:47], v[168:171], a[252:255], v[44:47]\n"
    "v_cvt_pk_bf16_f32 v84, v12, v13\n"
    "v_mfma_f32_16x16x32_bf16 v[44:47], v[172:175], v[124:127], v[44:47]\n"
    "v_exp_f32 v39, v39\n"
    "v_mfma_f32_16x16x32_bf16 v[44:47], v[176:179], v[128:131], v[44:47]\n"
    "v_cvt_pk_bf16_f32 v73, v38, v39\n"
    "v_mfma_f32_16x16x32_bf16 v[48:51], v[176:179], v[152:155], v[100:103]\n"
    "ds_read_b32 v245, v247\n"
    "v_cvt_pk_bf16_f32 v85, v14, v15\n"
    "v_mfma_f32_16x16x32_bf16 v[48:51], v[172:175], v[148:151], v[48:51]\n"
    "s_add_u32 m0, s68, 0\n"
    "v_exp_f32 v40, v40\n"
    "v_mfma_f32_16x16x32_bf16 v[48:51], v[168:171], v[144:147], v[48:51]\n"
    "v_mfma_f32_16x16x32_bf16 v[48:51], v[164:167], v[140:143], v[48:51]\n"
    "v_mfma_f32_16x16x32_bf16 v[48:51], v[160:163], v[136:139], v[48:51]\n"
    "s_waitcnt vmcnt(10)\n"
    "v_cmp_eq_u64 s[54:55], v[236:237], v[238:239]\n"
    "v_cvt_pk_bf16_f32 v92, v16, v17\n"
    "v_mfma_f32_16x16x32_bf16 v[48:51], v[156:159], v[132:135], v[48:51]\n"
    "s_waitcnt lgkmcnt(1)\n"
    "v_exp_f32 v41, v41\n"
    "v_mfma_f32_16x16x32_bf16 v[52:55], v[180:183], a[192:195], v[104:107]\n"
    "s_waitcnt lgkmcnt(0)\n"
    "v_cmp_le_u32 s[56:57], s59, v245\n"
    "v_cvt_pk_bf16_f32 v80, v40, v41\n"
    "v_mfma_f32_16x16x32_bf16 v[52:55], v[184:187], a[196:199], v[52:55]\n"
    "v_exp_f32 v42, v42\n"
    "v_mfma_f32_16x16x32_bf16 v[52:55], v[188:191], a[200:203], v[52:55]\n"
    "s_and_b64 s[54:55], s[54:55], s[56:57]\n"
    "s_cmp_eq_u64 s[54:55], -1\n"
    "v_mfma_f32_16x16x32_bf16 v[52:55], v[192:195], a[204:207], v[52:55]\n"
    "s_cbranch_scc0 .Lpoll1\n"
    ".Lpoll1r:\n"
    "v_mfma_f32_16x16x32_bf16 v[52:55], v[196:199], a[208:211], v[52:55]\n"
    "buffer_load_dwordx4 v1, s[16:19], s67 offen offset:0 sc1 lds\n"
    "v_mfma_f32_16x16x32_bf16 v[52:55], v[200:203], a[212:215], v[52:55]\n"
    "ds_read_b128 v[156:159], v3 offset:0\n"
    "ds_read_b128 v[160:163], v3 offset:2048\n"
    "v_mfma_f32_16x16x32_bf16 v[56:59], v[200:203], a[236:239], v[104:107]\n"
    "ds_read_b128 v[164:167], v3 offset:4096\n"
    "ds_read_b128 v[168:171], v3 offset:6144\n"
    "v_mfma_f32_16x16x32_bf16 v[56:59], v[196:199], a[232:235], v[56:59]\n"
    "ds_read_b128 v[172:175], v3 offset:8192\n"
    "ds_read_b128 v[176:179], v3 offset:10240\n"
    "v_mfma_f32_16x16x32_bf16 v[56:59], v[192:195], a[228:231], v[56:59]\n"
    "buffer_load_dwordx4 v1, s[16:19], s67 offen offset:1024 sc1 lds\n"
    "v_mfma_f32_16x16x32_bf16 v[56:59], v[188:191], a[224:227], v[56:59]\n"
    "ds_read_b128 v[100:103], v250 offset:768\n"
    "v_cvt_pk_bf16_f32 v93, v18, v19\n"
    "v_mfma_f32_16x16x32_bf16 v[56:59], v[184:187], a[220:223], v[56:59]\n"
    "v_cvt_pk_bf16_f32 v70, v20, v21\n"
    "v_cvt_pk_bf16_f32 v71, v22, v23\n"
    "v_mfma_f32_16x16x32_bf16 v[56:59], v[180:183], a[216:219], v[56:59]\n"
    "v_exp_f32 v43, v43\n"
    "v_mfma_f32_16x16x32_bf16 v[60:63], v[180:183], a[240:243], v[104:107]\n"
    "buffer_load_dwordx4 v1, s[16:19], s67 offen offset:2048 sc1 lds\n"
    "v_mfma_f32_16x16x32_bf16 v[60:63], v[184:187], a[244:247], v[60:63]\n"
    "v_cvt_pk_bf16_f32 v78, v24, v25\n"
    "v_cvt_pk_bf16_f32 v81, v42, v43\n"
    "v_mfma_f32_16x16x32_bf16 v[60:63], v[188:191], a[248:251], v[60:63]\n"
    "v_exp_f32 v44, v44\n"
    "v_mfma_f32_16x16x32_bf16 v[60:63], v[192:195], a[252:255], v[60:63]\n"
    "v_exp_f32 v45, v45\n"
    "v_mfma_f32_16x16x32_bf16 v[60:63], v[196:199], v[124:127], v[60:63]\n"
    "buffer_load_dwordx4 v1, s[16:19], s67 offen offset:3072 sc1 lds\n"
    "v_mfma_f32_16x16x32_bf16 v[60:63], v[200:203], v[128:131], v[60:63]\n"
    "s_add_u32 m0, s68, 4096\n"
    "v_cvt_pk_bf16_f32 v79, v26, v27\n"
    "v_cvt_pk_bf16_f32 v88, v44, v45\n"
    "v_mfma_f32_16x16x32_bf16 v[64:67], v[200:203], v[152:155], v[104:107]\n"
    "s_add_u32 s83, s67, 4096\n"
    "v_exp_f32 v46, v46\n"
    "v_mfma_f32_16x16x32_bf16 v[64:67], v[196:199], v[148:151], v[64:67]\n"
    "v_exp_f32 v47, v47\n"
    "v_mfma_f32_16x16x32_bf16 v[64:67], v[192:195], v[144:147], v[64:67]\n"
    "buffer_load_dwordx4 v1, s[16:19], s83 offen offset:0 sc1 lds\n"
    "v_mfma_f32_16x16x32_bf16 v[64:67], v[188:191], v[140:143], v[64:67]\n"
    "v_cvt_pk_bf16_f32 v86, v28, v29\n"
    "v_cvt_pk_bf16_f32 v89, v46, v47\n"
    "v_mfma_f32_16x16x32_bf16 v[64:67], v[184:187], v[136:139], v[64:67]\n"
    "v_exp_f32 v48, v48\n"
    "v_mfma_f32_16x16x32_bf16 v[64:67], v[180:183], v[132:135], v[64:67]\n"
    "s_waitcnt lgkmcnt(0)\n"
    "v_exp_f32 v49, v49\n"
    "v_mfma_f32_16x16x32_bf16 v[4:7], v[156:159], a[192:195], v[100:103]\n"
    "buffer_load_dwordx4 v1, s[16:19], s83 offen offset:1024 sc1 lds\n"
    "v_mfma_f32_16x16x32_bf16 v[4:7], v[160:163], a[196:199], v[4:7]\n"
    "s_add_u32 m0, s64, 0\n"
    "ds_read_b128 v[180:183], v3 offset:1024\n"
    "ds_read_b128 v[184:187], v3 offset:3072\n"
    "v_mfma_f32_16x16x32_bf16 v[4:7], v[164:167], a[200:203], v[4:7]\n"
    "s_add_u32 s83, s67, 24576\n"
    "ds_read_b128 v[188:191], v3 offset:5120\n"
    "v_cvt_pk_bf16_f32 v87, v30, v31\n"
    "v_mfma_f32_16x16x32_bf16 v[4:7], v[168:171], a[204:207], v[4:7]\n"
    "ds_read_b128 v[192:195], v3 offset:7168\n"
    "v_cvt_pk_bf16_f32 v96, v48, v49\n"
    "v_mfma_f32_16x16x32_bf16 v[4:7], v[172:175], a[208:211], v[4:7]\n"
    "buffer_load_dwordx4 v1, s[16:19], s83 offen offset:0 sc1 lds\n"
    "v_mfma_f32_16x16x32_bf16 v[4:7], v[176:179], a[212:215], v[4:7]\n"
    "ds_read_b128 v[196:199], v3 offset:9216\n"
    "ds_read_b128 v[200:203], v3 offset:11264\n"
    "v_mfma_f32_16x16x32_bf16 v[8:11], v[176:179], a[236:239], v[100:103]\n"
    "ds_read_b128 v[104:107], v250 offset:784\n"
    "v_cvt_pk_bf16_f32 v94, v32, v33\n"
    "v_mfma_f32_16x16x32_bf16 v[8:11], v[172:175], a[232:235], v[8:11]\n"
    "v_exp_f32 v52, v52\n"
    "v_mfma_f32_16x16x32_bf16 v[8:11], v[168:171], a[228:231], v[8:11]\n"
    "buffer_load_dwordx4 v1, s[16:19], s83 offen offset:1024 sc1 lds\n"
    "v_mfma_f32_16x16x32_bf16 v[8:11], v[164:167], a[224:227], v[8:11]\n"
    "v_cvt_pk_bf16_f32 v95, v34, v35\n"
    "v_mfma_f32_16x16x32_bf16 v[8:11], v[160:163], a[220:223], v[8:11]\n"
    "v_exp_f32 v53, v53\n"
    "v_mfma_f32_16x16x32_bf16 v[8:11], v[156:159], a[216:219], v[8:11]\n"
    "v_exp_f32 v54, v54\n"
    "v_mfma_f32_16x16x32_bf16 v[12:15], v[156:159], a[240:243], v[100:103]\n"
    "buffer_load_dwordx4 v1, s[16:19], s83 offen offset:2048 sc1 lds\n"
    "v_mfma_f32_16x16x32_bf16 v[12:15], v[160:163], a[244:247], v[12:15]\n"
    "v_cvt_pk_bf16_f32 v74, v52, v53\n"
    "v_mfma_f32_16x16x32_bf16 v[12:15], v[164:167], a[248:251], v[12:15]\n"
    "v_exp_f32 v55, v55\n"
    "v_mfma_f32_16x16x32_bf16 v[12:15], v[168:171], a[252:255], v[12:15]\n"
    "v_cvt_pk_bf16_f32 v75, v54, v55\n"
    "v_mfma_f32_16x16x32_bf16 v[12:15], v[172:175], v[124:127], v[12:15]\n"
    "buffer_load_dwordx4 v1, s[16:19], s83 offen offset:3072 sc1 lds\n"
    "v_mfma_f32_16x16x32_bf16 v[12:15], v[176:179], v[128:131], v[12:15]\n"
    "s_add_u32 m0, s64, 4096\n"
    "v_exp_f32 v56, v56\n"
    "v_mfma_f32_16x16x32_bf16 v[16:19], v[176:179], v[152:155], v[100:103]\n"
    "s_add_u32 s83, s67, 28672\n"
    "v_exp_f32 v50, v50\n"
    "v_mfma_f32_16x16x32_bf16 v[16:19], v[172:175], v[148:151], v[16:19]\n"
    "v_exp_f32 v51, v51\n"
    "v_mfma_f32_16x16x32_bf16 v[16:19], v[168:171], v[144:147], v[16:19]\n"
    "buffer_load_dwordx4 v1, s[16:19], s83 offen offset:0 sc1 lds\n"
    "v_mfma_f32_16x16x32_bf16 v[16:19], v[164:167], v[140:143], v[16:19]\n"
    "v_exp_f32 v57, v57\n"
    "v_mfma_f32_16x16x32_bf16 v[16:19], v[160:163], v[136:139], v[16:19]\n"
    "v_cvt_pk_bf16_f32 v82, v56, v57\n"
    "v_cvt_pk_bf16_f32 v97, v50, v51\n"
    "v_mfma_f32_16x16x32_bf16 v[16:19], v[156:159], v[132:135], v[16:19]\n"
    "s_waitcnt lgkmcnt(0)\n"
    "v_exp_f32 v58, v58\n"
    "v_mfma_f32_16x16x32_bf16 v[20:23], v[180:183], a[192:195], v[104:107]\n"
    "buffer_load_dwordx4 v1, s[16:19], s83 offen offset:1024 sc1 lds\n"
    "v_mfma_f32_16x16x32_bf16 v[20:23], v[184:187], a[196:199], v[20:23]\n"
    "s_add_u32 s67, s67, 49152\n"
    "ds_read_b128 v[156:159], v248 offset:0\n"
    "ds_read_b128 v[160:163], v248 offset:12288\n"
    "v_mfma_f32_16x16x32_bf16 v[20:23], v[188:191], a[200:203], v[20:23]\n"
    "s_add_u32 s59, s59, 4\n"
    "ds_read_b128 v[164:167], v248 offset:1024\n"
    "v_mfma_f32_16x16x32_bf16 v[20:23], v[192:195], a[204:207], v[20:23]\n"
    "s_add_u32 s81, s81, 128\n"
    "s_waitcnt vmcnt(12)\n"
    "ds_read_b128 v[168:171], v248 offset:13312\n"
    "v_mfma_f32_16x16x32_bf16 v[20:23], v[196:199], a[208:211], v[20:23]\n"
    "s_min_u32 s81, s81, 4096\n"
    "ds_read_b128 v[172:175], v248 offset:2048\n"
    "v_mfma_f32_16x16x32_bf16 v[20:23], v[200:203], a[212:215], v[20:23]\n"
    "buffer_store_dwordx2 v[238:239], v240, s[36:39], s80 offen offset:0 sc1\n"
    "v_mfma_f32_16x16x32_bf16 v[24:27], v[200:203], a[236:239], v[104:107]\n"
    "s_add_u32 s80, s80, 256\n"
    "ds_read_b128 v[176:179], v248 offset:14336\n"
    "v_mfma_f32_16x16x32_bf16 v[24:27], v[196:199], a[232:235], v[24:27]\n"
    "v_exp_f32 v59, v59\n"
    "v_mfma_f32_16x16x32_bf16 v[24:27], v[192:195], a[228:231], v[24:27]\n"
    "buffer_load_dwordx2 v[204:205], v242, s[28:31], s75 offen offset:0 nt\n"
    "v_mfma_f32_16x16x32_bf16 v[24:27], v[188:191], a[224:227], v[24:27]\n"
    "v_cvt_pk_bf16_f32 v83, v58, v59\n"
    "v_mfma_f32_16x16x32_bf16 v[24:27], v[184:187], a[220:223], v[24:27]\n"
    "buffer_load_dwordx2 v[206:207], v242, s[28:31], s75 offen offset:768 nt\n"
    "v_mfma_f32_16x16x32_bf16 v[24:27], v[180:183], a[216:219], v[24:27]\n"
    "v_exp_f32 v60, v60\n"
    "v_mfma_f32_16x16x32_bf16 v[28:31], v[180:183], a[240:243], v[104:107]\n"
    "buffer_load_dwordx2 v[208:209], v242, s[28:31], s75 offen offset:3072 nt\n"
    "v_mfma_f32_16x16x32_bf16 v[28:31], v[184:187], a[244:247], v[28:31]\n"
    "v_exp_f32 v61, v61\n"
    "v_mfma_f32_16x16x32_bf16 v[28:31], v[188:191], a[248:251], v[28:31]\n"
    "buffer_load_dwordx2 v[210:211], v242, s[28:31], s75 offen offset:3840 nt\n"
    "v_mfma_f32_16x16x32_bf16 v[28:31], v[192:195], a[252:255], v[28:31]\n"
    "v_exp_f32 v62, v62\n"
    "v_mfma_f32_16x16x32_bf16 v[28:31], v[196:199], v[124:127], v[28:31]\n"
    "buffer_load_dwordx2 v[212:213], v242, s[28:31], s76 offen offset:0 nt\n"
    "v_mfma_f32_16x16x32_bf16 v[28:31], v[200:203], v[128:131], v[28:31]\n"
    "v_exp_f32 v63, v63\n"
    "v_mfma_f32_16x16x32_bf16 v[32:35], v[200:203], v[152:155], v[104:107]\n"
    "v_cvt_pk_bf16_f32 v90, v60, v61\n"
    "v_cvt_pk_bf16_f32 v91, v62, v63\n"
    "v_mfma_f32_16x16x32_bf16 v[32:35], v[196:199], v[148:151], v[32:35]\n"
    "buffer_load_dwordx2 v[214:215], v242, s[28:31], s76 offen offset:768 nt\n"
    "v_mfma_f32_16x16x32_bf16 v[32:35], v[192:195], v[144:147], v[32:35]\n"
    "v_exp_f32 v64, v64\n"
    "v_mfma_f32_16x16x32_bf16 v[32:35], v[188:191], v[140:143], v[32:35]\n"
    "buffer_load_dwordx2 v[216:217], v242, s[28:31], s76 offen offset:3072 nt\n"
    "v_mfma_f32_16x16x32_bf16 v[32:35], v[184:187], v[136:139], v[32:35]\n"
    "v_exp_f32 v65, v65\n"
    "v_mfma_f32_16x16x32_bf16 v[32:35], v[180:183], v[132:135], v[32:35]\n"
    "s_waitcnt lgkmcnt(0)\n"
    "v_exp_f32 v66, v66\n"
    "v_mfma_f32_16x16x32_bf16 a[0:3], v[156:159], v[68:71], a[0:3]\n"
    "ds_read_b128 v[180:183], v248 offset:3072\n"
    "v_cvt_pk_bf16_f32 v98, v64, v65\n"
    "v_mfma_f32_16x16x32_bf16 a[0:3], v[160:163], v[72:75], a[0:3]\n"
    "ds_read_b128 v[184:187], v248 offset:15360\n"
    "v_mfma_f32_16x16x32_bf16 a[16:19], v[168:171], v[72:75], a[16:19]\n"
    "ds_read_b128 v[188:191], v248 offset:4096\n"
    "v_mfma_f32_16x16x32_bf16 a[16:19], v[164:167], v[68:71], a[16:19]\n"
    "ds_read_b128 v[192:195], v248 offset:16384\n"
    "v_mfma_f32_16x16x32_bf16 a[32:35], v[172:175], v[68:71], a[32:35]\n"
    "ds_read_b128 v[196:199], v248 offset:5120\n"
    "v_mfma_f32_16x16x32_bf16 a[32:35], v[176:179], v[72:75], a[32:35]\n"
    "ds_read_b128 v[200:203], v248 offset:17408\n"
    "v_mfma_f32_16x16x32_bf16 a[36:39], v[176:179], v[80:83], a[36:39]\n"
    "ds_read_b128 v[104:107], v251 offset:0\n"
    "v_mfma_f32_16x16x32_bf16 a[36:39], v[172:175], v[76:79], a[36:39]\n"
    "v_exp_f32 v67, v67\n"
    "v_mfma_f32_16x16x32_bf16 a[20:23], v[164:167], v[76:79], a[20:23]\n"
    "v_cvt_pk_bf16_f32 v99, v66, v67\n"
    "v_mfma_f32_16x16x32_bf16 a[20:23], v[168:171], v[80:83], a[20:23]\n"
    "buffer_load_dwordx2 v[218:219], v242, s[28:31], s76 offen offset:3840 nt\n"
    "v_mfma_f32_16x16x32_bf16 a[4:7], v[160:163], v[80:83], a[4:7]\n"
    "v_exp_f32 v4, v4\n"
    "v_mfma_f32_16x16x32_bf16 a[4:7], v[156:159], v[76:79], a[4:7]\n"
    "buffer_load_dwordx2 v[220:221], v242, s[28:31], s77 offen offset:0 nt\n"
    "v_mfma_f32_16x16x32_bf16 a[8:11], v[156:159], v[84:87], a[8:11]\n"
    "v_exp_f32 v5, v5\n"
    "v_mfma_f32_16x16x32_bf16 a[8:11], v[160:163], v[88:91], a[8:11]\n"
    "buffer_load_dwordx2 v[222:223], v242, s[28:31], s77 offen offset:768 nt\n"
    "v_mfma_f32_16x16x32_bf16 a[24:27], v[168:171], v[88:91], a[24:27]\n"
    "v_exp_f32 v6, v6\n"
    "v_mfma_f32_16x16x32_bf16 a[24:27], v[164:167], v[84:87], a[24:27]\n"
    "buffer_load_dwordx2 v[224:225], v242, s[28:31], s77 offen offset:3072 nt\n"
    "v_mfma_f32_16x16x32_bf16 a[40:43], v[172:175], v[84:87], a[40:43]\n"
    "v_exp_f32 v7, v7\n"
    "v_mfma_f32_16x16x32_bf16 a[40:43], v[176:179], v[88:91], a[40:43]\n"
    "buffer_load_dwordx2 v[226:227], v242, s[28:31], s77 offen offset:3840 nt\n"
    "v_mfma_f32_16x16x32_bf16 a[44:47], v[176:179], v[96:99], a[44:47]\n"
    "v_exp_f32 v8, v8\n"
    "v_mfma_f32_16x16x32_bf16 a[44:47], v[172:175], v[92:95], a[44:47]\n"
    "buffer_load_dwordx2 v[228:229], v242, s[28:31], s78 offen offset:0 nt\n"
    "v_mfma_f32_16x16x32_bf16 a[28:31], v[164:167], v[92:95], a[28:31]\n"
    "v_exp_f32 v9, v9\n"
    "v_mfma_f32_16x16x32_bf16 a[28:31], v[168:171], v[96:99], a[28:31]\n"
    "buffer_load_dwordx2 v[230:231], v242, s[28:31], s78 offen offset:768 nt\n"
    "v_mfma_f32_16x16x32_bf16 a[12:15], v[160:163], v[96:99], a[12:15]\n"
    "v_exp_f32 v10, v10\n"
    "v_mfma_f32_16x16x32_bf16 a[12:15], v[156:159], v[92:95], a[12:15]\n"
    "s_waitcnt lgkmcnt(0)\n"
    "v_exp_f32 v11, v11\n"
    "v_mfma_f32_16x16x32_bf16 a[48:51], v[180:183], v[68:71], a[48:51]\n"
    "ds_read_b128 v[156:159], v248 offset:6144\n"
    "v_mfma_f32_16x16x32_bf16 a[48:51], v[184:187], v[72:75], a[48:51]\n"
    "ds_read_b128 v[160:163], v248 offset:18432\n"
    "v_mfma_f32_16x16x32_bf16 a[64:67], v[192:195], v[72:75], a[64:67]\n"
    "ds_read_b128 v[164:167], v248 offset:7168\n"
    "v_mfma_f32_16x16x32_bf16 a[64:67], v[188:191], v[68:71], a[64:67]\n"
    "ds_read_b128 v[168:171], v248 offset:19456\n"
    "v_mfma_f32_16x16x32_bf16 a[80:83], v[196:199], v[68:71], a[80:83]\n"
    "ds_read_b128 v[172:175], v248 offset:8192\n"
    "v_mfma_f32_16x16x32_bf16 a[80:83], v[200:203], v[72:75], a[80:83]\n"
    "ds_read_b128 v[176:179], v248 offset:20480\n"
    "v_smfmac_f32_16x16x64_bf16 v[108:111], v[104:107], v[68:75], v253\n"
    "v_smfmac_f32_16x16x64_bf16 v[112:115], v[104:107], v[76:83], v253\n"
    "v_mfma_f32_16x16x32_bf16 a[84:87], v[200:203], v[80:83], a[84:87]\n"
    "buffer_load_dwordx2 v[232:233], v242, s[28:31], s78 offen offset:3072 nt\n"
    "v_mfma_f32_16x16x32_bf16 a[84:87], v[196:199], v[76:79], a[84:87]\n"
    "v_exp_f32 v12, v12\n"
    "v_mfma_f32_16x16x32_bf16 a[68:71], v[188:191], v[76:79], a[68:71]\n"
    "buffer_load_dwordx2 v[234:235], v242, s[28:31], s78 offen offset:3840 nt\n"
    "v_mfma_f32_16x16x32_bf16 a[68:71], v[192:195], v[80:83], a[68:71]\n"
    "v_exp_f32 v13, v13\n"
    "v_mfma_f32_16x16x32_bf16 a[52:55], v[184:187], v[80:83], a[52:55]\n"
    "v_exp_f32 v14, v14\n"
    "v_mfma_f32_16x16x32_bf16 a[52:55], v[180:183], v[76:79], a[52:55]\n"
    "v_exp_f32 v15, v15\n"
    "v_mfma_f32_16x16x32_bf16 a[56:59], v[180:183], v[84:87], a[56:59]\n"
    "v_exp_f32 v16, v16\n"
    "v_mfma_f32_16x16x32_bf16 a[56:59], v[184:187], v[88:91], a[56:59]\n"
    "v_exp_f32 v17, v17\n"
    "v_mfma_f32_16x16x32_bf16 a[72:75], v[192:195], v[88:91], a[72:75]\n"
    "v_mfma_f32_16x16x32_bf16 a[72:75], v[188:191], v[84:87], a[72:75]\n"
    "v_exp_f32 v18, v18\n"
    "v_mfma_f32_16x16x32_bf16 a[88:91], v[196:199], v[84:87], a[88:91]\n"
    "v_mfma_f32_16x16x32_bf16 a[88:91], v[200:203], v[88:91], a[88:91]\n"
    "v_smfmac_f32_16x16x64_bf16 v[116:119], v[104:107], v[84:91], v253\n"
    "v_smfmac_f32_16x16x64_bf16 v[120:123], v[104:107], v[92:99], v253\n"
    "v_mfma_f32_16x16x32_bf16 a[92:95], v[200:203], v[96:99], a[92:95]\n"
    "v_exp_f32 v19, v19\n"
    "v_mfma_f32_16x16x32_bf16 a[92:95], v[196:199], v[92:95], a[92:95]\n"
    "v_exp_f32 v20, v20\n"
    "v_mfma_f32_16x16x32_bf16 a[76:79], v[188:191], v[92:95], a[76:79]\n"
    "v_mfma_f32_16x16x32_bf16 a[76:79], v[192:195], v[96:99], a[76:79]\n"
    "v_exp_f32 v21, v21\n"
    "v_mfma_f32_16x16x32_bf16 a[60:63], v[184:187], v[96:99], a[60:63]\n"
    "v_mfma_f32_16x16x32_bf16 a[60:63], v[180:183], v[92:95], a[60:63]\n"
    "s_waitcnt lgkmcnt(0)\n"
    "v_mfma_f32_16x16x32_bf16 a[96:99], v[156:159], v[68:71], a[96:99]\n"
    "ds_read_b128 v[180:183], v248 offset:9216\n"
    "v_mfma_f32_16x16x32_bf16 a[96:99], v[160:163], v[72:75], a[96:99]\n"
    "ds_read_b128 v[184:187], v248 offset:21504\n"
    "v_mfma_f32_16x16x32_bf16 a[112:115], v[168:171], v[72:75], a[112:115]\n"
    "ds_read_b128 v[188:191], v248 offset:10240\n"
    "v_mfma_f32_16x16x32_bf16 a[112:115], v[164:167], v[68:71], a[112:115]\n"
    "ds_read_b128 v[192:195], v248 offset:22528\n"
    "v_mfma_f32_16x16x32_bf16 a[128:131], v[172:175], v[68:71], a[128:131]\n"
    "buffer_load_dwordx2 v[236:237], v241, s[36:39], s81 offen offset:0 sc1\n"
    "v_mfma_f32_16x16x32_bf16 a[128:131], v[176:179], v[72:75], a[128:131]\n"
    "ds_read_b128 v[196:199], v248 offset:11264\n"
    "ds_read_b128 v[200:203], v248 offset:23552\n"
    "v_mfma_f32_16x16x32_bf16 a[132:135], v[176:179], v[80:83], a[132:135]\n"
    "v_exp_f32 v22, v22\n"
    "v_mfma_f32_16x16x32_bf16 a[132:135], v[172:175], v[76:79], a[132:135]\n"
    "v_exp_f32 v23, v23\n"
    "v_mfma_f32_16x16x32_bf16 a[116:119], v[164:167], v[76:79], a[116:119]\n"
    "v_exp_f32 v24, v24\n"
    "v_mfma_f32_16x16x32_bf16 a[116:119], v[168:171], v[80:83], a[116:119]\n"
    "v_exp_f32 v25, v25\n"
    "v_mfma_f32_16x16x32_bf16 a[100:103], v[160:163], v[80:83], a[100:103]\n"
    "v_mfma_f32_16x16x32_bf16 a[100:103], v[156:159], v[76:79], a[100:103]\n"
    "v_exp_f32 v26, v26\n"
    "v_mfma_f32_16x16x32_bf16 a[104:107], v[156:159], v[84:87], a[104:107]\n"
    "v_mfma_f32_16x16x32_bf16 a[104:107], v[160:163], v[88:91], a[104:107]\n"
    "v_exp_f32 v27, v27\n"
    "v_mfma_f32_16x16x32_bf16 a[120:123], v[168:171], v[88:91], a[120:123]\n"
    "v_mfma_f32_16x16x32_bf16 a[120:123], v[164:167], v[84:87], a[120:123]\n"
    "v_mfma_f32_16x16x32_bf16 a[136:139], v[172:175], v[84:87], a[136:139]\n"
    "v_exp_f32 v28, v28\n"
    "v_mfma_f32_16x16x32_bf16 a[136:139], v[176:179], v[88:91], a[136:139]\n"
    "v_mfma_f32_16x16x32_bf16 a[140:143], v[176:179], v[96:99], a[140:143]\n"
    "v_mfma_f32_16x16x32_bf16 a[140:143], v[172:175], v[92:95], a[140:143]\n"
    "v_exp_f32 v29, v29\n"
    "v_mfma_f32_16x16x32_bf16 a[124:127], v[164:167], v[92:95], a[124:127]\n"
    "v_mfma_f32_16x16x32_bf16 a[124:127], v[168:171], v[96:99], a[124:127]\n"
    "v_exp_f32 v30, v30\n"
    "v_mfma_f32_16x16x32_bf16 a[108:111], v[160:163], v[96:99], a[108:111]\n"
    "v_mfma_f32_16x16x32_bf16 a[108:111], v[156:159], v[92:95], a[108:111]\n"
    "s_waitcnt lgkmcnt(0)\n"
    "v_mfma_f32_16x16x32_bf16 a[144:147], v[180:183], v[68:71], a[144:147]\n"
    "ds_read_b128 v[156:159], v3 offset:12288\n"
    "v_mfma_f32_16x16x32_bf16 a[144:147], v[184:187], v[72:75], a[144:147]\n"
    "ds_read_b128 v[160:163], v3 offset:14336\n"
    "v_mfma_f32_16x16x32_bf16 a[160:163], v[192:195], v[72:75], a[160:163]\n"
    "ds_read_b128 v[164:167], v3 offset:16384\n"
    "v_mfma_f32_16x16x32_bf16 a[160:163], v[188:191], v[68:71], a[160:163]\n"
    "ds_read_b128 v[168:171], v3 offset:18432\n"
    "v_mfma_f32_16x16x32_bf16 a[176:179], v[196:199], v[68:71], a[176:179]\n"
    "ds_read_b128 v[172:175], v3 offset:20480\n"
    "v_mfma_f32_16x16x32_bf16 a[176:179], v[200:203], v[72:75], a[176:179]\n"
    "ds_read_b128 v[176:179], v3 offset:22528\n"
    "v_mfma_f32_16x16x32_bf16 a[180:183], v[200:203], v[80:83], a[180:183]\n"
    "ds_read_b128 v[100:103], v250 offset:896\n"
    "v_mfma_f32_16x16x32_bf16 a[180:183], v[196:199], v[76:79], a[180:183]\n"
    "v_exp_f32 v31, v31\n"
    "v_mfma_f32_16x16x32_bf16 a[164:167], v[188:191], v[76:79], a[164:167]\n"
    "v_exp_f32 v32, v32\n"
    "v_mfma_f32_16x16x32_bf16 a[164:167], v[192:195], v[80:83], a[164:167]\n"
    "v_exp_f32 v33, v33\n"
    "v_mfma_f32_16x16x32_bf16 a[148:151], v[184:187], v[80:83], a[148:151]\n"
    "v_exp_f32 v34, v34\n"
    "v_mfma_f32_16x16x32_bf16 a[148:151], v[180:183], v[76:79], a[148:151]\n"
    "v_exp_f32 v35, v35\n"
    "v_mfma_f32_16x16x32_bf16 a[152:155], v[180:183], v[84:87], a[152:155]\n"
    "v_mfma_f32_16x16x32_bf16 a[152:155], v[184:187], v[88:91], a[152:155]\n"
    "v_mfma_f32_16x16x32_bf16 a[168:171], v[192:195], v[88:91], a[168:171]\n"
    "s_waitcnt vmcnt(18)\n"
    "ds_add_u32 v247, v246\n"
    "v_mfma_f32_16x16x32_bf16 a[168:171], v[188:191], v[84:87], a[168:171]\n"
    "v_mfma_f32_16x16x32_bf16 a[184:187], v[196:199], v[84:87], a[184:187]\n"
    "v_mfma_f32_16x16x32_bf16 a[184:187], v[200:203], v[88:91], a[184:187]\n"
    "v_mfma_f32_16x16x32_bf16 a[188:191], v[200:203], v[96:99], a[188:191]\n"
    "v_mfma_f32_16x16x32_bf16 a[188:191], v[196:199], v[92:95], a[188:191]\n"
    "v_mfma_f32_16x16x32_bf16 a[172:175], v[188:191], v[92:95], a[172:175]\n"
    "s_sub_u32 s74, s68, s65\n"
    "v_mfma_f32_16x16x32_bf16 a[172:175], v[192:195], v[96:99], a[172:175]\n"
    "s_add_u32 s66, s66, 1\n"
    "v_mov_b32 v2, v3\n"
    "v_add_u32 v3, s74, v252\n"
    "v_mfma_f32_16x16x32_bf16 a[156:159], v[184:187], v[96:99], a[156:159]\n"
    "s_waitcnt lgkmcnt(1)\n"
    "s_cmp_eq_u32 s66, 31\n"
    "v_mfma_f32_16x16x32_bf16 a[156:159], v[180:183], v[92:95], a[156:159]\n"
    "s_cbranch_scc1 .LtailP\n"
    ".LNK:\n"
    "v_mfma_f32_16x16x32_bf16 v[36:39], v[156:159], a[192:195], v[100:103]\n"
    "ds_read_b128 v[180:183], v2 offset:13312\n"
    "v_cvt_pk_bf16_f32 v68, v4, v5\n"
    "v_mfma_f32_16x16x32_bf16 v[36:39], v[160:163], a[196:199], v[36:39]\n"
    "ds_read_b128 v[184:187], v2 offset:15360\n"
    "v_cvt_pk_bf16_f32 v69, v6, v7\n"
    "v_mfma_f32_16x16x32_bf16 v[36:39], v[164:167], a[200:203], v[36:39]\n"
    "ds_read_b128 v[188:191], v2 offset:17408\n"
    "v_cvt_pk_bf16_f32 v76, v8, v9\n"
    "v_mfma_f32_16x16x32_bf16 v[36:39], v[168:171], a[204:207], v[36:39]\n"
    "ds_read_b128 v[192:195], v2 offset:19456\n"
    "v_cvt_pk_bf16_f32 v77, v10, v11\n"
    "v_mfma_f32_16x16x32_bf16 v[36:39], v[172:175], a[208:211], v[36:39]\n"
    "ds_read_b128 v[196:199], v2 offset:21504\n"
    "v_cvt_pk_bf16_f32 v84, v12, v13\n"
    "v_mfma_f32_16x16x32_bf16 v[36:39], v[176:179], a[212:215], v[36:39]\n"
    "ds_read_b128 v[200:203], v2 offset:23552\n"
    "v_cvt_pk_bf16_f32 v85, v14, v15\n"
    "v_mfma_f32_16x16x32_bf16 v[40:43], v[176:179], a[236:239], v[100:103]\n"
    "ds_read_b128 v[104:107], v250 offset:912\n"
    "v_cvt_pk_bf16_f32 v92, v16, v17\n"
    "v_mfma_f32_16x16x32_bf16 v[40:43], v[172:175], a[232:235], v[40:43]\n"
    "s_add_u32 s68, s68, 24576\n"
    "v_cvt_pk_bf16_f32 v93, v18, v19\n"
    "v_cvt_pk_bf16_f32 v70, v20, v21\n"
    "v_mfma_f32_16x16x32_bf16 v[40:43], v[168:171], a[228:231], v[40:43]\n"
    "s_cmp_ge_u32 s68, s69\n"
    "s_cselect_b32 s68, s65, s68\n"
    "v_cvt_pk_bf16_f32 v71, v22, v23\n"
    "v_mfma_f32_16x16x32_bf16 v[40:43], v[164:167], a[224:227], v[40:43]\n"
    "v_cvt_pk_bf16_f32 v78, v24, v25\n"
    "v_cvt_pk_bf16_f32 v79, v26, v27\n"
    "v_mfma_f32_16x16x32_bf16 v[40:43], v[160:163], a[220:223], v[40:43]\n"
    "v_cvt_pk_bf16_f32 v86, v28, v29\n"
    "v_cvt_pk_bf16_f32 v87, v30, v31\n"
    "v_mfma_f32_16x16x32_bf16 v[40:43], v[156:159], a[216:219], v[40:43]\n"
    "v_cvt_pk_bf16_f32 v94, v32, v33\n"
    "v_cvt_pk_bf16_f32 v95, v34, v35\n"
    "v_mfma_f32_16x16x32_bf16 v[44:47], v[156:159], a[240:243], v[100:103]\n"
    "v_exp_f32 v36, v36\n"
    "v_mfma_f32_16x16x32_bf16 v[44:47], v[160:163], a[244:247], v[44:47]\n"
    "v_exp_f32 v37, v37\n"
    "v_mfma_f32_16x16x32_bf16 v[44:47], v[164:167], a[248:251], v[44:47]\n"
    "v_exp_f32 v38, v38\n"
    "v_mfma_f32_16x16x32_bf16 v[44:47], v[168:171], a[252:255], v[44:47]\n"
    "v_cvt_pk_bf16_f32 v72, v36, v37\n"
    "v_mfma_f32_16x16x32_bf16 v[44:47], v[172:175], v[124:127], v[44:47]\n"
    "v_exp_f32 v39, v39\n"
    "v_mfma_f32_16x16x32_bf16 v[44:47], v[176:179], v[128:131], v[44:47]\n"
    "v_cvt_pk_bf16_f32 v73, v38, v39\n"
    "v_mfma_f32_16x16x32_bf16 v[48:51], v[176:179], v[152:155], v[100:103]\n"
    "ds_read_b32 v245, v247\n"
    "v_mfma_f32_16x16x32_bf16 v[48:51], v[172:175], v[148:151], v[48:51]\n"
    "s_add_u32 m0, s68, 0\n"
    "v_exp_f32 v40, v40\n"
    "v_mfma_f32_16x16x32_bf16 v[48:51], v[168:171], v[144:147], v[48:51]\n"
    "v_mfma_f32_16x16x32_bf16 v[48:51], v[164:167], v[140:143], v[48:51]\n"
    "v_mfma_f32_16x16x32_bf16 v[48:51], v[160:163], v[136:139], v[48:51]\n"
    "s_waitcnt vmcnt(0)\n"
    "v_cmp_eq_u64 s[54:55], v[236:237], v[238:239]\n"
    "v_mfma_f32_16x16x32_bf16 v[48:51], v[156:159], v[132:135], v[48:51]\n"
    "s_waitcnt lgkmcnt(1)\n"
    "v_exp_f32 v41, v41\n"
    "v_mfma_f32_16x16x32_bf16 v[52:55], v[180:183], a[192:195], v[104:107]\n"
    "s_waitcnt lgkmcnt(0)\n"
    "s_waitcnt vmcnt(1)\n"
    "v_cmp_le_u32 s[56:57], s59, v245\n"
    "v_mfma_f32_16x16x32_bf16 v[52:55], v[184:187], a[196:199], v[52:55]\n"
    "s_add_u32 s76, s71, s60\n"
    "v_exp_f32 v42, v42\n"
    "v_mfma_f32_16x16x32_bf16 v[52:55], v[188:191], a[200:203], v[52:55]\n"
    "s_and_b64 s[54:55], s[54:55], s[56:57]\n"
    "s_cmp_eq_u64 s[54:55], -1\n"
    "v_mfma_f32_16x16x32_bf16 v[52:55], v[192:195], a[204:207], v[52:55]\n"
    "s_cbranch_scc0 .Lpoll2\n"
    ".Lpoll2r:\n"
    "v_mfma_f32_16x16x32_bf16 v[52:55], v[196:199], a[208:211], v[52:55]\n"
    "buffer_load_dwordx4 v1, s[16:19], s67 offen offset:0 sc1 lds\n"
    "v_mfma_f32_16x16x32_bf16 v[52:55], v[200:203], a[212:215], v[52:55]\n"
    "s_add_u32 s77, s71, s61\n"
    "ds_read_b128 v[156:159], v3 offset:0\n"
    "ds_read_b128 v[160:163], v3 offset:2048\n"
    "v_mfma_f32_16x16x32_bf16 v[56:59], v[200:203], a[236:239], v[104:107]\n"
    "ds_read_b128 v[164:167], v3 offset:4096\n"
    "ds_read_b128 v[168:171], v3 offset:6144\n"
    "v_mfma_f32_16x16x32_bf16 v[56:59], v[196:199], a[232:235], v[56:59]\n"
    "ds_read_b128 v[172:175], v3 offset:8192\n"
    "ds_read_b128 v[176:179], v3 offset:10240\n"
    "v_mfma_f32_16x16x32_bf16 v[56:59], v[192:195], a[228:231], v[56:59]\n"
    "buffer_load_dwordx4 v1, s[16:19], s67 offen offset:1024 sc1 lds\n"
    "v_mfma_f32_16x16x32_bf16 v[56:59], v[188:191], a[224:227], v[56:59]\n"
    "ds_read_b128 v[100:103], v250 offset:1536\n"
    "v_cvt_pk_bf16_f32 v80, v40, v41\n"
    "v_mfma_f32_16x16x32_bf16 v[56:59], v[184:187], a[220:223], v[56:59]\n"
    "v_exp_f32 v43, v43\n"
    "v_mfma_f32_16x16x32_bf16 v[56:59], v[180:183], a[216:219], v[56:59]\n"
    "v_cvt_pk_bf16_f32 v81, v42, v43\n"
    "v_cvt_f32_i32 v204, v204\n"
    "v_mfma_f32_16x16x32_bf16 v[60:63], v[180:183], a[240:243], v[104:107]\n"
    "buffer_load_dwordx4 v1, s[16:19], s67 offen offset:2048 sc1 lds\n"
    "v_mfma_f32_16x16x32_bf16 v[60:63], v[184:187], a[244:247], v[60:63]\n"
    "v_exp_f32 v44, v44\n"
    "v_mfma_f32_16x16x32_bf16 v[60:63], v[188:191], a[248:251], v[60:63]\n"
    "v_exp_f32 v45, v45\n"
    "v_mfma_f32_16x16x32_bf16 v[60:63], v[192:195], a[252:255], v[60:63]\n"
    "v_exp_f32 v46, v46\n"
    "v_mfma_f32_16x16x32_bf16 v[60:63], v[196:199], v[124:127], v[60:63]\n"
    "buffer_load_dwordx4 v1, s[16:19], s67 offen offset:3072 sc1 lds\n"
    "v_mfma_f32_16x16x32_bf16 v[60:63], v[200:203], v[128:131], v[60:63]\n"
    "s_add_u32 m0, s68, 4096\n"
    "v_exp_f32 v47, v47\n"
    "v_mfma_f32_16x16x32_bf16 v[64:67], v[200:203], v[152:155], v[104:107]\n"
    "s_add_u32 s83, s67, 4096\n"
    "v_cvt_pk_bf16_f32 v88, v44, v45\n"
    "v_cvt_pk_bf16_f32 v89, v46, v47\n"
    "v_mfma_f32_16x16x32_bf16 v[64:67], v[196:199], v[148:151], v[64:67]\n"
    "v_exp_f32 v48, v48\n"
    "v_mfma_f32_16x16x32_bf16 v[64:67], v[192:195], v[144:147], v[64:67]\n"
    "buffer_load_dwordx4 v1, s[16:19], s83 offen offset:0 sc1 lds\n"
    "v_mfma_f32_16x16x32_bf16 v[64:67], v[188:191], v[140:143], v[64:67]\n"
    "v_exp_f32 v49, v49\n"
    "v_mfma_f32_16x16x32_bf16 v[64:67], v[184:187], v[136:139], v[64:67]\n"
    "v_cvt_pk_bf16_f32 v96, v48, v49\n"
    "v_cvt_f32_i32 v205, v205\n"
    "v_mfma_f32_16x16x32_bf16 v[64:67], v[180:183], v[132:135], v[64:67]\n"
    "s_waitcnt lgkmcnt(0)\n"
    "v_cvt_f32_i32 v206, v206\n"
    "v_cvt_f32_i32 v207, v207\n"
    "v_mfma_f32_16x16x32_bf16 v[4:7], v[156:159], a[192:195], v[100:103]\n"
    "buffer_load_dwordx4 v1, s[16:19], s83 offen offset:1024 sc1 lds\n"
    "v_mfma_f32_16x16x32_bf16 v[4:7], v[160:163], a[196:199], v[4:7]\n"
    "s_add_u32 m0, s70, 0\n"
    "ds_read_b128 v[180:183], v3 offset:1024\n"
    "ds_read_b128 v[184:187], v3 offset:3072\n"
    "v_mfma_f32_16x16x32_bf16 v[4:7], v[164:167], a[200:203], v[4:7]\n"
    "s_add_u32 s83, s67, 24576\n"
    "ds_read_b128 v[188:191], v3 offset:5120\n"
    "v_cvt_f32_i32 v208, v208\n"
    "v_mfma_f32_16x16x32_bf16 v[4:7], v[168:171], a[204:207], v[4:7]\n"
    "ds_read_b128 v[192:195], v3 offset:7168\n"
    "v_cvt_f32_i32 v209, v209\n"
    "v_mfma_f32_16x16x32_bf16 v[4:7], v[172:175], a[208:211], v[4:7]\n"
    "buffer_load_dwordx4 v1, s[16:19], s83 offen offset:0 sc1 lds\n"
    "v_mfma_f32_16x16x32_bf16 v[4:7], v[176:179], a[212:215], v[4:7]\n"
    "ds_read_b128 v[196:199], v3 offset:9216\n"
    "ds_read_b128 v[200:203], v3 offset:11264\n"
    "v_mfma_f32_16x16x32_bf16 v[8:11], v[176:179], a[236:239], v[100:103]\n"
    "ds_read_b128 v[104:107], v250 offset:1552\n"
    "v_cvt_f32_i32 v210, v210\n"
    "v_mfma_f32_16x16x32_bf16 v[8:11], v[172:175], a[232:235], v[8:11]\n"
    "v_exp_f32 v52, v52\n"
    "v_mfma_f32_16x16x32_bf16 v[8:11], v[168:171], a[228:231], v[8:11]\n"
    "buffer_load_dwordx4 v1, s[16:19], s83 offen offset:1024 sc1 lds\n"
    "v_mfma_f32_16x16x32_bf16 v[8:11], v[164:167], a[224:227], v[8:11]\n"
    "v_exp_f32 v53, v53\n"
    "v_mfma_f32_16x16x32_bf16 v[8:11], v[160:163], a[220:223], v[8:11]\n"
    "v_cvt_pk_bf16_f32 v74, v52, v53\n"
    "v_cvt_f32_i32 v211, v211\n"
    "v_mfma_f32_16x16x32_bf16 v[8:11], v[156:159], a[216:219], v[8:11]\n"
    "v_exp_f32 v54, v54\n"
    "v_mfma_f32_16x16x32_bf16 v[12:15], v[156:159], a[240:243], v[100:103]\n"
    "buffer_load_dwordx4 v1, s[16:19], s83 offen offset:2048 sc1 lds\n"
    "v_mfma_f32_16x16x32_bf16 v[12:15], v[160:163], a[244:247], v[12:15]\n"
    "v_exp_f32 v50, v50\n"
    "v_mfma_f32_16x16x32_bf16 v[12:15], v[164:167], a[248:251], v[12:15]\n"
    "v_exp_f32 v55, v55\n"
    "v_mfma_f32_16x16x32_bf16 v[12:15], v[168:171], a[252:255], v[12:15]\n"
    "v_cvt_pk_bf16_f32 v75, v54, v55\n"
    "v_cvt_f32_i32 v212, v212\n"
    "v_mfma_f32_16x16x32_bf16 v[12:15], v[172:175], v[124:127], v[12:15]\n"
    "buffer_load_dwordx4 v1, s[16:19], s83 offen offset:3072 sc1 lds\n"
    "v_mfma_f32_16x16x32_bf16 v[12:15], v[176:179], v[128:131], v[12:15]\n"
    "s_add_u32 m0, s70, 4096\n"
    "v_exp_f32 v56, v56\n"
    "v_mfma_f32_16x16x32_bf16 v[16:19], v[176:179], v[152:155], v[100:103]\n"
    "s_add_u32 s83, s67, 28672\n"
    "v_exp_f32 v51, v51\n"
    "v_mfma_f32_16x16x32_bf16 v[16:19], v[172:175], v[148:151], v[16:19]\n"
    "v_cvt_pk_bf16_f32 v97, v50, v51\n"
    "v_cvt_f32_i32 v213, v213\n"
    "v_mfma_f32_16x16x32_bf16 v[16:19], v[168:171], v[144:147], v[16:19]\n"
    "buffer_load_dwordx4 v1, s[16:19], s83 offen offset:0 sc1 lds\n"
    "v_mfma_f32_16x16x32_bf16 v[16:19], v[164:167], v[140:143], v[16:19]\n"
    "v_exp_f32 v57, v57\n"
    "v_mfma_f32_16x16x32_bf16 v[16:19], v[160:163], v[136:139], v[16:19]\n"
    "v_cvt_pk_bf16_f32 v82, v56, v57\n"
    "v_cvt_f32_i32 v214, v214\n"
    "v_mfma_f32_16x16x32_bf16 v[16:19], v[156:159], v[132:135], v[16:19]\n"
    "s_waitcnt lgkmcnt(0)\n"
    "v_exp_f32 v58, v58\n"
    "v_mfma_f32_16x16x32_bf16 v[20:23], v[180:183], a[192:195], v[104:107]\n"
    "buffer_load_dwordx4 v1, s[16:19], s83 offen offset:1024 sc1 lds\n"
    "v_mfma_f32_16x16x32_bf16 v[20:23], v[184:187], a[196:199], v[20:23]\n"
    "s_add_u32 s67, s67, 49152\n"
    "ds_read_b128 v[156:159], v249 offset:0\n"
    "ds_read_b128 v[160:163], v249 offset:12288\n"
    "v_mfma_f32_16x16x32_bf16 v[20:23], v[188:191], a[200:203], v[20:23]\n"
    "s_add_u32 s59, s59, 4\n"
    "ds_read_b128 v[164:167], v249 offset:1024\n"
    "v_cvt_f32_i32 v215, v215\n"
    "v_mfma_f32_16x16x32_bf16 v[20:23], v[192:195], a[204:207], v[20:23]\n"
    "s_add_u32 s81, s81, 128\n"
    "ds_read_b128 v[168:171], v249 offset:13312\n"
    "ds_read_b128 v[36:39], v243 offset:0\n"
    "v_mfma_f32_16x16x32_bf16 v[20:23], v[196:199], a[208:211], v[20:23]\n"
    "s_min_u32 s81, s81, 4096\n"
    "ds_read_b128 v[172:175], v249 offset:2048\n"
    "v_add_u32 v243, 512, v243\n"
    "v_mfma_f32_16x16x32_bf16 v[20:23], v[200:203], a[212:215], v[20:23]\n"
    "ds_read_b128 v[176:179], v249 offset:14336\n"
    "v_cvt_f32_i32 v216, v216\n"
    "v_mfma_f32_16x16x32_bf16 v[24:27], v[200:203], a[236:239], v[104:107]\n"
    "v_exp_f32 v59, v59\n"
    "v_mfma_f32_16x16x32_bf16 v[24:27], v[196:199], a[232:235], v[24:27]\n"
    "v_cvt_pk_bf16_f32 v83, v58, v59\n"
    "v_cvt_f32_i32 v217, v217\n"
    "v_mfma_f32_16x16x32_bf16 v[24:27], v[192:195], a[228:231], v[24:27]\n"
    "v_exp_f32 v60, v60\n"
    "v_mfma_f32_16x16x32_bf16 v[24:27], v[188:191], a[224:227], v[24:27]\n"
    "v_exp_f32 v61, v61\n"
    "v_mfma_f32_16x16x32_bf16 v[24:27], v[184:187], a[220:223], v[24:27]\n"
    "v_exp_f32 v62, v62\n"
    "v_mfma_f32_16x16x32_bf16 v[24:27], v[180:183], a[216:219], v[24:27]\n"
    "v_cvt_pk_bf16_f32 v90, v60, v61\n"
    "v_cvt_f32_i32 v218, v218\n"
    "v_mfma_f32_16x16x32_bf16 v[28:31], v[180:183], a[240:243], v[104:107]\n"
    "v_cvt_f32_i32 v219, v219\n"
    "v_cvt_f32_i32 v220, v220\n"
    "v_mfma_f32_16x16x32_bf16 v[28:31], v[184:187], a[244:247], v[28:31]\n"
    "v_exp_f32 v63, v63\n"
    "v_mfma_f32_16x16x32_bf16 v[28:31], v[188:191], a[248:251], v[28:31]\n"
    "v_cvt_pk_bf16_f32 v91, v62, v63\n"
    "v_cvt_f32_i32 v221, v221\n"
    "v_mfma_f32_16x16x32_bf16 v[28:31], v[192:195], a[252:255], v[28:31]\n"
    "v_cvt_f32_i32 v222, v222\n"
    "v_cvt_f32_i32 v223, v223\n"
    "v_mfma_f32_16x16x32_bf16 v[28:31], v[196:199], v[124:127], v[28:31]\n"
    "v_exp_f32 v64, v64\n"
    "v_mfma_f32_16x16x32_bf16 v[28:31], v[200:203], v[128:131], v[28:31]\n"
    "v_cvt_f32_i32 v224, v224\n"
    "v_cvt_f32_i32 v225, v225\n"
    "v_mfma_f32_16x16x32_bf16 v[32:35], v[200:203], v[152:155], v[104:107]\n"
    "v_cvt_f32_i32 v226, v226\n"
    "v_cvt_f32_i32 v227, v227\n"
    "v_mfma_f32_16x16x32_bf16 v[32:35], v[196:199], v[148:151], v[32:35]\n"
    "v_exp_f32 v65, v65\n"
    "v_mfma_f32_16x16x32_bf16 v[32:35], v[192:195], v[144:147], v[32:35]\n"
    "s_waitcnt lgkmcnt(2)\n"
    "v_cvt_pk_bf16_f32 v98, v64, v65\n"
    "v_add_f32 v38, s35, v38\n"
    "v_mfma_f32_16x16x32_bf16 v[32:35], v[188:191], v[140:143], v[32:35]\n"
    "v_add_f32 v39, s35, v39\n"
    "v_fma_f32 v204, v204, v36, v38\n"
    "v_mfma_f32_16x16x32_bf16 v[32:35], v[184:187], v[136:139], v[32:35]\n"
    "v_exp_f32 v66, v66\n"
    "v_mfma_f32_16x16x32_bf16 v[32:35], v[180:183], v[132:135], v[32:35]\n"
    "s_waitcnt lgkmcnt(0)\n"
    "v_fma_f32 v205, v205, v37, v39\n"
    "v_fma_f32 v206, v206, v36, v38\n"
    "v_mfma_f32_16x16x32_bf16 a[0:3], v[156:159], v[68:71], a[0:3]\n"
    "ds_read_b128 v[180:183], v249 offset:3072\n"
    "v_fma_f32 v207, v207, v37, v39\n"
    "v_mfma_f32_16x16x32_bf16 a[0:3], v[160:163], v[72:75], a[0:3]\n"
    "ds_read_b128 v[184:187], v249 offset:15360\n"
    "v_cvt_pk_bf16_f32 v204, v204, v205\n"
    "v_mfma_f32_16x16x32_bf16 a[16:19], v[168:171], v[72:75], a[16:19]\n"
    "ds_read_b128 v[188:191], v249 offset:4096\n"
    "v_cvt_pk_bf16_f32 v206, v206, v207\n"
    "v_mfma_f32_16x16x32_bf16 a[16:19], v[164:167], v[68:71], a[16:19]\n"
    "ds_read_b128 v[192:195], v249 offset:16384\n"
    "v_cvt_f32_i32 v228, v228\n"
    "v_mfma_f32_16x16x32_bf16 a[32:35], v[172:175], v[68:71], a[32:35]\n"
    "ds_read_b128 v[196:199], v249 offset:5120\n"
    "v_cvt_f32_i32 v229, v229\n"
    "v_mfma_f32_16x16x32_bf16 a[32:35], v[176:179], v[72:75], a[32:35]\n"
    "ds_read_b128 v[200:203], v249 offset:17408\n"
    "v_cvt_f32_i32 v230, v230\n"
    "v_mfma_f32_16x16x32_bf16 a[36:39], v[176:179], v[80:83], a[36:39]\n"
    "ds_read_b128 v[104:107], v251 offset:768\n"
    "v_cvt_f32_i32 v231, v231\n"
    "v_mfma_f32_16x16x32_bf16 a[36:39], v[172:175], v[76:79], a[36:39]\n"
    "v_exp_f32 v67, v67\n"
    "v_mfma_f32_16x16x32_bf16 a[20:23], v[164:167], v[76:79], a[20:23]\n"
    "v_cvt_pk_bf16_f32 v99, v66, v67\n"
    "v_fma_f32 v208, v208, v36, v38\n"
    "v_mfma_f32_16x16x32_bf16 a[20:23], v[168:171], v[80:83], a[20:23]\n"
    "buffer_store_dword v204, v244, s[16:19], s76 offen offset:0 sc1\n"
    "v_mfma_f32_16x16x32_bf16 a[4:7], v[160:163], v[80:83], a[4:7]\n"
    "v_fma_f32 v209, v209, v37, v39\n"
    "v_fma_f32 v210, v210, v36, v38\n"
    "v_mfma_f32_16x16x32_bf16 a[4:7], v[156:159], v[76:79], a[4:7]\n"
    "buffer_store_dword v206, v244, s[16:19], s76 offen offset:128 sc1\n"
    "v_mfma_f32_16x16x32_bf16 a[8:11], v[156:159], v[84:87], a[8:11]\n"
    "v_fma_f32 v211, v211, v37, v39\n"
    "v_cvt_pk_bf16_f32 v208, v208, v209\n"
    "v_mfma_f32_16x16x32_bf16 a[8:11], v[160:163], v[88:91], a[8:11]\n"
    "buffer_store_dword v208, v244, s[16:19], s76 offen offset:1024 sc1\n"
    "v_mfma_f32_16x16x32_bf16 a[24:27], v[168:171], v[88:91], a[24:27]\n"
    "v_cvt_pk_bf16_f32 v210, v210, v211\n"
    "v_cvt_f32_i32 v232, v232\n"
    "v_mfma_f32_16x16x32_bf16 a[24:27], v[164:167], v[84:87], a[24:27]\n"
    "buffer_store_dword v210, v244, s[16:19], s76 offen offset:1152 sc1\n"
    "v_mfma_f32_16x16x32_bf16 a[40:43], v[172:175], v[84:87], a[40:43]\n"
    "v_exp_f32 v4, v4\n"
    "v_mfma_f32_16x16x32_bf16 a[40:43], v[176:179], v[88:91], a[40:43]\n"
    "v_cvt_f32_i32 v233, v233\n"
    "v_cvt_f32_i32 v234, v234\n"
    "v_mfma_f32_16x16x32_bf16 a[44:47], v[176:179], v[96:99], a[44:47]\n"
    "v_exp_f32 v5, v5\n"
    "v_mfma_f32_16x16x32_bf16 a[44:47], v[172:175], v[92:95], a[44:47]\n"
    "v_cvt_f32_i32 v235, v235\n"
    "v_fma_f32 v212, v212, v36, v38\n"
    "v_mfma_f32_16x16x32_bf16 a[28:31], v[164:167], v[92:95], a[28:31]\n"
    "v_exp_f32 v6, v6\n"
    "v_mfma_f32_16x16x32_bf16 a[28:31], v[168:171], v[96:99], a[28:31]\n"
    "v_exp_f32 v7, v7\n"
    "v_mfma_f32_16x16x32_bf16 a[12:15], v[160:163], v[96:99], a[12:15]\n"
    "v_fma_f32 v213, v213, v37, v39\n"
    "v_fma_f32 v214, v214, v36, v38\n"
    "v_mfma_f32_16x16x32_bf16 a[12:15], v[156:159], v[92:95], a[12:15]\n"
    "s_waitcnt lgkmcnt(0)\n"
    "v_exp_f32 v8, v8\n"
    "v_mfma_f32_16x16x32_bf16 a[48:51], v[180:183], v[68:71], a[48:51]\n"
    "ds_read_b128 v[156:159], v249 offset:6144\n"
    "v_fma_f32 v215, v215, v37, v39\n"
    "v_mfma_f32_16x16x32_bf16 a[48:51], v[184:187], v[72:75], a[48:51]\n"
    "ds_read_b128 v[160:163], v249 offset:18432\n"
    "v_cvt_pk_bf16_f32 v212, v212, v213\n"
    "v_mfma_f32_16x16x32_bf16 a[64:67], v[192:195], v[72:75], a[64:67]\n"
    "buffer_store_dword v212, v244, s[16:19], s76 offen offset:256 sc1\n"
    "v_mfma_f32_16x16x32_bf16 a[64:67], v[188:191], v[68:71], a[64:67]\n"
    "ds_read_b128 v[164:167], v249 offset:7168\n"
    "ds_read_b128 v[168:171], v249 offset:19456\n"
    "v_mfma_f32_16x16x32_bf16 a[80:83], v[196:199], v[68:71], a[80:83]\n"
    "ds_read_b128 v[172:175], v249 offset:8192\n"
    "v_cvt_pk_bf16_f32 v214, v214, v215\n"
    "v_mfma_f32_16x16x32_bf16 a[80:83], v[200:203], v[72:75], a[80:83]\n"
    "buffer_store_dword v214, v244, s[16:19], s76 offen offset:384 sc1\n"
    "v_smfmac_f32_16x16x64_bf16 v[108:111], v[104:107], v[68:75], v253\n"
    "v_smfmac_f32_16x16x64_bf16 v[112:115], v[104:107], v[76:83], v253\n"
    "v_mfma_f32_16x16x32_bf16 a[84:87], v[200:203], v[80:83], a[84:87]\n"
    "ds_read_b128 v[176:179], v249 offset:20480\n"
    "v_fma_f32 v216, v216, v36, v38\n"
    "v_mfma_f32_16x16x32_bf16 a[84:87], v[196:199], v[76:79], a[84:87]\n"
    "v_exp_f32 v9, v9\n"
    "v_mfma_f32_16x16x32_bf16 a[68:71], v[188:191], v[76:79], a[68:71]\n"
    "v_exp_f32 v10, v10\n"
    "v_mfma_f32_16x16x32_bf16 a[68:71], v[192:195], v[80:83], a[68:71]\n"
    "v_fma_f32 v217, v217, v37, v39\n"
    "v_fma_f32 v218, v218, v36, v38\n"
    "v_mfma_f32_16x16x32_bf16 a[52:55], v[184:187], v[80:83], a[52:55]\n"
    "v_exp_f32 v11, v11\n"
    "v_mfma_f32_16x16x32_bf16 a[52:55], v[180:183], v[76:79], a[52:55]\n"
    "v_exp_f32 v12, v12\n"
    "v_mfma_f32_16x16x32_bf16 a[56:59], v[180:183], v[84:87], a[56:59]\n"
    "v_fma_f32 v219, v219, v37, v39\n"
    "v_cvt_pk_bf16_f32 v216, v216, v217\n"
    "v_mfma_f32_16x16x32_bf16 a[56:59], v[184:187], v[88:91], a[56:59]\n"
    "v_exp_f32 v13, v13\n"
    "v_mfma_f32_16x16x32_bf16 a[72:75], v[192:195], v[88:91], a[72:75]\n"
    "v_cvt_pk_bf16_f32 v218, v218, v219\n"
    "v_fma_f32 v220, v220, v36, v38\n"
    "v_mfma_f32_16x16x32_bf16 a[72:75], v[188:191], v[84:87], a[72:75]\n"
    "v_exp_f32 v14, v14\n"
    "v_mfma_f32_16x16x32_bf16 a[88:91], v[196:199], v[84:87], a[88:91]\n"
    "v_fma_f32 v221, v221, v37, v39\n"
    "v_fma_f32 v222, v222, v36, v38\n"
    "v_mfma_f32_16x16x32_bf16 a[88:91], v[200:203], v[88:91], a[88:91]\n"
    "v_exp_f32 v15, v15\n"
    "v_smfmac_f32_16x16x64_bf16 v[116:119], v[104:107], v[84:91], v253\n"
    "v_smfmac_f32_16x16x64_bf16 v[120:123], v[104:107], v[92:99], v253\n"
    "v_mfma_f32_16x16x32_bf16 a[92:95], v[200:203], v[96:99], a[92:95]\n"
    "v_fma_f32 v223, v223, v37, v39\n"
    "v_cvt_pk_bf16_f32 v220, v220, v221\n"
    "v_mfma_f32_16x16x32_bf16 a[92:95], v[196:199], v[92:95], a[92:95]\n"
    "v_exp_f32 v16, v16\n"
    "v_mfma_f32_16x16x32_bf16 a[76:79], v[188:191], v[92:95], a[76:79]\n"
    "v_exp_f32 v17, v17\n"
    "v_mfma_f32_16x16x32_bf16 a[76:79], v[192:195], v[96:99], a[76:79]\n"
    "v_cvt_pk_bf16_f32 v222, v222, v223\n"
    "v_fma_f32 v224, v224, v36, v38\n"
    "v_mfma_f32_16x16x32_bf16 a[60:63], v[184:187], v[96:99], a[60:63]\n"
    "v_exp_f32 v18, v18\n"
    "v_mfma_f32_16x16x32_bf16 a[60:63], v[180:183], v[92:95], a[60:63]\n"
    "s_waitcnt lgkmcnt(0)\n"
    "v_fma_f32 v225, v225, v37, v39\n"
    "v_fma_f32 v226, v226, v36, v38\n"
    "v_mfma_f32_16x16x32_bf16 a[96:99], v[156:159], v[68:71], a[96:99]\n"
    "ds_read_b128 v[180:183], v249 offset:9216\n"
    "v_fma_f32 v227, v227, v37, v39\n"
    "v_mfma_f32_16x16x32_bf16 a[96:99], v[160:163], v[72:75], a[96:99]\n"
    "ds_read_b128 v[184:187], v249 offset:21504\n"
    "v_cvt_pk_bf16_f32 v224, v224, v225\n"
    "v_mfma_f32_16x16x32_bf16 a[112:115], v[168:171], v[72:75], a[112:115]\n"
    "ds_read_b128 v[188:191], v249 offset:10240\n"
    "v_cvt_pk_bf16_f32 v226, v226, v227\n"
    "v_mfma_f32_16x16x32_bf16 a[112:115], v[164:167], v[68:71], a[112:115]\n"
    "ds_read_b128 v[192:195], v249 offset:22528\n"
    "v_fma_f32 v228, v228, v36, v38\n"
    "v_mfma_f32_16x16x32_bf16 a[128:131], v[172:175], v[68:71], a[128:131]\n"
    "buffer_load_dwordx2 v[236:237], v241, s[36:39], s81 offen offset:0 sc1\n"
    "v_mfma_f32_16x16x32_bf16 a[128:131], v[176:179], v[72:75], a[128:131]\n"
    "ds_read_b128 v[196:199], v249 offset:11264\n"
    "ds_read_b128 v[200:203], v249 offset:23552\n"
    "v_mfma_f32_16x16x32_bf16 a[132:135], v[176:179], v[80:83], a[132:135]\n"
    "buffer_store_dword v216, v244, s[16:19], s76 offen offset:1280 sc1\n"
    "v_mfma_f32_16x16x32_bf16 a[132:135], v[172:175], v[76:79], a[132:135]\n"
    "v_exp_f32 v19, v19\n"
    "v_mfma_f32_16x16x32_bf16 a[116:119], v[164:167], v[76:79], a[116:119]\n"
    "buffer_store_dword v218, v244, s[16:19], s76 offen offset:1408 sc1\n"
    "v_mfma_f32_16x16x32_bf16 a[116:119], v[168:171], v[80:83], a[116:119]\n"
    "v_exp_f32 v20, v20\n"
    "v_mfma_f32_16x16x32_bf16 a[100:103], v[160:163], v[80:83], a[100:103]\n"
    "buffer_store_dword v220, v244, s[16:19], s77 offen offset:0 sc1\n"
    "v_mfma_f32_16x16x32_bf16 a[100:103], v[156:159], v[76:79], a[100:103]\n"
    "v_exp_f32 v21, v21\n"
    "v_mfma_f32_16x16x32_bf16 a[104:107], v[156:159], v[84:87], a[104:107]\n"
    "buffer_store_dword v222, v244, s[16:19], s77 offen offset:128 sc1\n"
    "v_mfma_f32_16x16x32_bf16 a[104:107], v[160:163], v[88:91], a[104:107]\n"
    "v_fma_f32 v229, v229, v37, v39\n"
    "v_fma_f32 v230, v230, v36, v38\n"
    "v_mfma_f32_16x16x32_bf16 a[120:123], v[168:171], v[88:91], a[120:123]\n"
    "buffer_store_dword v224, v244, s[16:19], s77 offen offset:1024 sc1\n"
    "v_mfma_f32_16x16x32_bf16 a[120:123], v[164:167], v[84:87], a[120:123]\n"
    "v_exp_f32 v22, v22\n"
    "v_mfma_f32_16x16x32_bf16 a[136:139], v[172:175], v[84:87], a[136:139]\n"
    "buffer_store_dword v226, v244, s[16:19], s77 offen offset:1152 sc1\n"
    "v_mfma_f32_16x16x32_bf16 a[136:139], v[176:179], v[88:91], a[136:139]\n"
    "v_fma_f32 v231, v231, v37, v39\n"
    "v_cvt_pk_bf16_f32 v228, v228, v229\n"
    "v_mfma_f32_16x16x32_bf16 a[140:143], v[176:179], v[96:99], a[140:143]\n"
    "buffer_store_dword v228, v244, s[16:19], s77 offen offset:256 sc1\n"
    "v_mfma_f32_16x16x32_bf16 a[140:143], v[172:175], v[92:95], a[140:143]\n"
    "v_exp_f32 v23, v23\n"
    "v_mfma_f32_16x16x32_bf16 a[124:127], v[164:167], v[92:95], a[124:127]\n"
    "v_exp_f32 v24, v24\n"
    "v_mfma_f32_16x16x32_bf16 a[124:127], v[168:171], v[96:99], a[124:127]\n"
    "v_cvt_pk_bf16_f32 v230, v230, v231\n"
    "v_fma_f32 v232, v232, v36, v38\n"
    "v_mfma_f32_16x16x32_bf16 a[108:111], v[160:163], v[96:99], a[108:111]\n"
    "buffer_store_dword v230, v244, s[16:19], s77 offen offset:384 sc1\n"
    "v_mfma_f32_16x16x32_bf16 a[108:111], v[156:159], v[92:95], a[108:111]\n"
    "s_waitcnt lgkmcnt(0)\n"
    "v_exp_f32 v25, v25\n"
    "v_mfma_f32_16x16x32_bf16 a[144:147], v[180:183], v[68:71], a[144:147]\n"
    "ds_read_b128 v[156:159], v3 offset:12288\n"
    "v_fma_f32 v233, v233, v37, v39\n"
    "v_mfma_f32_16x16x32_bf16 a[144:147], v[184:187], v[72:75], a[144:147]\n"
    "ds_read_b128 v[160:163], v3 offset:14336\n"
    "v_fma_f32 v234, v234, v36, v38\n"
    "v_mfma_f32_16x16x32_bf16 a[160:163], v[192:195], v[72:75], a[160:163]\n"
    "ds_read_b128 v[164:167], v3 offset:16384\n"
    "v_fma_f32 v235, v235, v37, v39\n"
    "v_mfma_f32_16x16x32_bf16 a[160:163], v[188:191], v[68:71], a[160:163]\n"
    "ds_read_b128 v[168:171], v3 offset:18432\n"
    "v_cvt_pk_bf16_f32 v232, v232, v233\n"
    "v_mfma_f32_16x16x32_bf16 a[176:179], v[196:199], v[68:71], a[176:179]\n"
    "ds_read_b128 v[172:175], v3 offset:20480\n"
    "v_cvt_pk_bf16_f32 v234, v234, v235\n"
    "v_mfma_f32_16x16x32_bf16 a[176:179], v[200:203], v[72:75], a[176:179]\n"
    "ds_read_b128 v[176:179], v3 offset:22528\n"
    "v_mfma_f32_16x16x32_bf16 a[180:183], v[200:203], v[80:83], a[180:183]\n"
    "ds_read_b128 v[100:103], v250 offset:1664\n"
    "v_mfma_f32_16x16x32_bf16 a[180:183], v[196:199], v[76:79], a[180:183]\n"
    "buffer_store_dword v232, v244, s[16:19], s77 offen offset:1280 sc1\n"
    "v_mfma_f32_16x16x32_bf16 a[164:167], v[188:191], v[76:79], a[164:167]\n"
    "v_exp_f32 v26, v26\n"
    "v_mfma_f32_16x16x32_bf16 a[164:167], v[192:195], v[80:83], a[164:167]\n"
    "buffer_store_dword v234, v244, s[16:19], s77 offen offset:1408 sc1\n"
    "v_mfma_f32_16x16x32_bf16 a[148:151], v[184:187], v[80:83], a[148:151]\n"
    "v_exp_f32 v27, v27\n"
    "v_mfma_f32_16x16x32_bf16 a[148:151], v[180:183], v[76:79], a[148:151]\n"
    "v_exp_f32 v28, v28\n"
    "v_mfma_f32_16x16x32_bf16 a[152:155], v[180:183], v[84:87], a[152:155]\n"
    "v_exp_f32 v29, v29\n"
    "v_mfma_f32_16x16x32_bf16 a[152:155], v[184:187], v[88:91], a[152:155]\n"
    "v_exp_f32 v30, v30\n"
    "v_mfma_f32_16x16x32_bf16 a[168:171], v[192:195], v[88:91], a[168:171]\n"
    "s_waitcnt vmcnt(17)\n"
    "ds_add_u32 v247, v246\n"
    "v_mfma_f32_16x16x32_bf16 a[168:171], v[188:191], v[84:87], a[168:171]\n"
    "v_exp_f32 v31, v31\n"
    "v_mfma_f32_16x16x32_bf16 a[184:187], v[196:199], v[84:87], a[184:187]\n"
    "v_exp_f32 v32, v32\n"
    "v_mfma_f32_16x16x32_bf16 a[184:187], v[200:203], v[88:91], a[184:187]\n"
    "v_exp_f32 v33, v33\n"
    "v_mfma_f32_16x16x32_bf16 a[188:191], v[200:203], v[96:99], a[188:191]\n"
    "v_exp_f32 v34, v34\n"
    "v_mfma_f32_16x16x32_bf16 a[188:191], v[196:199], v[92:95], a[188:191]\n"
    "v_exp_f32 v35, v35\n"
    "v_mfma_f32_16x16x32_bf16 a[172:175], v[188:191], v[92:95], a[172:175]\n"
    "s_sub_u32 s74, s68, s65\n"
    "v_add_u32 v250, 1536, v250\n"
    "v_add_u32 v251, 1536, v251\n"
    "v_mfma_f32_16x16x32_bf16 a[172:175], v[192:195], v[96:99], a[172:175]\n"
    "s_add_u32 s66, s66, 1\n"
    "v_mov_b32 v2, v3\n"
    "v_add_u32 v3, s74, v252\n"
    "v_mfma_f32_16x16x32_bf16 a[156:159], v[184:187], v[96:99], a[156:159]\n"
    "s_waitcnt lgkmcnt(1)\n"
    "s_cmp_lt_u32 s66, 31\n"
    "v_mfma_f32_16x16x32_bf16 a[156:159], v[180:183], v[92:95], a[156:159]\n"
    "s_cbranch_scc1 .LPK\n"
    "s_branch .Ltail\n"
    ".LPV:\n"
    "v_mfma_f32_16x16x32_bf16 v[36:39], v[156:159], a[192:195], v[100:103]\n"
    "s_add_u32 s71, s71, 98304\n"
    "ds_read_b128 v[180:183], v2 offset:13312\n"
    "v_cvt_pk_bf16_f32 v68, v4, v5\n"
    "v_mfma_f32_16x16x32_bf16 v[36:39], v[160:163], a[196:199], v[36:39]\n"
    "s_add_u32 s75, s71, s63\n"
    "ds_read_b128 v[184:187], v2 offset:15360\n"
    "v_mfma_f32_16x16x32_bf16 v[36:39], v[164:167], a[200:203], v[36:39]\n"
    "ds_read_b128 v[188:191], v2 offset:17408\n"
    "v_mfma_f32_16x16x32_bf16 v[36:39], v[168:171], a[204:207], v[36:39]\n"
    "ds_read_b128 v[192:195], v2 offset:19456\n"
    "v_cvt_pk_bf16_f32 v69, v6, v7\n"
    "v_mfma_f32_16x16x32_bf16 v[36:39], v[172:175], a[208:211], v[36:39]\n"
    "ds_read_b128 v[196:199], v2 offset:21504\n"
    "v_mfma_f32_16x16x32_bf16 v[36:39], v[176:179], a[212:215], v[36:39]\n"
    "ds_read_b128 v[200:203], v2 offset:23552\n"
    "v_mfma_f32_16x16x32_bf16 v[40:43], v[176:179], a[236:239], v[100:103]\n"
    "ds_read_b128 v[104:107], v250 offset:144\n"
    "v_mfma_f32_16x16x32_bf16 v[40:43], v[172:175], a[232:235], v[40:43]\n"
    "s_add_u32 s68, s68, 24576\n"
    "v_cvt_pk_bf16_f32 v76, v8, v9\n"
    "v_mfma_f32_16x16x32_bf16 v[40:43], v[168:171], a[228:231], v[40:43]\n"
    "s_cmp_ge_u32 s68, s69\n"
    "s_cselect_b32 s68, s65, s68\n"
    "v_mfma_f32_16x16x32_bf16 v[40:43], v[164:167], a[224:227], v[40:43]\n"
    "v_exp_f32 v36, v36\n"
    "v_mfma_f32_16x16x32_bf16 v[40:43], v[160:163], a[220:223], v[40:43]\n"
    "v_exp_f32 v37, v37\n"
    "v_mfma_f32_16x16x32_bf16 v[40:43], v[156:159], a[216:219], v[40:43]\n"
    "v_cvt_pk_bf16_f32 v77, v10, v11\n"
    "v_cvt_pk_bf16_f32 v72, v36, v37\n"
    "v_mfma_f32_16x16x32_bf16 v[44:47], v[156:159], a[240:243], v[100:103]\n"
    "v_mfma_f32_16x16x32_bf16 v[44:47], v[160:163], a[244:247], v[44:47]\n"
    "v_exp_f32 v38, v38\n"
    "v_mfma_f32_16x16x32_bf16 v[44:47], v[164:167], a[248:251], v[44:47]\n"
    "v_mfma_f32_16x16x32_bf16 v[44:47], v[168:171], a[252:255], v[44:47]\n"
    "v_cvt_pk_bf16_f32 v84, v12, v13\n"
    "v_mfma_f32_16x16x32_bf16 v[44:47], v[172:175], v[124:127], v[44:47]\n"
    "v_exp_f32 v39, v39\n"
    "v_mfma_f32_16x16x32_bf16 v[44:47], v[176:179], v[128:131], v[44:47]\n"
    "v_cvt_pk_bf16_f32 v73, v38, v39\n"
    "v_mfma_f32_16x16x32_bf16 v[48:51], v[176:179], v[152:155], v[100:103]\n"
    "ds_read_b32 v245, v247\n"
    "v_cvt_pk_bf16_f32 v85, v14, v15\n"
    "v_mfma_f32_16x16x32_bf16 v[48:51], v[172:175], v[148:151], v[48:51]\n"
    "s_add_u32 m0, s68, 0\n"
    "v_exp_f32 v40, v40\n"
    "v_mfma_f32_16x16x32_bf16 v[48:51], v[168:171], v[144:147], v[48:51]\n"
    "v_mfma_f32_16x16x32_bf16 v[48:51], v[164:167], v[140:143], v[48:51]\n"
    "v_mfma_f32_16x16x32_bf16 v[48:51], v[160:163], v[136:139], v[48:51]\n"
    "s_waitcnt vmcnt(12)\n"
    "v_cmp_eq_u64 s[54:55], v[236:237], v[238:239]\n"
    "v_cvt_pk_bf16_f32 v92, v16, v17\n"
    "v_mfma_f32_16x16x32_bf16 v[48:51], v[156:159], v[132:135], v[48:51]\n"
    "s_waitcnt lgkmcnt(1)\n"
    "v_exp_f32 v41, v41\n"
    "v_mfma_f32_16x16x32_bf16 v[52:55], v[180:183], a[192:195], v[104:107]\n"
    "s_waitcnt lgkmcnt(0)\n"
    "v_cmp_le_u32 s[56:57], s59, v245\n"
    "v_cvt_pk_bf16_f32 v80, v40, v41\n"
    "v_mfma_f32_16x16x32_bf16 v[52:55], v[184:187], a[196:199], v[52:55]\n"
    "v_exp_f32 v42, v42\n"
    "v_mfma_f32_16x16x32_bf16 v[52:55], v[188:191], a[200:203], v[52:55]\n"
    "s_and_b64 s[54:55], s[54:55], s[56:57]\n"
    "s_cmp_eq_u64 s[54:55], -1\n"
    "v_mfma_f32_16x16x32_bf16 v[52:55], v[192:195], a[204:207], v[52:55]\n"
    "s_cbranch_scc0 .Lpoll3\n"
    ".Lpoll3r:\n"
    "v_mfma_f32_16x16x32_bf16 v[52:55], v[196:199], a[208:211], v[52:55]\n"
    "buffer_load_dwordx4 v1, s[16:19], s67 offen offset:0 sc1 lds\n"
    "v_mfma_f32_16x16x32_bf16 v[52:55], v[200:203], a[212:215], v[52:55]\n"
    "ds_read_b128 v[156:159], v3 offset:0\n"
    "ds_read_b128 v[160:163], v3 offset:2048\n"
    "v_mfma_f32_16x16x32_bf16 v[56:59], v[200:203], a[236:239], v[104:107]\n"
    "ds_read_b128 v[164:167], v3 offset:4096\n"
    "ds_read_b128 v[168:171], v3 offset:6144\n"
    "v_mfma_f32_16x16x32_bf16 v[56:59], v[196:199], a[232:235], v[56:59]\n"
    "ds_read_b128 v[172:175], v3 offset:8192\n"
    "ds_read_b128 v[176:179], v3 offset:10240\n"
    "v_mfma_f32_16x16x32_bf16 v[56:59], v[192:195], a[228:231], v[56:59]\n"
    "buffer_load_dwordx4 v1, s[16:19], s67 offen offset:1024 sc1 lds\n"
    "v_mfma_f32_16x16x32_bf16 v[56:59], v[188:191], a[224:227], v[56:59]\n"
    "ds_read_b128 v[100:103], v250 offset:768\n"
    "v_cvt_pk_bf16_f32 v93, v18, v19\n"
    "v_mfma_f32_16x16x32_bf16 v[56:59], v[184:187], a[220:223], v[56:59]\n"
    "v_cvt_pk_bf16_f32 v70, v20, v21\n"
    "v_cvt_pk_bf16_f32 v71, v22, v23\n"
    "v_mfma_f32_16x16x32_bf16 v[56:59], v[180:183], a[216:219], v[56:59]\n"
    "v_exp_f32 v43, v43\n"
    "v_mfma_f32_16x16x32_bf16 v[60:63], v[180:183], a[240:243], v[104:107]\n"
    "buffer_load_dwordx4 v1, s[16:19], s67 offen offset:2048 sc1 lds\n"
    "v_mfma_f32_16x16x32_bf16 v[60:63], v[184:187], a[244:247], v[60:63]\n"
    "v_cvt_pk_bf16_f32 v78, v24, v25\n"
    "v_cvt_pk_bf16_f32 v81, v42, v43\n"
    "v_mfma_f32_16x16x32_bf16 v[60:63], v[188:191], a[248:251], v[60:63]\n"
    "v_exp_f32 v44, v44\n"
    "v_mfma_f32_16x16x32_bf16 v[60:63], v[192:195], a[252:255], v[60:63]\n"
    "v_exp_f32 v45, v45\n"
    "v_mfma_f32_16x16x32_bf16 v[60:63], v[196:199], v[124:127], v[60:63]\n"
    "buffer_load_dwordx4 v1, s[16:19], s67 offen offset:3072 sc1 lds\n"
    "v_mfma_f32_16x16x32_bf16 v[60:63], v[200:203], v[128:131], v[60:63]\n"
    "s_add_u32 m0, s68, 4096\n"
    "v_cvt_pk_bf16_f32 v79, v26, v27\n"
    "v_cvt_pk_bf16_f32 v88, v44, v45\n"
    "v_mfma_f32_16x16x32_bf16 v[64:67], v[200:203], v[152:155], v[104:107]\n"
    "s_add_u32 s83, s67, 4096\n"
    "v_exp_f32 v46, v46\n"
    "v_mfma_f32_16x16x32_bf16 v[64:67], v[196:199], v[148:151], v[64:67]\n"
    "v_exp_f32 v47, v47\n"
    "v_mfma_f32_16x16x32_bf16 v[64:67], v[192:195], v[144:147], v[64:67]\n"
    "buffer_load_dwordx4 v1, s[16:19], s83 offen offset:0 sc1 lds\n"
    "v_mfma_f32_16x16x32_bf16 v[64:67], v[188:191], v[140:143], v[64:67]\n"
    "v_cvt_pk_bf16_f32 v86, v28, v29\n"
    "v_cvt_pk_bf16_f32 v89, v46, v47\n"
    "v_mfma_f32_16x16x32_bf16 v[64:67], v[184:187], v[136:139], v[64:67]\n"
    "v_exp_f32 v48, v48\n"
    "v_mfma_f32_16x16x32_bf16 v[64:67], v[180:183], v[132:135], v[64:67]\n"
    "s_waitcnt lgkmcnt(0)\n"
    "v_exp_f32 v49, v49\n"
    "v_mfma_f32_16x16x32_bf16 v[4:7], v[156:159], a[192:195], v[100:103]\n"
    "buffer_load_dwordx4 v1, s[16:19], s83 offen offset:1024 sc1 lds\n"
    "v_mfma_f32_16x16x32_bf16 v[4:7], v[160:163], a[196:199], v[4:7]\n"
    "s_add_u32 m0, s64, 0\n"
    "ds_read_b128 v[180:183], v3 offset:1024\n"
    "ds_read_b128 v[184:187], v3 offset:3072\n"
    "v_mfma_f32_16x16x32_bf16 v[4:7], v[164:167], a[200:203], v[4:7]\n"
    "s_add_u32 s83, s67, 24576\n"
    "ds_read_b128 v[188:191], v3 offset:5120\n"
    "v_cvt_pk_bf16_f32 v87, v30, v31\n"
    "v_mfma_f32_16x16x32_bf16 v[4:7], v[168:171], a[204:207], v[4:7]\n"
    "ds_read_b128 v[192:195], v3 offset:7168\n"
    "v_cvt_pk_bf16_f32 v96, v48, v49\n"
    "v_mfma_f32_16x16x32_bf16 v[4:7], v[172:175], a[208:211], v[4:7]\n"
    "buffer_load_dwordx4 v1, s[16:19], s83 offen offset:0 sc1 lds\n"
    "v_mfma_f32_16x16x32_bf16 v[4:7], v[176:179], a[212:215], v[4:7]\n"
    "ds_read_b128 v[196:199], v3 offset:9216\n"
    "ds_read_b128 v[200:203], v3 offset:11264\n"
    "v_mfma_f32_16x16x32_bf16 v[8:11], v[176:179], a[236:239], v[100:103]\n"
    "ds_read_b128 v[104:107], v250 offset:784\n"
    "v_cvt_pk_bf16_f32 v94, v32, v33\n"
    "v_mfma_f32_16x16x32_bf16 v[8:11], v[172:175], a[232:235], v[8:11]\n"
    "v_exp_f32 v52, v52\n"
    "v_mfma_f32_16x16x32_bf16 v[8:11], v[168:171], a[228:231], v[8:11]\n"
    "buffer_load_dwordx4 v1, s[16:19], s83 offen offset:1024 sc1 lds\n"
    "v_mfma_f32_16x16x32_bf16 v[8:11], v[164:167], a[224:227], v[8:11]\n"
    "v_cvt_pk_bf16_f32 v95, v34, v35\n"
    "v_mfma_f32_16x16x32_bf16 v[8:11], v[160:163], a[220:223], v[8:11]\n"
    "v_exp_f32 v53, v53\n"
    "v_mfma_f32_16x16x32_bf16 v[8:11], v[156:159], a[216:219], v[8:11]\n"
    "v_exp_f32 v54, v54\n"
    "v_mfma_f32_16x16x32_bf16 v[12:15], v[156:159], a[240:243], v[100:103]\n"
    "buffer_load_dwordx4 v1, s[16:19], s83 offen offset:2048 sc1 lds\n"
    "v_mfma_f32_16x16x32_bf16 v[12:15], v[160:163], a[244:247], v[12:15]\n"
    "v_cvt_pk_bf16_f32 v74, v52, v53\n"
    "v_mfma_f32_16x16x32_bf16 v[12:15], v[164:167], a[248:251], v[12:15]\n"
    "v_exp_f32 v55, v55\n"
    "v_mfma_f32_16x16x32_bf16 v[12:15], v[168:171], a[252:255], v[12:15]\n"
    "v_cvt_pk_bf16_f32 v75, v54, v55\n"
    "v_mfma_f32_16x16x32_bf16 v[12:15], v[172:175], v[124:127], v[12:15]\n"
    "buffer_load_dwordx4 v1, s[16:19], s83 offen offset:3072 sc1 lds\n"
    "v_mfma_f32_16x16x32_bf16 v[12:15], v[176:179], v[128:131], v[12:15]\n"
    "s_add_u32 m0, s64, 4096\n"
    "v_exp_f32 v56, v56\n"
    "v_mfma_f32_16x16x32_bf16 v[16:19], v[176:179], v[152:155], v[100:103]\n"
    "s_add_u32 s83, s67, 28672\n"
    "v_exp_f32 v50, v50\n"
    "v_mfma_f32_16x16x32_bf16 v[16:19], v[172:175], v[148:151], v[16:19]\n"
    "v_exp_f32 v51, v51\n"
    "v_mfma_f32_16x16x32_bf16 v[16:19], v[168:171], v[144:147], v[16:19]\n"
    "buffer_load_dwordx4 v1, s[16:19], s83 offen offset:0 sc1 lds\n"
    "v_mfma_f32_16x16x32_bf16 v[16:19], v[164:167], v[140:143], v[16:19]\n"
    "v_exp_f32 v57, v57\n"
    "v_mfma_f32_16x16x32_bf16 v[16:19], v[160:163], v[136:139], v[16:19]\n"
    "v_cvt_pk_bf16_f32 v82, v56, v57\n"
    "v_cvt_pk_bf16_f32 v97, v50, v51\n"
    "v_mfma_f32_16x16x32_bf16 v[16:19], v[156:159], v[132:135], v[16:19]\n"
    "s_waitcnt lgkmcnt(0)\n"
    "v_exp_f32 v58, v58\n"
    "v_mfma_f32_16x16x32_bf16 v[20:23], v[180:183], a[192:195], v[104:107]\n"
    "buffer_load_dwordx4 v1, s[16:19], s83 offen offset:1024 sc1 lds\n"
    "v_mfma_f32_16x16x32_bf16 v[20:23], v[184:187], a[196:199], v[20:23]\n"
    "s_add_u32 s67, s67, 49152\n"
    "ds_read_b128 v[156:159], v248 offset:0\n"
    "ds_read_b128 v[160:163], v248 offset:12288\n"
    "v_mfma_f32_16x16x32_bf16 v[20:23], v[188:191], a[200:203], v[20:23]\n"
    "s_add_u32 s59, s59, 4\n"
    "ds_read_b128 v[164:167], v248 offset:1024\n"
    "v_mfma_f32_16x16x32_bf16 v[20:23], v[192:195], a[204:207], v[20:23]\n"
    "s_add_u32 s81, s81, 128\n"
    "s_waitcnt vmcnt(12)\n"
    "ds_read_b128 v[168:171], v248 offset:13312\n"
    "v_mfma_f32_16x16x32_bf16 v[20:23], v[196:199], a[208:211], v[20:23]\n"
    "s_min_u32 s81, s81, 4096\n"
    "ds_read_b128 v[172:175], v248 offset:2048\n"
    "v_mfma_f32_16x16x32_bf16 v[20:23], v[200:203], a[212:215], v[20:23]\n"
    "buffer_store_dwordx2 v[238:239], v240, s[36:39], s80 offen offset:0 sc1\n"
    "v_mfma_f32_16x16x32_bf16 v[24:27], v[200:203], a[236:239], v[104:107]\n"
    "s_add_u32 s80, s80, 256\n"
    "ds_read_b128 v[176:179], v248 offset:14336\n"
    "v_mfma_f32_16x16x32_bf16 v[24:27], v[196:199], a[232:235], v[24:27]\n"
    "v_exp_f32 v59, v59\n"
    "v_mfma_f32_16x16x32_bf16 v[24:27], v[192:195], a[228:231], v[24:27]\n"
    "buffer_load_dwordx4 v[204:207], v242, s[40:43], s75 offen offset:0 nt\n"
    "v_mfma_f32_16x16x32_bf16 v[24:27], v[188:191], a[224:227], v[24:27]\n"
    "v_cvt_pk_bf16_f32 v83, v58, v59\n"
    "v_mfma_f32_16x16x32_bf16 v[24:27], v[184:187], a[220:223], v[24:27]\n"
    "buffer_load_dwordx4 v[208:211], v242, s[40:43], s75 offen offset:768 nt\n"
    "v_mfma_f32_16x16x32_bf16 v[24:27], v[180:183], a[216:219], v[24:27]\n"
    "v_exp_f32 v60, v60\n"
    "v_mfma_f32_16x16x32_bf16 v[28:31], v[180:183], a[240:243], v[104:107]\n"
    "buffer_load_dwordx4 v[212:215], v242, s[40:43], s75 offen offset:256 nt\n"
    "v_mfma_f32_16x16x32_bf16 v[28:31], v[184:187], a[244:247], v[28:31]\n"
    "v_exp_f32 v61, v61\n"
    "v_mfma_f32_16x16x32_bf16 v[28:31], v[188:191], a[248:251], v[28:31]\n"
    "buffer_load_dwordx4 v[216:219], v242, s[40:43], s75 offen offset:1024 nt\n"
    "v_mfma_f32_16x16x32_bf16 v[28:31], v[192:195], a[252:255], v[28:31]\n"
    "v_exp_f32 v62, v62\n"
    "v_mfma_f32_16x16x32_bf16 v[28:31], v[196:199], v[124:127], v[28:31]\n"
    "buffer_load_dwordx4 v[220:223], v242, s[40:43], s75 offen offset:512 nt\n"
    "v_mfma_f32_16x16x32_bf16 v[28:31], v[200:203], v[128:131], v[28:31]\n"
    "v_exp_f32 v63, v63\n"
    "v_mfma_f32_16x16x32_bf16 v[32:35], v[200:203], v[152:155], v[104:107]\n"
    "v_cvt_pk_bf16_f32 v90, v60, v61\n"
    "v_cvt_pk_bf16_f32 v91, v62, v63\n"
    "v_mfma_f32_16x16x32_bf16 v[32:35], v[196:199], v[148:151], v[32:35]\n"
    "buffer_load_dwordx4 v[224:227], v242, s[40:43], s75 offen offset:1280 nt\n"
    "v_mfma_f32_16x16x32_bf16 v[32:35], v[192:195], v[144:147], v[32:35]\n"
    "v_exp_f32 v64, v64\n"
    "v_mfma_f32_16x16x32_bf16 v[32:35], v[188:191], v[140:143], v[32:35]\n"
    "v_exp_f32 v65, v65\n"
    "v_mfma_f32_16x16x32_bf16 v[32:35], v[184:187], v[136:139], v[32:35]\n"
    "v_exp_f32 v66, v66\n"
    "v_mfma_f32_16x16x32_bf16 v[32:35], v[180:183], v[132:135], v[32:35]\n"
    "s_waitcnt lgkmcnt(0)\n"
    "v_cvt_pk_bf16_f32 v98, v64, v65\n"
    "v_mfma_f32_16x16x32_bf16 a[0:3], v[156:159], v[68:71], a[0:3]\n"
    "ds_read_b128 v[180:183], v248 offset:3072\n"
    "v_mfma_f32_16x16x32_bf16 a[0:3], v[160:163], v[72:75], a[0:3]\n"
    "ds_read_b128 v[184:187], v248 offset:15360\n"
    "v_mfma_f32_16x16x32_bf16 a[16:19], v[168:171], v[72:75], a[16:19]\n"
    "ds_read_b128 v[188:191], v248 offset:4096\n"
    "v_mfma_f32_16x16x32_bf16 a[16:19], v[164:167], v[68:71], a[16:19]\n"
    "ds_read_b128 v[192:195], v248 offset:16384\n"
    "v_mfma_f32_16x16x32_bf16 a[32:35], v[172:175], v[68:71], a[32:35]\n"
    "ds_read_b128 v[196:199], v248 offset:5120\n"
    "v_mfma_f32_16x16x32_bf16 a[32:35], v[176:179], v[72:75], a[32:35]\n"
    "ds_read_b128 v[200:203], v248 offset:17408\n"
    "v_mfma_f32_16x16x32_bf16 a[36:39], v[176:179], v[80:83], a[36:39]\n"
    "ds_read_b128 v[104:107], v251 offset:0\n"
    "v_mfma_f32_16x16x32_bf16 a[36:39], v[172:175], v[76:79], a[36:39]\n"
    "v_exp_f32 v67, v67\n"
    "v_mfma_f32_16x16x32_bf16 a[20:23], v[164:167], v[76:79], a[20:23]\n"
    "v_cvt_pk_bf16_f32 v99, v66, v67\n"
    "v_mfma_f32_16x16x32_bf16 a[20:23], v[168:171], v[80:83], a[20:23]\n"
    "v_exp_f32 v4, v4\n"
    "v_mfma_f32_16x16x32_bf16 a[4:7], v[160:163], v[80:83], a[4:7]\n"
    "v_exp_f32 v5, v5\n"
    "v_mfma_f32_16x16x32_bf16 a[4:7], v[156:159], v[76:79], a[4:7]\n"
    "v_exp_f32 v6, v6\n"
    "v_mfma_f32_16x16x32_bf16 a[8:11], v[156:159], v[84:87], a[8:11]\n"
    "v_exp_f32 v7, v7\n"
    "v_mfma_f32_16x16x32_bf16 a[8:11], v[160:163], v[88:91], a[8:11]\n"
    "v_mfma_f32_16x16x32_bf16 a[24:27], v[168:171], v[88:91], a[24:27]\n"
    "v_exp_f32 v8, v8\n"
    "v_mfma_f32_16x16x32_bf16 a[24:27], v[164:167], v[84:87], a[24:27]\n"
    "v_mfma_f32_16x16x32_bf16 a[40:43], v[172:175], v[84:87], a[40:43]\n"
    "v_mfma_f32_16x16x32_bf16 a[40:43], v[176:179], v[88:91], a[40:43]\n"
    "v_exp_f32 v9, v9\n"
    "v_mfma_f32_16x16x32_bf16 a[44:47], v[176:179], v[96:99], a[44:47]\n"
    "v_mfma_f32_16x16x32_bf16 a[44:47], v[172:175], v[92:95], a[44:47]\n"
    "v_mfma_f32_16x16x32_bf16 a[28:31], v[164:167], v[92:95], a[28:31]\n"
    "v_exp_f32 v10, v10\n"
    "v_mfma_f32_16x16x32_bf16 a[28:31], v[168:171], v[96:99], a[28:31]\n"
    "v_mfma_f32_16x16x32_bf16 a[12:15], v[160:163], v[96:99], a[12:15]\n"
    "v_exp_f32 v11, v11\n"
    "v_mfma_f32_16x16x32_bf16 a[12:15], v[156:159], v[92:95], a[12:15]\n"
    "s_waitcnt lgkmcnt(0)\n"
    "v_mfma_f32_16x16x32_bf16 a[48:51], v[180:183], v[68:71], a[48:51]\n"
    "ds_read_b128 v[156:159], v248 offset:6144\n"
    "v_mfma_f32_16x16x32_bf16 a[48:51], v[184:187], v[72:75], a[48:51]\n"
    "ds_read_b128 v[160:163], v248 offset:18432\n"
    "v_mfma_f32_16x16x32_bf16 a[64:67], v[192:195], v[72:75], a[64:67]\n"
    "ds_read_b128 v[164:167], v248 offset:7168\n"
    "v_mfma_f32_16x16x32_bf16 a[64:67], v[188:191], v[68:71], a[64:67]\n"
    "ds_read_b128 v[168:171], v248 offset:19456\n"
    "v_mfma_f32_16x16x32_bf16 a[80:83], v[196:199], v[68:71], a[80:83]\n"
    "ds_read_b128 v[172:175], v248 offset:8192\n"
    "v_mfma_f32_16x16x32_bf16 a[80:83], v[200:203], v[72:75], a[80:83]\n"
    "ds_read_b128 v[176:179], v248 offset:20480\n"
    "v_smfmac_f32_16x16x64_bf16 v[108:111], v[104:107], v[68:75], v253\n"
    "v_smfmac_f32_16x16x64_bf16 v[112:115], v[104:107], v[76:83], v253\n"
    "v_mfma_f32_16x16x32_bf16 a[84:87], v[200:203], v[80:83], a[84:87]\n"
    "v_exp_f32 v12, v12\n"
    "v_mfma_f32_16x16x32_bf16 a[84:87], v[196:199], v[76:79], a[84:87]\n"
    "v_exp_f32 v13, v13\n"
    "v_mfma_f32_16x16x32_bf16 a[68:71], v[188:191], v[76:79], a[68:71]\n"
    "v_exp_f32 v14, v14\n"
    "v_mfma_f32_16x16x32_bf16 a[68:71], v[192:195], v[80:83], a[68:71]\n"
    "v_exp_f32 v15, v15\n"
    "v_mfma_f32_16x16x32_bf16 a[52:55], v[184:187], v[80:83], a[52:55]\n"
    "v_exp_f32 v16, v16\n"
    "v_mfma_f32_16x16x32_bf16 a[52:55], v[180:183], v[76:79], a[52:55]\n"
    "v_mfma_f32_16x16x32_bf16 a[56:59], v[180:183], v[84:87], a[56:59]\n"
    "v_exp_f32 v17, v17\n"
    "v_mfma_f32_16x16x32_bf16 a[56:59], v[184:187], v[88:91], a[56:59]\n"
    "v_mfma_f32_16x16x32_bf16 a[72:75], v[192:195], v[88:91], a[72:75]\n"
    "v_mfma_f32_16x16x32_bf16 a[72:75], v[188:191], v[84:87], a[72:75]\n"
    "v_exp_f32 v18, v18\n"
    "v_mfma_f32_16x16x32_bf16 a[88:91], v[196:199], v[84:87], a[88:91]\n"
    "v_mfma_f32_16x16x32_bf16 a[88:91], v[200:203], v[88:91], a[88:91]\n"
    "v_smfmac_f32_16x16x64_bf16 v[116:119], v[104:107], v[84:91], v253\n"
    "v_smfmac_f32_16x16x64_bf16 v[120:123], v[104:107], v[92:99], v253\n"
    "v_mfma_f32_16x16x32_bf16 a[92:95], v[200:203], v[96:99], a[92:95]\n"
    "v_exp_f32 v19, v19\n"
    "v_mfma_f32_16x16x32_bf16 a[92:95], v[196:199], v[92:95], a[92:95]\n"
    "v_exp_f32 v20, v20\n"
    "v_mfma_f32_16x16x32_bf16 a[76:79], v[188:191], v[92:95], a[76:79]\n"
    "v_mfma_f32_16x16x32_bf16 a[76:79], v[192:195], v[96:99], a[76:79]\n"
    "v_exp_f32 v21, v21\n"
    "v_mfma_f32_16x16x32_bf16 a[60:63], v[184:187], v[96:99], a[60:63]\n"
    "v_mfma_f32_16x16x32_bf16 a[60:63], v[180:183], v[92:95], a[60:63]\n"
    "s_waitcnt lgkmcnt(0)\n"
    "v_mfma_f32_16x16x32_bf16 a[96:99], v[156:159], v[68:71], a[96:99]\n"
    "ds_read_b128 v[180:183], v248 offset:9216\n"
    "v_mfma_f32_16x16x32_bf16 a[96:99], v[160:163], v[72:75], a[96:99]\n"
    "ds_read_b128 v[184:187], v248 offset:21504\n"
    "v_mfma_f32_16x16x32_bf16 a[112:115], v[168:171], v[72:75], a[112:115]\n"
    "ds_read_b128 v[188:191], v248 offset:10240\n"
    "v_mfma_f32_16x16x32_bf16 a[112:115], v[164:167], v[68:71], a[112:115]\n"
    "ds_read_b128 v[192:195], v248 offset:22528\n"
    "v_mfma_f32_16x16x32_bf16 a[128:131], v[172:175], v[68:71], a[128:131]\n"
    "buffer_load_dwordx2 v[236:237], v241, s[36:39], s81 offen offset:0 sc1\n"
    "v_mfma_f32_16x16x32_bf16 a[128:131], v[176:179], v[72:75], a[128:131]\n"
    "ds_read_b128 v[196:199], v248 offset:11264\n"
    "ds_read_b128 v[200:203], v248 offset:23552\n"
    "v_mfma_f32_16x16x32_bf16 a[132:135], v[176:179], v[80:83], a[132:135]\n"
    "v_exp_f32 v22, v22\n"
    "v_mfma_f32_16x16x32_bf16 a[132:135], v[172:175], v[76:79], a[132:135]\n"
    "v_exp_f32 v23, v23\n"
    "v_mfma_f32_16x16x32_bf16 a[116:119], v[164:167], v[76:79], a[116:119]\n"
    "v_exp_f32 v24, v24\n"
    "v_mfma_f32_16x16x32_bf16 a[116:119], v[168:171], v[80:83], a[116:119]\n"
    "v_exp_f32 v25, v25\n"
    "v_mfma_f32_16x16x32_bf16 a[100:103], v[160:163], v[80:83], a[100:103]\n"
    "v_mfma_f32_16x16x32_bf16 a[100:103], v[156:159], v[76:79], a[100:103]\n"
    "v_exp_f32 v26, v26\n"
    "v_mfma_f32_16x16x32_bf16 a[104:107], v[156:159], v[84:87], a[104:107]\n"
    "v_mfma_f32_16x16x32_bf16 a[104:107], v[160:163], v[88:91], a[104:107]\n"
    "v_exp_f32 v27, v27\n"
    "v_mfma_f32_16x16x32_bf16 a[120:123], v[168:171], v[88:91], a[120:123]\n"
    "v_mfma_f32_16x16x32_bf16 a[120:123], v[164:167], v[84:87], a[120:123]\n"
    "v_mfma_f32_16x16x32_bf16 a[136:139], v[172:175], v[84:87], a[136:139]\n"
    "v_exp_f32 v28, v28\n"
    "v_mfma_f32_16x16x32_bf16 a[136:139], v[176:179], v[88:91], a[136:139]\n"
    "v_mfma_f32_16x16x32_bf16 a[140:143], v[176:179], v[96:99], a[140:143]\n"
    "v_mfma_f32_16x16x32_bf16 a[140:143], v[172:175], v[92:95], a[140:143]\n"
    "v_exp_f32 v29, v29\n"
    "v_mfma_f32_16x16x32_bf16 a[124:127], v[164:167], v[92:95], a[124:127]\n"
    "v_mfma_f32_16x16x32_bf16 a[124:127], v[168:171], v[96:99], a[124:127]\n"
    "v_exp_f32 v30, v30\n"
    "v_mfma_f32_16x16x32_bf16 a[108:111], v[160:163], v[96:99], a[108:111]\n"
    "v_mfma_f32_16x16x32_bf16 a[108:111], v[156:159], v[92:95], a[108:111]\n"
    "s_waitcnt lgkmcnt(0)\n"
    "v_mfma_f32_16x16x32_bf16 a[144:147], v[180:183], v[68:71], a[144:147]\n"
    "ds_read_b128 v[156:159], v3 offset:12288\n"
    "v_mfma_f32_16x16x32_bf16 a[144:147], v[184:187], v[72:75], a[144:147]\n"
    "ds_read_b128 v[160:163], v3 offset:14336\n"
    "v_mfma_f32_16x16x32_bf16 a[160:163], v[192:195], v[72:75], a[160:163]\n"
    "ds_read_b128 v[164:167], v3 offset:16384\n"
    "v_mfma_f32_16x16x32_bf16 a[160:163], v[188:191], v[68:71], a[160:163]\n"
    "ds_read_b128 v[168:171], v3 offset:18432\n"
    "v_mfma_f32_16x16x32_bf16 a[176:179], v[196:199], v[68:71], a[176:179]\n"
    "ds_read_b128 v[172:175], v3 offset:20480\n"
    "v_mfma_f32_16x16x32_bf16 a[176:179], v[200:203], v[72:75], a[176:179]\n"
    "ds_read_b128 v[176:179], v3 offset:22528\n"
    "v_mfma_f32_16x16x32_bf16 a[180:183], v[200:203], v[80:83], a[180:183]\n"
    "ds_read_b128 v[100:103], v250 offset:896\n"
    "v_mfma_f32_16x16x32_bf16 a[180:183], v[196:199], v[76:79], a[180:183]\n"
    "v_exp_f32 v31, v31\n"
    "v_mfma_f32_16x16x32_bf16 a[164:167], v[188:191], v[76:79], a[164:167]\n"
    "v_exp_f32 v32, v32\n"
    "v_mfma_f32_16x16x32_bf16 a[164:167], v[192:195], v[80:83], a[164:167]\n"
    "v_exp_f32 v33, v33\n"
    "v_mfma_f32_16x16x32_bf16 a[148:151], v[184:187], v[80:83], a[148:151]\n"
    "v_exp_f32 v34, v34\n"
    "v_mfma_f32_16x16x32_bf16 a[148:151], v[180:183], v[76:79], a[148:151]\n"
    "v_exp_f32 v35, v35\n"
    "v_mfma_f32_16x16x32_bf16 a[152:155], v[180:183], v[84:87], a[152:155]\n"
    "v_mfma_f32_16x16x32_bf16 a[152:155], v[184:187], v[88:91], a[152:155]\n"
    "v_mfma_f32_16x16x32_bf16 a[168:171], v[192:195], v[88:91], a[168:171]\n"
    "s_waitcnt vmcnt(8)\n"
    "ds_add_u32 v247, v246\n"
    "v_mfma_f32_16x16x32_bf16 a[168:171], v[188:191], v[84:87], a[168:171]\n"
    "v_mfma_f32_16x16x32_bf16 a[184:187], v[196:199], v[84:87], a[184:187]\n"
    "v_mfma_f32_16x16x32_bf16 a[184:187], v[200:203], v[88:91], a[184:187]\n"
    "v_mfma_f32_16x16x32_bf16 a[188:191], v[200:203], v[96:99], a[188:191]\n"
    "v_mfma_f32_16x16x32_bf16 a[188:191], v[196:199], v[92:95], a[188:191]\n"
    "v_mfma_f32_16x16x32_bf16 a[172:175], v[188:191], v[92:95], a[172:175]\n"
    "s_sub_u32 s74, s68, s65\n"
    "v_mfma_f32_16x16x32_bf16 a[172:175], v[192:195], v[96:99], a[172:175]\n"
    "s_add_u32 s66, s66, 1\n"
    "v_mov_b32 v2, v3\n"
    "v_add_u32 v3, s74, v252\n"
    "v_mfma_f32_16x16x32_bf16 a[156:159], v[184:187], v[96:99], a[156:159]\n"
    "s_waitcnt lgkmcnt(1)\n"
    "s_cmp_eq_u32 s66, 31\n"
    "v_mfma_f32_16x16x32_bf16 a[156:159], v[180:183], v[92:95], a[156:159]\n"
    "s_cbranch_scc1 .LtailP\n"
    ".LNV:\n"
    "v_mfma_f32_16x16x32_bf16 v[36:39], v[156:159], a[192:195], v[100:103]\n"
    "ds_read_b128 v[180:183], v2 offset:13312\n"
    "v_cvt_pk_bf16_f32 v68, v4, v5\n"
    "v_mfma_f32_16x16x32_bf16 v[36:39], v[160:163], a[196:199], v[36:39]\n"
    "ds_read_b128 v[184:187], v2 offset:15360\n"
    "v_mfma_f32_16x16x32_bf16 v[36:39], v[164:167], a[200:203], v[36:39]\n"
    "ds_read_b128 v[188:191], v2 offset:17408\n"
    "v_mfma_f32_16x16x32_bf16 v[36:39], v[168:171], a[204:207], v[36:39]\n"
    "ds_read_b128 v[192:195], v2 offset:19456\n"
    "v_cvt_pk_bf16_f32 v69, v6, v7\n"
    "v_mfma_f32_16x16x32_bf16 v[36:39], v[172:175], a[208:211], v[36:39]\n"
    "ds_read_b128 v[196:199], v2 offset:21504\n"
    "v_mfma_f32_16x16x32_bf16 v[36:39], v[176:179], a[212:215], v[36:39]\n"
    "ds_read_b128 v[200:203], v2 offset:23552\n"
    "v_mfma_f32_16x16x32_bf16 v[40:43], v[176:179], a[236:239], v[100:103]\n"
    "ds_read_b128 v[104:107], v250 offset:912\n"
    "v_mfma_f32_16x16x32_bf16 v[40:43], v[172:175], a[232:235], v[40:43]\n"
    "s_add_u32 s68, s68, 24576\n"
    "v_cvt_pk_bf16_f32 v76, v8, v9\n"
    "v_mfma_f32_16x16x32_bf16 v[40:43], v[168:171], a[228:231], v[40:43]\n"
    "s_cmp_ge_u32 s68, s69\n"
    "s_cselect_b32 s68, s65, s68\n"
    "v_mfma_f32_16x16x32_bf16 v[40:43], v[164:167], a[224:227], v[40:43]\n"
    "v_exp_f32 v36, v36\n"
    "v_mfma_f32_16x16x32_bf16 v[40:43], v[160:163], a[220:223], v[40:43]\n"
    "v_exp_f32 v37, v37\n"
    "v_mfma_f32_16x16x32_bf16 v[40:43], v[156:159], a[216:219], v[40:43]\n"
    "v_cvt_pk_bf16_f32 v77, v10, v11\n"
    "v_cvt_pk_bf16_f32 v72, v36, v37\n"
    "v_mfma_f32_16x16x32_bf16 v[44:47], v[156:159], a[240:243], v[100:103]\n"
    "v_mfma_f32_16x16x32_bf16 v[44:47], v[160:163], a[244:247], v[44:47]\n"
    "v_exp_f32 v38, v38\n"
    "v_mfma_f32_16x16x32_bf16 v[44:47], v[164:167], a[248:251], v[44:47]\n"
    "v_mfma_f32_16x16x32_bf16 v[44:47], v[168:171], a[252:255], v[44:47]\n"
    "v_cvt_pk_bf16_f32 v84, v12, v13\n"
    "v_mfma_f32_16x16x32_bf16 v[44:47], v[172:175], v[124:127], v[44:47]\n"
    "v_exp_f32 v39, v39\n"
    "v_mfma_f32_16x16x32_bf16 v[44:47], v[176:179], v[128:131], v[44:47]\n"
    "v_cvt_pk_bf16_f32 v73, v38, v39\n"
    "v_mfma_f32_16x16x32_bf16 v[48:51], v[176:179], v[152:155], v[100:103]\n"
    "ds_read_b32 v245, v247\n"
    "v_cvt_pk_bf16_f32 v85, v14, v15\n"
    "v_mfma_f32_16x16x32_bf16 v[48:51], v[172:175], v[148:151], v[48:51]\n"
    "s_add_u32 m0, s68, 0\n"
    "v_exp_f32 v40, v40\n"
    "v_mfma_f32_16x16x32_bf16 v[48:51], v[168:171], v[144:147], v[48:51]\n"
    "v_mfma_f32_16x16x32_bf16 v[48:51], v[164:167], v[140:143], v[48:51]\n"
    "v_mfma_f32_16x16x32_bf16 v[48:51], v[160:163], v[136:139], v[48:51]\n"
    "s_waitcnt vmcnt(0)\n"
    "v_cmp_eq_u64 s[54:55], v[236:237], v[238:239]\n"
    "v_cvt_pk_bf16_f32 v92, v16, v17\n"
    "v_mfma_f32_16x16x32_bf16 v[48:51], v[156:159], v[132:135], v[48:51]\n"
    "s_waitcnt lgkmcnt(1)\n"
    "v_exp_f32 v41, v41\n"
    "v_mfma_f32_16x16x32_bf16 v[52:55], v[180:183], a[192:195], v[104:107]\n"
    "s_waitcnt lgkmcnt(0)\n"
    "s_waitcnt vmcnt(1)\n"
    "v_cmp_le_u32 s[56:57], s59, v245\n"
    "v_mfma_f32_16x16x32_bf16 v[52:55], v[184:187], a[196:199], v[52:55]\n"
    "s_add_u32 s76, s71, s60\n"
    "v_exp_f32 v42, v42\n"
    "v_mfma_f32_16x16x32_bf16 v[52:55], v[188:191], a[200:203], v[52:55]\n"
    "s_and_b64 s[54:55], s[54:55], s[56:57]\n"
    "s_cmp_eq_u64 s[54:55], -1\n"
    "v_mfma_f32_16x16x32_bf16 v[52:55], v[192:195], a[204:207], v[52:55]\n"
    "s_cbranch_scc0 .Lpoll4\n"
    ".Lpoll4r:\n"
    "v_mfma_f32_16x16x32_bf16 v[52:55], v[196:199], a[208:211], v[52:55]\n"
    "buffer_load_dwordx4 v1, s[16:19], s67 offen offset:0 sc1 lds\n"
    "v_mfma_f32_16x16x32_bf16 v[52:55], v[200:203], a[212:215], v[52:55]\n"
    "s_add_u32 s77, s71, s61\n"
    "ds_read_b128 v[156:159], v3 offset:0\n"
    "ds_read_b128 v[160:163], v3 offset:2048\n"
    "v_mfma_f32_16x16x32_bf16 v[56:59], v[200:203], a[236:239], v[104:107]\n"
    "s_add_u32 s78, s71, s62\n"
    "ds_read_b128 v[164:167], v3 offset:4096\n"
    "ds_read_b128 v[168:171], v3 offset:6144\n"
    "v_mfma_f32_16x16x32_bf16 v[56:59], v[196:199], a[232:235], v[56:59]\n"
    "ds_read_b128 v[172:175], v3 offset:8192\n"
    "ds_read_b128 v[176:179], v3 offset:10240\n"
    "v_mfma_f32_16x16x32_bf16 v[56:59], v[192:195], a[228:231], v[56:59]\n"
    "buffer_load_dwordx4 v1, s[16:19], s67 offen offset:1024 sc1 lds\n"
    "v_mfma_f32_16x16x32_bf16 v[56:59], v[188:191], a[224:227], v[56:59]\n"
    "ds_read_b128 v[100:103], v250 offset:1536\n"
    "v_cvt_pk_bf16_f32 v93, v18, v19\n"
    "v_mfma_f32_16x16x32_bf16 v[56:59], v[184:187], a[220:223], v[56:59]\n"
    "v_cvt_pk_bf16_f32 v70, v20, v21\n"
    "v_cvt_pk_bf16_f32 v71, v22, v23\n"
    "v_mfma_f32_16x16x32_bf16 v[56:59], v[180:183], a[216:219], v[56:59]\n"
    "v_exp_f32 v43, v43\n"
    "v_mfma_f32_16x16x32_bf16 v[60:63], v[180:183], a[240:243], v[104:107]\n"
    "buffer_load_dwordx4 v1, s[16:19], s67 offen offset:2048 sc1 lds\n"
    "v_mfma_f32_16x16x32_bf16 v[60:63], v[184:187], a[244:247], v[60:63]\n"
    "v_cvt_pk_bf16_f32 v78, v24, v25\n"
    "v_cvt_pk_bf16_f32 v80, v40, v41\n"
    "v_mfma_f32_16x16x32_bf16 v[60:63], v[188:191], a[248:251], v[60:63]\n"
    "v_cvt_pk_bf16_f32 v81, v42, v43\n"
    "v_cvt_f32_i32 v204, v204\n"
    "v_mfma_f32_16x16x32_bf16 v[60:63], v[192:195], a[252:255], v[60:63]\n"
    "v_exp_f32 v44, v44\n"
    "v_mfma_f32_16x16x32_bf16 v[60:63], v[196:199], v[124:127], v[60:63]\n"
    "buffer_load_dwordx4 v1, s[16:19], s67 offen offset:3072 sc1 lds\n"
    "v_mfma_f32_16x16x32_bf16 v[60:63], v[200:203], v[128:131], v[60:63]\n"
    "s_add_u32 m0, s68, 4096\n"
    "v_cvt_pk_bf16_f32 v79, v26, v27\n"
    "v_cvt_f32_i32 v208, v208\n"
    "v_mfma_f32_16x16x32_bf16 v[64:67], v[200:203], v[152:155], v[104:107]\n"
    "s_add_u32 s83, s67, 4096\n"
    "v_exp_f32 v45, v45\n"
    "v_mfma_f32_16x16x32_bf16 v[64:67], v[196:199], v[148:151], v[64:67]\n"
    "v_exp_f32 v46, v46\n"
    "v_mfma_f32_16x16x32_bf16 v[64:67], v[192:195], v[144:147], v[64:67]\n"
    "buffer_load_dwordx4 v1, s[16:19], s83 offen offset:0 sc1 lds\n"
    "v_mfma_f32_16x16x32_bf16 v[64:67], v[188:191], v[140:143], v[64:67]\n"
    "v_cvt_pk_bf16_f32 v86, v28, v29\n"
    "v_cvt_pk_bf16_f32 v88, v44, v45\n"
    "v_mfma_f32_16x16x32_bf16 v[64:67], v[184:187], v[136:139], v[64:67]\n"
    "v_exp_f32 v47, v47\n"
    "v_mfma_f32_16x16x32_bf16 v[64:67], v[180:183], v[132:135], v[64:67]\n"
    "s_waitcnt lgkmcnt(0)\n"
    "v_cvt_pk_bf16_f32 v89, v46, v47\n"
    "v_cvt_f32_i32 v205, v205\n"
    "v_mfma_f32_16x16x32_bf16 v[4:7], v[156:159], a[192:195], v[100:103]\n"
    "buffer_load_dwordx4 v1, s[16:19], s83 offen offset:1024 sc1 lds\n"
    "v_mfma_f32_16x16x32_bf16 v[4:7], v[160:163], a[196:199], v[4:7]\n"
    "s_add_u32 m0, s70, 0\n"
    "ds_read_b128 v[180:183], v3 offset:1024\n"
    "ds_read_b128 v[184:187], v3 offset:3072\n"
    "v_mfma_f32_16x16x32_bf16 v[4:7], v[164:167], a[200:203], v[4:7]\n"
    "s_add_u32 s83, s67, 24576\n"
    "ds_read_b128 v[188:191], v3 offset:5120\n"
    "v_cvt_pk_bf16_f32 v87, v30, v31\n"
    "v_mfma_f32_16x16x32_bf16 v[4:7], v[168:171], a[204:207], v[4:7]\n"
    "ds_read_b128 v[192:195], v3 offset:7168\n"
    "v_cvt_f32_i32 v209, v209\n"
    "v_mfma_f32_16x16x32_bf16 v[4:7], v[172:175], a[208:211], v[4:7]\n"
    "buffer_load_dwordx4 v1, s[16:19], s83 offen offset:0 sc1 lds\n"
    "v_mfma_f32_16x16x32_bf16 v[4:7], v[176:179], a[212:215], v[4:7]\n"
    "ds_read_b128 v[196:199], v3 offset:9216\n"
    "ds_read_b128 v[200:203], v3 offset:11264\n"
    "v_mfma_f32_16x16x32_bf16 v[8:11], v[176:179], a[236:239], v[100:103]\n"
    "ds_read_b128 v[104:107], v250 offset:1552\n"
    "v_cvt_pk_bf16_f32 v94, v32, v33\n"
    "v_mfma_f32_16x16x32_bf16 v[8:11], v[172:175], a[232:235], v[8:11]\n"
    "v_exp_f32 v52, v52\n"
    "v_mfma_f32_16x16x32_bf16 v[8:11], v[168:171], a[228:231], v[8:11]\n"
    "buffer_load_dwordx4 v1, s[16:19], s83 offen offset:1024 sc1 lds\n"
    "v_mfma_f32_16x16x32_bf16 v[8:11], v[164:167], a[224:227], v[8:11]\n"
    "v_cvt_pk_bf16_f32 v95, v34, v35\n"
    "v_cvt_f32_i32 v206, v206\n"
    "v_mfma_f32_16x16x32_bf16 v[8:11], v[160:163], a[220:223], v[8:11]\n"
    "v_exp_f32 v53, v53\n"
    "v_mfma_f32_16x16x32_bf16 v[8:11], v[156:159], a[216:219], v[8:11]\n"
    "v_exp_f32 v54, v54\n"
    "v_mfma_f32_16x16x32_bf16 v[12:15], v[156:159], a[240:243], v[100:103]\n"
    "buffer_load_dwordx4 v1, s[16:19], s83 offen offset:2048 sc1 lds\n"
    "v_mfma_f32_16x16x32_bf16 v[12:15], v[160:163], a[244:247], v[12:15]\n"
    "v_cvt_pk_bf16_f32 v74, v52, v53\n"
    "v_cvt_f32_i32 v210, v210\n"
    "v_mfma_f32_16x16x32_bf16 v[12:15], v[164:167], a[248:251], v[12:15]\n"
    "v_exp_f32 v55, v55\n"
    "v_mfma_f32_16x16x32_bf16 v[12:15], v[168:171], a[252:255], v[12:15]\n"
    "v_cvt_pk_bf16_f32 v75, v54, v55\n"
    "v_cvt_f32_i32 v207, v207\n"
    "v_mfma_f32_16x16x32_bf16 v[12:15], v[172:175], v[124:127], v[12:15]\n"
    "buffer_load_dwordx4 v1, s[16:19], s83 offen offset:3072 sc1 lds\n"
    "v_mfma_f32_16x16x32_bf16 v[12:15], v[176:179], v[128:131], v[12:15]\n"
    "s_add_u32 m0, s70, 4096\n"
    "v_exp_f32 v56, v56\n"
    "v_mfma_f32_16x16x32_bf16 v[16:19], v[176:179], v[152:155], v[100:103]\n"
    "s_add_u32 s83, s67, 28672\n"
    "v_exp_f32 v48, v48\n"
    "v_mfma_f32_16x16x32_bf16 v[16:19], v[172:175], v[148:151], v[16:19]\n"
    "v_exp_f32 v49, v49\n"
    "v_mfma_f32_16x16x32_bf16 v[16:19], v[168:171], v[144:147], v[16:19]\n"
    "buffer_load_dwordx4 v1, s[16:19], s83 offen offset:0 sc1 lds\n"
    "v_mfma_f32_16x16x32_bf16 v[16:19], v[164:167], v[140:143], v[16:19]\n"
    "v_exp_f32 v57, v57\n"
    "v_mfma_f32_16x16x32_bf16 v[16:19], v[160:163], v[136:139], v[16:19]\n"
    "v_cvt_pk_bf16_f32 v82, v56, v57\n"
    "v_cvt_pk_bf16_f32 v96, v48, v49\n"
    "v_mfma_f32_16x16x32_bf16 v[16:19], v[156:159], v[132:135], v[16:19]\n"
    "s_waitcnt lgkmcnt(0)\n"
    "v_exp_f32 v58, v58\n"
    "v_mfma_f32_16x16x32_bf16 v[20:23], v[180:183], a[192:195], v[104:107]\n"
    "buffer_load_dwordx4 v1, s[16:19], s83 offen offset:1024 sc1 lds\n"
    "v_mfma_f32_16x16x32_bf16 v[20:23], v[184:187], a[196:199], v[20:23]\n"
    "s_add_u32 s67, s67, 49152\n"
    "ds_read_b128 v[156:159], v249 offset:0\n"
    "ds_read_b128 v[160:163], v249 offset:12288\n"
    "v_mfma_f32_16x16x32_bf16 v[20:23], v[188:191], a[200:203], v[20:23]\n"
    "s_add_u32 s59, s59, 4\n"
    "ds_read_b128 v[164:167], v249 offset:1024\n"
    "v_cvt_f32_i32 v211, v211\n"
    "v_mfma_f32_16x16x32_bf16 v[20:23], v[192:195], a[204:207], v[20:23]\n"
    "s_add_u32 s81, s81, 128\n"
    "ds_read_b128 v[168:171], v249 offset:13312\n"
    "v_cvt_pk_bf16_f32 v204, v204, v208\n"
    "v_mfma_f32_16x16x32_bf16 v[20:23], v[196:199], a[208:211], v[20:23]\n"
    "s_min_u32 s81, s81, 4096\n"
    "ds_read_b128 v[172:175], v249 offset:2048\n"
    "v_cvt_pk_bf16_f32 v205, v205, v209\n"
    "v_mfma_f32_16x16x32_bf16 v[20:23], v[200:203], a[212:215], v[20:23]\n"
    "ds_read_b128 v[176:179], v249 offset:14336\n"
    "v_cvt_pk_bf16_f32 v206, v206, v210\n"
    "v_mfma_f32_16x16x32_bf16 v[24:27], v[200:203], a[236:239], v[104:107]\n"
    "v_exp_f32 v59, v59\n"
    "v_mfma_f32_16x16x32_bf16 v[24:27], v[196:199], a[232:235], v[24:27]\n"
    "v_cvt_pk_bf16_f32 v83, v58, v59\n"
    "v_cvt_pk_bf16_f32 v207, v207, v211\n"
    "v_mfma_f32_16x16x32_bf16 v[24:27], v[192:195], a[228:231], v[24:27]\n"
    "v_exp_f32 v60, v60\n"
    "v_mfma_f32_16x16x32_bf16 v[24:27], v[188:191], a[224:227], v[24:27]\n"
    "v_exp_f32 v61, v61\n"
    "v_mfma_f32_16x16x32_bf16 v[24:27], v[184:187], a[220:223], v[24:27]\n"
    "v_exp_f32 v62, v62\n"
    "v_mfma_f32_16x16x32_bf16 v[24:27], v[180:183], a[216:219], v[24:27]\n"
    "v_cvt_pk_bf16_f32 v90, v60, v61\n"
    "v_cvt_f32_i32 v212, v212\n"
    "v_mfma_f32_16x16x32_bf16 v[28:31], v[180:183], a[240:243], v[104:107]\n"
    "v_exp_f32 v50, v50\n"
    "v_mfma_f32_16x16x32_bf16 v[28:31], v[184:187], a[244:247], v[28:31]\n"
    "v_exp_f32 v63, v63\n"
    "v_mfma_f32_16x16x32_bf16 v[28:31], v[188:191], a[248:251], v[28:31]\n"
    "v_cvt_pk_bf16_f32 v91, v62, v63\n"
    "v_cvt_f32_i32 v216, v216\n"
    "v_mfma_f32_16x16x32_bf16 v[28:31], v[192:195], a[252:255], v[28:31]\n"
    "v_exp_f32 v51, v51\n"
    "v_mfma_f32_16x16x32_bf16 v[28:31], v[196:199], v[124:127], v[28:31]\n"
    "v_exp_f32 v64, v64\n"
    "v_mfma_f32_16x16x32_bf16 v[28:31], v[200:203], v[128:131], v[28:31]\n"
    "v_cvt_pk_bf16_f32 v97, v50, v51\n"
    "v_cvt_f32_i32 v213, v213\n"
    "v_mfma_f32_16x16x32_bf16 v[32:35], v[200:203], v[152:155], v[104:107]\n"
    "v_cvt_f32_i32 v217, v217\n"
    "v_cvt_f32_i32 v214, v214\n"
    "v_mfma_f32_16x16x32_bf16 v[32:35], v[196:199], v[148:151], v[32:35]\n"
    "v_exp_f32 v65, v65\n"
    "v_mfma_f32_16x16x32_bf16 v[32:35], v[192:195], v[144:147], v[32:35]\n"
    "v_cvt_pk_bf16_f32 v98, v64, v65\n"
    "v_cvt_f32_i32 v218, v218\n"
    "v_mfma_f32_16x16x32_bf16 v[32:35], v[188:191], v[140:143], v[32:35]\n"
    "v_cvt_f32_i32 v215, v215\n"
    "v_cvt_f32_i32 v219, v219\n"
    "v_mfma_f32_16x16x32_bf16 v[32:35], v[184:187], v[136:139], v[32:35]\n"
    "v_exp_f32 v66, v66\n"
    "v_mfma_f32_16x16x32_bf16 v[32:35], v[180:183], v[132:135], v[32:35]\n"
    "s_waitcnt lgkmcnt(0)\n"
    "v_mfma_f32_16x16x32_bf16 a[0:3], v[156:159], v[68:71], a[0:3]\n"
    "ds_read_b128 v[180:183], v249 offset:3072\n"
    "v_mfma_f32_16x16x32_bf16 a[0:3], v[160:163], v[72:75], a[0:3]\n"
    "ds_read_b128 v[184:187], v249 offset:15360\n"
    "v_cvt_pk_bf16_f32 v212, v212, v216\n"
    "v_mfma_f32_16x16x32_bf16 a[16:19], v[168:171], v[72:75], a[16:19]\n"
    "ds_read_b128 v[188:191], v249 offset:4096\n"
    "v_mfma_f32_16x16x32_bf16 a[16:19], v[164:167], v[68:71], a[16:19]\n"
    "ds_read_b128 v[192:195], v249 offset:16384\n"
    "v_mfma_f32_16x16x32_bf16 a[32:35], v[172:175], v[68:71], a[32:35]\n"
    "ds_read_b128 v[196:199], v249 offset:5120\n"
    "v_cvt_pk_bf16_f32 v213, v213, v217\n"
    "v_mfma_f32_16x16x32_bf16 a[32:35], v[176:179], v[72:75], a[32:35]\n"
    "ds_read_b128 v[200:203], v249 offset:17408\n"
    "v_mfma_f32_16x16x32_bf16 a[36:39], v[176:179], v[80:83], a[36:39]\n"
    "ds_read_b128 v[104:107], v251 offset:768\n"
    "v_mfma_f32_16x16x32_bf16 a[36:39], v[172:175], v[76:79], a[36:39]\n"
    "v_exp_f32 v67, v67\n"
    "v_mfma_f32_16x16x32_bf16 a[20:23], v[164:167], v[76:79], a[20:23]\n"
    "v_cvt_pk_bf16_f32 v99, v66, v67\n"
    "v_cvt_pk_bf16_f32 v214, v214, v218\n"
    "v_mfma_f32_16x16x32_bf16 a[20:23], v[168:171], v[80:83], a[20:23]\n"
    "v_exp_f32 v4, v4\n"
    "v_mfma_f32_16x16x32_bf16 a[4:7], v[160:163], v[80:83], a[4:7]\n"
    "v_exp_f32 v5, v5\n"
    "v_mfma_f32_16x16x32_bf16 a[4:7], v[156:159], v[76:79], a[4:7]\n"
    "v_exp_f32 v6, v6\n"
    "v_mfma_f32_16x16x32_bf16 a[8:11], v[156:159], v[84:87], a[8:11]\n"
    "v_exp_f32 v7, v7\n"
    "v_mfma_f32_16x16x32_bf16 a[8:11], v[160:163], v[88:91], a[8:11]\n"
    "v_cvt_pk_bf16_f32 v215, v215, v219\n"
    "v_cvt_f32_i32 v220, v220\n"
    "v_mfma_f32_16x16x32_bf16 a[24:27], v[168:171], v[88:91], a[24:27]\n"
    "v_exp_f32 v8, v8\n"
    "v_mfma_f32_16x16x32_bf16 a[24:27], v[164:167], v[84:87], a[24:27]\n"
    "v_mfma_f32_16x16x32_bf16 a[40:43], v[172:175], v[84:87], a[40:43]\n"
    "v_mfma_f32_16x16x32_bf16 a[40:43], v[176:179], v[88:91], a[40:43]\n"
    "v_exp_f32 v9, v9\n"
    "v_mfma_f32_16x16x32_bf16 a[44:47], v[176:179], v[96:99], a[44:47]\n"
    "v_cvt_f32_i32 v224, v224\n"
    "v_mfma_f32_16x16x32_bf16 a[44:47], v[172:175], v[92:95], a[44:47]\n"
    "v_mfma_f32_16x16x32_bf16 a[28:31], v[164:167], v[92:95], a[28:31]\n"
    "v_exp_f32 v10, v10\n"
    "v_mfma_f32_16x16x32_bf16 a[28:31], v[168:171], v[96:99], a[28:31]\n"
    "v_cvt_f32_i32 v221, v221\n"
    "v_mfma_f32_16x16x32_bf16 a[12:15], v[160:163], v[96:99], a[12:15]\n"
    "v_exp_f32 v11, v11\n"
    "v_mfma_f32_16x16x32_bf16 a[12:15], v[156:159], v[92:95], a[12:15]\n"
    "s_waitcnt lgkmcnt(0)\n"
    "v_cvt_f32_i32 v225, v225\n"
    "v_mfma_f32_16x16x32_bf16 a[48:51], v[180:183], v[68:71], a[48:51]\n"
    "ds_read_b128 v[156:159], v249 offset:6144\n"
    "v_mfma_f32_16x16x32_bf16 a[48:51], v[184:187], v[72:75], a[48:51]\n"
    "ds_read_b128 v[160:163], v249 offset:18432\n"
    "v_mfma_f32_16x16x32_bf16 a[64:67], v[192:195], v[72:75], a[64:67]\n"
    "ds_read_b128 v[164:167], v249 offset:7168\n"
    "v_cvt_f32_i32 v222, v222\n"
    "v_mfma_f32_16x16x32_bf16 a[64:67], v[188:191], v[68:71], a[64:67]\n"
    "ds_read_b128 v[168:171], v249 offset:19456\n"
    "v_mfma_f32_16x16x32_bf16 a[80:83], v[196:199], v[68:71], a[80:83]\n"
    "ds_read_b128 v[172:175], v249 offset:8192\n"
    "v_mfma_f32_16x16x32_bf16 a[80:83], v[200:203], v[72:75], a[80:83]\n"
    "ds_read_b128 v[176:179], v249 offset:20480\n"
    "v_cvt_f32_i32 v226, v226\n"
    "v_smfmac_f32_16x16x64_bf16 v[108:111], v[104:107], v[68:75], v253\n"
    "v_smfmac_f32_16x16x64_bf16 v[112:115], v[104:107], v[76:83], v253\n"
    "v_mfma_f32_16x16x32_bf16 a[84:87], v[200:203], v[80:83], a[84:87]\n"
    "v_exp_f32 v12, v12\n"
    "v_mfma_f32_16x16x32_bf16 a[84:87], v[196:199], v[76:79], a[84:87]\n"
    "v_exp_f32 v13, v13\n"
    "v_mfma_f32_16x16x32_bf16 a[68:71], v[188:191], v[76:79], a[68:71]\n"
    "v_exp_f32 v14, v14\n"
    "v_mfma_f32_16x16x32_bf16 a[68:71], v[192:195], v[80:83], a[68:71]\n"
    "v_exp_f32 v15, v15\n"
    "v_mfma_f32_16x16x32_bf16 a[52:55], v[184:187], v[80:83], a[52:55]\n"
    "v_cvt_f32_i32 v223, v223\n"
    "v_cvt_f32_i32 v227, v227\n"
    "v_mfma_f32_16x16x32_bf16 a[52:55], v[180:183], v[76:79], a[52:55]\n"
    "v_exp_f32 v16, v16\n"
    "v_mfma_f32_16x16x32_bf16 a[56:59], v[180:183], v[84:87], a[56:59]\n"
    "v_exp_f32 v17, v17\n"
    "v_mfma_f32_16x16x32_bf16 a[56:59], v[184:187], v[88:91], a[56:59]\n"
    "v_cvt_pk_bf16_f32 v220, v220, v224\n"
    "v_mfma_f32_16x16x32_bf16 a[72:75], v[192:195], v[88:91], a[72:75]\n"
    "v_mfma_f32_16x16x32_bf16 a[72:75], v[188:191], v[84:87], a[72:75]\n"
    "v_exp_f32 v18, v18\n"
    "v_mfma_f32_16x16x32_bf16 a[88:91], v[196:199], v[84:87], a[88:91]\n"
    "v_cvt_pk_bf16_f32 v221, v221, v225\n"
    "v_mfma_f32_16x16x32_bf16 a[88:91], v[200:203], v[88:91], a[88:91]\n"
    "v_smfmac_f32_16x16x64_bf16 v[116:119], v[104:107], v[84:91], v253\n"
    "v_smfmac_f32_16x16x64_bf16 v[120:123], v[104:107], v[92:99], v253\n"
    "v_mfma_f32_16x16x32_bf16 a[92:95], v[200:203], v[96:99], a[92:95]\n"
    "v_exp_f32 v19, v19\n"
    "v_mfma_f32_16x16x32_bf16 a[92:95], v[196:199], v[92:95], a[92:95]\n"
    "v_exp_f32 v20, v20\n"
    "v_mfma_f32_16x16x32_bf16 a[76:79], v[188:191], v[92:95], a[76:79]\n"
    "v_cvt_pk_bf16_f32 v222, v222, v226\n"
    "v_cvt_pk_bf16_f32 v223, v223, v227\n"
    "v_mfma_f32_16x16x32_bf16 a[76:79], v[192:195], v[96:99], a[76:79]\n"
    "v_exp_f32 v21, v21\n"
    "v_mfma_f32_16x16x32_bf16 a[60:63], v[184:187], v[96:99], a[60:63]\n"
    "v_mfma_f32_16x16x32_bf16 a[60:63], v[180:183], v[92:95], a[60:63]\n"
    "s_waitcnt lgkmcnt(0)\n"
    "v_mfma_f32_16x16x32_bf16 a[96:99], v[156:159], v[68:71], a[96:99]\n"
    "ds_read_b128 v[180:183], v249 offset:9216\n"
    "v_mfma_f32_16x16x32_bf16 a[96:99], v[160:163], v[72:75], a[96:99]\n"
    "ds_read_b128 v[184:187], v249 offset:21504\n"
    "v_mfma_f32_16x16x32_bf16 a[112:115], v[168:171], v[72:75], a[112:115]\n"
    "ds_read_b128 v[188:191], v249 offset:10240\n"
    "v_mfma_f32_16x16x32_bf16 a[112:115], v[164:167], v[68:71], a[112:115]\n"
    "ds_read_b128 v[192:195], v249 offset:22528\n"
    "v_mfma_f32_16x16x32_bf16 a[128:131], v[172:175], v[68:71], a[128:131]\n"
    "buffer_load_dwordx2 v[236:237], v241, s[36:39], s81 offen offset:0 sc1\n"
    "v_mfma_f32_16x16x32_bf16 a[128:131], v[176:179], v[72:75], a[128:131]\n"
    "ds_read_b128 v[196:199], v249 offset:11264\n"
    "ds_read_b128 v[200:203], v249 offset:23552\n"
    "v_mfma_f32_16x16x32_bf16 a[132:135], v[176:179], v[80:83], a[132:135]\n"
    "buffer_store_dword v204, v244, s[16:19], s76 offen offset:0 sc1\n"
    "v_mfma_f32_16x16x32_bf16 a[132:135], v[172:175], v[76:79], a[132:135]\n"
    "v_exp_f32 v22, v22\n"
    "v_mfma_f32_16x16x32_bf16 a[116:119], v[164:167], v[76:79], a[116:119]\n"
    "buffer_store_dword v205, v244, s[16:19], s76 offen offset:1024 sc1\n"
    "v_mfma_f32_16x16x32_bf16 a[116:119], v[168:171], v[80:83], a[116:119]\n"
    "v_exp_f32 v23, v23\n"
    "v_mfma_f32_16x16x32_bf16 a[100:103], v[160:163], v[80:83], a[100:103]\n"
    "buffer_store_dword v206, v244, s[16:19], s76 offen offset:2048 sc1\n"
    "v_mfma_f32_16x16x32_bf16 a[100:103], v[156:159], v[76:79], a[100:103]\n"
    "v_exp_f32 v24, v24\n"
    "v_mfma_f32_16x16x32_bf16 a[104:107], v[156:159], v[84:87], a[104:107]\n"
    "buffer_store_dword v207, v244, s[16:19], s76 offen offset:3072 sc1\n"
    "v_mfma_f32_16x16x32_bf16 a[104:107], v[160:163], v[88:91], a[104:107]\n"
    "v_exp_f32 v25, v25\n"
    "v_mfma_f32_16x16x32_bf16 a[120:123], v[168:171], v[88:91], a[120:123]\n"
    "buffer_store_dword v212, v244, s[16:19], s77 offen offset:0 sc1\n"
    "v_mfma_f32_16x16x32_bf16 a[120:123], v[164:167], v[84:87], a[120:123]\n"
    "v_exp_f32 v26, v26\n"
    "v_mfma_f32_16x16x32_bf16 a[136:139], v[172:175], v[84:87], a[136:139]\n"
    "buffer_store_dword v213, v244, s[16:19], s77 offen offset:1024 sc1\n"
    "v_mfma_f32_16x16x32_bf16 a[136:139], v[176:179], v[88:91], a[136:139]\n"
    "v_exp_f32 v27, v27\n"
    "v_mfma_f32_16x16x32_bf16 a[140:143], v[176:179], v[96:99], a[140:143]\n"
    "buffer_store_dword v214, v244, s[16:19], s77 offen offset:2048 sc1\n"
    "v_mfma_f32_16x16x32_bf16 a[140:143], v[172:175], v[92:95], a[140:143]\n"
    "v_exp_f32 v28, v28\n"
    "v_mfma_f32_16x16x32_bf16 a[124:127], v[164:167], v[92:95], a[124:127]\n"
    "buffer_store_dword v215, v244, s[16:19], s77 offen offset:3072 sc1\n"
    "v_mfma_f32_16x16x32_bf16 a[124:127], v[168:171], v[96:99], a[124:127]\n"
    "v_exp_f32 v29, v29\n"
    "v_mfma_f32_16x16x32_bf16 a[108:111], v[160:163], v[96:99], a[108:111]\n"
    "buffer_store_dword v220, v244, s[16:19], s78 offen offset:0 sc1\n"
    "v_mfma_f32_16x16x32_bf16 a[108:111], v[156:159], v[92:95], a[108:111]\n"
    "s_waitcnt lgkmcnt(0)\n"
    "v_exp_f32 v30, v30\n"
    "v_mfma_f32_16x16x32_bf16 a[144:147], v[180:183], v[68:71], a[144:147]\n"
    "ds_read_b128 v[156:159], v3 offset:12288\n"
    "v_mfma_f32_16x16x32_bf16 a[144:147], v[184:187], v[72:75], a[144:147]\n"
    "ds_read_b128 v[160:163], v3 offset:14336\n"
    "v_mfma_f32_16x16x32_bf16 a[160:163], v[192:195], v[72:75], a[160:163]\n"
    "ds_read_b128 v[164:167], v3 offset:16384\n"
    "v_mfma_f32_16x16x32_bf16 a[160:163], v[188:191], v[68:71], a[160:163]\n"
    "ds_read_b128 v[168:171], v3 offset:18432\n"
    "v_mfma_f32_16x16x32_bf16 a[176:179], v[196:199], v[68:71], a[176:179]\n"
    "ds_read_b128 v[172:175], v3 offset:20480\n"
    "v_mfma_f32_16x16x32_bf16 a[176:179], v[200:203], v[72:75], a[176:179]\n"
    "ds_read_b128 v[176:179], v3 offset:22528\n"
    "v_mfma_f32_16x16x32_bf16 a[180:183], v[200:203], v[80:83], a[180:183]\n"
    "ds_read_b128 v[100:103], v250 offset:1664\n"
    "v_mfma_f32_16x16x32_bf16 a[180:183], v[196:199], v[76:79], a[180:183]\n"
    "buffer_store_dword v221, v244, s[16:19], s78 offen offset:1024 sc1\n"
    "v_mfma_f32_16x16x32_bf16 a[164:167], v[188:191], v[76:79], a[164:167]\n"
    "v_exp_f32 v31, v31\n"
    "v_mfma_f32_16x16x32_bf16 a[164:167], v[192:195], v[80:83], a[164:167]\n"
    "buffer_store_dword v222, v244, s[16:19], s78 offen offset:2048 sc1\n"
    "v_mfma_f32_16x16x32_bf16 a[148:151], v[184:187], v[80:83], a[148:151]\n"
    "v_exp_f32 v32, v32\n"
    "v_mfma_f32_16x16x32_bf16 a[148:151], v[180:183], v[76:79], a[148:151]\n"
    "buffer_store_dword v223, v244, s[16:19], s78 offen offset:3072 sc1\n"
    "v_mfma_f32_16x16x32_bf16 a[152:155], v[180:183], v[84:87], a[152:155]\n"
    "v_exp_f32 v33, v33\n"
    "v_mfma_f32_16x16x32_bf16 a[152:155], v[184:187], v[88:91], a[152:155]\n"
    "v_exp_f32 v34, v34\n"
    "v_mfma_f32_16x16x32_bf16 a[168:171], v[192:195], v[88:91], a[168:171]\n"
    "s_waitcnt vmcnt(13)\n"
    "ds_add_u32 v247, v246\n"
    "v_mfma_f32_16x16x32_bf16 a[168:171], v[188:191], v[84:87], a[168:171]\n"
    "v_exp_f32 v35, v35\n"
    "v_mfma_f32_16x16x32_bf16 a[184:187], v[196:199], v[84:87], a[184:187]\n"
    "v_mfma_f32_16x16x32_bf16 a[184:187], v[200:203], v[88:91], a[184:187]\n"
    "v_mfma_f32_16x16x32_bf16 a[188:191], v[200:203], v[96:99], a[188:191]\n"
    "v_mfma_f32_16x16x32_bf16 a[188:191], v[196:199], v[92:95], a[188:191]\n"
    "v_mfma_f32_16x16x32_bf16 a[172:175], v[188:191], v[92:95], a[172:175]\n"
    "s_sub_u32 s74, s68, s65\n"
    "v_add_u32 v250, 1536, v250\n"
    "v_add_u32 v251, 1536, v251\n"
    "v_mfma_f32_16x16x32_bf16 a[172:175], v[192:195], v[96:99], a[172:175]\n"
    "s_add_u32 s66, s66, 1\n"
    "v_mov_b32 v2, v3\n"
    "v_add_u32 v3, s74, v252\n"
    "v_mfma_f32_16x16x32_bf16 a[156:159], v[184:187], v[96:99], a[156:159]\n"
    "s_waitcnt lgkmcnt(1)\n"
    "s_cmp_lt_u32 s66, 31\n"
    "v_mfma_f32_16x16x32_bf16 a[156:159], v[180:183], v[92:95], a[156:159]\n"
    "s_cbranch_scc1 .LPV\n"
    "s_branch .Ltail\n"
    ".LtailP:\n"
    "v_mov_b32 v248, v249\n"
    "v_add_u32 v250, 768, v250\n"
    "v_add_u32 v251, 768, v251\n"
    ".Ltail:\n"
    "v_mfma_f32_16x16x32_bf16 v[36:39], v[156:159], a[192:195], v[100:103]\n"
    "s_mul_i32 s75, s84, 24576\n"
    "ds_read_b128 v[180:183], v2 offset:13312\n"
    "ds_read_b128 v[104:107], v250 offset:144\n"
    "v_mfma_f32_16x16x32_bf16 v[36:39], v[160:163], a[196:199], v[36:39]\n"
    "s_add_u32 s76, s75, 6144\n"
    "ds_read_b128 v[184:187], v2 offset:15360\n"
    "ds_read_b128 v[188:191], v2 offset:17408\n"
    "v_mfma_f32_16x16x32_bf16 v[36:39], v[164:167], a[200:203], v[36:39]\n"
    "s_add_u32 s77, s76, 6144\n"
    "ds_read_b128 v[192:195], v2 offset:19456\n"
    "ds_read_b128 v[196:199], v2 offset:21504\n"
    "v_mfma_f32_16x16x32_bf16 v[36:39], v[168:171], a[204:207], v[36:39]\n"
    "s_add_u32 s78, s77, 6144\n"
    "ds_read_b128 v[200:203], v2 offset:23552\n"
    "v_cvt_pk_bf16_f32 v68, v4, v5\n"
    "v_mfma_f32_16x16x32_bf16 v[36:39], v[172:175], a[208:211], v[36:39]\n"
    "s_add_u32 s72, s65, 122880\n"
    "v_cvt_pk_bf16_f32 v69, v6, v7\n"
    "v_cvt_pk_bf16_f32 v84, v12, v13\n"
    "v_mfma_f32_16x16x32_bf16 v[36:39], v[176:179], a[212:215], v[36:39]\n"
    "s_mov_b32 s74, 0x5b800000\n"
    "v_cvt_pk_bf16_f32 v85, v14, v15\n"
    "v_cvt_pk_bf16_f32 v70, v20, v21\n"
    "v_mfma_f32_16x16x32_bf16 v[40:43], v[176:179], a[236:239], v[100:103]\n"
    "s_mov_b32 s73, 0x7f800000\n"
    "v_cvt_pk_bf16_f32 v71, v22, v23\n"
    "v_cvt_pk_bf16_f32 v86, v28, v29\n"
    "v_mfma_f32_16x16x32_bf16 v[40:43], v[172:175], a[232:235], v[40:43]\n"
    "s_waitcnt lgkmcnt(6)\n"
    "v_cvt_pk_bf16_f32 v87, v30, v31\n"
    "ds_read_b128 v[244:247], v251 offset:0\n"
    "v_mfma_f32_16x16x32_bf16 v[40:43], v[168:171], a[228:231], v[40:43]\n"
    "s_waitcnt lgkmcnt(5)\n"
    "v_cvt_pk_bf16_f32 v76, v8, v9\n"
    "v_cvt_pk_bf16_f32 v77, v10, v11\n"
    "v_mfma_f32_16x16x32_bf16 v[40:43], v[164:167], a[224:227], v[40:43]\n"
    "s_waitcnt lgkmcnt(3)\n"
    "v_cvt_pk_bf16_f32 v92, v16, v17\n"
    "v_cvt_pk_bf16_f32 v93, v18, v19\n"
    "v_mfma_f32_16x16x32_bf16 v[40:43], v[160:163], a[220:223], v[40:43]\n"
    "s_waitcnt lgkmcnt(1)\n"
    "v_cvt_pk_bf16_f32 v78, v24, v25\n"
    "v_cvt_pk_bf16_f32 v79, v26, v27\n"
    "v_mfma_f32_16x16x32_bf16 v[40:43], v[156:159], a[216:219], v[40:43]\n"
    "v_cvt_pk_bf16_f32 v94, v32, v33\n"
    "v_cvt_pk_bf16_f32 v95, v34, v35\n"
    "v_mfma_f32_16x16x32_bf16 v[52:55], v[180:183], a[192:195], v[104:107]\n"
    "v_exp_f32 v36, v36\n"
    "v_mfma_f32_16x16x32_bf16 v[52:55], v[184:187], a[196:199], v[52:55]\n"
    "v_exp_f32 v37, v37\n"
    "v_mfma_f32_16x16x32_bf16 v[52:55], v[188:191], a[200:203], v[52:55]\n"
    "v_exp_f32 v38, v38\n"
    "v_mfma_f32_16x16x32_bf16 v[52:55], v[192:195], a[204:207], v[52:55]\n"
    "s_waitcnt lgkmcnt(0)\n"
    "v_exp_f32 v39, v39\n"
    "v_mfma_f32_16x16x32_bf16 v[52:55], v[196:199], a[208:211], v[52:55]\n"
    "v_cvt_pk_bf16_f32 v72, v36, v37\n"
    "v_cvt_pk_bf16_f32 v73, v38, v39\n"
    "v_mfma_f32_16x16x32_bf16 v[52:55], v[200:203], a[212:215], v[52:55]\n"
    "s_barrier\n"
    "ds_read_b128 v[4:7], v248 offset:0\n"
    "ds_read_b128 v[12:15], v248 offset:1024\n"
    "v_mfma_f32_16x16x32_bf16 v[56:59], v[200:203], a[236:239], v[104:107]\n"
    "ds_read_b128 v[20:23], v248 offset:2048\n"
    "ds_read_b128 v[28:31], v248 offset:3072\n"
    "v_mfma_f32_16x16x32_bf16 v[56:59], v[196:199], a[232:235], v[56:59]\n"
    "ds_read_b128 v[8:11], v248 offset:12288\n"
    "ds_read_b128 v[16:19], v248 offset:13312\n"
    "v_mfma_f32_16x16x32_bf16 v[56:59], v[192:195], a[228:231], v[56:59]\n"
    "ds_read_b128 v[24:27], v248 offset:14336\n"
    "ds_read_b128 v[32:35], v248 offset:15360\n"
    "v_mfma_f32_16x16x32_bf16 v[56:59], v[188:191], a[224:227], v[56:59]\n"
    "s_waitcnt vmcnt(0)\n"
    "v_exp_f32 v52, v52\n"
    "v_mfma_f32_16x16x32_bf16 v[56:59], v[184:187], a[220:223], v[56:59]\n"
    "v_exp_f32 v53, v53\n"
    "v_mfma_f32_16x16x32_bf16 v[56:59], v[180:183], a[216:219], v[56:59]\n"
    "v_exp_f32 v54, v54\n"
    "v_exp_f32 v55, v55\n"
    "v_cvt_pk_bf16_f32 v74, v52, v53\n"
    "v_cvt_pk_bf16_f32 v75, v54, v55\n"
    "s_waitcnt lgkmcnt(7)\n"
    "v_mfma_f32_16x16x32_bf16 a[0:3], v[4:7], v[68:71], a[0:3]\n"
    "s_waitcnt lgkmcnt(4)\n"
    "v_exp_f32 v40, v40\n"
    "v_mfma_f32_16x16x32_bf16 a[16:19], v[12:15], v[68:71], a[16:19]\n"
    "s_waitcnt lgkmcnt(2)\n"
    "v_exp_f32 v41, v41\n"
    "v_mfma_f32_16x16x32_bf16 a[32:35], v[20:23], v[68:71], a[32:35]\n"
    "s_waitcnt lgkmcnt(0)\n"
    "v_exp_f32 v42, v42\n"
    "v_mfma_f32_16x16x32_bf16 a[48:51], v[28:31], v[68:71], a[48:51]\n"
    "v_exp_f32 v43, v43\n"
    "v_smfmac_f32_16x16x64_bf16 v[108:111], v[244:247], v[68:75], v253\n"
    "v_exp_f32 v56, v56\n"
    "v_mfma_f32_16x16x32_bf16 a[0:3], v[8:11], v[72:75], a[0:3]\n"
    "v_exp_f32 v57, v57\n"
    "v_mfma_f32_16x16x32_bf16 a[16:19], v[16:19], v[72:75], a[16:19]\n"
    "v_exp_f32 v58, v58\n"
    "v_mfma_f32_16x16x32_bf16 a[32:35], v[24:27], v[72:75], a[32:35]\n"
    "v_exp_f32 v59, v59\n"
    "v_mfma_f32_16x16x32_bf16 a[48:51], v[32:35], v[72:75], a[48:51]\n"
    "v_cvt_pk_bf16_f32 v80, v40, v41\n"
    "v_cvt_pk_bf16_f32 v81, v42, v43\n"
    "v_cvt_pk_bf16_f32 v82, v56, v57\n"
    "v_cvt_pk_bf16_f32 v83, v58, v59\n"
    "v_add_f32 v108, v108, v109\n"
    "v_mov_b32 v109, v108\n"
    "v_smfmac_f32_16x16x64_bf16 v[112:115], v[244:247], v[76:83], v253\n"
    "v_mov_b32 v111, v110\n"
    "v_mfma_f32_16x16x32_bf16 v[44:47], v[156:159], a[240:243], v[100:103]\n"
    "v_permlane16_swap_b32 v108, v109\n"
    "v_add_f32 v108, v108, v109\n"
    "v_mfma_f32_16x16x32_bf16 v[44:47], v[160:163], a[244:247], v[44:47]\n"
    "v_lshrrev_b32 v204, 4, v1\n"
    "v_rcp_f32 v109, v108\n"
    "v_mfma_f32_16x16x32_bf16 v[44:47], v[164:167], a[248:251], v[44:47]\n"
    "v_and_b32 v204, 48, v204\n"
    "v_and_b32 v207, 0x70, v1\n"
    "v_mfma_f32_16x16x32_bf16 v[44:47], v[168:171], a[252:255], v[44:47]\n"
    "v_and_b32 v206, 0xf0, v1\n"
    "v_permlane16_swap_b32 v110, v111\n"
    "v_mfma_f32_16x16x32_bf16 v[44:47], v[172:175], v[124:127], v[44:47]\n"
    "v_xor_b32 v204, v204, v207\n"
    "v_lshl_add_u32 v206, v206, 3, s72\n"
    "v_mfma_f32_16x16x32_bf16 v[44:47], v[176:179], v[128:131], v[44:47]\n"
    "v_add_f32 v110, v110, v111\n"
    "v_add_u32 v206, v206, v204\n"
    "v_mfma_f32_16x16x32_bf16 a[52:55], v[28:31], v[76:79], a[52:55]\n"
    "v_mul_f32 v111, -v110, v109\n"
    "v_accvgpr_read_b32 v216, a0\n"
    "v_mfma_f32_16x16x32_bf16 a[52:55], v[32:35], v[80:83], a[52:55]\n"
    "v_accvgpr_read_b32 v217, a16\n"
    "v_accvgpr_read_b32 v224, a2\n"
    "v_mfma_f32_16x16x32_bf16 a[36:39], v[24:27], v[80:83], a[36:39]\n"
    "v_accvgpr_read_b32 v225, a18\n"
    "v_lshrrev_b32 v204, 3, v1\n"
    "v_mfma_f32_16x16x32_bf16 a[36:39], v[20:23], v[76:79], a[36:39]\n"
    "v_accvgpr_read_b32 v218, a32\n"
    "v_accvgpr_read_b32 v219, a48\n"
    "v_mfma_f32_16x16x32_bf16 a[20:23], v[12:15], v[76:79], a[20:23]\n"
    "v_accvgpr_read_b32 v226, a34\n"
    "v_accvgpr_read_b32 v227, a50\n"
    "v_mfma_f32_16x16x32_bf16 a[20:23], v[16:19], v[80:83], a[20:23]\n"
    "v_and_b32 v204, 0x70, v204\n"
    "v_accvgpr_read_b32 v220, a1\n"
    "v_mfma_f32_16x16x32_bf16 a[4:7], v[8:11], v[80:83], a[4:7]\n"
    "v_accvgpr_read_b32 v221, a17\n"
    "v_accvgpr_read_b32 v222, a33\n"
    "v_mfma_f32_16x16x32_bf16 a[4:7], v[4:7], v[76:79], a[4:7]\n"
    "v_accvgpr_read_b32 v223, a49\n"
    "v_accvgpr_read_b32 v228, a3\n"
    "v_mfma_f32_16x16x32_bf16 v[60:63], v[180:183], a[240:243], v[104:107]\n"
    "v_accvgpr_read_b32 v229, a19\n"
    "v_accvgpr_read_b32 v230, a35\n"
    "v_mfma_f32_16x16x32_bf16 v[60:63], v[184:187], a[244:247], v[60:63]\n"
    "v_accvgpr_read_b32 v231, a51\n"
    "v_mad_u32_u24 v205, v204, 24, v207\n"
    "v_mfma_f32_16x16x32_bf16 v[60:63], v[188:191], a[248:251], v[60:63]\n"
    "v_xor_b32 v207, 64, v206\n"
    "v_xor_b32 v208, v204, v1\n"
    "v_mfma_f32_16x16x32_bf16 v[60:63], v[192:195], a[252:255], v[60:63]\n"
    "v_add_u32 v208, s72, v208\n"
    "v_add_f32 v112, v112, v113\n"
    "v_mov_b32 v113, v112\n"
    "v_mov_b32 v115, v114\n"
    "v_accvgpr_read_b32 v36, a4\n"
    "v_pk_fma_f32 v[216:217], v[216:217], v[108:109], v[110:111] op_sel:[0,1,1] op_sel_hi:[1,1,1]\n"
    "v_pk_fma_f32 v[224:225], v[224:225], v[108:109], v[110:111] op_sel:[0,1,1] op_sel_hi:[1,1,1]\n"
    "v_pk_fma_f32 v[218:219], v[218:219], v[108:109], v[110:111] op_sel:[0,1,1] op_sel_hi:[1,1,1]\n"
    "v_cvt_pk_bf16_f32 v216, v216, v217\n"
    "v_pk_fma_f32 v[226:227], v[226:227], v[108:109], v[110:111] op_sel:[0,1,1] op_sel_hi:[1,1,1]\n"
    "v_cvt_pk_bf16_f32 v224, v224, v225\n"
    "v_cvt_pk_bf16_f32 v217, v218, v219\n"
    "v_pk_fma_f32 v[220:221], v[220:221], v[108:109], v[110:111] op_sel:[0,1,1] op_sel_hi:[1,1,1]\n"
    "v_pk_fma_f32 v[222:223], v[222:223], v[108:109], v[110:111] op_sel:[0,1,1] op_sel_hi:[1,1,1]\n"
    "v_cvt_pk_bf16_f32 v225, v226, v227\n"
    "v_pk_fma_f32 v[228:229], v[228:229], v[108:109], v[110:111] op_sel:[0,1,1] op_sel_hi:[1,1,1]\n"
    "v_pk_fma_f32 v[230:231], v[230:231], v[108:109], v[110:111] op_sel:[0,1,1] op_sel_hi:[1,1,1]\n"
    "v_cvt_pk_bf16_f32 v218, v220, v221\n"
    "v_cvt_pk_bf16_f32 v219, v222, v223\n"
    "v_cvt_pk_bf16_f32 v226, v228, v229\n"
    "v_cvt_pk_bf16_f32 v227, v230, v231\n"
    "v_mfma_f32_16x16x32_bf16 v[60:63], v[196:199], v[124:127], v[60:63]\n"
    "ds_write_b128 v206, v[216:219] offset:0\n"
    "ds_write_b128 v207, v[224:227] offset:0\n"
    "ds_read_b128 v[220:223], v208 offset:0\n"
    "v_mfma_f32_16x16x32_bf16 v[60:63], v[200:203], v[128:131], v[60:63]\n"
    "ds_read_b128 v[228:231], v208 offset:1024\n"
    "v_permlane16_swap_b32 v112, v113\n"
    "v_mfma_f32_16x16x32_bf16 v[48:51], v[176:179], v[152:155], v[100:103]\n"
    "v_add_f32 v112, v112, v113\n"
    "v_rcp_f32 v113, v112\n"
    "v_mfma_f32_16x16x32_bf16 v[48:51], v[172:175], v[148:151], v[48:51]\n"
    "v_permlane16_swap_b32 v114, v115\n"
    "v_add_f32 v114, v114, v115\n"
    "v_mfma_f32_16x16x32_bf16 v[48:51], v[168:171], v[144:147], v[48:51]\n"
    "v_mul_f32 v115, -v114, v113\n"
    "v_accvgpr_read_b32 v37, a20\n"
    "v_mfma_f32_16x16x32_bf16 v[48:51], v[164:167], v[140:143], v[48:51]\n"
    "v_accvgpr_read_b32 v52, a6\n"
    "v_accvgpr_read_b32 v53, a22\n"
    "v_mfma_f32_16x16x32_bf16 v[48:51], v[160:163], v[136:139], v[48:51]\n"
    "v_accvgpr_read_b32 v38, a36\n"
    "v_accvgpr_read_b32 v39, a52\n"
    "v_mfma_f32_16x16x32_bf16 v[48:51], v[156:159], v[132:135], v[48:51]\n"
    "s_waitcnt lgkmcnt(1)\n"
    "v_accvgpr_read_b32 v54, a38\n"
    "v_accvgpr_read_b32 v55, a54\n"
    "v_mfma_f32_16x16x32_bf16 v[64:67], v[200:203], v[152:155], v[104:107]\n"
    "s_waitcnt lgkmcnt(0)\n"
    "buffer_store_dwordx4 v[220:223], v205, s[24:27], s75 offen offset:0 sc1\n"
    "v_mfma_f32_16x16x32_bf16 v[64:67], v[196:199], v[148:151], v[64:67]\n"
    "v_accvgpr_read_b32 v40, a5\n"
    "v_accvgpr_read_b32 v41, a21\n"
    "v_mfma_f32_16x16x32_bf16 v[64:67], v[192:195], v[144:147], v[64:67]\n"
    "v_accvgpr_read_b32 v42, a37\n"
    "v_accvgpr_read_b32 v43, a53\n"
    "v_mfma_f32_16x16x32_bf16 v[64:67], v[188:191], v[140:143], v[64:67]\n"
    "v_accvgpr_read_b32 v56, a7\n"
    "v_accvgpr_read_b32 v57, a23\n"
    "v_accvgpr_read_b32 v58, a39\n"
    "v_accvgpr_read_b32 v59, a55\n"
    "v_exp_f32 v44, v44\n"
    "v_pk_fma_f32 v[36:37], v[36:37], v[112:113], v[114:115] op_sel:[0,1,1] op_sel_hi:[1,1,1]\n"
    "v_pk_fma_f32 v[52:53], v[52:53], v[112:113], v[114:115] op_sel:[0,1,1] op_sel_hi:[1,1,1]\n"
    "v_pk_fma_f32 v[38:39], v[38:39], v[112:113], v[114:115] op_sel:[0,1,1] op_sel_hi:[1,1,1]\n"
    "v_cvt_pk_bf16_f32 v36, v36, v37\n"
    "v_pk_fma_f32 v[54:55], v[54:55], v[112:113], v[114:115] op_sel:[0,1,1] op_sel_hi:[1,1,1]\n"
    "v_cvt_pk_bf16_f32 v52, v52, v53\n"
    "v_cvt_pk_bf16_f32 v37, v38, v39\n"
    "v_mfma_f32_16x16x32_bf16 v[64:67], v[184:187], v[136:139], v[64:67]\n"
    "v_cvt_pk_bf16_f32 v53, v54, v55\n"
    "v_exp_f32 v45, v45\n"
    "v_exp_f32 v46, v46\n"
    "v_pk_fma_f32 v[40:41], v[40:41], v[112:113], v[114:115] op_sel:[0,1,1] op_sel_hi:[1,1,1]\n"
    "v_pk_fma_f32 v[42:43], v[42:43], v[112:113], v[114:115] op_sel:[0,1,1] op_sel_hi:[1,1,1]\n"
    "v_pk_fma_f32 v[56:57], v[56:57], v[112:113], v[114:115] op_sel:[0,1,1] op_sel_hi:[1,1,1]\n"
    "v_pk_fma_f32 v[58:59], v[58:59], v[112:113], v[114:115] op_sel:[0,1,1] op_sel_hi:[1,1,1]\n"
    "v_cvt_pk_bf16_f32 v38, v40, v41\n"
    "v_cvt_pk_bf16_f32 v39, v42, v43\n"
    "v_cvt_pk_bf16_f32 v54, v56, v57\n"
    "v_cvt_pk_bf16_f32 v55, v58, v59\n"
    "v_exp_f32 v47, v47\n"
    "v_exp_f32 v60, v60\n"
    "v_exp_f32 v61, v61\n"
    "v_exp_f32 v62, v62\n"
    "v_exp_f32 v63, v63\n"
    "ds_write_b128 v206, v[36:39] offset:2048\n"
    "ds_write_b128 v207, v[52:55] offset:2048\n"
    "v_mfma_f32_16x16x32_bf16 v[64:67], v[180:183], v[132:135], v[64:67]\n"
    "ds_read_b128 v[40:43], v208 offset:2048\n"
    "v_cvt_pk_bf16_f32 v88, v44, v45\n"
    "v_cvt_pk_bf16_f32 v89, v46, v47\n"
    "v_cvt_pk_bf16_f32 v90, v60, v61\n"
    "v_cvt_pk_bf16_f32 v91, v62, v63\n"
    "buffer_store_dwordx4 v[228:231], v205, s[24:27], s75 offen offset:3072 sc1\n"
    "ds_read_b128 v[56:59], v208 offset:3072\n"
    "v_smfmac_f32_16x16x64_bf16 v[116:119], v[244:247], v[84:91], v253\n"
    "v_exp_f32 v48, v48\n"
    "v_mfma_f32_16x16x32_bf16 a[8:11], v[4:7], v[84:87], a[8:11]\n"
    "v_exp_f32 v49, v49\n"
    "v_mfma_f32_16x16x32_bf16 a[8:11], v[8:11], v[88:91], a[8:11]\n"
    "v_exp_f32 v50, v50\n"
    "v_mfma_f32_16x16x32_bf16 a[24:27], v[16:19], v[88:91], a[24:27]\n"
    "v_exp_f32 v51, v51\n"
    "v_mfma_f32_16x16x32_bf16 a[24:27], v[12:15], v[84:87], a[24:27]\n"
    "s_waitcnt lgkmcnt(1)\n"
    "buffer_store_dwordx4 v[40:43], v205, s[24:27], s76 offen offset:0 sc1\n"
    "v_mfma_f32_16x16x32_bf16 a[40:43], v[20:23], v[84:87], a[40:43]\n"
    "v_add_f32 v116, v116, v117\n"
    "v_mov_b32 v117, v116\n"
    "v_mfma_f32_16x16x32_bf16 a[40:43], v[24:27], v[88:91], a[40:43]\n"
    "s_waitcnt lgkmcnt(0)\n"
    "v_mov_b32 v119, v118\n"
    "v_permlane16_swap_b32 v116, v117\n"
    "v_mfma_f32_16x16x32_bf16 a[56:59], v[32:35], v[88:91], a[56:59]\n"
    "v_add_f32 v116, v116, v117\n"
    "v_rcp_f32 v117, v116\n"
    "v_mfma_f32_16x16x32_bf16 a[56:59], v[28:31], v[84:87], a[56:59]\n"
    "v_exp_f32 v64, v64\n"
    "v_exp_f32 v65, v65\n"
    "v_exp_f32 v66, v66\n"
    "v_exp_f32 v67, v67\n"
    "v_permlane16_swap_b32 v118, v119\n"
    "v_add_f32 v118, v118, v119\n"
    "v_mul_f32 v119, -v118, v117\n"
    "v_accvgpr_read_b32 v44, a8\n"
    "v_accvgpr_read_b32 v45, a24\n"
    "v_accvgpr_read_b32 v60, a10\n"
    "v_accvgpr_read_b32 v61, a26\n"
    "v_cvt_pk_bf16_f32 v96, v48, v49\n"
    "v_cvt_pk_bf16_f32 v97, v50, v51\n"
    "v_cvt_pk_bf16_f32 v98, v64, v65\n"
    "v_cvt_pk_bf16_f32 v99, v66, v67\n"
    "v_accvgpr_read_b32 v46, a40\n"
    "v_accvgpr_read_b32 v47, a56\n"
    "v_pk_fma_f32 v[44:45], v[44:45], v[116:117], v[118:119] op_sel:[0,1,1] op_sel_hi:[1,1,1]\n"
    "v_accvgpr_read_b32 v62, a42\n"
    "v_accvgpr_read_b32 v63, a58\n"
    "v_pk_fma_f32 v[60:61], v[60:61], v[116:117], v[118:119] op_sel:[0,1,1] op_sel_hi:[1,1,1]\n"
    "v_pk_fma_f32 v[46:47], v[46:47], v[116:117], v[118:119] op_sel:[0,1,1] op_sel_hi:[1,1,1]\n"
    "v_cvt_pk_bf16_f32 v44, v44, v45\n"
    "v_accvgpr_read_b32 v48, a9\n"
    "v_accvgpr_read_b32 v49, a25\n"
    "v_accvgpr_read_b32 v50, a41\n"
    "v_accvgpr_read_b32 v51, a57\n"
    "v_pk_fma_f32 v[62:63], v[62:63], v[116:117], v[118:119] op_sel:[0,1,1] op_sel_hi:[1,1,1]\n"
    "v_cvt_pk_bf16_f32 v60, v60, v61\n"
    "v_accvgpr_read_b32 v64, a11\n"
    "v_accvgpr_read_b32 v65, a27\n"
    "v_accvgpr_read_b32 v66, a43\n"
    "v_accvgpr_read_b32 v67, a59\n"
    "v_cvt_pk_bf16_f32 v45, v46, v47\n"
    "v_pk_fma_f32 v[48:49], v[48:49], v[116:117], v[118:119] op_sel:[0,1,1] op_sel_hi:[1,1,1]\n"
    "v_pk_fma_f32 v[50:51], v[50:51], v[116:117], v[118:119] op_sel:[0,1,1] op_sel_hi:[1,1,1]\n"
    "v_cvt_pk_bf16_f32 v61, v62, v63\n"
    "v_pk_fma_f32 v[64:65], v[64:65], v[116:117], v[118:119] op_sel:[0,1,1] op_sel_hi:[1,1,1]\n"
    "v_pk_fma_f32 v[66:67], v[66:67], v[116:117], v[118:119] op_sel:[0,1,1] op_sel_hi:[1,1,1]\n"
    "v_cvt_pk_bf16_f32 v46, v48, v49\n"
    "v_cvt_pk_bf16_f32 v47, v50, v51\n"
    "v_cvt_pk_bf16_f32 v62, v64, v65\n"
    "v_cvt_pk_bf16_f32 v63, v66, v67\n"
    "ds_write_b128 v206, v[44:47] offset:4096\n"
    "ds_write_b128 v207, v[60:63] offset:4096\n"
    "ds_read_b128 v[48:51], v208 offset:4096\n"
    "v_smfmac_f32_16x16x64_bf16 v[120:123], v[244:247], v[92:99], v253\n"
    "buffer_store_dwordx4 v[56:59], v205, s[24:27], s76 offen offset:3072 sc1\n"
    "v_mfma_f32_16x16x32_bf16 a[60:63], v[28:31], v[92:95], a[60:63]\n"
    "ds_read_b128 v[64:67], v208 offset:5120\n"
    "ds_read_b128 v[156:159], v248 offset:4096\n"
    "v_mfma_f32_16x16x32_bf16 a[60:63], v[32:35], v[96:99], a[60:63]\n"
    "ds_read_b128 v[160:163], v248 offset:16384\n"
    "ds_read_b128 v[168:171], v248 offset:17408\n"
    "v_mfma_f32_16x16x32_bf16 a[44:47], v[24:27], v[96:99], a[44:47]\n"
    "v_add_f32 v120, v120, v121\n"
    "v_mov_b32 v121, v120\n"
    "v_mfma_f32_16x16x32_bf16 a[44:47], v[20:23], v[92:95], a[44:47]\n"
    "v_mov_b32 v123, v122\n"
    "v_permlane16_swap_b32 v120, v121\n"
    "v_mfma_f32_16x16x32_bf16 a[28:31], v[12:15], v[92:95], a[28:31]\n"
    "ds_read_b128 v[164:167], v248 offset:5120\n"
    "v_add_f32 v120, v120, v121\n"
    "v_mfma_f32_16x16x32_bf16 a[28:31], v[16:19], v[96:99], a[28:31]\n"
    "s_waitcnt lgkmcnt(5)\n"
    "buffer_store_dwordx4 v[48:51], v205, s[24:27], s77 offen offset:0 sc1\n"
    "v_mfma_f32_16x16x32_bf16 a[12:15], v[8:11], v[96:99], a[12:15]\n"
    "v_rcp_f32 v121, v120\n"
    "v_mfma_f32_16x16x32_bf16 a[12:15], v[4:7], v[92:95], a[12:15]\n"
    "s_waitcnt lgkmcnt(3)\n"
    "v_permlane16_swap_b32 v122, v123\n"
    "v_add_f32 v122, v122, v123\n"
    "ds_read_b128 v[172:175], v248 offset:6144\n"
    "v_mul_f32 v123, -v122, v121\n"
    "v_accvgpr_read_b32 v217, a28\n"
    "v_accvgpr_read_b32 v225, a30\n"
    "v_accvgpr_read_b32 v218, a44\n"
    "v_accvgpr_read_b32 v216, a12\n"
    "v_accvgpr_read_b32 v224, a14\n"
    "v_accvgpr_read_b32 v219, a60\n"
    "v_pk_fma_f32 v[216:217], v[216:217], v[120:121], v[122:123] op_sel:[0,1,1] op_sel_hi:[1,1,1]\n"
    "v_accvgpr_read_b32 v226, a46\n"
    "v_accvgpr_read_b32 v227, a62\n"
    "v_pk_fma_f32 v[224:225], v[224:225], v[120:121], v[122:123] op_sel:[0,1,1] op_sel_hi:[1,1,1]\n"
    "v_pk_fma_f32 v[218:219], v[218:219], v[120:121], v[122:123] op_sel:[0,1,1] op_sel_hi:[1,1,1]\n"
    "v_cvt_pk_bf16_f32 v216, v216, v217\n"
    "v_accvgpr_read_b32 v220, a13\n"
    "v_accvgpr_read_b32 v221, a29\n"
    "v_accvgpr_read_b32 v222, a45\n"
    "v_accvgpr_read_b32 v223, a61\n"
    "v_pk_fma_f32 v[226:227], v[226:227], v[120:121], v[122:123] op_sel:[0,1,1] op_sel_hi:[1,1,1]\n"
    "v_cvt_pk_bf16_f32 v224, v224, v225\n"
    "v_accvgpr_read_b32 v228, a15\n"
    "v_accvgpr_read_b32 v229, a31\n"
    "v_accvgpr_read_b32 v230, a47\n"
    "v_accvgpr_read_b32 v231, a63\n"
    "v_cvt_pk_bf16_f32 v217, v218, v219\n"
    "v_pk_fma_f32 v[220:221], v[220:221], v[120:121], v[122:123] op_sel:[0,1,1] op_sel_hi:[1,1,1]\n"
    "v_pk_fma_f32 v[222:223], v[222:223], v[120:121], v[122:123] op_sel:[0,1,1] op_sel_hi:[1,1,1]\n"
    "v_cvt_pk_bf16_f32 v225, v226, v227\n"
    "v_pk_fma_f32 v[228:229], v[228:229], v[120:121], v[122:123] op_sel:[0,1,1] op_sel_hi:[1,1,1]\n"
    "v_pk_fma_f32 v[230:231], v[230:231], v[120:121], v[122:123] op_sel:[0,1,1] op_sel_hi:[1,1,1]\n"
    "ds_read_b128 v[176:179], v248 offset:18432\n"
    "v_cvt_pk_bf16_f32 v218, v220, v221\n"
    "v_cvt_pk_bf16_f32 v219, v222, v223\n"
    "v_cvt_pk_bf16_f32 v226, v228, v229\n"
    "v_cvt_pk_bf16_f32 v227, v230, v231\n"
    "ds_write_b128 v206, v[216:219] offset:0\n"
    "ds_write_b128 v207, v[224:227] offset:0\n"
    "ds_read_b128 v[220:223], v208 offset:0\n"
    "ds_read_b128 v[184:187], v248 offset:19456\n"
    "ds_read_b128 v[180:183], v248 offset:7168\n"
    "buffer_store_dwordx4 v[64:67], v205, s[24:27], s77 offen offset:3072 sc1\n"
    "v_mfma_f32_16x16x32_bf16 a[64:67], v[156:159], v[68:71], a[64:67]\n"
    "s_waitcnt lgkmcnt(6)\n"
    "ds_read_b128 v[228:231], v208 offset:1024\n"
    "ds_read_b128 v[188:191], v248 offset:8192\n"
    "v_mfma_f32_16x16x32_bf16 a[64:67], v[160:163], v[72:75], a[64:67]\n"
    "ds_read_b128 v[192:195], v248 offset:20480\n"
    "ds_read_b128 v[200:203], v248 offset:21504\n"
    "v_mfma_f32_16x16x32_bf16 a[80:83], v[168:171], v[72:75], a[80:83]\n"
    "ds_read_b128 v[196:199], v248 offset:9216\n"
    "ds_read_b128 v[124:127], v248 offset:10240\n"
    "v_mfma_f32_16x16x32_bf16 a[80:83], v[164:167], v[68:71], a[80:83]\n"
    "s_waitcnt lgkmcnt(11)\n"
    "ds_read_b128 v[128:131], v248 offset:22528\n"
    "v_accvgpr_read_b32 v36, a64\n"
    "v_mfma_f32_16x16x32_bf16 a[96:99], v[172:175], v[68:71], a[96:99]\n"
    "v_accvgpr_read_b32 v52, a66\n"
    "v_accvgpr_read_b32 v40, a65\n"
    "v_mfma_f32_16x16x32_bf16 a[96:99], v[176:179], v[72:75], a[96:99]\n"
    "s_waitcnt lgkmcnt(7)\n"
    "buffer_store_dwordx4 v[220:223], v205, s[24:27], s78 offen offset:0 sc1\n"
    "v_mfma_f32_16x16x32_bf16 a[112:115], v[184:187], v[72:75], a[112:115]\n"
    "v_accvgpr_read_b32 v37, a80\n"
    "v_accvgpr_read_b32 v53, a82\n"
    "v_mfma_f32_16x16x32_bf16 a[112:115], v[180:183], v[68:71], a[112:115]\n"
    "s_waitcnt lgkmcnt(5)\n"
    "v_accvgpr_read_b32 v41, a81\n"
    "v_accvgpr_read_b32 v38, a96\n"
    "v_accvgpr_read_b32 v54, a98\n"
    "v_accvgpr_read_b32 v42, a97\n"
    "v_accvgpr_read_b32 v56, a67\n"
    "v_pk_fma_f32 v[36:37], v[36:37], v[108:109], v[110:111] op_sel:[0,1,1] op_sel_hi:[1,1,1]\n"
    "v_pk_fma_f32 v[52:53], v[52:53], v[108:109], v[110:111] op_sel:[0,1,1] op_sel_hi:[1,1,1]\n"
    "v_accvgpr_read_b32 v39, a112\n"
    "v_accvgpr_read_b32 v55, a114\n"
    "v_pk_fma_f32 v[38:39], v[38:39], v[108:109], v[110:111] op_sel:[0,1,1] op_sel_hi:[1,1,1]\n"
    "v_cvt_pk_bf16_f32 v36, v36, v37\n"
    "v_accvgpr_read_b32 v43, a113\n"
    "v_pk_fma_f32 v[54:55], v[54:55], v[108:109], v[110:111] op_sel:[0,1,1] op_sel_hi:[1,1,1]\n"
    "v_cvt_pk_bf16_f32 v52, v52, v53\n"
    "v_accvgpr_read_b32 v57, a83\n"
    "v_accvgpr_read_b32 v58, a99\n"
    "v_accvgpr_read_b32 v59, a115\n"
    "v_cvt_pk_bf16_f32 v37, v38, v39\n"
    "v_pk_fma_f32 v[40:41], v[40:41], v[108:109], v[110:111] op_sel:[0,1,1] op_sel_hi:[1,1,1]\n"
    "v_pk_fma_f32 v[42:43], v[42:43], v[108:109], v[110:111] op_sel:[0,1,1] op_sel_hi:[1,1,1]\n"
    "v_cvt_pk_bf16_f32 v53, v54, v55\n"
    "v_pk_fma_f32 v[56:57], v[56:57], v[108:109], v[110:111] op_sel:[0,1,1] op_sel_hi:[1,1,1]\n"
    "v_pk_fma_f32 v[58:59], v[58:59], v[108:109], v[110:111] op_sel:[0,1,1] op_sel_hi:[1,1,1]\n"
    "v_cvt_pk_bf16_f32 v38, v40, v41\n"
    "v_cvt_pk_bf16_f32 v39, v42, v43\n"
    "v_cvt_pk_bf16_f32 v54, v56, v57\n"
    "v_cvt_pk_bf16_f32 v55, v58, v59\n"
    "ds_write_b128 v206, v[36:39] offset:2048\n"
    "ds_write_b128 v207, v[52:55] offset:2048\n"
    "ds_read_b128 v[40:43], v208 offset:2048\n"
    "buffer_store_dwordx4 v[228:231], v205, s[24:27], s78 offen offset:3072 sc1\n"
    "v_mfma_f32_16x16x32_bf16 a[116:119], v[180:183], v[76:79], a[116:119]\n"
    "s_waitcnt lgkmcnt(3)\n"
    "ds_read_b128 v[56:59], v208 offset:3072\n"
    "ds_read_b128 v[136:139], v248 offset:23552\n"
    "v_mfma_f32_16x16x32_bf16 a[116:119], v[184:187], v[80:83], a[116:119]\n"
    "ds_read_b128 v[132:135], v248 offset:11264\n"
    "v_mov_b32 v212, 0\n"
    "v_mfma_f32_16x16x32_bf16 a[100:103], v[176:179], v[80:83], a[100:103]\n"
    "v_fma_f32 v212, |v110|, 4.0, v212\n"
    "v_fma_f32 v212, v108, s74, v212\n"
    "v_mfma_f32_16x16x32_bf16 a[100:103], v[172:175], v[76:79], a[100:103]\n"
    "v_fma_f32 v212, v109, s74, v212\n"
    "v_fma_f32 v212, |v114|, 4.0, v212\n"
    "v_mfma_f32_16x16x32_bf16 a[84:87], v[164:167], v[76:79], a[84:87]\n"
    "v_accvgpr_read_b32 v47, a116\n"
    "v_accvgpr_read_b32 v63, a118\n"
    "v_mfma_f32_16x16x32_bf16 a[84:87], v[168:171], v[80:83], a[84:87]\n"
    "s_waitcnt lgkmcnt(3)\n"
    "v_accvgpr_read_b32 v51, a117\n"
    "v_accvgpr_read_b32 v46, a100\n"
    "buffer_store_dwordx4 v[40:43], v205, s[24:27], s75 offen offset:128 sc1\n"
    "v_mfma_f32_16x16x32_bf16 a[68:71], v[160:163], v[80:83], a[68:71]\n"
    "s_waitcnt lgkmcnt(1)\n"
    "v_accvgpr_read_b32 v62, a102\n"
    "v_accvgpr_read_b32 v50, a101\n"
    "v_mfma_f32_16x16x32_bf16 a[68:71], v[156:159], v[76:79], a[68:71]\n"
    "s_waitcnt lgkmcnt(0)\n"
    "v_accvgpr_read_b32 v45, a84\n"
    "v_accvgpr_read_b32 v61, a86\n"
    "v_accvgpr_read_b32 v49, a85\n"
    "v_accvgpr_read_b32 v65, a87\n"
    "v_accvgpr_read_b32 v66, a103\n"
    "v_pk_fma_f32 v[46:47], v[46:47], v[112:113], v[114:115] op_sel:[0,1,1] op_sel_hi:[1,1,1]\n"
    "v_pk_fma_f32 v[62:63], v[62:63], v[112:113], v[114:115] op_sel:[0,1,1] op_sel_hi:[1,1,1]\n"
    "v_accvgpr_read_b32 v44, a68\n"
    "v_accvgpr_read_b32 v60, a70\n"
    "v_pk_fma_f32 v[44:45], v[44:45], v[112:113], v[114:115] op_sel:[0,1,1] op_sel_hi:[1,1,1]\n"
    "v_pk_fma_f32 v[60:61], v[60:61], v[112:113], v[114:115] op_sel:[0,1,1] op_sel_hi:[1,1,1]\n"
    "v_cvt_pk_bf16_f32 v44, v44, v45\n"
    "v_accvgpr_read_b32 v48, a69\n"
    "v_cvt_pk_bf16_f32 v60, v60, v61\n"
    "v_accvgpr_read_b32 v64, a71\n"
    "v_accvgpr_read_b32 v67, a119\n"
    "v_cvt_pk_bf16_f32 v45, v46, v47\n"
    "v_pk_fma_f32 v[48:49], v[48:49], v[112:113], v[114:115] op_sel:[0,1,1] op_sel_hi:[1,1,1]\n"
    "v_pk_fma_f32 v[50:51], v[50:51], v[112:113], v[114:115] op_sel:[0,1,1] op_sel_hi:[1,1,1]\n"
    "v_cvt_pk_bf16_f32 v61, v62, v63\n"
    "v_pk_fma_f32 v[64:65], v[64:65], v[112:113], v[114:115] op_sel:[0,1,1] op_sel_hi:[1,1,1]\n"
    "v_pk_fma_f32 v[66:67], v[66:67], v[112:113], v[114:115] op_sel:[0,1,1] op_sel_hi:[1,1,1]\n"
    "v_cvt_pk_bf16_f32 v46, v48, v49\n"
    "v_cvt_pk_bf16_f32 v47, v50, v51\n"
    "v_cvt_pk_bf16_f32 v62, v64, v65\n"
    "v_cvt_pk_bf16_f32 v63, v66, v67\n"
    "ds_write_b128 v206, v[44:47] offset:4096\n"
    "ds_write_b128 v207, v[60:63] offset:4096\n"
    "ds_read_b128 v[48:51], v208 offset:4096\n"
    "buffer_store_dwordx4 v[56:59], v205, s[24:27], s75 offen offset:3200 sc1\n"
    "v_mfma_f32_16x16x32_bf16 a[72:75], v[156:159], v[84:87], a[72:75]\n"
    "ds_read_b128 v[64:67], v208 offset:5120\n"
    "v_fma_f32 v212, v112, s74, v212\n"
    "v_mfma_f32_16x16x32_bf16 a[72:75], v[160:163], v[88:91], a[72:75]\n"
    "v_fma_f32 v212, v113, s74, v212\n"
    "v_fma_f32 v212, |v118|, 4.0, v212\n"
    "v_mfma_f32_16x16x32_bf16 a[88:91], v[168:171], v[88:91], a[88:91]\n"
    "v_fma_f32 v212, v116, s74, v212\n"
    "v_fma_f32 v212, v117, s74, v212\n"
    "v_mfma_f32_16x16x32_bf16 a[88:91], v[164:167], v[84:87], a[88:91]\n"
    "v_fma_f32 v212, |v122|, 4.0, v212\n"
    "v_fma_f32 v212, v120, s74, v212\n"
    "v_mfma_f32_16x16x32_bf16 a[104:107], v[172:175], v[84:87], a[104:107]\n"
    "v_accvgpr_read_b32 v216, a72\n"
    "v_accvgpr_read_b32 v224, a74\n"
    "v_mfma_f32_16x16x32_bf16 a[104:107], v[176:179], v[88:91], a[104:107]\n"
    "s_waitcnt lgkmcnt(1)\n"
    "v_accvgpr_read_b32 v220, a73\n"
    "v_accvgpr_read_b32 v217, a88\n"
    "buffer_store_dwordx4 v[48:51], v205, s[24:27], s76 offen offset:128 sc1\n"
    "v_mfma_f32_16x16x32_bf16 a[120:123], v[184:187], v[88:91], a[120:123]\n"
    "s_waitcnt lgkmcnt(0)\n"
    "v_accvgpr_read_b32 v225, a90\n"
    "v_accvgpr_read_b32 v221, a89\n"
    "v_mfma_f32_16x16x32_bf16 a[120:123], v[180:183], v[84:87], a[120:123]\n"
    "v_accvgpr_read_b32 v218, a104\n"
    "v_accvgpr_read_b32 v226, a106\n"
    "v_accvgpr_read_b32 v222, a105\n"
    "v_accvgpr_read_b32 v228, a75\n"
    "v_accvgpr_read_b32 v229, a91\n"
    "v_pk_fma_f32 v[216:217], v[216:217], v[116:117], v[118:119] op_sel:[0,1,1] op_sel_hi:[1,1,1]\n"
    "v_pk_fma_f32 v[224:225], v[224:225], v[116:117], v[118:119] op_sel:[0,1,1] op_sel_hi:[1,1,1]\n"
    "v_cvt_pk_bf16_f32 v216, v216, v217\n"
    "v_accvgpr_read_b32 v219, a120\n"
    "v_accvgpr_read_b32 v227, a122\n"
    "v_pk_fma_f32 v[218:219], v[218:219], v[116:117], v[118:119] op_sel:[0,1,1] op_sel_hi:[1,1,1]\n"
    "v_accvgpr_read_b32 v223, a121\n"
    "v_pk_fma_f32 v[226:227], v[226:227], v[116:117], v[118:119] op_sel:[0,1,1] op_sel_hi:[1,1,1]\n"
    "v_cvt_pk_bf16_f32 v224, v224, v225\n"
    "v_accvgpr_read_b32 v230, a107\n"
    "v_accvgpr_read_b32 v231, a123\n"
    "v_cvt_pk_bf16_f32 v217, v218, v219\n"
    "v_pk_fma_f32 v[220:221], v[220:221], v[116:117], v[118:119] op_sel:[0,1,1] op_sel_hi:[1,1,1]\n"
    "v_pk_fma_f32 v[222:223], v[222:223], v[116:117], v[118:119] op_sel:[0,1,1] op_sel_hi:[1,1,1]\n"
    "v_cvt_pk_bf16_f32 v225, v226, v227\n"
    "v_pk_fma_f32 v[228:229], v[228:229], v[116:117], v[118:119] op_sel:[0,1,1] op_sel_hi:[1,1,1]\n"
    "v_pk_fma_f32 v[230:231], v[230:231], v[116:117], v[118:119] op_sel:[0,1,1] op_sel_hi:[1,1,1]\n"
    "v_cvt_pk_bf16_f32 v218, v220, v221\n"
    "v_cvt_pk_bf16_f32 v219, v222, v223\n"
    "v_cvt_pk_bf16_f32 v226, v228, v229\n"
    "v_cvt_pk_bf16_f32 v227, v230, v231\n"
    "ds_write_b128 v206, v[216:219] offset:0\n"
    "ds_write_b128 v207, v[224:227] offset:0\n"
    "ds_read_b128 v[220:223], v208 offset:0\n"
    "buffer_store_dwordx4 v[64:67], v205, s[24:27], s76 offen offset:3200 sc1\n"
    "v_mfma_f32_16x16x32_bf16 a[124:127], v[180:183], v[92:95], a[124:127]\n"
    "ds_read_b128 v[228:231], v208 offset:1024\n"
    "v_fma_f32 v212, v121, s74, v212\n"
    "v_mfma_f32_16x16x32_bf16 a[124:127], v[184:187], v[96:99], a[124:127]\n"
    "v_cmp_nlt_f32 s[54:55], |v212|, s73\n"
    "v_lshlrev_b32 v209, 16, v1\n"
    "v_mfma_f32_16x16x32_bf16 a[108:111], v[176:179], v[96:99], a[108:111]\n"
    "s_or_b32 %0, s54, s55\n"
    "buffer_store_dwordx2 v[238:239], v209, s[36:39], s82 offen offset:0 sc1\n"
    "v_mfma_f32_16x16x32_bf16 a[108:111], v[172:175], v[92:95], a[108:111]\n"
    "v_mfma_f32_16x16x32_bf16 a[92:95], v[164:167], v[92:95], a[92:95]\n"
    "v_mfma_f32_16x16x32_bf16 a[92:95], v[168:171], v[96:99], a[92:95]\n"
    "s_waitcnt lgkmcnt(1)\n"
    "v_accvgpr_read_b32 v39, a124\n"
    "buffer_store_dwordx4 v[220:223], v205, s[24:27], s77 offen offset:128 sc1\n"
    "v_mfma_f32_16x16x32_bf16 a[76:79], v[160:163], v[96:99], a[76:79]\n"
    "s_waitcnt lgkmcnt(0)\n"
    "v_accvgpr_read_b32 v55, a126\n"
    "v_accvgpr_read_b32 v38, a108\n"
    "v_mfma_f32_16x16x32_bf16 a[76:79], v[156:159], v[92:95], a[76:79]\n"
    "s_nop 0\n"
    "v_accvgpr_read_b32 v37, a92\n"
    "v_accvgpr_read_b32 v53, a94\n"
    "v_accvgpr_read_b32 v54, a110\n"
    "v_accvgpr_read_b32 v41, a93\n"
    "v_accvgpr_read_b32 v42, a109\n"
    "v_pk_fma_f32 v[38:39], v[38:39], v[120:121], v[122:123] op_sel:[0,1,1] op_sel_hi:[1,1,1]\n"
    "v_accvgpr_read_b32 v43, a125\n"
    "v_accvgpr_read_b32 v36, a76\n"
    "v_accvgpr_read_b32 v52, a78\n"
    "v_pk_fma_f32 v[36:37], v[36:37], v[120:121], v[122:123] op_sel:[0,1,1] op_sel_hi:[1,1,1]\n"
    "v_pk_fma_f32 v[52:53], v[52:53], v[120:121], v[122:123] op_sel:[0,1,1] op_sel_hi:[1,1,1]\n"
    "v_cvt_pk_bf16_f32 v36, v36, v37\n"
    "v_accvgpr_read_b32 v40, a77\n"
    "v_pk_fma_f32 v[54:55], v[54:55], v[120:121], v[122:123] op_sel:[0,1,1] op_sel_hi:[1,1,1]\n"
    "v_cvt_pk_bf16_f32 v52, v52, v53\n"
    "v_accvgpr_read_b32 v56, a79\n"
    "v_accvgpr_read_b32 v57, a95\n"
    "v_accvgpr_read_b32 v58, a111\n"
    "v_accvgpr_read_b32 v59, a127\n"
    "v_cvt_pk_bf16_f32 v37, v38, v39\n"
    "v_pk_fma_f32 v[40:41], v[40:41], v[120:121], v[122:123] op_sel:[0,1,1] op_sel_hi:[1,1,1]\n"
    "v_pk_fma_f32 v[42:43], v[42:43], v[120:121], v[122:123] op_sel:[0,1,1] op_sel_hi:[1,1,1]\n"
    "v_cvt_pk_bf16_f32 v53, v54, v55\n"
    "v_pk_fma_f32 v[56:57], v[56:57], v[120:121], v[122:123] op_sel:[0,1,1] op_sel_hi:[1,1,1]\n"
    "v_pk_fma_f32 v[58:59], v[58:59], v[120:121], v[122:123] op_sel:[0,1,1] op_sel_hi:[1,1,1]\n"
    "v_cvt_pk_bf16_f32 v38, v40, v41\n"
    "v_cvt_pk_bf16_f32 v39, v42, v43\n"
    "v_cvt_pk_bf16_f32 v54, v56, v57\n"
    "v_cvt_pk_bf16_f32 v55, v58, v59\n"
    "ds_write_b128 v206, v[36:39] offset:2048\n"
    "ds_write_b128 v207, v[52:55] offset:2048\n"
    "ds_read_b128 v[40:43], v208 offset:2048\n"
    "buffer_store_dwordx4 v[228:231], v205, s[24:27], s77 offen offset:3200 sc1\n"
    "v_mfma_f32_16x16x32_bf16 a[128:131], v[188:191], v[68:71], a[128:131]\n"
    "ds_read_b128 v[56:59], v208 offset:3072\n"
    "v_mfma_f32_16x16x32_bf16 a[128:131], v[192:195], v[72:75], a[128:131]\n"
    "v_mfma_f32_16x16x32_bf16 a[144:147], v[200:203], v[72:75], a[144:147]\n"
    "v_mfma_f32_16x16x32_bf16 a[144:147], v[196:199], v[68:71], a[144:147]\n"
    "v_mfma_f32_16x16x32_bf16 a[160:163], v[124:127], v[68:71], a[160:163]\n"
    "v_mfma_f32_16x16x32_bf16 a[160:163], v[128:131], v[72:75], a[160:163]\n"
    "s_waitcnt lgkmcnt(1)\n"
    "buffer_store_dwordx4 v[40:43], v205, s[24:27], s78 offen offset:128 sc1\n"
    "v_mfma_f32_16x16x32_bf16 a[176:179], v[136:139], v[72:75], a[176:179]\n"
    "s_waitcnt lgkmcnt(0)\n"
    "v_accvgpr_read_b32 v44, a128\n"
    "v_accvgpr_read_b32 v60, a130\n"
    "v_mfma_f32_16x16x32_bf16 a[176:179], v[132:135], v[68:71], a[176:179]\n"
    "v_accvgpr_read_b32 v45, a144\n"
    "v_accvgpr_read_b32 v61, a146\n"
    "v_accvgpr_read_b32 v46, a160\n"
    "v_accvgpr_read_b32 v62, a162\n"
    "v_accvgpr_read_b32 v48, a129\n"
    "v_pk_fma_f32 v[44:45], v[44:45], v[108:109], v[110:111] op_sel:[0,1,1] op_sel_hi:[1,1,1]\n"
    "v_pk_fma_f32 v[60:61], v[60:61], v[108:109], v[110:111] op_sel:[0,1,1] op_sel_hi:[1,1,1]\n"
    "v_cvt_pk_bf16_f32 v44, v44, v45\n"
    "v_accvgpr_read_b32 v47, a176\n"
    "v_accvgpr_read_b32 v63, a178\n"
    "v_pk_fma_f32 v[46:47], v[46:47], v[108:109], v[110:111] op_sel:[0,1,1] op_sel_hi:[1,1,1]\n"
    "v_accvgpr_read_b32 v49, a145\n"
    "v_accvgpr_read_b32 v50, a161\n"
    "v_accvgpr_read_b32 v51, a177\n"
    "v_pk_fma_f32 v[62:63], v[62:63], v[108:109], v[110:111] op_sel:[0,1,1] op_sel_hi:[1,1,1]\n"
    "v_cvt_pk_bf16_f32 v60, v60, v61\n"
    "v_accvgpr_read_b32 v64, a131\n"
    "v_accvgpr_read_b32 v65, a147\n"
    "v_accvgpr_read_b32 v66, a163\n"
    "v_accvgpr_read_b32 v67, a179\n"
    "v_cvt_pk_bf16_f32 v45, v46, v47\n"
    "v_pk_fma_f32 v[48:49], v[48:49], v[108:109], v[110:111] op_sel:[0,1,1] op_sel_hi:[1,1,1]\n"
    "v_pk_fma_f32 v[50:51], v[50:51], v[108:109], v[110:111] op_sel:[0,1,1] op_sel_hi:[1,1,1]\n"
    "v_cvt_pk_bf16_f32 v61, v62, v63\n"
    "v_pk_fma_f32 v[64:65], v[64:65], v[108:109], v[110:111] op_sel:[0,1,1] op_sel_hi:[1,1,1]\n"
    "v_pk_fma_f32 v[66:67], v[66:67], v[108:109], v[110:111] op_sel:[0,1,1] op_sel_hi:[1,1,1]\n"
    "v_cvt_pk_bf16_f32 v46, v48, v49\n"
    "v_cvt_pk_bf16_f32 v47, v50, v51\n"
    "v_cvt_pk_bf16_f32 v62, v64, v65\n"
    "v_cvt_pk_bf16_f32 v63, v66, v67\n"
    "ds_write_b128 v206, v[44:47] offset:4096\n"
    "ds_write_b128 v207, v[60:63] offset:4096\n"
    "ds_read_b128 v[48:51], v208 offset:4096\n"
    "buffer_store_dwordx4 v[56:59], v205, s[24:27], s78 offen offset:3200 sc1\n"
    "v_mfma_f32_16x16x32_bf16 a[180:183], v[132:135], v[76:79], a[180:183]\n"
    "ds_read_b128 v[64:67], v208 offset:5120\n"
    "v_mfma_f32_16x16x32_bf16 a[180:183], v[136:139], v[80:83], a[180:183]\n"
    "v_mfma_f32_16x16x32_bf16 a[164:167], v[128:131], v[80:83], a[164:167]\n"
    "v_mfma_f32_16x16x32_bf16 a[164:167], v[124:127], v[76:79], a[164:167]\n"
    "v_mfma_f32_16x16x32_bf16 a[148:151], v[196:199], v[76:79], a[148:151]\n"
    "v_mfma_f32_16x16x32_bf16 a[148:151], v[200:203], v[80:83], a[148:151]\n"
    "s_waitcnt lgkmcnt(1)\n"
    "buffer_store_dwordx4 v[48:51], v205, s[24:27], s75 offen offset:256 sc1\n"
    "v_mfma_f32_16x16x32_bf16 a[132:135], v[192:195], v[80:83], a[132:135]\n"
    "s_waitcnt lgkmcnt(0)\n"
    "v_accvgpr_read_b32 v219, a180\n"
    "v_accvgpr_read_b32 v227, a182\n"
    "v_mfma_f32_16x16x32_bf16 a[132:135], v[188:191], v[76:79], a[132:135]\n"
    "s_nop 0\n"
    "v_accvgpr_read_b32 v217, a148\n"
    "v_accvgpr_read_b32 v225, a150\n"
    "v_accvgpr_read_b32 v218, a164\n"
    "v_accvgpr_read_b32 v226, a166\n"
    "v_accvgpr_read_b32 v221, a149\n"
    "v_pk_fma_f32 v[218:219], v[218:219], v[112:113], v[114:115] op_sel:[0,1,1] op_sel_hi:[1,1,1]\n"
    "v_accvgpr_read_b32 v222, a165\n"
    "v_accvgpr_read_b32 v216, a132\n"
    "v_accvgpr_read_b32 v224, a134\n"
    "v_pk_fma_f32 v[216:217], v[216:217], v[112:113], v[114:115] op_sel:[0,1,1] op_sel_hi:[1,1,1]\n"
    "v_pk_fma_f32 v[224:225], v[224:225], v[112:113], v[114:115] op_sel:[0,1,1] op_sel_hi:[1,1,1]\n"
    "v_cvt_pk_bf16_f32 v216, v216, v217\n"
    "v_accvgpr_read_b32 v220, a133\n"
    "v_accvgpr_read_b32 v223, a181\n"
    "v_pk_fma_f32 v[226:227], v[226:227], v[112:113], v[114:115] op_sel:[0,1,1] op_sel_hi:[1,1,1]\n"
    "v_cvt_pk_bf16_f32 v224, v224, v225\n"
    "v_accvgpr_read_b32 v228, a135\n"
    "v_accvgpr_read_b32 v229, a151\n"
    "v_accvgpr_read_b32 v230, a167\n"
    "v_accvgpr_read_b32 v231, a183\n"
    "v_cvt_pk_bf16_f32 v217, v218, v219\n"
    "v_pk_fma_f32 v[220:221], v[220:221], v[112:113], v[114:115] op_sel:[0,1,1] op_sel_hi:[1,1,1]\n"
    "v_pk_fma_f32 v[222:223], v[222:223], v[112:113], v[114:115] op_sel:[0,1,1] op_sel_hi:[1,1,1]\n"
    "v_cvt_pk_bf16_f32 v225, v226, v227\n"
    "v_pk_fma_f32 v[228:229], v[228:229], v[112:113], v[114:115] op_sel:[0,1,1] op_sel_hi:[1,1,1]\n"
    "v_pk_fma_f32 v[230:231], v[230:231], v[112:113], v[114:115] op_sel:[0,1,1] op_sel_hi:[1,1,1]\n"
    "v_cvt_pk_bf16_f32 v218, v220, v221\n"
    "v_cvt_pk_bf16_f32 v219, v222, v223\n"
    "v_cvt_pk_bf16_f32 v226, v228, v229\n"
    "v_cvt_pk_bf16_f32 v227, v230, v231\n"
    "ds_write_b128 v206, v[216:219] offset:0\n"
    "ds_write_b128 v207, v[224:227] offset:0\n"
    "ds_read_b128 v[220:223], v208 offset:0\n"
    "buffer_store_dwordx4 v[64:67], v205, s[24:27], s75 offen offset:3328 sc1\n"
    "v_mfma_f32_16x16x32_bf16 a[136:139], v[188:191], v[84:87], a[136:139]\n"
    "ds_read_b128 v[228:231], v208 offset:1024\n"
    "v_mfma_f32_16x16x32_bf16 a[136:139], v[192:195], v[88:91], a[136:139]\n"
    "v_mfma_f32_16x16x32_bf16 a[152:155], v[200:203], v[88:91], a[152:155]\n"
    "v_mfma_f32_16x16x32_bf16 a[152:155], v[196:199], v[84:87], a[152:155]\n"
    "v_mfma_f32_16x16x32_bf16 a[168:171], v[124:127], v[84:87], a[168:171]\n"
    "v_mfma_f32_16x16x32_bf16 a[168:171], v[128:131], v[88:91], a[168:171]\n"
    "s_waitcnt lgkmcnt(1)\n"
    "buffer_store_dwordx4 v[220:223], v205, s[24:27], s76 offen offset:256 sc1\n"
    "v_mfma_f32_16x16x32_bf16 a[184:187], v[136:139], v[88:91], a[184:187]\n"
    "s_waitcnt lgkmcnt(0)\n"
    "v_accvgpr_read_b32 v36, a136\n"
    "v_accvgpr_read_b32 v52, a138\n"
    "v_mfma_f32_16x16x32_bf16 a[184:187], v[132:135], v[84:87], a[184:187]\n"
    "v_accvgpr_read_b32 v37, a152\n"
    "v_accvgpr_read_b32 v53, a154\n"
    "v_accvgpr_read_b32 v38, a168\n"
    "v_accvgpr_read_b32 v54, a170\n"
    "v_accvgpr_read_b32 v40, a137\n"
    "v_pk_fma_f32 v[36:37], v[36:37], v[116:117], v[118:119] op_sel:[0,1,1] op_sel_hi:[1,1,1]\n"
    "v_pk_fma_f32 v[52:53], v[52:53], v[116:117], v[118:119] op_sel:[0,1,1] op_sel_hi:[1,1,1]\n"
    "v_cvt_pk_bf16_f32 v36, v36, v37\n"
    "v_accvgpr_read_b32 v39, a184\n"
    "v_accvgpr_read_b32 v55, a186\n"
    "v_pk_fma_f32 v[38:39], v[38:39], v[116:117], v[118:119] op_sel:[0,1,1] op_sel_hi:[1,1,1]\n"
    "v_accvgpr_read_b32 v41, a153\n"
    "v_accvgpr_read_b32 v42, a169\n"
    "v_accvgpr_read_b32 v43, a185\n"
    "v_pk_fma_f32 v[54:55], v[54:55], v[116:117], v[118:119] op_sel:[0,1,1] op_sel_hi:[1,1,1]\n"
    "v_cvt_pk_bf16_f32 v52, v52, v53\n"
    "v_accvgpr_read_b32 v56, a139\n"
    "v_accvgpr_read_b32 v57, a155\n"
    "v_accvgpr_read_b32 v58, a171\n"
    "v_accvgpr_read_b32 v59, a187\n"
    "v_cvt_pk_bf16_f32 v37, v38, v39\n"
    "v_pk_fma_f32 v[40:41], v[40:41], v[116:117], v[118:119] op_sel:[0,1,1] op_sel_hi:[1,1,1]\n"
    "v_pk_fma_f32 v[42:43], v[42:43], v[116:117], v[118:119] op_sel:[0,1,1] op_sel_hi:[1,1,1]\n"
    "v_cvt_pk_bf16_f32 v53, v54, v55\n"
    "v_pk_fma_f32 v[56:57], v[56:57], v[116:117], v[118:119] op_sel:[0,1,1] op_sel_hi:[1,1,1]\n"
    "v_pk_fma_f32 v[58:59], v[58:59], v[116:117], v[118:119] op_sel:[0,1,1] op_sel_hi:[1,1,1]\n"
    "v_cvt_pk_bf16_f32 v38, v40, v41\n"
    "v_cvt_pk_bf16_f32 v39, v42, v43\n"
    "v_cvt_pk_bf16_f32 v54, v56, v57\n"
    "v_cvt_pk_bf16_f32 v55, v58, v59\n"
    "ds_write_b128 v206, v[36:39] offset:2048\n"
    "ds_write_b128 v207, v[52:55] offset:2048\n"
    "ds_read_b128 v[40:43], v208 offset:2048\n"
    "buffer_store_dwordx4 v[228:231], v205, s[24:27], s76 offen offset:3328 sc1\n"
    "v_mfma_f32_16x16x32_bf16 a[188:191], v[132:135], v[92:95], a[188:191]\n"
    "ds_read_b128 v[56:59], v208 offset:3072\n"
    "v_mfma_f32_16x16x32_bf16 a[188:191], v[136:139], v[96:99], a[188:191]\n"
    "v_mfma_f32_16x16x32_bf16 a[172:175], v[128:131], v[96:99], a[172:175]\n"
    "v_mfma_f32_16x16x32_bf16 a[172:175], v[124:127], v[92:95], a[172:175]\n"
    "v_mfma_f32_16x16x32_bf16 a[156:159], v[196:199], v[92:95], a[156:159]\n"
    "v_mfma_f32_16x16x32_bf16 a[156:159], v[200:203], v[96:99], a[156:159]\n"
    "s_waitcnt lgkmcnt(1)\n"
    "buffer_store_dwordx4 v[40:43], v205, s[24:27], s77 offen offset:256 sc1\n"
    "v_mfma_f32_16x16x32_bf16 a[140:143], v[192:195], v[96:99], a[140:143]\n"
    "s_waitcnt lgkmcnt(0)\n"
    "v_accvgpr_read_b32 v47, a188\n"
    "v_accvgpr_read_b32 v63, a190\n"
    "v_mfma_f32_16x16x32_bf16 a[140:143], v[188:191], v[92:95], a[140:143]\n"
    "s_nop 0\n"
    "v_accvgpr_read_b32 v45, a156\n"
    "v_accvgpr_read_b32 v61, a158\n"
    "v_accvgpr_read_b32 v46, a172\n"
    "v_accvgpr_read_b32 v62, a174\n"
    "v_accvgpr_read_b32 v49, a157\n"
    "v_pk_fma_f32 v[46:47], v[46:47], v[120:121], v[122:123] op_sel:[0,1,1] op_sel_hi:[1,1,1]\n"
    "v_accvgpr_read_b32 v50, a173\n"
    "v_accvgpr_read_b32 v44, a140\n"
    "v_accvgpr_read_b32 v60, a142\n"
    "v_pk_fma_f32 v[44:45], v[44:45], v[120:121], v[122:123] op_sel:[0,1,1] op_sel_hi:[1,1,1]\n"
    "v_pk_fma_f32 v[60:61], v[60:61], v[120:121], v[122:123] op_sel:[0,1,1] op_sel_hi:[1,1,1]\n"
    "v_cvt_pk_bf16_f32 v44, v44, v45\n"
    "v_accvgpr_read_b32 v48, a141\n"
    "v_accvgpr_read_b32 v51, a189\n"
    "v_pk_fma_f32 v[62:63], v[62:63], v[120:121], v[122:123] op_sel:[0,1,1] op_sel_hi:[1,1,1]\n"
    "v_cvt_pk_bf16_f32 v60, v60, v61\n"
    "v_accvgpr_read_b32 v64, a143\n"
    "v_accvgpr_read_b32 v65, a159\n"
    "v_accvgpr_read_b32 v66, a175\n"
    "v_accvgpr_read_b32 v67, a191\n"
    "v_cvt_pk_bf16_f32 v45, v46, v47\n"
    "v_pk_fma_f32 v[48:49], v[48:49], v[120:121], v[122:123] op_sel:[0,1,1] op_sel_hi:[1,1,1]\n"
    "v_pk_fma_f32 v[50:51], v[50:51], v[120:121], v[122:123] op_sel:[0,1,1] op_sel_hi:[1,1,1]\n"
    "v_cvt_pk_bf16_f32 v61, v62, v63\n"
    "v_pk_fma_f32 v[64:65], v[64:65], v[120:121], v[122:123] op_sel:[0,1,1] op_sel_hi:[1,1,1]\n"
    "v_pk_fma_f32 v[66:67], v[66:67], v[120:121], v[122:123] op_sel:[0,1,1] op_sel_hi:[1,1,1]\n"
    "v_cvt_pk_bf16_f32 v46, v48, v49\n"
    "v_cvt_pk_bf16_f32 v47, v50, v51\n"
    "v_cvt_pk_bf16_f32 v62, v64, v65\n"
    "v_cvt_pk_bf16_f32 v63, v66, v67\n"
    "ds_write_b128 v206, v[44:47] offset:4096\n"
    "ds_write_b128 v207, v[60:63] offset:4096\n"
    "ds_read_b128 v[48:51], v208 offset:4096\n"
    "buffer_store_dwordx4 v[56:59], v205, s[24:27], s77 offen offset:3328 sc1\n"
    "ds_read_b128 v[64:67], v208 offset:5120\n"
    "s_waitcnt lgkmcnt(1)\n"
    "buffer_store_dwordx4 v[48:51], v205, s[24:27], s78 offen offset:256 sc1\n"
    "s_waitcnt lgkmcnt(0)\n"
    "buffer_store_dwordx4 v[64:67], v205, s[24:27], s78 offen offset:3328 sc1\n"
    "s_cmp_eq_u32 %0, 0\n"
    "s_cbranch_scc1 .Lfin\n"
    "s_waitcnt vmcnt(0)\n"
    ".Lfin:\n"
    "s_branch .Lend\n"
    ".LpollAK:\n"
    "s_sleep 1\n"
    "buffer_load_dwordx2 v[236:237], v87, s[36:39], 0 offen offset:0 sc1\n"
    "s_waitcnt vmcnt(0)\n"
    "v_cmp_eq_u64 s[54:55], v[236:237], v[238:239]\n"
    "s_add_u32 s58, s58, 1\n"
    "s_cmp_eq_u64 s[54:55], -1\n"
    "s_cbranch_scc1 .LpollAKr\n"
    "s_cmp_lt_u32 s58, 0x80000\n"
    "s_cbranch_scc1 .LpollAK\n"
    "s_branch .LpollAKr\n"
    ".LpollAV:\n"
    "s_sleep 1\n"
    "buffer_load_dwordx2 v[236:237], v87, s[36:39], 0 offen offset:0 sc1\n"
    "s_waitcnt vmcnt(0)\n"
    "v_cmp_eq_u64 s[54:55], v[236:237], v[238:239]\n"
    "s_add_u32 s58, s58, 1\n"
    "s_cmp_eq_u64 s[54:55], -1\n"
    "s_cbranch_scc1 .LpollAVr\n"
    "s_cmp_lt_u32 s58, 0x80000\n"
    "s_cbranch_scc1 .LpollAV\n"
    "s_branch .LpollAVr\n"
    ".Lpoll1:\n"
    "s_sleep 1\n"
    "v_cmp_eq_u64 s[54:55], v[236:237], v[238:239]\n"
    "s_nop 1\n"
    "s_cmp_eq_u64 s[54:55], -1\n"
    "s_cbranch_scc1 .Lpoll1l\n"
    "buffer_load_dwordx2 v[236:237], v241, s[36:39], s81 offen offset:0 sc1\n"
    "s_waitcnt vmcnt(0)\n"
    "v_cmp_eq_u64 s[54:55], v[236:237], v[238:239]\n"
    ".Lpoll1l:\n"
    "ds_read_b32 v245, v247\n"
    "s_waitcnt lgkmcnt(0)\n"
    "v_cmp_le_u32 s[56:57], s59, v245\n"
    "s_add_u32 s58, s58, 1\n"
    "s_nop 1\n"
    "s_and_b64 s[54:55], s[54:55], s[56:57]\n"
    "s_cmp_eq_u64 s[54:55], -1\n"
    "s_cbranch_scc1 .Lpoll1r\n"
    "s_cmp_lt_u32 s58, 0x80000\n"
    "s_cbranch_scc1 .Lpoll1\n"
    "s_branch .Lpoll1r\n"
    ".Lpoll2:\n"
    "s_sleep 1\n"
    "v_cmp_eq_u64 s[54:55], v[236:237], v[238:239]\n"
    "s_nop 1\n"
    "s_cmp_eq_u64 s[54:55], -1\n"
    "s_cbranch_scc1 .Lpoll2l\n"
    "buffer_load_dwordx2 v[236:237], v241, s[36:39], s81 offen offset:0 sc1\n"
    "s_waitcnt vmcnt(0)\n"
    "v_cmp_eq_u64 s[54:55], v[236:237], v[238:239]\n"
    ".Lpoll2l:\n"
    "ds_read_b32 v245, v247\n"
    "s_waitcnt lgkmcnt(0)\n"
    "v_cmp_le_u32 s[56:57], s59, v245\n"
    "s_add_u32 s58, s58, 1\n"
    "s_nop 1\n"
    "s_and_b64 s[54:55], s[54:55], s[56:57]\n"
    "s_cmp_eq_u64 s[54:55], -1\n"
    "s_cbranch_scc1 .Lpoll2r\n"
    "s_cmp_lt_u32 s58, 0x80000\n"
    "s_cbranch_scc1 .Lpoll2\n"
    "s_branch .Lpoll2r\n"
    ".Lpoll3:\n"
    "s_sleep 1\n"
    "v_cmp_eq_u64 s[54:55], v[236:237], v[238:239]\n"
    "s_nop 1\n"
    "s_cmp_eq_u64 s[54:55], -1\n"
    "s_cbranch_scc1 .Lpoll3l\n"
    "buffer_load_dwordx2 v[236:237], v241, s[36:39], s81 offen offset:0 sc1\n"
    "s_waitcnt vmcnt(0)\n"
    "v_cmp_eq_u64 s[54:55], v[236:237], v[238:239]\n"
    ".Lpoll3l:\n"
    "ds_read_b32 v245, v247\n"
    "s_waitcnt lgkmcnt(0)\n"
    "v_cmp_le_u32 s[56:57], s59, v245\n"
    "s_add_u32 s58, s58, 1\n"
    "s_nop 1\n"
    "s_and_b64 s[54:55], s[54:55], s[56:57]\n"
    "s_cmp_eq_u64 s[54:55], -1\n"
    "s_cbranch_scc1 .Lpoll3r\n"
    "s_cmp_lt_u32 s58, 0x80000\n"
    "s_cbranch_scc1 .Lpoll3\n"
    "s_branch .Lpoll3r\n"
    ".Lpoll4:\n"
    "s_sleep 1\n"
    "v_cmp_eq_u64 s[54:55], v[236:237], v[238:239]\n"
    "s_nop 1\n"
    "s_cmp_eq_u64 s[54:55], -1\n"
    "s_cbranch_scc1 .Lpoll4l\n"
    "buffer_load_dwordx2 v[236:237], v241, s[36:39], s81 offen offset:0 sc1\n"
    "s_waitcnt vmcnt(0)\n"
    "v_cmp_eq_u64 s[54:55], v[236:237], v[238:239]\n"
    ".Lpoll4l:\n"
    "ds_read_b32 v245, v247\n"
    "s_waitcnt lgkmcnt(0)\n"
    "v_cmp_le_u32 s[56:57], s59, v245\n"
    "s_add_u32 s58, s58, 1\n"
    "s_nop 1\n"
    "s_and_b64 s[54:55], s[54:55], s[56:57]\n"
    "s_cmp_eq_u64 s[54:55], -1\n"
    "s_cbranch_scc1 .Lpoll4r\n"
    "s_cmp_lt_u32 s58, 0x80000\n"
    "s_cbranch_scc1 .Lpoll4\n"
    "s_branch .Lpoll4r\n"
    ".Lend:\n"
    : "=&s"(bad), "+s"(ka) : "s"(b), "v"(tid)
    : "memory", "vcc", "s16", "s17", "s18", "s19", "s20", "s21", "s22", "s23", "s24", "s25", "s26", "s27", "s28", "s29", "s30", "s31", "s35", "s36", "s37", "s38", "s39", "s40", "s41", "s42", "s43", "s44", "s45", "s46", "s47", "s48", "s49", "s50", "s51", "s52", "s53", "s54", "s55", "s56", "s57", "s58", "s59", "s60", "s61", "s62", "s63", "s64", "s65", "s66", "s67", "s68", "s69", "s70", "s71", "s72", "s73", "s74", "s75", "s76", "s77", "s78", "s79", "s80", "s81", "s82", "s83", "s84", "s85", "s86", "s87", "s88", "s89", "s90", "s91", "s92", "s93", "s94", "s95", "s96", "s97", "s98", "s99", "v1", "v2", "v3", "v4", "v5", "v6", "v7", "v8", "v9", "v10", "v11", "v12", "v13", "v14", "v15", "v16", "v17", "v18", "v19", "v20", "v21", "v22", "v23", "v24", "v25", "v26", "v27", "v28", "v29", "v30", "v31", "v32", "v33", "v34", "v35", "v36", "v37", "v38", "v39", "v40", "v41", "v42", "v43", "v44", "v45", "v46", "v47", "v48", "v49", "v50", "v51", "v52", "v53", "v54", "v55", "v56", "v57", "v58", "v59", "v60", "v61", "v62", "v63", "v64", "v65", "v66", "v67", "v68", "v69", "v70", "v71", "v72", "v73", "v74", "v75", "v76", "v77", "v78", "v79", "v80", "v81", "v82", "v83", "v84", "v85", "v86", "v87", "v88", "v89", "v90", "v91", "v92", "v93", "v94", "v95", "v96", "v97", "v98", "v99", "v100", "v101", "v102", "v103", "v104", "v105", "v106", "v107", "v108", "v109", "v110", "v111", "v112", "v113", "v114", "v115", "v116", "v117", "v118", "v119", "v120", "v121", "v122", "v123", "v124", "v125", "v126", "v127", "v128", "v129", "v130", "v131", "v132", "v133", "v134", "v135", "v136", "v137", "v138", "v139", "v140", "v141", "v142", "v143", "v144", "v145", "v146", "v147", "v148", "v149", "v150", "v151", "v152", "v153", "v154", "v155", "v156", "v157", "v158", "v159", "v160", "v161", "v162", "v163", "v164", "v165", "v166", "v167", "v168", "v169", "v170", "v171", "v172", "v173", "v174", "v175", "v176", "v177", "v178", "v179", "v180", "v181", "v182", "v183", "v184", "v185", "v186", "v187", "v188", "v189", "v190", "v191", "v192", "v193", "v194", "v195", "v196", "v197", "v198", "v199", "v200", "v201", "v202", "v203", "v204", "v205", "v206", "v207", "v208", "v209", "v210", "v211", "v212", "v213", "v214", "v215", "v216", "v217", "v218", "v219", "v220", "v221", "v222", "v223", "v224", "v225", "v226", "v227", "v228", "v229", "v230", "v231", "v232", "v233", "v234", "v235", "v236", "v237", "v238", "v239", "v240", "v241", "v242", "v243", "v244", "v245", "v246", "v247", "v248", "v249", "v250", "v251", "v252", "v253", "v254", "v255", "a0", "a1", "a2", "a3", "a4", "a5", "a6", "a7", "a8", "a9", "a10", "a11", "a12", "a13", "a14", "a15", "a16", "a17", "a18", "a19", "a20", "a21", "a22", "a23", "a24", "a25", "a26", "a27", "a28", "a29", "a30", "a31", "a32", "a33", "a34", "a35", "a36", "a37", "a38", "a39", "a40", "a41", "a42", "a43", "a44", "a45", "a46", "a47", "a48", "a49", "a50", "a51", "a52", "a53", "a54", "a55", "a56", "a57", "a58", "a59", "a60", "a61", "a62", "a63", "a64", "a65", "a66", "a67", "a68", "a69", "a70", "a71", "a72", "a73", "a74", "a75", "a76", "a77", "a78", "a79", "a80", "a81", "a82", "a83", "a84", "a85", "a86", "a87", "a88", "a89", "a90", "a91", "a92", "a93", "a94", "a95", "a96", "a97", "a98", "a99", "a100", "a101", "a102", "a103", "a104", "a105", "a106", "a107", "a108", "a109", "a110", "a111", "a112", "a113", "a114", "a115", "a116", "a117", "a118", "a119", "a120", "a121", "a122", "a123", "a124", "a125", "a126", "a127", "a128", "a129", "a130", "a131", "a132", "a133", "a134", "a135", "a136", "a137", "a138", "a139", "a140", "a141", "a142", "a143", "a144", "a145", "a146", "a147", "a148", "a149", "a150", "a151", "a152", "a153", "a154", "a155", "a156", "a157", "a158", "a159", "a160", "a161", "a162", "a163", "a164", "a165", "a166", "a167", "a168", "a169", "a170", "a171", "a172", "a173", "a174", "a175", "a176", "a177", "a178", "a179", "a180", "a181", "a182", "a183", "a184", "a185", "a186", "a187", "a188", "a189", "a190", "a191", "a192", "a193", "a194", "a195", "a196", "a197", "a198", "a199", "a200", "a201", "a202", "a203", "a204", "a205", "a206", "a207", "a208", "a209", "a210", "a211", "a212", "a213", "a214", "a215", "a216", "a217", "a218", "a219", "a220", "a221", "a222", "a223", "a224", "a225", "a226", "a227", "a228", "a229", "a230", "a231", "a232", "a233", "a234", "a235", "a236", "a237", "a238", "a239", "a240", "a241", "a242", "a243", "a244", "a245", "a246", "a247", "a248", "a249", "a250", "a251", "a252", "a253", "a254", "a255");
  if (bad) {
    slow_rows((const Args*)ka, (b & 7) * 4 + ((b >> 3) & 3), (b >> 5) * 256 + (tid & 192), tid & 63);
  }
}

extern "C" void kernel_launch(void* const* d_in, const int* in_sizes, int n_in,
                              void* d_out, int out_size, void* d_ws, size_t ws_size,
                              hipStream_t stream) {
  (void)hipFuncSetAttribute((const void*)attn_fwd, hipFuncAttributeMaxDynamicSharedMemorySize, SHM);
  attn_fwd<<<256, 256, SHM, stream>>>((const unsigned short*)d_in[0], (const int*)d_in[1], (const int*)d_in[2], (const unsigned short*)d_in[3], (const unsigned short*)d_in[4],
                                      (const unsigned short*)d_in[5], (const unsigned short*)d_in[6], (char*)d_ws, (unsigned short*)d_out);
}
```

```cpp
#include <hip/hip_runtime.h>

constexpr int D = 192, NH = 32, S = 2048;
constexpr int SHM = 155968;

struct Args { const unsigned short* q; const int* qk; const int* qv; const unsigned short* ksc; const unsigned short* kzr;
              const unsigned short* vsc; const unsigned short* vzr; char* ws; unsigned short* o; };

__device__ __forceinline__ float bf2f(unsigned short x) { return __uint_as_float((unsigned)x << 16); }
__device__ __forceinline__ float rbf(float x) { return (float)(__bf16)x; }

__device__ __forceinline__ void slow_rows(const Args* a, unsigned h, unsigned row0, unsigned lane) {
  const unsigned short* q = a->q + ((size_t)h * S + row0) * D;
  unsigned short* o = a->o + ((size_t)h * S + row0) * D;
  const int* qk = a->qk + (size_t)h * S * D; const int* qv = a->qv + (size_t)h * S * D;
  const unsigned short* ks = a->ksc + h * 32 * D; const unsigned short* kz = a->kzr + h * 32 * D;
  const unsigned short* vs = a->vsc + h * S; const unsigned short* vz = a->vzr + h * S;
#pragma unroll 1
  for (int r = 0; r < 64; ++r) {
    float m = -INFINITY, l = 0.f, a0 = 0.f, a1 = 0.f, a2 = 0.f;
#pragma unroll 1
    for (int t = 0; t < 32; ++t) {
      const int* kr = qk + (size_t)(t * 64 + lane) * D;
      float s = 0.f;
#pragma unroll 4
      for (int d = 0; d < D; ++d) s = fmaf(bf2f(q[r * D + d]), rbf(rbf(rbf((float)kr[d]) - bf2f(kz[t * D + d])) * bf2f(ks[t * D + d])), s);
      s *= 0.07216878364870322f;
      float mx = s;
      for (int x = 32; x; x >>= 1) mx = fmaxf(mx, __shfl_xor(mx, x));
      const float mn = fmaxf(m, mx), c = __expf(m - mn), p = __expf(s - mn);
      float ps = p;
      for (int x = 32; x; x >>= 1) ps += __shfl_xor(ps, x);
      l = l * c + ps; a0 *= c; a1 *= c; a2 *= c; m = mn;
#pragma unroll 2
      for (int k = 0; k < 64; ++k) {
        const float pk = __shfl(p, k), z = bf2f(vz[t * 64 + k]), sc = bf2f(vs[t * 64 + k]);
        const int* vr = qv + (size_t)(t * 64 + k) * D + lane;
        a0 = fmaf(pk, rbf(rbf(rbf((float)vr[0]) - z) * sc), a0); a1 = fmaf(pk, rbf(rbf(rbf((float)vr[64]) - z) * sc), a1); a2 = fmaf(pk, rbf(rbf(rbf((float)vr[128]) - z) * sc), a2);
      }
    }
    const float i = 1.f / l;
    o[r * D + lane] = __builtin_bit_cast(unsigned short, (__bf16)(a0 * i)); o[r * D + 64 + lane] = __builtin_bit_cast(unsigned short, (__bf16)(a1 * i));
    o[r * D + 128 + lane] = __builtin_bit_cast(unsigned short, (__bf16)(a2 * i));
  }
}

__global__ __launch_bounds__(256, 1) void attn_fwd(const unsigned short* q, const int* qk, const int* qv, const unsigned short* ksc, const unsigned short* kzr,
                                                   const unsigned short* vsc, const unsigned short* vzr, char* ws, unsigned short* o) {
  unsigned long long ka = (unsigned long long)__builtin_amdgcn_kernarg_segment_ptr();
  unsigned bad, b = blockIdx.x;
  const unsigned tid = threadIdx.x;
  asm volatile(
    "s_load_dwordx2 s[28:29], %1, 8\n"
    "s_load_dwordx2 s[40:41], %1, 16\n"
    "s_load_dwordx2 s[44:45], %1, 24\n"
    "s_load_dwordx2 s[88:89], %1, 32\n"
    "s_load_dwordx2 s[36:37], %1, 56\n"
    "s_load_dwordx2 s[92:93], %1, 40\n"
    "s_load_dwordx2 s[96:97], %1, 48\n"
    "s_load_dwordx2 s[20:21], %1, 0\n"
    "s_load_dwordx2 s[16:17], %1, 56\n"
    "s_load_dwordx2 s[24:25], %1, 64\n"
    "v_readfirstlane_b32 s84, %3\n"
    "v_and_b32 v156, 63, %3\n"
    "s_and_b32 s52, %2, 7\n"
    "s_bfe_u32 s72, %2, 0x20003\n"
    "s_lshr_b32 s53, %2, 5\n"
    "s_lshl_b32 s52, s52, 2\n"
    "s_lshr_b32 s84, s84, 6\n"
    "s_or_b32 s52, s52, s72\n"
    "v_lshlrev_b32 v1, 4, v156\n"
    "s_getpc_b64 s[54:55]\n"
    ".Lpc:\n"
    "s_lshl_b32 s72, s72, 14\n"
    "v_and_b32 v246, 127, %3\n"
    "v_lshl_add_u32 v246, v246, 7, s72\n"
    "v_min_u32 v246, .Lend-.Lpc-4, v246\n"
    "global_load_dword v246, v246, s[54:55]\n"
    "s_mov_b32 s30, 1572864\n"
    "s_mov_b32 s31, 0x20000\n"
    "s_mov_b32 s42, 1572864\n"
    "s_mov_b32 s43, 0x20000\n"
    "s_mov_b32 s46, 12288\n"
    "s_mov_b32 s47, 0x20000\n"
    "s_mov_b32 s90, 12288\n"
    "s_mov_b32 s91, 0x20000\n"
    "s_lshr_b32 s79, s84, 1\n"
    "s_and_b32 s59, s84, 1\n"
    "s_cmp_ge_u32 s53, 6\n"
    "s_cselect_b32 s83, 1, 0\n"
    "s_or_b32 s83, s83, s84\n"
    "s_sub_u32 s58, 2, s79\n"
    "s_mul_i32 s71, s58, 49152\n"
    "s_mul_i32 s72, s52, 1572864\n"
    "s_mul_i32 s73, s52, 12288\n"
    "v_lshrrev_b32 v157, 2, v156\n"
    "v_and_b32 v158, 3, v156\n"
    "s_mul_i32 s35, s84, 3072\n"
    "s_bitcmp1_b32 s83, 0\n"
    "s_cbranch_scc1 .Lf2b\n"
    "v_and_b32 v159, 32, v156\n"
    "v_mul_u32_u24 v242, 376, v159\n"
    "v_and_b32 v159, 16, v156\n"
    "v_mul_u32_u24 v159, 88, v159\n"
    "v_lshl_add_u32 v242, v156, 3, v242\n"
    "v_add_u32 v242, v159, v242\n"
    "s_lshl_b32 s63, s53, 7\n"
    "v_mul_u32_u24 v160, 768, v157\n"
    "v_lshl_add_u32 v160, v158, 4, v160\n"
    "s_and_b32 s81, s58, 1\n"
    "s_mul_i32 s81, s81, 384\n"
    "s_lshl_b32 s75, s53, 6\n"
    "s_add_u32 s81, s81, s75\n"
    "s_add_u32 s75, s71, s63\n"
    "s_add_u32 s76, s75, 6144\n"
    "s_add_u32 s77, s75, 24576\n"
    "s_add_u32 s78, s75, 30720\n"
    "s_waitcnt lgkmcnt(0)\n"
    "s_add_u32 s44, s44, s73\n"
    "s_addc_u32 s45, s45, 0\n"
    "s_add_u32 s88, s88, s73\n"
    "s_addc_u32 s89, s89, 0\n"
    "buffer_load_dwordx4 v[4:7], v1, s[44:47], s35 offen offset:0\n"
    "buffer_load_dwordx4 v[8:11], v1, s[44:47], s35 offen offset:1024\n"
    "buffer_load_dwordx4 v[12:15], v1, s[44:47], s35 offen offset:2048\n"
    "buffer_load_dwordx4 v[16:19], v1, s[88:91], 0 offen offset:0\n"
    "buffer_load_dwordx4 v[164:167], v160, s[44:47], s81 offen offset:0\n"
    "buffer_load_dwordx4 v[168:171], v160, s[88:91], s81 offen offset:0\n"
    "s_add_u32 s28, s28, s72\n"
    "s_addc_u32 s29, s29, 0\n"
    "buffer_load_dwordx2 v[204:205], v242, s[28:31], s75 offen offset:0 nt\n"
    "buffer_load_dwordx2 v[206:207], v242, s[28:31], s75 offen offset:768 nt\n"
    "buffer_load_dwordx2 v[208:209], v242, s[28:31], s75 offen offset:3072 nt\n"
    "buffer_load_dwordx2 v[210:211], v242, s[28:31], s75 offen offset:3840 nt\n"
    "buffer_load_dwordx2 v[212:213], v242, s[28:31], s76 offen offset:0 nt\n"
    "buffer_load_dwordx2 v[214:215], v242, s[28:31], s76 offen offset:768 nt\n"
    "buffer_load_dwordx2 v[216:217], v242, s[28:31], s76 offen offset:3072 nt\n"
    "buffer_load_dwordx2 v[218:219], v242, s[28:31], s76 offen offset:3840 nt\n"
    "buffer_load_dwordx2 v[220:221], v242, s[28:31], s77 offen offset:0 nt\n"
    "buffer_load_dwordx2 v[222:223], v242, s[28:31], s77 offen offset:768 nt\n"
    "buffer_load_dwordx2 v[224:225], v242, s[28:31], s77 offen offset:3072 nt\n"
    "buffer_load_dwordx2 v[226:227], v242, s[28:31], s77 offen offset:3840 nt\n"
    "buffer_load_dwordx2 v[228:229], v242, s[28:31], s78 offen offset:0 nt\n"
    "buffer_load_dwordx2 v[230:231], v242, s[28:31], s78 offen offset:768 nt\n"
    "buffer_load_dwordx2 v[232:233], v242, s[28:31], s78 offen offset:3072 nt\n"
    "buffer_load_dwordx2 v[234:235], v242, s[28:31], s78 offen offset:3840 nt\n"
    "s_add_u32 s75, s71, s63\n"
    "s_add_u32 s75, s75, 98304\n"
    "s_add_u32 s76, s75, 6144\n"
    "s_add_u32 s77, s75, 24576\n"
    "s_add_u32 s78, s75, 30720\n"
    "buffer_load_dwordx2 v[36:37], v242, s[28:31], s75 offen offset:0 nt\n"
    "buffer_load_dwordx2 v[38:39], v242, s[28:31], s75 offen offset:768 nt\n"
    "v_lshrrev_b32 v162, 1, v156\n"
    "v_and_b32 v162, 16, v162\n"
    "v_bfe_u32 v159, v156, 4, 1\n"
    "v_or_b32 v162, v162, v159\n"
    "v_and_or_b32 v162, v157, 2, v162\n"
    "v_mul_u32_u24 v162, 768, v162\n"
    "v_and_b32 v163, 0x70, v1\n"
    "v_add_u32 v162, v162, v163\n"
    "s_branch .Lf2e\n"
    ".Lf2b:\n"
    "v_and_b32 v159, 48, v156\n"
    "v_lshlrev_b32 v242, 2, v159\n"
    "v_lshl_add_u32 v243, v158, 4, v242\n"
    "v_and_b32 v159, 12, v156\n"
    "v_mul_u32_u24 v242, 384, v159\n"
    "v_add_u32 v242, v243, v242\n"
    "v_add_u32 v243, v243, v159\n"
    "s_mul_i32 s63, s53, 6144\n"
    "s_sub_u32 s63, s63, 49152\n"
    "s_cmp_eq_u32 s59, 0\n"
    "s_cselect_b32 s63, 0x7f000000, s63\n"
    "s_add_u32 s75, s71, s63\n"
    "s_waitcnt lgkmcnt(0)\n"
    "s_add_u32 s44, s44, s73\n"
    "s_addc_u32 s45, s45, 0\n"
    "s_add_u32 s88, s88, s73\n"
    "s_addc_u32 s89, s89, 0\n"
    "buffer_load_dwordx4 v[4:7], v1, s[44:47], s35 offen offset:0\n"
    "buffer_load_dwordx4 v[8:11], v1, s[44:47], s35 offen offset:1024\n"
    "buffer_load_dwordx4 v[12:15], v1, s[44:47], s35 offen offset:2048\n"
    "buffer_load_dwordx4 v[16:19], v1, s[88:91], 0 offen offset:0\n"
    "s_add_u32 s40, s40, s72\n"
    "s_addc_u32 s41, s41, 0\n"
    "buffer_load_dwordx4 v[204:207], v242, s[40:43], s75 offen offset:0 nt\n"
    "buffer_load_dwordx4 v[208:211], v242, s[40:43], s75 offen offset:768 nt\n"
    "buffer_load_dwordx4 v[212:215], v242, s[40:43], s75 offen offset:256 nt\n"
    "buffer_load_dwordx4 v[216:219], v242, s[40:43], s75 offen offset:1024 nt\n"
    "buffer_load_dwordx4 v[220:223], v242, s[40:43], s75 offen offset:512 nt\n"
    "buffer_load_dwordx4 v[224:227], v242, s[40:43], s75 offen offset:1280 nt\n"
    "s_add_u32 s75, s71, s63\n"
    "s_add_u32 s75, s75, 98304\n"
    "buffer_load_dwordx4 v[36:39], v242, s[40:43], s75 offen offset:0 nt\n"
    "s_add_u32 s28, s28, s72\n"
    "s_addc_u32 s29, s29, 0\n"
    "v_lshrrev_b32 v162, 1, v156\n"
    "v_and_b32 v162, 16, v162\n"
    "v_bfe_u32 v159, v156, 4, 1\n"
    "v_or_b32 v162, v162, v159\n"
    "v_and_or_b32 v162, v157, 2, v162\n"
    "v_mul_u32_u24 v162, 768, v162\n"
    "v_and_b32 v163, 0x70, v1\n"
    "v_add_u32 v162, v162, v163\n"
    ".Lf2e:\n"
    "s_nop 4\n"
    "v_lshlrev_b32 v247, 2, v156\n"
    "v_add_u32 v247, 155648, v247\n"
    "v_mov_b32 v159, 0\n"
    "ds_write_b32 v247, v159\n"
    "v_mov_b32 v159, 0xff800000\n"
    "v_mov_b32 v160, 155904\n"
    "ds_write_b32 v160, v159\n"
    "s_mov_b32 s38, 5376\n"
    "s_mov_b32 s39, 0x20000\n"
    "s_mov_b32 s94, 4096\n"
    "s_mov_b32 s95, 0x20000\n"
    "s_mov_b32 s98, 4096\n"
    "s_mov_b32 s99, 0x20000\n"
    "s_mov_b32 s22, 98304\n"
    "s_mov_b32 s23, 0x20000\n"
    "s_mov_b32 s18, 1622016\n"
    "s_mov_b32 s19, 0x20000\n"
    "s_mov_b32 s26, 98304\n"
    "s_mov_b32 s27, 0x20000\n"
    "s_lshl_b32 s80, s58, 7\n"
    "s_add_u32 s80, s80, 256\n"
    "s_lshl_b32 s74, s52, 16\n"
    "s_mul_i32 s69, s59, 384\n"
    "s_mul_i32 s57, s79, 6144\n"
    "s_add_u32 s57, s57, s69\n"
    "s_add_u32 s66, s57, 24576\n"
    "s_add_u32 s36, s36, s74\n"
    "s_addc_u32 s37, s37, 0\n"
    "s_lshr_b32 s74, s74, 4\n"
    "s_add_u32 s92, s92, s74\n"
    "s_addc_u32 s93, s93, 0\n"
    "s_add_u32 s96, s96, s74\n"
    "s_addc_u32 s97, s97, 0\n"
    "v_lshlrev_b32 v128, 3, v156\n"
    "v_mov_b32 v129, 0\n"
    "v_lshlrev_b32 v130, 4, %3\n"
    "buffer_load_dwordx2 v[124:125], v128, s[44:47], 0 offen offset:0\n"
    "buffer_load_dwordx2 v[126:127], v128, s[88:91], 0 offen offset:0\n"
    "buffer_load_dwordx2 v[238:239], v129, s[36:39], 0 offen offset:0 sc1\n"
    "s_mul_i32 s74, s52, 786432\n"
    "s_mul_i32 s73, s53, 98304\n"
    "s_add_u32 s74, s74, s73\n"
    "s_add_u32 s20, s20, s74\n"
    "s_addc_u32 s21, s21, 0\n"
    "s_add_u32 s24, s24, s74\n"
    "s_addc_u32 s25, s25, 0\n"
    "s_mul_i32 s72, s52, 1626112\n"
    "s_add_u32 s72, s72, 2097152\n"
    "s_add_u32 s16, s16, s72\n"
    "s_addc_u32 s17, s17, 0\n"
    "s_mul_i32 s65, s84, 6144\n"
    "s_mul_i32 s85, s84, 24576\n"
    "s_waitcnt lgkmcnt(0)\n"
    "s_barrier\n"
    "s_add_u32 s73, s65, 49152\n"
    "v_add_u32 v131, s73, v1\n"
    "s_add_u32 s73, s73, s69\n"
    "v_add_u32 v132, s73, v163\n"
    "v_and_b32 v159, 32, v156\n"
    "v_lshlrev_b32 v244, 3, v156\n"
    "v_xor_b32 v244, v244, v159\n"
    "v_lshl_add_u32 v244, v159, 3, v244\n"
    "s_bitcmp1_b32 s83, 0\n"
    "s_cbranch_scc1 .Lf5b\n"
    "s_lshl_b32 s60, s53, 11\n"
    "s_add_u32 s61, s60, 12288\n"
    "s_lshl_b32 s73, s58, 8\n"
    "s_add_u32 s73, s73, 122880\n"
    "v_and_b32 v243, 0xf0, v1\n"
    "v_add_u32 v243, s73, v243\n"
    "s_and_b32 s73, s73, 0x1fd00\n"
    "v_lshlrev_b32 v160, 9, v157\n"
    "v_lshl_add_u32 v160, v158, 6, v160\n"
    "v_add_u32 v160, s73, v160\n"
    "s_lshl_b32 s73, s79, 8\n"
    "s_mul_i32 s74, s59, 6144\n"
    "s_add_u32 s73, s73, s74\n"
    "v_add_u32 v163, s73, v244\n"
    "v_and_b32 v159, 32, v156\n"
    "v_lshlrev_b32 v244, 2, v156\n"
    "v_xor_b32 v244, v244, v159\n"
    "v_mad_u32_u24 v244, v159, 12, v244\n"
    "buffer_load_dwordx2 v[40:41], v242, s[28:31], s75 offen offset:3072 nt\n"
    "buffer_load_dwordx2 v[42:43], v242, s[28:31], s75 offen offset:3840 nt\n"
    "v_add_u32 v159, 0, v156\n"
    "v_mul_u32_u24 v159, 2731, v159\n"
    "v_lshrrev_b32 v159, 16, v159\n"
    "v_xor_b32 v159, v159, v156\n"
    "v_and_b32 v159, 7, v159\n"
    "v_and_b32 v88, 0x380, v1\n"
    "v_lshl_add_u32 v88, v159, 4, v88\n"
    "v_add_u32 v159, 64, v156\n"
    "v_mul_u32_u24 v159, 2731, v159\n"
    "v_lshrrev_b32 v159, 16, v159\n"
    "v_xor_b32 v159, v159, v156\n"
    "v_and_b32 v159, 7, v159\n"
    "v_and_b32 v89, 0x380, v1\n"
    "v_lshl_add_u32 v89, v159, 4, v89\n"
    "v_add_u32 v159, 128, v156\n"
    "v_mul_u32_u24 v159, 2731, v159\n"
    "v_lshrrev_b32 v159, 16, v159\n"
    "v_xor_b32 v159, v159, v156\n"
    "v_and_b32 v159, 7, v159\n"
    "v_and_b32 v90, 0x380, v1\n"
    "v_lshl_add_u32 v90, v159, 4, v90\n"
    "v_and_b32 v2, 7, v156\n"
    "v_lshrrev_b32 v144, 3, %3\n"
    "v_mul_u32_u24 v144, 768, v144\n"
    "v_lshl_add_u32 v144, v2, 3, v144\n"
    "buffer_load_dwordx2 v[44:45], v242, s[28:31], s76 offen offset:0 nt\n"
    "buffer_load_dwordx2 v[46:47], v242, s[28:31], s76 offen offset:768 nt\n"
    "v_add_u32 v144, 131072, v144\n"
    "v_mad_u32_u24 v86, v2, 24, v144\n"
    "v_and_b32 v84, 15, v156\n"
    "v_lshrrev_b32 v159, 4, v156\n"
    "v_xor_b32 v159, v159, v2\n"
    "v_mul_u32_u24 v84, 384, v84\n"
    "v_lshl_add_u32 v84, v159, 4, v84\n"
    "s_add_u32 s73, s65, 49152\n"
    "v_add_u32 v84, s73, v84\n"
    "v_xor_b32 v85, 64, v84\n"
    "v_subrev_u32 v154, 24576, v84\n"
    "v_subrev_u32 v155, 24576, v85\n"
    "s_add_u32 s67, s65, 98304\n"
    "s_add_u32 s68, s65, 24576\n"
    "s_add_u32 s69, s65, 73728\n"
    "s_mov_b32 s81, 256\n"
    "s_mul_i32 s74, s79, 24576\n"
    "s_add_u32 s70, s74, s65\n"
    "s_add_u32 s70, s70, 73728\n"
    "s_sub_u32 s64, s65, s74\n"
    "s_add_u32 s64, s64, 98304\n"
    "v_and_b32 v249, 0x310, v1\n"
    "v_and_b32 v159, 12, v156\n"
    "v_lshl_add_u32 v249, v159, 3, v249\n"
    "v_and_b32 v159, 2, v156\n"
    "buffer_load_dwordx2 v[48:49], v242, s[28:31], s76 offen offset:3072 nt\n"
    "buffer_load_dwordx2 v[50:51], v242, s[28:31], s76 offen offset:3840 nt\n"
    "v_lshl_add_u32 v249, v159, 6, v249\n"
    "s_add_u32 s73, s74, 73728\n"
    "v_add_u32 v248, s73, v249\n"
    "s_sub_u32 s73, 98304, s74\n"
    "v_add_u32 v249, s73, v249\n"
    "s_or_b32 s73, s53, s84\n"
    "s_cmp_eq_u32 s73, 0\n"
    "s_cselect_b32 s82, 0, 0x7fff0000\n"
    "v_and_b32 v252, 12, v156\n"
    "v_bfe_u32 v159, v156, 1, 1\n"
    "v_or_b32 v252, v252, v159\n"
    "v_and_b32 v159, 1, v156\n"
    "v_lshl_or_b32 v252, v159, 1, v252\n"
    "v_lshlrev_b32 v252, 6, v252\n"
    "v_lshlrev_b32 v159, 2, v156\n"
    "v_and_b32 v159, 32, v159\n"
    "v_xor_b32 v159, v159, v156\n"
    "v_and_or_b32 v252, v159, 48, v252\n"
    "v_mov_b32 v2, v252\n"
    "v_add_u32 v3, 24576, v252\n"
    "v_lshrrev_b32 v91, 3, v156\n"
    "v_lshl_add_u32 v87, v91, 6, 64\n"
    "v_add_u32 v87, 64, v87\n"
    "v_and_b32 v241, 1, v156\n"
    "v_lshlrev_b32 v241, 3, v241\n"
    "buffer_load_dwordx2 v[52:53], v242, s[28:31], s77 offen offset:0 nt\n"
    "buffer_load_dwordx2 v[54:55], v242, s[28:31], s77 offen offset:768 nt\n"
    "v_lshl_add_u32 v241, v91, 4, v241\n"
    "v_add_u32 v241, 1152, v241\n"
    "v_lshlrev_b32 v91, 24, v91\n"
    "v_lshl_add_u32 v91, v156, 3, v91\n"
    "s_lshl_b32 s73, s53, 4\n"
    "s_lshl_b32 s74, s59, 3\n"
    "s_add_u32 s73, s73, s74\n"
    "s_add_u32 s73, s73, 1152\n"
    "v_mov_b32 v240, 0x7fff0000\n"
    "v_writelane_b32 v240, s73, 0\n"
    "v_and_b32 v159, 48, v156\n"
    "s_mul_i32 s73, s79, 768\n"
    "s_sub_u32 s73, 131072, s73\n"
    "v_lshlrev_b32 v250, 1, v159\n"
    "v_add_u32 v250, s73, v250\n"
    "v_and_b32 v251, 3, v156\n"
    "v_lshl_add_u32 v251, v251, 7, v159\n"
    "s_add_u32 s73, s73, 256\n"
    "v_add_u32 v251, s73, v251\n"
    "v_and_b32 v159, 64, v1\n"
    "v_add_u32 v251, v159, v251\n"
    "v_bfe_i32 v159, v156, 2, 1\n"
    "v_and_b32 v159, 0xaaaa, v159\n"
    "v_or_b32 v253, 0x4444, v159\n"
    "s_mov_b32 s58, 0\n"
    "s_mov_b32 s59, 8\n"
    "s_waitcnt vmcnt(31)\n"
    "v_pk_min_i16 v4, v4, v10\n"
    "v_pk_min_i16 v5, v5, v11\n"
    "v_pk_min_i16 v6, v6, v12\n"
    "v_pk_min_i16 v7, v7, v13\n"
    "v_pk_min_i16 v8, v8, v14\n"
    "v_pk_min_i16 v9, v9, v15\n"
    "v_pk_min_i16 v16, v16, v18\n"
    "v_pk_min_i16 v17, v17, v19\n"
    "v_pk_min_i16 v4, v4, v7\n"
    "v_pk_min_i16 v5, v5, v8\n"
    "v_pk_min_i16 v6, v6, v9\n"
    "v_pk_min_i16 v16, v16, v17\n"
    "v_pk_min_i16 v4, v4, v5\n"
    "v_lshlrev_b32 v17, 16, v16\n"
    "v_pk_min_i16 v4, v4, v6\n"
    "v_min_i32 v16, v16, v17\n"
    "v_lshlrev_b32 v5, 16, v4\n"
    "v_mov_b32 v7, 155920\n"
    "v_min_i32 v4, v4, v5\n"
    "v_lshl_add_u32 v6, s84, 2, v7\n"
    "v_min_i32_dpp v16, v16, v16 row_shr:1\n"
    "v_min_i32_dpp v4, v4, v4 row_shr:1\n"
    "s_nop 0\n"
    "v_min_i32_dpp v16, v16, v16 row_shr:2\n"
    "v_min_i32_dpp v4, v4, v4 row_shr:2\n"
    "s_nop 0\n"
    "v_min_i32_dpp v16, v16, v16 row_shr:4\n"
    "v_min_i32_dpp v4, v4, v4 row_shr:4\n"
    "s_nop 0\n"
    "v_min_i32_dpp v16, v16, v16 row_shr:8\n"
    "v_min_i32_dpp v4, v4, v4 row_shr:8\n"
    "s_nop 0\n"
    "v_min_i32_dpp v16, v16, v16 row_bcast:15 row_mask:0xa\n"
    "v_min_i32_dpp v4, v4, v4 row_bcast:15 row_mask:0xa\n"
    "s_nop 0\n"
    "v_min_i32_dpp v16, v16, v16 row_bcast:31 row_mask:0xc\n"
    "v_min_i32_dpp v4, v4, v4 row_bcast:31 row_mask:0xc\n"
    "v_readlane_b32 s35, v16, 63\n"
    "v_readlane_b32 s73, v4, 63\n"
    "s_max_i32 s35, s35, 0\n"
    "s_and_b32 s35, s35, 0xffff0000\n"
    "v_mov_b32 v4, s73\n"
    "ds_write_b32 v6, v4\n"
    "v_mov_b32 v12, -126\n"
    "v_mov_b32 v13, 0x3dd53b95\n"
    "v_mov_b32 v80, 0\n"
    "s_waitcnt vmcnt(29)\n"
    "v_lshlrev_b32 v180, 16, v164\n"
    "v_and_b32 v181, 0xffff0000, v164\n"
    "v_lshlrev_b32 v182, 16, v168\n"
    "v_and_b32 v183, 0xffff0000, v168\n"
    "v_lshlrev_b32 v184, 16, v165\n"
    "v_and_b32 v185, 0xffff0000, v165\n"
    "v_lshlrev_b32 v186, 16, v169\n"
    "v_and_b32 v187, 0xffff0000, v169\n"
    "v_lshlrev_b32 v188, 16, v166\n"
    "v_and_b32 v189, 0xffff0000, v166\n"
    "v_lshlrev_b32 v190, 16, v170\n"
    "v_and_b32 v191, 0xffff0000, v170\n"
    "v_lshlrev_b32 v192, 16, v167\n"
    "v_and_b32 v193, 0xffff0000, v167\n"
    "v_lshlrev_b32 v194, 16, v171\n"
    "v_and_b32 v195, 0xffff0000, v171\n"
    "s_waitcnt lgkmcnt(0)\n"
    "s_barrier\n"
    "ds_read_b128 v[8:11], v7 offset:0\n"
    "buffer_load_dwordx2 v[56:57], v242, s[28:31], s77 offen offset:3072 nt\n"
    "buffer_load_dwordx2 v[58:59], v242, s[28:31], s77 offen offset:3840 nt\n"
    "buffer_load_dwordx4 v[136:139], v130, s[92:95], 0 offen offset:0\n"
    "buffer_load_dwordx4 v[140:143], v130, s[96:99], 0 offen offset:0\n"
    "v_accvgpr_write_b32 a0, 0\n"
    "v_accvgpr_write_b32 a1, 0\n"
    "v_accvgpr_write_b32 a2, 0\n"
    "v_accvgpr_write_b32 a3, 0\n"
    "v_accvgpr_write_b32 a4, 0\n"
    "v_accvgpr_write_b32 a5, 0\n"
    "v_accvgpr_write_b32 a6, 0\n"
    "v_accvgpr_write_b32 a7, 0\n"
    "v_accvgpr_write_b32 a8, 0\n"
    "v_accvgpr_write_b32 a9, 0\n"
    "v_accvgpr_write_b32 a10, 0\n"
    "v_accvgpr_write_b32 a11, 0\n"
    "v_accvgpr_write_b32 a12, 0\n"
    "v_accvgpr_write_b32 a13, 0\n"
    "v_accvgpr_write_b32 a14, 0\n"
    "v_accvgpr_write_b32 a15, 0\n"
    "v_accvgpr_write_b32 a16, 0\n"
    "v_accvgpr_write_b32 a17, 0\n"
    "v_accvgpr_write_b32 a18, 0\n"
    "v_accvgpr_write_b32 a19, 0\n"
    "v_accvgpr_write_b32 a20, 0\n"
    "v_accvgpr_write_b32 a21, 0\n"
    "v_accvgpr_write_b32 a22, 0\n"
    "v_accvgpr_write_b32 a23, 0\n"
    "v_accvgpr_write_b32 a24, 0\n"
    "v_accvgpr_write_b32 a25, 0\n"
    "v_accvgpr_write_b32 a26, 0\n"
    "v_accvgpr_write_b32 a27, 0\n"
    "v_accvgpr_write_b32 a28, 0\n"
    "v_accvgpr_write_b32 a29, 0\n"
    "v_accvgpr_write_b32 a30, 0\n"
    "v_accvgpr_write_b32 a31, 0\n"
    "v_accvgpr_write_b32 a32, 0\n"
    "v_accvgpr_write_b32 a33, 0\n"
    "v_accvgpr_write_b32 a34, 0\n"
    "v_accvgpr_write_b32 a35, 0\n"
    "v_accvgpr_write_b32 a36, 0\n"
    "v_accvgpr_write_b32 a37, 0\n"
    "v_accvgpr_write_b32 a38, 0\n"
    "v_accvgpr_write_b32 a39, 0\n"
    "v_accvgpr_write_b32 a40, 0\n"
    "v_accvgpr_write_b32 a41, 0\n"
    "v_accvgpr_write_b32 a42, 0\n"
    "v_accvgpr_write_b32 a43, 0\n"
    "v_accvgpr_write_b32 a44, 0\n"
    "v_accvgpr_write_b32 a45, 0\n"
    "v_accvgpr_write_b32 a46, 0\n"
    "v_accvgpr_write_b32 a47, 0\n"
    "v_accvgpr_write_b32 a48, 0\n"
    "v_accvgpr_write_b32 a49, 0\n"
    "v_accvgpr_write_b32 a50, 0\n"
    "v_accvgpr_write_b32 a51, 0\n"
    "v_accvgpr_write_b32 a52, 0\n"
    "v_accvgpr_write_b32 a53, 0\n"
    "v_accvgpr_write_b32 a54, 0\n"
    "v_accvgpr_write_b32 a55, 0\n"
    "v_accvgpr_write_b32 a56, 0\n"
    "v_accvgpr_write_b32 a57, 0\n"
    "v_accvgpr_write_b32 a58, 0\n"
    "v_accvgpr_write_b32 a59, 0\n"
    "s_waitcnt lgkmcnt(0)\n"
    "v_min3_i32 v4, v8, v9, v10\n"
    "v_min_i32 v4, v4, v11\n"
    "v_max_i32 v4, 0, v4\n"
    "v_and_b32 v4, 0xffff0000, v4\n"
    "v_mul_f32 v4, s35, v4\n"
    "v_mul_f32 v4, 0x3ed53b95, v4\n"
    "v_lshrrev_b32 v4, 23, v4\n"
    "v_sub_u32 v4, 2, v4\n"
    "v_med3_i32 v4, v4, 0, v12\n"
    "v_ldexp_f32 v13, v13, v4\n"
    "v_readfirstlane_b32 s89, v4\n"
    "v_readfirstlane_b32 s88, v13\n"
    "s_mul_i32 s89, s89, 0xff7fff80\n"
    "s_nop 0\n"
    "v_mul_f32 v180, s88, v180\n"
    "v_mul_f32 v181, s88, v181\n"
    "v_mul_f32 v184, s88, v184\n"
    "v_mul_f32 v185, s88, v185\n"
    "v_mul_f32 v188, s88, v188\n"
    "v_mul_f32 v189, s88, v189\n"
    "v_mul_f32 v192, s88, v192\n"
    "v_mul_f32 v193, s88, v193\n"
    "v_fmaak_f32 v182, v182, v180, 0x80730000\n"
    "v_fmaak_f32 v183, v183, v181, 0x80730000\n"
    "v_fmaak_f32 v186, v186, v184, 0x80730000\n"
    "v_fmaak_f32 v187, v187, v185, 0x80730000\n"
    "v_fmaak_f32 v190, v190, v188, 0x80730000\n"
    "v_fmaak_f32 v191, v191, v189, 0x80730000\n"
    "v_fmaak_f32 v194, v194, v192, 0x80730000\n"
    "v_fmaak_f32 v195, v195, v193, 0x80730000\n"
    "ds_write_b128 v160, v[180:183] offset:0\n"
    "ds_write_b128 v160, v[184:187] offset:16\n"
    "ds_write_b128 v160, v[188:191] offset:32\n"
    "ds_write_b128 v160, v[192:195] offset:48\n"
    "s_waitcnt vmcnt(17)\n"
    "s_add_u32 s54, s71, s60\n"
    "s_add_u32 s55, s71, s61\n"
    "v_cvt_f32_i32 v204, v204\n"
    "v_cvt_f32_i32 v205, v205\n"
    "v_cvt_f32_i32 v206, v206\n"
    "v_cvt_f32_i32 v207, v207\n"
    "v_cvt_f32_i32 v208, v208\n"
    "v_cvt_f32_i32 v209, v209\n"
    "v_cvt_f32_i32 v210, v210\n"
    "v_cvt_f32_i32 v211, v211\n"
    "v_cvt_f32_i32 v212, v212\n"
    "v_cvt_f32_i32 v213, v213\n"
    "v_cvt_f32_i32 v214, v214\n"
    "v_cvt_f32_i32 v215, v215\n"
    "ds_read_b128 v[92:95], v243 offset:0\n"
    "v_cvt_f32_i32 v216, v216\n"
    "v_cvt_f32_i32 v217, v217\n"
    "v_cvt_f32_i32 v218, v218\n"
    "v_cvt_f32_i32 v219, v219\n"
    "v_cvt_f32_i32 v220, v220\n"
    "v_cvt_f32_i32 v221, v221\n"
    "v_cvt_f32_i32 v222, v222\n"
    "v_cvt_f32_i32 v223, v223\n"
    "v_cvt_f32_i32 v224, v224\n"
    "v_cvt_f32_i32 v225, v225\n"
    "v_cvt_f32_i32 v226, v226\n"
    "v_cvt_f32_i32 v227, v227\n"
    "s_waitcnt lgkmcnt(0)\n"
    "v_fma_f32 v204, v204, v92, -v94\n"
    "v_fma_f32 v205, v205, v93, -v95\n"
    "v_fma_f32 v206, v206, v92, -v94\n"
    "v_fma_f32 v207, v207, v93, -v95\n"
    "v_cvt_pk_bf16_f32 v204, v204, v205\n"
    "buffer_load_dwordx2 v[60:61], v242, s[28:31], s78 offen offset:0 nt\n"
    "buffer_load_dwordx2 v[62:63], v242, s[28:31], s78 offen offset:768 nt\n"
    "buffer_load_dwordx4 v[4:7], v162, s[28:31], s57 offen offset:0\n"
    "buffer_load_dwordx4 v[8:11], v162, s[28:31], s57 offen offset:3072\n"
    "v_cvt_pk_bf16_f32 v206, v206, v207\n"
    "v_cvt_f32_i32 v228, v228\n"
    "v_cvt_f32_i32 v229, v229\n"
    "v_cvt_f32_i32 v230, v230\n"
    "v_cvt_f32_i32 v231, v231\n"
    "buffer_store_dword v204, v244, s[16:19], s54 offen offset:0 sc1\n"
    "buffer_store_dword v206, v244, s[16:19], s54 offen offset:128 sc1\n"
    "v_fma_f32 v208, v208, v92, -v94\n"
    "v_fma_f32 v209, v209, v93, -v95\n"
    "v_fma_f32 v210, v210, v92, -v94\n"
    "v_fma_f32 v211, v211, v93, -v95\n"
    "v_cvt_pk_bf16_f32 v208, v208, v209\n"
    "v_cvt_pk_bf16_f32 v210, v210, v211\n"
    "v_cvt_f32_i32 v232, v232\n"
    "v_cvt_f32_i32 v233, v233\n"
    "v_cvt_f32_i32 v234, v234\n"
    "v_cvt_f32_i32 v235, v235\n"
    "buffer_store_dword v208, v244, s[16:19], s54 offen offset:1024 sc1\n"
    "buffer_store_dword v210, v244, s[16:19], s54 offen offset:1152 sc1\n"
    "v_fma_f32 v212, v212, v92, -v94\n"
    "v_fma_f32 v213, v213, v93, -v95\n"
    "v_fma_f32 v214, v214, v92, -v94\n"
    "v_fma_f32 v215, v215, v93, -v95\n"
    "v_cvt_pk_bf16_f32 v212, v212, v213\n"
    "v_cvt_pk_bf16_f32 v214, v214, v215\n"
    "buffer_store_dword v212, v244, s[16:19], s54 offen offset:256 sc1\n"
    "buffer_store_dword v214, v244, s[16:19], s54 offen offset:384 sc1\n"
    "v_fma_f32 v216, v216, v92, -v94\n"
    "v_fma_f32 v217, v217, v93, -v95\n"
    "v_fma_f32 v218, v218, v92, -v94\n"
    "v_fma_f32 v219, v219, v93, -v95\n"
    "v_cvt_pk_bf16_f32 v216, v216, v217\n"
    "v_cvt_pk_bf16_f32 v218, v218, v219\n"
    "buffer_load_dwordx2 v[64:65], v242, s[28:31], s78 offen offset:3072 nt\n"
    "buffer_load_dwordx2 v[66:67], v242, s[28:31], s78 offen offset:3840 nt\n"
    "buffer_load_dwordx4 v[12:15], v162, s[28:31], s57 offen offset:128\n"
    "buffer_store_dword v216, v244, s[16:19], s54 offen offset:1280 sc1\n"
    "buffer_store_dword v218, v244, s[16:19], s54 offen offset:1408 sc1\n"
    "v_fma_f32 v220, v220, v92, -v94\n"
    "v_fma_f32 v221, v221, v93, -v95\n"
    "v_fma_f32 v222, v222, v92, -v94\n"
    "v_fma_f32 v223, v223, v93, -v95\n"
    "v_cvt_pk_bf16_f32 v220, v220, v221\n"
    "v_cvt_pk_bf16_f32 v222, v222, v223\n"
    "buffer_store_dword v220, v244, s[16:19], s55 offen offset:0 sc1\n"
    "buffer_store_dword v222, v244, s[16:19], s55 offen offset:128 sc1\n"
    "v_fma_f32 v224, v224, v92, -v94\n"
    "v_fma_f32 v225, v225, v93, -v95\n"
    "v_fma_f32 v226, v226, v92, -v94\n"
    "v_fma_f32 v227, v227, v93, -v95\n"
    "v_cvt_pk_bf16_f32 v224, v224, v225\n"
    "v_cvt_pk_bf16_f32 v226, v226, v227\n"
    "buffer_store_dword v224, v244, s[16:19], s55 offen offset:1024 sc1\n"
    "buffer_store_dword v226, v244, s[16:19], s55 offen offset:1152 sc1\n"
    "v_fma_f32 v228, v228, v92, -v94\n"
    "v_fma_f32 v229, v229, v93, -v95\n"
    "v_fma_f32 v230, v230, v92, -v94\n"
    "v_fma_f32 v231, v231, v93, -v95\n"
    "v_cvt_pk_bf16_f32 v228, v228, v229\n"
    "v_cvt_pk_bf16_f32 v230, v230, v231\n"
    "buffer_store_dword v228, v244, s[16:19], s55 offen offset:256 sc1\n"
    "buffer_store_dword v230, v244, s[16:19], s55 offen offset:384 sc1\n"
    "v_fma_f32 v232, v232, v92, -v94\n"
    "v_fma_f32 v233, v233, v93, -v95\n"
    "v_fma_f32 v234, v234, v92, -v94\n"
    "v_fma_f32 v235, v235, v93, -v95\n"
    "v_cvt_pk_bf16_f32 v232, v232, v233\n"
    "v_cvt_pk_bf16_f32 v234, v234, v235\n"
    "buffer_store_dword v232, v244, s[16:19], s55 offen offset:1280 sc1\n"
    "buffer_store_dword v234, v244, s[16:19], s55 offen offset:1408 sc1\n"
    "v_add_u32 v243, 512, v243\n"
    "s_add_u32 s71, s71, 98304\n"
    "s_add_u32 m0, s65, 24576\n"
    "s_add_u32 s72, s85, 0\n"
    "buffer_load_dwordx4 v88, s[20:23], s72 offen offset:0 nt lds\n"
    "buffer_load_dwordx4 v[16:19], v162, s[28:31], s57 offen offset:3200\n"
    "buffer_load_dwordx4 v[20:23], v162, s[28:31], s57 offen offset:256\n"
    "s_waitcnt vmcnt(38)\n"
    "v_add_co_u32 v238, vcc, 0x7f4a7c15, v238\n"
    "v_mov_b32 v159, 0x9e3779b9\n"
    "s_nop 0\n"
    "v_addc_co_u32 v239, vcc, v159, v239, vcc\n"
    "v_mov_b32 v246, 1\n"
    "s_waitcnt vmcnt(39)\n"
    "v_lshlrev_b32 v180, 16, v124\n"
    "v_and_b32 v181, 0xffff0000, v124\n"
    "v_lshlrev_b32 v182, 16, v125\n"
    "v_and_b32 v183, 0xffff0000, v125\n"
    "v_lshlrev_b32 v184, 16, v126\n"
    "v_and_b32 v185, 0xffff0000, v126\n"
    "v_lshlrev_b32 v186, 16, v127\n"
    "v_and_b32 v187, 0xffff0000, v127\n"
    "v_mul_f32 v180, s88, v180\n"
    "v_mul_f32 v181, s88, v181\n"
    "v_mul_f32 v182, s88, v182\n"
    "v_mul_f32 v183, s88, v183\n"
    "v_fmaak_f32 v184, v184, v180, 0x80730000\n"
    "v_fmaak_f32 v185, v185, v181, 0x80730000\n"
    "v_fmaak_f32 v186, v186, v182, 0x80730000\n"
    "v_fmaak_f32 v187, v187, v183, 0x80730000\n"
    "ds_write_b128 v131, v[180:183] offset:0\n"
    "ds_write_b128 v131, v[184:187] offset:1024\n"
    "ds_read_b128 v[180:183], v132 offset:0\n"
    "ds_read_b128 v[184:187], v132 offset:1024\n"
    "ds_read_b128 v[188:191], v132 offset:128\n"
    "ds_read_b128 v[192:195], v132 offset:1152\n"
    "ds_read_b128 v[196:199], v132 offset:256\n"
    "ds_read_b128 v[200:203], v132 offset:1280\n"
    "v_accvgpr_write_b32 a60, 0\n"
    "v_accvgpr_write_b32 a61, 0\n"
    "v_accvgpr_write_b32 a62, 0\n"
    "v_accvgpr_write_b32 a63, 0\n"
    "v_accvgpr_write_b32 a64, 0\n"
    "v_accvgpr_write_b32 a65, 0\n"
    "v_accvgpr_write_b32 a66, 0\n"
    "v_accvgpr_write_b32 a67, 0\n"
    "v_accvgpr_write_b32 a68, 0\n"
    "v_accvgpr_write_b32 a69, 0\n"
    "s_add_u32 m0, s65, 25600\n"
    "s_add_u32 s72, s85, 1024\n"
    "buffer_load_dwordx4 v89, s[20:23], s72 offen offset:0 nt lds\n"
    "buffer_load_dwordx4 v[24:27], v162, s[28:31], s57 offen offset:3328\n"
    "v_accvgpr_write_b32 a70, 0\n"
    "v_accvgpr_write_b32 a71, 0\n"
    "v_accvgpr_write_b32 a72, 0\n"
    "v_accvgpr_write_b32 a73, 0\n"
    "v_accvgpr_write_b32 a74, 0\n"
    "v_accvgpr_write_b32 a75, 0\n"
    "v_accvgpr_write_b32 a76, 0\n"
    "v_accvgpr_write_b32 a77, 0\n"
    "v_accvgpr_write_b32 a78, 0\n"
    "v_accvgpr_write_b32 a79, 0\n"
    "v_accvgpr_write_b32 a80, 0\n"
    "v_accvgpr_write_b32 a81, 0\n"
    "v_accvgpr_write_b32 a82, 0\n"
    "v_accvgpr_write_b32 a83, 0\n"
    "v_accvgpr_write_b32 a84, 0\n"
    "v_accvgpr_write_b32 a85, 0\n"
    "v_accvgpr_write_b32 a86, 0\n"
    "v_accvgpr_write_b32 a87, 0\n"
    "v_accvgpr_write_b32 a88, 0\n"
    "v_accvgpr_write_b32 a89, 0\n"
    "v_accvgpr_write_b32 a90, 0\n"
    "v_accvgpr_write_b32 a91, 0\n"
    "v_accvgpr_write_b32 a92, 0\n"
    "v_accvgpr_write_b32 a93, 0\n"
    "v_accvgpr_write_b32 a94, 0\n"
    "v_accvgpr_write_b32 a95, 0\n"
    "v_accvgpr_write_b32 a96, 0\n"
    "v_accvgpr_write_b32 a97, 0\n"
    "v_accvgpr_write_b32 a98, 0\n"
    "v_accvgpr_write_b32 a99, 0\n"
    "v_accvgpr_write_b32 a100, 0\n"
    "v_accvgpr_write_b32 a101, 0\n"
    "v_accvgpr_write_b32 a102, 0\n"
    "v_accvgpr_write_b32 a103, 0\n"
    "v_accvgpr_write_b32 a104, 0\n"
    "v_accvgpr_write_b32 a105, 0\n"
    "v_accvgpr_write_b32 a106, 0\n"
    "v_accvgpr_write_b32 a107, 0\n"
    "v_accvgpr_write_b32 a108, 0\n"
    "v_accvgpr_write_b32 a109, 0\n"
    "v_accvgpr_write_b32 a110, 0\n"
    "s_add_u32 m0, s65, 26624\n"
    "s_add_u32 s72, s85, 2048\n"
    "buffer_load_dwordx4 v90, s[20:23], s72 offen offset:0 nt lds\n"
    "buffer_load_dwordx4 v[100:103], v162, s[28:31], s66 offen offset:0\n"
    "buffer_load_dwordx4 v[104:107], v162, s[28:31], s66 offen offset:3072\n"
    "v_accvgpr_write_b32 a111, 0\n"
    "v_accvgpr_write_b32 a112, 0\n"
    "v_accvgpr_write_b32 a113, 0\n"
    "v_accvgpr_write_b32 a114, 0\n"
    "v_accvgpr_write_b32 a115, 0\n"
    "v_accvgpr_write_b32 a116, 0\n"
    "v_accvgpr_write_b32 a117, 0\n"
    "v_accvgpr_write_b32 a118, 0\n"
    "v_accvgpr_write_b32 a119, 0\n"
    "v_accvgpr_write_b32 a120, 0\n"
    "v_accvgpr_write_b32 a121, 0\n"
    "v_accvgpr_write_b32 a122, 0\n"
    "v_accvgpr_write_b32 a123, 0\n"
    "v_accvgpr_write_b32 a124, 0\n"
    "v_accvgpr_write_b32 a125, 0\n"
    "v_accvgpr_write_b32 a126, 0\n"
    "v_accvgpr_write_b32 a127, 0\n"
    "v_accvgpr_write_b32 a128, 0\n"
    "v_accvgpr_write_b32 a129, 0\n"
    "v_accvgpr_write_b32 a130, 0\n"
    "v_accvgpr_write_b32 a131, 0\n"
    "v_accvgpr_write_b32 a132, 0\n"
    "v_accvgpr_write_b32 a133, 0\n"
    "v_accvgpr_write_b32 a134, 0\n"
    "v_accvgpr_write_b32 a135, 0\n"
    "v_accvgpr_write_b32 a136, 0\n"
    "v_accvgpr_write_b32 a137, 0\n"
    "v_accvgpr_write_b32 a138, 0\n"
    "v_accvgpr_write_b32 a139, 0\n"
    "v_accvgpr_write_b32 a140, 0\n"
    "v_accvgpr_write_b32 a141, 0\n"
    "v_accvgpr_write_b32 a142, 0\n"
    "v_accvgpr_write_b32 a143, 0\n"
    "v_accvgpr_write_b32 a144, 0\n"
    "v_accvgpr_write_b32 a145, 0\n"
    "v_accvgpr_write_b32 a146, 0\n"
    "v_accvgpr_write_b32 a147, 0\n"
    "v_accvgpr_write_b32 a148, 0\n"
    "v_accvgpr_write_b32 a149, 0\n"
    "v_accvgpr_write_b32 a150, 0\n"
    "s_add_u32 m0, s65, 27648\n"
    "s_add_u32 s72, s85, 3072\n"
    "buffer_load_dwordx4 v88, s[20:23], s72 offen offset:0 nt lds\n"
    "buffer_load_dwordx4 v[108:111], v162, s[28:31], s66 offen offset:128\n"
    "v_accvgpr_write_b32 a151, 0\n"
    "v_accvgpr_write_b32 a152, 0\n"
    "v_accvgpr_write_b32 a153, 0\n"
    "v_accvgpr_write_b32 a154, 0\n"
    "v_accvgpr_write_b32 a155, 0\n"
    "v_accvgpr_write_b32 a156, 0\n"
    "v_accvgpr_write_b32 a157, 0\n"
    "v_accvgpr_write_b32 a158, 0\n"
    "v_accvgpr_write_b32 a159, 0\n"
    "v_accvgpr_write_b32 a160, 0\n"
    "v_accvgpr_write_b32 a161, 0\n"
    "v_accvgpr_write_b32 a162, 0\n"
    "v_accvgpr_write_b32 a163, 0\n"
    "v_accvgpr_write_b32 a164, 0\n"
    "v_accvgpr_write_b32 a165, 0\n"
    "v_accvgpr_write_b32 a166, 0\n"
    "v_accvgpr_write_b32 a167, 0\n"
    "v_accvgpr_write_b32 a168, 0\n"
    "v_accvgpr_write_b32 a169, 0\n"
    "v_accvgpr_write_b32 a170, 0\n"
    "v_accvgpr_write_b32 a171, 0\n"
    "v_accvgpr_write_b32 a172, 0\n"
    "v_accvgpr_write_b32 a173, 0\n"
    "v_accvgpr_write_b32 a174, 0\n"
    "v_accvgpr_write_b32 a175, 0\n"
    "v_accvgpr_write_b32 a176, 0\n"
    "v_accvgpr_write_b32 a177, 0\n"
    "v_accvgpr_write_b32 a178, 0\n"
    "v_accvgpr_write_b32 a179, 0\n"
    "v_accvgpr_write_b32 a180, 0\n"
    "v_accvgpr_write_b32 a181, 0\n"
    "v_accvgpr_write_b32 a182, 0\n"
    "v_accvgpr_write_b32 a183, 0\n"
    "v_accvgpr_write_b32 a184, 0\n"
    "v_accvgpr_write_b32 a185, 0\n"
    "v_accvgpr_write_b32 a186, 0\n"
    "v_accvgpr_write_b32 a187, 0\n"
    "v_accvgpr_write_b32 a188, 0\n"
    "v_accvgpr_write_b32 a189, 0\n"
    "v_accvgpr_write_b32 a190, 0\n"
    "v_accvgpr_write_b32 a191, 0\n"
    "s_waitcnt lgkmcnt(0)\n"
    "s_waitcnt vmcnt(10)\n"
    "ds_add_rtn_u32 v245, v247, v246\n"
    "s_lshl_b32 s73, s53, 6\n"
    "s_add_u32 s73, s73, 128\n"
    "s_waitcnt lgkmcnt(0)\n"
    "v_readfirstlane_b32 s74, v245\n"
    "s_nop 0\n"
    "s_cmp_eq_u32 s74, 3\n"
    "s_cselect_b32 s73, s73, 0x7fff0000\n"
    "buffer_store_dwordx2 v[238:239], v91, s[36:39], s73 offen offset:0 sc1\n"
    "s_waitcnt vmcnt(22)\n"
    "s_add_u32 m0, s65, 28672\n"
    "s_add_u32 s72, s85, 4096\n"
    "buffer_load_dwordx4 v89, s[20:23], s72 offen offset:0 nt lds\n"
    "buffer_load_dwordx4 v[112:115], v162, s[28:31], s66 offen offset:3200\n"
    "buffer_load_dwordx4 v[116:119], v162, s[28:31], s66 offen offset:256\n"
    "s_add_u32 s54, s71, s60\n"
    "s_add_u32 s55, s71, s61\n"
    "v_cvt_f32_i32 v36, v36\n"
    "v_cvt_f32_i32 v37, v37\n"
    "v_cvt_f32_i32 v38, v38\n"
    "v_cvt_f32_i32 v39, v39\n"
    "v_cvt_f32_i32 v40, v40\n"
    "v_cvt_f32_i32 v41, v41\n"
    "v_cvt_f32_i32 v42, v42\n"
    "v_cvt_f32_i32 v43, v43\n"
    "v_cvt_f32_i32 v44, v44\n"
    "v_cvt_f32_i32 v45, v45\n"
    "v_cvt_f32_i32 v46, v46\n"
    "v_cvt_f32_i32 v47, v47\n"
    "ds_read_b128 v[92:95], v243 offset:0\n"
    "v_cvt_f32_i32 v48, v48\n"
    "v_cvt_f32_i32 v49, v49\n"
    "v_cvt_f32_i32 v50, v50\n"
    "v_cvt_f32_i32 v51, v51\n"
    "v_cvt_f32_i32 v52, v52\n"
    "v_cvt_f32_i32 v53, v53\n"
    "v_cvt_f32_i32 v54, v54\n"
    "v_cvt_f32_i32 v55, v55\n"
    "v_cvt_f32_i32 v56, v56\n"
    "v_cvt_f32_i32 v57, v57\n"
    "v_cvt_f32_i32 v58, v58\n"
    "v_cvt_f32_i32 v59, v59\n"
    "s_waitcnt lgkmcnt(0)\n"
    "v_fma_f32 v36, v36, v92, -v94\n"
    "v_fma_f32 v37, v37, v93, -v95\n"
    "v_fma_f32 v38, v38, v92, -v94\n"
    "v_fma_f32 v39, v39, v93, -v95\n"
    "v_cvt_pk_bf16_f32 v36, v36, v37\n"
    "v_cvt_pk_bf16_f32 v38, v38, v39\n"
    "v_cvt_f32_i32 v60, v60\n"
    "v_cvt_f32_i32 v61, v61\n"
    "v_cvt_f32_i32 v62, v62\n"
    "v_cvt_f32_i32 v63, v63\n"
    "buffer_store_dword v36, v244, s[16:19], s54 offen offset:0 sc1\n"
    "buffer_store_dword v38, v244, s[16:19], s54 offen offset:128 sc1\n"
    "v_fma_f32 v40, v40, v92, -v94\n"
    "v_fma_f32 v41, v41, v93, -v95\n"
    "v_fma_f32 v42, v42, v92, -v94\n"
    "v_fma_f32 v43, v43, v93, -v95\n"
    "v_cvt_pk_bf16_f32 v40, v40, v41\n"
    "v_cvt_pk_bf16_f32 v42, v42, v43\n"
    "v_cvt_f32_i32 v64, v64\n"
    "v_cvt_f32_i32 v65, v65\n"
    "v_cvt_f32_i32 v66, v66\n"
    "v_cvt_f32_i32 v67, v67\n"
    "s_add_u32 m0, s65, 29696\n"
    "s_add_u32 s72, s85, 5120\n"
    "buffer_load_dwordx4 v90, s[20:23], s72 offen offset:0 nt lds\n"
    "buffer_load_dwordx4 v[120:123], v162, s[28:31], s66 offen offset:3328\n"
    "buffer_store_dword v40, v244, s[16:19], s54 offen offset:1024 sc1\n"
    "buffer_store_dword v42, v244, s[16:19], s54 offen offset:1152 sc1\n"
    "v_fma_f32 v44, v44, v92, -v94\n"
    "v_fma_f32 v45, v45, v93, -v95\n"
    "v_fma_f32 v46, v46, v92, -v94\n"
    "v_fma_f32 v47, v47, v93, -v95\n"
    "v_cvt_pk_bf16_f32 v44, v44, v45\n"
    "v_cvt_pk_bf16_f32 v46, v46, v47\n"
    "buffer_store_dword v44, v244, s[16:19], s54 offen offset:256 sc1\n"
    "buffer_store_dword v46, v244, s[16:19], s54 offen offset:384 sc1\n"
    "v_fma_f32 v48, v48, v92, -v94\n"
    "v_fma_f32 v49, v49, v93, -v95\n"
    "v_fma_f32 v50, v50, v92, -v94\n"
    "v_fma_f32 v51, v51, v93, -v95\n"
    "v_cvt_pk_bf16_f32 v48, v48, v49\n"
    "v_cvt_pk_bf16_f32 v50, v50, v51\n"
    "buffer_store_dword v48, v244, s[16:19], s54 offen offset:1280 sc1\n"
    "buffer_store_dword v50, v244, s[16:19], s54 offen offset:1408 sc1\n"
    "v_fma_f32 v52, v52, v92, -v94\n"
    "v_fma_f32 v53, v53, v93, -v95\n"
    "v_fma_f32 v54, v54, v92, -v94\n"
    "v_fma_f32 v55, v55, v93, -v95\n"
    "v_cvt_pk_bf16_f32 v52, v52, v53\n"
    "v_cvt_pk_bf16_f32 v54, v54, v55\n"
    "buffer_store_dword v52, v244, s[16:19], s55 offen offset:0 sc1\n"
    "buffer_store_dword v54, v244, s[16:19], s55 offen offset:128 sc1\n"
    "v_fma_f32 v56, v56, v92, -v94\n"
    "v_fma_f32 v57, v57, v93, -v95\n"
    "v_fma_f32 v58, v58, v92, -v94\n"
    "v_fma_f32 v59, v59, v93, -v95\n"
    "v_cvt_pk_bf16_f32 v56, v56, v57\n"
    "v_cvt_pk_bf16_f32 v58, v58, v59\n"
    "buffer_store_dword v56, v244, s[16:19], s55 offen offset:1024 sc1\n"
    "buffer_store_dword v58, v244, s[16:19], s55 offen offset:1152 sc1\n"
    "v_fma_f32 v60, v60, v92, -v94\n"
    "v_fma_f32 v61, v61, v93, -v95\n"
    "v_fma_f32 v62, v62, v92, -v94\n"
    "v_fma_f32 v63, v63, v93, -v95\n"
    "v_cvt_pk_bf16_f32 v60, v60, v61\n"
    "v_cvt_pk_bf16_f32 v62, v62, v63\n"
    "buffer_store_dword v60, v244, s[16:19], s55 offen offset:256 sc1\n"
    "buffer_store_dword v62, v244, s[16:19], s55 offen offset:384 sc1\n"
    "v_fma_f32 v64, v64, v92, -v94\n"
    "v_fma_f32 v65, v65, v93, -v95\n"
    "v_fma_f32 v66, v66, v92, -v94\n"
    "v_fma_f32 v67, v67, v93, -v95\n"
    "v_cvt_pk_bf16_f32 v64, v64, v65\n"
    "v_cvt_pk_bf16_f32 v66, v66, v67\n"
    "buffer_store_dword v64, v244, s[16:19], s55 offen offset:1280 sc1\n"
    "buffer_store_dword v66, v244, s[16:19], s55 offen offset:1408 sc1\n"
    "v_add_u32 v243, 512, v243\n"
    "s_add_u32 m0, s65, 73728\n"
    "s_add_u32 s72, s85, 6144\n"
    "buffer_load_dwordx4 v88, s[20:23], s72 offen offset:0 nt lds\n"
    "s_waitcnt vmcnt(56)\n"
    "v_lshlrev_b32 v204, 16, v136\n"
    "v_and_b32 v205, 0xffff0000, v136\n"
    "v_lshlrev_b32 v206, 16, v137\n"
    "v_and_b32 v207, 0xffff0000, v137\n"
    "v_lshlrev_b32 v208, 16, v138\n"
    "v_and_b32 v209, 0xffff0000, v138\n"
    "v_lshlrev_b32 v210, 16, v139\n"
    "v_and_b32 v211, 0xffff0000, v139\n"
    "v_log_f32 v68, v204\n"
    "v_log_f32 v69, v205\n"
    "v_log_f32 v70, v206\n"
    "v_log_f32 v71, v207\n"
    "v_log_f32 v72, v208\n"
    "v_log_f32 v73, v209\n"
    "v_log_f32 v74, v210\n"
    "v_log_f32 v75, v211\n"
    "v_rcp_f32 v204, v204\n"
    "v_rcp_f32 v205, v205\n"
    "v_rcp_f32 v206, v206\n"
    "v_rcp_f32 v207, v207\n"
    "v_rcp_f32 v208, v208\n"
    "v_rcp_f32 v209, v209\n"
    "v_rcp_f32 v210, v210\n"
    "v_rcp_f32 v211, v211\n"
    "s_mov_b32 s73, 0x41a00000\n"
    "v_mov_b32 v228, 0x7fc00000\n"
    "v_cmp_le_f32 vcc, |v68|, s73\n"
    "s_nop 1\n"
    "v_cndmask_b32 v68, v228, v68, vcc\n"
    "v_cmp_le_f32 vcc, |v69|, s73\n"
    "s_nop 1\n"
    "v_cndmask_b32 v69, v228, v69, vcc\n"
    "v_cmp_le_f32 vcc, |v70|, s73\n"
    "s_nop 1\n"
    "v_cndmask_b32 v70, v228, v70, vcc\n"
    "v_cmp_le_f32 vcc, |v71|, s73\n"
    "s_nop 1\n"
    "v_cndmask_b32 v71, v228, v71, vcc\n"
    "s_add_u32 m0, s65, 74752\n"
    "s_add_u32 s72, s85, 7168\n"
    "buffer_load_dwordx4 v89, s[20:23], s72 offen offset:0 nt lds\n"
    "v_cmp_le_f32 vcc, |v72|, s73\n"
    "s_nop 1\n"
    "v_cndmask_b32 v72, v228, v72, vcc\n"
    "v_cmp_le_f32 vcc, |v73|, s73\n"
    "s_nop 1\n"
    "v_cndmask_b32 v73, v228, v73, vcc\n"
    "v_cmp_le_f32 vcc, |v74|, s73\n"
    "s_nop 1\n"
    "v_cndmask_b32 v74, v228, v74, vcc\n"
    "v_cmp_le_f32 vcc, |v75|, s73\n"
    "s_nop 1\n"
    "v_cndmask_b32 v75, v228, v75, vcc\n"
    "v_cvt_pk_bf16_f32 v220, v204, v205\n"
    "v_cvt_pk_bf16_f32 v221, v206, v207\n"
    "v_cvt_pk_bf16_f32 v222, v208, v209\n"
    "v_cvt_pk_bf16_f32 v223, v210, v211\n"
    "v_lshlrev_b32 v212, 16, v220\n"
    "v_and_b32 v213, 0xffff0000, v220\n"
    "v_lshlrev_b32 v214, 16, v221\n"
    "v_and_b32 v215, 0xffff0000, v221\n"
    "v_lshlrev_b32 v216, 16, v222\n"
    "v_and_b32 v217, 0xffff0000, v222\n"
    "v_lshlrev_b32 v218, 16, v223\n"
    "v_and_b32 v219, 0xffff0000, v223\n"
    "v_sub_f32 v204, v204, v212\n"
    "v_sub_f32 v205, v205, v213\n"
    "v_sub_f32 v206, v206, v214\n"
    "v_sub_f32 v207, v207, v215\n"
    "v_sub_f32 v208, v208, v216\n"
    "v_sub_f32 v209, v209, v217\n"
    "v_sub_f32 v210, v210, v218\n"
    "v_sub_f32 v211, v211, v219\n"
    "v_cvt_pk_bf16_f32 v224, v204, v205\n"
    "v_cvt_pk_bf16_f32 v225, v206, v207\n"
    "v_cvt_pk_bf16_f32 v226, v208, v209\n"
    "v_cvt_pk_bf16_f32 v227, v210, v211\n"
    "v_mov_b32 v204, 0\n"
    "v_mov_b32 v205, 0\n"
    "v_mov_b32 v206, 0\n"
    "s_add_u32 m0, s65, 75776\n"
    "s_add_u32 s72, s85, 8192\n"
    "buffer_load_dwordx4 v90, s[20:23], s72 offen offset:0 nt lds\n"
    "v_mov_b32 v207, 0\n"
    "v_swap_b32 v221, v222\n"
    "v_swap_b32 v225, v226\n"
    "v_swap_b32 v141, v142\n"
    "ds_write2_b64 v144, v[220:221], v[222:223] offset0:32 offset1:40\n"
    "ds_write2_b64 v144, v[224:225], v[226:227] offset0:48 offset1:56\n"
    "ds_write2_b64 v144, v[140:141], v[142:143] offset0:64 offset1:72\n"
    "ds_write2_b64 v144, v[204:205], v[206:207] offset0:80 offset1:88\n"
    "s_waitcnt vmcnt(30)\n"
    "v_cvt_f32_i32 v4, v4\n"
    "v_cvt_f32_i32 v5, v5\n"
    "v_cvt_f32_i32 v6, v6\n"
    "v_cvt_f32_i32 v7, v7\n"
    "v_cvt_f32_i32 v8, v8\n"
    "v_cvt_f32_i32 v9, v9\n"
    "v_cvt_f32_i32 v10, v10\n"
    "v_cvt_f32_i32 v11, v11\n"
    "v_cvt_f32_i32 v12, v12\n"
    "v_cvt_f32_i32 v13, v13\n"
    "v_cvt_f32_i32 v14, v14\n"
    "v_cvt_f32_i32 v15, v15\n"
    "v_cvt_f32_i32 v16, v16\n"
    "v_cvt_f32_i32 v17, v17\n"
    "v_cvt_f32_i32 v18, v18\n"
    "v_cvt_f32_i32 v19, v19\n"
    "v_cvt_f32_i32 v20, v20\n"
    "v_cvt_f32_i32 v21, v21\n"
    "v_cvt_f32_i32 v22, v22\n"
    "v_cvt_f32_i32 v23, v23\n"
    "v_cvt_f32_i32 v24, v24\n"
    "v_cvt_f32_i32 v25, v25\n"
    "v_cvt_f32_i32 v26, v26\n"
    "v_cvt_f32_i32 v27, v27\n"
    "v_fma_f32 v4, v4, v180, -v184\n"
    "v_fma_f32 v5, v5, v181, -v185\n"
    "v_fma_f32 v6, v6, v182, -v186\n"
    "s_add_u32 m0, s65, 76800\n"
    "s_add_u32 s72, s85, 9216\n"
    "buffer_load_dwordx4 v88, s[20:23], s72 offen offset:0 nt lds\n"
    "v_fma_f32 v7, v7, v183, -v187\n"
    "v_fma_f32 v8, v8, v180, -v184\n"
    "v_fma_f32 v9, v9, v181, -v185\n"
    "v_fma_f32 v10, v10, v182, -v186\n"
    "v_fma_f32 v11, v11, v183, -v187\n"
    "v_cvt_pk_bf16_f32 v4, v4, v5\n"
    "v_cvt_pk_bf16_f32 v5, v6, v7\n"
    "v_cvt_pk_bf16_f32 v6, v8, v9\n"
    "v_cvt_pk_bf16_f32 v7, v10, v11\n"
    "ds_write_b64 v163, v[4:5] offset:0\n"
    "ds_write_b64 v163, v[6:7] offset:1024\n"
    "v_fma_f32 v12, v12, v188, -v192\n"
    "v_fma_f32 v13, v13, v189, -v193\n"
    "v_fma_f32 v14, v14, v190, -v194\n"
    "v_fma_f32 v15, v15, v191, -v195\n"
    "v_fma_f32 v16, v16, v188, -v192\n"
    "v_fma_f32 v17, v17, v189, -v193\n"
    "v_fma_f32 v18, v18, v190, -v194\n"
    "v_fma_f32 v19, v19, v191, -v195\n"
    "v_cvt_pk_bf16_f32 v12, v12, v13\n"
    "v_cvt_pk_bf16_f32 v13, v14, v15\n"
    "v_cvt_pk_bf16_f32 v14, v16, v17\n"
    "v_cvt_pk_bf16_f32 v15, v18, v19\n"
    "ds_write_b64 v163, v[12:13] offset:2048\n"
    "ds_write_b64 v163, v[14:15] offset:3072\n"
    "v_fma_f32 v20, v20, v196, -v200\n"
    "v_fma_f32 v21, v21, v197, -v201\n"
    "v_fma_f32 v22, v22, v198, -v202\n"
    "v_fma_f32 v23, v23, v199, -v203\n"
    "v_fma_f32 v24, v24, v196, -v200\n"
    "v_fma_f32 v25, v25, v197, -v201\n"
    "v_fma_f32 v26, v26, v198, -v202\n"
    "v_fma_f32 v27, v27, v199, -v203\n"
    "v_cvt_pk_bf16_f32 v20, v20, v21\n"
    "v_cvt_pk_bf16_f32 v21, v22, v23\n"
    "s_add_u32 m0, s65, 77824\n"
    "s_add_u32 s72, s85, 10240\n"
    "buffer_load_dwordx4 v89, s[20:23], s72 offen offset:0 nt lds\n"
    "v_cvt_pk_bf16_f32 v22, v24, v25\n"
    "v_cvt_pk_bf16_f32 v23, v26, v27\n"
    "ds_write_b64 v163, v[20:21] offset:4096\n"
    "ds_write_b64 v163, v[22:23] offset:5120\n"
    "s_waitcnt vmcnt(19)\n"
    "v_cvt_f32_i32 v100, v100\n"
    "v_cvt_f32_i32 v101, v101\n"
    "v_cvt_f32_i32 v102, v102\n"
    "v_cvt_f32_i32 v103, v103\n"
    "v_cvt_f32_i32 v104, v104\n"
    "v_cvt_f32_i32 v105, v105\n"
    "v_cvt_f32_i32 v106, v106\n"
    "v_cvt_f32_i32 v107, v107\n"
    "v_cvt_f32_i32 v108, v108\n"
    "v_cvt_f32_i32 v109, v109\n"
    "v_cvt_f32_i32 v110, v110\n"
    "v_cvt_f32_i32 v111, v111\n"
    "v_cvt_f32_i32 v112, v112\n"
    "v_cvt_f32_i32 v113, v113\n"
    "v_cvt_f32_i32 v114, v114\n"
    "v_cvt_f32_i32 v115, v115\n"
    "v_cvt_f32_i32 v116, v116\n"
    "v_cvt_f32_i32 v117, v117\n"
    "v_cvt_f32_i32 v118, v118\n"
    "v_cvt_f32_i32 v119, v119\n"
    "v_cvt_f32_i32 v120, v120\n"
    "v_cvt_f32_i32 v121, v121\n"
    "v_cvt_f32_i32 v122, v122\n"
    "v_cvt_f32_i32 v123, v123\n"
    "v_fma_f32 v100, v100, v180, -v184\n"
    "v_fma_f32 v101, v101, v181, -v185\n"
    "v_fma_f32 v102, v102, v182, -v186\n"
    "v_fma_f32 v103, v103, v183, -v187\n"
    "v_fma_f32 v104, v104, v180, -v184\n"
    "v_fma_f32 v105, v105, v181, -v185\n"
    "s_add_u32 m0, s65, 78848\n"
    "s_add_u32 s72, s85, 11264\n"
    "buffer_load_dwordx4 v90, s[20:23], s72 offen offset:0 nt lds\n"
    "v_fma_f32 v106, v106, v182, -v186\n"
    "v_fma_f32 v107, v107, v183, -v187\n"
    "v_cvt_pk_bf16_f32 v100, v100, v101\n"
    "v_cvt_pk_bf16_f32 v101, v102, v103\n"
    "v_cvt_pk_bf16_f32 v102, v104, v105\n"
    "v_cvt_pk_bf16_f32 v103, v106, v107\n"
    "ds_write_b64 v163, v[100:101] offset:12288\n"
    "ds_write_b64 v163, v[102:103] offset:13312\n"
    "v_fma_f32 v108, v108, v188, -v192\n"
    "v_fma_f32 v109, v109, v189, -v193\n"
    "v_fma_f32 v110, v110, v190, -v194\n"
    "v_fma_f32 v111, v111, v191, -v195\n"
    "v_fma_f32 v112, v112, v188, -v192\n"
    "v_fma_f32 v113, v113, v189, -v193\n"
    "v_fma_f32 v114, v114, v190, -v194\n"
    "v_fma_f32 v115, v115, v191, -v195\n"
    "v_cvt_pk_bf16_f32 v108, v108, v109\n"
    "v_cvt_pk_bf16_f32 v109, v110, v111\n"
    "v_cvt_pk_bf16_f32 v110, v112, v113\n"
    "v_cvt_pk_bf16_f32 v111, v114, v115\n"
    "ds_write_b64 v163, v[108:109] offset:14336\n"
    "ds_write_b64 v163, v[110:111] offset:15360\n"
    "v_fma_f32 v116, v116, v196, -v200\n"
    "v_fma_f32 v117, v117, v197, -v201\n"
    "v_fma_f32 v118, v118, v198, -v202\n"
    "v_fma_f32 v119, v119, v199, -v203\n"
    "v_fma_f32 v120, v120, v196, -v200\n"
    "v_fma_f32 v121, v121, v197, -v201\n"
    "v_fma_f32 v122, v122, v198, -v202\n"
    "v_fma_f32 v123, v123, v199, -v203\n"
    "v_cvt_pk_bf16_f32 v116, v116, v117\n"
    "v_cvt_pk_bf16_f32 v117, v118, v119\n"
    "v_cvt_pk_bf16_f32 v118, v120, v121\n"
    "v_cvt_pk_bf16_f32 v119, v122, v123\n"
    "ds_write_b64 v163, v[116:117] offset:16384\n"
    "ds_write_b64 v163, v[118:119] offset:17408\n"
    "s_waitcnt vmcnt(21)\n"
    "ds_read_b128 v[36:39], v154 offset:0\n"
    "ds_read_b128 v[40:43], v155 offset:0\n"
    "ds_read_b128 v[44:47], v154 offset:128\n"
    "ds_read_b128 v[48:51], v155 offset:128\n"
    "ds_read_b128 v[52:55], v154 offset:256\n"
    "ds_read_b128 v[56:59], v155 offset:256\n"
    "s_waitcnt lgkmcnt(0)\n"
    "s_barrier\n"
    "ds_read_b128 v[156:159], v2 offset:0\n"
    "ds_read_b128 v[160:163], v2 offset:2048\n"
    "ds_read_b128 v[164:167], v2 offset:4096\n"
    "ds_read_b128 v[168:171], v2 offset:6144\n"
    "ds_read_b128 v[172:175], v2 offset:8192\n"
    "ds_read_b128 v[176:179], v2 offset:10240\n"
    "ds_read_b128 v[180:183], v2 offset:1024\n"
    "ds_read_b128 v[184:187], v2 offset:3072\n"
    "ds_read_b128 v[188:191], v2 offset:5120\n"
    "ds_read_b128 v[192:195], v2 offset:7168\n"
    "ds_read_b128 v[196:199], v2 offset:9216\n"
    "ds_read_b128 v[200:203], v2 offset:11264\n"
    "s_add_u32 m0, s65, 49152\n"
    "s_add_u32 s72, s85, 12288\n"
    "buffer_load_dwordx4 v88, s[20:23], s72 offen offset:0 nt lds\n"
    "v_pk_maximum3_f16 v80, v36, v37, v80\n"
    "v_pk_maximum3_f16 v80, v38, v39, v80\n"
    "v_pk_add_u16 v36, s89, v36 clamp\n"
    "v_pk_add_u16 v37, s89, v37 clamp\n"
    "v_pk_add_u16 v38, s89, v38 clamp\n"
    "v_pk_add_u16 v39, s89, v39 clamp\n"
    "v_pk_maximum3_f16 v80, v40, v41, v80\n"
    "v_pk_maximum3_f16 v80, v42, v43, v80\n"
    "v_pk_add_u16 v40, s89, v40 clamp\n"
    "v_pk_add_u16 v41, s89, v41 clamp\n"
    "v_pk_add_u16 v42, s89, v42 clamp\n"
    "v_pk_add_u16 v43, s89, v43 clamp\n"
    "v_pk_maximum3_f16 v80, v44, v45, v80\n"
    "v_pk_maximum3_f16 v80, v46, v47, v80\n"
    "v_pk_add_u16 v44, s89, v44 clamp\n"
    "v_pk_add_u16 v45, s89, v45 clamp\n"
    "v_pk_add_u16 v46, s89, v46 clamp\n"
    "v_pk_add_u16 v47, s89, v47 clamp\n"
    "v_pk_maximum3_f16 v80, v48, v49, v80\n"
    "v_pk_maximum3_f16 v80, v50, v51, v80\n"
    "v_pk_add_u16 v48, s89, v48 clamp\n"
    "v_pk_add_u16 v49, s89, v49 clamp\n"
    "v_pk_add_u16 v50, s89, v50 clamp\n"
    "v_pk_add_u16 v51, s89, v51 clamp\n"
    "v_pk_maximum3_f16 v80, v52, v53, v80\n"
    "v_pk_maximum3_f16 v80, v54, v55, v80\n"
    "v_pk_add_u16 v52, s89, v52 clamp\n"
    "v_pk_add_u16 v53, s89, v53 clamp\n"
    "v_pk_add_u16 v54, s89, v54 clamp\n"
    "v_pk_add_u16 v55, s89, v55 clamp\n"
    "v_pk_maximum3_f16 v80, v56, v57, v80\n"
    "v_pk_maximum3_f16 v80, v58, v59, v80\n"
    "v_pk_add_u16 v56, s89, v56 clamp\n"
    "v_pk_add_u16 v57, s89, v57 clamp\n"
    "v_pk_add_u16 v58, s89, v58 clamp\n"
    "v_pk_add_u16 v59, s89, v59 clamp\n"
    "v_mov_b32 v108, 0\n"
    "v_mov_b32 v109, 0\n"
    "v_mov_b32 v110, 0\n"
    "v_mov_b32 v111, 0\n"
    "v_mov_b32 v112, 0\n"
    "v_mov_b32 v113, 0\n"
    "v_mov_b32 v114, 0\n"
    "v_mov_b32 v115, 0\n"
    "v_mov_b32 v116, 0\n"
    "v_mov_b32 v117, 0\n"
    "v_mov_b32 v118, 0\n"
    "v_mov_b32 v119, 0\n"
    "v_mov_b32 v120, 0\n"
    "v_mov_b32 v121, 0\n"
    "v_mov_b32 v122, 0\n"
    "v_mov_b32 v123, 0\n"
    "s_waitcnt lgkmcnt(11)\n"
    "v_mfma_f32_16x16x32_bf16 v[4:7], v[156:159], v[36:39], 0\n"
    "s_waitcnt lgkmcnt(10)\n"
    "v_mfma_f32_16x16x32_bf16 v[4:7], v[160:163], v[40:43], v[4:7]\n"
    "s_waitcnt lgkmcnt(9)\n"
    "v_mfma_f32_16x16x32_bf16 v[4:7], v[164:167], v[44:47], v[4:7]\n"
    "s_waitcnt lgkmcnt(8)\n"
    "v_mfma_f32_16x16x32_bf16 v[4:7], v[168:171], v[48:51], v[4:7]\n"
    "s_waitcnt lgkmcnt(7)\n"
    "v_mfma_f32_16x16x32_bf16 v[4:7], v[172:175], v[52:55], v[4:7]\n"
    "s_waitcnt lgkmcnt(6)\n"
    "v_mfma_f32_16x16x32_bf16 v[4:7], v[176:179], v[56:59], v[4:7]\n"
    "s_waitcnt lgkmcnt(5)\n"
    "v_mfma_f32_16x16x32_bf16 v[20:23], v[180:183], v[36:39], 0\n"
    "v_accvgpr_write_b32 a192, v36\n"
    "v_accvgpr_write_b32 a193, v37\n"
    "v_accvgpr_write_b32 a194, v38\n"
    "v_accvgpr_write_b32 a195, v39\n"
    "s_waitcnt lgkmcnt(4)\n"
    "v_mfma_f32_16x16x32_bf16 v[20:23], v[184:187], v[40:43], v[20:23]\n"
    "v_accvgpr_write_b32 a196, v40\n"
    "v_accvgpr_write_b32 a197, v41\n"
    "v_accvgpr_write_b32 a198, v42\n"
    "v_accvgpr_write_b32 a199, v43\n"
    "s_waitcnt lgkmcnt(3)\n"
    "v_mfma_f32_16x16x32_bf16 v[20:23], v[188:191], v[44:47], v[20:23]\n"
    "v_accvgpr_write_b32 a200, v44\n"
    "v_accvgpr_write_b32 a201, v45\n"
    "v_accvgpr_write_b32 a202, v46\n"
    "v_accvgpr_write_b32 a203, v47\n"
    "s_waitcnt lgkmcnt(2)\n"
    "v_mfma_f32_16x16x32_bf16 v[20:23], v[192:195], v[48:51], v[20:23]\n"
    "v_accvgpr_write_b32 a204, v48\n"
    "v_accvgpr_write_b32 a205, v49\n"
    "v_accvgpr_write_b32 a206, v50\n"
    "v_accvgpr_write_b32 a207, v51\n"
    "s_waitcnt lgkmcnt(1)\n"
    "v_mfma_f32_16x16x32_bf16 v[20:23], v[196:199], v[52:55], v[20:23]\n"
    "v_accvgpr_write_b32 a208, v52\n"
    "v_accvgpr_write_b32 a209, v53\n"
    "v_accvgpr_write_b32 a210, v54\n"
    "v_accvgpr_write_b32 a211, v55\n"
    "s_waitcnt lgkmcnt(0)\n"
    "v_mfma_f32_16x16x32_bf16 v[20:23], v[200:203], v[56:59], v[20:23]\n"
    "v_accvgpr_write_b32 a212, v56\n"
    "v_accvgpr_write_b32 a213, v57\n"
    "v_accvgpr_write_b32 a214, v58\n"
    "v_accvgpr_write_b32 a215, v59\n"
    "s_add_u32 m0, s65, 50176\n"
    "s_add_u32 s72, s85, 13312\n"
    "buffer_load_dwordx4 v89, s[20:23], s72 offen offset:0 nt lds\n"
    "v_mov_b32 v77, 155904\n"
    "v_max3_f32 v76, v4, v5, v6\n"
    "v_max3_f32 v76, v76, v7, v20\n"
    "v_max3_f32 v76, v76, v21, v22\n"
    "v_max_f32 v76, v76, v23\n"
    "v_mov_b32 v78, v76\n"
    "s_nop 1\n"
    "v_permlane32_swap_b32 v76, v78\n"
    "v_max_f32 v76, v76, v78\n"
    "v_mov_b32 v78, v76\n"
    "s_nop 1\n"
    "v_permlane16_swap_b32 v76, v78\n"
    "v_max_f32 v76, v76, v78\n"
    "ds_max_f32 v77, v76\n"
    "s_add_u32 m0, s65, 51200\n"
    "s_add_u32 s72, s85, 14336\n"
    "buffer_load_dwordx4 v90, s[20:23], s72 offen offset:0 nt lds\n"
    "buffer_load_dwordx2 v[236:237], v87, s[36:39], 0 offen offset:0 sc1\n"
    "s_waitcnt lgkmcnt(0)\n"
    "s_barrier\n"
    "ds_read_b32 v76, v77\n"
    "s_waitcnt vmcnt(10)\n"
    "buffer_store_dwordx2 v[238:239], v240, s[36:39], s80 offen offset:0 sc1\n"
    "s_lshl_b32 s73, s79, 8\n"
    "s_add_u32 s80, s80, s73\n"
    "s_mul_i32 s73, s79, 98304\n"
    "s_add_u32 s71, s71, s73\n"
    "s_waitcnt lgkmcnt(0)\n"
    "v_add_f32 v76, 0xc2b00000, v76\n"
    "v_sub_f32 v68, v68, v76\n"
    "v_sub_f32 v69, v69, v76\n"
    "v_sub_f32 v70, v70, v76\n"
    "v_sub_f32 v71, v71, v76\n"
    "v_sub_f32 v72, v72, v76\n"
    "v_sub_f32 v73, v73, v76\n"
    "v_sub_f32 v74, v74, v76\n"
    "v_sub_f32 v75, v75, v76\n"
    "ds_write_b128 v86, v[68:71] offset:0\n"
    "ds_write_b128 v86, v[72:75] offset:16\n"
    "s_mul_i32 s73, s79, 768\n"
    "v_add_u32 v77, s73, v250\n"
    "s_add_u32 m0, s65, 52224\n"
    "s_add_u32 s72, s85, 15360\n"
    "buffer_load_dwordx4 v88, s[20:23], s72 offen offset:0 nt lds\n"
    "s_waitcnt lgkmcnt(0)\n"
    "s_barrier\n"
    "ds_read_b128 v[100:103], v77 offset:0\n"
    "ds_read_b128 v[104:107], v77 offset:16\n"
    "s_add_u32 m0, s65, 53248\n"
    "s_add_u32 s72, s85, 16384\n"
    "buffer_load_dwordx4 v89, s[20:23], s72 offen offset:0 nt lds\n"
    "s_waitcnt lgkmcnt(1)\n"
    "v_add_f32 v4, v4, v100\n"
    "v_add_f32 v5, v5, v101\n"
    "v_add_f32 v6, v6, v102\n"
    "v_add_f32 v7, v7, v103\n"
    "s_waitcnt lgkmcnt(0)\n"
    "v_add_f32 v20, v20, v104\n"
    "v_add_f32 v21, v21, v105\n"
    "v_add_f32 v22, v22, v106\n"
    "v_add_f32 v23, v23, v107\n"
    "v_exp_f32 v4, v4\n"
    "v_exp_f32 v5, v5\n"
    "v_exp_f32 v6, v6\n"
    "v_exp_f32 v7, v7\n"
    "v_exp_f32 v20, v20\n"
    "v_exp_f32 v21, v21\n"
    "v_exp_f32 v22, v22\n"
    "v_exp_f32 v23, v23\n"
    "s_add_u32 m0, s65, 54272\n"
    "s_add_u32 s72, s85, 17408\n"
    "buffer_load_dwordx4 v90, s[20:23], s72 offen offset:0 nt lds\n"
    "s_waitcnt vmcnt(8)\n"
    "ds_read_b128 v[56:59], v85 offset:24832\n"
    "ds_read_b128 v[52:55], v84 offset:24832\n"
    "ds_read_b128 v[48:51], v85 offset:24704\n"
    "ds_read_b128 v[44:47], v84 offset:24704\n"
    "ds_read_b128 v[40:43], v85 offset:24576\n"
    "ds_read_b128 v[36:39], v84 offset:24576\n"
    "s_waitcnt lgkmcnt(5)\n"
    "v_pk_maximum3_f16 v80, v56, v57, v80\n"
    "v_pk_maximum3_f16 v80, v58, v59, v80\n"
    "v_pk_add_u16 v56, s89, v56 clamp\n"
    "v_pk_add_u16 v57, s89, v57 clamp\n"
    "v_pk_add_u16 v58, s89, v58 clamp\n"
    "v_pk_add_u16 v59, s89, v59 clamp\n"
    "s_waitcnt lgkmcnt(4)\n"
    "v_pk_maximum3_f16 v80, v52, v53, v80\n"
    "v_pk_maximum3_f16 v80, v54, v55, v80\n"
    "v_pk_add_u16 v52, s89, v52 clamp\n"
    "v_pk_add_u16 v53, s89, v53 clamp\n"
    "v_pk_add_u16 v54, s89, v54 clamp\n"
    "v_pk_add_u16 v55, s89, v55 clamp\n"
    "v_mfma_f32_16x16x32_bf16 v[8:11], v[176:179], v[56:59], v[100:103]\n"
    "s_waitcnt lgkmcnt(3)\n"
    "v_pk_maximum3_f16 v80, v48, v49, v80\n"
    "v_pk_maximum3_f16 v80, v50, v51, v80\n"
    "v_pk_add_u16 v48, s89, v48 clamp\n"
    "v_pk_add_u16 v49, s89, v49 clamp\n"
    "v_pk_add_u16 v50, s89, v50 clamp\n"
    "v_pk_add_u16 v51, s89, v51 clamp\n"
    "v_mfma_f32_16x16x32_bf16 v[8:11], v[172:175], v[52:55], v[8:11]\n"
    "s_waitcnt lgkmcnt(2)\n"
    "v_pk_maximum3_f16 v80, v44, v45, v80\n"
    "v_pk_maximum3_f16 v80, v46, v47, v80\n"
    "v_pk_add_u16 v44, s89, v44 clamp\n"
    "v_pk_add_u16 v45, s89, v45 clamp\n"
    "v_pk_add_u16 v46, s89, v46 clamp\n"
    "v_pk_add_u16 v47, s89, v47 clamp\n"
    "v_mfma_f32_16x16x32_bf16 v[8:11], v[168:171], v[48:51], v[8:11]\n"
    "s_waitcnt lgkmcnt(1)\n"
    "v_pk_maximum3_f16 v80, v40, v41, v80\n"
    "v_pk_maximum3_f16 v80, v42, v43, v80\n"
    "v_pk_add_u16 v40, s89, v40 clamp\n"
    "v_pk_add_u16 v41, s89, v41 clamp\n"
    "v_pk_add_u16 v42, s89, v42 clamp\n"
    "v_pk_add_u16 v43, s89, v43 clamp\n"
    "v_mfma_f32_16x16x32_bf16 v[8:11], v[164:167], v[44:47], v[8:11]\n"
    "s_waitcnt lgkmcnt(0)\n"
    "v_pk_maximum3_f16 v80, v36, v37, v80\n"
    "v_pk_maximum3_f16 v80, v38, v39, v80\n"
    "v_pk_add_u16 v36, s89, v36 clamp\n"
    "v_pk_add_u16 v37, s89, v37 clamp\n"
    "v_pk_add_u16 v38, s89, v38 clamp\n"
    "v_pk_add_u16 v39, s89, v39 clamp\n"
    "v_mfma_f32_16x16x32_bf16 v[8:11], v[160:163], v[40:43], v[8:11]\n"
    "s_nop 0\n"
    "v_mfma_f32_16x16x32_bf16 v[8:11], v[156:159], v[36:39], v[8:11]\n"
    "v_mfma_f32_16x16x32_bf16 v[24:27], v[200:203], v[56:59], v[104:107]\n"
    "v_accvgpr_write_b32 a236, v56\n"
    "v_accvgpr_write_b32 a237, v57\n"
    "v_accvgpr_write_b32 a238, v58\n"
    "v_accvgpr_write_b32 a239, v59\n"
    "v_mfma_f32_16x16x32_bf16 v[24:27], v[196:199], v[52:55], v[24:27]\n"
    "v_accvgpr_write_b32 a232, v52\n"
    "v_accvgpr_write_b32 a233, v53\n"
    "v_accvgpr_write_b32 a234, v54\n"
    "v_accvgpr_write_b32 a235, v55\n"
    "v_mfma_f32_16x16x32_bf16 v[24:27], v[192:195], v[48:51], v[24:27]\n"
    "v_accvgpr_write_b32 a228, v48\n"
    "v_accvgpr_write_b32 a229, v49\n"
    "v_accvgpr_write_b32 a230, v50\n"
    "v_accvgpr_write_b32 a231, v51\n"
    "v_mfma_f32_16x16x32_bf16 v[24:27], v[188:191], v[44:47], v[24:27]\n"
    "v_accvgpr_write_b32 a224, v44\n"
    "v_accvgpr_write_b32 a225, v45\n"
    "v_accvgpr_write_b32 a226, v46\n"
    "v_accvgpr_write_b32 a227, v47\n"
    "v_mfma_f32_16x16x32_bf16 v[24:27], v[184:187], v[40:43], v[24:27]\n"
    "v_accvgpr_write_b32 a220, v40\n"
    "v_accvgpr_write_b32 a221, v41\n"
    "v_accvgpr_write_b32 a222, v42\n"
    "v_accvgpr_write_b32 a223, v43\n"
    "v_mfma_f32_16x16x32_bf16 v[24:27], v[180:183], v[36:39], v[24:27]\n"
    "v_accvgpr_write_b32 a216, v36\n"
    "v_accvgpr_write_b32 a217, v37\n"
    "v_accvgpr_write_b32 a218, v38\n"
    "v_accvgpr_write_b32 a219, v39\n"
    "s_add_u32 m0, s65, 98304\n"
    "s_add_u32 s72, s85, 18432\n"
    "buffer_load_dwordx4 v88, s[20:23], s72 offen offset:0 nt lds\n"
    "v_exp_f32 v8, v8\n"
    "v_exp_f32 v9, v9\n"
    "v_exp_f32 v10, v10\n"
    "v_exp_f32 v11, v11\n"
    "v_exp_f32 v24, v24\n"
    "v_exp_f32 v25, v25\n"
    "v_exp_f32 v26, v26\n"
    "v_exp_f32 v27, v27\n"
    "s_add_u32 m0, s65, 99328\n"
    "s_add_u32 s72, s85, 19456\n"
    "buffer_load_dwordx4 v89, s[20:23], s72 offen offset:0 nt lds\n"
    "s_add_u32 m0, s65, 100352\n"
    "s_add_u32 s72, s85, 20480\n"
    "buffer_load_dwordx4 v90, s[20:23], s72 offen offset:0 nt lds\n"
    "s_waitcnt vmcnt(7)\n"
    "v_cmp_eq_u64 s[54:55], v[236:237], v[238:239]\n"
    "s_nop 0\n"
    "s_cmp_eq_u64 s[54:55], -1\n"
    "s_cbranch_scc0 .LpollAK\n"
    ".LpollAKr:\n"
    "s_add_u32 m0, s65, 101376\n"
    "s_add_u32 s72, s85, 21504\n"
    "buffer_load_dwordx4 v88, s[20:23], s72 offen offset:0 nt lds\n"
    "s_add_u32 m0, s65, 24576\n"
    "s_add_u32 s72, s65, 49152\n"
    "buffer_load_dwordx4 v1, s[16:19], s72 offen offset:0 sc1 lds\n"
    "s_add_u32 m0, s65, 25600\n"
    "s_add_u32 s72, s65, 50176\n"
    "buffer_load_dwordx4 v1, s[16:19], s72 offen offset:0 sc1 lds\n"
    "s_add_u32 m0, s65, 102400\n"
    "s_add_u32 s72, s85, 22528\n"
    "buffer_load_dwordx4 v89, s[20:23], s72 offen offset:0 nt lds\n"
    "s_add_u32 m0, s65, 26624\n"
    "s_add_u32 s72, s65, 51200\n"
    "buffer_load_dwordx4 v1, s[16:19], s72 offen offset:0 sc1 lds\n"
    "s_add_u32 m0, s65, 27648\n"
    "s_add_u32 s72, s65, 52224\n"
    "buffer_load_dwordx4 v1, s[16:19], s72 offen offset:0 sc1 lds\n"
    "s_add_u32 m0, s65, 103424\n"
    "s_add_u32 s72, s85, 23552\n"
    "buffer_load_dwordx4 v90, s[20:23], s72 offen offset:0 nt lds\n"
    "s_add_u32 m0, s65, 28672\n"
    "s_add_u32 s72, s65, 53248\n"
    "buffer_load_dwordx4 v1, s[16:19], s72 offen offset:0 sc1 lds\n"
    "s_add_u32 m0, s65, 29696\n"
    "s_add_u32 s72, s65, 54272\n"
    "buffer_load_dwordx4 v1, s[16:19], s72 offen offset:0 sc1 lds\n"
    "s_add_u32 m0, s65, 73728\n"
    "s_add_u32 s72, s65, 73728\n"
    "buffer_load_dwordx4 v1, s[16:19], s72 offen offset:0 sc1 lds\n"
    "s_add_u32 m0, s65, 74752\n"
    "s_add_u32 s72, s65, 74752\n"
    "buffer_load_dwordx4 v1, s[16:19], s72 offen offset:0 sc1 lds\n"
    "s_add_u32 m0, s65, 75776\n"
    "s_add_u32 s72, s65, 75776\n"
    "buffer_load_dwordx4 v1, s[16:19], s72 offen offset:0 sc1 lds\n"
    "s_add_u32 m0, s65, 76800\n"
    "s_add_u32 s72, s65, 76800\n"
    "buffer_load_dwordx4 v1, s[16:19], s72 offen offset:0 sc1 lds\n"
    "s_add_u32 m0, s65, 77824\n"
    "s_add_u32 s72, s65, 77824\n"
    "buffer_load_dwordx4 v1, s[16:19], s72 offen offset:0 sc1 lds\n"
    "s_add_u32 m0, s65, 78848\n"
    "s_add_u32 s72, s65, 78848\n"
    "buffer_load_dwordx4 v1, s[16:19], s72 offen offset:0 sc1 lds\n"
    "s_bitcmp1_b32 s83, 1\n"
    "s_cbranch_scc1 .Lf3b\n"
    "s_waitcnt vmcnt(18)\n"
    "ds_read_b128 v[36:39], v84 offset:0\n"
    "ds_read_b128 v[40:43], v85 offset:0\n"
    "ds_read_b128 v[44:47], v84 offset:128\n"
    "ds_read_b128 v[48:51], v85 offset:128\n"
    "ds_read_b128 v[124:127], v84 offset:256\n"
    "ds_read_b128 v[128:131], v85 offset:256\n"
    "s_waitcnt lgkmcnt(5)\n"
    "v_pk_maximum3_f16 v80, v36, v37, v80\n"
    "v_pk_maximum3_f16 v80, v38, v39, v80\n"
    "v_pk_add_u16 v36, s89, v36 clamp\n"
    "v_pk_add_u16 v37, s89, v37 clamp\n"
    "v_pk_add_u16 v38, s89, v38 clamp\n"
    "v_pk_add_u16 v39, s89, v39 clamp\n"
    "s_waitcnt lgkmcnt(4)\n"
    "v_pk_maximum3_f16 v80, v40, v41, v80\n"
    "v_pk_maximum3_f16 v80, v42, v43, v80\n"
    "v_pk_add_u16 v40, s89, v40 clamp\n"
    "v_pk_add_u16 v41, s89, v41 clamp\n"
    "v_pk_add_u16 v42, s89, v42 clamp\n"
    "v_pk_add_u16 v43, s89, v43 clamp\n"
    "v_mfma_f32_16x16x32_bf16 v[12:15], v[156:159], v[36:39], v[100:103]\n"
    "s_waitcnt lgkmcnt(3)\n"
    "v_pk_maximum3_f16 v80, v44, v45, v80\n"
    "v_pk_maximum3_f16 v80, v46, v47, v80\n"
    "v_pk_add_u16 v44, s89, v44 clamp\n"
    "v_pk_add_u16 v45, s89, v45 clamp\n"
    "v_pk_add_u16 v46, s89, v46 clamp\n"
    "v_pk_add_u16 v47, s89, v47 clamp\n"
    "v_mfma_f32_16x16x32_bf16 v[12:15], v[160:163], v[40:43], v[12:15]\n"
    "s_waitcnt lgkmcnt(2)\n"
    "v_pk_maximum3_f16 v80, v48, v49, v80\n"
    "v_pk_maximum3_f16 v80, v50, v51, v80\n"
    "v_pk_add_u16 v48, s89, v48 clamp\n"
    "v_pk_add_u16 v49, s89, v49 clamp\n"
    "v_pk_add_u16 v50, s89, v50 clamp\n"
    "v_pk_add_u16 v51, s89, v51 clamp\n"
    "v_mfma_f32_16x16x32_bf16 v[12:15], v[164:167], v[44:47], v[12:15]\n"
    "s_waitcnt lgkmcnt(1)\n"
    "v_pk_maximum3_f16 v80, v124, v125, v80\n"
    "v_pk_maximum3_f16 v80, v126, v127, v80\n"
    "v_pk_add_u16 v124, s89, v124 clamp\n"
    "v_pk_add_u16 v125, s89, v125 clamp\n"
    "v_pk_add_u16 v126, s89, v126 clamp\n"
    "v_pk_add_u16 v127, s89, v127 clamp\n"
    "v_mfma_f32_16x16x32_bf16 v[12:15], v[168:171], v[48:51], v[12:15]\n"
    "s_waitcnt lgkmcnt(0)\n"
    "v_pk_maximum3_f16 v80, v128, v129, v80\n"
    "v_pk_maximum3_f16 v80, v130, v131, v80\n"
    "v_pk_add_u16 v128, s89, v128 clamp\n"
    "v_pk_add_u16 v129, s89, v129 clamp\n"
    "v_pk_add_u16 v130, s89, v130 clamp\n"
    "v_pk_add_u16 v131, s89, v131 clamp\n"
    "v_mfma_f32_16x16x32_bf16 v[12:15], v[172:175], v[124:127], v[12:15]\n"
    "s_nop 0\n"
    "v_mfma_f32_16x16x32_bf16 v[12:15], v[176:179], v[128:131], v[12:15]\n"
    "v_mfma_f32_16x16x32_bf16 v[28:31], v[180:183], v[36:39], v[104:107]\n"
    "v_accvgpr_write_b32 a240, v36\n"
    "v_accvgpr_write_b32 a241, v37\n"
    "v_accvgpr_write_b32 a242, v38\n"
    "v_accvgpr_write_b32 a243, v39\n"
    "v_mfma_f32_16x16x32_bf16 v[28:31], v[184:187], v[40:43], v[28:31]\n"
    "v_accvgpr_write_b32 a244, v40\n"
    "v_accvgpr_write_b32 a245, v41\n"
    "v_accvgpr_write_b32 a246, v42\n"
    "v_accvgpr_write_b32 a247, v43\n"
    "v_mfma_f32_16x16x32_bf16 v[28:31], v[188:191], v[44:47], v[28:31]\n"
    "v_accvgpr_write_b32 a248, v44\n"
    "v_accvgpr_write_b32 a249, v45\n"
    "v_accvgpr_write_b32 a250, v46\n"
    "v_accvgpr_write_b32 a251, v47\n"
    "v_mfma_f32_16x16x32_bf16 v[28:31], v[192:195], v[48:51], v[28:31]\n"
    "v_accvgpr_write_b32 a252, v48\n"
    "v_accvgpr_write_b32 a253, v49\n"
    "v_accvgpr_write_b32 a254, v50\n"
    "v_accvgpr_write_b32 a255, v51\n"
    "v_mfma_f32_16x16x32_bf16 v[28:31], v[196:199], v[124:127], v[28:31]\n"
    "v_mfma_f32_16x16x32_bf16 v[28:31], v[200:203], v[128:131], v[28:31]\n"
    "v_exp_f32 v12, v12\n"
    "v_exp_f32 v13, v13\n"
    "v_exp_f32 v14, v14\n"
    "v_exp_f32 v15, v15\n"
    "s_nop 3\n"
    "v_exp_f32 v28, v28\n"
    "v_exp_f32 v29, v29\n"
    "v_exp_f32 v30, v30\n"
    "v_exp_f32 v31, v31\n"
    "s_waitcnt vmcnt(8)\n"
    "ds_read_b128 v[152:155], v85 offset:49408\n"
    "ds_read_b128 v[148:151], v84 offset:49408\n"
    "ds_read_b128 v[144:147], v85 offset:49280\n"
    "ds_read_b128 v[140:143], v84 offset:49280\n"
    "ds_read_b128 v[136:139], v85 offset:49152\n"
    "ds_read_b128 v[132:135], v84 offset:49152\n"
    "s_waitcnt lgkmcnt(5)\n"
    "v_pk_maximum3_f16 v80, v152, v153, v80\n"
    "v_pk_maximum3_f16 v80, v154, v155, v80\n"
    "v_pk_add_u16 v152, s89, v152 clamp\n"
    "v_pk_add_u16 v153, s89, v153 clamp\n"
    "v_pk_add_u16 v154, s89, v154 clamp\n"
    "v_pk_add_u16 v155, s89, v155 clamp\n"
    "s_waitcnt lgkmcnt(4)\n"
    "v_pk_maximum3_f16 v80, v148, v149, v80\n"
    "v_pk_maximum3_f16 v80, v150, v151, v80\n"
    "v_pk_add_u16 v148, s89, v148 clamp\n"
    "v_pk_add_u16 v149, s89, v149 clamp\n"
    "v_pk_add_u16 v150, s89, v150 clamp\n"
    "v_pk_add_u16 v151, s89, v151 clamp\n"
    "v_mfma_f32_16x16x32_bf16 v[16:19], v[176:179], v[152:155], v[100:103]\n"
    "s_waitcnt lgkmcnt(3)\n"
    "v_pk_maximum3_f16 v80, v144, v145, v80\n"
    "v_pk_maximum3_f16 v80, v146, v147, v80\n"
    "v_pk_add_u16 v144, s89, v144 clamp\n"
    "v_pk_add_u16 v145, s89, v145 clamp\n"
    "v_pk_add_u16 v146, s89, v146 clamp\n"
    "v_pk_add_u16 v147, s89, v147 clamp\n"
    "v_mfma_f32_16x16x32_bf16 v[16:19], v[172:175], v[148:151], v[16:19]\n"
    "s_waitcnt lgkmcnt(2)\n"
    "v_pk_maximum3_f16 v80, v140, v141, v80\n"
    "v_pk_maximum3_f16 v80, v142, v143, v80\n"
    "v_pk_add_u16 v140, s89, v140 clamp\n"
    "v_pk_add_u16 v141, s89, v141 clamp\n"
    "v_pk_add_u16 v142, s89, v142 clamp\n"
    "v_pk_add_u16 v143, s89, v143 clamp\n"
    "v_mfma_f32_16x16x32_bf16 v[16:19], v[168:171], v[144:147], v[16:19]\n"
    "s_waitcnt lgkmcnt(1)\n"
    "v_pk_maximum3_f16 v80, v136, v137, v80\n"
    "v_pk_maximum3_f16 v80, v138, v139, v80\n"
    "v_pk_add_u16 v136, s89, v136 clamp\n"
    "v_pk_add_u16 v137, s89, v137 clamp\n"
    "v_pk_add_u16 v138, s89, v138 clamp\n"
    "v_pk_add_u16 v139, s89, v139 clamp\n"
    "v_mfma_f32_16x16x32_bf16 v[16:19], v[164:167], v[140:143], v[16:19]\n"
    "s_waitcnt lgkmcnt(0)\n"
    "v_pk_maximum3_f16 v80, v132, v133, v80\n"
    "v_pk_maximum3_f16 v80, v134, v135, v80\n"
    "v_pk_add_u16 v132, s89, v132 clamp\n"
    "v_pk_add_u16 v133, s89, v133 clamp\n"
    "v_pk_add_u16 v134, s89, v134 clamp\n"
    "v_pk_add_u16 v135, s89, v135 clamp\n"
    "v_mfma_f32_16x16x32_bf16 v[16:19], v[160:163], v[136:139], v[16:19]\n"
    "s_nop 0\n"
    "v_mfma_f32_16x16x32_bf16 v[16:19], v[156:159], v[132:135], v[16:19]\n"
    "v_mfma_f32_16x16x32_bf16 v[32:35], v[200:203], v[152:155], v[104:107]\n"
    "v_mfma_f32_16x16x32_bf16 v[32:35], v[196:199], v[148:151], v[32:35]\n"
    "v_mfma_f32_16x16x32_bf16 v[32:35], v[192:195], v[144:147], v[32:35]\n"
    "v_mfma_f32_16x16x32_bf16 v[32:35], v[188:191], v[140:143], v[32:35]\n"
    "v_mfma_f32_16x16x32_bf16 v[32:35], v[184:187], v[136:139], v[32:35]\n"
    "v_mfma_f32_16x16x32_bf16 v[32:35], v[180:183], v[132:135], v[32:35]\n"
    "ds_read_b128 v[156:159], v2 offset:12288\n"
    "ds_read_b128 v[160:163], v2 offset:14336\n"
    "ds_read_b128 v[164:167], v2 offset:16384\n"
    "ds_read_b128 v[168:171], v2 offset:18432\n"
    "ds_read_b128 v[172:175], v2 offset:20480\n"
    "ds_read_b128 v[176:179], v2 offset:22528\n"
    "ds_read_b128 v[100:103], v77 offset:128\n"
    "v_pk_add_u16 v80, s89, v80\n"
    "v_and_b32 v80, 0x80008000, v80\n"
    "v_cmp_ne_u32 s[34:35], 0, v80\n"
    "v_exp_f32 v16, v16\n"
    "v_exp_f32 v17, v17\n"
    "v_exp_f32 v18, v18\n"
    "v_exp_f32 v19, v19\n"
    "v_exp_f32 v32, v32\n"
    "v_exp_f32 v33, v33\n"
    "v_exp_f32 v34, v34\n"
    "v_exp_f32 v35, v35\n"
    "s_mov_b32 s66, 0\n"
    "s_waitcnt lgkmcnt(0)\n"
    "s_waitcnt vmcnt(0)\n"
    "ds_add_u32 v247, v246\n"
    "s_nop 7\n"
    "s_branch .LPK\n"
    "s_branch .Lf3e\n"
    ".Lf3b:\n"
    "s_add_u32 s75, s71, s63\n"
    "s_add_u32 s76, s75, 6144\n"
    "s_add_u32 s77, s75, 24576\n"
    "s_add_u32 s78, s75, 30720\n"
    "buffer_load_dwordx2 v[204:205], v242, s[28:31], s75 offen offset:0 nt\n"
    "buffer_load_dwordx2 v[206:207], v242, s[28:31], s75 offen offset:768 nt\n"
    "buffer_load_dwordx2 v[208:209], v242, s[28:31], s75 offen offset:3072 nt\n"
    "buffer_load_dwordx2 v[210:211], v242, s[28:31], s75 offen offset:3840 nt\n"
    "buffer_load_dwordx2 v[212:213], v242, s[28:31], s76 offen offset:0 nt\n"
    "buffer_load_dwordx2 v[214:215], v242, s[28:31], s76 offen offset:768 nt\n"
    "buffer_load_dwordx2 v[216:217], v242, s[28:31], s76 offen offset:3072 nt\n"
    "buffer_load_dwordx2 v[218:219], v242, s[28:31], s76 offen offset:3840 nt\n"
    "buffer_load_dwordx2 v[220:221], v242, s[28:31], s77 offen offset:0 nt\n"
    "buffer_load_dwordx2 v[222:223], v242, s[28:31], s77 offen offset:768 nt\n"
    "buffer_load_dwordx2 v[224:225], v242, s[28:31], s77 offen offset:3072 nt\n"
    "buffer_load_dwordx2 v[226:227], v242, s[28:31], s77 offen offset:3840 nt\n"
    "buffer_load_dwordx2 v[228:229], v242, s[28:31], s78 offen offset:0 nt\n"
    "buffer_load_dwordx2 v[230:231], v242, s[28:31], s78 offen offset:768 nt\n"
    "buffer_load_dwordx2 v[232:233], v242, s[28:31], s78 offen offset:3072 nt\n"
    "buffer_load_dwordx2 v[234:235], v242, s[28:31], s78 offen offset:3840 nt\n"
    "s_waitcnt vmcnt(34)\n"
    "ds_read_b128 v[36:39], v84 offset:0\n"
    "ds_read_b128 v[40:43], v85 offset:0\n"
    "ds_read_b128 v[44:47], v84 offset:128\n"
    "ds_read_b128 v[48:51], v85 offset:128\n"
    "ds_read_b128 v[124:127], v84 offset:256\n"
    "ds_read_b128 v[128:131], v85 offset:256\n"
    "s_waitcnt lgkmcnt(5)\n"
    "v_pk_maximum3_f16 v80, v36, v37, v80\n"
    "v_pk_maximum3_f16 v80, v38, v39, v80\n"
    "v_pk_add_u16 v36, s89, v36 clamp\n"
    "v_pk_add_u16 v37, s89, v37 clamp\n"
    "v_pk_add_u16 v38, s89, v38 clamp\n"
    "v_pk_add_u16 v39, s89, v39 clamp\n"
    "s_waitcnt lgkmcnt(4)\n"
    "v_pk_maximum3_f16 v80, v40, v41, v80\n"
    "v_pk_maximum3_f16 v80, v42, v43, v80\n"
    "v_pk_add_u16 v40, s89, v40 clamp\n"
    "v_pk_add_u16 v41, s89, v41 clamp\n"
    "v_pk_add_u16 v42, s89, v42 clamp\n"
    "v_pk_add_u16 v43, s89, v43 clamp\n"
    "v_mfma_f32_16x16x32_bf16 v[12:15], v[156:159], v[36:39], v[100:103]\n"
    "s_waitcnt lgkmcnt(3)\n"
    "v_pk_maximum3_f16 v80, v44, v45, v80\n"
    "v_pk_maximum3_f16 v80, v46, v47, v80\n"
    "v_pk_add_u16 v44, s89, v44 clamp\n"
    "v_pk_add_u16 v45, s89, v45 clamp\n"
    "v_pk_add_u16 v46, s89, v46 clamp\n"
    "v_pk_add_u16 v47, s89, v47 clamp\n"
    "v_mfma_f32_16x16x32_bf16 v[12:15], v[160:163], v[40:43], v[12:15]\n"
    "s_waitcnt lgkmcnt(2)\n"
    "v_pk_maximum3_f16 v80, v48, v49, v80\n"
    "v_pk_maximum3_f16 v80, v50, v51, v80\n"
    "v_pk_add_u16 v48, s89, v48 clamp\n"
    "v_pk_add_u16 v49, s89, v49 clamp\n"
    "v_pk_add_u16 v50, s89, v50 clamp\n"
    "v_pk_add_u16 v51, s89, v51 clamp\n"
    "v_mfma_f32_16x16x32_bf16 v[12:15], v[164:167], v[44:47], v[12:15]\n"
    "s_waitcnt lgkmcnt(1)\n"
    "v_pk_maximum3_f16 v80, v124, v125, v80\n"
    "v_pk_maximum3_f16 v80, v126, v127, v80\n"
    "v_pk_add_u16 v124, s89, v124 clamp\n"
    "v_pk_add_u16 v125, s89, v125 clamp\n"
    "v_pk_add_u16 v126, s89, v126 clamp\n"
    "v_pk_add_u16 v127, s89, v127 clamp\n"
    "v_mfma_f32_16x16x32_bf16 v[12:15], v[168:171], v[48:51], v[12:15]\n"
    "s_waitcnt lgkmcnt(0)\n"
    "v_pk_maximum3_f16 v80, v128, v129, v80\n"
    "v_pk_maximum3_f16 v80, v130, v131, v80\n"
    "v_pk_add_u16 v128, s89, v128 clamp\n"
    "v_pk_add_u16 v129, s89, v129 clamp\n"
    "v_pk_add_u16 v130, s89, v130 clamp\n"
    "v_pk_add_u16 v131, s89, v131 clamp\n"
    "v_mfma_f32_16x16x32_bf16 v[12:15], v[172:175], v[124:127], v[12:15]\n"
    "s_nop 0\n"
    "v_mfma_f32_16x16x32_bf16 v[12:15], v[176:179], v[128:131], v[12:15]\n"
    "v_mfma_f32_16x16x32_bf16 v[28:31], v[180:183], v[36:39], v[104:107]\n"
    "v_accvgpr_write_b32 a240, v36\n"
    "v_accvgpr_write_b32 a241, v37\n"
    "v_accvgpr_write_b32 a242, v38\n"
    "v_accvgpr_write_b32 a243, v39\n"
    "v_mfma_f32_16x16x32_bf16 v[28:31], v[184:187], v[40:43], v[28:31]\n"
    "v_accvgpr_write_b32 a244, v40\n"
    "v_accvgpr_write_b32 a245, v41\n"
    "v_accvgpr_write_b32 a246, v42\n"
    "v_accvgpr_write_b32 a247, v43\n"
    "v_mfma_f32_16x16x32_bf16 v[28:31], v[188:191], v[44:47], v[28:31]\n"
    "v_accvgpr_write_b32 a248, v44\n"
    "v_accvgpr_write_b32 a249, v45\n"
    "v_accvgpr_write_b32 a250, v46\n"
    "v_accvgpr_write_b32 a251, v47\n"
    "v_mfma_f32_16x16x32_bf16 v[28:31], v[192:195], v[48:51], v[28:31]\n"
    "v_accvgpr_write_b32 a252, v48\n"
    "v_accvgpr_write_b32 a253, v49\n"
    "v_accvgpr_write_b32 a254, v50\n"
    "v_accvgpr_write_b32 a255, v51\n"
    "v_mfma_f32_16x16x32_bf16 v[28:31], v[196:199], v[124:127], v[28:31]\n"
    "v_mfma_f32_16x16x32_bf16 v[28:31], v[200:203], v[128:131], v[28:31]\n"
    "v_exp_f32 v12, v12\n"
    "v_exp_f32 v13, v13\n"
    "v_exp_f32 v14, v14\n"
    "v_exp_f32 v15, v15\n"
    "s_nop 3\n"
    "v_exp_f32 v28, v28\n"
    "v_exp_f32 v29, v29\n"
    "v_exp_f32 v30, v30\n"
    "v_exp_f32 v31, v31\n"
    "s_waitcnt vmcnt(24)\n"
    "ds_read_b128 v[152:155], v85 offset:49408\n"
    "ds_read_b128 v[148:151], v84 offset:49408\n"
    "ds_read_b128 v[144:147], v85 offset:49280\n"
    "ds_read_b128 v[140:143], v84 offset:49280\n"
    "ds_read_b128 v[136:139], v85 offset:49152\n"
    "ds_read_b128 v[132:135], v84 offset:49152\n"
    "s_waitcnt lgkmcnt(5)\n"
    "v_pk_maximum3_f16 v80, v152, v153, v80\n"
    "v_pk_maximum3_f16 v80, v154, v155, v80\n"
    "v_pk_add_u16 v152, s89, v152 clamp\n"
    "v_pk_add_u16 v153, s89, v153 clamp\n"
    "v_pk_add_u16 v154, s89, v154 clamp\n"
    "v_pk_add_u16 v155, s89, v155 clamp\n"
    "s_waitcnt lgkmcnt(4)\n"
    "v_pk_maximum3_f16 v80, v148, v149, v80\n"
    "v_pk_maximum3_f16 v80, v150, v151, v80\n"
    "v_pk_add_u16 v148, s89, v148 clamp\n"
    "v_pk_add_u16 v149, s89, v149 clamp\n"
    "v_pk_add_u16 v150, s89, v150 clamp\n"
    "v_pk_add_u16 v151, s89, v151 clamp\n"
    "v_mfma_f32_16x16x32_bf16 v[16:19], v[176:179], v[152:155], v[100:103]\n"
    "s_waitcnt lgkmcnt(3)\n"
    "v_pk_maximum3_f16 v80, v144, v145, v80\n"
    "v_pk_maximum3_f16 v80, v146, v147, v80\n"
    "v_pk_add_u16 v144, s89, v144 clamp\n"
    "v_pk_add_u16 v145, s89, v145 clamp\n"
    "v_pk_add_u16 v146, s89, v146 clamp\n"
    "v_pk_add_u16 v147, s89, v147 clamp\n"
    "v_mfma_f32_16x16x32_bf16 v[16:19], v[172:175], v[148:151], v[16:19]\n"
    "s_waitcnt lgkmcnt(2)\n"
    "v_pk_maximum3_f16 v80, v140, v141, v80\n"
    "v_pk_maximum3_f16 v80, v142, v143, v80\n"
    "v_pk_add_u16 v140, s89, v140 clamp\n"
    "v_pk_add_u16 v141, s89, v141 clamp\n"
    "v_pk_add_u16 v142, s89, v142 clamp\n"
    "v_pk_add_u16 v143, s89, v143 clamp\n"
    "v_mfma_f32_16x16x32_bf16 v[16:19], v[168:171], v[144:147], v[16:19]\n"
    "s_waitcnt lgkmcnt(1)\n"
    "v_pk_maximum3_f16 v80, v136, v137, v80\n"
    "v_pk_maximum3_f16 v80, v138, v139, v80\n"
    "v_pk_add_u16 v136, s89, v136 clamp\n"
    "v_pk_add_u16 v137, s89, v137 clamp\n"
    "v_pk_add_u16 v138, s89, v138 clamp\n"
    "v_pk_add_u16 v139, s89, v139 clamp\n"
    "v_mfma_f32_16x16x32_bf16 v[16:19], v[164:167], v[140:143], v[16:19]\n"
    "s_waitcnt lgkmcnt(0)\n"
    "v_pk_maximum3_f16 v80, v132, v133, v80\n"
    "v_pk_maximum3_f16 v80, v134, v135, v80\n"
    "v_pk_add_u16 v132, s89, v132 clamp\n"
    "v_pk_add_u16 v133, s89, v133 clamp\n"
    "v_pk_add_u16 v134, s89, v134 clamp\n"
    "v_pk_add_u16 v135, s89, v135 clamp\n"
    "v_mfma_f32_16x16x32_bf16 v[16:19], v[160:163], v[136:139], v[16:19]\n"
    "s_nop 0\n"
    "v_mfma_f32_16x16x32_bf16 v[16:19], v[156:159], v[132:135], v[16:19]\n"
    "v_mfma_f32_16x16x32_bf16 v[32:35], v[200:203], v[152:155], v[104:107]\n"
    "v_mfma_f32_16x16x32_bf16 v[32:35], v[196:199], v[148:151], v[32:35]\n"
    "v_mfma_f32_16x16x32_bf16 v[32:35], v[192:195], v[144:147], v[32:35]\n"
    "v_mfma_f32_16x16x32_bf16 v[32:35], v[188:191], v[140:143], v[32:35]\n"
    "v_mfma_f32_16x16x32_bf16 v[32:35], v[184:187], v[136:139], v[32:35]\n"
    "v_mfma_f32_16x16x32_bf16 v[32:35], v[180:183], v[132:135], v[32:35]\n"
    "ds_read_b128 v[156:159], v2 offset:12288\n"
    "ds_read_b128 v[160:163], v2 offset:14336\n"
    "ds_read_b128 v[164:167], v2 offset:16384\n"
    "ds_read_b128 v[168:171], v2 offset:18432\n"
    "ds_read_b128 v[172:175], v2 offset:20480\n"
    "ds_read_b128 v[176:179], v2 offset:22528\n"
    "ds_read_b128 v[100:103], v77 offset:128\n"
    "v_pk_add_u16 v80, s89, v80\n"
    "v_and_b32 v80, 0x80008000, v80\n"
    "v_cmp_ne_u32 s[34:35], 0, v80\n"
    "v_exp_f32 v16, v16\n"
    "v_exp_f32 v17, v17\n"
    "v_exp_f32 v18, v18\n"
    "v_exp_f32 v19, v19\n"
    "v_exp_f32 v32, v32\n"
    "v_exp_f32 v33, v33\n"
    "v_exp_f32 v34, v34\n"
    "v_exp_f32 v35, v35\n"
    "s_mov_b32 s66, 0\n"
    "s_waitcnt lgkmcnt(0)\n"
    "s_waitcnt vmcnt(16)\n"
    "ds_add_u32 v247, v246\n"
    "s_nop 7\n"
    "s_branch .LNK\n"
    ".Lf3e:\n"
    "s_nop 4\n"
    "s_branch .Lf5e\n"
    ".Lf5b:\n"
    "s_lshl_b32 s73, s79, 8\n"
    "s_mul_i32 s74, s59, 6144\n"
    "s_add_u32 s73, s73, s74\n"
    "v_add_u32 v163, s73, v244\n"
    "s_lshr_b32 s73, s53, 2\n"
    "s_mul_i32 s73, s73, 12288\n"
    "s_and_b32 s74, s53, 3\n"
    "s_lshl_b32 s74, s74, 8\n"
    "s_add_u32 s73, s73, s74\n"
    "s_cmp_eq_u32 s59, 0\n"
    "s_cselect_b32 s73, 0x7f000000, s73\n"
    "s_add_u32 s60, s73, 24576\n"
    "s_add_u32 s61, s73, 28672\n"
    "s_add_u32 s62, s73, 32768\n"
    "v_mov_b32 v244, v243\n"
    "v_mov_b32 v243, 0\n"
    "buffer_load_dwordx4 v[40:43], v242, s[40:43], s75 offen offset:768 nt\n"
    "v_add_u32 v159, 0, v156\n"
    "v_mul_u32_u24 v159, 2731, v159\n"
    "v_lshrrev_b32 v159, 16, v159\n"
    "v_xor_b32 v159, v159, v156\n"
    "v_and_b32 v159, 7, v159\n"
    "v_and_b32 v88, 0x380, v1\n"
    "v_lshl_add_u32 v88, v159, 4, v88\n"
    "v_add_u32 v159, 64, v156\n"
    "v_mul_u32_u24 v159, 2731, v159\n"
    "v_lshrrev_b32 v159, 16, v159\n"
    "v_xor_b32 v159, v159, v156\n"
    "v_and_b32 v159, 7, v159\n"
    "v_and_b32 v89, 0x380, v1\n"
    "v_lshl_add_u32 v89, v159, 4, v89\n"
    "v_add_u32 v159, 128, v156\n"
    "v_mul_u32_u24 v159, 2731, v159\n"
    "v_lshrrev_b32 v159, 16, v159\n"
    "v_xor_b32 v159, v159, v156\n"
    "v_and_b32 v159, 7, v159\n"
    "v_and_b32 v90, 0x380, v1\n"
    "v_lshl_add_u32 v90, v159, 4, v90\n"
    "v_and_b32 v2, 7, v156\n"
    "v_lshrrev_b32 v144, 3, %3\n"
    "v_mul_u32_u24 v144, 768, v144\n"
    "v_lshl_add_u32 v144, v2, 3, v144\n"
    "buffer_load_dwordx4 v[44:47], v242, s[40:43], s75 offen offset:256 nt\n"
    "v_add_u32 v144, 131072, v144\n"
    "v_mad_u32_u24 v86, v2, 24, v144\n"
    "v_and_b32 v84, 15, v156\n"
    "v_lshrrev_b32 v159, 4, v156\n"
    "v_xor_b32 v159, v159, v2\n"
    "v_mul_u32_u24 v84, 384, v84\n"
    "v_lshl_add_u32 v84, v159, 4, v84\n"
    "s_add_u32 s73, s65, 49152\n"
    "v_add_u32 v84, s73, v84\n"
    "v_xor_b32 v85, 64, v84\n"
    "v_subrev_u32 v154, 24576, v84\n"
    "v_subrev_u32 v155, 24576, v85\n"
    "s_add_u32 s67, s65, 98304\n"
    "s_add_u32 s68, s65, 24576\n"
    "s_add_u32 s69, s65, 73728\n"
    "s_mov_b32 s81, 256\n"
    "s_mul_i32 s74, s79, 24576\n"
    "s_add_u32 s70, s74, s65\n"
    "s_add_u32 s70, s70, 73728\n"
    "s_sub_u32 s64, s65, s74\n"
    "s_add_u32 s64, s64, 98304\n"
    "v_and_b32 v249, 0x310, v1\n"
    "v_and_b32 v159, 12, v156\n"
    "v_lshl_add_u32 v249, v159, 3, v249\n"
    "v_and_b32 v159, 2, v156\n"
    "buffer_load_dwordx4 v[48:51], v242, s[40:43], s75 offen offset:1024 nt\n"
    "v_lshl_add_u32 v249, v159, 6, v249\n"
    "s_add_u32 s73, s74, 73728\n"
    "v_add_u32 v248, s73, v249\n"
    "s_sub_u32 s73, 98304, s74\n"
    "v_add_u32 v249, s73, v249\n"
    "s_or_b32 s73, s53, s84\n"
    "s_cmp_eq_u32 s73, 0\n"
    "s_cselect_b32 s82, 0, 0x7fff0000\n"
    "v_and_b32 v252, 12, v156\n"
    "v_bfe_u32 v159, v156, 1, 1\n"
    "v_or_b32 v252, v252, v159\n"
    "v_and_b32 v159, 1, v156\n"
    "v_lshl_or_b32 v252, v159, 1, v252\n"
    "v_lshlrev_b32 v252, 6, v252\n"
    "v_lshlrev_b32 v159, 2, v156\n"
    "v_and_b32 v159, 32, v159\n"
    "v_xor_b32 v159, v159, v156\n"
    "v_and_or_b32 v252, v159, 48, v252\n"
    "v_mov_b32 v2, v252\n"
    "v_add_u32 v3, 24576, v252\n"
    "v_lshrrev_b32 v91, 3, v156\n"
    "v_lshl_add_u32 v87, v91, 6, 64\n"
    "v_add_u32 v87, 64, v87\n"
    "v_and_b32 v241, 1, v156\n"
    "v_lshlrev_b32 v241, 3, v241\n"
    "buffer_load_dwordx4 v[52:55], v242, s[40:43], s75 offen offset:512 nt\n"
    "v_lshl_add_u32 v241, v91, 4, v241\n"
    "v_add_u32 v241, 1152, v241\n"
    "v_lshlrev_b32 v91, 24, v91\n"
    "v_lshl_add_u32 v91, v156, 3, v91\n"
    "s_lshl_b32 s73, s53, 4\n"
    "s_lshl_b32 s74, s59, 3\n"
    "s_add_u32 s73, s73, s74\n"
    "s_add_u32 s73, s73, 1152\n"
    "v_mov_b32 v240, 0x7fff0000\n"
    "v_writelane_b32 v240, s73, 0\n"
    "v_and_b32 v159, 48, v156\n"
    "s_mul_i32 s73, s79, 768\n"
    "s_sub_u32 s73, 131072, s73\n"
    "v_lshlrev_b32 v250, 1, v159\n"
    "v_add_u32 v250, s73, v250\n"
    "v_and_b32 v251, 3, v156\n"
    "v_lshl_add_u32 v251, v251, 7, v159\n"
    "s_add_u32 s73, s73, 256\n"
    "v_add_u32 v251, s73, v251\n"
    "v_and_b32 v159, 64, v1\n"
    "v_add_u32 v251, v159, v251\n"
    "v_bfe_i32 v159, v156, 2, 1\n"
    "v_and_b32 v159, 0xaaaa, v159\n"
    "v_or_b32 v253, 0x4444, v159\n"
    "s_mov_b32 s58, 0\n"
    "s_mov_b32 s59, 8\n"
    "s_waitcnt vmcnt(14)\n"
    "v_pk_min_i16 v4, v4, v10\n"
    "v_pk_min_i16 v5, v5, v11\n"
    "v_pk_min_i16 v6, v6, v12\n"
    "v_pk_min_i16 v7, v7, v13\n"
    "v_pk_min_i16 v8, v8, v14\n"
    "v_pk_min_i16 v9, v9, v15\n"
    "v_pk_min_i16 v16, v16, v18\n"
    "v_pk_min_i16 v17, v17, v19\n"
    "v_pk_min_i16 v4, v4, v7\n"
    "v_pk_min_i16 v5, v5, v8\n"
    "v_pk_min_i16 v6, v6, v9\n"
    "v_pk_min_i16 v16, v16, v17\n"
    "v_pk_min_i16 v4, v4, v5\n"
    "v_lshlrev_b32 v17, 16, v16\n"
    "v_pk_min_i16 v4, v4, v6\n"
    "v_min_i32 v16, v16, v17\n"
    "v_lshlrev_b32 v5, 16, v4\n"
    "v_mov_b32 v7, 155920\n"
    "v_min_i32 v4, v4, v5\n"
    "v_lshl_add_u32 v6, s84, 2, v7\n"
    "v_min_i32_dpp v16, v16, v16 row_shr:1\n"
    "v_min_i32_dpp v4, v4, v4 row_shr:1\n"
    "s_nop 0\n"
    "v_min_i32_dpp v16, v16, v16 row_shr:2\n"
    "v_min_i32_dpp v4, v4, v4 row_shr:2\n"
    "s_nop 0\n"
    "v_min_i32_dpp v16, v16, v16 row_shr:4\n"
    "v_min_i32_dpp v4, v4, v4 row_shr:4\n"
    "s_nop 0\n"
    "v_min_i32_dpp v16, v16, v16 row_shr:8\n"
    "v_min_i32_dpp v4, v4, v4 row_shr:8\n"
    "s_nop 0\n"
    "v_min_i32_dpp v16, v16, v16 row_bcast:15 row_mask:0xa\n"
    "v_min_i32_dpp v4, v4, v4 row_bcast:15 row_mask:0xa\n"
    "s_nop 0\n"
    "v_min_i32_dpp v16, v16, v16 row_bcast:31 row_mask:0xc\n"
    "v_min_i32_dpp v4, v4, v4 row_bcast:31 row_mask:0xc\n"
    "v_readlane_b32 s35, v16, 63\n"
    "v_readlane_b32 s73, v4, 63\n"
    "s_max_i32 s35, s35, 0\n"
    "s_and_b32 s35, s35, 0xffff0000\n"
    "v_mov_b32 v4, s73\n"
    "ds_write_b32 v6, v4\n"
    "v_mov_b32 v12, -126\n"
    "v_mov_b32 v13, 0x3dd53b95\n"
    "v_mov_b32 v80, 0\n"
    "s_waitcnt lgkmcnt(0)\n"
    "s_barrier\n"
    "ds_read_b128 v[8:11], v7 offset:0\n"
    "buffer_load_dwordx4 v[56:59], v242, s[40:43], s75 offen offset:1280 nt\n"
    "buffer_load_dwordx4 v[136:139], v130, s[92:95], 0 offen offset:0\n"
    "buffer_load_dwordx4 v[140:143], v130, s[96:99], 0 offen offset:0\n"
    "v_accvgpr_write_b32 a0, 0\n"
    "v_accvgpr_write_b32 a1, 0\n"
    "v_accvgpr_write_b32 a2, 0\n"
    "v_accvgpr_write_b32 a3, 0\n"
    "v_accvgpr_write_b32 a4, 0\n"
    "v_accvgpr_write_b32 a5, 0\n"
    "v_accvgpr_write_b32 a6, 0\n"
    "v_accvgpr_write_b32 a7, 0\n"
    "v_accvgpr_write_b32 a8, 0\n"
    "v_accvgpr_write_b32 a9, 0\n"
    "v_accvgpr_write_b32 a10, 0\n"
    "v_accvgpr_write_b32 a11, 0\n"
    "v_accvgpr_write_b32 a12, 0\n"
    "v_accvgpr_write_b32 a13, 0\n"
    "v_accvgpr_write_b32 a14, 0\n"
    "v_accvgpr_write_b32 a15, 0\n"
    "v_accvgpr_write_b32 a16, 0\n"
    "v_accvgpr_write_b32 a17, 0\n"
    "v_accvgpr_write_b32 a18, 0\n"
    "v_accvgpr_write_b32 a19, 0\n"
    "v_accvgpr_write_b32 a20, 0\n"
    "v_accvgpr_write_b32 a21, 0\n"
    "v_accvgpr_write_b32 a22, 0\n"
    "v_accvgpr_write_b32 a23, 0\n"
    "v_accvgpr_write_b32 a24, 0\n"
    "v_accvgpr_write_b32 a25, 0\n"
    "v_accvgpr_write_b32 a26, 0\n"
    "v_accvgpr_write_b32 a27, 0\n"
    "v_accvgpr_write_b32 a28, 0\n"
    "v_accvgpr_write_b32 a29, 0\n"
    "v_accvgpr_write_b32 a30, 0\n"
    "v_accvgpr_write_b32 a31, 0\n"
    "v_accvgpr_write_b32 a32, 0\n"
    "v_accvgpr_write_b32 a33, 0\n"
    "v_accvgpr_write_b32 a34, 0\n"
    "v_accvgpr_write_b32 a35, 0\n"
    "v_accvgpr_write_b32 a36, 0\n"
    "v_accvgpr_write_b32 a37, 0\n"
    "v_accvgpr_write_b32 a38, 0\n"
    "v_accvgpr_write_b32 a39, 0\n"
    "v_accvgpr_write_b32 a40, 0\n"
    "v_accvgpr_write_b32 a41, 0\n"
    "v_accvgpr_write_b32 a42, 0\n"
    "v_accvgpr_write_b32 a43, 0\n"
    "v_accvgpr_write_b32 a44, 0\n"
    "v_accvgpr_write_b32 a45, 0\n"
    "v_accvgpr_write_b32 a46, 0\n"
    "v_accvgpr_write_b32 a47, 0\n"
    "v_accvgpr_write_b32 a48, 0\n"
    "v_accvgpr_write_b32 a49, 0\n"
    "v_accvgpr_write_b32 a50, 0\n"
    "v_accvgpr_write_b32 a51, 0\n"
    "v_accvgpr_write_b32 a52, 0\n"
    "v_accvgpr_write_b32 a53, 0\n"
    "v_accvgpr_write_b32 a54, 0\n"
    "v_accvgpr_write_b32 a55, 0\n"
    "v_accvgpr_write_b32 a56, 0\n"
    "v_accvgpr_write_b32 a57, 0\n"
    "v_accvgpr_write_b32 a58, 0\n"
    "v_accvgpr_write_b32 a59, 0\n"
    "s_waitcnt lgkmcnt(0)\n"
    "v_min3_i32 v4, v8, v9, v10\n"
    "v_min_i32 v4, v4, v11\n"
    "v_max_i32 v4, 0, v4\n"
    "v_and_b32 v4, 0xffff0000, v4\n"
    "v_mul_f32 v4, s35, v4\n"
    "v_mul_f32 v4, 0x3ed53b95, v4\n"
    "v_lshrrev_b32 v4, 23, v4\n"
    "v_sub_u32 v4, 2, v4\n"
    "v_med3_i32 v4, v4, 0, v12\n"
    "v_ldexp_f32 v13, v13, v4\n"
    "v_readfirstlane_b32 s89, v4\n"
    "v_readfirstlane_b32 s88, v13\n"
    "s_mul_i32 s89, s89, 0xff7fff80\n"
    "s_waitcnt vmcnt(11)\n"
    "s_add_u32 s54, s71, s60\n"
    "s_add_u32 s55, s71, s61\n"
    "s_add_u32 s56, s71, s62\n"
    "v_cvt_pk_u16_u32 v204, v204, v208\n"
    "v_cvt_pk_u16_u32 v205, v205, v209\n"
    "v_cvt_pk_u16_u32 v206, v206, v210\n"
    "v_cvt_pk_u16_u32 v207, v207, v211\n"
    "v_or3_b32 v243, v204, v205, v243\n"
    "v_or3_b32 v243, v206, v207, v243\n"
    "buffer_store_dword v204, v244, s[16:19], s54 offen offset:0 sc1\n"
    "buffer_store_dword v205, v244, s[16:19], s54 offen offset:1024 sc1\n"
    "buffer_load_dwordx4 v[4:7], v162, s[28:31], s57 offen offset:0\n"
    "buffer_load_dwordx4 v[8:11], v162, s[28:31], s57 offen offset:3072\n"
    "buffer_store_dword v206, v244, s[16:19], s54 offen offset:2048 sc1\n"
    "buffer_store_dword v207, v244, s[16:19], s54 offen offset:3072 sc1\n"
    "v_cvt_pk_u16_u32 v212, v212, v216\n"
    "v_cvt_pk_u16_u32 v213, v213, v217\n"
    "v_cvt_pk_u16_u32 v214, v214, v218\n"
    "v_cvt_pk_u16_u32 v215, v215, v219\n"
    "v_or3_b32 v243, v212, v213, v243\n"
    "v_or3_b32 v243, v214, v215, v243\n"
    "buffer_store_dword v212, v244, s[16:19], s55 offen offset:0 sc1\n"
    "buffer_store_dword v213, v244, s[16:19], s55 offen offset:1024 sc1\n"
    "buffer_store_dword v214, v244, s[16:19], s55 offen offset:2048 sc1\n"
    "buffer_load_dwordx4 v[12:15], v162, s[28:31], s57 offen offset:128\n"
    "buffer_store_dword v215, v244, s[16:19], s55 offen offset:3072 sc1\n"
    "v_cvt_pk_u16_u32 v220, v220, v224\n"
    "v_cvt_pk_u16_u32 v221, v221, v225\n"
    "v_cvt_pk_u16_u32 v222, v222, v226\n"
    "v_cvt_pk_u16_u32 v223, v223, v227\n"
    "v_or3_b32 v243, v220, v221, v243\n"
    "v_or3_b32 v243, v222, v223, v243\n"
    "buffer_store_dword v220, v244, s[16:19], s56 offen offset:0 sc1\n"
    "buffer_store_dword v221, v244, s[16:19], s56 offen offset:1024 sc1\n"
    "buffer_store_dword v222, v244, s[16:19], s56 offen offset:2048 sc1\n"
    "buffer_store_dword v223, v244, s[16:19], s56 offen offset:3072 sc1\n"
    "s_add_u32 s71, s71, 98304\n"
    "s_add_u32 m0, s65, 24576\n"
    "s_add_u32 s72, s85, 0\n"
    "buffer_load_dwordx4 v88, s[20:23], s72 offen offset:0 nt lds\n"
    "buffer_load_dwordx4 v[16:19], v162, s[28:31], s57 offen offset:3200\n"
    "buffer_load_dwordx4 v[20:23], v162, s[28:31], s57 offen offset:256\n"
    "s_waitcnt vmcnt(25)\n"
    "v_add_co_u32 v238, vcc, 0x7f4a7c15, v238\n"
    "v_mov_b32 v159, 0x9e3779b9\n"
    "s_nop 0\n"
    "v_addc_co_u32 v239, vcc, v159, v239, vcc\n"
    "v_mov_b32 v246, 1\n"
    "s_waitcnt vmcnt(26)\n"
    "v_lshlrev_b32 v180, 16, v124\n"
    "v_and_b32 v181, 0xffff0000, v124\n"
    "v_lshlrev_b32 v182, 16, v125\n"
    "v_and_b32 v183, 0xffff0000, v125\n"
    "v_lshlrev_b32 v184, 16, v126\n"
    "v_and_b32 v185, 0xffff0000, v126\n"
    "v_lshlrev_b32 v186, 16, v127\n"
    "v_and_b32 v187, 0xffff0000, v127\n"
    "v_mul_f32 v180, s88, v180\n"
    "v_mul_f32 v181, s88, v181\n"
    "v_mul_f32 v182, s88, v182\n"
    "v_mul_f32 v183, s88, v183\n"
    "v_fmaak_f32 v184, v184, v180, 0x80730000\n"
    "v_fmaak_f32 v185, v185, v181, 0x80730000\n"
    "v_fmaak_f32 v186, v186, v182, 0x80730000\n"
    "v_fmaak_f32 v187, v187, v183, 0x80730000\n"
    "ds_write_b128 v131, v[180:183] offset:0\n"
    "ds_write_b128 v131, v[184:187] offset:1024\n"
    "ds_read_b128 v[180:183], v132 offset:0\n"
    "ds_read_b128 v[184:187], v132 offset:1024\n"
    "ds_read_b128 v[188:191], v132 offset:128\n"
    "ds_read_b128 v[192:195], v132 offset:1152\n"
    "ds_read_b128 v[196:199], v132 offset:256\n"
    "ds_read_b128 v[200:203], v132 offset:1280\n"
    "v_accvgpr_write_b32 a60, 0\n"
    "v_accvgpr_write_b32 a61, 0\n"
    "v_accvgpr_write_b32 a62, 0\n"
    "v_accvgpr_write_b32 a63, 0\n"
    "v_accvgpr_write_b32 a64, 0\n"
    "v_accvgpr_write_b32 a65, 0\n"
    "v_accvgpr_write_b32 a66, 0\n"
    "v_accvgpr_write_b32 a67, 0\n"
    "v_accvgpr_write_b32 a68, 0\n"
    "v_accvgpr_write_b32 a69, 0\n"
    "s_add_u32 m0, s65, 25600\n"
    "s_add_u32 s72, s85, 1024\n"
    "buffer_load_dwordx4 v89, s[20:23], s72 offen offset:0 nt lds\n"
    "buffer_load_dwordx4 v[24:27], v162, s[28:31], s57 offen offset:3328\n"
    "v_accvgpr_write_b32 a70, 0\n"
    "v_accvgpr_write_b32 a71, 0\n"
    "v_accvgpr_write_b32 a72, 0\n"
    "v_accvgpr_write_b32 a73, 0\n"
    "v_accvgpr_write_b32 a74, 0\n"
    "v_accvgpr_write_b32 a75, 0\n"
    "v_accvgpr_write_b32 a76, 0\n"
    "v_accvgpr_write_b32 a77, 0\n"
    "v_accvgpr_write_b32 a78, 0\n"
    "v_accvgpr_write_b32 a79, 0\n"
    "v_accvgpr_write_b32 a80, 0\n"
    "v_accvgpr_write_b32 a81, 0\n"
    "v_accvgpr_write_b32 a82, 0\n"
    "v_accvgpr_write_b32 a83, 0\n"
    "v_accvgpr_write_b32 a84, 0\n"
    "v_accvgpr_write_b32 a85, 0\n"
    "v_accvgpr_write_b32 a86, 0\n"
    "v_accvgpr_write_b32 a87, 0\n"
    "v_accvgpr_write_b32 a88, 0\n"
    "v_accvgpr_write_b32 a89, 0\n"
    "v_accvgpr_write_b32 a90, 0\n"
    "v_accvgpr_write_b32 a91, 0\n"
    "v_accvgpr_write_b32 a92, 0\n"
    "v_accvgpr_write_b32 a93, 0\n"
    "v_accvgpr_write_b32 a94, 0\n"
    "v_accvgpr_write_b32 a95, 0\n"
    "v_accvgpr_write_b32 a96, 0\n"
    "v_accvgpr_write_b32 a97, 0\n"
    "v_accvgpr_write_b32 a98, 0\n"
    "v_accvgpr_write_b32 a99, 0\n"
    "v_accvgpr_write_b32 a100, 0\n"
    "v_accvgpr_write_b32 a101, 0\n"
    "v_accvgpr_write_b32 a102, 0\n"
    "v_accvgpr_write_b32 a103, 0\n"
    "v_accvgpr_write_b32 a104, 0\n"
    "v_accvgpr_write_b32 a105, 0\n"
    "v_accvgpr_write_b32 a106, 0\n"
    "v_accvgpr_write_b32 a107, 0\n"
    "v_accvgpr_write_b32 a108, 0\n"
    "v_accvgpr_write_b32 a109, 0\n"
    "v_accvgpr_write_b32 a110, 0\n"
    "s_add_u32 m0, s65, 26624\n"
    "s_add_u32 s72, s85, 2048\n"
    "buffer_load_dwordx4 v90, s[20:23], s72 offen offset:0 nt lds\n"
    "buffer_load_dwordx4 v[100:103], v162, s[28:31], s66 offen offset:0\n"
    "buffer_load_dwordx4 v[104:107], v162, s[28:31], s66 offen offset:3072\n"
    "v_accvgpr_write_b32 a111, 0\n"
    "v_accvgpr_write_b32 a112, 0\n"
    "v_accvgpr_write_b32 a113, 0\n"
    "v_accvgpr_write_b32 a114, 0\n"
    "v_accvgpr_write_b32 a115, 0\n"
    "v_accvgpr_write_b32 a116, 0\n"
    "v_accvgpr_write_b32 a117, 0\n"
    "v_accvgpr_write_b32 a118, 0\n"
    "v_accvgpr_write_b32 a119, 0\n"
    "v_accvgpr_write_b32 a120, 0\n"
    "v_accvgpr_write_b32 a121, 0\n"
    "v_accvgpr_write_b32 a122, 0\n"
    "v_accvgpr_write_b32 a123, 0\n"
    "v_accvgpr_write_b32 a124, 0\n"
    "v_accvgpr_write_b32 a125, 0\n"
    "v_accvgpr_write_b32 a126, 0\n"
    "v_accvgpr_write_b32 a127, 0\n"
    "v_accvgpr_write_b32 a128, 0\n"
    "v_accvgpr_write_b32 a129, 0\n"
    "v_accvgpr_write_b32 a130, 0\n"
    "v_accvgpr_write_b32 a131, 0\n"
    "v_accvgpr_write_b32 a132, 0\n"
    "v_accvgpr_write_b32 a133, 0\n"
    "v_accvgpr_write_b32 a134, 0\n"
    "v_accvgpr_write_b32 a135, 0\n"
    "v_accvgpr_write_b32 a136, 0\n"
    "v_accvgpr_write_b32 a137, 0\n"
    "v_accvgpr_write_b32 a138, 0\n"
    "v_accvgpr_write_b32 a139, 0\n"
    "v_accvgpr_write_b32 a140, 0\n"
    "v_accvgpr_write_b32 a141, 0\n"
    "v_accvgpr_write_b32 a142, 0\n"
    "v_accvgpr_write_b32 a143, 0\n"
    "v_accvgpr_write_b32 a144, 0\n"
    "v_accvgpr_write_b32 a145, 0\n"
    "v_accvgpr_write_b32 a146, 0\n"
    "v_accvgpr_write_b32 a147, 0\n"
    "v_accvgpr_write_b32 a148, 0\n"
    "v_accvgpr_write_b32 a149, 0\n"
    "v_accvgpr_write_b32 a150, 0\n"
    "s_add_u32 m0, s65, 27648\n"
    "s_add_u32 s72, s85, 3072\n"
    "buffer_load_dwordx4 v88, s[20:23], s72 offen offset:0 nt lds\n"
    "buffer_load_dwordx4 v[108:111], v162, s[28:31], s66 offen offset:128\n"
    "v_accvgpr_write_b32 a151, 0\n"
    "v_accvgpr_write_b32 a152, 0\n"
    "v_accvgpr_write_b32 a153, 0\n"
    "v_accvgpr_write_b32 a154, 0\n"
    "v_accvgpr_write_b32 a155, 0\n"
    "v_accvgpr_write_b32 a156, 0\n"
    "v_accvgpr_write_b32 a157, 0\n"
    "v_accvgpr_write_b32 a158, 0\n"
    "v_accvgpr_write_b32 a159, 0\n"
    "v_accvgpr_write_b32 a160, 0\n"
    "v_accvgpr_write_b32 a161, 0\n"
    "v_accvgpr_write_b32 a162, 0\n"
    "v_accvgpr_write_b32 a163, 0\n"
    "v_accvgpr_write_b32 a164, 0\n"
    "v_accvgpr_write_b32 a165, 0\n"
    "v_accvgpr_write_b32 a166, 0\n"
    "v_accvgpr_write_b32 a167, 0\n"
    "v_accvgpr_write_b32 a168, 0\n"
    "v_accvgpr_write_b32 a169, 0\n"
    "v_accvgpr_write_b32 a170, 0\n"
    "v_accvgpr_write_b32 a171, 0\n"
    "v_accvgpr_write_b32 a172, 0\n"
    "v_accvgpr_write_b32 a173, 0\n"
    "v_accvgpr_write_b32 a174, 0\n"
    "v_accvgpr_write_b32 a175, 0\n"
    "v_accvgpr_write_b32 a176, 0\n"
    "v_accvgpr_write_b32 a177, 0\n"
    "v_accvgpr_write_b32 a178, 0\n"
    "v_accvgpr_write_b32 a179, 0\n"
    "v_accvgpr_write_b32 a180, 0\n"
    "v_accvgpr_write_b32 a181, 0\n"
    "v_accvgpr_write_b32 a182, 0\n"
    "v_accvgpr_write_b32 a183, 0\n"
    "v_accvgpr_write_b32 a184, 0\n"
    "v_accvgpr_write_b32 a185, 0\n"
    "v_accvgpr_write_b32 a186, 0\n"
    "v_accvgpr_write_b32 a187, 0\n"
    "v_accvgpr_write_b32 a188, 0\n"
    "v_accvgpr_write_b32 a189, 0\n"
    "v_accvgpr_write_b32 a190, 0\n"
    "v_accvgpr_write_b32 a191, 0\n"
    "s_waitcnt lgkmcnt(0)\n"
    "s_waitcnt vmcnt(10)\n"
    "ds_add_rtn_u32 v245, v247, v246\n"
    "s_lshl_b32 s73, s53, 6\n"
    "s_add_u32 s73, s73, 128\n"
    "s_waitcnt lgkmcnt(0)\n"
    "v_readfirstlane_b32 s74, v245\n"
    "s_nop 0\n"
    "s_cmp_eq_u32 s74, 3\n"
    "s_cselect_b32 s73, s73, 0x7fff0000\n"
    "buffer_store_dwordx2 v[238:239], v91, s[36:39], s73 offen offset:0 sc1\n"
    "s_waitcnt vmcnt(28)\n"
    "s_add_u32 m0, s65, 28672\n"
    "s_add_u32 s72, s85, 4096\n"
    "buffer_load_dwordx4 v89, s[20:23], s72 offen offset:0 nt lds\n"
    "buffer_load_dwordx4 v[112:115], v162, s[28:31], s66 offen offset:3200\n"
    "buffer_load_dwordx4 v[116:119], v162, s[28:31], s66 offen offset:256\n"
    "s_add_u32 s54, s71, s60\n"
    "s_add_u32 s55, s71, s61\n"
    "s_add_u32 s56, s71, s62\n"
    "v_cvt_pk_u16_u32 v36, v36, v40\n"
    "v_cvt_pk_u16_u32 v37, v37, v41\n"
    "v_cvt_pk_u16_u32 v38, v38, v42\n"
    "v_cvt_pk_u16_u32 v39, v39, v43\n"
    "v_or3_b32 v243, v36, v37, v243\n"
    "v_or3_b32 v243, v38, v39, v243\n"
    "buffer_store_dword v36, v244, s[16:19], s54 offen offset:0 sc1\n"
    "buffer_store_dword v37, v244, s[16:19], s54 offen offset:1024 sc1\n"
    "buffer_store_dword v38, v244, s[16:19], s54 offen offset:2048 sc1\n"
    "buffer_store_dword v39, v244, s[16:19], s54 offen offset:3072 sc1\n"
    "v_cvt_pk_u16_u32 v44, v44, v48\n"
    "v_cvt_pk_u16_u32 v45, v45, v49\n"
    "v_cvt_pk_u16_u32 v46, v46, v50\n"
    "s_add_u32 m0, s65, 29696\n"
    "s_add_u32 s72, s85, 5120\n"
    "buffer_load_dwordx4 v90, s[20:23], s72 offen offset:0 nt lds\n"
    "buffer_load_dwordx4 v[120:123], v162, s[28:31], s66 offen offset:3328\n"
    "v_cvt_pk_u16_u32 v47, v47, v51\n"
    "v_or3_b32 v243, v44, v45, v243\n"
    "v_or3_b32 v243, v46, v47, v243\n"
    "buffer_store_dword v44, v244, s[16:19], s55 offen offset:0 sc1\n"
    "buffer_store_dword v45, v244, s[16:19], s55 offen offset:1024 sc1\n"
    "buffer_store_dword v46, v244, s[16:19], s55 offen offset:2048 sc1\n"
    "buffer_store_dword v47, v244, s[16:19], s55 offen offset:3072 sc1\n"
    "v_cvt_pk_u16_u32 v52, v52, v56\n"
    "v_cvt_pk_u16_u32 v53, v53, v57\n"
    "v_cvt_pk_u16_u32 v54, v54, v58\n"
    "v_cvt_pk_u16_u32 v55, v55, v59\n"
    "v_or3_b32 v243, v52, v53, v243\n"
    "v_or3_b32 v243, v54, v55, v243\n"
    "buffer_store_dword v52, v244, s[16:19], s56 offen offset:0 sc1\n"
    "buffer_store_dword v53, v244, s[16:19], s56 offen offset:1024 sc1\n"
    "buffer_store_dword v54, v244, s[16:19], s56 offen offset:2048 sc1\n"
    "buffer_store_dword v55, v244, s[16:19], s56 offen offset:3072 sc1\n"
    "s_add_u32 m0, s65, 73728\n"
    "s_add_u32 s72, s85, 6144\n"
    "buffer_load_dwordx4 v88, s[20:23], s72 offen offset:0 nt lds\n"
    "s_waitcnt vmcnt(44)\n"
    "v_lshlrev_b32 v204, 16, v136\n"
    "v_and_b32 v205, 0xffff0000, v136\n"
    "v_lshlrev_b32 v206, 16, v137\n"
    "v_and_b32 v207, 0xffff0000, v137\n"
    "v_lshlrev_b32 v208, 16, v138\n"
    "v_and_b32 v209, 0xffff0000, v138\n"
    "v_lshlrev_b32 v210, 16, v139\n"
    "v_and_b32 v211, 0xffff0000, v139\n"
    "v_log_f32 v68, v204\n"
    "v_log_f32 v69, v205\n"
    "v_log_f32 v70, v206\n"
    "v_log_f32 v71, v207\n"
    "v_log_f32 v72, v208\n"
    "v_log_f32 v73, v209\n"
    "v_log_f32 v74, v210\n"
    "v_log_f32 v75, v211\n"
    "v_rcp_f32 v204, v204\n"
    "v_rcp_f32 v205, v205\n"
    "v_rcp_f32 v206, v206\n"
    "v_rcp_f32 v207, v207\n"
    "v_rcp_f32 v208, v208\n"
    "v_rcp_f32 v209, v209\n"
    "v_rcp_f32 v210, v210\n"
    "v_rcp_f32 v211, v211\n"
    "s_mov_b32 s73, 0x41a00000\n"
    "v_mov_b32 v228, 0x7fc00000\n"
    "v_cmp_le_f32 vcc, |v68|, s73\n"
    "s_nop 1\n"
    "v_cndmask_b32 v68, v228, v68, vcc\n"
    "v_cmp_le_f32 vcc, |v69|, s73\n"
    "s_nop 1\n"
    "v_cndmask_b32 v69, v228, v69, vcc\n"
    "v_cmp_le_f32 vcc, |v70|, s73\n"
    "s_nop 1\n"
    "v_cndmask_b32 v70, v228, v70, vcc\n"
    "v_cmp_le_f32 vcc, |v71|, s73\n"
    "s_nop 1\n"
    "v_cndmask_b32 v71, v228, v71, vcc\n"
    "s_add_u32 m0, s65, 74752\n"
    "s_add_u32 s72, s85, 7168\n"
    "buffer_load_dwordx4 v89, s[20:23], s72 offen offset:0 nt lds\n"
    "v_cmp_le_f32 vcc, |v72|, s73\n"
    "s_nop 1\n"
    "v_cndmask_b32 v72, v228, v72, vcc\n"
    "v_cmp_le_f32 vcc, |v73|, s73\n"
    "s_nop 1\n"
    "v_cndmask_b32 v73, v228, v73, vcc\n"
    "v_cmp_le_f32 vcc, |v74|, s73\n"
    "s_nop 1\n"
    "v_cndmask_b32 v74, v228, v74, vcc\n"
    "v_cmp_le_f32 vcc, |v75|, s73\n"
    "s_nop 1\n"
    "v_cndmask_b32 v75, v228, v75, vcc\n"
    "v_cvt_pk_bf16_f32 v220, v204, v205\n"
    "v_cvt_pk_bf16_f32 v221, v206, v207\n"
    "v_cvt_pk_bf16_f32 v222, v208, v209\n"
    "v_cvt_pk_bf16_f32 v223, v210, v211\n"
    "v_lshlrev_b32 v212, 16, v220\n"
    "v_and_b32 v213, 0xffff0000, v220\n"
    "v_lshlrev_b32 v214, 16, v221\n"
    "v_and_b32 v215, 0xffff0000, v221\n"
    "v_lshlrev_b32 v216, 16, v222\n"
    "v_and_b32 v217, 0xffff0000, v222\n"
    "v_lshlrev_b32 v218, 16, v223\n"
    "v_and_b32 v219, 0xffff0000, v223\n"
    "v_sub_f32 v204, v204, v212\n"
    "v_sub_f32 v205, v205, v213\n"
    "v_sub_f32 v206, v206, v214\n"
    "v_sub_f32 v207, v207, v215\n"
    "v_sub_f32 v208, v208, v216\n"
    "v_sub_f32 v209, v209, v217\n"
    "v_sub_f32 v210, v210, v218\n"
    "v_sub_f32 v211, v211, v219\n"
    "v_cvt_pk_bf16_f32 v224, v204, v205\n"
    "v_cvt_pk_bf16_f32 v225, v206, v207\n"
    "v_cvt_pk_bf16_f32 v226, v208, v209\n"
    "v_cvt_pk_bf16_f32 v227, v210, v211\n"
    "v_mov_b32 v204, 0\n"
    "v_mov_b32 v205, 0\n"
    "v_mov_b32 v206, 0\n"
    "s_add_u32 m0, s65, 75776\n"
    "s_add_u32 s72, s85, 8192\n"
    "buffer_load_dwordx4 v90, s[20:23], s72 offen offset:0 nt lds\n"
    "v_mov_b32 v207, 0\n"
    "v_swap_b32 v221, v222\n"
    "v_swap_b32 v225, v226\n"
    "v_swap_b32 v141, v142\n"
    "ds_write2_b64 v144, v[220:221], v[222:223] offset0:32 offset1:40\n"
    "ds_write2_b64 v144, v[224:225], v[226:227] offset0:48 offset1:56\n"
    "ds_write2_b64 v144, v[140:141], v[142:143] offset0:64 offset1:72\n"
    "ds_write2_b64 v144, v[204:205], v[206:207] offset0:80 offset1:88\n"
    "s_waitcnt vmcnt(26)\n"
    "v_cvt_f32_i32 v4, v4\n"
    "v_cvt_f32_i32 v5, v5\n"
    "v_cvt_f32_i32 v6, v6\n"
    "v_cvt_f32_i32 v7, v7\n"
    "v_cvt_f32_i32 v8, v8\n"
    "v_cvt_f32_i32 v9, v9\n"
    "v_cvt_f32_i32 v10, v10\n"
    "v_cvt_f32_i32 v11, v11\n"
    "v_cvt_f32_i32 v12, v12\n"
    "v_cvt_f32_i32 v13, v13\n"
    "v_cvt_f32_i32 v14, v14\n"
    "v_cvt_f32_i32 v15, v15\n"
    "v_cvt_f32_i32 v16, v16\n"
    "v_cvt_f32_i32 v17, v17\n"
    "v_cvt_f32_i32 v18, v18\n"
    "v_cvt_f32_i32 v19, v19\n"
    "v_cvt_f32_i32 v20, v20\n"
    "v_cvt_f32_i32 v21, v21\n"
    "v_cvt_f32_i32 v22, v22\n"
    "v_cvt_f32_i32 v23, v23\n"
    "v_cvt_f32_i32 v24, v24\n"
    "v_cvt_f32_i32 v25, v25\n"
    "v_cvt_f32_i32 v26, v26\n"
    "v_cvt_f32_i32 v27, v27\n"
    "v_fma_f32 v4, v4, v180, -v184\n"
    "v_fma_f32 v5, v5, v181, -v185\n"
    "v_fma_f32 v6, v6, v182, -v186\n"
    "s_add_u32 m0, s65, 76800\n"
    "s_add_u32 s72, s85, 9216\n"
    "buffer_load_dwordx4 v88, s[20:23], s72 offen offset:0 nt lds\n"
    "v_fma_f32 v7, v7, v183, -v187\n"
    "v_fma_f32 v8, v8, v180, -v184\n"
    "v_fma_f32 v9, v9, v181, -v185\n"
    "v_fma_f32 v10, v10, v182, -v186\n"
    "v_fma_f32 v11, v11, v183, -v187\n"
    "v_cvt_pk_bf16_f32 v4, v4, v5\n"
    "v_cvt_pk_bf16_f32 v5, v6, v7\n"
    "v_cvt_pk_bf16_f32 v6, v8, v9\n"
    "v_cvt_pk_bf16_f32 v7, v10, v11\n"
    "ds_write_b64 v163, v[4:5] offset:0\n"
    "ds_write_b64 v163, v[6:7] offset:1024\n"
    "v_fma_f32 v12, v12, v188, -v192\n"
    "v_fma_f32 v13, v13, v189, -v193\n"
    "v_fma_f32 v14, v14, v190, -v194\n"
    "v_fma_f32 v15, v15, v191, -v195\n"
    "v_fma_f32 v16, v16, v188, -v192\n"
    "v_fma_f32 v17, v17, v189, -v193\n"
    "v_fma_f32 v18, v18, v190, -v194\n"
    "v_fma_f32 v19, v19, v191, -v195\n"
    "v_cvt_pk_bf16_f32 v12, v12, v13\n"
    "v_cvt_pk_bf16_f32 v13, v14, v15\n"
    "v_cvt_pk_bf16_f32 v14, v16, v17\n"
    "v_cvt_pk_bf16_f32 v15, v18, v19\n"
    "ds_write_b64 v163, v[12:13] offset:2048\n"
    "ds_write_b64 v163, v[14:15] offset:3072\n"
    "v_fma_f32 v20, v20, v196, -v200\n"
    "v_fma_f32 v21, v21, v197, -v201\n"
    "v_fma_f32 v22, v22, v198, -v202\n"
    "v_fma_f32 v23, v23, v199, -v203\n"
    "v_fma_f32 v24, v24, v196, -v200\n"
    "v_fma_f32 v25, v25, v197, -v201\n"
    "v_fma_f32 v26, v26, v198, -v202\n"
    "v_fma_f32 v27, v27, v199, -v203\n"
    "v_cvt_pk_bf16_f32 v20, v20, v21\n"
    "v_cvt_pk_bf16_f32 v21, v22, v23\n"
    "s_add_u32 m0, s65, 77824\n"
    "s_add_u32 s72, s85, 10240\n"
    "buffer_load_dwordx4 v89, s[20:23], s72 offen offset:0 nt lds\n"
    "v_cvt_pk_bf16_f32 v22, v24, v25\n"
    "v_cvt_pk_bf16_f32 v23, v26, v27\n"
    "ds_write_b64 v163, v[20:21] offset:4096\n"
    "ds_write_b64 v163, v[22:23] offset:5120\n"
    "s_waitcnt vmcnt(13)\n"
    "v_cvt_f32_i32 v100, v100\n"
    "v_cvt_f32_i32 v101, v101\n"
    "v_cvt_f32_i32 v102, v102\n"
    "v_cvt_f32_i32 v103, v103\n"
    "v_cvt_f32_i32 v104, v104\n"
    "v_cvt_f32_i32 v105, v105\n"
    "v_cvt_f32_i32 v106, v106\n"
    "v_cvt_f32_i32 v107, v107\n"
    "v_cvt_f32_i32 v108, v108\n"
    "v_cvt_f32_i32 v109, v109\n"
    "v_cvt_f32_i32 v110, v110\n"
    "v_cvt_f32_i32 v111, v111\n"
    "v_cvt_f32_i32 v112, v112\n"
    "v_cvt_f32_i32 v113, v113\n"
    "v_cvt_f32_i32 v114, v114\n"
    "v_cvt_f32_i32 v115, v115\n"
    "v_cvt_f32_i32 v116, v116\n"
    "v_cvt_f32_i32 v117, v117\n"
    "v_cvt_f32_i32 v118, v118\n"
    "v_cvt_f32_i32 v119, v119\n"
    "v_cvt_f32_i32 v120, v120\n"
    "v_cvt_f32_i32 v121, v121\n"
    "v_cvt_f32_i32 v122, v122\n"
    "v_cvt_f32_i32 v123, v123\n"
    "v_fma_f32 v100, v100, v180, -v184\n"
    "v_fma_f32 v101, v101, v181, -v185\n"
    "v_fma_f32 v102, v102, v182, -v186\n"
    "v_fma_f32 v103, v103, v183, -v187\n"
    "v_fma_f32 v104, v104, v180, -v184\n"
    "v_fma_f32 v105, v105, v181, -v185\n"
    "s_add_u32 m0, s65, 78848\n"
    "s_add_u32 s72, s85, 11264\n"
    "buffer_load_dwordx4 v90, s[20:23], s72 offen offset:0 nt lds\n"
    "v_fma_f32 v106, v106, v182, -v186\n"
    "v_fma_f32 v107, v107, v183, -v187\n"
    "v_cvt_pk_bf16_f32 v100, v100, v101\n"
    "v_cvt_pk_bf16_f32 v101, v102, v103\n"
    "v_cvt_pk_bf16_f32 v102, v104, v105\n"
    "v_cvt_pk_bf16_f32 v103, v106, v107\n"
    "ds_write_b64 v163, v[100:101] offset:12288\n"
    "ds_write_b64 v163, v[102:103] offset:13312\n"
    "v_fma_f32 v108, v108, v188, -v192\n"
    "v_fma_f32 v109, v109, v189, -v193\n"
    "v_fma_f32 v110, v110, v190, -v194\n"
    "v_fma_f32 v111, v111, v191, -v195\n"
    "v_fma_f32 v112, v112, v188, -v192\n"
    "v_fma_f32 v113, v113, v189, -v193\n"
    "v_fma_f32 v114, v114, v190, -v194\n"
    "v_fma_f32 v115, v115, v191, -v195\n"
    "v_cvt_pk_bf16_f32 v108, v108, v109\n"
    "v_cvt_pk_bf16_f32 v109, v110, v111\n"
    "v_cvt_pk_bf16_f32 v110, v112, v113\n"
    "v_cvt_pk_bf16_f32 v111, v114, v115\n"
    "ds_write_b64 v163, v[108:109] offset:14336\n"
    "ds_write_b64 v163, v[110:111] offset:15360\n"
    "v_fma_f32 v116, v116, v196, -v200\n"
    "v_fma_f32 v117, v117, v197, -v201\n"
    "v_fma_f32 v118, v118, v198, -v202\n"
    "v_fma_f32 v119, v119, v199, -v203\n"
    "v_fma_f32 v120, v120, v196, -v200\n"
    "v_fma_f32 v121, v121, v197, -v201\n"
    "v_fma_f32 v122, v122, v198, -v202\n"
    "v_fma_f32 v123, v123, v199, -v203\n"
    "v_cvt_pk_bf16_f32 v116, v116, v117\n"
    "v_cvt_pk_bf16_f32 v117, v118, v119\n"
    "v_cvt_pk_bf16_f32 v118, v120, v121\n"
    "v_cvt_pk_bf16_f32 v119, v122, v123\n"
    "ds_write_b64 v163, v[116:117] offset:16384\n"
    "ds_write_b64 v163, v[118:119] offset:17408\n"
    "s_waitcnt vmcnt(15)\n"
    "ds_read_b128 v[36:39], v154 offset:0\n"
    "ds_read_b128 v[40:43], v155 offset:0\n"
    "ds_read_b128 v[44:47], v154 offset:128\n"
    "ds_read_b128 v[48:51], v155 offset:128\n"
    "ds_read_b128 v[52:55], v154 offset:256\n"
    "ds_read_b128 v[56:59], v155 offset:256\n"
    "s_waitcnt lgkmcnt(0)\n"
    "s_barrier\n"
    "ds_read_b128 v[156:159], v2 offset:0\n"
    "ds_read_b128 v[160:163], v2 offset:2048\n"
    "ds_read_b128 v[164:167], v2 offset:4096\n"
    "ds_read_b128 v[168:171], v2 offset:6144\n"
    "ds_read_b128 v[172:175], v2 offset:8192\n"
    "ds_read_b128 v[176:179], v2 offset:10240\n"
    "ds_read_b128 v[180:183], v2 offset:1024\n"
    "ds_read_b128 v[184:187], v2 offset:3072\n"
    "ds_read_b128 v[188:191], v2 offset:5120\n"
    "ds_read_b128 v[192:195], v2 offset:7168\n"
    "ds_read_b128 v[196:199], v2 offset:9216\n"
    "ds_read_b128 v[200:203], v2 offset:11264\n"
    "s_add_u32 m0, s65, 49152\n"
    "s_add_u32 s72, s85, 12288\n"
    "buffer_load_dwordx4 v88, s[20:23], s72 offen offset:0 nt lds\n"
    "v_pk_maximum3_f16 v80, v36, v37, v80\n"
    "v_pk_maximum3_f16 v80, v38, v39, v80\n"
    "v_pk_add_u16 v36, s89, v36 clamp\n"
    "v_pk_add_u16 v37, s89, v37 clamp\n"
    "v_pk_add_u16 v38, s89, v38 clamp\n"
    "v_pk_add_u16 v39, s89, v39 clamp\n"
    "v_pk_maximum3_f16 v80, v40, v41, v80\n"
    "v_pk_maximum3_f16 v80, v42, v43, v80\n"
    "v_pk_add_u16 v40, s89, v40 clamp\n"
    "v_pk_add_u16 v41, s89, v41 clamp\n"
    "v_pk_add_u16 v42, s89, v42 clamp\n"
    "v_pk_add_u16 v43, s89, v43 clamp\n"
    "v_pk_maximum3_f16 v80, v44, v45, v80\n"
    "v_pk_maximum3_f16 v80, v46, v47, v80\n"
    "v_pk_add_u16 v44, s89, v44 clamp\n"
    "v_pk_add_u16 v45, s89, v45 clamp\n"
    "v_pk_add_u16 v46, s89, v46 clamp\n"
    "v_pk_add_u16 v47, s89, v47 clamp\n"
    "v_pk_maximum3_f16 v80, v48, v49, v80\n"
    "v_pk_maximum3_f16 v80, v50, v51, v80\n"
    "v_pk_add_u16 v48, s89, v48 clamp\n"
    "v_pk_add_u16 v49, s89, v49 clamp\n"
    "v_pk_add_u16 v50, s89, v50 clamp\n"
    "v_pk_add_u16 v51, s89, v51 clamp\n"
    "v_pk_maximum3_f16 v80, v52, v53, v80\n"
    "v_pk_maximum3_f16 v80, v54, v55, v80\n"
    "v_pk_add_u16 v52, s89, v52 clamp\n"
    "v_pk_add_u16 v53, s89, v53 clamp\n"
    "v_pk_add_u16 v54, s89, v54 clamp\n"
    "v_pk_add_u16 v55, s89, v55 clamp\n"
    "v_pk_maximum3_f16 v80, v56, v57, v80\n"
    "v_pk_maximum3_f16 v80, v58, v59, v80\n"
    "v_pk_add_u16 v56, s89, v56 clamp\n"
    "v_pk_add_u16 v57, s89, v57 clamp\n"
    "v_pk_add_u16 v58, s89, v58 clamp\n"
    "v_pk_add_u16 v59, s89, v59 clamp\n"
    "v_mov_b32 v108, 0\n"
    "v_mov_b32 v109, 0\n"
    "v_mov_b32 v110, 0\n"
    "v_mov_b32 v111, 0\n"
    "v_mov_b32 v112, 0\n"
    "v_mov_b32 v113, 0\n"
    "v_mov_b32 v114, 0\n"
    "v_mov_b32 v115, 0\n"
    "v_mov_b32 v116, 0\n"
    "v_mov_b32 v117, 0\n"
    "v_mov_b32 v118, 0\n"
    "v_mov_b32 v119, 0\n"
    "v_mov_b32 v120, 0\n"
    "v_mov_b32 v121, 0\n"
    "v_mov_b32 v122, 0\n"
    "v_mov_b32 v123, 0\n"
    "s_waitcnt lgkmcnt(11)\n"
    "v_mfma_f32_16x16x32_bf16 v[4:7], v[156:159], v[36:39], 0\n"
    "s_waitcnt lgkmcnt(10)\n"
    "v_mfma_f32_16x16x32_bf16 v[4:7], v[160:163], v[40:43], v[4:7]\n"
    "s_waitcnt lgkmcnt(9)\n"
    "v_mfma_f32_16x16x32_bf16 v[4:7], v[164:167], v[44:47], v[4:7]\n"
    "s_waitcnt lgkmcnt(8)\n"
    "v_mfma_f32_16x16x32_bf16 v[4:7], v[168:171], v[48:51], v[4:7]\n"
    "s_waitcnt lgkmcnt(7)\n"
    "v_mfma_f32_16x16x32_bf16 v[4:7], v[172:175], v[52:55], v[4:7]\n"
    "s_waitcnt lgkmcnt(6)\n"
    "v_mfma_f32_16x16x32_bf16 v[4:7], v[176:179], v[56:59], v[4:7]\n"
    "s_waitcnt lgkmcnt(5)\n"
    "v_mfma_f32_16x16x32_bf16 v[20:23], v[180:183], v[36:39], 0\n"
    "v_accvgpr_write_b32 a192, v36\n"
    "v_accvgpr_write_b32 a193, v37\n"
    "v_accvgpr_write_b32 a194, v38\n"
    "v_accvgpr_write_b32 a195, v39\n"
    "s_waitcnt lgkmcnt(4)\n"
    "v_mfma_f32_16x16x32_bf16 v[20:23], v[184:187], v[40:43], v[20:23]\n"
    "v_accvgpr_write_b32 a196, v40\n"
    "v_accvgpr_write_b32 a197, v41\n"
    "v_accvgpr_write_b32 a198, v42\n"
    "v_accvgpr_write_b32 a199, v43\n"
    "s_waitcnt lgkmcnt(3)\n"
    "v_mfma_f32_16x16x32_bf16 v[20:23], v[188:191], v[44:47], v[20:23]\n"
    "v_accvgpr_write_b32 a200, v44\n"
    "v_accvgpr_write_b32 a201, v45\n"
    "v_accvgpr_write_b32 a202, v46\n"
    "v_accvgpr_write_b32 a203, v47\n"
    "s_waitcnt lgkmcnt(2)\n"
    "v_mfma_f32_16x16x32_bf16 v[20:23], v[192:195], v[48:51], v[20:23]\n"
    "v_accvgpr_write_b32 a204, v48\n"
    "v_accvgpr_write_b32 a205, v49\n"
    "v_accvgpr_write_b32 a206, v50\n"
    "v_accvgpr_write_b32 a207, v51\n"
    "s_waitcnt lgkmcnt(1)\n"
    "v_mfma_f32_16x16x32_bf16 v[20:23], v[196:199], v[52:55], v[20:23]\n"
    "v_accvgpr_write_b32 a208, v52\n"
    "v_accvgpr_write_b32 a209, v53\n"
    "v_accvgpr_write_b32 a210, v54\n"
    "v_accvgpr_write_b32 a211, v55\n"
    "s_waitcnt lgkmcnt(0)\n"
    "v_mfma_f32_16x16x32_bf16 v[20:23], v[200:203], v[56:59], v[20:23]\n"
    "v_accvgpr_write_b32 a212, v56\n"
    "v_accvgpr_write_b32 a213, v57\n"
    "v_accvgpr_write_b32 a214, v58\n"
    "v_accvgpr_write_b32 a215, v59\n"
    "s_add_u32 m0, s65, 50176\n"
    "s_add_u32 s72, s85, 13312\n"
    "buffer_load_dwordx4 v89, s[20:23], s72 offen offset:0 nt lds\n"
    "v_mov_b32 v77, 155904\n"
    "v_max3_f32 v76, v4, v5, v6\n"
    "v_max3_f32 v76, v76, v7, v20\n"
    "v_max3_f32 v76, v76, v21, v22\n"
    "v_max_f32 v76, v76, v23\n"
    "v_mov_b32 v78, v76\n"
    "s_nop 1\n"
    "v_permlane32_swap_b32 v76, v78\n"
    "v_max_f32 v76, v76, v78\n"
    "v_mov_b32 v78, v76\n"
    "s_nop 1\n"
    "v_permlane16_swap_b32 v76, v78\n"
    "v_max_f32 v76, v76, v78\n"
    "ds_max_f32 v77, v76\n"
    "s_add_u32 m0, s65, 51200\n"
    "s_add_u32 s72, s85, 14336\n"
    "buffer_load_dwordx4 v90, s[20:23], s72 offen offset:0 nt lds\n"
    "buffer_load_dwordx2 v[236:237], v87, s[36:39], 0 offen offset:0 sc1\n"
    "s_waitcnt lgkmcnt(0)\n"
    "s_barrier\n"
    "ds_read_b32 v76, v77\n"
    "s_waitcnt vmcnt(10)\n"
    "buffer_store_dwordx2 v[238:239], v240, s[36:39], s80 offen offset:0 sc1\n"
    "s_lshl_b32 s73, s79, 8\n"
    "s_add_u32 s80, s80, s73\n"
    "s_mul_i32 s73, s79, 98304\n"
    "s_add_u32 s71, s71, s73\n"
    "s_waitcnt lgkmcnt(0)\n"
    "v_add_f32 v76, 0xc2b00000, v76\n"
    "v_sub_f32 v68, v68, v76\n"
    "v_sub_f32 v69, v69, v76\n"
    "v_sub_f32 v70, v70, v76\n"
    "v_sub_f32 v71, v71, v76\n"
    "v_sub_f32 v72, v72, v76\n"
    "v_sub_f32 v73, v73, v76\n"
    "v_sub_f32 v74, v74, v76\n"
    "v_sub_f32 v75, v75, v76\n"
    "ds_write_b128 v86, v[68:71] offset:0\n"
    "ds_write_b128 v86, v[72:75] offset:16\n"
    "s_mul_i32 s73, s79, 768\n"
    "v_add_u32 v77, s73, v250\n"
    "s_add_u32 m0, s65, 52224\n"
    "s_add_u32 s72, s85, 15360\n"
    "buffer_load_dwordx4 v88, s[20:23], s72 offen offset:0 nt lds\n"
    "s_waitcnt lgkmcnt(0)\n"
    "s_barrier\n"
    "ds_read_b128 v[100:103], v77 offset:0\n"
    "ds_read_b128 v[104:107], v77 offset:16\n"
    "s_add_u32 m0, s65, 53248\n"
    "s_add_u32 s72, s85, 16384\n"
    "buffer_load_dwordx4 v89, s[20:23], s72 offen offset:0 nt lds\n"
    "s_waitcnt lgkmcnt(1)\n"
    "v_add_f32 v4, v4, v100\n"
    "v_add_f32 v5, v5, v101\n"
    "v_add_f32 v6, v6, v102\n"
    "v_add_f32 v7, v7, v103\n"
    "s_waitcnt lgkmcnt(0)\n"
    "v_add_f32 v20, v20, v104\n"
    "v_add_f32 v21, v21, v105\n"
    "v_add_f32 v22, v22, v106\n"
    "v_add_f32 v23, v23, v107\n"
    "v_exp_f32 v4, v4\n"
    "v_exp_f32 v5, v5\n"
    "v_exp_f32 v6, v6\n"
    "v_exp_f32 v7, v7\n"
    "v_exp_f32 v20, v20\n"
    "v_exp_f32 v21, v21\n"
    "v_exp_f32 v22, v22\n"
    "v_exp_f32 v23, v23\n"
    "s_add_u32 m0, s65, 54272\n"
    "s_add_u32 s72, s85, 17408\n"
    "buffer_load_dwordx4 v90, s[20:23], s72 offen offset:0 nt lds\n"
    "s_waitcnt vmcnt(8)\n"
    "ds_read_b128 v[56:59], v85 offset:24832\n"
    "ds_read_b128 v[52:55], v84 offset:24832\n"
    "ds_read_b128 v[48:51], v85 offset:24704\n"
    "ds_read_b128 v[44:47], v84 offset:24704\n"
    "ds_read_b128 v[40:43], v85 offset:24576\n"
    "ds_read_b128 v[36:39], v84 offset:24576\n"
    "s_waitcnt lgkmcnt(5)\n"
    "v_pk_maximum3_f16 v80, v56, v57, v80\n"
    "v_pk_maximum3_f16 v80, v58, v59, v80\n"
    "v_pk_add_u16 v56, s89, v56 clamp\n"
    "v_pk_add_u16 v57, s89, v57 clamp\n"
    "v_pk_add_u16 v58, s89, v58 clamp\n"
    "v_pk_add_u16 v59, s89, v59 clamp\n"
    "s_waitcnt lgkmcnt(4)\n"
    "v_pk_maximum3_f16 v80, v52, v53, v80\n"
    "v_pk_maximum3_f16 v80, v54, v55, v80\n"
    "v_pk_add_u16 v52, s89, v52 clamp\n"
    "v_pk_add_u16 v53, s89, v53 clamp\n"
    "v_pk_add_u16 v54, s89, v54 clamp\n"
    "v_pk_add_u16 v55, s89, v55 clamp\n"
    "v_mfma_f32_16x16x32_bf16 v[8:11], v[176:179], v[56:59], v[100:103]\n"
    "s_waitcnt lgkmcnt(3)\n"
    "v_pk_maximum3_f16 v80, v48, v49, v80\n"
    "v_pk_maximum3_f16 v80, v50, v51, v80\n"
    "v_pk_add_u16 v48, s89, v48 clamp\n"
    "v_pk_add_u16 v49, s89, v49 clamp\n"
    "v_pk_add_u16 v50, s89, v50 clamp\n"
    "v_pk_add_u16 v51, s89, v51 clamp\n"
    "v_mfma_f32_16x16x32_bf16 v[8:11], v[172:175], v[52:55], v[8:11]\n"
    "s_waitcnt lgkmcnt(2)\n"
    "v_pk_maximum3_f16 v80, v44, v45, v80\n"
    "v_pk_maximum3_f16 v80, v46, v47, v80\n"
    "v_pk_add_u16 v44, s89, v44 clamp\n"
    "v_pk_add_u16 v45, s89, v45 clamp\n"
    "v_pk_add_u16 v46, s89, v46 clamp\n"
    "v_pk_add_u16 v47, s89, v47 clamp\n"
    "v_mfma_f32_16x16x32_bf16 v[8:11], v[168:171], v[48:51], v[8:11]\n"
    "s_waitcnt lgkmcnt(1)\n"
    "v_pk_maximum3_f16 v80, v40, v41, v80\n"
    "v_pk_maximum3_f16 v80, v42, v43, v80\n"
    "v_pk_add_u16 v40, s89, v40 clamp\n"
    "v_pk_add_u16 v41, s89, v41 clamp\n"
    "v_pk_add_u16 v42, s89, v42 clamp\n"
    "v_pk_add_u16 v43, s89, v43 clamp\n"
    "v_mfma_f32_16x16x32_bf16 v[8:11], v[164:167], v[44:47], v[8:11]\n"
    "s_waitcnt lgkmcnt(0)\n"
    "v_pk_maximum3_f16 v80, v36, v37, v80\n"
    "v_pk_maximum3_f16 v80, v38, v39, v80\n"
    "v_pk_add_u16 v36, s89, v36 clamp\n"
    "v_pk_add_u16 v37, s89, v37 clamp\n"
    "v_pk_add_u16 v38, s89, v38 clamp\n"
    "v_pk_add_u16 v39, s89, v39 clamp\n"
    "v_mfma_f32_16x16x32_bf16 v[8:11], v[160:163], v[40:43], v[8:11]\n"
    "s_nop 0\n"
    "v_mfma_f32_16x16x32_bf16 v[8:11], v[156:159], v[36:39], v[8:11]\n"
    "v_mfma_f32_16x16x32_bf16 v[24:27], v[200:203], v[56:59], v[104:107]\n"
    "v_accvgpr_write_b32 a236, v56\n"
    "v_accvgpr_write_b32 a237, v57\n"
    "v_accvgpr_write_b32 a238, v58\n"
    "v_accvgpr_write_b32 a239, v59\n"
    "v_mfma_f32_16x16x32_bf16 v[24:27], v[196:199], v[52:55], v[24:27]\n"
    "v_accvgpr_write_b32 a232, v52\n"
    "v_accvgpr_write_b32 a233, v53\n"
    "v_accvgpr_write_b32 a234, v54\n"
    "v_accvgpr_write_b32 a235, v55\n"
    "v_mfma_f32_16x16x32_bf16 v[24:27], v[192:195], v[48:51], v[24:27]\n"
    "v_accvgpr_write_b32 a228, v48\n"
    "v_accvgpr_write_b32 a229, v49\n"
    "v_accvgpr_write_b32 a230, v50\n"
    "v_accvgpr_write_b32 a231, v51\n"
    "v_mfma_f32_16x16x32_bf16 v[24:27], v[188:191], v[44:47], v[24:27]\n"
    "v_accvgpr_write_b32 a224, v44\n"
    "v_accvgpr_write_b32 a225, v45\n"
    "v_accvgpr_write_b32 a226, v46\n"
    "v_accvgpr_write_b32 a227, v47\n"
    "v_mfma_f32_16x16x32_bf16 v[24:27], v[184:187], v[40:43], v[24:27]\n"
    "v_accvgpr_write_b32 a220, v40\n"
    "v_accvgpr_write_b32 a221, v41\n"
    "v_accvgpr_write_b32 a222, v42\n"
    "v_accvgpr_write_b32 a223, v43\n"
    "v_mfma_f32_16x16x32_bf16 v[24:27], v[180:183], v[36:39], v[24:27]\n"
    "v_accvgpr_write_b32 a216, v36\n"
    "v_accvgpr_write_b32 a217, v37\n"
    "v_accvgpr_write_b32 a218, v38\n"
    "v_accvgpr_write_b32 a219, v39\n"
    "s_add_u32 m0, s65, 98304\n"
    "s_add_u32 s72, s85, 18432\n"
    "buffer_load_dwordx4 v88, s[20:23], s72 offen offset:0 nt lds\n"
    "v_exp_f32 v8, v8\n"
    "v_exp_f32 v9, v9\n"
    "v_exp_f32 v10, v10\n"
    "v_exp_f32 v11, v11\n"
    "v_exp_f32 v24, v24\n"
    "v_exp_f32 v25, v25\n"
    "v_exp_f32 v26, v26\n"
    "v_exp_f32 v27, v27\n"
    "s_add_u32 m0, s65, 99328\n"
    "s_add_u32 s72, s85, 19456\n"
    "buffer_load_dwordx4 v89, s[20:23], s72 offen offset:0 nt lds\n"
    "s_add_u32 m0, s65, 100352\n"
    "s_add_u32 s72, s85, 20480\n"
    "buffer_load_dwordx4 v90, s[20:23], s72 offen offset:0 nt lds\n"
    "s_waitcnt vmcnt(7)\n"
    "v_cmp_eq_u64 s[54:55], v[236:237], v[238:239]\n"
    "s_nop 0\n"
    "s_cmp_eq_u64 s[54:55], -1\n"
    "s_cbranch_scc0 .LpollAV\n"
    ".LpollAVr:\n"
    "s_add_u32 m0, s65, 101376\n"
    "s_add_u32 s72, s85, 21504\n"
    "buffer_load_dwordx4 v88, s[20:23], s72 offen offset:0 nt lds\n"
    "s_add_u32 m0, s65, 24576\n"
    "s_add_u32 s72, s65, 49152\n"
    "buffer_load_dwordx4 v1, s[16:19], s72 offen offset:0 sc1 lds\n"
    "s_add_u32 m0, s65, 25600\n"
    "s_add_u32 s72, s65, 50176\n"
    "buffer_load_dwordx4 v1, s[16:19], s72 offen offset:0 sc1 lds\n"
    "s_add_u32 m0, s65, 102400\n"
    "s_add_u32 s72, s85, 22528\n"
    "buffer_load_dwordx4 v89, s[20:23], s72 offen offset:0 nt lds\n"
    "s_add_u32 m0, s65, 26624\n"
    "s_add_u32 s72, s65, 51200\n"
    "buffer_load_dwordx4 v1, s[16:19], s72 offen offset:0 sc1 lds\n"
    "s_add_u32 m0, s65, 27648\n"
    "s_add_u32 s72, s65, 52224\n"
    "buffer_load_dwordx4 v1, s[16:19], s72 offen offset:0 sc1 lds\n"
    "s_add_u32 m0, s65, 103424\n"
    "s_add_u32 s72, s85, 23552\n"
    "buffer_load_dwordx4 v90, s[20:23], s72 offen offset:0 nt lds\n"
    "s_add_u32 m0, s65, 28672\n"
    "s_add_u32 s72, s65, 53248\n"
    "buffer_load_dwordx4 v1, s[16:19], s72 offen offset:0 sc1 lds\n"
    "s_add_u32 m0, s65, 29696\n"
    "s_add_u32 s72, s65, 54272\n"
    "buffer_load_dwordx4 v1, s[16:19], s72 offen offset:0 sc1 lds\n"
    "s_add_u32 m0, s65, 73728\n"
    "s_add_u32 s72, s65, 73728\n"
    "buffer_load_dwordx4 v1, s[16:19], s72 offen offset:0 sc1 lds\n"
    "s_add_u32 m0, s65, 74752\n"
    "s_add_u32 s72, s65, 74752\n"
    "buffer_load_dwordx4 v1, s[16:19], s72 offen offset:0 sc1 lds\n"
    "s_add_u32 m0, s65, 75776\n"
    "s_add_u32 s72, s65, 75776\n"
    "buffer_load_dwordx4 v1, s[16:19], s72 offen offset:0 sc1 lds\n"
    "s_add_u32 m0, s65, 76800\n"
    "s_add_u32 s72, s65, 76800\n"
    "buffer_load_dwordx4 v1, s[16:19], s72 offen offset:0 sc1 lds\n"
    "s_add_u32 m0, s65, 77824\n"
    "s_add_u32 s72, s65, 77824\n"
    "buffer_load_dwordx4 v1, s[16:19], s72 offen offset:0 sc1 lds\n"
    "s_add_u32 m0, s65, 78848\n"
    "s_add_u32 s72, s65, 78848\n"
    "buffer_load_dwordx4 v1, s[16:19], s72 offen offset:0 sc1 lds\n"
    "s_bitcmp1_b32 s83, 1\n"
    "s_cbranch_scc1 .Lf4b\n"
    "s_waitcnt vmcnt(18)\n"
    "ds_read_b128 v[36:39], v84 offset:0\n"
    "ds_read_b128 v[40:43], v85 offset:0\n"
    "ds_read_b128 v[44:47], v84 offset:128\n"
    "ds_read_b128 v[48:51], v85 offset:128\n"
    "ds_read_b128 v[124:127], v84 offset:256\n"
    "ds_read_b128 v[128:131], v85 offset:256\n"
    "s_waitcnt lgkmcnt(5)\n"
    "v_pk_maximum3_f16 v80, v36, v37, v80\n"
    "v_pk_maximum3_f16 v80, v38, v39, v80\n"
    "v_pk_add_u16 v36, s89, v36 clamp\n"
    "v_pk_add_u16 v37, s89, v37 clamp\n"
    "v_pk_add_u16 v38, s89, v38 clamp\n"
    "v_pk_add_u16 v39, s89, v39 clamp\n"
    "s_waitcnt lgkmcnt(4)\n"
    "v_pk_maximum3_f16 v80, v40, v41, v80\n"
    "v_pk_maximum3_f16 v80, v42, v43, v80\n"
    "v_pk_add_u16 v40, s89, v40 clamp\n"
    "v_pk_add_u16 v41, s89, v41 clamp\n"
    "v_pk_add_u16 v42, s89, v42 clamp\n"
    "v_pk_add_u16 v43, s89, v43 clamp\n"
    "v_mfma_f32_16x16x32_bf16 v[12:15], v[156:159], v[36:39], v[100:103]\n"
    "s_waitcnt lgkmcnt(3)\n"
    "v_pk_maximum3_f16 v80, v44, v45, v80\n"
    "v_pk_maximum3_f16 v80, v46, v47, v80\n"
    "v_pk_add_u16 v44, s89, v44 clamp\n"
    "v_pk_add_u16 v45, s89, v45 clamp\n"
    "v_pk_add_u16 v46, s89, v46 clamp\n"
    "v_pk_add_u16 v47, s89, v47 clamp\n"
    "v_mfma_f32_16x16x32_bf16 v[12:15], v[160:163], v[40:43], v[12:15]\n"
    "s_waitcnt lgkmcnt(2)\n"
    "v_pk_maximum3_f16 v80, v48, v49, v80\n"
    "v_pk_maximum3_f16 v80, v50, v51, v80\n"
    "v_pk_add_u16 v48, s89, v48 clamp\n"
    "v_pk_add_u16 v49, s89, v49 clamp\n"
    "v_pk_add_u16 v50, s89, v50 clamp\n"
    "v_pk_add_u16 v51, s89, v51 clamp\n"
    "v_mfma_f32_16x16x32_bf16 v[12:15], v[164:167], v[44:47], v[12:15]\n"
    "s_waitcnt lgkmcnt(1)\n"
    "v_pk_maximum3_f16 v80, v124, v125, v80\n"
    "v_pk_maximum3_f16 v80, v126, v127, v80\n"
    "v_pk_add_u16 v124, s89, v124 clamp\n"
    "v_pk_add_u16 v125, s89, v125 clamp\n"
    "v_pk_add_u16 v126, s89, v126 clamp\n"
    "v_pk_add_u16 v127, s89, v127 clamp\n"
    "v_mfma_f32_16x16x32_bf16 v[12:15], v[168:171], v[48:51], v[12:15]\n"
    "s_waitcnt lgkmcnt(0)\n"
    "v_pk_maximum3_f16 v80, v128, v129, v80\n"
    "v_pk_maximum3_f16 v80, v130, v131, v80\n"
    "v_pk_add_u16 v128, s89, v128 clamp\n"
    "v_pk_add_u16 v129, s89, v129 clamp\n"
    "v_pk_add_u16 v130, s89, v130 clamp\n"
    "v_pk_add_u16 v131, s89, v131 clamp\n"
    "v_mfma_f32_16x16x32_bf16 v[12:15], v[172:175], v[124:127], v[12:15]\n"
    "s_nop 0\n"
    "v_mfma_f32_16x16x32_bf16 v[12:15], v[176:179], v[128:131], v[12:15]\n"
    "v_mfma_f32_16x16x32_bf16 v[28:31], v[180:183], v[36:39], v[104:107]\n"
    "v_accvgpr_write_b32 a240, v36\n"
    "v_accvgpr_write_b32 a241, v37\n"
    "v_accvgpr_write_b32 a242, v38\n"
    "v_accvgpr_write_b32 a243, v39\n"
    "v_mfma_f32_16x16x32_bf16 v[28:31], v[184:187], v[40:43], v[28:31]\n"
    "v_accvgpr_write_b32 a244, v40\n"
    "v_accvgpr_write_b32 a245, v41\n"
    "v_accvgpr_write_b32 a246, v42\n"
    "v_accvgpr_write_b32 a247, v43\n"
    "v_mfma_f32_16x16x32_bf16 v[28:31], v[188:191], v[44:47], v[28:31]\n"
    "v_accvgpr_write_b32 a248, v44\n"
    "v_accvgpr_write_b32 a249, v45\n"
    "v_accvgpr_write_b32 a250, v46\n"
    "v_accvgpr_write_b32 a251, v47\n"
    "v_mfma_f32_16x16x32_bf16 v[28:31], v[192:195], v[48:51], v[28:31]\n"
    "v_accvgpr_write_b32 a252, v48\n"
    "v_accvgpr_write_b32 a253, v49\n"
    "v_accvgpr_write_b32 a254, v50\n"
    "v_accvgpr_write_b32 a255, v51\n"
    "v_mfma_f32_16x16x32_bf16 v[28:31], v[196:199], v[124:127], v[28:31]\n"
    "v_mfma_f32_16x16x32_bf16 v[28:31], v[200:203], v[128:131], v[28:31]\n"
    "v_exp_f32 v12, v12\n"
    "v_exp_f32 v13, v13\n"
    "v_exp_f32 v14, v14\n"
    "v_exp_f32 v15, v15\n"
    "s_nop 3\n"
    "v_exp_f32 v28, v28\n"
    "v_exp_f32 v29, v29\n"
    "v_exp_f32 v30, v30\n"
    "v_exp_f32 v31, v31\n"
    "s_waitcnt vmcnt(8)\n"
    "ds_read_b128 v[152:155], v85 offset:49408\n"
    "ds_read_b128 v[148:151], v84 offset:49408\n"
    "ds_read_b128 v[144:147], v85 offset:49280\n"
    "ds_read_b128 v[140:143], v84 offset:49280\n"
    "ds_read_b128 v[136:139], v85 offset:49152\n"
    "ds_read_b128 v[132:135], v84 offset:49152\n"
    "s_waitcnt lgkmcnt(5)\n"
    "v_pk_maximum3_f16 v80, v152, v153, v80\n"
    "v_pk_maximum3_f16 v80, v154, v155, v80\n"
    "v_pk_add_u16 v152, s89, v152 clamp\n"
    "v_pk_add_u16 v153, s89, v153 clamp\n"
    "v_pk_add_u16 v154, s89, v154 clamp\n"
    "v_pk_add_u16 v155, s89, v155 clamp\n"
    "s_waitcnt lgkmcnt(4)\n"
    "v_pk_maximum3_f16 v80, v148, v149, v80\n"
    "v_pk_maximum3_f16 v80, v150, v151, v80\n"
    "v_pk_add_u16 v148, s89, v148 clamp\n"
    "v_pk_add_u16 v149, s89, v149 clamp\n"
    "v_pk_add_u16 v150, s89, v150 clamp\n"
    "v_pk_add_u16 v151, s89, v151 clamp\n"
    "v_mfma_f32_16x16x32_bf16 v[16:19], v[176:179], v[152:155], v[100:103]\n"
    "s_waitcnt lgkmcnt(3)\n"
    "v_pk_maximum3_f16 v80, v144, v145, v80\n"
    "v_pk_maximum3_f16 v80, v146, v147, v80\n"
    "v_pk_add_u16 v144, s89, v144 clamp\n"
    "v_pk_add_u16 v145, s89, v145 clamp\n"
    "v_pk_add_u16 v146, s89, v146 clamp\n"
    "v_pk_add_u16 v147, s89, v147 clamp\n"
    "v_mfma_f32_16x16x32_bf16 v[16:19], v[172:175], v[148:151], v[16:19]\n"
    "s_waitcnt lgkmcnt(2)\n"
    "v_pk_maximum3_f16 v80, v140, v141, v80\n"
    "v_pk_maximum3_f16 v80, v142, v143, v80\n"
    "v_pk_add_u16 v140, s89, v140 clamp\n"
    "v_pk_add_u16 v141, s89, v141 clamp\n"
    "v_pk_add_u16 v142, s89, v142 clamp\n"
    "v_pk_add_u16 v143, s89, v143 clamp\n"
    "v_mfma_f32_16x16x32_bf16 v[16:19], v[168:171], v[144:147], v[16:19]\n"
    "s_waitcnt lgkmcnt(1)\n"
    "v_pk_maximum3_f16 v80, v136, v137, v80\n"
    "v_pk_maximum3_f16 v80, v138, v139, v80\n"
    "v_pk_add_u16 v136, s89, v136 clamp\n"
    "v_pk_add_u16 v137, s89, v137 clamp\n"
    "v_pk_add_u16 v138, s89, v138 clamp\n"
    "v_pk_add_u16 v139, s89, v139 clamp\n"
    "v_mfma_f32_16x16x32_bf16 v[16:19], v[164:167], v[140:143], v[16:19]\n"
    "s_waitcnt lgkmcnt(0)\n"
    "v_pk_maximum3_f16 v80, v132, v133, v80\n"
    "v_pk_maximum3_f16 v80, v134, v135, v80\n"
    "v_pk_add_u16 v132, s89, v132 clamp\n"
    "v_pk_add_u16 v133, s89, v133 clamp\n"
    "v_pk_add_u16 v134, s89, v134 clamp\n"
    "v_pk_add_u16 v135, s89, v135 clamp\n"
    "v_mfma_f32_16x16x32_bf16 v[16:19], v[160:163], v[136:139], v[16:19]\n"
    "s_nop 0\n"
    "v_mfma_f32_16x16x32_bf16 v[16:19], v[156:159], v[132:135], v[16:19]\n"
    "v_mfma_f32_16x16x32_bf16 v[32:35], v[200:203], v[152:155], v[104:107]\n"
    "v_mfma_f32_16x16x32_bf16 v[32:35], v[196:199], v[148:151], v[32:35]\n"
    "v_mfma_f32_16x16x32_bf16 v[32:35], v[192:195], v[144:147], v[32:35]\n"
    "v_mfma_f32_16x16x32_bf16 v[32:35], v[188:191], v[140:143], v[32:35]\n"
    "v_mfma_f32_16x16x32_bf16 v[32:35], v[184:187], v[136:139], v[32:35]\n"
    "v_mfma_f32_16x16x32_bf16 v[32:35], v[180:183], v[132:135], v[32:35]\n"
    "ds_read_b128 v[156:159], v2 offset:12288\n"
    "ds_read_b128 v[160:163], v2 offset:14336\n"
    "ds_read_b128 v[164:167], v2 offset:16384\n"
    "ds_read_b128 v[168:171], v2 offset:18432\n"
    "ds_read_b128 v[172:175], v2 offset:20480\n"
    "ds_read_b128 v[176:179], v2 offset:22528\n"
    "ds_read_b128 v[100:103], v77 offset:128\n"
    "v_pk_add_u16 v80, s89, v80\n"
    "v_and_b32 v80, 0x80008000, v80\n"
    "v_cmp_ne_u32 s[34:35], 0, v80\n"
    "v_exp_f32 v16, v16\n"
    "v_exp_f32 v17, v17\n"
    "v_exp_f32 v18, v18\n"
    "v_exp_f32 v19, v19\n"
    "v_exp_f32 v32, v32\n"
    "v_exp_f32 v33, v33\n"
    "v_exp_f32 v34, v34\n"
    "v_exp_f32 v35, v35\n"
    "s_mov_b32 s66, 0\n"
    "s_waitcnt lgkmcnt(0)\n"
    "s_waitcnt vmcnt(0)\n"
    "ds_add_u32 v247, v246\n"
    "s_nop 7\n"
    "s_branch .LPV\n"
    "s_branch .Lf4e\n"
    ".Lf4b:\n"
    "s_add_u32 s75, s71, s63\n"
    "buffer_load_dwordx4 v[204:207], v242, s[40:43], s75 offen offset:0 nt\n"
    "buffer_load_dwordx4 v[208:211], v242, s[40:43], s75 offen offset:768 nt\n"
    "buffer_load_dwordx4 v[212:215], v242, s[40:43], s75 offen offset:256 nt\n"
    "buffer_load_dwordx4 v[216:219], v242, s[40:43], s75 offen offset:1024 nt\n"
    "buffer_load_dwordx4 v[220:223], v242, s[40:43], s75 offen offset:512 nt\n"
    "buffer_load_dwordx4 v[224:227], v242, s[40:43], s75 offen offset:1280 nt\n"
    "s_waitcnt vmcnt(24)\n"
    "ds_read_b128 v[36:39], v84 offset:0\n"
    "ds_read_b128 v[40:43], v85 offset:0\n"
    "ds_read_b128 v[44:47], v84 offset:128\n"
    "ds_read_b128 v[48:51], v85 offset:128\n"
    "ds_read_b128 v[124:127], v84 offset:256\n"
    "ds_read_b128 v[128:131], v85 offset:256\n"
    "s_waitcnt lgkmcnt(5)\n"
    "v_pk_maximum3_f16 v80, v36, v37, v80\n"
    "v_pk_maximum3_f16 v80, v38, v39, v80\n"
    "v_pk_add_u16 v36, s89, v36 clamp\n"
    "v_pk_add_u16 v37, s89, v37 clamp\n"
    "v_pk_add_u16 v38, s89, v38 clamp\n"
    "v_pk_add_u16 v39, s89, v39 clamp\n"
    "s_waitcnt lgkmcnt(4)\n"
    "v_pk_maximum3_f16 v80, v40, v41, v80\n"
    "v_pk_maximum3_f16 v80, v42, v43, v80\n"
    "v_pk_add_u16 v40, s89, v40 clamp\n"
    "v_pk_add_u16 v41, s89, v41 clamp\n"
    "v_pk_add_u16 v42, s89, v42 clamp\n"
    "v_pk_add_u16 v43, s89, v43 clamp\n"
    "v_mfma_f32_16x16x32_bf16 v[12:15], v[156:159], v[36:39], v[100:103]\n"
    "s_waitcnt lgkmcnt(3)\n"
    "v_pk_maximum3_f16 v80, v44, v45, v80\n"
    "v_pk_maximum3_f16 v80, v46, v47, v80\n"
    "v_pk_add_u16 v44, s89, v44 clamp\n"
    "v_pk_add_u16 v45, s89, v45 clamp\n"
    "v_pk_add_u16 v46, s89, v46 clamp\n"
    "v_pk_add_u16 v47, s89, v47 clamp\n"
    "v_mfma_f32_16x16x32_bf16 v[12:15], v[160:163], v[40:43], v[12:15]\n"
    "s_waitcnt lgkmcnt(2)\n"
    "v_pk_maximum3_f16 v80, v48, v49, v80\n"
    "v_pk_maximum3_f16 v80, v50, v51, v80\n"
    "v_pk_add_u16 v48, s89, v48 clamp\n"
    "v_pk_add_u16 v49, s89, v49 clamp\n"
    "v_pk_add_u16 v50, s89, v50 clamp\n"
    "v_pk_add_u16 v51, s89, v51 clamp\n"
    "v_mfma_f32_16x16x32_bf16 v[12:15], v[164:167], v[44:47], v[12:15]\n"
    "s_waitcnt lgkmcnt(1)\n"
    "v_pk_maximum3_f16 v80, v124, v125, v80\n"
    "v_pk_maximum3_f16 v80, v126, v127, v80\n"
    "v_pk_add_u16 v124, s89, v124 clamp\n"
    "v_pk_add_u16 v125, s89, v125 clamp\n"
    "v_pk_add_u16 v126, s89, v126 clamp\n"
    "v_pk_add_u16 v127, s89, v127 clamp\n"
    "v_mfma_f32_16x16x32_bf16 v[12:15], v[168:171], v[48:51], v[12:15]\n"
    "s_waitcnt lgkmcnt(0)\n"
    "v_pk_maximum3_f16 v80, v128, v129, v80\n"
    "v_pk_maximum3_f16 v80, v130, v131, v80\n"
    "v_pk_add_u16 v128, s89, v128 clamp\n"
    "v_pk_add_u16 v129, s89, v129 clamp\n"
    "v_pk_add_u16 v130, s89, v130 clamp\n"
    "v_pk_add_u16 v131, s89, v131 clamp\n"
    "v_mfma_f32_16x16x32_bf16 v[12:15], v[172:175], v[124:127], v[12:15]\n"
    "s_nop 0\n"
    "v_mfma_f32_16x16x32_bf16 v[12:15], v[176:179], v[128:131], v[12:15]\n"
    "v_mfma_f32_16x16x32_bf16 v[28:31], v[180:183], v[36:39], v[104:107]\n"
    "v_accvgpr_write_b32 a240, v36\n"
    "v_accvgpr_write_b32 a241, v37\n"
    "v_accvgpr_write_b32 a242, v38\n"
    "v_accvgpr_write_b32 a243, v39\n"
    "v_mfma_f32_16x16x32_bf16 v[28:31], v[184:187], v[40:43], v[28:31]\n"
    "v_accvgpr_write_b32 a244, v40\n"
    "v_accvgpr_write_b32 a245, v41\n"
    "v_accvgpr_write_b32 a246, v42\n"
    "v_accvgpr_write_b32 a247, v43\n"
    "v_mfma_f32_16x16x32_bf16 v[28:31], v[188:191], v[44:47], v[28:31]\n"
    "v_accvgpr_write_b32 a248, v44\n"
    "v_accvgpr_write_b32 a249, v45\n"
    "v_accvgpr_write_b32 a250, v46\n"
    "v_accvgpr_write_b32 a251, v47\n"
    "v_mfma_f32_16x16x32_bf16 v[28:31], v[192:195], v[48:51], v[28:31]\n"
    "v_accvgpr_write_b32 a252, v48\n"
    "v_accvgpr_write_b32 a253, v49\n"
    "v_accvgpr_write_b32 a254, v50\n"
    "v_accvgpr_write_b32 a255, v51\n"
    "v_mfma_f32_16x16x32_bf16 v[28:31], v[196:199], v[124:127], v[28:31]\n"
    "v_mfma_f32_16x16x32_bf16 v[28:31], v[200:203], v[128:131], v[28:31]\n"
    "v_exp_f32 v12, v12\n"
    "v_exp_f32 v13, v13\n"
    "v_exp_f32 v14, v14\n"
    "v_exp_f32 v15, v15\n"
    "s_nop 3\n"
    "v_exp_f32 v28, v28\n"
    "v_exp_f32 v29, v29\n"
    "v_exp_f32 v30, v30\n"
    "v_exp_f32 v31, v31\n"
    "s_waitcnt vmcnt(14)\n"
    "ds_read_b128 v[152:155], v85 offset:49408\n"
    "ds_read_b128 v[148:151], v84 offset:49408\n"
    "ds_read_b128 v[144:147], v85 offset:49280\n"
    "ds_read_b128 v[140:143], v84 offset:49280\n"
    "ds_read_b128 v[136:139], v85 offset:49152\n"
    "ds_read_b128 v[132:135], v84 offset:49152\n"
    "s_waitcnt lgkmcnt(5)\n"
    "v_pk_maximum3_f16 v80, v152, v153, v80\n"
    "v_pk_maximum3_f16 v80, v154, v155, v80\n"
    "v_pk_add_u16 v152, s89, v152 clamp\n"
    "v_pk_add_u16 v153, s89, v153 clamp\n"
    "v_pk_add_u16 v154, s89, v154 clamp\n"
    "v_pk_add_u16 v155, s89, v155 clamp\n"
    "s_waitcnt lgkmcnt(4)\n"
    "v_pk_maximum3_f16 v80, v148, v149, v80\n"
    "v_pk_maximum3_f16 v80, v150, v151, v80\n"
    "v_pk_add_u16 v148, s89, v148 clamp\n"
    "v_pk_add_u16 v149, s89, v149 clamp\n"
    "v_pk_add_u16 v150, s89, v150 clamp\n"
    "v_pk_add_u16 v151, s89, v151 clamp\n"
    "v_mfma_f32_16x16x32_bf16 v[16:19], v[176:179], v[152:155], v[100:103]\n"
    "s_waitcnt lgkmcnt(3)\n"
    "v_pk_maximum3_f16 v80, v144, v145, v80\n"
    "v_pk_maximum3_f16 v80, v146, v147, v80\n"
    "v_pk_add_u16 v144, s89, v144 clamp\n"
    "v_pk_add_u16 v145, s89, v145 clamp\n"
    "v_pk_add_u16 v146, s89, v146 clamp\n"
    "v_pk_add_u16 v147, s89, v147 clamp\n"
    "v_mfma_f32_16x16x32_bf16 v[16:19], v[172:175], v[148:151], v[16:19]\n"
    "s_waitcnt lgkmcnt(2)\n"
    "v_pk_maximum3_f16 v80, v140, v141, v80\n"
    "v_pk_maximum3_f16 v80, v142, v143, v80\n"
    "v_pk_add_u16 v140, s89, v140 clamp\n"
    "v_pk_add_u16 v141, s89, v141 clamp\n"
    "v_pk_add_u16 v142, s89, v142 clamp\n"
    "v_pk_add_u16 v143, s89, v143 clamp\n"
    "v_mfma_f32_16x16x32_bf16 v[16:19], v[168:171], v[144:147], v[16:19]\n"
    "s_waitcnt lgkmcnt(1)\n"
    "v_pk_maximum3_f16 v80, v136, v137, v80\n"
    "v_pk_maximum3_f16 v80, v138, v139, v80\n"
    "v_pk_add_u16 v136, s89, v136 clamp\n"
    "v_pk_add_u16 v137, s89, v137 clamp\n"
    "v_pk_add_u16 v138, s89, v138 clamp\n"
    "v_pk_add_u16 v139, s89, v139 clamp\n"
    "v_mfma_f32_16x16x32_bf16 v[16:19], v[164:167], v[140:143], v[16:19]\n"
    "s_waitcnt lgkmcnt(0)\n"
    "v_pk_maximum3_f16 v80, v132, v133, v80\n"
    "v_pk_maximum3_f16 v80, v134, v135, v80\n"
    "v_pk_add_u16 v132, s89, v132 clamp\n"
    "v_pk_add_u16 v133, s89, v133 clamp\n"
    "v_pk_add_u16 v134, s89, v134 clamp\n"
    "v_pk_add_u16 v135, s89, v135 clamp\n"
    "v_mfma_f32_16x16x32_bf16 v[16:19], v[160:163], v[136:139], v[16:19]\n"
    "s_nop 0\n"
    "v_mfma_f32_16x16x32_bf16 v[16:19], v[156:159], v[132:135], v[16:19]\n"
    "v_mfma_f32_16x16x32_bf16 v[32:35], v[200:203], v[152:155], v[104:107]\n"
    "v_mfma_f32_16x16x32_bf16 v[32:35], v[196:199], v[148:151], v[32:35]\n"
    "v_mfma_f32_16x16x32_bf16 v[32:35], v[192:195], v[144:147], v[32:35]\n"
    "v_mfma_f32_16x16x32_bf16 v[32:35], v[188:191], v[140:143], v[32:35]\n"
    "v_mfma_f32_16x16x32_bf16 v[32:35], v[184:187], v[136:139], v[32:35]\n"
    "v_mfma_f32_16x16x32_bf16 v[32:35], v[180:183], v[132:135], v[32:35]\n"
    "ds_read_b128 v[156:159], v2 offset:12288\n"
    "ds_read_b128 v[160:163], v2 offset:14336\n"
    "ds_read_b128 v[164:167], v2 offset:16384\n"
    "ds_read_b128 v[168:171], v2 offset:18432\n"
    "ds_read_b128 v[172:175], v2 offset:20480\n"
    "ds_read_b128 v[176:179], v2 offset:22528\n"
    "ds_read_b128 v[100:103], v77 offset:128\n"
    "v_pk_add_u16 v80, s89, v80\n"
    "v_and_b32 v80, 0x80008000, v80\n"
    "v_cmp_ne_u32 s[34:35], 0, v80\n"
    "v_exp_f32 v16, v16\n"
    "v_exp_f32 v17, v17\n"
    "v_exp_f32 v18, v18\n"
    "v_exp_f32 v19, v19\n"
    "v_exp_f32 v32, v32\n"
    "v_exp_f32 v33, v33\n"
    "v_exp_f32 v34, v34\n"
    "v_exp_f32 v35, v35\n"
    "s_mov_b32 s66, 0\n"
    "s_waitcnt lgkmcnt(0)\n"
    "s_waitcnt vmcnt(6)\n"
    "ds_add_u32 v247, v246\n"
    "s_nop 7\n"
    "s_branch .LNV\n"
    ".Lf4e:\n"
    "s_nop 4\n"
    ".Lf5e:\n"
    "s_nop 4\n"
    ".p2align 6\n"
    ".LPK:\n"
    "v_mfma_f32_16x16x32_bf16 v[36:39], v[156:159], a[192:195], v[100:103]\n"
    "s_add_u32 s71, s71, 98304\n"
    "ds_read_b128 v[180:183], v2 offset:13312\n"
    "v_cvt_pk_bf16_f32 v68, v4, v5\n"
    "v_mfma_f32_16x16x32_bf16 v[36:39], v[160:163], a[196:199], v[36:39]\n"
    "s_add_u32 s75, s71, s63\n"
    "ds_read_b128 v[184:187], v2 offset:15360\n"
    "v_mfma_f32_16x16x32_bf16 v[36:39], v[164:167], a[200:203], v[36:39]\n"
    "s_add_u32 s76, s75, 6144\n"
    "ds_read_b128 v[188:191], v2 offset:17408\n"
    "v_mfma_f32_16x16x32_bf16 v[36:39], v[168:171], a[204:207], v[36:39]\n"
    "s_add_u32 s77, s75, 24576\n"
    "ds_read_b128 v[192:195], v2 offset:19456\n"
    "v_cvt_pk_bf16_f32 v69, v6, v7\n"
    "v_mfma_f32_16x16x32_bf16 v[36:39], v[172:175], a[208:211], v[36:39]\n"
    "s_add_u32 s78, s75, 30720\n"
    "ds_read_b128 v[196:199], v2 offset:21504\n"
    "v_mfma_f32_16x16x32_bf16 v[36:39], v[176:179], a[212:215], v[36:39]\n"
    "ds_read_b128 v[200:203], v2 offset:23552\n"
    "v_mfma_f32_16x16x32_bf16 v[40:43], v[176:179], a[236:239], v[100:103]\n"
    "ds_read_b128 v[104:107], v250 offset:144\n"
    "v_mfma_f32_16x16x32_bf16 v[40:43], v[172:175], a[232:235], v[40:43]\n"
    "s_add_u32 s68, s68, 24576\n"
    "v_cvt_pk_bf16_f32 v76, v8, v9\n"
    "v_mfma_f32_16x16x32_bf16 v[40:43], v[168:171], a[228:231], v[40:43]\n"
    "s_cmp_ge_u32 s68, s69\n"
    "s_cselect_b32 s68, s65, s68\n"
    "v_mfma_f32_16x16x32_bf16 v[40:43], v[164:167], a[224:227], v[40:43]\n"
    "v_exp_f32 v36, v36\n"
    "v_mfma_f32_16x16x32_bf16 v[40:43], v[160:163], a[220:223], v[40:43]\n"
    "v_exp_f32 v37, v37\n"
    "v_mfma_f32_16x16x32_bf16 v[40:43], v[156:159], a[216:219], v[40:43]\n"
    "v_cvt_pk_bf16_f32 v77, v10, v11\n"
    "v_cvt_pk_bf16_f32 v72, v36, v37\n"
    "v_mfma_f32_16x16x32_bf16 v[44:47], v[156:159], a[240:243], v[100:103]\n"
    "v_mfma_f32_16x16x32_bf16 v[44:47], v[160:163], a[244:247], v[44:47]\n"
    "v_exp_f32 v38, v38\n"
    "v_mfma_f32_16x16x32_bf16 v[44:47], v[164:167], a[248:251], v[44:47]\n"
    "v_mfma_f32_16x16x32_bf16 v[44:47], v[168:171], a[252:255], v[44:47]\n"
    "v_cvt_pk_bf16_f32 v84, v12, v13\n"
    "v_mfma_f32_16x16x32_bf16 v[44:47], v[172:175], v[124:127], v[44:47]\n"
    "v_exp_f32 v39, v39\n"
    "v_mfma_f32_16x16x32_bf16 v[44:47], v[176:179], v[128:131], v[44:47]\n"
    "v_cvt_pk_bf16_f32 v73, v38, v39\n"
    "v_mfma_f32_16x16x32_bf16 v[48:51], v[176:179], v[152:155], v[100:103]\n"
    "ds_read_b32 v245, v247\n"
    "v_cvt_pk_bf16_f32 v85, v14, v15\n"
    "v_mfma_f32_16x16x32_bf16 v[48:51], v[172:175], v[148:151], v[48:51]\n"
    "s_add_u32 m0, s68, 0\n"
    "v_exp_f32 v40, v40\n"
    "v_mfma_f32_16x16x32_bf16 v[48:51], v[168:171], v[144:147], v[48:51]\n"
    "v_mfma_f32_16x16x32_bf16 v[48:51], v[164:167], v[140:143], v[48:51]\n"
    "v_mfma_f32_16x16x32_bf16 v[48:51], v[160:163], v[136:139], v[48:51]\n"
    "s_waitcnt vmcnt(10)\n"
    "v_cmp_eq_u64 s[54:55], v[236:237], v[238:239]\n"
    "v_cvt_pk_bf16_f32 v92, v16, v17\n"
    "v_mfma_f32_16x16x32_bf16 v[48:51], v[156:159], v[132:135], v[48:51]\n"
    "s_waitcnt lgkmcnt(1)\n"
    "v_exp_f32 v41, v41\n"
    "v_mfma_f32_16x16x32_bf16 v[52:55], v[180:183], a[192:195], v[104:107]\n"
    "s_waitcnt lgkmcnt(0)\n"
    "v_cmp_le_u32 s[56:57], s59, v245\n"
    "v_cvt_pk_bf16_f32 v80, v40, v41\n"
    "v_mfma_f32_16x16x32_bf16 v[52:55], v[184:187], a[196:199], v[52:55]\n"
    "v_exp_f32 v42, v42\n"
    "v_mfma_f32_16x16x32_bf16 v[52:55], v[188:191], a[200:203], v[52:55]\n"
    "s_and_b64 s[54:55], s[54:55], s[56:57]\n"
    "s_cmp_eq_u64 s[54:55], -1\n"
    "v_mfma_f32_16x16x32_bf16 v[52:55], v[192:195], a[204:207], v[52:55]\n"
    "s_cbranch_scc0 .Lpoll1\n"
    ".Lpoll1r:\n"
    "v_mfma_f32_16x16x32_bf16 v[52:55], v[196:199], a[208:211], v[52:55]\n"
    "buffer_load_dwordx4 v1, s[16:19], s67 offen offset:0 sc1 lds\n"
    "v_mfma_f32_16x16x32_bf16 v[52:55], v[200:203], a[212:215], v[52:55]\n"
    "ds_read_b128 v[156:159], v3 offset:0\n"
    "ds_read_b128 v[160:163], v3 offset:2048\n"
    "v_mfma_f32_16x16x32_bf16 v[56:59], v[200:203], a[236:239], v[104:107]\n"
    "ds_read_b128 v[164:167], v3 offset:4096\n"
    "ds_read_b128 v[168:171], v3 offset:6144\n"
    "v_mfma_f32_16x16x32_bf16 v[56:59], v[196:199], a[232:235], v[56:59]\n"
    "ds_read_b128 v[172:175], v3 offset:8192\n"
    "ds_read_b128 v[176:179], v3 offset:10240\n"
    "v_mfma_f32_16x16x32_bf16 v[56:59], v[192:195], a[228:231], v[56:59]\n"
    "buffer_load_dwordx4 v1, s[16:19], s67 offen offset:1024 sc1 lds\n"
    "v_mfma_f32_16x16x32_bf16 v[56:59], v[188:191], a[224:227], v[56:59]\n"
    "ds_read_b128 v[100:103], v250 offset:768\n"
    "v_cvt_pk_bf16_f32 v93, v18, v19\n"
    "v_mfma_f32_16x16x32_bf16 v[56:59], v[184:187], a[220:223], v[56:59]\n"
    "v_cvt_pk_bf16_f32 v70, v20, v21\n"
    "v_cvt_pk_bf16_f32 v71, v22, v23\n"
    "v_mfma_f32_16x16x32_bf16 v[56:59], v[180:183], a[216:219], v[56:59]\n"
    "v_exp_f32 v43, v43\n"
    "v_mfma_f32_16x16x32_bf16 v[60:63], v[180:183], a[240:243], v[104:107]\n"
    "buffer_load_dwordx4 v1, s[16:19], s67 offen offset:2048 sc1 lds\n"
    "v_mfma_f32_16x16x32_bf16 v[60:63], v[184:187], a[244:247], v[60:63]\n"
    "v_cvt_pk_bf16_f32 v78, v24, v25\n"
    "v_cvt_pk_bf16_f32 v81, v42, v43\n"
    "v_mfma_f32_16x16x32_bf16 v[60:63], v[188:191], a[248:251], v[60:63]\n"
    "v_exp_f32 v44, v44\n"
    "v_mfma_f32_16x16x32_bf16 v[60:63], v[192:195], a[252:255], v[60:63]\n"
    "v_exp_f32 v45, v45\n"
    "v_mfma_f32_16x16x32_bf16 v[60:63], v[196:199], v[124:127], v[60:63]\n"
    "buffer_load_dwordx4 v1, s[16:19], s67 offen offset:3072 sc1 lds\n"
    "v_mfma_f32_16x16x32_bf16 v[60:63], v[200:203], v[128:131], v[60:63]\n"
    "s_add_u32 m0, s68, 4096\n"
    "v_cvt_pk_bf16_f32 v79, v26, v27\n"
    "v_cvt_pk_bf16_f32 v88, v44, v45\n"
    "v_mfma_f32_16x16x32_bf16 v[64:67], v[200:203], v[152:155], v[104:107]\n"
    "s_add_u32 s83, s67, 4096\n"
    "v_exp_f32 v46, v46\n"
    "v_mfma_f32_16x16x32_bf16 v[64:67], v[196:199], v[148:151], v[64:67]\n"
    "v_exp_f32 v47, v47\n"
    "v_mfma_f32_16x16x32_bf16 v[64:67], v[192:195], v[144:147], v[64:67]\n"
    "buffer_load_dwordx4 v1, s[16:19], s83 offen offset:0 sc1 lds\n"
    "v_mfma_f32_16x16x32_bf16 v[64:67], v[188:191], v[140:143], v[64:67]\n"
    "v_cvt_pk_bf16_f32 v86, v28, v29\n"
    "v_cvt_pk_bf16_f32 v89, v46, v47\n"
    "v_mfma_f32_16x16x32_bf16 v[64:67], v[184:187], v[136:139], v[64:67]\n"
    "v_exp_f32 v48, v48\n"
    "v_mfma_f32_16x16x32_bf16 v[64:67], v[180:183], v[132:135], v[64:67]\n"
    "s_waitcnt lgkmcnt(0)\n"
    "v_exp_f32 v49, v49\n"
    "v_mfma_f32_16x16x32_bf16 v[4:7], v[156:159], a[192:195], v[100:103]\n"
    "buffer_load_dwordx4 v1, s[16:19], s83 offen offset:1024 sc1 lds\n"
    "v_mfma_f32_16x16x32_bf16 v[4:7], v[160:163], a[196:199], v[4:7]\n"
    "s_add_u32 m0, s64, 0\n"
    "ds_read_b128 v[180:183], v3 offset:1024\n"
    "ds_read_b128 v[184:187], v3 offset:3072\n"
    "v_mfma_f32_16x16x32_bf16 v[4:7], v[164:167], a[200:203], v[4:7]\n"
    "s_add_u32 s83, s67, 24576\n"
    "ds_read_b128 v[188:191], v3 offset:5120\n"
    "v_cvt_pk_bf16_f32 v87, v30, v31\n"
    "v_mfma_f32_16x16x32_bf16 v[4:7], v[168:171], a[204:207], v[4:7]\n"
    "ds_read_b128 v[192:195], v3 offset:7168\n"
    "v_cvt_pk_bf16_f32 v96, v48, v49\n"
    "v_mfma_f32_16x16x32_bf16 v[4:7], v[172:175], a[208:211], v[4:7]\n"
    "buffer_load_dwordx4 v1, s[16:19], s83 offen offset:0 sc1 lds\n"
    "v_mfma_f32_16x16x32_bf16 v[4:7], v[176:179], a[212:215], v[4:7]\n"
    "ds_read_b128 v[196:199], v3 offset:9216\n"
    "ds_read_b128 v[200:203], v3 offset:11264\n"
    "v_mfma_f32_16x16x32_bf16 v[8:11], v[176:179], a[236:239], v[100:103]\n"
    "ds_read_b128 v[104:107], v250 offset:784\n"
    "v_cvt_pk_bf16_f32 v94, v32, v33\n"
    "v_mfma_f32_16x16x32_bf16 v[8:11], v[172:175], a[232:235], v[8:11]\n"
    "v_exp_f32 v52, v52\n"
    "v_mfma_f32_16x16x32_bf16 v[8:11], v[168:171], a[228:231], v[8:11]\n"
    "buffer_load_dwordx4 v1, s[16:19], s83 offen offset:1024 sc1 lds\n"
    "v_mfma_f32_16x16x32_bf16 v[8:11], v[164:167], a[224:227], v[8:11]\n"
    "v_cvt_pk_bf16_f32 v95, v34, v35\n"
    "v_mfma_f32_16x16x32_bf16 v[8:11], v[160:163], a[220:223], v[8:11]\n"
    "v_exp_f32 v53, v53\n"
    "v_mfma_f32_16x16x32_bf16 v[8:11], v[156:159], a[216:219], v[8:11]\n"
    "v_exp_f32 v54, v54\n"
    "v_mfma_f32_16x16x32_bf16 v[12:15], v[156:159], a[240:243], v[100:103]\n"
    "buffer_load_dwordx4 v1, s[16:19], s83 offen offset:2048 sc1 lds\n"
    "v_mfma_f32_16x16x32_bf16 v[12:15], v[160:163], a[244:247], v[12:15]\n"
    "v_cvt_pk_bf16_f32 v74, v52, v53\n"
    "v_mfma_f32_16x16x32_bf16 v[12:15], v[164:167], a[248:251], v[12:15]\n"
    "v_exp_f32 v55, v55\n"
    "v_mfma_f32_16x16x32_bf16 v[12:15], v[168:171], a[252:255], v[12:15]\n"
    "v_cvt_pk_bf16_f32 v75, v54, v55\n"
    "v_mfma_f32_16x16x32_bf16 v[12:15], v[172:175], v[124:127], v[12:15]\n"
    "buffer_load_dwordx4 v1, s[16:19], s83 offen offset:3072 sc1 lds\n"
    "v_mfma_f32_16x16x32_bf16 v[12:15], v[176:179], v[128:131], v[12:15]\n"
    "s_add_u32 m0, s64, 4096\n"
    "v_exp_f32 v56, v56\n"
    "v_mfma_f32_16x16x32_bf16 v[16:19], v[176:179], v[152:155], v[100:103]\n"
    "s_add_u32 s83, s67, 28672\n"
    "v_exp_f32 v50, v50\n"
    "v_mfma_f32_16x16x32_bf16 v[16:19], v[172:175], v[148:151], v[16:19]\n"
    "v_exp_f32 v51, v51\n"
    "v_mfma_f32_16x16x32_bf16 v[16:19], v[168:171], v[144:147], v[16:19]\n"
    "buffer_load_dwordx4 v1, s[16:19], s83 offen offset:0 sc1 lds\n"
    "v_mfma_f32_16x16x32_bf16 v[16:19], v[164:167], v[140:143], v[16:19]\n"
    "v_exp_f32 v57, v57\n"
    "v_mfma_f32_16x16x32_bf16 v[16:19], v[160:163], v[136:139], v[16:19]\n"
    "v_cvt_pk_bf16_f32 v82, v56, v57\n"
    "v_cvt_pk_bf16_f32 v97, v50, v51\n"
    "v_mfma_f32_16x16x32_bf16 v[16:19], v[156:159], v[132:135], v[16:19]\n"
    "s_waitcnt lgkmcnt(0)\n"
    "v_exp_f32 v58, v58\n"
    "v_mfma_f32_16x16x32_bf16 v[20:23], v[180:183], a[192:195], v[104:107]\n"
    "buffer_load_dwordx4 v1, s[16:19], s83 offen offset:1024 sc1 lds\n"
    "v_mfma_f32_16x16x32_bf16 v[20:23], v[184:187], a[196:199], v[20:23]\n"
    "s_add_u32 s67, s67, 49152\n"
    "ds_read_b128 v[156:159], v248 offset:0\n"
    "ds_read_b128 v[160:163], v248 offset:12288\n"
    "v_mfma_f32_16x16x32_bf16 v[20:23], v[188:191], a[200:203], v[20:23]\n"
    "s_add_u32 s59, s59, 4\n"
    "ds_read_b128 v[164:167], v248 offset:1024\n"
    "v_mfma_f32_16x16x32_bf16 v[20:23], v[192:195], a[204:207], v[20:23]\n"
    "s_waitcnt vmcnt(12)\n"
    "ds_read_b128 v[168:171], v248 offset:13312\n"
    "v_mfma_f32_16x16x32_bf16 v[20:23], v[196:199], a[208:211], v[20:23]\n"
    "buffer_store_dwordx2 v[238:239], v240, s[36:39], s80 offen offset:0 sc1\n"
    "v_mfma_f32_16x16x32_bf16 v[20:23], v[200:203], a[212:215], v[20:23]\n"
    "s_add_u32 s80, s80, 256\n"
    "ds_read_b128 v[172:175], v248 offset:2048\n"
    "ds_read_b128 v[176:179], v248 offset:14336\n"
    "v_mfma_f32_16x16x32_bf16 v[24:27], v[200:203], a[236:239], v[104:107]\n"
    "v_exp_f32 v59, v59\n"
    "v_mfma_f32_16x16x32_bf16 v[24:27], v[196:199], a[232:235], v[24:27]\n"
    "v_cvt_pk_bf16_f32 v83, v58, v59\n"
    "v_mfma_f32_16x16x32_bf16 v[24:27], v[192:195], a[228:231], v[24:27]\n"
    "buffer_load_dwordx2 v[204:205], v242, s[28:31], s75 offen offset:0 nt\n"
    "v_mfma_f32_16x16x32_bf16 v[24:27], v[188:191], a[224:227], v[24:27]\n"
    "v_exp_f32 v60, v60\n"
    "v_mfma_f32_16x16x32_bf16 v[24:27], v[184:187], a[220:223], v[24:27]\n"
    "buffer_load_dwordx2 v[206:207], v242, s[28:31], s75 offen offset:768 nt\n"
    "v_mfma_f32_16x16x32_bf16 v[24:27], v[180:183], a[216:219], v[24:27]\n"
    "v_exp_f32 v61, v61\n"
    "v_mfma_f32_16x16x32_bf16 v[28:31], v[180:183], a[240:243], v[104:107]\n"
    "buffer_load_dwordx2 v[208:209], v242, s[28:31], s75 offen offset:3072 nt\n"
    "v_mfma_f32_16x16x32_bf16 v[28:31], v[184:187], a[244:247], v[28:31]\n"
    "v_exp_f32 v62, v62\n"
    "v_mfma_f32_16x16x32_bf16 v[28:31], v[188:191], a[248:251], v[28:31]\n"
    "buffer_load_dwordx2 v[210:211], v242, s[28:31], s75 offen offset:3840 nt\n"
    "v_mfma_f32_16x16x32_bf16 v[28:31], v[192:195], a[252:255], v[28:31]\n"
    "v_exp_f32 v63, v63\n"
    "v_mfma_f32_16x16x32_bf16 v[28:31], v[196:199], v[124:127], v[28:31]\n"
    "buffer_load_dwordx2 v[212:213], v242, s[28:31], s76 offen offset:0 nt\n"
    "v_mfma_f32_16x16x32_bf16 v[28:31], v[200:203], v[128:131], v[28:31]\n"
    "v_cvt_pk_bf16_f32 v90, v60, v61\n"
    "v_cvt_pk_bf16_f32 v91, v62, v63\n"
    "v_mfma_f32_16x16x32_bf16 v[32:35], v[200:203], v[152:155], v[104:107]\n"
    "buffer_load_dwordx2 v[214:215], v242, s[28:31], s76 offen offset:768 nt\n"
    "v_mfma_f32_16x16x32_bf16 v[32:35], v[196:199], v[148:151], v[32:35]\n"
    "v_exp_f32 v64, v64\n"
    "v_mfma_f32_16x16x32_bf16 v[32:35], v[192:195], v[144:147], v[32:35]\n"
    "buffer_load_dwordx2 v[216:217], v242, s[28:31], s76 offen offset:3072 nt\n"
    "v_mfma_f32_16x16x32_bf16 v[32:35], v[188:191], v[140:143], v[32:35]\n"
    "v_exp_f32 v65, v65\n"
    "v_mfma_f32_16x16x32_bf16 v[32:35], v[184:187], v[136:139], v[32:35]\n"
    "v_exp_f32 v66, v66\n"
    "v_mfma_f32_16x16x32_bf16 v[32:35], v[180:183], v[132:135], v[32:35]\n"
    "s_waitcnt lgkmcnt(0)\n"
    "v_cvt_pk_bf16_f32 v98, v64, v65\n"
    "v_mfma_f32_16x16x32_bf16 a[0:3], v[156:159], v[68:71], a[0:3]\n"
    "ds_read_b128 v[180:183], v248 offset:3072\n"
    "v_mfma_f32_16x16x32_bf16 a[0:3], v[160:163], v[72:75], a[0:3]\n"
    "ds_read_b128 v[184:187], v248 offset:15360\n"
    "v_mfma_f32_16x16x32_bf16 a[16:19], v[168:171], v[72:75], a[16:19]\n"
    "ds_read_b128 v[188:191], v248 offset:4096\n"
    "v_mfma_f32_16x16x32_bf16 a[16:19], v[164:167], v[68:71], a[16:19]\n"
    "ds_read_b128 v[192:195], v248 offset:16384\n"
    "v_mfma_f32_16x16x32_bf16 a[32:35], v[172:175], v[68:71], a[32:35]\n"
    "ds_read_b128 v[196:199], v248 offset:5120\n"
    "v_mfma_f32_16x16x32_bf16 a[32:35], v[176:179], v[72:75], a[32:35]\n"
    "ds_read_b128 v[200:203], v248 offset:17408\n"
    "v_mfma_f32_16x16x32_bf16 a[36:39], v[176:179], v[80:83], a[36:39]\n"
    "ds_read_b128 v[104:107], v251 offset:0\n"
    "v_mfma_f32_16x16x32_bf16 a[36:39], v[172:175], v[76:79], a[36:39]\n"
    "v_exp_f32 v67, v67\n"
    "v_mfma_f32_16x16x32_bf16 a[20:23], v[164:167], v[76:79], a[20:23]\n"
    "v_cvt_pk_bf16_f32 v99, v66, v67\n"
    "v_mfma_f32_16x16x32_bf16 a[20:23], v[168:171], v[80:83], a[20:23]\n"
    "buffer_load_dwordx2 v[218:219], v242, s[28:31], s76 offen offset:3840 nt\n"
    "v_mfma_f32_16x16x32_bf16 a[4:7], v[160:163], v[80:83], a[4:7]\n"
    "v_exp_f32 v4, v4\n"
    "v_mfma_f32_16x16x32_bf16 a[4:7], v[156:159], v[76:79], a[4:7]\n"
    "buffer_load_dwordx2 v[220:221], v242, s[28:31], s77 offen offset:0 nt\n"
    "v_mfma_f32_16x16x32_bf16 a[8:11], v[156:159], v[84:87], a[8:11]\n"
    "v_exp_f32 v5, v5\n"
    "v_mfma_f32_16x16x32_bf16 a[8:11], v[160:163], v[88:91], a[8:11]\n"
    "buffer_load_dwordx2 v[222:223], v242, s[28:31], s77 offen offset:768 nt\n"
    "v_mfma_f32_16x16x32_bf16 a[24:27], v[168:171], v[88:91], a[24:27]\n"
    "v_exp_f32 v6, v6\n"
    "v_mfma_f32_16x16x32_bf16 a[24:27], v[164:167], v[84:87], a[24:27]\n"
    "buffer_load_dwordx2 v[224:225], v242, s[28:31], s77 offen offset:3072 nt\n"
    "v_mfma_f32_16x16x32_bf16 a[40:43], v[172:175], v[84:87], a[40:43]\n"
    "v_exp_f32 v7, v7\n"
    "v_mfma_f32_16x16x32_bf16 a[40:43], v[176:179], v[88:91], a[40:43]\n"
    "buffer_load_dwordx2 v[226:227], v242, s[28:31], s77 offen offset:3840 nt\n"
    "v_mfma_f32_16x16x32_bf16 a[44:47], v[176:179], v[96:99], a[44:47]\n"
    "v_exp_f32 v8, v8\n"
    "v_mfma_f32_16x16x32_bf16 a[44:47], v[172:175], v[92:95], a[44:47]\n"
    "buffer_load_dwordx2 v[228:229], v242, s[28:31], s78 offen offset:0 nt\n"
    "v_mfma_f32_16x16x32_bf16 a[28:31], v[164:167], v[92:95], a[28:31]\n"
    "v_exp_f32 v9, v9\n"
    "v_mfma_f32_16x16x32_bf16 a[28:31], v[168:171], v[96:99], a[28:31]\n"
    "buffer_load_dwordx2 v[230:231], v242, s[28:31], s78 offen offset:768 nt\n"
    "v_mfma_f32_16x16x32_bf16 a[12:15], v[160:163], v[96:99], a[12:15]\n"
    "v_exp_f32 v10, v10\n"
    "v_mfma_f32_16x16x32_bf16 a[12:15], v[156:159], v[92:95], a[12:15]\n"
    "s_waitcnt lgkmcnt(0)\n"
    "v_exp_f32 v11, v11\n"
    "v_mfma_f32_16x16x32_bf16 a[48:51], v[180:183], v[68:71], a[48:51]\n"
    "ds_read_b128 v[156:159], v248 offset:6144\n"
    "v_mfma_f32_16x16x32_bf16 a[48:51], v[184:187], v[72:75], a[48:51]\n"
    "ds_read_b128 v[160:163], v248 offset:18432\n"
    "v_mfma_f32_16x16x32_bf16 a[64:67], v[192:195], v[72:75], a[64:67]\n"
    "ds_read_b128 v[164:167], v248 offset:7168\n"
    "v_mfma_f32_16x16x32_bf16 a[64:67], v[188:191], v[68:71], a[64:67]\n"
    "ds_read_b128 v[168:171], v248 offset:19456\n"
    "v_mfma_f32_16x16x32_bf16 a[80:83], v[196:199], v[68:71], a[80:83]\n"
    "ds_read_b128 v[172:175], v248 offset:8192\n"
    "v_mfma_f32_16x16x32_bf16 a[80:83], v[200:203], v[72:75], a[80:83]\n"
    "ds_read_b128 v[176:179], v248 offset:20480\n"
    "v_smfmac_f32_16x16x64_bf16 v[108:111], v[104:107], v[68:75], v253\n"
    "s_add_u32 s81, s81, 128\n"
    "s_cmp_gt_u32 s81, 4096\n"
    "s_cselect_b32 s81, 0, s81\n"
    "v_smfmac_f32_16x16x64_bf16 v[112:115], v[104:107], v[76:83], v253\n"
    "v_mfma_f32_16x16x32_bf16 a[84:87], v[200:203], v[80:83], a[84:87]\n"
    "buffer_load_dwordx2 v[232:233], v242, s[28:31], s78 offen offset:3072 nt\n"
    "v_mfma_f32_16x16x32_bf16 a[84:87], v[196:199], v[76:79], a[84:87]\n"
    "v_exp_f32 v12, v12\n"
    "v_mfma_f32_16x16x32_bf16 a[68:71], v[188:191], v[76:79], a[68:71]\n"
    "buffer_load_dwordx2 v[234:235], v242, s[28:31], s78 offen offset:3840 nt\n"
    "v_mfma_f32_16x16x32_bf16 a[68:71], v[192:195], v[80:83], a[68:71]\n"
    "v_exp_f32 v13, v13\n"
    "v_mfma_f32_16x16x32_bf16 a[52:55], v[184:187], v[80:83], a[52:55]\n"
    "v_exp_f32 v14, v14\n"
    "v_mfma_f32_16x16x32_bf16 a[52:55], v[180:183], v[76:79], a[52:55]\n"
    "v_exp_f32 v15, v15\n"
    "v_mfma_f32_16x16x32_bf16 a[56:59], v[180:183], v[84:87], a[56:59]\n"
    "v_exp_f32 v16, v16\n"
    "v_mfma_f32_16x16x32_bf16 a[56:59], v[184:187], v[88:91], a[56:59]\n"
    "v_exp_f32 v17, v17\n"
    "v_mfma_f32_16x16x32_bf16 a[72:75], v[192:195], v[88:91], a[72:75]\n"
    "v_mfma_f32_16x16x32_bf16 a[72:75], v[188:191], v[84:87], a[72:75]\n"
    "v_exp_f32 v18, v18\n"
    "v_mfma_f32_16x16x32_bf16 a[88:91], v[196:199], v[84:87], a[88:91]\n"
    "v_mfma_f32_16x16x32_bf16 a[88:91], v[200:203], v[88:91], a[88:91]\n"
    "v_smfmac_f32_16x16x64_bf16 v[116:119], v[104:107], v[84:91], v253\n"
    "v_smfmac_f32_16x16x64_bf16 v[120:123], v[104:107], v[92:99], v253\n"
    "v_mfma_f32_16x16x32_bf16 a[92:95], v[200:203], v[96:99], a[92:95]\n"
    "v_exp_f32 v19, v19\n"
    "v_mfma_f32_16x16x32_bf16 a[92:95], v[196:199], v[92:95], a[92:95]\n"
    "v_exp_f32 v20, v20\n"
    "v_mfma_f32_16x16x32_bf16 a[76:79], v[188:191], v[92:95], a[76:79]\n"
    "v_mfma_f32_16x16x32_bf16 a[76:79], v[192:195], v[96:99], a[76:79]\n"
    "v_exp_f32 v21, v21\n"
    "v_mfma_f32_16x16x32_bf16 a[60:63], v[184:187], v[96:99], a[60:63]\n"
    "v_mfma_f32_16x16x32_bf16 a[60:63], v[180:183], v[92:95], a[60:63]\n"
    "s_waitcnt lgkmcnt(0)\n"
    "v_mfma_f32_16x16x32_bf16 a[96:99], v[156:159], v[68:71], a[96:99]\n"
    "ds_read_b128 v[180:183], v248 offset:9216\n"
    "v_mfma_f32_16x16x32_bf16 a[96:99], v[160:163], v[72:75], a[96:99]\n"
    "ds_read_b128 v[184:187], v248 offset:21504\n"
    "v_mfma_f32_16x16x32_bf16 a[112:115], v[168:171], v[72:75], a[112:115]\n"
    "ds_read_b128 v[188:191], v248 offset:10240\n"
    "v_mfma_f32_16x16x32_bf16 a[112:115], v[164:167], v[68:71], a[112:115]\n"
    "ds_read_b128 v[192:195], v248 offset:22528\n"
    "v_mfma_f32_16x16x32_bf16 a[128:131], v[172:175], v[68:71], a[128:131]\n"
    "buffer_load_dwordx2 v[236:237], v241, s[36:39], s81 offen offset:0 sc1\n"
    "v_mfma_f32_16x16x32_bf16 a[128:131], v[176:179], v[72:75], a[128:131]\n"
    "ds_read_b128 v[196:199], v248 offset:11264\n"
    "ds_read_b128 v[200:203], v248 offset:23552\n"
    "v_mfma_f32_16x16x32_bf16 a[132:135], v[176:179], v[80:83], a[132:135]\n"
    "v_exp_f32 v22, v22\n"
    "v_mfma_f32_16x16x32_bf16 a[132:135], v[172:175], v[76:79], a[132:135]\n"
    "v_exp_f32 v23, v23\n"
    "v_mfma_f32_16x16x32_bf16 a[116:119], v[164:167], v[76:79], a[116:119]\n"
    "v_exp_f32 v24, v24\n"
    "v_mfma_f32_16x16x32_bf16 a[116:119], v[168:171], v[80:83], a[116:119]\n"
    "v_exp_f32 v25, v25\n"
    "v_mfma_f32_16x16x32_bf16 a[100:103], v[160:163], v[80:83], a[100:103]\n"
    "v_mfma_f32_16x16x32_bf16 a[100:103], v[156:159], v[76:79], a[100:103]\n"
    "v_exp_f32 v26, v26\n"
    "v_mfma_f32_16x16x32_bf16 a[104:107], v[156:159], v[84:87], a[104:107]\n"
    "v_mfma_f32_16x16x32_bf16 a[104:107], v[160:163], v[88:91], a[104:107]\n"
    "v_exp_f32 v27, v27\n"
    "v_mfma_f32_16x16x32_bf16 a[120:123], v[168:171], v[88:91], a[120:123]\n"
    "v_mfma_f32_16x16x32_bf16 a[120:123], v[164:167], v[84:87], a[120:123]\n"
    "v_mfma_f32_16x16x32_bf16 a[136:139], v[172:175], v[84:87], a[136:139]\n"
    "v_exp_f32 v28, v28\n"
    "v_mfma_f32_16x16x32_bf16 a[136:139], v[176:179], v[88:91], a[136:139]\n"
    "v_mfma_f32_16x16x32_bf16 a[140:143], v[176:179], v[96:99], a[140:143]\n"
    "v_mfma_f32_16x16x32_bf16 a[140:143], v[172:175], v[92:95], a[140:143]\n"
    "v_exp_f32 v29, v29\n"
    "v_mfma_f32_16x16x32_bf16 a[124:127], v[164:167], v[92:95], a[124:127]\n"
    "v_mfma_f32_16x16x32_bf16 a[124:127], v[168:171], v[96:99], a[124:127]\n"
    "v_exp_f32 v30, v30\n"
    "v_mfma_f32_16x16x32_bf16 a[108:111], v[160:163], v[96:99], a[108:111]\n"
    "v_mfma_f32_16x16x32_bf16 a[108:111], v[156:159], v[92:95], a[108:111]\n"
    "s_waitcnt lgkmcnt(0)\n"
    "v_mfma_f32_16x16x32_bf16 a[144:147], v[180:183], v[68:71], a[144:147]\n"
    "ds_read_b128 v[156:159], v3 offset:12288\n"
    "v_mfma_f32_16x16x32_bf16 a[144:147], v[184:187], v[72:75], a[144:147]\n"
    "ds_read_b128 v[160:163], v3 offset:14336\n"
    "v_mfma_f32_16x16x32_bf16 a[160:163], v[192:195], v[72:75], a[160:163]\n"
    "ds_read_b128 v[164:167], v3 offset:16384\n"
    "v_mfma_f32_16x16x32_bf16 a[160:163], v[188:191], v[68:71], a[160:163]\n"
    "ds_read_b128 v[168:171], v3 offset:18432\n"
    "v_mfma_f32_16x16x32_bf16 a[176:179], v[196:199], v[68:71], a[176:179]\n"
    "ds_read_b128 v[172:175], v3 offset:20480\n"
    "v_mfma_f32_16x16x32_bf16 a[176:179], v[200:203], v[72:75], a[176:179]\n"
    "ds_read_b128 v[176:179], v3 offset:22528\n"
    "v_mfma_f32_16x16x32_bf16 a[180:183], v[200:203], v[80:83], a[180:183]\n"
    "ds_read_b128 v[100:103], v250 offset:896\n"
    "v_mfma_f32_16x16x32_bf16 a[180:183], v[196:199], v[76:79], a[180:183]\n"
    "v_exp_f32 v31, v31\n"
    "v_mfma_f32_16x16x32_bf16 a[164:167], v[188:191], v[76:79], a[164:167]\n"
    "v_exp_f32 v32, v32\n"
    "v_mfma_f32_16x16x32_bf16 a[164:167], v[192:195], v[80:83], a[164:167]\n"
    "v_exp_f32 v33, v33\n"
    "v_mfma_f32_16x16x32_bf16 a[148:151], v[184:187], v[80:83], a[148:151]\n"
    "v_exp_f32 v34, v34\n"
    "v_mfma_f32_16x16x32_bf16 a[148:151], v[180:183], v[76:79], a[148:151]\n"
    "v_exp_f32 v35, v35\n"
    "v_mfma_f32_16x16x32_bf16 a[152:155], v[180:183], v[84:87], a[152:155]\n"
    "v_mfma_f32_16x16x32_bf16 a[152:155], v[184:187], v[88:91], a[152:155]\n"
    "v_mfma_f32_16x16x32_bf16 a[168:171], v[192:195], v[88:91], a[168:171]\n"
    "s_waitcnt vmcnt(18)\n"
    "ds_add_u32 v247, v246\n"
    "v_mfma_f32_16x16x32_bf16 a[168:171], v[188:191], v[84:87], a[168:171]\n"
    "v_mfma_f32_16x16x32_bf16 a[184:187], v[196:199], v[84:87], a[184:187]\n"
    "v_mfma_f32_16x16x32_bf16 a[184:187], v[200:203], v[88:91], a[184:187]\n"
    "v_mfma_f32_16x16x32_bf16 a[188:191], v[200:203], v[96:99], a[188:191]\n"
    "v_mfma_f32_16x16x32_bf16 a[188:191], v[196:199], v[92:95], a[188:191]\n"
    "v_mfma_f32_16x16x32_bf16 a[172:175], v[188:191], v[92:95], a[172:175]\n"
    "s_sub_u32 s74, s68, s65\n"
    "v_mfma_f32_16x16x32_bf16 a[172:175], v[192:195], v[96:99], a[172:175]\n"
    "s_add_u32 s66, s66, 1\n"
    "v_mov_b32 v2, v3\n"
    "v_add_u32 v3, s74, v252\n"
    "v_mfma_f32_16x16x32_bf16 a[156:159], v[184:187], v[96:99], a[156:159]\n"
    "s_waitcnt lgkmcnt(1)\n"
    "s_cmp_eq_u32 s66, 31\n"
    "v_mfma_f32_16x16x32_bf16 a[156:159], v[180:183], v[92:95], a[156:159]\n"
    "s_cbranch_scc1 .LtailP\n"
    ".LNK:\n"
    "v_mfma_f32_16x16x32_bf16 v[36:39], v[156:159], a[192:195], v[100:103]\n"
    "ds_read_b128 v[180:183], v2 offset:13312\n"
    "v_cvt_pk_bf16_f32 v68, v4, v5\n"
    "v_mfma_f32_16x16x32_bf16 v[36:39], v[160:163], a[196:199], v[36:39]\n"
    "ds_read_b128 v[184:187], v2 offset:15360\n"
    "v_cvt_pk_bf16_f32 v69, v6, v7\n"
    "v_mfma_f32_16x16x32_bf16 v[36:39], v[164:167], a[200:203], v[36:39]\n"
    "ds_read_b128 v[188:191], v2 offset:17408\n"
    "v_cvt_pk_bf16_f32 v76, v8, v9\n"
    "v_mfma_f32_16x16x32_bf16 v[36:39], v[168:171], a[204:207], v[36:39]\n"
    "ds_read_b128 v[192:195], v2 offset:19456\n"
    "v_cvt_pk_bf16_f32 v77, v10, v11\n"
    "v_mfma_f32_16x16x32_bf16 v[36:39], v[172:175], a[208:211], v[36:39]\n"
    "ds_read_b128 v[196:199], v2 offset:21504\n"
    "v_cvt_pk_bf16_f32 v84, v12, v13\n"
    "v_mfma_f32_16x16x32_bf16 v[36:39], v[176:179], a[212:215], v[36:39]\n"
    "ds_read_b128 v[200:203], v2 offset:23552\n"
    "v_cvt_pk_bf16_f32 v85, v14, v15\n"
    "v_mfma_f32_16x16x32_bf16 v[40:43], v[176:179], a[236:239], v[100:103]\n"
    "ds_read_b128 v[104:107], v250 offset:912\n"
    "v_cvt_pk_bf16_f32 v92, v16, v17\n"
    "v_mfma_f32_16x16x32_bf16 v[40:43], v[172:175], a[232:235], v[40:43]\n"
    "s_add_u32 s68, s68, 24576\n"
    "v_cvt_pk_bf16_f32 v93, v18, v19\n"
    "v_cvt_pk_bf16_f32 v70, v20, v21\n"
    "v_mfma_f32_16x16x32_bf16 v[40:43], v[168:171], a[228:231], v[40:43]\n"
    "s_cmp_ge_u32 s68, s69\n"
    "s_cselect_b32 s68, s65, s68\n"
    "v_cvt_pk_bf16_f32 v71, v22, v23\n"
    "v_mfma_f32_16x16x32_bf16 v[40:43], v[164:167], a[224:227], v[40:43]\n"
    "v_cvt_pk_bf16_f32 v78, v24, v25\n"
    "v_cvt_pk_bf16_f32 v79, v26, v27\n"
    "v_mfma_f32_16x16x32_bf16 v[40:43], v[160:163], a[220:223], v[40:43]\n"
    "v_cvt_pk_bf16_f32 v86, v28, v29\n"
    "v_cvt_pk_bf16_f32 v87, v30, v31\n"
    "v_mfma_f32_16x16x32_bf16 v[40:43], v[156:159], a[216:219], v[40:43]\n"
    "v_cvt_pk_bf16_f32 v94, v32, v33\n"
    "v_cvt_pk_bf16_f32 v95, v34, v35\n"
    "v_mfma_f32_16x16x32_bf16 v[44:47], v[156:159], a[240:243], v[100:103]\n"
    "v_exp_f32 v36, v36\n"
    "v_mfma_f32_16x16x32_bf16 v[44:47], v[160:163], a[244:247], v[44:47]\n"
    "v_exp_f32 v37, v37\n"
    "v_mfma_f32_16x16x32_bf16 v[44:47], v[164:167], a[248:251], v[44:47]\n"
    "v_exp_f32 v38, v38\n"
    "v_mfma_f32_16x16x32_bf16 v[44:47], v[168:171], a[252:255], v[44:47]\n"
    "v_cvt_pk_bf16_f32 v72, v36, v37\n"
    "v_mfma_f32_16x16x32_bf16 v[44:47], v[172:175], v[124:127], v[44:47]\n"
    "v_exp_f32 v39, v39\n"
    "v_mfma_f32_16x16x32_bf16 v[44:47], v[176:179], v[128:131], v[44:47]\n"
    "v_cvt_pk_bf16_f32 v73, v38, v39\n"
    "v_mfma_f32_16x16x32_bf16 v[48:51], v[176:179], v[152:155], v[100:103]\n"
    "ds_read_b32 v245, v247\n"
    "v_mfma_f32_16x16x32_bf16 v[48:51], v[172:175], v[148:151], v[48:51]\n"
    "s_add_u32 m0, s68, 0\n"
    "v_exp_f32 v40, v40\n"
    "v_mfma_f32_16x16x32_bf16 v[48:51], v[168:171], v[144:147], v[48:51]\n"
    "v_mfma_f32_16x16x32_bf16 v[48:51], v[164:167], v[140:143], v[48:51]\n"
    "v_mfma_f32_16x16x32_bf16 v[48:51], v[160:163], v[136:139], v[48:51]\n"
    "s_waitcnt vmcnt(0)\n"
    "v_cmp_eq_u64 s[54:55], v[236:237], v[238:239]\n"
    "v_mfma_f32_16x16x32_bf16 v[48:51], v[156:159], v[132:135], v[48:51]\n"
    "s_waitcnt lgkmcnt(1)\n"
    "v_exp_f32 v41, v41\n"
    "v_mfma_f32_16x16x32_bf16 v[52:55], v[180:183], a[192:195], v[104:107]\n"
    "s_waitcnt lgkmcnt(0)\n"
    "s_waitcnt vmcnt(1)\n"
    "v_cmp_le_u32 s[56:57], s59, v245\n"
    "v_mfma_f32_16x16x32_bf16 v[52:55], v[184:187], a[196:199], v[52:55]\n"
    "s_add_u32 s76, s71, s60\n"
    "v_exp_f32 v42, v42\n"
    "v_mfma_f32_16x16x32_bf16 v[52:55], v[188:191], a[200:203], v[52:55]\n"
    "s_and_b64 s[54:55], s[54:55], s[56:57]\n"
    "s_cmp_eq_u64 s[54:55], -1\n"
    "v_mfma_f32_16x16x32_bf16 v[52:55], v[192:195], a[204:207], v[52:55]\n"
    "s_cbranch_scc0 .Lpoll2\n"
    ".Lpoll2r:\n"
    "v_mfma_f32_16x16x32_bf16 v[52:55], v[196:199], a[208:211], v[52:55]\n"
    "buffer_load_dwordx4 v1, s[16:19], s67 offen offset:0 sc1 lds\n"
    "v_mfma_f32_16x16x32_bf16 v[52:55], v[200:203], a[212:215], v[52:55]\n"
    "s_add_u32 s77, s71, s61\n"
    "ds_read_b128 v[156:159], v3 offset:0\n"
    "ds_read_b128 v[160:163], v3 offset:2048\n"
    "v_mfma_f32_16x16x32_bf16 v[56:59], v[200:203], a[236:239], v[104:107]\n"
    "ds_read_b128 v[164:167], v3 offset:4096\n"
    "ds_read_b128 v[168:171], v3 offset:6144\n"
    "v_mfma_f32_16x16x32_bf16 v[56:59], v[196:199], a[232:235], v[56:59]\n"
    "ds_read_b128 v[172:175], v3 offset:8192\n"
    "ds_read_b128 v[176:179], v3 offset:10240\n"
    "v_mfma_f32_16x16x32_bf16 v[56:59], v[192:195], a[228:231], v[56:59]\n"
    "buffer_load_dwordx4 v1, s[16:19], s67 offen offset:1024 sc1 lds\n"
    "v_mfma_f32_16x16x32_bf16 v[56:59], v[188:191], a[224:227], v[56:59]\n"
    "ds_read_b128 v[100:103], v250 offset:1536\n"
    "v_cvt_pk_bf16_f32 v80, v40, v41\n"
    "v_mfma_f32_16x16x32_bf16 v[56:59], v[184:187], a[220:223], v[56:59]\n"
    "v_exp_f32 v43, v43\n"
    "v_mfma_f32_16x16x32_bf16 v[56:59], v[180:183], a[216:219], v[56:59]\n"
    "v_cvt_pk_bf16_f32 v81, v42, v43\n"
    "v_cvt_f32_i32 v204, v204\n"
    "v_mfma_f32_16x16x32_bf16 v[60:63], v[180:183], a[240:243], v[104:107]\n"
    "buffer_load_dwordx4 v1, s[16:19], s67 offen offset:2048 sc1 lds\n"
    "v_mfma_f32_16x16x32_bf16 v[60:63], v[184:187], a[244:247], v[60:63]\n"
    "v_exp_f32 v44, v44\n"
    "v_mfma_f32_16x16x32_bf16 v[60:63], v[188:191], a[248:251], v[60:63]\n"
    "v_exp_f32 v45, v45\n"
    "v_mfma_f32_16x16x32_bf16 v[60:63], v[192:195], a[252:255], v[60:63]\n"
    "v_exp_f32 v46, v46\n"
    "v_mfma_f32_16x16x32_bf16 v[60:63], v[196:199], v[124:127], v[60:63]\n"
    "buffer_load_dwordx4 v1, s[16:19], s67 offen offset:3072 sc1 lds\n"
    "v_mfma_f32_16x16x32_bf16 v[60:63], v[200:203], v[128:131], v[60:63]\n"
    "s_add_u32 m0, s68, 4096\n"
    "v_exp_f32 v47, v47\n"
    "v_mfma_f32_16x16x32_bf16 v[64:67], v[200:203], v[152:155], v[104:107]\n"
    "s_add_u32 s83, s67, 4096\n"
    "v_cvt_pk_bf16_f32 v88, v44, v45\n"
    "v_cvt_pk_bf16_f32 v89, v46, v47\n"
    "v_mfma_f32_16x16x32_bf16 v[64:67], v[196:199], v[148:151], v[64:67]\n"
    "v_exp_f32 v48, v48\n"
    "v_mfma_f32_16x16x32_bf16 v[64:67], v[192:195], v[144:147], v[64:67]\n"
    "buffer_load_dwordx4 v1, s[16:19], s83 offen offset:0 sc1 lds\n"
    "v_mfma_f32_16x16x32_bf16 v[64:67], v[188:191], v[140:143], v[64:67]\n"
    "v_exp_f32 v49, v49\n"
    "v_mfma_f32_16x16x32_bf16 v[64:67], v[184:187], v[136:139], v[64:67]\n"
    "v_cvt_pk_bf16_f32 v96, v48, v49\n"
    "v_cvt_f32_i32 v205, v205\n"
    "v_mfma_f32_16x16x32_bf16 v[64:67], v[180:183], v[132:135], v[64:67]\n"
    "s_waitcnt lgkmcnt(0)\n"
    "v_cvt_f32_i32 v206, v206\n"
    "v_cvt_f32_i32 v207, v207\n"
    "v_mfma_f32_16x16x32_bf16 v[4:7], v[156:159], a[192:195], v[100:103]\n"
    "buffer_load_dwordx4 v1, s[16:19], s83 offen offset:1024 sc1 lds\n"
    "v_mfma_f32_16x16x32_bf16 v[4:7], v[160:163], a[196:199], v[4:7]\n"
    "s_add_u32 m0, s70, 0\n"
    "ds_read_b128 v[180:183], v3 offset:1024\n"
    "ds_read_b128 v[184:187], v3 offset:3072\n"
    "v_mfma_f32_16x16x32_bf16 v[4:7], v[164:167], a[200:203], v[4:7]\n"
    "s_add_u32 s83, s67, 24576\n"
    "ds_read_b128 v[188:191], v3 offset:5120\n"
    "v_cvt_f32_i32 v208, v208\n"
    "v_mfma_f32_16x16x32_bf16 v[4:7], v[168:171], a[204:207], v[4:7]\n"
    "ds_read_b128 v[192:195], v3 offset:7168\n"
    "v_cvt_f32_i32 v209, v209\n"
    "v_mfma_f32_16x16x32_bf16 v[4:7], v[172:175], a[208:211], v[4:7]\n"
    "buffer_load_dwordx4 v1, s[16:19], s83 offen offset:0 sc1 lds\n"
    "v_mfma_f32_16x16x32_bf16 v[4:7], v[176:179], a[212:215], v[4:7]\n"
    "ds_read_b128 v[196:199], v3 offset:9216\n"
    "ds_read_b128 v[200:203], v3 offset:11264\n"
    "v_mfma_f32_16x16x32_bf16 v[8:11], v[176:179], a[236:239], v[100:103]\n"
    "ds_read_b128 v[104:107], v250 offset:1552\n"
    "v_cvt_f32_i32 v210, v210\n"
    "v_mfma_f32_16x16x32_bf16 v[8:11], v[172:175], a[232:235], v[8:11]\n"
    "v_exp_f32 v52, v52\n"
    "v_mfma_f32_16x16x32_bf16 v[8:11], v[168:171], a[228:231], v[8:11]\n"
    "buffer_load_dwordx4 v1, s[16:19], s83 offen offset:1024 sc1 lds\n"
    "v_mfma_f32_16x16x32_bf16 v[8:11], v[164:167], a[224:227], v[8:11]\n"
    "v_exp_f32 v53, v53\n"
    "v_mfma_f32_16x16x32_bf16 v[8:11], v[160:163], a[220:223], v[8:11]\n"
    "v_cvt_pk_bf16_f32 v74, v52, v53\n"
    "v_cvt_f32_i32 v211, v211\n"
    "v_mfma_f32_16x16x32_bf16 v[8:11], v[156:159], a[216:219], v[8:11]\n"
    "v_exp_f32 v54, v54\n"
    "v_mfma_f32_16x16x32_bf16 v[12:15], v[156:159], a[240:243], v[100:103]\n"
    "buffer_load_dwordx4 v1, s[16:19], s83 offen offset:2048 sc1 lds\n"
    "v_mfma_f32_16x16x32_bf16 v[12:15], v[160:163], a[244:247], v[12:15]\n"
    "v_exp_f32 v50, v50\n"
    "v_mfma_f32_16x16x32_bf16 v[12:15], v[164:167], a[248:251], v[12:15]\n"
    "v_exp_f32 v55, v55\n"
    "v_mfma_f32_16x16x32_bf16 v[12:15], v[168:171], a[252:255], v[12:15]\n"
    "v_cvt_pk_bf16_f32 v75, v54, v55\n"
    "v_cvt_f32_i32 v212, v212\n"
    "v_mfma_f32_16x16x32_bf16 v[12:15], v[172:175], v[124:127], v[12:15]\n"
    "buffer_load_dwordx4 v1, s[16:19], s83 offen offset:3072 sc1 lds\n"
    "v_mfma_f32_16x16x32_bf16 v[12:15], v[176:179], v[128:131], v[12:15]\n"
    "s_add_u32 m0, s70, 4096\n"
    "v_exp_f32 v56, v56\n"
    "v_mfma_f32_16x16x32_bf16 v[16:19], v[176:179], v[152:155], v[100:103]\n"
    "s_add_u32 s83, s67, 28672\n"
    "v_exp_f32 v51, v51\n"
    "v_mfma_f32_16x16x32_bf16 v[16:19], v[172:175], v[148:151], v[16:19]\n"
    "v_cvt_pk_bf16_f32 v97, v50, v51\n"
    "v_cvt_f32_i32 v213, v213\n"
    "v_mfma_f32_16x16x32_bf16 v[16:19], v[168:171], v[144:147], v[16:19]\n"
    "buffer_load_dwordx4 v1, s[16:19], s83 offen offset:0 sc1 lds\n"
    "v_mfma_f32_16x16x32_bf16 v[16:19], v[164:167], v[140:143], v[16:19]\n"
    "v_exp_f32 v57, v57\n"
    "v_mfma_f32_16x16x32_bf16 v[16:19], v[160:163], v[136:139], v[16:19]\n"
    "v_cvt_pk_bf16_f32 v82, v56, v57\n"
    "v_cvt_f32_i32 v214, v214\n"
    "v_mfma_f32_16x16x32_bf16 v[16:19], v[156:159], v[132:135], v[16:19]\n"
    "s_waitcnt lgkmcnt(0)\n"
    "v_exp_f32 v58, v58\n"
    "v_mfma_f32_16x16x32_bf16 v[20:23], v[180:183], a[192:195], v[104:107]\n"
    "buffer_load_dwordx4 v1, s[16:19], s83 offen offset:1024 sc1 lds\n"
    "v_mfma_f32_16x16x32_bf16 v[20:23], v[184:187], a[196:199], v[20:23]\n"
    "s_add_u32 s67, s67, 49152\n"
    "ds_read_b128 v[156:159], v249 offset:0\n"
    "ds_read_b128 v[160:163], v249 offset:12288\n"
    "v_mfma_f32_16x16x32_bf16 v[20:23], v[188:191], a[200:203], v[20:23]\n"
    "s_add_u32 s59, s59, 4\n"
    "ds_read_b128 v[164:167], v249 offset:1024\n"
    "v_cvt_f32_i32 v215, v215\n"
    "v_mfma_f32_16x16x32_bf16 v[20:23], v[192:195], a[204:207], v[20:23]\n"
    "ds_read_b128 v[168:171], v249 offset:13312\n"
    "ds_read_b128 v[36:39], v243 offset:0\n"
    "v_mfma_f32_16x16x32_bf16 v[20:23], v[196:199], a[208:211], v[20:23]\n"
    "ds_read_b128 v[172:175], v249 offset:2048\n"
    "v_add_u32 v243, 512, v243\n"
    "v_mfma_f32_16x16x32_bf16 v[20:23], v[200:203], a[212:215], v[20:23]\n"
    "ds_read_b128 v[176:179], v249 offset:14336\n"
    "v_cvt_f32_i32 v216, v216\n"
    "v_mfma_f32_16x16x32_bf16 v[24:27], v[200:203], a[236:239], v[104:107]\n"
    "v_exp_f32 v59, v59\n"
    "v_mfma_f32_16x16x32_bf16 v[24:27], v[196:199], a[232:235], v[24:27]\n"
    "v_cvt_pk_bf16_f32 v83, v58, v59\n"
    "v_cvt_f32_i32 v217, v217\n"
    "v_mfma_f32_16x16x32_bf16 v[24:27], v[192:195], a[228:231], v[24:27]\n"
    "v_exp_f32 v60, v60\n"
    "v_mfma_f32_16x16x32_bf16 v[24:27], v[188:191], a[224:227], v[24:27]\n"
    "v_exp_f32 v61, v61\n"
    "v_mfma_f32_16x16x32_bf16 v[24:27], v[184:187], a[220:223], v[24:27]\n"
    "v_exp_f32 v62, v62\n"
    "v_mfma_f32_16x16x32_bf16 v[24:27], v[180:183], a[216:219], v[24:27]\n"
    "v_cvt_pk_bf16_f32 v90, v60, v61\n"
    "v_cvt_f32_i32 v218, v218\n"
    "v_mfma_f32_16x16x32_bf16 v[28:31], v[180:183], a[240:243], v[104:107]\n"
    "v_cvt_f32_i32 v219, v219\n"
    "v_cvt_f32_i32 v220, v220\n"
    "v_mfma_f32_16x16x32_bf16 v[28:31], v[184:187], a[244:247], v[28:31]\n"
    "v_exp_f32 v63, v63\n"
    "v_mfma_f32_16x16x32_bf16 v[28:31], v[188:191], a[248:251], v[28:31]\n"
    "v_cvt_pk_bf16_f32 v91, v62, v63\n"
    "v_cvt_f32_i32 v221, v221\n"
    "v_mfma_f32_16x16x32_bf16 v[28:31], v[192:195], a[252:255], v[28:31]\n"
    "v_cvt_f32_i32 v222, v222\n"
    "v_cvt_f32_i32 v223, v223\n"
    "v_mfma_f32_16x16x32_bf16 v[28:31], v[196:199], v[124:127], v[28:31]\n"
    "v_exp_f32 v64, v64\n"
    "v_mfma_f32_16x16x32_bf16 v[28:31], v[200:203], v[128:131], v[28:31]\n"
    "v_cvt_f32_i32 v224, v224\n"
    "v_cvt_f32_i32 v225, v225\n"
    "v_mfma_f32_16x16x32_bf16 v[32:35], v[200:203], v[152:155], v[104:107]\n"
    "v_cvt_f32_i32 v226, v226\n"
    "v_cvt_f32_i32 v227, v227\n"
    "v_mfma_f32_16x16x32_bf16 v[32:35], v[196:199], v[148:151], v[32:35]\n"
    "v_exp_f32 v65, v65\n"
    "v_mfma_f32_16x16x32_bf16 v[32:35], v[192:195], v[144:147], v[32:35]\n"
    "s_waitcnt lgkmcnt(2)\n"
    "v_cvt_pk_bf16_f32 v98, v64, v65\n"
    "v_fma_f32 v204, v204, v36, -v38\n"
    "v_mfma_f32_16x16x32_bf16 v[32:35], v[188:191], v[140:143], v[32:35]\n"
    "v_fma_f32 v205, v205, v37, -v39\n"
    "v_fma_f32 v206, v206, v36, -v38\n"
    "v_mfma_f32_16x16x32_bf16 v[32:35], v[184:187], v[136:139], v[32:35]\n"
    "v_exp_f32 v66, v66\n"
    "v_mfma_f32_16x16x32_bf16 v[32:35], v[180:183], v[132:135], v[32:35]\n"
    "s_waitcnt lgkmcnt(0)\n"
    "v_fma_f32 v207, v207, v37, -v39\n"
    "v_cvt_pk_bf16_f32 v204, v204, v205\n"
    "v_mfma_f32_16x16x32_bf16 a[0:3], v[156:159], v[68:71], a[0:3]\n"
    "ds_read_b128 v[180:183], v249 offset:3072\n"
    "v_cvt_pk_bf16_f32 v206, v206, v207\n"
    "v_mfma_f32_16x16x32_bf16 a[0:3], v[160:163], v[72:75], a[0:3]\n"
    "ds_read_b128 v[184:187], v249 offset:15360\n"
    "v_cvt_f32_i32 v228, v228\n"
    "v_mfma_f32_16x16x32_bf16 a[16:19], v[168:171], v[72:75], a[16:19]\n"
    "ds_read_b128 v[188:191], v249 offset:4096\n"
    "v_cvt_f32_i32 v229, v229\n"
    "v_mfma_f32_16x16x32_bf16 a[16:19], v[164:167], v[68:71], a[16:19]\n"
    "ds_read_b128 v[192:195], v249 offset:16384\n"
    "v_cvt_f32_i32 v230, v230\n"
    "v_mfma_f32_16x16x32_bf16 a[32:35], v[172:175], v[68:71], a[32:35]\n"
    "ds_read_b128 v[196:199], v249 offset:5120\n"
    "v_cvt_f32_i32 v231, v231\n"
    "v_mfma_f32_16x16x32_bf16 a[32:35], v[176:179], v[72:75], a[32:35]\n"
    "ds_read_b128 v[200:203], v249 offset:17408\n"
    "v_fma_f32 v208, v208, v36, -v38\n"
    "v_mfma_f32_16x16x32_bf16 a[36:39], v[176:179], v[80:83], a[36:39]\n"
    "ds_read_b128 v[104:107], v251 offset:768\n"
    "v_fma_f32 v209, v209, v37, -v39\n"
    "v_mfma_f32_16x16x32_bf16 a[36:39], v[172:175], v[76:79], a[36:39]\n"
    "v_exp_f32 v67, v67\n"
    "v_mfma_f32_16x16x32_bf16 a[20:23], v[164:167], v[76:79], a[20:23]\n"
    "v_cvt_pk_bf16_f32 v99, v66, v67\n"
    "v_fma_f32 v210, v210, v36, -v38\n"
    "v_mfma_f32_16x16x32_bf16 a[20:23], v[168:171], v[80:83], a[20:23]\n"
    "buffer_store_dword v204, v244, s[16:19], s76 offen offset:0 sc1\n"
    "v_mfma_f32_16x16x32_bf16 a[4:7], v[160:163], v[80:83], a[4:7]\n"
    "v_fma_f32 v211, v211, v37, -v39\n"
    "v_cvt_pk_bf16_f32 v208, v208, v209\n"
    "v_mfma_f32_16x16x32_bf16 a[4:7], v[156:159], v[76:79], a[4:7]\n"
    "buffer_store_dword v206, v244, s[16:19], s76 offen offset:128 sc1\n"
    "v_mfma_f32_16x16x32_bf16 a[8:11], v[156:159], v[84:87], a[8:11]\n"
    "v_cvt_pk_bf16_f32 v210, v210, v211\n"
    "v_cvt_f32_i32 v232, v232\n"
    "v_mfma_f32_16x16x32_bf16 a[8:11], v[160:163], v[88:91], a[8:11]\n"
    "buffer_store_dword v208, v244, s[16:19], s76 offen offset:1024 sc1\n"
    "v_mfma_f32_16x16x32_bf16 a[24:27], v[168:171], v[88:91], a[24:27]\n"
    "v_exp_f32 v4, v4\n"
    "v_mfma_f32_16x16x32_bf16 a[24:27], v[164:167], v[84:87], a[24:27]\n"
    "buffer_store_dword v210, v244, s[16:19], s76 offen offset:1152 sc1\n"
    "v_mfma_f32_16x16x32_bf16 a[40:43], v[172:175], v[84:87], a[40:43]\n"
    "v_cvt_f32_i32 v233, v233\n"
    "v_cvt_f32_i32 v234, v234\n"
    "v_mfma_f32_16x16x32_bf16 a[40:43], v[176:179], v[88:91], a[40:43]\n"
    "v_exp_f32 v5, v5\n"
    "v_mfma_f32_16x16x32_bf16 a[44:47], v[176:179], v[96:99], a[44:47]\n"
    "v_cvt_f32_i32 v235, v235\n"
    "v_fma_f32 v212, v212, v36, -v38\n"
    "v_mfma_f32_16x16x32_bf16 a[44:47], v[172:175], v[92:95], a[44:47]\n"
    "v_exp_f32 v6, v6\n"
    "v_mfma_f32_16x16x32_bf16 a[28:31], v[164:167], v[92:95], a[28:31]\n"
    "v_fma_f32 v213, v213, v37, -v39\n"
    "v_fma_f32 v214, v214, v36, -v38\n"
    "v_mfma_f32_16x16x32_bf16 a[28:31], v[168:171], v[96:99], a[28:31]\n"
    "v_exp_f32 v7, v7\n"
    "v_mfma_f32_16x16x32_bf16 a[12:15], v[160:163], v[96:99], a[12:15]\n"
    "v_exp_f32 v8, v8\n"
    "v_mfma_f32_16x16x32_bf16 a[12:15], v[156:159], v[92:95], a[12:15]\n"
    "s_waitcnt lgkmcnt(0)\n"
    "v_fma_f32 v215, v215, v37, -v39\n"
    "v_cvt_pk_bf16_f32 v212, v212, v213\n"
    "v_mfma_f32_16x16x32_bf16 a[48:51], v[180:183], v[68:71], a[48:51]\n"
    "buffer_store_dword v212, v244, s[16:19], s76 offen offset:256 sc1\n"
    "v_mfma_f32_16x16x32_bf16 a[48:51], v[184:187], v[72:75], a[48:51]\n"
    "ds_read_b128 v[156:159], v249 offset:6144\n"
    "ds_read_b128 v[160:163], v249 offset:18432\n"
    "v_mfma_f32_16x16x32_bf16 a[64:67], v[192:195], v[72:75], a[64:67]\n"
    "ds_read_b128 v[164:167], v249 offset:7168\n"
    "v_cvt_pk_bf16_f32 v214, v214, v215\n"
    "v_mfma_f32_16x16x32_bf16 a[64:67], v[188:191], v[68:71], a[64:67]\n"
    "buffer_store_dword v214, v244, s[16:19], s76 offen offset:384 sc1\n"
    "v_mfma_f32_16x16x32_bf16 a[80:83], v[196:199], v[68:71], a[80:83]\n"
    "ds_read_b128 v[168:171], v249 offset:19456\n"
    "ds_read_b128 v[172:175], v249 offset:8192\n"
    "v_mfma_f32_16x16x32_bf16 a[80:83], v[200:203], v[72:75], a[80:83]\n"
    "ds_read_b128 v[176:179], v249 offset:20480\n"
    "v_fma_f32 v216, v216, v36, -v38\n"
    "v_smfmac_f32_16x16x64_bf16 v[108:111], v[104:107], v[68:75], v253\n"
    "s_add_u32 s81, s81, 128\n"
    "s_cmp_gt_u32 s81, 4096\n"
    "s_cselect_b32 s81, 0, s81\n"
    "v_smfmac_f32_16x16x64_bf16 v[112:115], v[104:107], v[76:83], v253\n"
    "v_mfma_f32_16x16x32_bf16 a[84:87], v[200:203], v[80:83], a[84:87]\n"
    "v_exp_f32 v9, v9\n"
    "v_mfma_f32_16x16x32_bf16 a[84:87], v[196:199], v[76:79], a[84:87]\n"
    "v_exp_f32 v10, v10\n"
    "v_mfma_f32_16x16x32_bf16 a[68:71], v[188:191], v[76:79], a[68:71]\n"
    "v_fma_f32 v217, v217, v37, -v39\n"
    "v_fma_f32 v218, v218, v36, -v38\n"
    "v_mfma_f32_16x16x32_bf16 a[68:71], v[192:195], v[80:83], a[68:71]\n"
    "v_exp_f32 v11, v11\n"
    "v_mfma_f32_16x16x32_bf16 a[52:55], v[184:187], v[80:83], a[52:55]\n"
    "v_fma_f32 v219, v219, v37, -v39\n"
    "v_cvt_pk_bf16_f32 v216, v216, v217\n"
    "v_mfma_f32_16x16x32_bf16 a[52:55], v[180:183], v[76:79], a[52:55]\n"
    "v_exp_f32 v12, v12\n"
    "v_mfma_f32_16x16x32_bf16 a[56:59], v[180:183], v[84:87], a[56:59]\n"
    "v_cvt_pk_bf16_f32 v218, v218, v219\n"
    "v_fma_f32 v220, v220, v36, -v38\n"
    "v_mfma_f32_16x16x32_bf16 a[56:59], v[184:187], v[88:91], a[56:59]\n"
    "v_exp_f32 v13, v13\n"
    "v_mfma_f32_16x16x32_bf16 a[72:75], v[192:195], v[88:91], a[72:75]\n"
    "v_exp_f32 v14, v14\n"
    "v_mfma_f32_16x16x32_bf16 a[72:75], v[188:191], v[84:87], a[72:75]\n"
    "v_fma_f32 v221, v221, v37, -v39\n"
    "v_fma_f32 v222, v222, v36, -v38\n"
    "v_mfma_f32_16x16x32_bf16 a[88:91], v[196:199], v[84:87], a[88:91]\n"
    "v_exp_f32 v15, v15\n"
    "v_mfma_f32_16x16x32_bf16 a[88:91], v[200:203], v[88:91], a[88:91]\n"
    "v_fma_f32 v223, v223, v37, -v39\n"
    "v_cvt_pk_bf16_f32 v220, v220, v221\n"
    "v_smfmac_f32_16x16x64_bf16 v[116:119], v[104:107], v[84:91], v253\n"
    "v_smfmac_f32_16x16x64_bf16 v[120:123], v[104:107], v[92:99], v253\n"
    "v_mfma_f32_16x16x32_bf16 a[92:95], v[200:203], v[96:99], a[92:95]\n"
    "v_exp_f32 v16, v16\n"
    "v_mfma_f32_16x16x32_bf16 a[92:95], v[196:199], v[92:95], a[92:95]\n"
    "v_cvt_pk_bf16_f32 v222, v222, v223\n"
    "v_fma_f32 v224, v224, v36, -v38\n"
    "v_mfma_f32_16x16x32_bf16 a[76:79], v[188:191], v[92:95], a[76:79]\n"
    "v_exp_f32 v17, v17\n"
    "v_mfma_f32_16x16x32_bf16 a[76:79], v[192:195], v[96:99], a[76:79]\n"
    "v_fma_f32 v225, v225, v37, -v39\n"
    "v_fma_f32 v226, v226, v36, -v38\n"
    "v_mfma_f32_16x16x32_bf16 a[60:63], v[184:187], v[96:99], a[60:63]\n"
    "v_exp_f32 v18, v18\n"
    "v_mfma_f32_16x16x32_bf16 a[60:63], v[180:183], v[92:95], a[60:63]\n"
    "s_waitcnt lgkmcnt(0)\n"
    "v_exp_f32 v19, v19\n"
    "v_mfma_f32_16x16x32_bf16 a[96:99], v[156:159], v[68:71], a[96:99]\n"
    "ds_read_b128 v[180:183], v249 offset:9216\n"
    "v_fma_f32 v227, v227, v37, -v39\n"
    "v_mfma_f32_16x16x32_bf16 a[96:99], v[160:163], v[72:75], a[96:99]\n"
    "ds_read_b128 v[184:187], v249 offset:21504\n"
    "v_cvt_pk_bf16_f32 v224, v224, v225\n"
    "v_mfma_f32_16x16x32_bf16 a[112:115], v[168:171], v[72:75], a[112:115]\n"
    "ds_read_b128 v[188:191], v249 offset:10240\n"
    "v_cvt_pk_bf16_f32 v226, v226, v227\n"
    "v_mfma_f32_16x16x32_bf16 a[112:115], v[164:167], v[68:71], a[112:115]\n"
    "ds_read_b128 v[192:195], v249 offset:22528\n"
    "v_fma_f32 v228, v228, v36, -v38\n"
    "v_mfma_f32_16x16x32_bf16 a[128:131], v[172:175], v[68:71], a[128:131]\n"
    "buffer_load_dwordx2 v[236:237], v241, s[36:39], s81 offen offset:0 sc1\n"
    "v_mfma_f32_16x16x32_bf16 a[128:131], v[176:179], v[72:75], a[128:131]\n"
    "ds_read_b128 v[196:199], v249 offset:11264\n"
    "ds_read_b128 v[200:203], v249 offset:23552\n"
    "v_mfma_f32_16x16x32_bf16 a[132:135], v[176:179], v[80:83], a[132:135]\n"
    "buffer_store_dword v216, v244, s[16:19], s76 offen offset:1280 sc1\n"
    "v_mfma_f32_16x16x32_bf16 a[132:135], v[172:175], v[76:79], a[132:135]\n"
    "v_exp_f32 v20, v20\n"
    "v_mfma_f32_16x16x32_bf16 a[116:119], v[164:167], v[76:79], a[116:119]\n"
    "buffer_store_dword v218, v244, s[16:19], s76 offen offset:1408 sc1\n"
    "v_mfma_f32_16x16x32_bf16 a[116:119], v[168:171], v[80:83], a[116:119]\n"
    "v_exp_f32 v21, v21\n"
    "v_mfma_f32_16x16x32_bf16 a[100:103], v[160:163], v[80:83], a[100:103]\n"
    "buffer_store_dword v220, v244, s[16:19], s77 offen offset:0 sc1\n"
    "v_mfma_f32_16x16x32_bf16 a[100:103], v[156:159], v[76:79], a[100:103]\n"
    "v_fma_f32 v229, v229, v37, -v39\n"
    "v_fma_f32 v230, v230, v36, -v38\n"
    "v_mfma_f32_16x16x32_bf16 a[104:107], v[156:159], v[84:87], a[104:107]\n"
    "buffer_store_dword v222, v244, s[16:19], s77 offen offset:128 sc1\n"
    "v_mfma_f32_16x16x32_bf16 a[104:107], v[160:163], v[88:91], a[104:107]\n"
    "v_exp_f32 v22, v22\n"
    "v_mfma_f32_16x16x32_bf16 a[120:123], v[168:171], v[88:91], a[120:123]\n"
    "buffer_store_dword v224, v244, s[16:19], s77 offen offset:1024 sc1\n"
    "v_mfma_f32_16x16x32_bf16 a[120:123], v[164:167], v[84:87], a[120:123]\n"
    "v_fma_f32 v231, v231, v37, -v39\n"
    "v_cvt_pk_bf16_f32 v228, v228, v229\n"
    "v_mfma_f32_16x16x32_bf16 a[136:139], v[172:175], v[84:87], a[136:139]\n"
    "buffer_store_dword v226, v244, s[16:19], s77 offen offset:1152 sc1\n"
    "v_mfma_f32_16x16x32_bf16 a[136:139], v[176:179], v[88:91], a[136:139]\n"
    "v_exp_f32 v23, v23\n"
    "v_mfma_f32_16x16x32_bf16 a[140:143], v[176:179], v[96:99], a[140:143]\n"
    "buffer_store_dword v228, v244, s[16:19], s77 offen offset:256 sc1\n"
    "v_mfma_f32_16x16x32_bf16 a[140:143], v[172:175], v[92:95], a[140:143]\n"
    "v_exp_f32 v24, v24\n"
    "v_mfma_f32_16x16x32_bf16 a[124:127], v[164:167], v[92:95], a[124:127]\n"
    "v_cvt_pk_bf16_f32 v230, v230, v231\n"
    "v_fma_f32 v232, v232, v36, -v38\n"
    "v_mfma_f32_16x16x32_bf16 a[124:127], v[168:171], v[96:99], a[124:127]\n"
    "buffer_store_dword v230, v244, s[16:19], s77 offen offset:384 sc1\n"
    "v_mfma_f32_16x16x32_bf16 a[108:111], v[160:163], v[96:99], a[108:111]\n"
    "v_exp_f32 v25, v25\n"
    "v_mfma_f32_16x16x32_bf16 a[108:111], v[156:159], v[92:95], a[108:111]\n"
    "s_waitcnt lgkmcnt(0)\n"
    "v_fma_f32 v233, v233, v37, -v39\n"
    "v_fma_f32 v234, v234, v36, -v38\n"
    "v_mfma_f32_16x16x32_bf16 a[144:147], v[180:183], v[68:71], a[144:147]\n"
    "ds_read_b128 v[156:159], v3 offset:12288\n"
    "v_fma_f32 v235, v235, v37, -v39\n"
    "v_mfma_f32_16x16x32_bf16 a[144:147], v[184:187], v[72:75], a[144:147]\n"
    "ds_read_b128 v[160:163], v3 offset:14336\n"
    "v_cvt_pk_bf16_f32 v232, v232, v233\n"
    "v_mfma_f32_16x16x32_bf16 a[160:163], v[192:195], v[72:75], a[160:163]\n"
    "ds_read_b128 v[164:167], v3 offset:16384\n"
    "v_cvt_pk_bf16_f32 v234, v234, v235\n"
    "v_mfma_f32_16x16x32_bf16 a[160:163], v[188:191], v[68:71], a[160:163]\n"
    "ds_read_b128 v[168:171], v3 offset:18432\n"
    "v_mfma_f32_16x16x32_bf16 a[176:179], v[196:199], v[68:71], a[176:179]\n"
    "ds_read_b128 v[172:175], v3 offset:20480\n"
    "v_mfma_f32_16x16x32_bf16 a[176:179], v[200:203], v[72:75], a[176:179]\n"
    "ds_read_b128 v[176:179], v3 offset:22528\n"
    "v_mfma_f32_16x16x32_bf16 a[180:183], v[200:203], v[80:83], a[180:183]\n"
    "ds_read_b128 v[100:103], v250 offset:1664\n"
    "v_mfma_f32_16x16x32_bf16 a[180:183], v[196:199], v[76:79], a[180:183]\n"
    "buffer_store_dword v232, v244, s[16:19], s77 offen offset:1280 sc1\n"
    "v_mfma_f32_16x16x32_bf16 a[164:167], v[188:191], v[76:79], a[164:167]\n"
    "v_exp_f32 v26, v26\n"
    "v_mfma_f32_16x16x32_bf16 a[164:167], v[192:195], v[80:83], a[164:167]\n"
    "buffer_store_dword v234, v244, s[16:19], s77 offen offset:1408 sc1\n"
    "v_mfma_f32_16x16x32_bf16 a[148:151], v[184:187], v[80:83], a[148:151]\n"
    "v_exp_f32 v27, v27\n"
    "v_mfma_f32_16x16x32_bf16 a[148:151], v[180:183], v[76:79], a[148:151]\n"
    "v_exp_f32 v28, v28\n"
    "v_mfma_f32_16x16x32_bf16 a[152:155], v[180:183], v[84:87], a[152:155]\n"
    "v_exp_f32 v29, v29\n"
    "v_mfma_f32_16x16x32_bf16 a[152:155], v[184:187], v[88:91], a[152:155]\n"
    "v_exp_f32 v30, v30\n"
    "v_mfma_f32_16x16x32_bf16 a[168:171], v[192:195], v[88:91], a[168:171]\n"
    "s_waitcnt vmcnt(17)\n"
    "ds_add_u32 v247, v246\n"
    "v_mfma_f32_16x16x32_bf16 a[168:171], v[188:191], v[84:87], a[168:171]\n"
    "v_exp_f32 v31, v31\n"
    "v_mfma_f32_16x16x32_bf16 a[184:187], v[196:199], v[84:87], a[184:187]\n"
    "v_exp_f32 v32, v32\n"
    "v_mfma_f32_16x16x32_bf16 a[184:187], v[200:203], v[88:91], a[184:187]\n"
    "v_exp_f32 v33, v33\n"
    "v_mfma_f32_16x16x32_bf16 a[188:191], v[200:203], v[96:99], a[188:191]\n"
    "v_exp_f32 v34, v34\n"
    "v_mfma_f32_16x16x32_bf16 a[188:191], v[196:199], v[92:95], a[188:191]\n"
    "v_exp_f32 v35, v35\n"
    "v_mfma_f32_16x16x32_bf16 a[172:175], v[188:191], v[92:95], a[172:175]\n"
    "s_sub_u32 s74, s68, s65\n"
    "v_add_u32 v250, 1536, v250\n"
    "v_add_u32 v251, 1536, v251\n"
    "v_mfma_f32_16x16x32_bf16 a[172:175], v[192:195], v[96:99], a[172:175]\n"
    "s_add_u32 s66, s66, 1\n"
    "v_mov_b32 v2, v3\n"
    "v_add_u32 v3, s74, v252\n"
    "v_mfma_f32_16x16x32_bf16 a[156:159], v[184:187], v[96:99], a[156:159]\n"
    "s_waitcnt lgkmcnt(1)\n"
    "s_cmp_lt_u32 s66, 31\n"
    "v_mfma_f32_16x16x32_bf16 a[156:159], v[180:183], v[92:95], a[156:159]\n"
    "s_cbranch_scc1 .LPK\n"
    "s_branch .Ltail\n"
    ".LPV:\n"
    "v_mfma_f32_16x16x32_bf16 v[36:39], v[156:159], a[192:195], v[100:103]\n"
    "s_add_u32 s71, s71, 98304\n"
    "ds_read_b128 v[180:183], v2 offset:13312\n"
    "v_cvt_pk_bf16_f32 v68, v4, v5\n"
    "v_mfma_f32_16x16x32_bf16 v[36:39], v[160:163], a[196:199], v[36:39]\n"
    "s_add_u32 s75, s71, s63\n"
    "ds_read_b128 v[184:187], v2 offset:15360\n"
    "v_mfma_f32_16x16x32_bf16 v[36:39], v[164:167], a[200:203], v[36:39]\n"
    "ds_read_b128 v[188:191], v2 offset:17408\n"
    "v_mfma_f32_16x16x32_bf16 v[36:39], v[168:171], a[204:207], v[36:39]\n"
    "ds_read_b128 v[192:195], v2 offset:19456\n"
    "v_cvt_pk_bf16_f32 v69, v6, v7\n"
    "v_mfma_f32_16x16x32_bf16 v[36:39], v[172:175], a[208:211], v[36:39]\n"
    "ds_read_b128 v[196:199], v2 offset:21504\n"
    "v_mfma_f32_16x16x32_bf16 v[36:39], v[176:179], a[212:215], v[36:39]\n"
    "ds_read_b128 v[200:203], v2 offset:23552\n"
    "v_mfma_f32_16x16x32_bf16 v[40:43], v[176:179], a[236:239], v[100:103]\n"
    "ds_read_b128 v[104:107], v250 offset:144\n"
    "v_mfma_f32_16x16x32_bf16 v[40:43], v[172:175], a[232:235], v[40:43]\n"
    "s_add_u32 s68, s68, 24576\n"
    "v_cvt_pk_bf16_f32 v76, v8, v9\n"
    "v_mfma_f32_16x16x32_bf16 v[40:43], v[168:171], a[228:231], v[40:43]\n"
    "s_cmp_ge_u32 s68, s69\n"
    "s_cselect_b32 s68, s65, s68\n"
    "v_mfma_f32_16x16x32_bf16 v[40:43], v[164:167], a[224:227], v[40:43]\n"
    "v_exp_f32 v36, v36\n"
    "v_mfma_f32_16x16x32_bf16 v[40:43], v[160:163], a[220:223], v[40:43]\n"
    "v_exp_f32 v37, v37\n"
    "v_mfma_f32_16x16x32_bf16 v[40:43], v[156:159], a[216:219], v[40:43]\n"
    "v_cvt_pk_bf16_f32 v77, v10, v11\n"
    "v_cvt_pk_bf16_f32 v72, v36, v37\n"
    "v_mfma_f32_16x16x32_bf16 v[44:47], v[156:159], a[240:243], v[100:103]\n"
    "v_mfma_f32_16x16x32_bf16 v[44:47], v[160:163], a[244:247], v[44:47]\n"
    "v_exp_f32 v38, v38\n"
    "v_mfma_f32_16x16x32_bf16 v[44:47], v[164:167], a[248:251], v[44:47]\n"
    "v_mfma_f32_16x16x32_bf16 v[44:47], v[168:171], a[252:255], v[44:47]\n"
    "v_cvt_pk_bf16_f32 v84, v12, v13\n"
    "v_mfma_f32_16x16x32_bf16 v[44:47], v[172:175], v[124:127], v[44:47]\n"
    "v_exp_f32 v39, v39\n"
    "v_mfma_f32_16x16x32_bf16 v[44:47], v[176:179], v[128:131], v[44:47]\n"
    "v_cvt_pk_bf16_f32 v73, v38, v39\n"
    "v_mfma_f32_16x16x32_bf16 v[48:51], v[176:179], v[152:155], v[100:103]\n"
    "ds_read_b32 v245, v247\n"
    "v_cvt_pk_bf16_f32 v85, v14, v15\n"
    "v_mfma_f32_16x16x32_bf16 v[48:51], v[172:175], v[148:151], v[48:51]\n"
    "s_add_u32 m0, s68, 0\n"
    "v_exp_f32 v40, v40\n"
    "v_mfma_f32_16x16x32_bf16 v[48:51], v[168:171], v[144:147], v[48:51]\n"
    "v_mfma_f32_16x16x32_bf16 v[48:51], v[164:167], v[140:143], v[48:51]\n"
    "v_mfma_f32_16x16x32_bf16 v[48:51], v[160:163], v[136:139], v[48:51]\n"
    "s_waitcnt vmcnt(13)\n"
    "v_cmp_eq_u64 s[54:55], v[236:237], v[238:239]\n"
    "v_cvt_pk_bf16_f32 v92, v16, v17\n"
    "v_mfma_f32_16x16x32_bf16 v[48:51], v[156:159], v[132:135], v[48:51]\n"
    "s_waitcnt lgkmcnt(1)\n"
    "v_exp_f32 v41, v41\n"
    "v_mfma_f32_16x16x32_bf16 v[52:55], v[180:183], a[192:195], v[104:107]\n"
    "s_waitcnt lgkmcnt(0)\n"
    "v_cmp_le_u32 s[56:57], s59, v245\n"
    "v_cvt_pk_bf16_f32 v80, v40, v41\n"
    "v_mfma_f32_16x16x32_bf16 v[52:55], v[184:187], a[196:199], v[52:55]\n"
    "v_exp_f32 v42, v42\n"
    "v_mfma_f32_16x16x32_bf16 v[52:55], v[188:191], a[200:203], v[52:55]\n"
    "s_and_b64 s[54:55], s[54:55], s[56:57]\n"
    "s_cmp_eq_u64 s[54:55], -1\n"
    "v_mfma_f32_16x16x32_bf16 v[52:55], v[192:195], a[204:207], v[52:55]\n"
    "s_cbranch_scc0 .Lpoll3\n"
    ".Lpoll3r:\n"
    "v_mfma_f32_16x16x32_bf16 v[52:55], v[196:199], a[208:211], v[52:55]\n"
    "buffer_load_dwordx4 v1, s[16:19], s67 offen offset:0 sc1 lds\n"
    "v_mfma_f32_16x16x32_bf16 v[52:55], v[200:203], a[212:215], v[52:55]\n"
    "ds_read_b128 v[156:159], v3 offset:0\n"
    "ds_read_b128 v[160:163], v3 offset:2048\n"
    "v_mfma_f32_16x16x32_bf16 v[56:59], v[200:203], a[236:239], v[104:107]\n"
    "ds_read_b128 v[164:167], v3 offset:4096\n"
    "ds_read_b128 v[168:171], v3 offset:6144\n"
    "v_mfma_f32_16x16x32_bf16 v[56:59], v[196:199], a[232:235], v[56:59]\n"
    "ds_read_b128 v[172:175], v3 offset:8192\n"
    "ds_read_b128 v[176:179], v3 offset:10240\n"
    "v_mfma_f32_16x16x32_bf16 v[56:59], v[192:195], a[228:231], v[56:59]\n"
    "buffer_load_dwordx4 v1, s[16:19], s67 offen offset:1024 sc1 lds\n"
    "v_mfma_f32_16x16x32_bf16 v[56:59], v[188:191], a[224:227], v[56:59]\n"
    "ds_read_b128 v[100:103], v250 offset:768\n"
    "v_cvt_pk_bf16_f32 v93, v18, v19\n"
    "v_mfma_f32_16x16x32_bf16 v[56:59], v[184:187], a[220:223], v[56:59]\n"
    "v_cvt_pk_bf16_f32 v70, v20, v21\n"
    "v_cvt_pk_bf16_f32 v71, v22, v23\n"
    "v_mfma_f32_16x16x32_bf16 v[56:59], v[180:183], a[216:219], v[56:59]\n"
    "v_exp_f32 v43, v43\n"
    "v_mfma_f32_16x16x32_bf16 v[60:63], v[180:183], a[240:243], v[104:107]\n"
    "buffer_load_dwordx4 v1, s[16:19], s67 offen offset:2048 sc1 lds\n"
    "v_mfma_f32_16x16x32_bf16 v[60:63], v[184:187], a[244:247], v[60:63]\n"
    "v_cvt_pk_bf16_f32 v78, v24, v25\n"
    "v_cvt_pk_bf16_f32 v81, v42, v43\n"
    "v_mfma_f32_16x16x32_bf16 v[60:63], v[188:191], a[248:251], v[60:63]\n"
    "v_exp_f32 v44, v44\n"
    "v_mfma_f32_16x16x32_bf16 v[60:63], v[192:195], a[252:255], v[60:63]\n"
    "v_exp_f32 v45, v45\n"
    "v_mfma_f32_16x16x32_bf16 v[60:63], v[196:199], v[124:127], v[60:63]\n"
    "buffer_load_dwordx4 v1, s[16:19], s67 offen offset:3072 sc1 lds\n"
    "v_mfma_f32_16x16x32_bf16 v[60:63], v[200:203], v[128:131], v[60:63]\n"
    "s_add_u32 m0, s68, 4096\n"
    "v_cvt_pk_bf16_f32 v79, v26, v27\n"
    "v_cvt_pk_bf16_f32 v88, v44, v45\n"
    "v_mfma_f32_16x16x32_bf16 v[64:67], v[200:203], v[152:155], v[104:107]\n"
    "s_add_u32 s83, s67, 4096\n"
    "v_exp_f32 v46, v46\n"
    "v_mfma_f32_16x16x32_bf16 v[64:67], v[196:199], v[148:151], v[64:67]\n"
    "v_exp_f32 v47, v47\n"
    "v_mfma_f32_16x16x32_bf16 v[64:67], v[192:195], v[144:147], v[64:67]\n"
    "buffer_load_dwordx4 v1, s[16:19], s83 offen offset:0 sc1 lds\n"
    "v_mfma_f32_16x16x32_bf16 v[64:67], v[188:191], v[140:143], v[64:67]\n"
    "v_cvt_pk_bf16_f32 v86, v28, v29\n"
    "v_cvt_pk_bf16_f32 v89, v46, v47\n"
    "v_mfma_f32_16x16x32_bf16 v[64:67], v[184:187], v[136:139], v[64:67]\n"
    "v_exp_f32 v48, v48\n"
    "v_mfma_f32_16x16x32_bf16 v[64:67], v[180:183], v[132:135], v[64:67]\n"
    "s_waitcnt lgkmcnt(0)\n"
    "v_exp_f32 v49, v49\n"
    "v_mfma_f32_16x16x32_bf16 v[4:7], v[156:159], a[192:195], v[100:103]\n"
    "buffer_load_dwordx4 v1, s[16:19], s83 offen offset:1024 sc1 lds\n"
    "v_mfma_f32_16x16x32_bf16 v[4:7], v[160:163], a[196:199], v[4:7]\n"
    "s_add_u32 m0, s64, 0\n"
    "ds_read_b128 v[180:183], v3 offset:1024\n"
    "ds_read_b128 v[184:187], v3 offset:3072\n"
    "v_mfma_f32_16x16x32_bf16 v[4:7], v[164:167], a[200:203], v[4:7]\n"
    "s_add_u32 s83, s67, 24576\n"
    "ds_read_b128 v[188:191], v3 offset:5120\n"
    "v_cvt_pk_bf16_f32 v87, v30, v31\n"
    "v_mfma_f32_16x16x32_bf16 v[4:7], v[168:171], a[204:207], v[4:7]\n"
    "ds_read_b128 v[192:195], v3 offset:7168\n"
    "v_cvt_pk_bf16_f32 v96, v48, v49\n"
    "v_mfma_f32_16x16x32_bf16 v[4:7], v[172:175], a[208:211], v[4:7]\n"
    "buffer_load_dwordx4 v1, s[16:19], s83 offen offset:0 sc1 lds\n"
    "v_mfma_f32_16x16x32_bf16 v[4:7], v[176:179], a[212:215], v[4:7]\n"
    "ds_read_b128 v[196:199], v3 offset:9216\n"
    "ds_read_b128 v[200:203], v3 offset:11264\n"
    "v_mfma_f32_16x16x32_bf16 v[8:11], v[176:179], a[236:239], v[100:103]\n"
    "ds_read_b128 v[104:107], v250 offset:784\n"
    "v_cvt_pk_bf16_f32 v94, v32, v33\n"
    "v_mfma_f32_16x16x32_bf16 v[8:11], v[172:175], a[232:235], v[8:11]\n"
    "v_exp_f32 v52, v52\n"
    "v_mfma_f32_16x16x32_bf16 v[8:11], v[168:171], a[228:231], v[8:11]\n"
    "buffer_load_dwordx4 v1, s[16:19], s83 offen offset:1024 sc1 lds\n"
    "v_mfma_f32_16x16x32_bf16 v[8:11], v[164:167], a[224:227], v[8:11]\n"
    "v_cvt_pk_bf16_f32 v95, v34, v35\n"
    "v_mfma_f32_16x16x32_bf16 v[8:11], v[160:163], a[220:223], v[8:11]\n"
    "v_exp_f32 v53, v53\n"
    "v_mfma_f32_16x16x32_bf16 v[8:11], v[156:159], a[216:219], v[8:11]\n"
    "v_exp_f32 v54, v54\n"
    "v_mfma_f32_16x16x32_bf16 v[12:15], v[156:159], a[240:243], v[100:103]\n"
    "buffer_load_dwordx4 v1, s[16:19], s83 offen offset:2048 sc1 lds\n"
    "v_mfma_f32_16x16x32_bf16 v[12:15], v[160:163], a[244:247], v[12:15]\n"
    "v_cvt_pk_bf16_f32 v74, v52, v53\n"
    "v_mfma_f32_16x16x32_bf16 v[12:15], v[164:167], a[248:251], v[12:15]\n"
    "v_exp_f32 v55, v55\n"
    "v_mfma_f32_16x16x32_bf16 v[12:15], v[168:171], a[252:255], v[12:15]\n"
    "v_cvt_pk_bf16_f32 v75, v54, v55\n"
    "v_mfma_f32_16x16x32_bf16 v[12:15], v[172:175], v[124:127], v[12:15]\n"
    "buffer_load_dwordx4 v1, s[16:19], s83 offen offset:3072 sc1 lds\n"
    "v_mfma_f32_16x16x32_bf16 v[12:15], v[176:179], v[128:131], v[12:15]\n"
    "s_add_u32 m0, s64, 4096\n"
    "v_exp_f32 v56, v56\n"
    "v_mfma_f32_16x16x32_bf16 v[16:19], v[176:179], v[152:155], v[100:103]\n"
    "s_add_u32 s83, s67, 28672\n"
    "v_exp_f32 v50, v50\n"
    "v_mfma_f32_16x16x32_bf16 v[16:19], v[172:175], v[148:151], v[16:19]\n"
    "v_exp_f32 v51, v51\n"
    "v_mfma_f32_16x16x32_bf16 v[16:19], v[168:171], v[144:147], v[16:19]\n"
    "buffer_load_dwordx4 v1, s[16:19], s83 offen offset:0 sc1 lds\n"
    "v_mfma_f32_16x16x32_bf16 v[16:19], v[164:167], v[140:143], v[16:19]\n"
    "v_exp_f32 v57, v57\n"
    "v_mfma_f32_16x16x32_bf16 v[16:19], v[160:163], v[136:139], v[16:19]\n"
    "v_cvt_pk_bf16_f32 v82, v56, v57\n"
    "v_cvt_pk_bf16_f32 v97, v50, v51\n"
    "v_mfma_f32_16x16x32_bf16 v[16:19], v[156:159], v[132:135], v[16:19]\n"
    "s_waitcnt lgkmcnt(0)\n"
    "v_exp_f32 v58, v58\n"
    "v_mfma_f32_16x16x32_bf16 v[20:23], v[180:183], a[192:195], v[104:107]\n"
    "buffer_load_dwordx4 v1, s[16:19], s83 offen offset:1024 sc1 lds\n"
    "v_mfma_f32_16x16x32_bf16 v[20:23], v[184:187], a[196:199], v[20:23]\n"
    "s_add_u32 s67, s67, 49152\n"
    "ds_read_b128 v[156:159], v248 offset:0\n"
    "ds_read_b128 v[160:163], v248 offset:12288\n"
    "v_mfma_f32_16x16x32_bf16 v[20:23], v[188:191], a[200:203], v[20:23]\n"
    "s_add_u32 s59, s59, 4\n"
    "ds_read_b128 v[164:167], v248 offset:1024\n"
    "v_mfma_f32_16x16x32_bf16 v[20:23], v[192:195], a[204:207], v[20:23]\n"
    "s_waitcnt vmcnt(12)\n"
    "ds_read_b128 v[168:171], v248 offset:13312\n"
    "v_mfma_f32_16x16x32_bf16 v[20:23], v[196:199], a[208:211], v[20:23]\n"
    "buffer_store_dwordx2 v[238:239], v240, s[36:39], s80 offen offset:0 sc1\n"
    "v_mfma_f32_16x16x32_bf16 v[20:23], v[200:203], a[212:215], v[20:23]\n"
    "s_add_u32 s80, s80, 256\n"
    "ds_read_b128 v[172:175], v248 offset:2048\n"
    "ds_read_b128 v[176:179], v248 offset:14336\n"
    "v_mfma_f32_16x16x32_bf16 v[24:27], v[200:203], a[236:239], v[104:107]\n"
    "v_exp_f32 v59, v59\n"
    "v_mfma_f32_16x16x32_bf16 v[24:27], v[196:199], a[232:235], v[24:27]\n"
    "v_cvt_pk_bf16_f32 v83, v58, v59\n"
    "v_mfma_f32_16x16x32_bf16 v[24:27], v[192:195], a[228:231], v[24:27]\n"
    "buffer_load_dwordx4 v[204:207], v242, s[40:43], s75 offen offset:0 nt\n"
    "v_mfma_f32_16x16x32_bf16 v[24:27], v[188:191], a[224:227], v[24:27]\n"
    "v_exp_f32 v60, v60\n"
    "v_mfma_f32_16x16x32_bf16 v[24:27], v[184:187], a[220:223], v[24:27]\n"
    "buffer_load_dwordx4 v[208:211], v242, s[40:43], s75 offen offset:768 nt\n"
    "v_mfma_f32_16x16x32_bf16 v[24:27], v[180:183], a[216:219], v[24:27]\n"
    "v_exp_f32 v61, v61\n"
    "v_mfma_f32_16x16x32_bf16 v[28:31], v[180:183], a[240:243], v[104:107]\n"
    "buffer_load_dwordx4 v[212:215], v242, s[40:43], s75 offen offset:256 nt\n"
    "v_mfma_f32_16x16x32_bf16 v[28:31], v[184:187], a[244:247], v[28:31]\n"
    "v_exp_f32 v62, v62\n"
    "v_mfma_f32_16x16x32_bf16 v[28:31], v[188:191], a[248:251], v[28:31]\n"
    "buffer_load_dwordx4 v[216:219], v242, s[40:43], s75 offen offset:1024 nt\n"
    "v_mfma_f32_16x16x32_bf16 v[28:31], v[192:195], a[252:255], v[28:31]\n"
    "v_exp_f32 v63, v63\n"
    "v_mfma_f32_16x16x32_bf16 v[28:31], v[196:199], v[124:127], v[28:31]\n"
    "buffer_load_dwordx4 v[220:223], v242, s[40:43], s75 offen offset:512 nt\n"
    "v_mfma_f32_16x16x32_bf16 v[28:31], v[200:203], v[128:131], v[28:31]\n"
    "v_cvt_pk_bf16_f32 v90, v60, v61\n"
    "v_cvt_pk_bf16_f32 v91, v62, v63\n"
    "v_mfma_f32_16x16x32_bf16 v[32:35], v[200:203], v[152:155], v[104:107]\n"
    "buffer_load_dwordx4 v[224:227], v242, s[40:43], s75 offen offset:1280 nt\n"
    "v_mfma_f32_16x16x32_bf16 v[32:35], v[196:199], v[148:151], v[32:35]\n"
    "v_exp_f32 v64, v64\n"
    "v_mfma_f32_16x16x32_bf16 v[32:35], v[192:195], v[144:147], v[32:35]\n"
    "v_exp_f32 v65, v65\n"
    "v_mfma_f32_16x16x32_bf16 v[32:35], v[188:191], v[140:143], v[32:35]\n"
    "v_cvt_pk_bf16_f32 v98, v64, v65\n"
    "v_mfma_f32_16x16x32_bf16 v[32:35], v[184:187], v[136:139], v[32:35]\n"
    "v_exp_f32 v66, v66\n"
    "v_mfma_f32_16x16x32_bf16 v[32:35], v[180:183], v[132:135], v[32:35]\n"
    "s_waitcnt lgkmcnt(0)\n"
    "v_mfma_f32_16x16x32_bf16 a[0:3], v[156:159], v[68:71], a[0:3]\n"
    "ds_read_b128 v[180:183], v248 offset:3072\n"
    "v_mfma_f32_16x16x32_bf16 a[0:3], v[160:163], v[72:75], a[0:3]\n"
    "ds_read_b128 v[184:187], v248 offset:15360\n"
    "v_mfma_f32_16x16x32_bf16 a[16:19], v[168:171], v[72:75], a[16:19]\n"
    "ds_read_b128 v[188:191], v248 offset:4096\n"
    "v_mfma_f32_16x16x32_bf16 a[16:19], v[164:167], v[68:71], a[16:19]\n"
    "ds_read_b128 v[192:195], v248 offset:16384\n"
    "v_mfma_f32_16x16x32_bf16 a[32:35], v[172:175], v[68:71], a[32:35]\n"
    "ds_read_b128 v[196:199], v248 offset:5120\n"
    "v_mfma_f32_16x16x32_bf16 a[32:35], v[176:179], v[72:75], a[32:35]\n"
    "ds_read_b128 v[200:203], v248 offset:17408\n"
    "v_mfma_f32_16x16x32_bf16 a[36:39], v[176:179], v[80:83], a[36:39]\n"
    "ds_read_b128 v[104:107], v251 offset:0\n"
    "v_mfma_f32_16x16x32_bf16 a[36:39], v[172:175], v[76:79], a[36:39]\n"
    "v_exp_f32 v67, v67\n"
    "v_mfma_f32_16x16x32_bf16 a[20:23], v[164:167], v[76:79], a[20:23]\n"
    "v_cvt_pk_bf16_f32 v99, v66, v67\n"
    "v_mfma_f32_16x16x32_bf16 a[20:23], v[168:171], v[80:83], a[20:23]\n"
    "v_exp_f32 v4, v4\n"
    "v_mfma_f32_16x16x32_bf16 a[4:7], v[160:163], v[80:83], a[4:7]\n"
    "v_exp_f32 v5, v5\n"
    "v_mfma_f32_16x16x32_bf16 a[4:7], v[156:159], v[76:79], a[4:7]\n"
    "v_exp_f32 v6, v6\n"
    "v_mfma_f32_16x16x32_bf16 a[8:11], v[156:159], v[84:87], a[8:11]\n"
    "v_exp_f32 v7, v7\n"
    "v_mfma_f32_16x16x32_bf16 a[8:11], v[160:163], v[88:91], a[8:11]\n"
    "v_mfma_f32_16x16x32_bf16 a[24:27], v[168:171], v[88:91], a[24:27]\n"
    "v_exp_f32 v8, v8\n"
    "v_mfma_f32_16x16x32_bf16 a[24:27], v[164:167], v[84:87], a[24:27]\n"
    "v_mfma_f32_16x16x32_bf16 a[40:43], v[172:175], v[84:87], a[40:43]\n"
    "v_mfma_f32_16x16x32_bf16 a[40:43], v[176:179], v[88:91], a[40:43]\n"
    "v_exp_f32 v9, v9\n"
    "v_mfma_f32_16x16x32_bf16 a[44:47], v[176:179], v[96:99], a[44:47]\n"
    "v_mfma_f32_16x16x32_bf16 a[44:47], v[172:175], v[92:95], a[44:47]\n"
    "v_mfma_f32_16x16x32_bf16 a[28:31], v[164:167], v[92:95], a[28:31]\n"
    "v_exp_f32 v10, v10\n"
    "v_mfma_f32_16x16x32_bf16 a[28:31], v[168:171], v[96:99], a[28:31]\n"
    "v_mfma_f32_16x16x32_bf16 a[12:15], v[160:163], v[96:99], a[12:15]\n"
    "v_exp_f32 v11, v11\n"
    "v_mfma_f32_16x16x32_bf16 a[12:15], v[156:159], v[92:95], a[12:15]\n"
    "s_waitcnt lgkmcnt(0)\n"
    "v_mfma_f32_16x16x32_bf16 a[48:51], v[180:183], v[68:71], a[48:51]\n"
    "ds_read_b128 v[156:159], v248 offset:6144\n"
    "v_mfma_f32_16x16x32_bf16 a[48:51], v[184:187], v[72:75], a[48:51]\n"
    "ds_read_b128 v[160:163], v248 offset:18432\n"
    "v_mfma_f32_16x16x32_bf16 a[64:67], v[192:195], v[72:75], a[64:67]\n"
    "ds_read_b128 v[164:167], v248 offset:7168\n"
    "v_mfma_f32_16x16x32_bf16 a[64:67], v[188:191], v[68:71], a[64:67]\n"
    "ds_read_b128 v[168:171], v248 offset:19456\n"
    "v_mfma_f32_16x16x32_bf16 a[80:83], v[196:199], v[68:71], a[80:83]\n"
    "ds_read_b128 v[172:175], v248 offset:8192\n"
    "v_mfma_f32_16x16x32_bf16 a[80:83], v[200:203], v[72:75], a[80:83]\n"
    "ds_read_b128 v[176:179], v248 offset:20480\n"
    "v_smfmac_f32_16x16x64_bf16 v[108:111], v[104:107], v[68:75], v253\n"
    "s_add_u32 s81, s81, 128\n"
    "s_cmp_gt_u32 s81, 4096\n"
    "s_cselect_b32 s81, 0, s81\n"
    "v_smfmac_f32_16x16x64_bf16 v[112:115], v[104:107], v[76:83], v253\n"
    "v_mfma_f32_16x16x32_bf16 a[84:87], v[200:203], v[80:83], a[84:87]\n"
    "v_exp_f32 v12, v12\n"
    "v_mfma_f32_16x16x32_bf16 a[84:87], v[196:199], v[76:79], a[84:87]\n"
    "v_exp_f32 v13, v13\n"
    "v_mfma_f32_16x16x32_bf16 a[68:71], v[188:191], v[76:79], a[68:71]\n"
    "v_exp_f32 v14, v14\n"
    "v_mfma_f32_16x16x32_bf16 a[68:71], v[192:195], v[80:83], a[68:71]\n"
    "v_exp_f32 v15, v15\n"
    "v_mfma_f32_16x16x32_bf16 a[52:55], v[184:187], v[80:83], a[52:55]\n"
    "v_exp_f32 v16, v16\n"
    "v_mfma_f32_16x16x32_bf16 a[52:55], v[180:183], v[76:79], a[52:55]\n"
    "v_mfma_f32_16x16x32_bf16 a[56:59], v[180:183], v[84:87], a[56:59]\n"
    "v_exp_f32 v17, v17\n"
    "v_mfma_f32_16x16x32_bf16 a[56:59], v[184:187], v[88:91], a[56:59]\n"
    "v_mfma_f32_16x16x32_bf16 a[72:75], v[192:195], v[88:91], a[72:75]\n"
    "v_mfma_f32_16x16x32_bf16 a[72:75], v[188:191], v[84:87], a[72:75]\n"
    "v_exp_f32 v18, v18\n"
    "v_mfma_f32_16x16x32_bf16 a[88:91], v[196:199], v[84:87], a[88:91]\n"
    "v_mfma_f32_16x16x32_bf16 a[88:91], v[200:203], v[88:91], a[88:91]\n"
    "v_smfmac_f32_16x16x64_bf16 v[116:119], v[104:107], v[84:91], v253\n"
    "v_smfmac_f32_16x16x64_bf16 v[120:123], v[104:107], v[92:99], v253\n"
    "v_mfma_f32_16x16x32_bf16 a[92:95], v[200:203], v[96:99], a[92:95]\n"
    "v_exp_f32 v19, v19\n"
    "v_mfma_f32_16x16x32_bf16 a[92:95], v[196:199], v[92:95], a[92:95]\n"
    "v_exp_f32 v20, v20\n"
    "v_mfma_f32_16x16x32_bf16 a[76:79], v[188:191], v[92:95], a[76:79]\n"
    "v_mfma_f32_16x16x32_bf16 a[76:79], v[192:195], v[96:99], a[76:79]\n"
    "v_exp_f32 v21, v21\n"
    "v_mfma_f32_16x16x32_bf16 a[60:63], v[184:187], v[96:99], a[60:63]\n"
    "v_mfma_f32_16x16x32_bf16 a[60:63], v[180:183], v[92:95], a[60:63]\n"
    "s_waitcnt lgkmcnt(0)\n"
    "v_mfma_f32_16x16x32_bf16 a[96:99], v[156:159], v[68:71], a[96:99]\n"
    "ds_read_b128 v[180:183], v248 offset:9216\n"
    "v_mfma_f32_16x16x32_bf16 a[96:99], v[160:163], v[72:75], a[96:99]\n"
    "ds_read_b128 v[184:187], v248 offset:21504\n"
    "v_mfma_f32_16x16x32_bf16 a[112:115], v[168:171], v[72:75], a[112:115]\n"
    "ds_read_b128 v[188:191], v248 offset:10240\n"
    "v_mfma_f32_16x16x32_bf16 a[112:115], v[164:167], v[68:71], a[112:115]\n"
    "ds_read_b128 v[192:195], v248 offset:22528\n"
    "v_mfma_f32_16x16x32_bf16 a[128:131], v[172:175], v[68:71], a[128:131]\n"
    "buffer_load_dwordx2 v[236:237], v241, s[36:39], s81 offen offset:0 sc1\n"
    "v_mfma_f32_16x16x32_bf16 a[128:131], v[176:179], v[72:75], a[128:131]\n"
    "ds_read_b128 v[196:199], v248 offset:11264\n"
    "ds_read_b128 v[200:203], v248 offset:23552\n"
    "v_mfma_f32_16x16x32_bf16 a[132:135], v[176:179], v[80:83], a[132:135]\n"
    "v_exp_f32 v22, v22\n"
    "v_mfma_f32_16x16x32_bf16 a[132:135], v[172:175], v[76:79], a[132:135]\n"
    "v_exp_f32 v23, v23\n"
    "v_mfma_f32_16x16x32_bf16 a[116:119], v[164:167], v[76:79], a[116:119]\n"
    "v_exp_f32 v24, v24\n"
    "v_mfma_f32_16x16x32_bf16 a[116:119], v[168:171], v[80:83], a[116:119]\n"
    "v_exp_f32 v25, v25\n"
    "v_mfma_f32_16x16x32_bf16 a[100:103], v[160:163], v[80:83], a[100:103]\n"
    "v_mfma_f32_16x16x32_bf16 a[100:103], v[156:159], v[76:79], a[100:103]\n"
    "v_exp_f32 v26, v26\n"
    "v_mfma_f32_16x16x32_bf16 a[104:107], v[156:159], v[84:87], a[104:107]\n"
    "v_mfma_f32_16x16x32_bf16 a[104:107], v[160:163], v[88:91], a[104:107]\n"
    "v_exp_f32 v27, v27\n"
    "v_mfma_f32_16x16x32_bf16 a[120:123], v[168:171], v[88:91], a[120:123]\n"
    "v_mfma_f32_16x16x32_bf16 a[120:123], v[164:167], v[84:87], a[120:123]\n"
    "v_mfma_f32_16x16x32_bf16 a[136:139], v[172:175], v[84:87], a[136:139]\n"
    "v_exp_f32 v28, v28\n"
    "v_mfma_f32_16x16x32_bf16 a[136:139], v[176:179], v[88:91], a[136:139]\n"
    "v_mfma_f32_16x16x32_bf16 a[140:143], v[176:179], v[96:99], a[140:143]\n"
    "v_mfma_f32_16x16x32_bf16 a[140:143], v[172:175], v[92:95], a[140:143]\n"
    "v_exp_f32 v29, v29\n"
    "v_mfma_f32_16x16x32_bf16 a[124:127], v[164:167], v[92:95], a[124:127]\n"
    "v_mfma_f32_16x16x32_bf16 a[124:127], v[168:171], v[96:99], a[124:127]\n"
    "v_exp_f32 v30, v30\n"
    "v_mfma_f32_16x16x32_bf16 a[108:111], v[160:163], v[96:99], a[108:111]\n"
    "v_mfma_f32_16x16x32_bf16 a[108:111], v[156:159], v[92:95], a[108:111]\n"
    "s_waitcnt lgkmcnt(0)\n"
    "v_mfma_f32_16x16x32_bf16 a[144:147], v[180:183], v[68:71], a[144:147]\n"
    "ds_read_b128 v[156:159], v3 offset:12288\n"
    "v_mfma_f32_16x16x32_bf16 a[144:147], v[184:187], v[72:75], a[144:147]\n"
    "ds_read_b128 v[160:163], v3 offset:14336\n"
    "v_mfma_f32_16x16x32_bf16 a[160:163], v[192:195], v[72:75], a[160:163]\n"
    "ds_read_b128 v[164:167], v3 offset:16384\n"
    "v_mfma_f32_16x16x32_bf16 a[160:163], v[188:191], v[68:71], a[160:163]\n"
    "ds_read_b128 v[168:171], v3 offset:18432\n"
    "v_mfma_f32_16x16x32_bf16 a[176:179], v[196:199], v[68:71], a[176:179]\n"
    "ds_read_b128 v[172:175], v3 offset:20480\n"
    "v_mfma_f32_16x16x32_bf16 a[176:179], v[200:203], v[72:75], a[176:179]\n"
    "ds_read_b128 v[176:179], v3 offset:22528\n"
    "v_mfma_f32_16x16x32_bf16 a[180:183], v[200:203], v[80:83], a[180:183]\n"
    "ds_read_b128 v[100:103], v250 offset:896\n"
    "v_mfma_f32_16x16x32_bf16 a[180:183], v[196:199], v[76:79], a[180:183]\n"
    "v_exp_f32 v31, v31\n"
    "v_mfma_f32_16x16x32_bf16 a[164:167], v[188:191], v[76:79], a[164:167]\n"
    "v_exp_f32 v32, v32\n"
    "v_mfma_f32_16x16x32_bf16 a[164:167], v[192:195], v[80:83], a[164:167]\n"
    "v_exp_f32 v33, v33\n"
    "v_mfma_f32_16x16x32_bf16 a[148:151], v[184:187], v[80:83], a[148:151]\n"
    "v_exp_f32 v34, v34\n"
    "v_mfma_f32_16x16x32_bf16 a[148:151], v[180:183], v[76:79], a[148:151]\n"
    "v_exp_f32 v35, v35\n"
    "v_mfma_f32_16x16x32_bf16 a[152:155], v[180:183], v[84:87], a[152:155]\n"
    "v_mfma_f32_16x16x32_bf16 a[152:155], v[184:187], v[88:91], a[152:155]\n"
    "v_mfma_f32_16x16x32_bf16 a[168:171], v[192:195], v[88:91], a[168:171]\n"
    "s_waitcnt vmcnt(8)\n"
    "ds_add_u32 v247, v246\n"
    "v_mfma_f32_16x16x32_bf16 a[168:171], v[188:191], v[84:87], a[168:171]\n"
    "v_mfma_f32_16x16x32_bf16 a[184:187], v[196:199], v[84:87], a[184:187]\n"
    "v_mfma_f32_16x16x32_bf16 a[184:187], v[200:203], v[88:91], a[184:187]\n"
    "v_mfma_f32_16x16x32_bf16 a[188:191], v[200:203], v[96:99], a[188:191]\n"
    "v_mfma_f32_16x16x32_bf16 a[188:191], v[196:199], v[92:95], a[188:191]\n"
    "v_mfma_f32_16x16x32_bf16 a[172:175], v[188:191], v[92:95], a[172:175]\n"
    "s_sub_u32 s74, s68, s65\n"
    "v_mfma_f32_16x16x32_bf16 a[172:175], v[192:195], v[96:99], a[172:175]\n"
    "s_add_u32 s66, s66, 1\n"
    "v_mov_b32 v2, v3\n"
    "v_add_u32 v3, s74, v252\n"
    "v_mfma_f32_16x16x32_bf16 a[156:159], v[184:187], v[96:99], a[156:159]\n"
    "s_waitcnt lgkmcnt(1)\n"
    "s_cmp_eq_u32 s66, 31\n"
    "v_mfma_f32_16x16x32_bf16 a[156:159], v[180:183], v[92:95], a[156:159]\n"
    "s_cbranch_scc1 .LtailP\n"
    ".LNV:\n"
    "v_mfma_f32_16x16x32_bf16 v[36:39], v[156:159], a[192:195], v[100:103]\n"
    "ds_read_b128 v[180:183], v2 offset:13312\n"
    "v_cvt_pk_bf16_f32 v68, v4, v5\n"
    "v_mfma_f32_16x16x32_bf16 v[36:39], v[160:163], a[196:199], v[36:39]\n"
    "ds_read_b128 v[184:187], v2 offset:15360\n"
    "v_mfma_f32_16x16x32_bf16 v[36:39], v[164:167], a[200:203], v[36:39]\n"
    "ds_read_b128 v[188:191], v2 offset:17408\n"
    "v_mfma_f32_16x16x32_bf16 v[36:39], v[168:171], a[204:207], v[36:39]\n"
    "ds_read_b128 v[192:195], v2 offset:19456\n"
    "v_cvt_pk_bf16_f32 v69, v6, v7\n"
    "v_mfma_f32_16x16x32_bf16 v[36:39], v[172:175], a[208:211], v[36:39]\n"
    "ds_read_b128 v[196:199], v2 offset:21504\n"
    "v_mfma_f32_16x16x32_bf16 v[36:39], v[176:179], a[212:215], v[36:39]\n"
    "ds_read_b128 v[200:203], v2 offset:23552\n"
    "v_mfma_f32_16x16x32_bf16 v[40:43], v[176:179], a[236:239], v[100:103]\n"
    "ds_read_b128 v[104:107], v250 offset:912\n"
    "v_mfma_f32_16x16x32_bf16 v[40:43], v[172:175], a[232:235], v[40:43]\n"
    "s_add_u32 s68, s68, 24576\n"
    "v_cvt_pk_bf16_f32 v76, v8, v9\n"
    "v_mfma_f32_16x16x32_bf16 v[40:43], v[168:171], a[228:231], v[40:43]\n"
    "s_cmp_ge_u32 s68, s69\n"
    "s_cselect_b32 s68, s65, s68\n"
    "v_mfma_f32_16x16x32_bf16 v[40:43], v[164:167], a[224:227], v[40:43]\n"
    "v_exp_f32 v36, v36\n"
    "v_mfma_f32_16x16x32_bf16 v[40:43], v[160:163], a[220:223], v[40:43]\n"
    "v_exp_f32 v37, v37\n"
    "v_mfma_f32_16x16x32_bf16 v[40:43], v[156:159], a[216:219], v[40:43]\n"
    "v_cvt_pk_bf16_f32 v77, v10, v11\n"
    "v_cvt_pk_bf16_f32 v72, v36, v37\n"
    "v_mfma_f32_16x16x32_bf16 v[44:47], v[156:159], a[240:243], v[100:103]\n"
    "v_mfma_f32_16x16x32_bf16 v[44:47], v[160:163], a[244:247], v[44:47]\n"
    "v_exp_f32 v38, v38\n"
    "v_mfma_f32_16x16x32_bf16 v[44:47], v[164:167], a[248:251], v[44:47]\n"
    "v_mfma_f32_16x16x32_bf16 v[44:47], v[168:171], a[252:255], v[44:47]\n"
    "v_cvt_pk_bf16_f32 v84, v12, v13\n"
    "v_mfma_f32_16x16x32_bf16 v[44:47], v[172:175], v[124:127], v[44:47]\n"
    "v_exp_f32 v39, v39\n"
    "v_mfma_f32_16x16x32_bf16 v[44:47], v[176:179], v[128:131], v[44:47]\n"
    "v_cvt_pk_bf16_f32 v73, v38, v39\n"
    "v_mfma_f32_16x16x32_bf16 v[48:51], v[176:179], v[152:155], v[100:103]\n"
    "ds_read_b32 v245, v247\n"
    "v_cvt_pk_bf16_f32 v85, v14, v15\n"
    "v_mfma_f32_16x16x32_bf16 v[48:51], v[172:175], v[148:151], v[48:51]\n"
    "s_add_u32 m0, s68, 0\n"
    "v_exp_f32 v40, v40\n"
    "v_mfma_f32_16x16x32_bf16 v[48:51], v[168:171], v[144:147], v[48:51]\n"
    "v_mfma_f32_16x16x32_bf16 v[48:51], v[164:167], v[140:143], v[48:51]\n"
    "v_mfma_f32_16x16x32_bf16 v[48:51], v[160:163], v[136:139], v[48:51]\n"
    "s_waitcnt vmcnt(0)\n"
    "v_cmp_eq_u64 s[54:55], v[236:237], v[238:239]\n"
    "v_cvt_pk_bf16_f32 v92, v16, v17\n"
    "v_mfma_f32_16x16x32_bf16 v[48:51], v[156:159], v[132:135], v[48:51]\n"
    "s_waitcnt lgkmcnt(1)\n"
    "v_exp_f32 v41, v41\n"
    "v_mfma_f32_16x16x32_bf16 v[52:55], v[180:183], a[192:195], v[104:107]\n"
    "s_waitcnt lgkmcnt(0)\n"
    "s_waitcnt vmcnt(1)\n"
    "v_cmp_le_u32 s[56:57], s59, v245\n"
    "v_mfma_f32_16x16x32_bf16 v[52:55], v[184:187], a[196:199], v[52:55]\n"
    "s_add_u32 s76, s71, s60\n"
    "v_exp_f32 v42, v42\n"
    "v_mfma_f32_16x16x32_bf16 v[52:55], v[188:191], a[200:203], v[52:55]\n"
    "s_and_b64 s[54:55], s[54:55], s[56:57]\n"
    "s_cmp_eq_u64 s[54:55], -1\n"
    "v_mfma_f32_16x16x32_bf16 v[52:55], v[192:195], a[204:207], v[52:55]\n"
    "s_cbranch_scc0 .Lpoll4\n"
    ".Lpoll4r:\n"
    "v_mfma_f32_16x16x32_bf16 v[52:55], v[196:199], a[208:211], v[52:55]\n"
    "buffer_load_dwordx4 v1, s[16:19], s67 offen offset:0 sc1 lds\n"
    "v_mfma_f32_16x16x32_bf16 v[52:55], v[200:203], a[212:215], v[52:55]\n"
    "s_add_u32 s77, s71, s61\n"
    "ds_read_b128 v[156:159], v3 offset:0\n"
    "ds_read_b128 v[160:163], v3 offset:2048\n"
    "v_mfma_f32_16x16x32_bf16 v[56:59], v[200:203], a[236:239], v[104:107]\n"
    "ds_read_b128 v[164:167], v3 offset:4096\n"
    "ds_read_b128 v[168:171], v3 offset:6144\n"
    "v_mfma_f32_16x16x32_bf16 v[56:59], v[196:199], a[232:235], v[56:59]\n"
    "ds_read_b128 v[172:175], v3 offset:8192\n"
    "ds_read_b128 v[176:179], v3 offset:10240\n"
    "v_mfma_f32_16x16x32_bf16 v[56:59], v[192:195], a[228:231], v[56:59]\n"
    "buffer_load_dwordx4 v1, s[16:19], s67 offen offset:1024 sc1 lds\n"
    "v_mfma_f32_16x16x32_bf16 v[56:59], v[188:191], a[224:227], v[56:59]\n"
    "ds_read_b128 v[100:103], v250 offset:1536\n"
    "v_cvt_pk_bf16_f32 v93, v18, v19\n"
    "v_mfma_f32_16x16x32_bf16 v[56:59], v[184:187], a[220:223], v[56:59]\n"
    "s_add_u32 s78, s71, s62\n"
    "v_cvt_pk_bf16_f32 v70, v20, v21\n"
    "v_cvt_pk_bf16_f32 v71, v22, v23\n"
    "v_mfma_f32_16x16x32_bf16 v[56:59], v[180:183], a[216:219], v[56:59]\n"
    "v_exp_f32 v43, v43\n"
    "v_mfma_f32_16x16x32_bf16 v[60:63], v[180:183], a[240:243], v[104:107]\n"
    "buffer_load_dwordx4 v1, s[16:19], s67 offen offset:2048 sc1 lds\n"
    "v_mfma_f32_16x16x32_bf16 v[60:63], v[184:187], a[244:247], v[60:63]\n"
    "v_cvt_pk_bf16_f32 v78, v24, v25\n"
    "v_cvt_pk_bf16_f32 v80, v40, v41\n"
    "v_mfma_f32_16x16x32_bf16 v[60:63], v[188:191], a[248:251], v[60:63]\n"
    "v_cvt_pk_bf16_f32 v81, v42, v43\n"
    "v_mfma_f32_16x16x32_bf16 v[60:63], v[192:195], a[252:255], v[60:63]\n"
    "v_exp_f32 v44, v44\n"
    "v_mfma_f32_16x16x32_bf16 v[60:63], v[196:199], v[124:127], v[60:63]\n"
    "buffer_load_dwordx4 v1, s[16:19], s67 offen offset:3072 sc1 lds\n"
    "v_mfma_f32_16x16x32_bf16 v[60:63], v[200:203], v[128:131], v[60:63]\n"
    "s_add_u32 m0, s68, 4096\n"
    "v_cvt_pk_bf16_f32 v79, v26, v27\n"
    "v_cvt_pk_u16_u32 v204, v204, v208\n"
    "v_mfma_f32_16x16x32_bf16 v[64:67], v[200:203], v[152:155], v[104:107]\n"
    "s_add_u32 s83, s67, 4096\n"
    "v_exp_f32 v45, v45\n"
    "v_mfma_f32_16x16x32_bf16 v[64:67], v[196:199], v[148:151], v[64:67]\n"
    "v_exp_f32 v46, v46\n"
    "v_mfma_f32_16x16x32_bf16 v[64:67], v[192:195], v[144:147], v[64:67]\n"
    "buffer_load_dwordx4 v1, s[16:19], s83 offen offset:0 sc1 lds\n"
    "v_mfma_f32_16x16x32_bf16 v[64:67], v[188:191], v[140:143], v[64:67]\n"
    "v_cvt_pk_bf16_f32 v86, v28, v29\n"
    "v_cvt_pk_bf16_f32 v88, v44, v45\n"
    "v_mfma_f32_16x16x32_bf16 v[64:67], v[184:187], v[136:139], v[64:67]\n"
    "v_exp_f32 v47, v47\n"
    "v_mfma_f32_16x16x32_bf16 v[64:67], v[180:183], v[132:135], v[64:67]\n"
    "s_waitcnt lgkmcnt(0)\n"
    "v_cvt_pk_bf16_f32 v89, v46, v47\n"
    "v_cvt_pk_u16_u32 v205, v205, v209\n"
    "v_mfma_f32_16x16x32_bf16 v[4:7], v[156:159], a[192:195], v[100:103]\n"
    "buffer_load_dwordx4 v1, s[16:19], s83 offen offset:1024 sc1 lds\n"
    "v_mfma_f32_16x16x32_bf16 v[4:7], v[160:163], a[196:199], v[4:7]\n"
    "s_add_u32 m0, s70, 0\n"
    "ds_read_b128 v[180:183], v3 offset:1024\n"
    "ds_read_b128 v[184:187], v3 offset:3072\n"
    "v_mfma_f32_16x16x32_bf16 v[4:7], v[164:167], a[200:203], v[4:7]\n"
    "s_add_u32 s83, s67, 24576\n"
    "ds_read_b128 v[188:191], v3 offset:5120\n"
    "v_cvt_pk_bf16_f32 v87, v30, v31\n"
    "v_mfma_f32_16x16x32_bf16 v[4:7], v[168:171], a[204:207], v[4:7]\n"
    "ds_read_b128 v[192:195], v3 offset:7168\n"
    "v_cvt_pk_u16_u32 v206, v206, v210\n"
    "v_mfma_f32_16x16x32_bf16 v[4:7], v[172:175], a[208:211], v[4:7]\n"
    "buffer_load_dwordx4 v1, s[16:19], s83 offen offset:0 sc1 lds\n"
    "v_mfma_f32_16x16x32_bf16 v[4:7], v[176:179], a[212:215], v[4:7]\n"
    "ds_read_b128 v[196:199], v3 offset:9216\n"
    "ds_read_b128 v[200:203], v3 offset:11264\n"
    "v_mfma_f32_16x16x32_bf16 v[8:11], v[176:179], a[236:239], v[100:103]\n"
    "ds_read_b128 v[104:107], v250 offset:1552\n"
    "v_cvt_pk_bf16_f32 v94, v32, v33\n"
    "v_mfma_f32_16x16x32_bf16 v[8:11], v[172:175], a[232:235], v[8:11]\n"
    "v_exp_f32 v52, v52\n"
    "v_mfma_f32_16x16x32_bf16 v[8:11], v[168:171], a[228:231], v[8:11]\n"
    "buffer_load_dwordx4 v1, s[16:19], s83 offen offset:1024 sc1 lds\n"
    "v_mfma_f32_16x16x32_bf16 v[8:11], v[164:167], a[224:227], v[8:11]\n"
    "v_cvt_pk_bf16_f32 v95, v34, v35\n"
    "v_cvt_pk_u16_u32 v207, v207, v211\n"
    "v_mfma_f32_16x16x32_bf16 v[8:11], v[160:163], a[220:223], v[8:11]\n"
    "v_exp_f32 v53, v53\n"
    "v_mfma_f32_16x16x32_bf16 v[8:11], v[156:159], a[216:219], v[8:11]\n"
    "v_exp_f32 v54, v54\n"
    "v_mfma_f32_16x16x32_bf16 v[12:15], v[156:159], a[240:243], v[100:103]\n"
    "buffer_load_dwordx4 v1, s[16:19], s83 offen offset:2048 sc1 lds\n"
    "v_mfma_f32_16x16x32_bf16 v[12:15], v[160:163], a[244:247], v[12:15]\n"
    "v_cvt_pk_bf16_f32 v74, v52, v53\n"
    "v_or3_b32 v243, v204, v205, v243\n"
    "v_mfma_f32_16x16x32_bf16 v[12:15], v[164:167], a[248:251], v[12:15]\n"
    "v_exp_f32 v55, v55\n"
    "v_mfma_f32_16x16x32_bf16 v[12:15], v[168:171], a[252:255], v[12:15]\n"
    "v_cvt_pk_bf16_f32 v75, v54, v55\n"
    "v_mfma_f32_16x16x32_bf16 v[12:15], v[172:175], v[124:127], v[12:15]\n"
    "buffer_load_dwordx4 v1, s[16:19], s83 offen offset:3072 sc1 lds\n"
    "v_mfma_f32_16x16x32_bf16 v[12:15], v[176:179], v[128:131], v[12:15]\n"
    "s_add_u32 m0, s70, 4096\n"
    "v_exp_f32 v56, v56\n"
    "v_mfma_f32_16x16x32_bf16 v[16:19], v[176:179], v[152:155], v[100:103]\n"
    "s_add_u32 s83, s67, 28672\n"
    "v_exp_f32 v48, v48\n"
    "v_mfma_f32_16x16x32_bf16 v[16:19], v[172:175], v[148:151], v[16:19]\n"
    "v_exp_f32 v49, v49\n"
    "v_mfma_f32_16x16x32_bf16 v[16:19], v[168:171], v[144:147], v[16:19]\n"
    "buffer_load_dwordx4 v1, s[16:19], s83 offen offset:0 sc1 lds\n"
    "v_mfma_f32_16x16x32_bf16 v[16:19], v[164:167], v[140:143], v[16:19]\n"
    "v_exp_f32 v57, v57\n"
    "v_mfma_f32_16x16x32_bf16 v[16:19], v[160:163], v[136:139], v[16:19]\n"
    "v_cvt_pk_bf16_f32 v82, v56, v57\n"
    "v_cvt_pk_bf16_f32 v96, v48, v49\n"
    "v_mfma_f32_16x16x32_bf16 v[16:19], v[156:159], v[132:135], v[16:19]\n"
    "s_waitcnt lgkmcnt(0)\n"
    "v_exp_f32 v58, v58\n"
    "v_mfma_f32_16x16x32_bf16 v[20:23], v[180:183], a[192:195], v[104:107]\n"
    "buffer_load_dwordx4 v1, s[16:19], s83 offen offset:1024 sc1 lds\n"
    "v_mfma_f32_16x16x32_bf16 v[20:23], v[184:187], a[196:199], v[20:23]\n"
    "s_add_u32 s67, s67, 49152\n"
    "ds_read_b128 v[156:159], v249 offset:0\n"
    "ds_read_b128 v[160:163], v249 offset:12288\n"
    "v_mfma_f32_16x16x32_bf16 v[20:23], v[188:191], a[200:203], v[20:23]\n"
    "s_add_u32 s59, s59, 4\n"
    "ds_read_b128 v[164:167], v249 offset:1024\n"
    "v_or3_b32 v243, v206, v207, v243\n"
    "v_mfma_f32_16x16x32_bf16 v[20:23], v[192:195], a[204:207], v[20:23]\n"
    "ds_read_b128 v[168:171], v249 offset:13312\n"
    "v_cvt_pk_u16_u32 v212, v212, v216\n"
    "v_mfma_f32_16x16x32_bf16 v[20:23], v[196:199], a[208:211], v[20:23]\n"
    "ds_read_b128 v[172:175], v249 offset:2048\n"
    "v_cvt_pk_u16_u32 v213, v213, v217\n"
    "v_mfma_f32_16x16x32_bf16 v[20:23], v[200:203], a[212:215], v[20:23]\n"
    "ds_read_b128 v[176:179], v249 offset:14336\n"
    "v_mfma_f32_16x16x32_bf16 v[24:27], v[200:203], a[236:239], v[104:107]\n"
    "v_exp_f32 v59, v59\n"
    "v_mfma_f32_16x16x32_bf16 v[24:27], v[196:199], a[232:235], v[24:27]\n"
    "v_cvt_pk_bf16_f32 v83, v58, v59\n"
    "v_mfma_f32_16x16x32_bf16 v[24:27], v[192:195], a[228:231], v[24:27]\n"
    "v_exp_f32 v60, v60\n"
    "v_mfma_f32_16x16x32_bf16 v[24:27], v[188:191], a[224:227], v[24:27]\n"
    "v_exp_f32 v61, v61\n"
    "v_mfma_f32_16x16x32_bf16 v[24:27], v[184:187], a[220:223], v[24:27]\n"
    "v_exp_f32 v62, v62\n"
    "v_mfma_f32_16x16x32_bf16 v[24:27], v[180:183], a[216:219], v[24:27]\n"
    "v_cvt_pk_bf16_f32 v90, v60, v61\n"
    "v_cvt_pk_u16_u32 v214, v214, v218\n"
    "v_mfma_f32_16x16x32_bf16 v[28:31], v[180:183], a[240:243], v[104:107]\n"
    "v_exp_f32 v50, v50\n"
    "v_mfma_f32_16x16x32_bf16 v[28:31], v[184:187], a[244:247], v[28:31]\n"
    "v_exp_f32 v63, v63\n"
    "v_mfma_f32_16x16x32_bf16 v[28:31], v[188:191], a[248:251], v[28:31]\n"
    "v_cvt_pk_bf16_f32 v91, v62, v63\n"
    "v_cvt_pk_u16_u32 v215, v215, v219\n"
    "v_mfma_f32_16x16x32_bf16 v[28:31], v[192:195], a[252:255], v[28:31]\n"
    "v_exp_f32 v51, v51\n"
    "v_mfma_f32_16x16x32_bf16 v[28:31], v[196:199], v[124:127], v[28:31]\n"
    "v_exp_f32 v64, v64\n"
    "v_mfma_f32_16x16x32_bf16 v[28:31], v[200:203], v[128:131], v[28:31]\n"
    "v_cvt_pk_bf16_f32 v97, v50, v51\n"
    "v_mfma_f32_16x16x32_bf16 v[32:35], v[200:203], v[152:155], v[104:107]\n"
    "v_mfma_f32_16x16x32_bf16 v[32:35], v[196:199], v[148:151], v[32:35]\n"
    "v_exp_f32 v65, v65\n"
    "v_mfma_f32_16x16x32_bf16 v[32:35], v[192:195], v[144:147], v[32:35]\n"
    "v_cvt_pk_bf16_f32 v98, v64, v65\n"
    "v_or3_b32 v243, v212, v213, v243\n"
    "v_mfma_f32_16x16x32_bf16 v[32:35], v[188:191], v[140:143], v[32:35]\n"
    "v_mfma_f32_16x16x32_bf16 v[32:35], v[184:187], v[136:139], v[32:35]\n"
    "v_exp_f32 v66, v66\n"
    "v_mfma_f32_16x16x32_bf16 v[32:35], v[180:183], v[132:135], v[32:35]\n"
    "s_waitcnt lgkmcnt(0)\n"
    "v_mfma_f32_16x16x32_bf16 a[0:3], v[156:159], v[68:71], a[0:3]\n"
    "ds_read_b128 v[180:183], v249 offset:3072\n"
    "v_or3_b32 v243, v214, v215, v243\n"
    "v_mfma_f32_16x16x32_bf16 a[0:3], v[160:163], v[72:75], a[0:3]\n"
    "ds_read_b128 v[184:187], v249 offset:15360\n"
    "v_mfma_f32_16x16x32_bf16 a[16:19], v[168:171], v[72:75], a[16:19]\n"
    "ds_read_b128 v[188:191], v249 offset:4096\n"
    "v_mfma_f32_16x16x32_bf16 a[16:19], v[164:167], v[68:71], a[16:19]\n"
    "ds_read_b128 v[192:195], v249 offset:16384\n"
    "v_mfma_f32_16x16x32_bf16 a[32:35], v[172:175], v[68:71], a[32:35]\n"
    "ds_read_b128 v[196:199], v249 offset:5120\n"
    "v_mfma_f32_16x16x32_bf16 a[32:35], v[176:179], v[72:75], a[32:35]\n"
    "ds_read_b128 v[200:203], v249 offset:17408\n"
    "v_cvt_pk_u16_u32 v220, v220, v224\n"
    "v_mfma_f32_16x16x32_bf16 a[36:39], v[176:179], v[80:83], a[36:39]\n"
    "ds_read_b128 v[104:107], v251 offset:768\n"
    "v_mfma_f32_16x16x32_bf16 a[36:39], v[172:175], v[76:79], a[36:39]\n"
    "v_exp_f32 v67, v67\n"
    "v_mfma_f32_16x16x32_bf16 a[20:23], v[164:167], v[76:79], a[20:23]\n"
    "v_cvt_pk_bf16_f32 v99, v66, v67\n"
    "v_mfma_f32_16x16x32_bf16 a[20:23], v[168:171], v[80:83], a[20:23]\n"
    "v_exp_f32 v4, v4\n"
    "v_mfma_f32_16x16x32_bf16 a[4:7], v[160:163], v[80:83], a[4:7]\n"
    "v_exp_f32 v5, v5\n"
    "v_mfma_f32_16x16x32_bf16 a[4:7], v[156:159], v[76:79], a[4:7]\n"
    "v_exp_f32 v6, v6\n"
    "v_mfma_f32_16x16x32_bf16 a[8:11], v[156:159], v[84:87], a[8:11]\n"
    "v_exp_f32 v7, v7\n"
    "v_mfma_f32_16x16x32_bf16 a[8:11], v[160:163], v[88:91], a[8:11]\n"
    "v_cvt_pk_u16_u32 v221, v221, v225\n"
    "v_mfma_f32_16x16x32_bf16 a[24:27], v[168:171], v[88:91], a[24:27]\n"
    "v_exp_f32 v8, v8\n"
    "v_mfma_f32_16x16x32_bf16 a[24:27], v[164:167], v[84:87], a[24:27]\n"
    "v_cvt_pk_u16_u32 v222, v222, v226\n"
    "v_mfma_f32_16x16x32_bf16 a[40:43], v[172:175], v[84:87], a[40:43]\n"
    "v_mfma_f32_16x16x32_bf16 a[40:43], v[176:179], v[88:91], a[40:43]\n"
    "v_exp_f32 v9, v9\n"
    "v_mfma_f32_16x16x32_bf16 a[44:47], v[176:179], v[96:99], a[44:47]\n"
    "v_mfma_f32_16x16x32_bf16 a[44:47], v[172:175], v[92:95], a[44:47]\n"
    "v_mfma_f32_16x16x32_bf16 a[28:31], v[164:167], v[92:95], a[28:31]\n"
    "v_exp_f32 v10, v10\n"
    "v_mfma_f32_16x16x32_bf16 a[28:31], v[168:171], v[96:99], a[28:31]\n"
    "v_cvt_pk_u16_u32 v223, v223, v227\n"
    "v_mfma_f32_16x16x32_bf16 a[12:15], v[160:163], v[96:99], a[12:15]\n"
    "v_exp_f32 v11, v11\n"
    "v_mfma_f32_16x16x32_bf16 a[12:15], v[156:159], v[92:95], a[12:15]\n"
    "s_waitcnt lgkmcnt(0)\n"
    "v_mfma_f32_16x16x32_bf16 a[48:51], v[180:183], v[68:71], a[48:51]\n"
    "ds_read_b128 v[156:159], v249 offset:6144\n"
    "v_mfma_f32_16x16x32_bf16 a[48:51], v[184:187], v[72:75], a[48:51]\n"
    "ds_read_b128 v[160:163], v249 offset:18432\n"
    "v_mfma_f32_16x16x32_bf16 a[64:67], v[192:195], v[72:75], a[64:67]\n"
    "ds_read_b128 v[164:167], v249 offset:7168\n"
    "v_or3_b32 v243, v220, v221, v243\n"
    "v_mfma_f32_16x16x32_bf16 a[64:67], v[188:191], v[68:71], a[64:67]\n"
    "ds_read_b128 v[168:171], v249 offset:19456\n"
    "v_mfma_f32_16x16x32_bf16 a[80:83], v[196:199], v[68:71], a[80:83]\n"
    "ds_read_b128 v[172:175], v249 offset:8192\n"
    "v_mfma_f32_16x16x32_bf16 a[80:83], v[200:203], v[72:75], a[80:83]\n"
    "ds_read_b128 v[176:179], v249 offset:20480\n"
    "v_smfmac_f32_16x16x64_bf16 v[108:111], v[104:107], v[68:75], v253\n"
    "s_add_u32 s81, s81, 128\n"
    "s_cmp_gt_u32 s81, 4096\n"
    "s_cselect_b32 s81, 0, s81\n"
    "v_smfmac_f32_16x16x64_bf16 v[112:115], v[104:107], v[76:83], v253\n"
    "v_mfma_f32_16x16x32_bf16 a[84:87], v[200:203], v[80:83], a[84:87]\n"
    "v_exp_f32 v12, v12\n"
    "v_mfma_f32_16x16x32_bf16 a[84:87], v[196:199], v[76:79], a[84:87]\n"
    "v_exp_f32 v13, v13\n"
    "v_mfma_f32_16x16x32_bf16 a[68:71], v[188:191], v[76:79], a[68:71]\n"
    "v_exp_f32 v14, v14\n"
    "v_mfma_f32_16x16x32_bf16 a[68:71], v[192:195], v[80:83], a[68:71]\n"
    "v_exp_f32 v15, v15\n"
    "v_mfma_f32_16x16x32_bf16 a[52:55], v[184:187], v[80:83], a[52:55]\n"
    "v_or3_b32 v243, v222, v223, v243\n"
    "v_and_b32 v208, 0xff00ff00, v243\n"
    "v_mfma_f32_16x16x32_bf16 a[52:55], v[180:183], v[76:79], a[52:55]\n"
    "v_exp_f32 v16, v16\n"
    "v_mfma_f32_16x16x32_bf16 a[56:59], v[180:183], v[84:87], a[56:59]\n"
    "v_exp_f32 v17, v17\n"
    "v_mfma_f32_16x16x32_bf16 a[56:59], v[184:187], v[88:91], a[56:59]\n"
    "v_mfma_f32_16x16x32_bf16 a[72:75], v[192:195], v[88:91], a[72:75]\n"
    "v_mfma_f32_16x16x32_bf16 a[72:75], v[188:191], v[84:87], a[72:75]\n"
    "v_exp_f32 v18, v18\n"
    "v_mfma_f32_16x16x32_bf16 a[88:91], v[196:199], v[84:87], a[88:91]\n"
    "v_cmp_ne_u32 vcc, 0, v208\n"
    "v_mfma_f32_16x16x32_bf16 a[88:91], v[200:203], v[88:91], a[88:91]\n"
    "v_smfmac_f32_16x16x64_bf16 v[116:119], v[104:107], v[84:91], v253\n"
    "v_smfmac_f32_16x16x64_bf16 v[120:123], v[104:107], v[92:99], v253\n"
    "v_mfma_f32_16x16x32_bf16 a[92:95], v[200:203], v[96:99], a[92:95]\n"
    "v_exp_f32 v19, v19\n"
    "v_mfma_f32_16x16x32_bf16 a[92:95], v[196:199], v[92:95], a[92:95]\n"
    "v_exp_f32 v20, v20\n"
    "v_mfma_f32_16x16x32_bf16 a[76:79], v[188:191], v[92:95], a[76:79]\n"
    "v_cndmask_b32 v208, 2.0, v241, vcc\n"
    "v_mfma_f32_16x16x32_bf16 a[76:79], v[192:195], v[96:99], a[76:79]\n"
    "v_exp_f32 v21, v21\n"
    "v_mfma_f32_16x16x32_bf16 a[60:63], v[184:187], v[96:99], a[60:63]\n"
    "v_mfma_f32_16x16x32_bf16 a[60:63], v[180:183], v[92:95], a[60:63]\n"
    "s_waitcnt lgkmcnt(0)\n"
    "v_mfma_f32_16x16x32_bf16 a[96:99], v[156:159], v[68:71], a[96:99]\n"
    "ds_read_b128 v[180:183], v249 offset:9216\n"
    "v_mfma_f32_16x16x32_bf16 a[96:99], v[160:163], v[72:75], a[96:99]\n"
    "ds_read_b128 v[184:187], v249 offset:21504\n"
    "v_mfma_f32_16x16x32_bf16 a[112:115], v[168:171], v[72:75], a[112:115]\n"
    "ds_read_b128 v[188:191], v249 offset:10240\n"
    "v_mfma_f32_16x16x32_bf16 a[112:115], v[164:167], v[68:71], a[112:115]\n"
    "ds_read_b128 v[192:195], v249 offset:22528\n"
    "v_mfma_f32_16x16x32_bf16 a[128:131], v[172:175], v[68:71], a[128:131]\n"
    "buffer_load_dwordx2 v[236:237], v241, s[36:39], s81 offen offset:0 sc1\n"
    "v_mfma_f32_16x16x32_bf16 a[128:131], v[176:179], v[72:75], a[128:131]\n"
    "ds_read_b128 v[196:199], v249 offset:11264\n"
    "ds_read_b128 v[200:203], v249 offset:23552\n"
    "v_mfma_f32_16x16x32_bf16 a[132:135], v[176:179], v[80:83], a[132:135]\n"
    "buffer_store_dword v204, v244, s[16:19], s76 offen offset:0 sc1\n"
    "v_mfma_f32_16x16x32_bf16 a[132:135], v[172:175], v[76:79], a[132:135]\n"
    "v_exp_f32 v22, v22\n"
    "v_mfma_f32_16x16x32_bf16 a[116:119], v[164:167], v[76:79], a[116:119]\n"
    "buffer_store_dword v205, v244, s[16:19], s76 offen offset:1024 sc1\n"
    "v_mfma_f32_16x16x32_bf16 a[116:119], v[168:171], v[80:83], a[116:119]\n"
    "v_exp_f32 v23, v23\n"
    "v_mfma_f32_16x16x32_bf16 a[100:103], v[160:163], v[80:83], a[100:103]\n"
    "buffer_store_dword v206, v244, s[16:19], s76 offen offset:2048 sc1\n"
    "v_mfma_f32_16x16x32_bf16 a[100:103], v[156:159], v[76:79], a[100:103]\n"
    "v_exp_f32 v24, v24\n"
    "v_mfma_f32_16x16x32_bf16 a[104:107], v[156:159], v[84:87], a[104:107]\n"
    "buffer_store_dword v207, v244, s[16:19], s76 offen offset:3072 sc1\n"
    "v_mfma_f32_16x16x32_bf16 a[104:107], v[160:163], v[88:91], a[104:107]\n"
    "v_exp_f32 v25, v25\n"
    "v_mfma_f32_16x16x32_bf16 a[120:123], v[168:171], v[88:91], a[120:123]\n"
    "buffer_store_dword v212, v244, s[16:19], s77 offen offset:0 sc1\n"
    "v_mfma_f32_16x16x32_bf16 a[120:123], v[164:167], v[84:87], a[120:123]\n"
    "v_exp_f32 v26, v26\n"
    "v_mfma_f32_16x16x32_bf16 a[136:139], v[172:175], v[84:87], a[136:139]\n"
    "buffer_store_dword v213, v244, s[16:19], s77 offen offset:1024 sc1\n"
    "v_mfma_f32_16x16x32_bf16 a[136:139], v[176:179], v[88:91], a[136:139]\n"
    "v_exp_f32 v27, v27\n"
    "v_mfma_f32_16x16x32_bf16 a[140:143], v[176:179], v[96:99], a[140:143]\n"
    "buffer_store_dword v214, v244, s[16:19], s77 offen offset:2048 sc1\n"
    "v_mfma_f32_16x16x32_bf16 a[140:143], v[172:175], v[92:95], a[140:143]\n"
    "v_exp_f32 v28, v28\n"
    "v_mfma_f32_16x16x32_bf16 a[124:127], v[164:167], v[92:95], a[124:127]\n"
    "buffer_store_dword v215, v244, s[16:19], s77 offen offset:3072 sc1\n"
    "v_mfma_f32_16x16x32_bf16 a[124:127], v[168:171], v[96:99], a[124:127]\n"
    "v_exp_f32 v29, v29\n"
    "v_mfma_f32_16x16x32_bf16 a[108:111], v[160:163], v[96:99], a[108:111]\n"
    "buffer_store_dword v220, v244, s[16:19], s78 offen offset:0 sc1\n"
    "v_mfma_f32_16x16x32_bf16 a[108:111], v[156:159], v[92:95], a[108:111]\n"
    "s_waitcnt lgkmcnt(0)\n"
    "v_exp_f32 v30, v30\n"
    "v_mfma_f32_16x16x32_bf16 a[144:147], v[180:183], v[68:71], a[144:147]\n"
    "ds_read_b128 v[156:159], v3 offset:12288\n"
    "v_mfma_f32_16x16x32_bf16 a[144:147], v[184:187], v[72:75], a[144:147]\n"
    "ds_read_b128 v[160:163], v3 offset:14336\n"
    "v_mfma_f32_16x16x32_bf16 a[160:163], v[192:195], v[72:75], a[160:163]\n"
    "ds_read_b128 v[164:167], v3 offset:16384\n"
    "v_mfma_f32_16x16x32_bf16 a[160:163], v[188:191], v[68:71], a[160:163]\n"
    "ds_read_b128 v[168:171], v3 offset:18432\n"
    "v_mfma_f32_16x16x32_bf16 a[176:179], v[196:199], v[68:71], a[176:179]\n"
    "ds_read_b128 v[172:175], v3 offset:20480\n"
    "v_mfma_f32_16x16x32_bf16 a[176:179], v[200:203], v[72:75], a[176:179]\n"
    "ds_read_b128 v[176:179], v3 offset:22528\n"
    "v_mfma_f32_16x16x32_bf16 a[180:183], v[200:203], v[80:83], a[180:183]\n"
    "ds_read_b128 v[100:103], v250 offset:1664\n"
    "v_mfma_f32_16x16x32_bf16 a[180:183], v[196:199], v[76:79], a[180:183]\n"
    "buffer_store_dword v221, v244, s[16:19], s78 offen offset:1024 sc1\n"
    "v_mfma_f32_16x16x32_bf16 a[164:167], v[188:191], v[76:79], a[164:167]\n"
    "v_exp_f32 v31, v31\n"
    "v_mfma_f32_16x16x32_bf16 a[164:167], v[192:195], v[80:83], a[164:167]\n"
    "buffer_store_dword v222, v244, s[16:19], s78 offen offset:2048 sc1\n"
    "v_mfma_f32_16x16x32_bf16 a[148:151], v[184:187], v[80:83], a[148:151]\n"
    "v_exp_f32 v32, v32\n"
    "v_mfma_f32_16x16x32_bf16 a[148:151], v[180:183], v[76:79], a[148:151]\n"
    "buffer_store_dword v223, v244, s[16:19], s78 offen offset:3072 sc1\n"
    "v_mfma_f32_16x16x32_bf16 a[152:155], v[180:183], v[84:87], a[152:155]\n"
    "v_exp_f32 v33, v33\n"
    "v_mfma_f32_16x16x32_bf16 a[152:155], v[184:187], v[88:91], a[152:155]\n"
    "buffer_store_dwordx2 v[238:239], v208, s[36:39], 0 offen offset:0 sc1\n"
    "v_mfma_f32_16x16x32_bf16 a[168:171], v[192:195], v[88:91], a[168:171]\n"
    "s_waitcnt vmcnt(14)\n"
    "ds_add_u32 v247, v246\n"
    "v_mfma_f32_16x16x32_bf16 a[168:171], v[188:191], v[84:87], a[168:171]\n"
    "v_exp_f32 v34, v34\n"
    "v_mfma_f32_16x16x32_bf16 a[184:187], v[196:199], v[84:87], a[184:187]\n"
    "v_exp_f32 v35, v35\n"
    "v_mfma_f32_16x16x32_bf16 a[184:187], v[200:203], v[88:91], a[184:187]\n"
    "v_mfma_f32_16x16x32_bf16 a[188:191], v[200:203], v[96:99], a[188:191]\n"
    "v_mfma_f32_16x16x32_bf16 a[188:191], v[196:199], v[92:95], a[188:191]\n"
    "v_mfma_f32_16x16x32_bf16 a[172:175], v[188:191], v[92:95], a[172:175]\n"
    "s_sub_u32 s74, s68, s65\n"
    "v_add_u32 v250, 1536, v250\n"
    "v_add_u32 v251, 1536, v251\n"
    "v_mfma_f32_16x16x32_bf16 a[172:175], v[192:195], v[96:99], a[172:175]\n"
    "s_add_u32 s66, s66, 1\n"
    "v_mov_b32 v2, v3\n"
    "v_add_u32 v3, s74, v252\n"
    "v_mfma_f32_16x16x32_bf16 a[156:159], v[184:187], v[96:99], a[156:159]\n"
    "s_waitcnt lgkmcnt(1)\n"
    "s_cmp_lt_u32 s66, 31\n"
    "v_mfma_f32_16x16x32_bf16 a[156:159], v[180:183], v[92:95], a[156:159]\n"
    "s_cbranch_scc1 .LPV\n"
    "s_branch .Ltail\n"
    ".LtailP:\n"
    "v_mov_b32 v248, v249\n"
    "v_add_u32 v250, 768, v250\n"
    "v_add_u32 v251, 768, v251\n"
    ".Ltail:\n"
    "v_mfma_f32_16x16x32_bf16 v[36:39], v[156:159], a[192:195], v[100:103]\n"
    "s_mul_i32 s75, s84, 24576\n"
    "ds_read_b128 v[180:183], v2 offset:13312\n"
    "ds_read_b128 v[104:107], v250 offset:144\n"
    "v_mfma_f32_16x16x32_bf16 v[36:39], v[160:163], a[196:199], v[36:39]\n"
    "s_add_u32 s76, s75, 6144\n"
    "ds_read_b128 v[184:187], v2 offset:15360\n"
    "ds_read_b128 v[188:191], v2 offset:17408\n"
    "v_mfma_f32_16x16x32_bf16 v[36:39], v[164:167], a[200:203], v[36:39]\n"
    "s_add_u32 s77, s76, 6144\n"
    "ds_read_b128 v[192:195], v2 offset:19456\n"
    "ds_read_b128 v[196:199], v2 offset:21504\n"
    "v_mfma_f32_16x16x32_bf16 v[36:39], v[168:171], a[204:207], v[36:39]\n"
    "s_add_u32 s78, s77, 6144\n"
    "ds_read_b128 v[200:203], v2 offset:23552\n"
    "v_cvt_pk_bf16_f32 v68, v4, v5\n"
    "v_mfma_f32_16x16x32_bf16 v[36:39], v[172:175], a[208:211], v[36:39]\n"
    "s_add_u32 s72, s65, 122880\n"
    "v_cvt_pk_bf16_f32 v69, v6, v7\n"
    "v_cvt_pk_bf16_f32 v84, v12, v13\n"
    "v_mfma_f32_16x16x32_bf16 v[36:39], v[176:179], a[212:215], v[36:39]\n"
    "s_mov_b32 s79, 133\n"
    "v_cvt_pk_bf16_f32 v85, v14, v15\n"
    "v_cvt_pk_bf16_f32 v70, v20, v21\n"
    "v_mfma_f32_16x16x32_bf16 v[40:43], v[176:179], a[236:239], v[100:103]\n"
    "s_mov_b32 s74, 0x47000000\n"
    "v_cvt_pk_bf16_f32 v71, v22, v23\n"
    "v_cvt_pk_bf16_f32 v86, v28, v29\n"
    "v_mfma_f32_16x16x32_bf16 v[40:43], v[172:175], a[232:235], v[40:43]\n"
    "s_mov_b32 s73, 0x7f800000\n"
    "v_cvt_pk_bf16_f32 v87, v30, v31\n"
    "ds_read_b128 v[244:247], v251 offset:0\n"
    "v_mfma_f32_16x16x32_bf16 v[40:43], v[168:171], a[228:231], v[40:43]\n"
    "s_waitcnt lgkmcnt(5)\n"
    "v_cvt_pk_bf16_f32 v76, v8, v9\n"
    "v_cvt_pk_bf16_f32 v77, v10, v11\n"
    "v_mfma_f32_16x16x32_bf16 v[40:43], v[164:167], a[224:227], v[40:43]\n"
    "s_waitcnt lgkmcnt(3)\n"
    "v_cvt_pk_bf16_f32 v92, v16, v17\n"
    "v_cvt_pk_bf16_f32 v93, v18, v19\n"
    "v_mfma_f32_16x16x32_bf16 v[40:43], v[160:163], a[220:223], v[40:43]\n"
    "s_waitcnt lgkmcnt(1)\n"
    "v_cvt_pk_bf16_f32 v78, v24, v25\n"
    "v_cvt_pk_bf16_f32 v79, v26, v27\n"
    "v_mfma_f32_16x16x32_bf16 v[40:43], v[156:159], a[216:219], v[40:43]\n"
    "v_cvt_pk_bf16_f32 v94, v32, v33\n"
    "v_cvt_pk_bf16_f32 v95, v34, v35\n"
    "v_mfma_f32_16x16x32_bf16 v[52:55], v[180:183], a[192:195], v[104:107]\n"
    "v_exp_f32 v36, v36\n"
    "v_mfma_f32_16x16x32_bf16 v[52:55], v[184:187], a[196:199], v[52:55]\n"
    "v_exp_f32 v37, v37\n"
    "v_mfma_f32_16x16x32_bf16 v[52:55], v[188:191], a[200:203], v[52:55]\n"
    "v_exp_f32 v38, v38\n"
    "v_mfma_f32_16x16x32_bf16 v[52:55], v[192:195], a[204:207], v[52:55]\n"
    "s_waitcnt lgkmcnt(0)\n"
    "v_exp_f32 v39, v39\n"
    "v_mfma_f32_16x16x32_bf16 v[52:55], v[196:199], a[208:211], v[52:55]\n"
    "v_cvt_pk_bf16_f32 v72, v36, v37\n"
    "v_cvt_pk_bf16_f32 v73, v38, v39\n"
    "v_mfma_f32_16x16x32_bf16 v[52:55], v[200:203], a[212:215], v[52:55]\n"
    "s_barrier\n"
    "ds_read_b128 v[4:7], v248 offset:0\n"
    "ds_read_b128 v[12:15], v248 offset:1024\n"
    "v_mfma_f32_16x16x32_bf16 v[56:59], v[200:203], a[236:239], v[104:107]\n"
    "ds_read_b128 v[20:23], v248 offset:2048\n"
    "ds_read_b128 v[28:31], v248 offset:3072\n"
    "v_mfma_f32_16x16x32_bf16 v[56:59], v[196:199], a[232:235], v[56:59]\n"
    "ds_read_b128 v[8:11], v248 offset:12288\n"
    "ds_read_b128 v[16:19], v248 offset:13312\n"
    "v_mfma_f32_16x16x32_bf16 v[56:59], v[192:195], a[228:231], v[56:59]\n"
    "ds_read_b128 v[24:27], v248 offset:14336\n"
    "ds_read_b128 v[32:35], v248 offset:15360\n"
    "v_mfma_f32_16x16x32_bf16 v[56:59], v[188:191], a[224:227], v[56:59]\n"
    "s_waitcnt vmcnt(0)\n"
    "v_exp_f32 v52, v52\n"
    "v_mfma_f32_16x16x32_bf16 v[56:59], v[184:187], a[220:223], v[56:59]\n"
    "v_exp_f32 v53, v53\n"
    "v_mfma_f32_16x16x32_bf16 v[56:59], v[180:183], a[216:219], v[56:59]\n"
    "v_exp_f32 v54, v54\n"
    "v_exp_f32 v55, v55\n"
    "v_cvt_pk_bf16_f32 v74, v52, v53\n"
    "v_cvt_pk_bf16_f32 v75, v54, v55\n"
    "s_waitcnt lgkmcnt(7)\n"
    "v_mfma_f32_16x16x32_bf16 a[0:3], v[4:7], v[68:71], a[0:3]\n"
    "s_waitcnt lgkmcnt(4)\n"
    "v_exp_f32 v40, v40\n"
    "v_mfma_f32_16x16x32_bf16 a[16:19], v[12:15], v[68:71], a[16:19]\n"
    "s_waitcnt lgkmcnt(2)\n"
    "v_exp_f32 v41, v41\n"
    "v_mfma_f32_16x16x32_bf16 a[32:35], v[20:23], v[68:71], a[32:35]\n"
    "s_waitcnt lgkmcnt(0)\n"
    "v_exp_f32 v42, v42\n"
    "v_mfma_f32_16x16x32_bf16 a[48:51], v[28:31], v[68:71], a[48:51]\n"
    "v_exp_f32 v43, v43\n"
    "v_smfmac_f32_16x16x64_bf16 v[108:111], v[244:247], v[68:75], v253\n"
    "v_exp_f32 v56, v56\n"
    "v_mfma_f32_16x16x32_bf16 a[0:3], v[8:11], v[72:75], a[0:3]\n"
    "v_exp_f32 v57, v57\n"
    "v_mfma_f32_16x16x32_bf16 a[16:19], v[16:19], v[72:75], a[16:19]\n"
    "v_exp_f32 v58, v58\n"
    "v_mfma_f32_16x16x32_bf16 a[32:35], v[24:27], v[72:75], a[32:35]\n"
    "v_exp_f32 v59, v59\n"
    "v_mfma_f32_16x16x32_bf16 a[48:51], v[32:35], v[72:75], a[48:51]\n"
    "v_cvt_pk_bf16_f32 v80, v40, v41\n"
    "v_cvt_pk_bf16_f32 v81, v42, v43\n"
    "v_cvt_pk_bf16_f32 v82, v56, v57\n"
    "v_cvt_pk_bf16_f32 v83, v58, v59\n"
    "v_add_f32 v108, v108, v109\n"
    "v_mov_b32 v109, v108\n"
    "v_smfmac_f32_16x16x64_bf16 v[112:115], v[244:247], v[76:83], v253\n"
    "v_mov_b32 v111, v110\n"
    "v_mfma_f32_16x16x32_bf16 v[44:47], v[156:159], a[240:243], v[100:103]\n"
    "v_permlane16_swap_b32 v108, v109\n"
    "v_add_f32 v108, v108, v109\n"
    "v_mfma_f32_16x16x32_bf16 v[44:47], v[160:163], a[244:247], v[44:47]\n"
    "v_rcp_f32 v109, v108\n"
    "v_mfma_f32_16x16x32_bf16 v[44:47], v[164:167], a[248:251], v[44:47]\n"
    "v_lshrrev_b32 v204, 4, v1\n"
    "v_permlane16_swap_b32 v110, v111\n"
    "v_mfma_f32_16x16x32_bf16 v[44:47], v[168:171], a[252:255], v[44:47]\n"
    "v_and_b32 v204, 48, v204\n"
    "v_and_b32 v207, 0x70, v1\n"
    "v_mfma_f32_16x16x32_bf16 v[44:47], v[172:175], v[124:127], v[44:47]\n"
    "v_and_b32 v206, 0xf0, v1\n"
    "v_add_f32 v110, v110, v111\n"
    "v_mfma_f32_16x16x32_bf16 v[44:47], v[176:179], v[128:131], v[44:47]\n"
    "v_xor_b32 v204, v204, v207\n"
    "v_lshl_add_u32 v206, v206, 3, s72\n"
    "v_mfma_f32_16x16x32_bf16 a[52:55], v[28:31], v[76:79], a[52:55]\n"
    "v_mul_f32 v111, -v110, v109\n"
    "v_add_u32 v206, v206, v204\n"
    "v_mfma_f32_16x16x32_bf16 a[52:55], v[32:35], v[80:83], a[52:55]\n"
    "v_ldexp_f32 v109, v109, s79\n"
    "v_accvgpr_read_b32 v216, a0\n"
    "v_mfma_f32_16x16x32_bf16 a[36:39], v[24:27], v[80:83], a[36:39]\n"
    "v_accvgpr_read_b32 v217, a16\n"
    "v_accvgpr_read_b32 v224, a2\n"
    "v_mfma_f32_16x16x32_bf16 a[36:39], v[20:23], v[76:79], a[36:39]\n"
    "v_accvgpr_read_b32 v225, a18\n"
    "v_lshrrev_b32 v204, 3, v1\n"
    "v_mfma_f32_16x16x32_bf16 a[20:23], v[12:15], v[76:79], a[20:23]\n"
    "v_accvgpr_read_b32 v218, a32\n"
    "v_accvgpr_read_b32 v219, a48\n"
    "v_mfma_f32_16x16x32_bf16 a[20:23], v[16:19], v[80:83], a[20:23]\n"
    "v_accvgpr_read_b32 v226, a34\n"
    "v_accvgpr_read_b32 v227, a50\n"
    "v_mfma_f32_16x16x32_bf16 a[4:7], v[8:11], v[80:83], a[4:7]\n"
    "v_and_b32 v204, 0x70, v204\n"
    "v_accvgpr_read_b32 v220, a1\n"
    "v_mfma_f32_16x16x32_bf16 a[4:7], v[4:7], v[76:79], a[4:7]\n"
    "v_accvgpr_read_b32 v221, a17\n"
    "v_accvgpr_read_b32 v222, a33\n"
    "v_mfma_f32_16x16x32_bf16 v[60:63], v[180:183], a[240:243], v[104:107]\n"
    "v_accvgpr_read_b32 v223, a49\n"
    "v_accvgpr_read_b32 v228, a3\n"
    "v_mfma_f32_16x16x32_bf16 v[60:63], v[184:187], a[244:247], v[60:63]\n"
    "v_accvgpr_read_b32 v229, a19\n"
    "v_accvgpr_read_b32 v230, a35\n"
    "v_mfma_f32_16x16x32_bf16 v[60:63], v[188:191], a[248:251], v[60:63]\n"
    "v_accvgpr_read_b32 v231, a51\n"
    "v_mad_u32_u24 v205, v204, 24, v207\n"
    "v_mfma_f32_16x16x32_bf16 v[60:63], v[192:195], a[252:255], v[60:63]\n"
    "v_xor_b32 v207, 64, v206\n"
    "v_xor_b32 v208, v204, v1\n"
    "v_add_u32 v208, s72, v208\n"
    "v_add_f32 v112, v112, v113\n"
    "v_mov_b32 v113, v112\n"
    "v_pk_fma_f32 v[216:217], v[216:217], v[108:109], v[110:111] op_sel:[0,1,1] op_sel_hi:[1,1,1]\n"
    "v_pk_fma_f32 v[224:225], v[224:225], v[108:109], v[110:111] op_sel:[0,1,1] op_sel_hi:[1,1,1]\n"
    "v_pk_fma_f32 v[218:219], v[218:219], v[108:109], v[110:111] op_sel:[0,1,1] op_sel_hi:[1,1,1]\n"
    "v_cvt_pk_bf16_f32 v216, v216, v217\n"
    "v_pk_fma_f32 v[226:227], v[226:227], v[108:109], v[110:111] op_sel:[0,1,1] op_sel_hi:[1,1,1]\n"
    "v_cvt_pk_bf16_f32 v224, v224, v225\n"
    "v_cvt_pk_bf16_f32 v217, v218, v219\n"
    "v_pk_fma_f32 v[220:221], v[220:221], v[108:109], v[110:111] op_sel:[0,1,1] op_sel_hi:[1,1,1]\n"
    "v_pk_fma_f32 v[222:223], v[222:223], v[108:109], v[110:111] op_sel:[0,1,1] op_sel_hi:[1,1,1]\n"
    "v_cvt_pk_bf16_f32 v225, v226, v227\n"
    "v_pk_fma_f32 v[228:229], v[228:229], v[108:109], v[110:111] op_sel:[0,1,1] op_sel_hi:[1,1,1]\n"
    "v_pk_fma_f32 v[230:231], v[230:231], v[108:109], v[110:111] op_sel:[0,1,1] op_sel_hi:[1,1,1]\n"
    "v_cvt_pk_bf16_f32 v218, v220, v221\n"
    "v_cvt_pk_bf16_f32 v219, v222, v223\n"
    "v_cvt_pk_bf16_f32 v226, v228, v229\n"
    "v_cvt_pk_bf16_f32 v227, v230, v231\n"
    "v_mfma_f32_16x16x32_bf16 v[60:63], v[196:199], v[124:127], v[60:63]\n"
    "ds_write_b128 v206, v[216:219] offset:0\n"
    "ds_write_b128 v207, v[224:227] offset:0\n"
    "ds_read_b128 v[220:223], v208 offset:0\n"
    "v_mfma_f32_16x16x32_bf16 v[60:63], v[200:203], v[128:131], v[60:63]\n"
    "ds_read_b128 v[228:231], v208 offset:1024\n"
    "v_mov_b32 v115, v114\n"
    "v_mfma_f32_16x16x32_bf16 v[48:51], v[176:179], v[152:155], v[100:103]\n"
    "v_permlane16_swap_b32 v112, v113\n"
    "v_add_f32 v112, v112, v113\n"
    "v_mfma_f32_16x16x32_bf16 v[48:51], v[172:175], v[148:151], v[48:51]\n"
    "v_rcp_f32 v113, v112\n"
    "v_mfma_f32_16x16x32_bf16 v[48:51], v[168:171], v[144:147], v[48:51]\n"
    "v_permlane16_swap_b32 v114, v115\n"
    "v_add_f32 v114, v114, v115\n"
    "v_mfma_f32_16x16x32_bf16 v[48:51], v[164:167], v[140:143], v[48:51]\n"
    "v_mul_f32 v115, -v114, v113\n"
    "v_ldexp_f32 v113, v113, s79\n"
    "v_mfma_f32_16x16x32_bf16 v[48:51], v[160:163], v[136:139], v[48:51]\n"
    "v_accvgpr_read_b32 v36, a4\n"
    "v_accvgpr_read_b32 v37, a20\n"
    "v_mfma_f32_16x16x32_bf16 v[48:51], v[156:159], v[132:135], v[48:51]\n"
    "v_accvgpr_read_b32 v52, a6\n"
    "v_accvgpr_read_b32 v53, a22\n"
    "v_mfma_f32_16x16x32_bf16 v[64:67], v[200:203], v[152:155], v[104:107]\n"
    "s_waitcnt lgkmcnt(0)\n"
    "buffer_store_dwordx4 v[220:223], v205, s[24:27], s75 offen offset:0 sc1\n"
    "v_mfma_f32_16x16x32_bf16 v[64:67], v[196:199], v[148:151], v[64:67]\n"
    "v_accvgpr_read_b32 v38, a36\n"
    "v_accvgpr_read_b32 v39, a52\n"
    "v_mfma_f32_16x16x32_bf16 v[64:67], v[192:195], v[144:147], v[64:67]\n"
    "v_accvgpr_read_b32 v54, a38\n"
    "v_accvgpr_read_b32 v55, a54\n"
    "v_mfma_f32_16x16x32_bf16 v[64:67], v[188:191], v[140:143], v[64:67]\n"
    "v_accvgpr_read_b32 v40, a5\n"
    "v_accvgpr_read_b32 v41, a21\n"
    "v_accvgpr_read_b32 v42, a37\n"
    "v_accvgpr_read_b32 v43, a53\n"
    "v_accvgpr_read_b32 v56, a7\n"
    "v_pk_fma_f32 v[36:37], v[36:37], v[112:113], v[114:115] op_sel:[0,1,1] op_sel_hi:[1,1,1]\n"
    "v_pk_fma_f32 v[52:53], v[52:53], v[112:113], v[114:115] op_sel:[0,1,1] op_sel_hi:[1,1,1]\n"
    "v_pk_fma_f32 v[38:39], v[38:39], v[112:113], v[114:115] op_sel:[0,1,1] op_sel_hi:[1,1,1]\n"
    "v_cvt_pk_bf16_f32 v36, v36, v37\n"
    "v_pk_fma_f32 v[54:55], v[54:55], v[112:113], v[114:115] op_sel:[0,1,1] op_sel_hi:[1,1,1]\n"
    "v_cvt_pk_bf16_f32 v52, v52, v53\n"
    "v_accvgpr_read_b32 v57, a23\n"
    "v_accvgpr_read_b32 v58, a39\n"
    "v_accvgpr_read_b32 v59, a55\n"
    "v_mfma_f32_16x16x32_bf16 v[64:67], v[184:187], v[136:139], v[64:67]\n"
    "v_cvt_pk_bf16_f32 v37, v38, v39\n"
    "v_cvt_pk_bf16_f32 v53, v54, v55\n"
    "v_exp_f32 v44, v44\n"
    "v_exp_f32 v45, v45\n"
    "v_pk_fma_f32 v[40:41], v[40:41], v[112:113], v[114:115] op_sel:[0,1,1] op_sel_hi:[1,1,1]\n"
    "v_pk_fma_f32 v[42:43], v[42:43], v[112:113], v[114:115] op_sel:[0,1,1] op_sel_hi:[1,1,1]\n"
    "v_pk_fma_f32 v[56:57], v[56:57], v[112:113], v[114:115] op_sel:[0,1,1] op_sel_hi:[1,1,1]\n"
    "v_pk_fma_f32 v[58:59], v[58:59], v[112:113], v[114:115] op_sel:[0,1,1] op_sel_hi:[1,1,1]\n"
    "v_cvt_pk_bf16_f32 v38, v40, v41\n"
    "v_cvt_pk_bf16_f32 v39, v42, v43\n"
    "v_cvt_pk_bf16_f32 v54, v56, v57\n"
    "v_cvt_pk_bf16_f32 v55, v58, v59\n"
    "v_exp_f32 v46, v46\n"
    "v_exp_f32 v47, v47\n"
    "v_exp_f32 v60, v60\n"
    "v_exp_f32 v61, v61\n"
    "v_exp_f32 v62, v62\n"
    "v_exp_f32 v63, v63\n"
    "ds_write_b128 v206, v[36:39] offset:2048\n"
    "ds_write_b128 v207, v[52:55] offset:2048\n"
    "v_mfma_f32_16x16x32_bf16 v[64:67], v[180:183], v[132:135], v[64:67]\n"
    "ds_read_b128 v[40:43], v208 offset:2048\n"
    "v_cvt_pk_bf16_f32 v88, v44, v45\n"
    "v_cvt_pk_bf16_f32 v89, v46, v47\n"
    "v_cvt_pk_bf16_f32 v90, v60, v61\n"
    "v_cvt_pk_bf16_f32 v91, v62, v63\n"
    "buffer_store_dwordx4 v[228:231], v205, s[24:27], s75 offen offset:3072 sc1\n"
    "ds_read_b128 v[56:59], v208 offset:3072\n"
    "v_smfmac_f32_16x16x64_bf16 v[116:119], v[244:247], v[84:91], v253\n"
    "v_exp_f32 v48, v48\n"
    "v_mfma_f32_16x16x32_bf16 a[8:11], v[4:7], v[84:87], a[8:11]\n"
    "v_exp_f32 v49, v49\n"
    "v_mfma_f32_16x16x32_bf16 a[8:11], v[8:11], v[88:91], a[8:11]\n"
    "v_exp_f32 v50, v50\n"
    "v_mfma_f32_16x16x32_bf16 a[24:27], v[16:19], v[88:91], a[24:27]\n"
    "v_exp_f32 v51, v51\n"
    "v_mfma_f32_16x16x32_bf16 a[24:27], v[12:15], v[84:87], a[24:27]\n"
    "s_waitcnt lgkmcnt(1)\n"
    "buffer_store_dwordx4 v[40:43], v205, s[24:27], s76 offen offset:0 sc1\n"
    "v_mfma_f32_16x16x32_bf16 a[40:43], v[20:23], v[84:87], a[40:43]\n"
    "v_add_f32 v116, v116, v117\n"
    "v_mov_b32 v117, v116\n"
    "v_mfma_f32_16x16x32_bf16 a[40:43], v[24:27], v[88:91], a[40:43]\n"
    "s_waitcnt lgkmcnt(0)\n"
    "v_mov_b32 v119, v118\n"
    "v_permlane16_swap_b32 v116, v117\n"
    "v_mfma_f32_16x16x32_bf16 a[56:59], v[32:35], v[88:91], a[56:59]\n"
    "v_add_f32 v116, v116, v117\n"
    "v_rcp_f32 v117, v116\n"
    "v_mfma_f32_16x16x32_bf16 a[56:59], v[28:31], v[84:87], a[56:59]\n"
    "v_permlane16_swap_b32 v118, v119\n"
    "v_exp_f32 v64, v64\n"
    "v_exp_f32 v65, v65\n"
    "v_exp_f32 v66, v66\n"
    "v_exp_f32 v67, v67\n"
    "v_add_f32 v118, v118, v119\n"
    "v_mul_f32 v119, -v118, v117\n"
    "v_ldexp_f32 v117, v117, s79\n"
    "v_accvgpr_read_b32 v44, a8\n"
    "v_accvgpr_read_b32 v45, a24\n"
    "v_accvgpr_read_b32 v60, a10\n"
    "v_accvgpr_read_b32 v61, a26\n"
    "v_cvt_pk_bf16_f32 v96, v48, v49\n"
    "v_cvt_pk_bf16_f32 v97, v50, v51\n"
    "v_cvt_pk_bf16_f32 v98, v64, v65\n"
    "v_cvt_pk_bf16_f32 v99, v66, v67\n"
    "v_accvgpr_read_b32 v46, a40\n"
    "v_accvgpr_read_b32 v47, a56\n"
    "v_pk_fma_f32 v[44:45], v[44:45], v[116:117], v[118:119] op_sel:[0,1,1] op_sel_hi:[1,1,1]\n"
    "v_accvgpr_read_b32 v62, a42\n"
    "v_accvgpr_read_b32 v63, a58\n"
    "v_pk_fma_f32 v[60:61], v[60:61], v[116:117], v[118:119] op_sel:[0,1,1] op_sel_hi:[1,1,1]\n"
    "v_pk_fma_f32 v[46:47], v[46:47], v[116:117], v[118:119] op_sel:[0,1,1] op_sel_hi:[1,1,1]\n"
    "v_cvt_pk_bf16_f32 v44, v44, v45\n"
    "v_accvgpr_read_b32 v48, a9\n"
    "v_accvgpr_read_b32 v49, a25\n"
    "v_accvgpr_read_b32 v50, a41\n"
    "v_accvgpr_read_b32 v51, a57\n"
    "v_pk_fma_f32 v[62:63], v[62:63], v[116:117], v[118:119] op_sel:[0,1,1] op_sel_hi:[1,1,1]\n"
    "v_cvt_pk_bf16_f32 v60, v60, v61\n"
    "v_accvgpr_read_b32 v64, a11\n"
    "v_accvgpr_read_b32 v65, a27\n"
    "v_accvgpr_read_b32 v66, a43\n"
    "v_accvgpr_read_b32 v67, a59\n"
    "v_cvt_pk_bf16_f32 v45, v46, v47\n"
    "v_pk_fma_f32 v[48:49], v[48:49], v[116:117], v[118:119] op_sel:[0,1,1] op_sel_hi:[1,1,1]\n"
    "v_pk_fma_f32 v[50:51], v[50:51], v[116:117], v[118:119] op_sel:[0,1,1] op_sel_hi:[1,1,1]\n"
    "v_cvt_pk_bf16_f32 v61, v62, v63\n"
    "v_pk_fma_f32 v[64:65], v[64:65], v[116:117], v[118:119] op_sel:[0,1,1] op_sel_hi:[1,1,1]\n"
    "v_pk_fma_f32 v[66:67], v[66:67], v[116:117], v[118:119] op_sel:[0,1,1] op_sel_hi:[1,1,1]\n"
    "v_cvt_pk_bf16_f32 v46, v48, v49\n"
    "v_cvt_pk_bf16_f32 v47, v50, v51\n"
    "v_cvt_pk_bf16_f32 v62, v64, v65\n"
    "v_cvt_pk_bf16_f32 v63, v66, v67\n"
    "ds_write_b128 v206, v[44:47] offset:4096\n"
    "ds_write_b128 v207, v[60:63] offset:4096\n"
    "ds_read_b128 v[48:51], v208 offset:4096\n"
    "v_smfmac_f32_16x16x64_bf16 v[120:123], v[244:247], v[92:99], v253\n"
    "buffer_store_dwordx4 v[56:59], v205, s[24:27], s76 offen offset:3072 sc1\n"
    "v_mfma_f32_16x16x32_bf16 a[60:63], v[28:31], v[92:95], a[60:63]\n"
    "ds_read_b128 v[64:67], v208 offset:5120\n"
    "ds_read_b128 v[156:159], v248 offset:4096\n"
    "v_mfma_f32_16x16x32_bf16 a[60:63], v[32:35], v[96:99], a[60:63]\n"
    "ds_read_b128 v[160:163], v248 offset:16384\n"
    "ds_read_b128 v[168:171], v248 offset:17408\n"
    "v_mfma_f32_16x16x32_bf16 a[44:47], v[24:27], v[96:99], a[44:47]\n"
    "v_add_f32 v120, v120, v121\n"
    "v_mov_b32 v121, v120\n"
    "v_mfma_f32_16x16x32_bf16 a[44:47], v[20:23], v[92:95], a[44:47]\n"
    "v_mov_b32 v123, v122\n"
    "v_permlane16_swap_b32 v120, v121\n"
    "v_mfma_f32_16x16x32_bf16 a[28:31], v[12:15], v[92:95], a[28:31]\n"
    "v_add_f32 v120, v120, v121\n"
    "ds_read_b128 v[164:167], v248 offset:5120\n"
    "v_mfma_f32_16x16x32_bf16 a[28:31], v[16:19], v[96:99], a[28:31]\n"
    "s_waitcnt lgkmcnt(5)\n"
    "buffer_store_dwordx4 v[48:51], v205, s[24:27], s77 offen offset:0 sc1\n"
    "v_mfma_f32_16x16x32_bf16 a[12:15], v[8:11], v[96:99], a[12:15]\n"
    "v_rcp_f32 v121, v120\n"
    "v_mfma_f32_16x16x32_bf16 a[12:15], v[4:7], v[92:95], a[12:15]\n"
    "s_waitcnt lgkmcnt(3)\n"
    "v_permlane16_swap_b32 v122, v123\n"
    "v_add_f32 v122, v122, v123\n"
    "v_mul_f32 v123, -v122, v121\n"
    "ds_read_b128 v[172:175], v248 offset:6144\n"
    "v_ldexp_f32 v121, v121, s79\n"
    "v_accvgpr_read_b32 v217, a28\n"
    "v_accvgpr_read_b32 v225, a30\n"
    "v_accvgpr_read_b32 v216, a12\n"
    "v_accvgpr_read_b32 v224, a14\n"
    "v_accvgpr_read_b32 v218, a44\n"
    "v_accvgpr_read_b32 v219, a60\n"
    "v_pk_fma_f32 v[216:217], v[216:217], v[120:121], v[122:123] op_sel:[0,1,1] op_sel_hi:[1,1,1]\n"
    "v_accvgpr_read_b32 v226, a46\n"
    "v_accvgpr_read_b32 v227, a62\n"
    "v_pk_fma_f32 v[224:225], v[224:225], v[120:121], v[122:123] op_sel:[0,1,1] op_sel_hi:[1,1,1]\n"
    "v_pk_fma_f32 v[218:219], v[218:219], v[120:121], v[122:123] op_sel:[0,1,1] op_sel_hi:[1,1,1]\n"
    "v_cvt_pk_bf16_f32 v216, v216, v217\n"
    "v_accvgpr_read_b32 v220, a13\n"
    "v_accvgpr_read_b32 v221, a29\n"
    "v_accvgpr_read_b32 v222, a45\n"
    "v_accvgpr_read_b32 v223, a61\n"
    "v_pk_fma_f32 v[226:227], v[226:227], v[120:121], v[122:123] op_sel:[0,1,1] op_sel_hi:[1,1,1]\n"
    "v_cvt_pk_bf16_f32 v224, v224, v225\n"
    "v_accvgpr_read_b32 v228, a15\n"
    "v_accvgpr_read_b32 v229, a31\n"
    "v_accvgpr_read_b32 v230, a47\n"
    "v_accvgpr_read_b32 v231, a63\n"
    "v_cvt_pk_bf16_f32 v217, v218, v219\n"
    "v_pk_fma_f32 v[220:221], v[220:221], v[120:121], v[122:123] op_sel:[0,1,1] op_sel_hi:[1,1,1]\n"
    "v_pk_fma_f32 v[222:223], v[222:223], v[120:121], v[122:123] op_sel:[0,1,1] op_sel_hi:[1,1,1]\n"
    "v_cvt_pk_bf16_f32 v225, v226, v227\n"
    "v_pk_fma_f32 v[228:229], v[228:229], v[120:121], v[122:123] op_sel:[0,1,1] op_sel_hi:[1,1,1]\n"
    "v_pk_fma_f32 v[230:231], v[230:231], v[120:121], v[122:123] op_sel:[0,1,1] op_sel_hi:[1,1,1]\n"
    "ds_read_b128 v[176:179], v248 offset:18432\n"
    "v_cvt_pk_bf16_f32 v218, v220, v221\n"
    "v_cvt_pk_bf16_f32 v219, v222, v223\n"
    "v_cvt_pk_bf16_f32 v226, v228, v229\n"
    "v_cvt_pk_bf16_f32 v227, v230, v231\n"
    "ds_write_b128 v206, v[216:219] offset:0\n"
    "ds_write_b128 v207, v[224:227] offset:0\n"
    "ds_read_b128 v[220:223], v208 offset:0\n"
    "ds_read_b128 v[184:187], v248 offset:19456\n"
    "ds_read_b128 v[180:183], v248 offset:7168\n"
    "buffer_store_dwordx4 v[64:67], v205, s[24:27], s77 offen offset:3072 sc1\n"
    "v_mfma_f32_16x16x32_bf16 a[64:67], v[156:159], v[68:71], a[64:67]\n"
    "s_waitcnt lgkmcnt(6)\n"
    "ds_read_b128 v[228:231], v208 offset:1024\n"
    "ds_read_b128 v[188:191], v248 offset:8192\n"
    "v_mfma_f32_16x16x32_bf16 a[64:67], v[160:163], v[72:75], a[64:67]\n"
    "ds_read_b128 v[192:195], v248 offset:20480\n"
    "ds_read_b128 v[200:203], v248 offset:21504\n"
    "v_mfma_f32_16x16x32_bf16 a[80:83], v[168:171], v[72:75], a[80:83]\n"
    "ds_read_b128 v[196:199], v248 offset:9216\n"
    "ds_read_b128 v[124:127], v248 offset:10240\n"
    "v_mfma_f32_16x16x32_bf16 a[80:83], v[164:167], v[68:71], a[80:83]\n"
    "s_waitcnt lgkmcnt(11)\n"
    "ds_read_b128 v[128:131], v248 offset:22528\n"
    "v_accvgpr_read_b32 v36, a64\n"
    "v_mfma_f32_16x16x32_bf16 a[96:99], v[172:175], v[68:71], a[96:99]\n"
    "v_accvgpr_read_b32 v52, a66\n"
    "v_accvgpr_read_b32 v40, a65\n"
    "v_mfma_f32_16x16x32_bf16 a[96:99], v[176:179], v[72:75], a[96:99]\n"
    "s_waitcnt lgkmcnt(7)\n"
    "buffer_store_dwordx4 v[220:223], v205, s[24:27], s78 offen offset:0 sc1\n"
    "v_mfma_f32_16x16x32_bf16 a[112:115], v[184:187], v[72:75], a[112:115]\n"
    "v_accvgpr_read_b32 v37, a80\n"
    "v_accvgpr_read_b32 v53, a82\n"
    "v_mfma_f32_16x16x32_bf16 a[112:115], v[180:183], v[68:71], a[112:115]\n"
    "s_waitcnt lgkmcnt(5)\n"
    "v_accvgpr_read_b32 v41, a81\n"
    "v_accvgpr_read_b32 v38, a96\n"
    "v_accvgpr_read_b32 v54, a98\n"
    "v_accvgpr_read_b32 v42, a97\n"
    "v_accvgpr_read_b32 v56, a67\n"
    "v_pk_fma_f32 v[36:37], v[36:37], v[108:109], v[110:111] op_sel:[0,1,1] op_sel_hi:[1,1,1]\n"
    "v_pk_fma_f32 v[52:53], v[52:53], v[108:109], v[110:111] op_sel:[0,1,1] op_sel_hi:[1,1,1]\n"
    "v_accvgpr_read_b32 v39, a112\n"
    "v_accvgpr_read_b32 v55, a114\n"
    "v_pk_fma_f32 v[38:39], v[38:39], v[108:109], v[110:111] op_sel:[0,1,1] op_sel_hi:[1,1,1]\n"
    "v_cvt_pk_bf16_f32 v36, v36, v37\n"
    "v_accvgpr_read_b32 v43, a113\n"
    "v_pk_fma_f32 v[54:55], v[54:55], v[108:109], v[110:111] op_sel:[0,1,1] op_sel_hi:[1,1,1]\n"
    "v_cvt_pk_bf16_f32 v52, v52, v53\n"
    "v_accvgpr_read_b32 v57, a83\n"
    "v_accvgpr_read_b32 v58, a99\n"
    "v_accvgpr_read_b32 v59, a115\n"
    "v_cvt_pk_bf16_f32 v37, v38, v39\n"
    "v_pk_fma_f32 v[40:41], v[40:41], v[108:109], v[110:111] op_sel:[0,1,1] op_sel_hi:[1,1,1]\n"
    "v_pk_fma_f32 v[42:43], v[42:43], v[108:109], v[110:111] op_sel:[0,1,1] op_sel_hi:[1,1,1]\n"
    "v_cvt_pk_bf16_f32 v53, v54, v55\n"
    "v_pk_fma_f32 v[56:57], v[56:57], v[108:109], v[110:111] op_sel:[0,1,1] op_sel_hi:[1,1,1]\n"
    "v_pk_fma_f32 v[58:59], v[58:59], v[108:109], v[110:111] op_sel:[0,1,1] op_sel_hi:[1,1,1]\n"
    "v_cvt_pk_bf16_f32 v38, v40, v41\n"
    "v_cvt_pk_bf16_f32 v39, v42, v43\n"
    "v_cvt_pk_bf16_f32 v54, v56, v57\n"
    "v_cvt_pk_bf16_f32 v55, v58, v59\n"
    "ds_write_b128 v206, v[36:39] offset:2048\n"
    "ds_write_b128 v207, v[52:55] offset:2048\n"
    "ds_read_b128 v[40:43], v208 offset:2048\n"
    "buffer_store_dwordx4 v[228:231], v205, s[24:27], s78 offen offset:3072 sc1\n"
    "v_mfma_f32_16x16x32_bf16 a[116:119], v[180:183], v[76:79], a[116:119]\n"
    "s_waitcnt lgkmcnt(3)\n"
    "ds_read_b128 v[56:59], v208 offset:3072\n"
    "ds_read_b128 v[136:139], v248 offset:23552\n"
    "v_mfma_f32_16x16x32_bf16 a[116:119], v[184:187], v[80:83], a[116:119]\n"
    "ds_read_b128 v[132:135], v248 offset:11264\n"
    "v_mov_b32 v212, 0\n"
    "v_mfma_f32_16x16x32_bf16 a[100:103], v[176:179], v[80:83], a[100:103]\n"
    "v_fma_f32 v212, |v110|, 4.0, v212\n"
    "v_fma_f32 v212, v108, 4.0, v212\n"
    "v_mfma_f32_16x16x32_bf16 a[100:103], v[172:175], v[76:79], a[100:103]\n"
    "v_fma_f32 v212, v109, s74, v212\n"
    "v_fma_f32 v212, |v114|, 4.0, v212\n"
    "v_mfma_f32_16x16x32_bf16 a[84:87], v[164:167], v[76:79], a[84:87]\n"
    "v_accvgpr_read_b32 v47, a116\n"
    "v_accvgpr_read_b32 v63, a118\n"
    "v_mfma_f32_16x16x32_bf16 a[84:87], v[168:171], v[80:83], a[84:87]\n"
    "s_waitcnt lgkmcnt(3)\n"
    "v_accvgpr_read_b32 v51, a117\n"
    "v_accvgpr_read_b32 v46, a100\n"
    "buffer_store_dwordx4 v[40:43], v205, s[24:27], s75 offen offset:128 sc1\n"
    "v_mfma_f32_16x16x32_bf16 a[68:71], v[160:163], v[80:83], a[68:71]\n"
    "s_waitcnt lgkmcnt(1)\n"
    "v_accvgpr_read_b32 v62, a102\n"
    "v_accvgpr_read_b32 v50, a101\n"
    "v_mfma_f32_16x16x32_bf16 a[68:71], v[156:159], v[76:79], a[68:71]\n"
    "s_waitcnt lgkmcnt(0)\n"
    "v_accvgpr_read_b32 v45, a84\n"
    "v_accvgpr_read_b32 v61, a86\n"
    "v_accvgpr_read_b32 v49, a85\n"
    "v_accvgpr_read_b32 v65, a87\n"
    "v_accvgpr_read_b32 v66, a103\n"
    "v_pk_fma_f32 v[46:47], v[46:47], v[112:113], v[114:115] op_sel:[0,1,1] op_sel_hi:[1,1,1]\n"
    "v_pk_fma_f32 v[62:63], v[62:63], v[112:113], v[114:115] op_sel:[0,1,1] op_sel_hi:[1,1,1]\n"
    "v_accvgpr_read_b32 v44, a68\n"
    "v_accvgpr_read_b32 v60, a70\n"
    "v_pk_fma_f32 v[44:45], v[44:45], v[112:113], v[114:115] op_sel:[0,1,1] op_sel_hi:[1,1,1]\n"
    "v_pk_fma_f32 v[60:61], v[60:61], v[112:113], v[114:115] op_sel:[0,1,1] op_sel_hi:[1,1,1]\n"
    "v_cvt_pk_bf16_f32 v44, v44, v45\n"
    "v_accvgpr_read_b32 v48, a69\n"
    "v_cvt_pk_bf16_f32 v60, v60, v61\n"
    "v_accvgpr_read_b32 v64, a71\n"
    "v_accvgpr_read_b32 v67, a119\n"
    "v_cvt_pk_bf16_f32 v45, v46, v47\n"
    "v_pk_fma_f32 v[48:49], v[48:49], v[112:113], v[114:115] op_sel:[0,1,1] op_sel_hi:[1,1,1]\n"
    "v_pk_fma_f32 v[50:51], v[50:51], v[112:113], v[114:115] op_sel:[0,1,1] op_sel_hi:[1,1,1]\n"
    "v_cvt_pk_bf16_f32 v61, v62, v63\n"
    "v_pk_fma_f32 v[64:65], v[64:65], v[112:113], v[114:115] op_sel:[0,1,1] op_sel_hi:[1,1,1]\n"
    "v_pk_fma_f32 v[66:67], v[66:67], v[112:113], v[114:115] op_sel:[0,1,1] op_sel_hi:[1,1,1]\n"
    "v_cvt_pk_bf16_f32 v46, v48, v49\n"
    "v_cvt_pk_bf16_f32 v47, v50, v51\n"
    "v_cvt_pk_bf16_f32 v62, v64, v65\n"
    "v_cvt_pk_bf16_f32 v63, v66, v67\n"
    "ds_write_b128 v206, v[44:47] offset:4096\n"
    "ds_write_b128 v207, v[60:63] offset:4096\n"
    "ds_read_b128 v[48:51], v208 offset:4096\n"
    "buffer_store_dwordx4 v[56:59], v205, s[24:27], s75 offen offset:3200 sc1\n"
    "v_mfma_f32_16x16x32_bf16 a[72:75], v[156:159], v[84:87], a[72:75]\n"
    "ds_read_b128 v[64:67], v208 offset:5120\n"
    "v_fma_f32 v212, v112, 4.0, v212\n"
    "v_mfma_f32_16x16x32_bf16 a[72:75], v[160:163], v[88:91], a[72:75]\n"
    "v_fma_f32 v212, v113, s74, v212\n"
    "v_fma_f32 v212, |v118|, 4.0, v212\n"
    "v_mfma_f32_16x16x32_bf16 a[88:91], v[168:171], v[88:91], a[88:91]\n"
    "v_fma_f32 v212, v116, 4.0, v212\n"
    "v_fma_f32 v212, v117, s74, v212\n"
    "v_mfma_f32_16x16x32_bf16 a[88:91], v[164:167], v[84:87], a[88:91]\n"
    "v_fma_f32 v212, |v122|, 4.0, v212\n"
    "v_fma_f32 v212, v120, 4.0, v212\n"
    "v_mfma_f32_16x16x32_bf16 a[104:107], v[172:175], v[84:87], a[104:107]\n"
    "v_accvgpr_read_b32 v216, a72\n"
    "v_accvgpr_read_b32 v224, a74\n"
    "v_mfma_f32_16x16x32_bf16 a[104:107], v[176:179], v[88:91], a[104:107]\n"
    "s_waitcnt lgkmcnt(1)\n"
    "v_accvgpr_read_b32 v220, a73\n"
    "v_accvgpr_read_b32 v217, a88\n"
    "buffer_store_dwordx4 v[48:51], v205, s[24:27], s76 offen offset:128 sc1\n"
    "v_mfma_f32_16x16x32_bf16 a[120:123], v[184:187], v[88:91], a[120:123]\n"
    "s_waitcnt lgkmcnt(0)\n"
    "v_accvgpr_read_b32 v225, a90\n"
    "v_accvgpr_read_b32 v221, a89\n"
    "v_mfma_f32_16x16x32_bf16 a[120:123], v[180:183], v[84:87], a[120:123]\n"
    "v_accvgpr_read_b32 v218, a104\n"
    "v_accvgpr_read_b32 v226, a106\n"
    "v_accvgpr_read_b32 v222, a105\n"
    "v_accvgpr_read_b32 v228, a75\n"
    "v_accvgpr_read_b32 v229, a91\n"
    "v_pk_fma_f32 v[216:217], v[216:217], v[116:117], v[118:119] op_sel:[0,1,1] op_sel_hi:[1,1,1]\n"
    "v_pk_fma_f32 v[224:225], v[224:225], v[116:117], v[118:119] op_sel:[0,1,1] op_sel_hi:[1,1,1]\n"
    "v_cvt_pk_bf16_f32 v216, v216, v217\n"
    "v_accvgpr_read_b32 v219, a120\n"
    "v_accvgpr_read_b32 v227, a122\n"
    "v_pk_fma_f32 v[218:219], v[218:219], v[116:117], v[118:119] op_sel:[0,1,1] op_sel_hi:[1,1,1]\n"
    "v_accvgpr_read_b32 v223, a121\n"
    "v_pk_fma_f32 v[226:227], v[226:227], v[116:117], v[118:119] op_sel:[0,1,1] op_sel_hi:[1,1,1]\n"
    "v_cvt_pk_bf16_f32 v224, v224, v225\n"
    "v_accvgpr_read_b32 v230, a107\n"
    "v_accvgpr_read_b32 v231, a123\n"
    "v_cvt_pk_bf16_f32 v217, v218, v219\n"
    "v_pk_fma_f32 v[220:221], v[220:221], v[116:117], v[118:119] op_sel:[0,1,1] op_sel_hi:[1,1,1]\n"
    "v_pk_fma_f32 v[222:223], v[222:223], v[116:117], v[118:119] op_sel:[0,1,1] op_sel_hi:[1,1,1]\n"
    "v_cvt_pk_bf16_f32 v225, v226, v227\n"
    "v_pk_fma_f32 v[228:229], v[228:229], v[116:117], v[118:119] op_sel:[0,1,1] op_sel_hi:[1,1,1]\n"
    "v_pk_fma_f32 v[230:231], v[230:231], v[116:117], v[118:119] op_sel:[0,1,1] op_sel_hi:[1,1,1]\n"
    "v_cvt_pk_bf16_f32 v218, v220, v221\n"
    "v_cvt_pk_bf16_f32 v219, v222, v223\n"
    "v_cvt_pk_bf16_f32 v226, v228, v229\n"
    "v_cvt_pk_bf16_f32 v227, v230, v231\n"
    "ds_write_b128 v206, v[216:219] offset:0\n"
    "ds_write_b128 v207, v[224:227] offset:0\n"
    "ds_read_b128 v[220:223], v208 offset:0\n"
    "buffer_store_dwordx4 v[64:67], v205, s[24:27], s76 offen offset:3200 sc1\n"
    "v_mfma_f32_16x16x32_bf16 a[124:127], v[180:183], v[92:95], a[124:127]\n"
    "ds_read_b128 v[228:231], v208 offset:1024\n"
    "v_fma_f32 v212, v121, s74, v212\n"
    "v_mfma_f32_16x16x32_bf16 a[124:127], v[184:187], v[96:99], a[124:127]\n"
    "v_cmp_eq_u64 s[56:57], v[236:237], v[238:239]\n"
    "v_cmp_nlt_f32 s[54:55], |v212|, s73\n"
    "v_mfma_f32_16x16x32_bf16 a[108:111], v[176:179], v[96:99], a[108:111]\n"
    "s_or_b64 s[56:57], s[56:57], s[34:35]\n"
    "v_lshlrev_b32 v209, 16, v1\n"
    "s_or_b64 s[54:55], s[54:55], s[56:57]\n"
    "v_mfma_f32_16x16x32_bf16 a[108:111], v[172:175], v[92:95], a[108:111]\n"
    "s_or_b32 %0, s54, s55\n"
    "v_accvgpr_read_b32 v39, a124\n"
    "v_accvgpr_read_b32 v55, a126\n"
    "v_mfma_f32_16x16x32_bf16 a[92:95], v[164:167], v[92:95], a[92:95]\n"
    "v_accvgpr_read_b32 v43, a125\n"
    "v_accvgpr_read_b32 v59, a127\n"
    "v_mfma_f32_16x16x32_bf16 a[92:95], v[168:171], v[96:99], a[92:95]\n"
    "s_waitcnt lgkmcnt(1)\n"
    "v_accvgpr_read_b32 v38, a108\n"
    "v_accvgpr_read_b32 v54, a110\n"
    "buffer_store_dwordx4 v[220:223], v205, s[24:27], s77 offen offset:128 sc1\n"
    "v_mfma_f32_16x16x32_bf16 a[76:79], v[160:163], v[96:99], a[76:79]\n"
    "s_waitcnt lgkmcnt(0)\n"
    "v_accvgpr_read_b32 v42, a109\n"
    "v_accvgpr_read_b32 v58, a111\n"
    "v_mfma_f32_16x16x32_bf16 a[76:79], v[156:159], v[92:95], a[76:79]\n"
    "s_nop 0\n"
    "v_accvgpr_read_b32 v37, a92\n"
    "v_accvgpr_read_b32 v53, a94\n"
    "v_accvgpr_read_b32 v41, a93\n"
    "v_accvgpr_read_b32 v57, a95\n"
    "buffer_store_dwordx4 v[228:231], v205, s[24:27], s77 offen offset:3200 sc1\n"
    "v_pk_fma_f32 v[38:39], v[38:39], v[120:121], v[122:123] op_sel:[0,1,1] op_sel_hi:[1,1,1]\n"
    "v_pk_fma_f32 v[54:55], v[54:55], v[120:121], v[122:123] op_sel:[0,1,1] op_sel_hi:[1,1,1]\n"
    "v_accvgpr_read_b32 v36, a76\n"
    "v_accvgpr_read_b32 v52, a78\n"
    "v_pk_fma_f32 v[36:37], v[36:37], v[120:121], v[122:123] op_sel:[0,1,1] op_sel_hi:[1,1,1]\n"
    "v_pk_fma_f32 v[52:53], v[52:53], v[120:121], v[122:123] op_sel:[0,1,1] op_sel_hi:[1,1,1]\n"
    "v_cvt_pk_bf16_f32 v36, v36, v37\n"
    "v_accvgpr_read_b32 v40, a77\n"
    "v_cvt_pk_bf16_f32 v52, v52, v53\n"
    "v_accvgpr_read_b32 v56, a79\n"
    "v_cvt_pk_bf16_f32 v37, v38, v39\n"
    "v_pk_fma_f32 v[40:41], v[40:41], v[120:121], v[122:123] op_sel:[0,1,1] op_sel_hi:[1,1,1]\n"
    "v_pk_fma_f32 v[42:43], v[42:43], v[120:121], v[122:123] op_sel:[0,1,1] op_sel_hi:[1,1,1]\n"
    "v_cvt_pk_bf16_f32 v53, v54, v55\n"
    "v_pk_fma_f32 v[56:57], v[56:57], v[120:121], v[122:123] op_sel:[0,1,1] op_sel_hi:[1,1,1]\n"
    "v_pk_fma_f32 v[58:59], v[58:59], v[120:121], v[122:123] op_sel:[0,1,1] op_sel_hi:[1,1,1]\n"
    "v_cvt_pk_bf16_f32 v38, v40, v41\n"
    "v_cvt_pk_bf16_f32 v39, v42, v43\n"
    "v_cvt_pk_bf16_f32 v54, v56, v57\n"
    "v_cvt_pk_bf16_f32 v55, v58, v59\n"
    "ds_write_b128 v206, v[36:39] offset:2048\n"
    "ds_write_b128 v207, v[52:55] offset:2048\n"
    "ds_read_b128 v[40:43], v208 offset:2048\n"
    "v_mfma_f32_16x16x32_bf16 a[128:131], v[188:191], v[68:71], a[128:131]\n"
    "ds_read_b128 v[56:59], v208 offset:3072\n"
    "buffer_store_dwordx2 v[238:239], v209, s[36:39], s82 offen offset:0 sc1\n"
    "v_mfma_f32_16x16x32_bf16 a[128:131], v[192:195], v[72:75], a[128:131]\n"
    "v_mfma_f32_16x16x32_bf16 a[144:147], v[200:203], v[72:75], a[144:147]\n"
    "v_mfma_f32_16x16x32_bf16 a[144:147], v[196:199], v[68:71], a[144:147]\n"
    "v_mfma_f32_16x16x32_bf16 a[160:163], v[124:127], v[68:71], a[160:163]\n"
    "v_mfma_f32_16x16x32_bf16 a[160:163], v[128:131], v[72:75], a[160:163]\n"
    "v_mfma_f32_16x16x32_bf16 a[176:179], v[136:139], v[72:75], a[176:179]\n"
    "s_waitcnt lgkmcnt(1)\n"
    "buffer_store_dwordx4 v[40:43], v205, s[24:27], s78 offen offset:128 sc1\n"
    "v_mfma_f32_16x16x32_bf16 a[176:179], v[132:135], v[68:71], a[176:179]\n"
    "s_waitcnt lgkmcnt(0)\n"
    "v_accvgpr_read_b32 v44, a128\n"
    "v_accvgpr_read_b32 v45, a144\n"
    "v_accvgpr_read_b32 v60, a130\n"
    "v_accvgpr_read_b32 v61, a146\n"
    "v_accvgpr_read_b32 v46, a160\n"
    "v_pk_fma_f32 v[44:45], v[44:45], v[108:109], v[110:111] op_sel:[0,1,1] op_sel_hi:[1,1,1]\n"
    "v_accvgpr_read_b32 v62, a162\n"
    "v_accvgpr_read_b32 v47, a176\n"
    "v_accvgpr_read_b32 v63, a178\n"
    "v_pk_fma_f32 v[60:61], v[60:61], v[108:109], v[110:111] op_sel:[0,1,1] op_sel_hi:[1,1,1]\n"
    "v_pk_fma_f32 v[46:47], v[46:47], v[108:109], v[110:111] op_sel:[0,1,1] op_sel_hi:[1,1,1]\n"
    "v_cvt_pk_bf16_f32 v44, v44, v45\n"
    "v_accvgpr_read_b32 v48, a129\n"
    "v_accvgpr_read_b32 v49, a145\n"
    "v_accvgpr_read_b32 v50, a161\n"
    "v_accvgpr_read_b32 v51, a177\n"
    "v_pk_fma_f32 v[62:63], v[62:63], v[108:109], v[110:111] op_sel:[0,1,1] op_sel_hi:[1,1,1]\n"
    "v_cvt_pk_bf16_f32 v60, v60, v61\n"
    "v_accvgpr_read_b32 v64, a131\n"
    "v_accvgpr_read_b32 v65, a147\n"
    "v_accvgpr_read_b32 v66, a163\n"
    "v_accvgpr_read_b32 v67, a179\n"
    "v_cvt_pk_bf16_f32 v45, v46, v47\n"
    "v_pk_fma_f32 v[48:49], v[48:49], v[108:109], v[110:111] op_sel:[0,1,1] op_sel_hi:[1,1,1]\n"
    "v_pk_fma_f32 v[50:51], v[50:51], v[108:109], v[110:111] op_sel:[0,1,1] op_sel_hi:[1,1,1]\n"
    "v_cvt_pk_bf16_f32 v61, v62, v63\n"
    "v_pk_fma_f32 v[64:65], v[64:65], v[108:109], v[110:111] op_sel:[0,1,1] op_sel_hi:[1,1,1]\n"
    "v_pk_fma_f32 v[66:67], v[66:67], v[108:109], v[110:111] op_sel:[0,1,1] op_sel_hi:[1,1,1]\n"
    "v_cvt_pk_bf16_f32 v46, v48, v49\n"
    "v_cvt_pk_bf16_f32 v47, v50, v51\n"
    "v_cvt_pk_bf16_f32 v62, v64, v65\n"
    "v_cvt_pk_bf16_f32 v63, v66, v67\n"
    "ds_write_b128 v206, v[44:47] offset:4096\n"
    "ds_write_b128 v207, v[60:63] offset:4096\n"
    "ds_read_b128 v[48:51], v208 offset:4096\n"
    "buffer_store_dwordx4 v[56:59], v205, s[24:27], s78 offen offset:3200 sc1\n"
    "v_mfma_f32_16x16x32_bf16 a[180:183], v[132:135], v[76:79], a[180:183]\n"
    "ds_read_b128 v[64:67], v208 offset:5120\n"
    "v_mfma_f32_16x16x32_bf16 a[180:183], v[136:139], v[80:83], a[180:183]\n"
    "v_mfma_f32_16x16x32_bf16 a[164:167], v[128:131], v[80:83], a[164:167]\n"
    "v_mfma_f32_16x16x32_bf16 a[164:167], v[124:127], v[76:79], a[164:167]\n"
    "v_mfma_f32_16x16x32_bf16 a[148:151], v[196:199], v[76:79], a[148:151]\n"
    "v_mfma_f32_16x16x32_bf16 a[148:151], v[200:203], v[80:83], a[148:151]\n"
    "s_waitcnt lgkmcnt(1)\n"
    "buffer_store_dwordx4 v[48:51], v205, s[24:27], s75 offen offset:256 sc1\n"
    "v_mfma_f32_16x16x32_bf16 a[132:135], v[192:195], v[80:83], a[132:135]\n"
    "s_waitcnt lgkmcnt(0)\n"
    "v_accvgpr_read_b32 v219, a180\n"
    "v_accvgpr_read_b32 v227, a182\n"
    "v_mfma_f32_16x16x32_bf16 a[132:135], v[188:191], v[76:79], a[132:135]\n"
    "s_nop 0\n"
    "v_accvgpr_read_b32 v217, a148\n"
    "v_accvgpr_read_b32 v225, a150\n"
    "v_accvgpr_read_b32 v218, a164\n"
    "v_accvgpr_read_b32 v226, a166\n"
    "v_accvgpr_read_b32 v221, a149\n"
    "v_pk_fma_f32 v[218:219], v[218:219], v[112:113], v[114:115] op_sel:[0,1,1] op_sel_hi:[1,1,1]\n"
    "v_accvgpr_read_b32 v222, a165\n"
    "v_accvgpr_read_b32 v216, a132\n"
    "v_accvgpr_read_b32 v224, a134\n"
    "v_pk_fma_f32 v[216:217], v[216:217], v[112:113], v[114:115] op_sel:[0,1,1] op_sel_hi:[1,1,1]\n"
    "v_pk_fma_f32 v[224:225], v[224:225], v[112:113], v[114:115] op_sel:[0,1,1] op_sel_hi:[1,1,1]\n"
    "v_cvt_pk_bf16_f32 v216, v216, v217\n"
    "v_accvgpr_read_b32 v220, a133\n"
    "v_accvgpr_read_b32 v223, a181\n"
    "v_pk_fma_f32 v[226:227], v[226:227], v[112:113], v[114:115] op_sel:[0,1,1] op_sel_hi:[1,1,1]\n"
    "v_cvt_pk_bf16_f32 v224, v224, v225\n"
    "v_accvgpr_read_b32 v228, a135\n"
    "v_accvgpr_read_b32 v229, a151\n"
    "v_accvgpr_read_b32 v230, a167\n"
    "v_accvgpr_read_b32 v231, a183\n"
    "v_cvt_pk_bf16_f32 v217, v218, v219\n"
    "v_pk_fma_f32 v[220:221], v[220:221], v[112:113], v[114:115] op_sel:[0,1,1] op_sel_hi:[1,1,1]\n"
    "v_pk_fma_f32 v[222:223], v[222:223], v[112:113], v[114:115] op_sel:[0,1,1] op_sel_hi:[1,1,1]\n"
    "v_cvt_pk_bf16_f32 v225, v226, v227\n"
    "v_pk_fma_f32 v[228:229], v[228:229], v[112:113], v[114:115] op_sel:[0,1,1] op_sel_hi:[1,1,1]\n"
    "v_pk_fma_f32 v[230:231], v[230:231], v[112:113], v[114:115] op_sel:[0,1,1] op_sel_hi:[1,1,1]\n"
    "v_cvt_pk_bf16_f32 v218, v220, v221\n"
    "v_cvt_pk_bf16_f32 v219, v222, v223\n"
    "v_cvt_pk_bf16_f32 v226, v228, v229\n"
    "v_cvt_pk_bf16_f32 v227, v230, v231\n"
    "ds_write_b128 v206, v[216:219] offset:0\n"
    "ds_write_b128 v207, v[224:227] offset:0\n"
    "ds_read_b128 v[220:223], v208 offset:0\n"
    "buffer_store_dwordx4 v[64:67], v205, s[24:27], s75 offen offset:3328 sc1\n"
    "v_mfma_f32_16x16x32_bf16 a[136:139], v[188:191], v[84:87], a[136:139]\n"
    "ds_read_b128 v[228:231], v208 offset:1024\n"
    "v_mfma_f32_16x16x32_bf16 a[136:139], v[192:195], v[88:91], a[136:139]\n"
    "v_mfma_f32_16x16x32_bf16 a[152:155], v[200:203], v[88:91], a[152:155]\n"
    "v_mfma_f32_16x16x32_bf16 a[152:155], v[196:199], v[84:87], a[152:155]\n"
    "v_mfma_f32_16x16x32_bf16 a[168:171], v[124:127], v[84:87], a[168:171]\n"
    "v_mfma_f32_16x16x32_bf16 a[168:171], v[128:131], v[88:91], a[168:171]\n"
    "s_waitcnt lgkmcnt(1)\n"
    "buffer_store_dwordx4 v[220:223], v205, s[24:27], s76 offen offset:256 sc1\n"
    "v_mfma_f32_16x16x32_bf16 a[184:187], v[136:139], v[88:91], a[184:187]\n"
    "s_waitcnt lgkmcnt(0)\n"
    "v_accvgpr_read_b32 v36, a136\n"
    "v_accvgpr_read_b32 v52, a138\n"
    "v_mfma_f32_16x16x32_bf16 a[184:187], v[132:135], v[84:87], a[184:187]\n"
    "v_accvgpr_read_b32 v37, a152\n"
    "v_accvgpr_read_b32 v53, a154\n"
    "v_accvgpr_read_b32 v38, a168\n"
    "v_accvgpr_read_b32 v54, a170\n"
    "v_accvgpr_read_b32 v40, a137\n"
    "v_pk_fma_f32 v[36:37], v[36:37], v[116:117], v[118:119] op_sel:[0,1,1] op_sel_hi:[1,1,1]\n"
    "v_pk_fma_f32 v[52:53], v[52:53], v[116:117], v[118:119] op_sel:[0,1,1] op_sel_hi:[1,1,1]\n"
    "v_cvt_pk_bf16_f32 v36, v36, v37\n"
    "v_accvgpr_read_b32 v39, a184\n"
    "v_accvgpr_read_b32 v55, a186\n"
    "v_pk_fma_f32 v[38:39], v[38:39], v[116:117], v[118:119] op_sel:[0,1,1] op_sel_hi:[1,1,1]\n"
    "v_accvgpr_read_b32 v41, a153\n"
    "v_accvgpr_read_b32 v42, a169\n"
    "v_accvgpr_read_b32 v43, a185\n"
    "v_pk_fma_f32 v[54:55], v[54:55], v[116:117], v[118:119] op_sel:[0,1,1] op_sel_hi:[1,1,1]\n"
    "v_cvt_pk_bf16_f32 v52, v52, v53\n"
    "v_accvgpr_read_b32 v56, a139\n"
    "v_accvgpr_read_b32 v57, a155\n"
    "v_accvgpr_read_b32 v58, a171\n"
    "v_accvgpr_read_b32 v59, a187\n"
    "v_cvt_pk_bf16_f32 v37, v38, v39\n"
    "v_pk_fma_f32 v[40:41], v[40:41], v[116:117], v[118:119] op_sel:[0,1,1] op_sel_hi:[1,1,1]\n"
    "v_pk_fma_f32 v[42:43], v[42:43], v[116:117], v[118:119] op_sel:[0,1,1] op_sel_hi:[1,1,1]\n"
    "v_cvt_pk_bf16_f32 v53, v54, v55\n"
    "v_pk_fma_f32 v[56:57], v[56:57], v[116:117], v[118:119] op_sel:[0,1,1] op_sel_hi:[1,1,1]\n"
    "v_pk_fma_f32 v[58:59], v[58:59], v[116:117], v[118:119] op_sel:[0,1,1] op_sel_hi:[1,1,1]\n"
    "v_cvt_pk_bf16_f32 v38, v40, v41\n"
    "v_cvt_pk_bf16_f32 v39, v42, v43\n"
    "v_cvt_pk_bf16_f32 v54, v56, v57\n"
    "v_cvt_pk_bf16_f32 v55, v58, v59\n"
    "ds_write_b128 v206, v[36:39] offset:2048\n"
    "ds_write_b128 v207, v[52:55] offset:2048\n"
    "ds_read_b128 v[40:43], v208 offset:2048\n"
    "buffer_store_dwordx4 v[228:231], v205, s[24:27], s76 offen offset:3328 sc1\n"
    "v_mfma_f32_16x16x32_bf16 a[188:191], v[132:135], v[92:95], a[188:191]\n"
    "ds_read_b128 v[56:59], v208 offset:3072\n"
    "v_mfma_f32_16x16x32_bf16 a[188:191], v[136:139], v[96:99], a[188:191]\n"
    "v_mfma_f32_16x16x32_bf16 a[172:175], v[128:131], v[96:99], a[172:175]\n"
    "v_mfma_f32_16x16x32_bf16 a[172:175], v[124:127], v[92:95], a[172:175]\n"
    "v_mfma_f32_16x16x32_bf16 a[156:159], v[196:199], v[92:95], a[156:159]\n"
    "v_mfma_f32_16x16x32_bf16 a[156:159], v[200:203], v[96:99], a[156:159]\n"
    "s_waitcnt lgkmcnt(1)\n"
    "buffer_store_dwordx4 v[40:43], v205, s[24:27], s77 offen offset:256 sc1\n"
    "v_mfma_f32_16x16x32_bf16 a[140:143], v[192:195], v[96:99], a[140:143]\n"
    "s_waitcnt lgkmcnt(0)\n"
    "v_accvgpr_read_b32 v47, a188\n"
    "v_accvgpr_read_b32 v63, a190\n"
    "v_mfma_f32_16x16x32_bf16 a[140:143], v[188:191], v[92:95], a[140:143]\n"
    "s_nop 0\n"
    "v_accvgpr_read_b32 v45, a156\n"
    "v_accvgpr_read_b32 v61, a158\n"
    "v_accvgpr_read_b32 v46, a172\n"
    "v_accvgpr_read_b32 v62, a174\n"
    "v_accvgpr_read_b32 v49, a157\n"
    "v_pk_fma_f32 v[46:47], v[46:47], v[120:121], v[122:123] op_sel:[0,1,1] op_sel_hi:[1,1,1]\n"
    "v_accvgpr_read_b32 v50, a173\n"
    "v_accvgpr_read_b32 v44, a140\n"
    "v_accvgpr_read_b32 v60, a142\n"
    "v_pk_fma_f32 v[44:45], v[44:45], v[120:121], v[122:123] op_sel:[0,1,1] op_sel_hi:[1,1,1]\n"
    "v_pk_fma_f32 v[60:61], v[60:61], v[120:121], v[122:123] op_sel:[0,1,1] op_sel_hi:[1,1,1]\n"
    "v_cvt_pk_bf16_f32 v44, v44, v45\n"
    "v_accvgpr_read_b32 v48, a141\n"
    "v_accvgpr_read_b32 v51, a189\n"
    "v_pk_fma_f32 v[62:63], v[62:63], v[120:121], v[122:123] op_sel:[0,1,1] op_sel_hi:[1,1,1]\n"
    "v_cvt_pk_bf16_f32 v60, v60, v61\n"
    "v_accvgpr_read_b32 v64, a143\n"
    "v_accvgpr_read_b32 v65, a159\n"
    "v_accvgpr_read_b32 v66, a175\n"
    "v_accvgpr_read_b32 v67, a191\n"
    "v_cvt_pk_bf16_f32 v45, v46, v47\n"
    "v_pk_fma_f32 v[48:49], v[48:49], v[120:121], v[122:123] op_sel:[0,1,1] op_sel_hi:[1,1,1]\n"
    "v_pk_fma_f32 v[50:51], v[50:51], v[120:121], v[122:123] op_sel:[0,1,1] op_sel_hi:[1,1,1]\n"
    "v_cvt_pk_bf16_f32 v61, v62, v63\n"
    "v_pk_fma_f32 v[64:65], v[64:65], v[120:121], v[122:123] op_sel:[0,1,1] op_sel_hi:[1,1,1]\n"
    "v_pk_fma_f32 v[66:67], v[66:67], v[120:121], v[122:123] op_sel:[0,1,1] op_sel_hi:[1,1,1]\n"
    "v_cvt_pk_bf16_f32 v46, v48, v49\n"
    "v_cvt_pk_bf16_f32 v47, v50, v51\n"
    "v_cvt_pk_bf16_f32 v62, v64, v65\n"
    "v_cvt_pk_bf16_f32 v63, v66, v67\n"
    "ds_write_b128 v206, v[44:47] offset:4096\n"
    "ds_write_b128 v207, v[60:63] offset:4096\n"
    "ds_read_b128 v[48:51], v208 offset:4096\n"
    "buffer_store_dwordx4 v[56:59], v205, s[24:27], s77 offen offset:3328 sc1\n"
    "ds_read_b128 v[64:67], v208 offset:5120\n"
    "s_waitcnt lgkmcnt(1)\n"
    "buffer_store_dwordx4 v[48:51], v205, s[24:27], s78 offen offset:256 sc1\n"
    "s_waitcnt lgkmcnt(0)\n"
    "buffer_store_dwordx4 v[64:67], v205, s[24:27], s78 offen offset:3328 sc1\n"
    "s_cmp_eq_u32 %0, 0\n"
    "s_cbranch_scc1 .Lfin\n"
    "s_waitcnt vmcnt(0)\n"
    ".Lfin:\n"
    "s_branch .Lend\n"
    ".LpollAK:\n"
    "s_sleep 1\n"
    "buffer_load_dwordx2 v[236:237], v87, s[36:39], 0 offen offset:0 sc1\n"
    "s_waitcnt vmcnt(0)\n"
    "v_cmp_eq_u64 s[54:55], v[236:237], v[238:239]\n"
    "s_add_u32 s58, s58, 1\n"
    "s_cmp_eq_u64 s[54:55], -1\n"
    "s_cbranch_scc1 .LpollAKr\n"
    "s_cmp_lt_u32 s58, 0x80000\n"
    "s_cbranch_scc1 .LpollAK\n"
    "s_branch .LpollAKr\n"
    ".LpollAV:\n"
    "s_sleep 1\n"
    "buffer_load_dwordx2 v[236:237], v87, s[36:39], 0 offen offset:0 sc1\n"
    "s_waitcnt vmcnt(0)\n"
    "v_cmp_eq_u64 s[54:55], v[236:237], v[238:239]\n"
    "s_add_u32 s58, s58, 1\n"
    "s_cmp_eq_u64 s[54:55], -1\n"
    "s_cbranch_scc1 .LpollAVr\n"
    "s_cmp_lt_u32 s58, 0x80000\n"
    "s_cbranch_scc1 .LpollAV\n"
    "s_branch .LpollAVr\n"
    ".Lpoll1:\n"
    "s_sleep 1\n"
    "v_cmp_eq_u64 s[54:55], v[236:237], v[238:239]\n"
    "s_nop 1\n"
    "s_cmp_eq_u64 s[54:55], -1\n"
    "s_cbranch_scc1 .Lpoll1l\n"
    "buffer_load_dwordx2 v[236:237], v241, s[36:39], s81 offen offset:0 sc1\n"
    "s_waitcnt vmcnt(0)\n"
    "v_cmp_eq_u64 s[54:55], v[236:237], v[238:239]\n"
    ".Lpoll1l:\n"
    "ds_read_b32 v245, v247\n"
    "s_waitcnt lgkmcnt(0)\n"
    "v_cmp_le_u32 s[56:57], s59, v245\n"
    "s_add_u32 s58, s58, 1\n"
    "s_nop 1\n"
    "s_and_b64 s[54:55], s[54:55], s[56:57]\n"
    "s_cmp_eq_u64 s[54:55], -1\n"
    "s_cbranch_scc1 .Lpoll1r\n"
    "s_cmp_lt_u32 s58, 0x80000\n"
    "s_cbranch_scc1 .Lpoll1\n"
    "s_branch .Lpoll1r\n"
    ".Lpoll2:\n"
    "s_sleep 1\n"
    "v_cmp_eq_u64 s[54:55], v[236:237], v[238:239]\n"
    "s_nop 1\n"
    "s_cmp_eq_u64 s[54:55], -1\n"
    "s_cbranch_scc1 .Lpoll2l\n"
    "buffer_load_dwordx2 v[236:237], v241, s[36:39], s81 offen offset:0 sc1\n"
    "s_waitcnt vmcnt(0)\n"
    "v_cmp_eq_u64 s[54:55], v[236:237], v[238:239]\n"
    ".Lpoll2l:\n"
    "ds_read_b32 v245, v247\n"
    "s_waitcnt lgkmcnt(0)\n"
    "v_cmp_le_u32 s[56:57], s59, v245\n"
    "s_add_u32 s58, s58, 1\n"
    "s_nop 1\n"
    "s_and_b64 s[54:55], s[54:55], s[56:57]\n"
    "s_cmp_eq_u64 s[54:55], -1\n"
    "s_cbranch_scc1 .Lpoll2r\n"
    "s_cmp_lt_u32 s58, 0x80000\n"
    "s_cbranch_scc1 .Lpoll2\n"
    "s_branch .Lpoll2r\n"
    ".Lpoll3:\n"
    "s_sleep 1\n"
    "v_cmp_eq_u64 s[54:55], v[236:237], v[238:239]\n"
    "s_nop 1\n"
    "s_cmp_eq_u64 s[54:55], -1\n"
    "s_cbranch_scc1 .Lpoll3l\n"
    "buffer_load_dwordx2 v[236:237], v241, s[36:39], s81 offen offset:0 sc1\n"
    "s_waitcnt vmcnt(0)\n"
    "v_cmp_eq_u64 s[54:55], v[236:237], v[238:239]\n"
    ".Lpoll3l:\n"
    "ds_read_b32 v245, v247\n"
    "s_waitcnt lgkmcnt(0)\n"
    "v_cmp_le_u32 s[56:57], s59, v245\n"
    "s_add_u32 s58, s58, 1\n"
    "s_nop 1\n"
    "s_and_b64 s[54:55], s[54:55], s[56:57]\n"
    "s_cmp_eq_u64 s[54:55], -1\n"
    "s_cbranch_scc1 .Lpoll3r\n"
    "s_cmp_lt_u32 s58, 0x80000\n"
    "s_cbranch_scc1 .Lpoll3\n"
    "s_branch .Lpoll3r\n"
    ".Lpoll4:\n"
    "s_sleep 1\n"
    "v_cmp_eq_u64 s[54:55], v[236:237], v[238:239]\n"
    "s_nop 1\n"
    "s_cmp_eq_u64 s[54:55], -1\n"
    "s_cbranch_scc1 .Lpoll4l\n"
    "buffer_load_dwordx2 v[236:237], v241, s[36:39], s81 offen offset:0 sc1\n"
    "s_waitcnt vmcnt(0)\n"
    "v_cmp_eq_u64 s[54:55], v[236:237], v[238:239]\n"
    ".Lpoll4l:\n"
    "ds_read_b32 v245, v247\n"
    "s_waitcnt lgkmcnt(0)\n"
    "v_cmp_le_u32 s[56:57], s59, v245\n"
    "s_add_u32 s58, s58, 1\n"
    "s_nop 1\n"
    "s_and_b64 s[54:55], s[54:55], s[56:57]\n"
    "s_cmp_eq_u64 s[54:55], -1\n"
    "s_cbranch_scc1 .Lpoll4r\n"
    "s_cmp_lt_u32 s58, 0x80000\n"
    "s_cbranch_scc1 .Lpoll4\n"
    "s_branch .Lpoll4r\n"
    ".Lend:\n"
    : "=&s"(bad), "+s"(ka) : "s"(b), "v"(tid)
    : "memory", "vcc", "s16", "s17", "s18", "s19", "s20", "s21", "s22", "s23", "s24", "s25", "s26", "s27", "s28", "s29", "s30", "s31", "s34", "s35", "s36", "s37", "s38", "s39", "s40", "s41", "s42", "s43", "s44", "s45", "s46", "s47", "s48", "s49", "s50", "s51", "s52", "s53", "s54", "s55", "s56", "s57", "s58", "s59", "s60", "s61", "s62", "s63", "s64", "s65", "s66", "s67", "s68", "s69", "s70", "s71", "s72", "s73", "s74", "s75", "s76", "s77", "s78", "s79", "s80", "s81", "s82", "s83", "s84", "s85", "s86", "s87", "s88", "s89", "s90", "s91", "s92", "s93", "s94", "s95", "s96", "s97", "s98", "s99", "v1", "v2", "v3", "v4", "v5", "v6", "v7", "v8", "v9", "v10", "v11", "v12", "v13", "v14", "v15", "v16", "v17", "v18", "v19", "v20", "v21", "v22", "v23", "v24", "v25", "v26", "v27", "v28", "v29", "v30", "v31", "v32", "v33", "v34", "v35", "v36", "v37", "v38", "v39", "v40", "v41", "v42", "v43", "v44", "v45", "v46", "v47", "v48", "v49", "v50", "v51", "v52", "v53", "v54", "v55", "v56", "v57", "v58", "v59", "v60", "v61", "v62", "v63", "v64", "v65", "v66", "v67", "v68", "v69", "v70", "v71", "v72", "v73", "v74", "v75", "v76", "v77", "v78", "v79", "v80", "v81", "v82", "v83", "v84", "v85", "v86", "v87", "v88", "v89", "v90", "v91", "v92", "v93", "v94", "v95", "v96", "v97", "v98", "v99", "v100", "v101", "v102", "v103", "v104", "v105", "v106", "v107", "v108", "v109", "v110", "v111", "v112", "v113", "v114", "v115", "v116", "v117", "v118", "v119", "v120", "v121", "v122", "v123", "v124", "v125", "v126", "v127", "v128", "v129", "v130", "v131", "v132", "v133", "v134", "v135", "v136", "v137", "v138", "v139", "v140", "v141", "v142", "v143", "v144", "v145", "v146", "v147", "v148", "v149", "v150", "v151", "v152", "v153", "v154", "v155", "v156", "v157", "v158", "v159", "v160", "v161", "v162", "v163", "v164", "v165", "v166", "v167", "v168", "v169", "v170", "v171", "v172", "v173", "v174", "v175", "v176", "v177", "v178", "v179", "v180", "v181", "v182", "v183", "v184", "v185", "v186", "v187", "v188", "v189", "v190", "v191", "v192", "v193", "v194", "v195", "v196", "v197", "v198", "v199", "v200", "v201", "v202", "v203", "v204", "v205", "v206", "v207", "v208", "v209", "v210", "v211", "v212", "v213", "v214", "v215", "v216", "v217", "v218", "v219", "v220", "v221", "v222", "v223", "v224", "v225", "v226", "v227", "v228", "v229", "v230", "v231", "v232", "v233", "v234", "v235", "v236", "v237", "v238", "v239", "v240", "v241", "v242", "v243", "v244", "v245", "v246", "v247", "v248", "v249", "v250", "v251", "v252", "v253", "v254", "v255", "a0", "a1", "a2", "a3", "a4", "a5", "a6", "a7", "a8", "a9", "a10", "a11", "a12", "a13", "a14", "a15", "a16", "a17", "a18", "a19", "a20", "a21", "a22", "a23", "a24", "a25", "a26", "a27", "a28", "a29", "a30", "a31", "a32", "a33", "a34", "a35", "a36", "a37", "a38", "a39", "a40", "a41", "a42", "a43", "a44", "a45", "a46", "a47", "a48", "a49", "a50", "a51", "a52", "a53", "a54", "a55", "a56", "a57", "a58", "a59", "a60", "a61", "a62", "a63", "a64", "a65", "a66", "a67", "a68", "a69", "a70", "a71", "a72", "a73", "a74", "a75", "a76", "a77", "a78", "a79", "a80", "a81", "a82", "a83", "a84", "a85", "a86", "a87", "a88", "a89", "a90", "a91", "a92", "a93", "a94", "a95", "a96", "a97", "a98", "a99", "a100", "a101", "a102", "a103", "a104", "a105", "a106", "a107", "a108", "a109", "a110", "a111", "a112", "a113", "a114", "a115", "a116", "a117", "a118", "a119", "a120", "a121", "a122", "a123", "a124", "a125", "a126", "a127", "a128", "a129", "a130", "a131", "a132", "a133", "a134", "a135", "a136", "a137", "a138", "a139", "a140", "a141", "a142", "a143", "a144", "a145", "a146", "a147", "a148", "a149", "a150", "a151", "a152", "a153", "a154", "a155", "a156", "a157", "a158", "a159", "a160", "a161", "a162", "a163", "a164", "a165", "a166", "a167", "a168", "a169", "a170", "a171", "a172", "a173", "a174", "a175", "a176", "a177", "a178", "a179", "a180", "a181", "a182", "a183", "a184", "a185", "a186", "a187", "a188", "a189", "a190", "a191", "a192", "a193", "a194", "a195", "a196", "a197", "a198", "a199", "a200", "a201", "a202", "a203", "a204", "a205", "a206", "a207", "a208", "a209", "a210", "a211", "a212", "a213", "a214", "a215", "a216", "a217", "a218", "a219", "a220", "a221", "a222", "a223", "a224", "a225", "a226", "a227", "a228", "a229", "a230", "a231", "a232", "a233", "a234", "a235", "a236", "a237", "a238", "a239", "a240", "a241", "a242", "a243", "a244", "a245", "a246", "a247", "a248", "a249", "a250", "a251", "a252", "a253", "a254", "a255");
  if (bad) {
    slow_rows((const Args*)ka, (b & 7) * 4 + ((b >> 3) & 3), (b >> 5) * 256 + (tid & 192), tid & 63);
  }
}

extern "C" void kernel_launch(void* const* d_in, const int* in_sizes, int n_in,
                              void* d_out, int out_size, void* d_ws, size_t ws_size,
                              hipStream_t stream) {
  (void)hipFuncSetAttribute((const void*)attn_fwd, hipFuncAttributeMaxDynamicSharedMemorySize, SHM);
  attn_fwd<<<256, 256, SHM, stream>>>((const unsigned short*)d_in[0], (const int*)d_in[1], (const int*)d_in[2], (const unsigned short*)d_in[3], (const unsigned short*)d_in[4],
                                      (const unsigned short*)d_in[5], (const unsigned short*)d_in[6], (char*)d_ws, (unsigned short*)d_out);
}
```

```cpp
#include <hip/hip_runtime.h>

constexpr int D = 192, NH = 32, S = 2048;
constexpr int SHM = 155968;

struct Args { const unsigned short* q; const int* qk; const int* qv; const unsigned short* ksc; const unsigned short* kzr;
              const unsigned short* vsc; const unsigned short* vzr; char* ws; unsigned short* o; };

__device__ __forceinline__ float bf2f(unsigned short x) { return __uint_as_float((unsigned)x << 16); }
__device__ __forceinline__ float rbf(float x) { return (float)(__bf16)x; }

__device__ __forceinline__ void slow_rows(const Args* a, unsigned h, unsigned row0, unsigned lane) {
  const unsigned short* q = a->q + ((size_t)h * S + row0) * D;
  unsigned short* o = a->o + ((size_t)h * S + row0) * D;
  const int* qk = a->qk + (size_t)h * S * D; const int* qv = a->qv + (size_t)h * S * D;
  const unsigned short* ks = a->ksc + h * 32 * D; const unsigned short* kz = a->kzr + h * 32 * D;
  const unsigned short* vs = a->vsc + h * S; const unsigned short* vz = a->vzr + h * S;
#pragma unroll 1
  for (int r = 0; r < 64; ++r) {
    float m = -INFINITY, l = 0.f, a0 = 0.f, a1 = 0.f, a2 = 0.f;
#pragma unroll 1
    for (int t = 0; t < 32; ++t) {
      const int* kr = qk + (size_t)(t * 64 + lane) * D;
      float s = 0.f;
#pragma unroll 4
      for (int d = 0; d < D; ++d) s = fmaf(bf2f(q[r * D + d]), rbf(rbf(rbf((float)kr[d]) - bf2f(kz[t * D + d])) * bf2f(ks[t * D + d])), s);
      s *= 0.07216878364870322f;
      float mx = s;
      for (int x = 32; x; x >>= 1) mx = fmaxf(mx, __shfl_xor(mx, x));
      const float mn = fmaxf(m, mx), c = __expf(m - mn), p = __expf(s - mn);
      float ps = p;
      for (int x = 32; x; x >>= 1) ps += __shfl_xor(ps, x);
      l = l * c + ps; a0 *= c; a1 *= c; a2 *= c; m = mn;
#pragma unroll 2
      for (int k = 0; k < 64; ++k) {
        const float pk = __shfl(p, k), z = bf2f(vz[t * 64 + k]), sc = bf2f(vs[t * 64 + k]);
        const int* vr = qv + (size_t)(t * 64 + k) * D + lane;
        a0 = fmaf(pk, rbf(rbf(rbf((float)vr[0]) - z) * sc), a0); a1 = fmaf(pk, rbf(rbf(rbf((float)vr[64]) - z) * sc), a1); a2 = fmaf(pk, rbf(rbf(rbf((float)vr[128]) - z) * sc), a2);
      }
    }
    const float i = 1.f / l;
    o[r * D + lane] = __builtin_bit_cast(unsigned short, (__bf16)(a0 * i)); o[r * D + 64 + lane] = __builtin_bit_cast(unsigned short, (__bf16)(a1 * i));
    o[r * D + 128 + lane] = __builtin_bit_cast(unsigned short, (__bf16)(a2 * i));
  }
}

__global__ __launch_bounds__(256, 1) __attribute__((aligned(4096))) void attn_fwd(const unsigned short* q, const int* qk, const int* qv, const unsigned short* ksc, const unsigned short* kzr,
                                                   const unsigned short* vsc, const unsigned short* vzr, char* ws, unsigned short* o) {
  unsigned long long ka = (unsigned long long)__builtin_amdgcn_kernarg_segment_ptr();
  unsigned bad, b = blockIdx.x;
  const unsigned tid = threadIdx.x;
  asm volatile(
    "s_load_dwordx2 s[28:29], %1, 8\n"
    "s_load_dwordx2 s[40:41], %1, 16\n"
    "s_load_dwordx2 s[44:45], %1, 24\n"
    "s_load_dwordx2 s[88:89], %1, 32\n"
    "s_load_dwordx2 s[36:37], %1, 56\n"
    "s_load_dwordx2 s[92:93], %1, 40\n"
    "s_load_dwordx2 s[96:97], %1, 48\n"
    "s_load_dwordx2 s[20:21], %1, 0\n"
    "s_load_dwordx2 s[16:17], %1, 56\n"
    "s_load_dwordx2 s[24:25], %1, 64\n"
    "v_readfirstlane_b32 s84, %3\n"
    "v_and_b32 v156, 63, %3\n"
    "s_and_b32 s52, %2, 7\n"
    "s_bfe_u32 s72, %2, 0x20003\n"
    "s_lshr_b32 s53, %2, 5\n"
    "s_lshl_b32 s52, s52, 2\n"
    "s_lshr_b32 s84, s84, 6\n"
    "s_or_b32 s52, s52, s72\n"
    "v_lshlrev_b32 v1, 4, v156\n"
    "s_getpc_b64 s[54:55]\n"
    ".Lpc:\n"
    "s_lshl_b32 s72, s72, 14\n"
    "v_and_b32 v246, 127, %3\n"
    "v_lshl_add_u32 v246, v246, 7, s72\n"
    "v_min_u32 v246, .Lend-.Lpc-4, v246\n"
    "global_load_dword v246, v246, s[54:55]\n"
    "s_mov_b32 s30, 1572864\n"
    "s_mov_b32 s31, 0x20000\n"
    "s_mov_b32 s42, 1572864\n"
    "s_mov_b32 s43, 0x20000\n"
    "s_mov_b32 s46, 12288\n"
    "s_mov_b32 s47, 0x20000\n"
    "s_mov_b32 s90, 12288\n"
    "s_mov_b32 s91, 0x20000\n"
    "s_lshr_b32 s79, s84, 1\n"
    "s_and_b32 s59, s84, 1\n"
    "s_cmp_ge_u32 s53, 6\n"
    "s_cselect_b32 s83, 1, 0\n"
    "s_or_b32 s83, s83, s84\n"
    "s_sub_u32 s58, 2, s79\n"
    "s_mul_i32 s71, s58, 49152\n"
    "s_mul_i32 s72, s52, 1572864\n"
    "s_mul_i32 s73, s52, 12288\n"
    "v_lshrrev_b32 v157, 2, v156\n"
    "v_and_b32 v158, 3, v156\n"
    "s_mul_i32 s35, s84, 3072\n"
    "s_bitcmp1_b32 s83, 0\n"
    "s_cbranch_scc1 .Lf2b\n"
    "v_and_b32 v159, 32, v156\n"
    "v_mul_u32_u24 v242, 376, v159\n"
    "v_and_b32 v159, 16, v156\n"
    "v_mul_u32_u24 v159, 88, v159\n"
    "v_lshl_add_u32 v242, v156, 3, v242\n"
    "v_add_u32 v242, v159, v242\n"
    "s_lshl_b32 s63, s53, 7\n"
    "v_mul_u32_u24 v160, 768, v157\n"
    "v_lshl_add_u32 v160, v158, 4, v160\n"
    "s_and_b32 s81, s58, 1\n"
    "s_mul_i32 s81, s81, 384\n"
    "s_lshl_b32 s75, s53, 6\n"
    "s_add_u32 s81, s81, s75\n"
    "s_add_u32 s75, s71, s63\n"
    "s_add_u32 s76, s75, 6144\n"
    "s_add_u32 s77, s75, 24576\n"
    "s_add_u32 s78, s75, 30720\n"
    "s_waitcnt lgkmcnt(0)\n"
    "s_add_u32 s44, s44, s73\n"
    "s_addc_u32 s45, s45, 0\n"
    "s_add_u32 s88, s88, s73\n"
    "s_addc_u32 s89, s89, 0\n"
    "buffer_load_dwordx4 v[4:7], v1, s[44:47], s35 offen offset:0\n"
    "buffer_load_dwordx4 v[8:11], v1, s[44:47], s35 offen offset:1024\n"
    "buffer_load_dwordx4 v[12:15], v1, s[44:47], s35 offen offset:2048\n"
    "buffer_load_dwordx4 v[16:19], v1, s[88:91], 0 offen offset:0\n"
    "buffer_load_dwordx4 v[164:167], v160, s[44:47], s81 offen offset:0\n"
    "buffer_load_dwordx4 v[168:171], v160, s[88:91], s81 offen offset:0\n"
    "s_add_u32 s28, s28, s72\n"
    "s_addc_u32 s29, s29, 0\n"
    "buffer_load_dwordx2 v[204:205], v242, s[28:31], s75 offen offset:0 nt\n"
    "buffer_load_dwordx2 v[206:207], v242, s[28:31], s75 offen offset:768 nt\n"
    "buffer_load_dwordx2 v[208:209], v242, s[28:31], s75 offen offset:3072 nt\n"
    "buffer_load_dwordx2 v[210:211], v242, s[28:31], s75 offen offset:3840 nt\n"
    "buffer_load_dwordx2 v[212:213], v242, s[28:31], s76 offen offset:0 nt\n"
    "buffer_load_dwordx2 v[214:215], v242, s[28:31], s76 offen offset:768 nt\n"
    "buffer_load_dwordx2 v[216:217], v242, s[28:31], s76 offen offset:3072 nt\n"
    "buffer_load_dwordx2 v[218:219], v242, s[28:31], s76 offen offset:3840 nt\n"
    "buffer_load_dwordx2 v[220:221], v242, s[28:31], s77 offen offset:0 nt\n"
    "buffer_load_dwordx2 v[222:223], v242, s[28:31], s77 offen offset:768 nt\n"
    "buffer_load_dwordx2 v[224:225], v242, s[28:31], s77 offen offset:3072 nt\n"
    "buffer_load_dwordx2 v[226:227], v242, s[28:31], s77 offen offset:3840 nt\n"
    "buffer_load_dwordx2 v[228:229], v242, s[28:31], s78 offen offset:0 nt\n"
    "buffer_load_dwordx2 v[230:231], v242, s[28:31], s78 offen offset:768 nt\n"
    "buffer_load_dwordx2 v[232:233], v242, s[28:31], s78 offen offset:3072 nt\n"
    "buffer_load_dwordx2 v[234:235], v242, s[28:31], s78 offen offset:3840 nt\n"
    "s_add_u32 s75, s71, s63\n"
    "s_add_u32 s75, s75, 98304\n"
    "s_add_u32 s76, s75, 6144\n"
    "s_add_u32 s77, s75, 24576\n"
    "s_add_u32 s78, s75, 30720\n"
    "buffer_load_dwordx2 v[36:37], v242, s[28:31], s75 offen offset:0 nt\n"
    "buffer_load_dwordx2 v[38:39], v242, s[28:31], s75 offen offset:768 nt\n"
    "v_lshrrev_b32 v162, 1, v156\n"
    "v_and_b32 v162, 16, v162\n"
    "v_bfe_u32 v159, v156, 4, 1\n"
    "v_or_b32 v162, v162, v159\n"
    "v_and_or_b32 v162, v157, 2, v162\n"
    "v_mul_u32_u24 v162, 768, v162\n"
    "v_and_b32 v163, 0x70, v1\n"
    "v_add_u32 v162, v162, v163\n"
    "s_branch .Lf2e\n"
    ".Lf2b:\n"
    "v_and_b32 v159, 48, v156\n"
    "v_lshlrev_b32 v242, 2, v159\n"
    "v_lshl_add_u32 v243, v158, 4, v242\n"
    "v_and_b32 v159, 12, v156\n"
    "v_mul_u32_u24 v242, 384, v159\n"
    "v_add_u32 v242, v243, v242\n"
    "v_add_u32 v243, v243, v159\n"
    "s_mul_i32 s63, s53, 6144\n"
    "s_sub_u32 s63, s63, 49152\n"
    "s_cmp_eq_u32 s59, 0\n"
    "s_cselect_b32 s63, 0x7f000000, s63\n"
    "s_add_u32 s75, s71, s63\n"
    "s_waitcnt lgkmcnt(0)\n"
    "s_add_u32 s44, s44, s73\n"
    "s_addc_u32 s45, s45, 0\n"
    "s_add_u32 s88, s88, s73\n"
    "s_addc_u32 s89, s89, 0\n"
    "buffer_load_dwordx4 v[4:7], v1, s[44:47], s35 offen offset:0\n"
    "buffer_load_dwordx4 v[8:11], v1, s[44:47], s35 offen offset:1024\n"
    "buffer_load_dwordx4 v[12:15], v1, s[44:47], s35 offen offset:2048\n"
    "buffer_load_dwordx4 v[16:19], v1, s[88:91], 0 offen offset:0\n"
    "s_add_u32 s40, s40, s72\n"
    "s_addc_u32 s41, s41, 0\n"
    "buffer_load_dwordx4 v[204:207], v242, s[40:43], s75 offen offset:0 nt\n"
    "buffer_load_dwordx4 v[208:211], v242, s[40:43], s75 offen offset:768 nt\n"
    "buffer_load_dwordx4 v[212:215], v242, s[40:43], s75 offen offset:256 nt\n"
    "buffer_load_dwordx4 v[216:219], v242, s[40:43], s75 offen offset:1024 nt\n"
    "buffer_load_dwordx4 v[220:223], v242, s[40:43], s75 offen offset:512 nt\n"
    "buffer_load_dwordx4 v[224:227], v242, s[40:43], s75 offen offset:1280 nt\n"
    "s_add_u32 s75, s71, s63\n"
    "s_add_u32 s75, s75, 98304\n"
    "buffer_load_dwordx4 v[36:39], v242, s[40:43], s75 offen offset:0 nt\n"
    "s_add_u32 s28, s28, s72\n"
    "s_addc_u32 s29, s29, 0\n"
    "v_lshrrev_b32 v162, 1, v156\n"
    "v_and_b32 v162, 16, v162\n"
    "v_bfe_u32 v159, v156, 4, 1\n"
    "v_or_b32 v162, v162, v159\n"
    "v_and_or_b32 v162, v157, 2, v162\n"
    "v_mul_u32_u24 v162, 768, v162\n"
    "v_and_b32 v163, 0x70, v1\n"
    "v_add_u32 v162, v162, v163\n"
    ".Lf2e:\n"
    "s_nop 4\n"
    "v_lshlrev_b32 v247, 2, v156\n"
    "v_add_u32 v247, 155648, v247\n"
    "v_mov_b32 v159, 0\n"
    "ds_write_b32 v247, v159\n"
    "v_mov_b32 v159, 0xff800000\n"
    "v_mov_b32 v160, 155904\n"
    "ds_write_b32 v160, v159\n"
    "s_mov_b32 s38, 5376\n"
    "s_mov_b32 s39, 0x20000\n"
    "s_mov_b32 s94, 4096\n"
    "s_mov_b32 s95, 0x20000\n"
    "s_mov_b32 s98, 4096\n"
    "s_mov_b32 s99, 0x20000\n"
    "s_mov_b32 s22, 98304\n"
    "s_mov_b32 s23, 0x20000\n"
    "s_mov_b32 s18, 1622016\n"
    "s_mov_b32 s19, 0x20000\n"
    "s_mov_b32 s26, 98304\n"
    "s_mov_b32 s27, 0x20000\n"
    "s_lshl_b32 s80, s58, 7\n"
    "s_add_u32 s80, s80, 256\n"
    "s_lshl_b32 s74, s52, 16\n"
    "s_mul_i32 s69, s59, 384\n"
    "s_mul_i32 s57, s79, 6144\n"
    "s_add_u32 s57, s57, s69\n"
    "s_add_u32 s66, s57, 24576\n"
    "s_add_u32 s36, s36, s74\n"
    "s_addc_u32 s37, s37, 0\n"
    "s_lshr_b32 s74, s74, 4\n"
    "s_add_u32 s92, s92, s74\n"
    "s_addc_u32 s93, s93, 0\n"
    "s_add_u32 s96, s96, s74\n"
    "s_addc_u32 s97, s97, 0\n"
    "v_lshlrev_b32 v128, 3, v156\n"
    "v_mov_b32 v129, 0\n"
    "v_lshlrev_b32 v130, 4, %3\n"
    "buffer_load_dwordx2 v[124:125], v128, s[44:47], 0 offen offset:0\n"
    "buffer_load_dwordx2 v[126:127], v128, s[88:91], 0 offen offset:0\n"
    "buffer_load_dwordx2 v[238:239], v129, s[36:39], 0 offen offset:0 sc1\n"
    "s_mul_i32 s74, s52, 786432\n"
    "s_mul_i32 s73, s53, 98304\n"
    "s_add_u32 s74, s74, s73\n"
    "s_add_u32 s20, s20, s74\n"
    "s_addc_u32 s21, s21, 0\n"
    "s_add_u32 s24, s24, s74\n"
    "s_addc_u32 s25, s25, 0\n"
    "s_mul_i32 s72, s52, 1626112\n"
    "s_add_u32 s72, s72, 2097152\n"
    "s_add_u32 s16, s16, s72\n"
    "s_addc_u32 s17, s17, 0\n"
    "s_mul_i32 s65, s84, 6144\n"
    "s_mul_i32 s85, s84, 24576\n"
    "s_waitcnt lgkmcnt(0)\n"
    "s_barrier\n"
    "s_add_u32 s73, s65, 49152\n"
    "v_add_u32 v131, s73, v1\n"
    "s_add_u32 s73, s73, s69\n"
    "v_add_u32 v132, s73, v163\n"
    "v_and_b32 v159, 32, v156\n"
    "v_lshlrev_b32 v244, 3, v156\n"
    "v_xor_b32 v244, v244, v159\n"
    "v_lshl_add_u32 v244, v159, 3, v244\n"
    "s_bitcmp1_b32 s83, 0\n"
    "s_cbranch_scc1 .Lf5b\n"
    "s_lshl_b32 s60, s53, 11\n"
    "s_add_u32 s61, s60, 12288\n"
    "s_lshl_b32 s73, s58, 8\n"
    "s_add_u32 s73, s73, 122880\n"
    "v_and_b32 v243, 0xf0, v1\n"
    "v_add_u32 v243, s73, v243\n"
    "s_and_b32 s73, s73, 0x1fd00\n"
    "v_lshlrev_b32 v160, 9, v157\n"
    "v_lshl_add_u32 v160, v158, 6, v160\n"
    "v_add_u32 v160, s73, v160\n"
    "s_lshl_b32 s73, s79, 8\n"
    "s_mul_i32 s74, s59, 6144\n"
    "s_add_u32 s73, s73, s74\n"
    "v_add_u32 v163, s73, v244\n"
    "v_and_b32 v159, 32, v156\n"
    "v_lshlrev_b32 v244, 2, v156\n"
    "v_xor_b32 v244, v244, v159\n"
    "v_mad_u32_u24 v244, v159, 12, v244\n"
    "buffer_load_dwordx2 v[40:41], v242, s[28:31], s75 offen offset:3072 nt\n"
    "buffer_load_dwordx2 v[42:43], v242, s[28:31], s75 offen offset:3840 nt\n"
    "v_add_u32 v159, 0, v156\n"
    "v_mul_u32_u24 v159, 2731, v159\n"
    "v_lshrrev_b32 v159, 16, v159\n"
    "v_xor_b32 v159, v159, v156\n"
    "v_and_b32 v159, 7, v159\n"
    "v_and_b32 v88, 0x380, v1\n"
    "v_lshl_add_u32 v88, v159, 4, v88\n"
    "v_add_u32 v159, 64, v156\n"
    "v_mul_u32_u24 v159, 2731, v159\n"
    "v_lshrrev_b32 v159, 16, v159\n"
    "v_xor_b32 v159, v159, v156\n"
    "v_and_b32 v159, 7, v159\n"
    "v_and_b32 v89, 0x380, v1\n"
    "v_lshl_add_u32 v89, v159, 4, v89\n"
    "v_add_u32 v159, 128, v156\n"
    "v_mul_u32_u24 v159, 2731, v159\n"
    "v_lshrrev_b32 v159, 16, v159\n"
    "v_xor_b32 v159, v159, v156\n"
    "v_and_b32 v159, 7, v159\n"
    "v_and_b32 v90, 0x380, v1\n"
    "v_lshl_add_u32 v90, v159, 4, v90\n"
    "v_and_b32 v2, 7, v156\n"
    "v_lshrrev_b32 v144, 3, %3\n"
    "v_mul_u32_u24 v144, 768, v144\n"
    "v_lshl_add_u32 v144, v2, 3, v144\n"
    "buffer_load_dwordx2 v[44:45], v242, s[28:31], s76 offen offset:0 nt\n"
    "buffer_load_dwordx2 v[46:47], v242, s[28:31], s76 offen offset:768 nt\n"
    "v_add_u32 v144, 131072, v144\n"
    "v_mad_u32_u24 v86, v2, 24, v144\n"
    "v_and_b32 v84, 15, v156\n"
    "v_lshrrev_b32 v159, 4, v156\n"
    "v_xor_b32 v159, v159, v2\n"
    "v_mul_u32_u24 v84, 384, v84\n"
    "v_lshl_add_u32 v84, v159, 4, v84\n"
    "s_add_u32 s73, s65, 49152\n"
    "v_add_u32 v84, s73, v84\n"
    "v_xor_b32 v85, 64, v84\n"
    "v_subrev_u32 v154, 24576, v84\n"
    "v_subrev_u32 v155, 24576, v85\n"
    "s_add_u32 s67, s65, 98304\n"
    "s_add_u32 s68, s65, 24576\n"
    "s_add_u32 s69, s65, 73728\n"
    "s_mov_b32 s81, 256\n"
    "s_mul_i32 s74, s79, 24576\n"
    "s_add_u32 s70, s74, s65\n"
    "s_add_u32 s70, s70, 73728\n"
    "s_sub_u32 s64, s65, s74\n"
    "s_add_u32 s64, s64, 98304\n"
    "v_and_b32 v249, 0x310, v1\n"
    "v_and_b32 v159, 12, v156\n"
    "v_lshl_add_u32 v249, v159, 3, v249\n"
    "v_and_b32 v159, 2, v156\n"
    "buffer_load_dwordx2 v[48:49], v242, s[28:31], s76 offen offset:3072 nt\n"
    "buffer_load_dwordx2 v[50:51], v242, s[28:31], s76 offen offset:3840 nt\n"
    "v_lshl_add_u32 v249, v159, 6, v249\n"
    "s_add_u32 s73, s74, 73728\n"
    "v_add_u32 v248, s73, v249\n"
    "s_sub_u32 s73, 98304, s74\n"
    "v_add_u32 v249, s73, v249\n"
    "s_or_b32 s73, s53, s84\n"
    "s_cmp_eq_u32 s73, 0\n"
    "s_cselect_b32 s82, 0, 0x7fff0000\n"
    "v_and_b32 v252, 12, v156\n"
    "v_bfe_u32 v159, v156, 1, 1\n"
    "v_or_b32 v252, v252, v159\n"
    "v_and_b32 v159, 1, v156\n"
    "v_lshl_or_b32 v252, v159, 1, v252\n"
    "v_lshlrev_b32 v252, 6, v252\n"
    "v_lshlrev_b32 v159, 2, v156\n"
    "v_and_b32 v159, 32, v159\n"
    "v_xor_b32 v159, v159, v156\n"
    "v_and_or_b32 v252, v159, 48, v252\n"
    "v_mov_b32 v2, v252\n"
    "v_add_u32 v3, 24576, v252\n"
    "v_lshrrev_b32 v91, 3, v156\n"
    "v_lshl_add_u32 v87, v91, 6, 64\n"
    "v_add_u32 v87, 64, v87\n"
    "v_and_b32 v241, 1, v156\n"
    "v_lshlrev_b32 v241, 3, v241\n"
    "buffer_load_dwordx2 v[52:53], v242, s[28:31], s77 offen offset:0 nt\n"
    "buffer_load_dwordx2 v[54:55], v242, s[28:31], s77 offen offset:768 nt\n"
    "v_lshl_add_u32 v241, v91, 4, v241\n"
    "v_add_u32 v241, 1152, v241\n"
    "v_lshlrev_b32 v91, 24, v91\n"
    "v_lshl_add_u32 v91, v156, 3, v91\n"
    "s_lshl_b32 s73, s53, 4\n"
    "s_lshl_b32 s74, s59, 3\n"
    "s_add_u32 s73, s73, s74\n"
    "s_add_u32 s73, s73, 1152\n"
    "v_mov_b32 v240, 0x7fff0000\n"
    "v_writelane_b32 v240, s73, 0\n"
    "v_and_b32 v159, 48, v156\n"
    "s_mul_i32 s73, s79, 768\n"
    "s_sub_u32 s73, 131072, s73\n"
    "v_lshlrev_b32 v250, 1, v159\n"
    "v_add_u32 v250, s73, v250\n"
    "v_and_b32 v251, 3, v156\n"
    "v_lshl_add_u32 v251, v251, 7, v159\n"
    "s_add_u32 s73, s73, 256\n"
    "v_add_u32 v251, s73, v251\n"
    "v_and_b32 v159, 64, v1\n"
    "v_add_u32 v251, v159, v251\n"
    "v_bfe_i32 v159, v156, 2, 1\n"
    "v_and_b32 v159, 0xaaaa, v159\n"
    "v_or_b32 v253, 0x4444, v159\n"
    "s_mov_b32 s58, 0\n"
    "s_mov_b32 s59, 8\n"
    "s_waitcnt vmcnt(31)\n"
    "v_pk_min_i16 v4, v4, v10\n"
    "v_pk_min_i16 v5, v5, v11\n"
    "v_pk_min_i16 v6, v6, v12\n"
    "v_pk_min_i16 v7, v7, v13\n"
    "v_pk_min_i16 v8, v8, v14\n"
    "v_pk_min_i16 v9, v9, v15\n"
    "v_pk_min_i16 v16, v16, v18\n"
    "v_pk_min_i16 v17, v17, v19\n"
    "v_pk_min_i16 v4, v4, v7\n"
    "v_pk_min_i16 v5, v5, v8\n"
    "v_pk_min_i16 v6, v6, v9\n"
    "v_pk_min_i16 v16, v16, v17\n"
    "v_pk_min_i16 v4, v4, v5\n"
    "v_lshlrev_b32 v17, 16, v16\n"
    "v_pk_min_i16 v4, v4, v6\n"
    "v_min_i32 v16, v16, v17\n"
    "v_lshlrev_b32 v5, 16, v4\n"
    "v_mov_b32 v7, 155920\n"
    "v_min_i32 v4, v4, v5\n"
    "v_lshl_add_u32 v6, s84, 2, v7\n"
    "v_min_i32_dpp v16, v16, v16 row_shr:1\n"
    "v_min_i32_dpp v4, v4, v4 row_shr:1\n"
    "s_nop 0\n"
    "v_min_i32_dpp v16, v16, v16 row_shr:2\n"
    "v_min_i32_dpp v4, v4, v4 row_shr:2\n"
    "s_nop 0\n"
    "v_min_i32_dpp v16, v16, v16 row_shr:4\n"
    "v_min_i32_dpp v4, v4, v4 row_shr:4\n"
    "s_nop 0\n"
    "v_min_i32_dpp v16, v16, v16 row_shr:8\n"
    "v_min_i32_dpp v4, v4, v4 row_shr:8\n"
    "s_nop 0\n"
    "v_min_i32_dpp v16, v16, v16 row_bcast:15 row_mask:0xa\n"
    "v_min_i32_dpp v4, v4, v4 row_bcast:15 row_mask:0xa\n"
    "s_nop 0\n"
    "v_min_i32_dpp v16, v16, v16 row_bcast:31 row_mask:0xc\n"
    "v_min_i32_dpp v4, v4, v4 row_bcast:31 row_mask:0xc\n"
    "v_readlane_b32 s35, v16, 63\n"
    "v_readlane_b32 s73, v4, 63\n"
    "s_max_i32 s35, s35, 0\n"
    "s_and_b32 s35, s35, 0xffff0000\n"
    "v_mov_b32 v4, s73\n"
    "ds_write_b32 v6, v4\n"
    "v_mov_b32 v12, -126\n"
    "v_mov_b32 v13, 0x3dd53b95\n"
    "v_mov_b32 v80, 0\n"
    "s_waitcnt vmcnt(29)\n"
    "v_lshlrev_b32 v180, 16, v164\n"
    "v_and_b32 v181, 0xffff0000, v164\n"
    "v_lshlrev_b32 v182, 16, v168\n"
    "v_and_b32 v183, 0xffff0000, v168\n"
    "v_lshlrev_b32 v184, 16, v165\n"
    "v_and_b32 v185, 0xffff0000, v165\n"
    "v_lshlrev_b32 v186, 16, v169\n"
    "v_and_b32 v187, 0xffff0000, v169\n"
    "v_lshlrev_b32 v188, 16, v166\n"
    "v_and_b32 v189, 0xffff0000, v166\n"
    "v_lshlrev_b32 v190, 16, v170\n"
    "v_and_b32 v191, 0xffff0000, v170\n"
    "v_lshlrev_b32 v192, 16, v167\n"
    "v_and_b32 v193, 0xffff0000, v167\n"
    "v_lshlrev_b32 v194, 16, v171\n"
    "v_and_b32 v195, 0xffff0000, v171\n"
    "s_waitcnt lgkmcnt(0)\n"
    "s_barrier\n"
    "ds_read_b128 v[8:11], v7 offset:0\n"
    "buffer_load_dwordx2 v[56:57], v242, s[28:31], s77 offen offset:3072 nt\n"
    "buffer_load_dwordx2 v[58:59], v242, s[28:31], s77 offen offset:3840 nt\n"
    "buffer_load_dwordx4 v[136:139], v130, s[92:95], 0 offen offset:0\n"
    "buffer_load_dwordx4 v[140:143], v130, s[96:99], 0 offen offset:0\n"
    "v_accvgpr_write_b32 a0, 0\n"
    "v_accvgpr_write_b32 a1, 0\n"
    "v_accvgpr_write_b32 a2, 0\n"
    "v_accvgpr_write_b32 a3, 0\n"
    "v_accvgpr_write_b32 a4, 0\n"
    "v_accvgpr_write_b32 a5, 0\n"
    "v_accvgpr_write_b32 a6, 0\n"
    "v_accvgpr_write_b32 a7, 0\n"
    "v_accvgpr_write_b32 a8, 0\n"
    "v_accvgpr_write_b32 a9, 0\n"
    "v_accvgpr_write_b32 a10, 0\n"
    "v_accvgpr_write_b32 a11, 0\n"
    "v_accvgpr_write_b32 a12, 0\n"
    "v_accvgpr_write_b32 a13, 0\n"
    "v_accvgpr_write_b32 a14, 0\n"
    "v_accvgpr_write_b32 a15, 0\n"
    "v_accvgpr_write_b32 a16, 0\n"
    "v_accvgpr_write_b32 a17, 0\n"
    "v_accvgpr_write_b32 a18, 0\n"
    "v_accvgpr_write_b32 a19, 0\n"
    "v_accvgpr_write_b32 a20, 0\n"
    "v_accvgpr_write_b32 a21, 0\n"
    "v_accvgpr_write_b32 a22, 0\n"
    "v_accvgpr_write_b32 a23, 0\n"
    "v_accvgpr_write_b32 a24, 0\n"
    "v_accvgpr_write_b32 a25, 0\n"
    "v_accvgpr_write_b32 a26, 0\n"
    "v_accvgpr_write_b32 a27, 0\n"
    "v_accvgpr_write_b32 a28, 0\n"
    "v_accvgpr_write_b32 a29, 0\n"
    "v_accvgpr_write_b32 a30, 0\n"
    "v_accvgpr_write_b32 a31, 0\n"
    "v_accvgpr_write_b32 a32, 0\n"
    "v_accvgpr_write_b32 a33, 0\n"
    "v_accvgpr_write_b32 a34, 0\n"
    "v_accvgpr_write_b32 a35, 0\n"
    "v_accvgpr_write_b32 a36, 0\n"
    "v_accvgpr_write_b32 a37, 0\n"
    "v_accvgpr_write_b32 a38, 0\n"
    "v_accvgpr_write_b32 a39, 0\n"
    "v_accvgpr_write_b32 a40, 0\n"
    "v_accvgpr_write_b32 a41, 0\n"
    "v_accvgpr_write_b32 a42, 0\n"
    "v_accvgpr_write_b32 a43, 0\n"
    "v_accvgpr_write_b32 a44, 0\n"
    "v_accvgpr_write_b32 a45, 0\n"
    "v_accvgpr_write_b32 a46, 0\n"
    "v_accvgpr_write_b32 a47, 0\n"
    "v_accvgpr_write_b32 a48, 0\n"
    "v_accvgpr_write_b32 a49, 0\n"
    "v_accvgpr_write_b32 a50, 0\n"
    "v_accvgpr_write_b32 a51, 0\n"
    "v_accvgpr_write_b32 a52, 0\n"
    "v_accvgpr_write_b32 a53, 0\n"
    "v_accvgpr_write_b32 a54, 0\n"
    "v_accvgpr_write_b32 a55, 0\n"
    "v_accvgpr_write_b32 a56, 0\n"
    "v_accvgpr_write_b32 a57, 0\n"
    "v_accvgpr_write_b32 a58, 0\n"
    "v_accvgpr_write_b32 a59, 0\n"
    "s_waitcnt lgkmcnt(0)\n"
    "v_min3_i32 v4, v8, v9, v10\n"
    "v_min_i32 v4, v4, v11\n"
    "v_max_i32 v4, 0, v4\n"
    "v_and_b32 v4, 0xffff0000, v4\n"
    "v_mul_f32 v4, s35, v4\n"
    "v_mul_f32 v4, 0x3ed53b95, v4\n"
    "v_lshrrev_b32 v4, 23, v4\n"
    "v_sub_u32 v4, 2, v4\n"
    "v_med3_i32 v4, v4, 0, v12\n"
    "v_ldexp_f32 v13, v13, v4\n"
    "v_readfirstlane_b32 s89, v4\n"
    "v_readfirstlane_b32 s88, v13\n"
    "s_mul_i32 s89, s89, 0xff7fff80\n"
    "s_nop 0\n"
    "v_mul_f32 v180, s88, v180\n"
    "v_mul_f32 v181, s88, v181\n"
    "v_mul_f32 v184, s88, v184\n"
    "v_mul_f32 v185, s88, v185\n"
    "v_mul_f32 v188, s88, v188\n"
    "v_mul_f32 v189, s88, v189\n"
    "v_mul_f32 v192, s88, v192\n"
    "v_mul_f32 v193, s88, v193\n"
    "v_fmaak_f32 v182, v182, v180, 0x80730000\n"
    "v_fmaak_f32 v183, v183, v181, 0x80730000\n"
    "v_fmaak_f32 v186, v186, v184, 0x80730000\n"
    "v_fmaak_f32 v187, v187, v185, 0x80730000\n"
    "v_fmaak_f32 v190, v190, v188, 0x80730000\n"
    "v_fmaak_f32 v191, v191, v189, 0x80730000\n"
    "v_fmaak_f32 v194, v194, v192, 0x80730000\n"
    "v_fmaak_f32 v195, v195, v193, 0x80730000\n"
    "ds_write_b128 v160, v[180:183] offset:0\n"
    "ds_write_b128 v160, v[184:187] offset:16\n"
    "ds_write_b128 v160, v[188:191] offset:32\n"
    "ds_write_b128 v160, v[192:195] offset:48\n"
    "s_waitcnt vmcnt(17)\n"
    "s_add_u32 s54, s71, s60\n"
    "s_add_u32 s55, s71, s61\n"
    "v_cvt_f32_i32 v204, v204\n"
    "v_cvt_f32_i32 v205, v205\n"
    "v_cvt_f32_i32 v206, v206\n"
    "v_cvt_f32_i32 v207, v207\n"
    "v_cvt_f32_i32 v208, v208\n"
    "v_cvt_f32_i32 v209, v209\n"
    "v_cvt_f32_i32 v210, v210\n"
    "v_cvt_f32_i32 v211, v211\n"
    "v_cvt_f32_i32 v212, v212\n"
    "v_cvt_f32_i32 v213, v213\n"
    "v_cvt_f32_i32 v214, v214\n"
    "v_cvt_f32_i32 v215, v215\n"
    "ds_read_b128 v[92:95], v243 offset:0\n"
    "v_cvt_f32_i32 v216, v216\n"
    "v_cvt_f32_i32 v217, v217\n"
    "v_cvt_f32_i32 v218, v218\n"
    "v_cvt_f32_i32 v219, v219\n"
    "v_cvt_f32_i32 v220, v220\n"
    "v_cvt_f32_i32 v221, v221\n"
    "v_cvt_f32_i32 v222, v222\n"
    "v_cvt_f32_i32 v223, v223\n"
    "v_cvt_f32_i32 v224, v224\n"
    "v_cvt_f32_i32 v225, v225\n"
    "v_cvt_f32_i32 v226, v226\n"
    "v_cvt_f32_i32 v227, v227\n"
    "s_waitcnt lgkmcnt(0)\n"
    "v_fma_f32 v204, v204, v92, -v94\n"
    "v_fma_f32 v205, v205, v93, -v95\n"
    "v_fma_f32 v206, v206, v92, -v94\n"
    "v_fma_f32 v207, v207, v93, -v95\n"
    "v_cvt_pk_bf16_f32 v204, v204, v205\n"
    "buffer_load_dwordx2 v[60:61], v242, s[28:31], s78 offen offset:0 nt\n"
    "buffer_load_dwordx2 v[62:63], v242, s[28:31], s78 offen offset:768 nt\n"
    "buffer_load_dwordx4 v[4:7], v162, s[28:31], s57 offen offset:0\n"
    "buffer_load_dwordx4 v[8:11], v162, s[28:31], s57 offen offset:3072\n"
    "v_cvt_pk_bf16_f32 v206, v206, v207\n"
    "v_cvt_f32_i32 v228, v228\n"
    "v_cvt_f32_i32 v229, v229\n"
    "v_cvt_f32_i32 v230, v230\n"
    "v_cvt_f32_i32 v231, v231\n"
    "buffer_store_dword v204, v244, s[16:19], s54 offen offset:0 sc1\n"
    "buffer_store_dword v206, v244, s[16:19], s54 offen offset:128 sc1\n"
    "v_fma_f32 v208, v208, v92, -v94\n"
    "v_fma_f32 v209, v209, v93, -v95\n"
    "v_fma_f32 v210, v210, v92, -v94\n"
    "v_fma_f32 v211, v211, v93, -v95\n"
    "v_cvt_pk_bf16_f32 v208, v208, v209\n"
    "v_cvt_pk_bf16_f32 v210, v210, v211\n"
    "v_cvt_f32_i32 v232, v232\n"
    "v_cvt_f32_i32 v233, v233\n"
    "v_cvt_f32_i32 v234, v234\n"
    "v_cvt_f32_i32 v235, v235\n"
    "buffer_store_dword v208, v244, s[16:19], s54 offen offset:1024 sc1\n"
    "buffer_store_dword v210, v244, s[16:19], s54 offen offset:1152 sc1\n"
    "v_fma_f32 v212, v212, v92, -v94\n"
    "v_fma_f32 v213, v213, v93, -v95\n"
    "v_fma_f32 v214, v214, v92, -v94\n"
    "v_fma_f32 v215, v215, v93, -v95\n"
    "v_cvt_pk_bf16_f32 v212, v212, v213\n"
    "v_cvt_pk_bf16_f32 v214, v214, v215\n"
    "buffer_store_dword v212, v244, s[16:19], s54 offen offset:256 sc1\n"
    "buffer_store_dword v214, v244, s[16:19], s54 offen offset:384 sc1\n"
    "v_fma_f32 v216, v216, v92, -v94\n"
    "v_fma_f32 v217, v217, v93, -v95\n"
    "v_fma_f32 v218, v218, v92, -v94\n"
    "v_fma_f32 v219, v219, v93, -v95\n"
    "v_cvt_pk_bf16_f32 v216, v216, v217\n"
    "v_cvt_pk_bf16_f32 v218, v218, v219\n"
    "buffer_load_dwordx2 v[64:65], v242, s[28:31], s78 offen offset:3072 nt\n"
    "buffer_load_dwordx2 v[66:67], v242, s[28:31], s78 offen offset:3840 nt\n"
    "buffer_load_dwordx4 v[12:15], v162, s[28:31], s57 offen offset:128\n"
    "buffer_store_dword v216, v244, s[16:19], s54 offen offset:1280 sc1\n"
    "buffer_store_dword v218, v244, s[16:19], s54 offen offset:1408 sc1\n"
    "v_fma_f32 v220, v220, v92, -v94\n"
    "v_fma_f32 v221, v221, v93, -v95\n"
    "v_fma_f32 v222, v222, v92, -v94\n"
    "v_fma_f32 v223, v223, v93, -v95\n"
    "v_cvt_pk_bf16_f32 v220, v220, v221\n"
    "v_cvt_pk_bf16_f32 v222, v222, v223\n"
    "buffer_store_dword v220, v244, s[16:19], s55 offen offset:0 sc1\n"
    "buffer_store_dword v222, v244, s[16:19], s55 offen offset:128 sc1\n"
    "v_fma_f32 v224, v224, v92, -v94\n"
    "v_fma_f32 v225, v225, v93, -v95\n"
    "v_fma_f32 v226, v226, v92, -v94\n"
    "v_fma_f32 v227, v227, v93, -v95\n"
    "v_cvt_pk_bf16_f32 v224, v224, v225\n"
    "v_cvt_pk_bf16_f32 v226, v226, v227\n"
    "buffer_store_dword v224, v244, s[16:19], s55 offen offset:1024 sc1\n"
    "buffer_store_dword v226, v244, s[16:19], s55 offen offset:1152 sc1\n"
    "v_fma_f32 v228, v228, v92, -v94\n"
    "v_fma_f32 v229, v229, v93, -v95\n"
    "v_fma_f32 v230, v230, v92, -v94\n"
    "v_fma_f32 v231, v231, v93, -v95\n"
    "v_cvt_pk_bf16_f32 v228, v228, v229\n"
    "v_cvt_pk_bf16_f32 v230, v230, v231\n"
    "buffer_store_dword v228, v244, s[16:19], s55 offen offset:256 sc1\n"
    "buffer_store_dword v230, v244, s[16:19], s55 offen offset:384 sc1\n"
    "v_fma_f32 v232, v232, v92, -v94\n"
    "v_fma_f32 v233, v233, v93, -v95\n"
    "v_fma_f32 v234, v234, v92, -v94\n"
    "v_fma_f32 v235, v235, v93, -v95\n"
    "v_cvt_pk_bf16_f32 v232, v232, v233\n"
    "v_cvt_pk_bf16_f32 v234, v234, v235\n"
    "buffer_store_dword v232, v244, s[16:19], s55 offen offset:1280 sc1\n"
    "buffer_store_dword v234, v244, s[16:19], s55 offen offset:1408 sc1\n"
    "v_add_u32 v243, 512, v243\n"
    "s_add_u32 s71, s71, 98304\n"
    "s_add_u32 m0, s65, 24576\n"
    "s_add_u32 s72, s85, 0\n"
    "buffer_load_dwordx4 v88, s[20:23], s72 offen offset:0 nt lds\n"
    "buffer_load_dwordx4 v[16:19], v162, s[28:31], s57 offen offset:3200\n"
    "buffer_load_dwordx4 v[20:23], v162, s[28:31], s57 offen offset:256\n"
    "s_waitcnt vmcnt(38)\n"
    "v_add_co_u32 v238, vcc, 0x7f4a7c15, v238\n"
    "v_mov_b32 v159, 0x9e3779b9\n"
    "s_nop 0\n"
    "v_addc_co_u32 v239, vcc, v159, v239, vcc\n"
    "v_mov_b32 v246, 1\n"
    "s_waitcnt vmcnt(39)\n"
    "v_lshlrev_b32 v180, 16, v124\n"
    "v_and_b32 v181, 0xffff0000, v124\n"
    "v_lshlrev_b32 v182, 16, v125\n"
    "v_and_b32 v183, 0xffff0000, v125\n"
    "v_lshlrev_b32 v184, 16, v126\n"
    "v_and_b32 v185, 0xffff0000, v126\n"
    "v_lshlrev_b32 v186, 16, v127\n"
    "v_and_b32 v187, 0xffff0000, v127\n"
    "v_mul_f32 v180, s88, v180\n"
    "v_mul_f32 v181, s88, v181\n"
    "v_mul_f32 v182, s88, v182\n"
    "v_mul_f32 v183, s88, v183\n"
    "v_fmaak_f32 v184, v184, v180, 0x80730000\n"
    "v_fmaak_f32 v185, v185, v181, 0x80730000\n"
    "v_fmaak_f32 v186, v186, v182, 0x80730000\n"
    "v_fmaak_f32 v187, v187, v183, 0x80730000\n"
    "ds_write_b128 v131, v[180:183] offset:0\n"
    "ds_write_b128 v131, v[184:187] offset:1024\n"
    "ds_read_b128 v[180:183], v132 offset:0\n"
    "ds_read_b128 v[184:187], v132 offset:1024\n"
    "ds_read_b128 v[188:191], v132 offset:128\n"
    "ds_read_b128 v[192:195], v132 offset:1152\n"
    "ds_read_b128 v[196:199], v132 offset:256\n"
    "ds_read_b128 v[200:203], v132 offset:1280\n"
    "v_accvgpr_write_b32 a60, 0\n"
    "v_accvgpr_write_b32 a61, 0\n"
    "v_accvgpr_write_b32 a62, 0\n"
    "v_accvgpr_write_b32 a63, 0\n"
    "v_accvgpr_write_b32 a64, 0\n"
    "v_accvgpr_write_b32 a65, 0\n"
    "v_accvgpr_write_b32 a66, 0\n"
    "v_accvgpr_write_b32 a67, 0\n"
    "v_accvgpr_write_b32 a68, 0\n"
    "v_accvgpr_write_b32 a69, 0\n"
    "s_add_u32 m0, s65, 25600\n"
    "s_add_u32 s72, s85, 1024\n"
    "buffer_load_dwordx4 v89, s[20:23], s72 offen offset:0 nt lds\n"
    "buffer_load_dwordx4 v[24:27], v162, s[28:31], s57 offen offset:3328\n"
    "v_accvgpr_write_b32 a70, 0\n"
    "v_accvgpr_write_b32 a71, 0\n"
    "v_accvgpr_write_b32 a72, 0\n"
    "v_accvgpr_write_b32 a73, 0\n"
    "v_accvgpr_write_b32 a74, 0\n"
    "v_accvgpr_write_b32 a75, 0\n"
    "v_accvgpr_write_b32 a76, 0\n"
    "v_accvgpr_write_b32 a77, 0\n"
    "v_accvgpr_write_b32 a78, 0\n"
    "v_accvgpr_write_b32 a79, 0\n"
    "v_accvgpr_write_b32 a80, 0\n"
    "v_accvgpr_write_b32 a81, 0\n"
    "v_accvgpr_write_b32 a82, 0\n"
    "v_accvgpr_write_b32 a83, 0\n"
    "v_accvgpr_write_b32 a84, 0\n"
    "v_accvgpr_write_b32 a85, 0\n"
    "v_accvgpr_write_b32 a86, 0\n"
    "v_accvgpr_write_b32 a87, 0\n"
    "v_accvgpr_write_b32 a88, 0\n"
    "v_accvgpr_write_b32 a89, 0\n"
    "v_accvgpr_write_b32 a90, 0\n"
    "v_accvgpr_write_b32 a91, 0\n"
    "v_accvgpr_write_b32 a92, 0\n"
    "v_accvgpr_write_b32 a93, 0\n"
    "v_accvgpr_write_b32 a94, 0\n"
    "v_accvgpr_write_b32 a95, 0\n"
    "v_accvgpr_write_b32 a96, 0\n"
    "v_accvgpr_write_b32 a97, 0\n"
    "v_accvgpr_write_b32 a98, 0\n"
    "v_accvgpr_write_b32 a99, 0\n"
    "v_accvgpr_write_b32 a100, 0\n"
    "v_accvgpr_write_b32 a101, 0\n"
    "v_accvgpr_write_b32 a102, 0\n"
    "v_accvgpr_write_b32 a103, 0\n"
    "v_accvgpr_write_b32 a104, 0\n"
    "v_accvgpr_write_b32 a105, 0\n"
    "v_accvgpr_write_b32 a106, 0\n"
    "v_accvgpr_write_b32 a107, 0\n"
    "v_accvgpr_write_b32 a108, 0\n"
    "v_accvgpr_write_b32 a109, 0\n"
    "v_accvgpr_write_b32 a110, 0\n"
    "s_add_u32 m0, s65, 26624\n"
    "s_add_u32 s72, s85, 2048\n"
    "buffer_load_dwordx4 v90, s[20:23], s72 offen offset:0 nt lds\n"
    "buffer_load_dwordx4 v[100:103], v162, s[28:31], s66 offen offset:0\n"
    "buffer_load_dwordx4 v[104:107], v162, s[28:31], s66 offen offset:3072\n"
    "v_accvgpr_write_b32 a111, 0\n"
    "v_accvgpr_write_b32 a112, 0\n"
    "v_accvgpr_write_b32 a113, 0\n"
    "v_accvgpr_write_b32 a114, 0\n"
    "v_accvgpr_write_b32 a115, 0\n"
    "v_accvgpr_write_b32 a116, 0\n"
    "v_accvgpr_write_b32 a117, 0\n"
    "v_accvgpr_write_b32 a118, 0\n"
    "v_accvgpr_write_b32 a119, 0\n"
    "v_accvgpr_write_b32 a120, 0\n"
    "v_accvgpr_write_b32 a121, 0\n"
    "v_accvgpr_write_b32 a122, 0\n"
    "v_accvgpr_write_b32 a123, 0\n"
    "v_accvgpr_write_b32 a124, 0\n"
    "v_accvgpr_write_b32 a125, 0\n"
    "v_accvgpr_write_b32 a126, 0\n"
    "v_accvgpr_write_b32 a127, 0\n"
    "v_accvgpr_write_b32 a128, 0\n"
    "v_accvgpr_write_b32 a129, 0\n"
    "v_accvgpr_write_b32 a130, 0\n"
    "v_accvgpr_write_b32 a131, 0\n"
    "v_accvgpr_write_b32 a132, 0\n"
    "v_accvgpr_write_b32 a133, 0\n"
    "v_accvgpr_write_b32 a134, 0\n"
    "v_accvgpr_write_b32 a135, 0\n"
    "v_accvgpr_write_b32 a136, 0\n"
    "v_accvgpr_write_b32 a137, 0\n"
    "v_accvgpr_write_b32 a138, 0\n"
    "v_accvgpr_write_b32 a139, 0\n"
    "v_accvgpr_write_b32 a140, 0\n"
    "v_accvgpr_write_b32 a141, 0\n"
    "v_accvgpr_write_b32 a142, 0\n"
    "v_accvgpr_write_b32 a143, 0\n"
    "v_accvgpr_write_b32 a144, 0\n"
    "v_accvgpr_write_b32 a145, 0\n"
    "v_accvgpr_write_b32 a146, 0\n"
    "v_accvgpr_write_b32 a147, 0\n"
    "v_accvgpr_write_b32 a148, 0\n"
    "v_accvgpr_write_b32 a149, 0\n"
    "v_accvgpr_write_b32 a150, 0\n"
    "s_add_u32 m0, s65, 27648\n"
    "s_add_u32 s72, s85, 3072\n"
    "buffer_load_dwordx4 v88, s[20:23], s72 offen offset:0 nt lds\n"
    "buffer_load_dwordx4 v[108:111], v162, s[28:31], s66 offen offset:128\n"
    "v_accvgpr_write_b32 a151, 0\n"
    "v_accvgpr_write_b32 a152, 0\n"
    "v_accvgpr_write_b32 a153, 0\n"
    "v_accvgpr_write_b32 a154, 0\n"
    "v_accvgpr_write_b32 a155, 0\n"
    "v_accvgpr_write_b32 a156, 0\n"
    "v_accvgpr_write_b32 a157, 0\n"
    "v_accvgpr_write_b32 a158, 0\n"
    "v_accvgpr_write_b32 a159, 0\n"
    "v_accvgpr_write_b32 a160, 0\n"
    "v_accvgpr_write_b32 a161, 0\n"
    "v_accvgpr_write_b32 a162, 0\n"
    "v_accvgpr_write_b32 a163, 0\n"
    "v_accvgpr_write_b32 a164, 0\n"
    "v_accvgpr_write_b32 a165, 0\n"
    "v_accvgpr_write_b32 a166, 0\n"
    "v_accvgpr_write_b32 a167, 0\n"
    "v_accvgpr_write_b32 a168, 0\n"
    "v_accvgpr_write_b32 a169, 0\n"
    "v_accvgpr_write_b32 a170, 0\n"
    "v_accvgpr_write_b32 a171, 0\n"
    "v_accvgpr_write_b32 a172, 0\n"
    "v_accvgpr_write_b32 a173, 0\n"
    "v_accvgpr_write_b32 a174, 0\n"
    "v_accvgpr_write_b32 a175, 0\n"
    "v_accvgpr_write_b32 a176, 0\n"
    "v_accvgpr_write_b32 a177, 0\n"
    "v_accvgpr_write_b32 a178, 0\n"
    "v_accvgpr_write_b32 a179, 0\n"
    "v_accvgpr_write_b32 a180, 0\n"
    "v_accvgpr_write_b32 a181, 0\n"
    "v_accvgpr_write_b32 a182, 0\n"
    "v_accvgpr_write_b32 a183, 0\n"
    "v_accvgpr_write_b32 a184, 0\n"
    "v_accvgpr_write_b32 a185, 0\n"
    "v_accvgpr_write_b32 a186, 0\n"
    "v_accvgpr_write_b32 a187, 0\n"
    "v_accvgpr_write_b32 a188, 0\n"
    "v_accvgpr_write_b32 a189, 0\n"
    "v_accvgpr_write_b32 a190, 0\n"
    "v_accvgpr_write_b32 a191, 0\n"
    "s_waitcnt lgkmcnt(0)\n"
    "s_waitcnt vmcnt(10)\n"
    "ds_add_rtn_u32 v245, v247, v246\n"
    "s_lshl_b32 s73, s53, 6\n"
    "s_add_u32 s73, s73, 128\n"
    "s_waitcnt lgkmcnt(0)\n"
    "v_readfirstlane_b32 s74, v245\n"
    "s_nop 0\n"
    "s_cmp_eq_u32 s74, 3\n"
    "s_cselect_b32 s73, s73, 0x7fff0000\n"
    "buffer_store_dwordx2 v[238:239], v91, s[36:39], s73 offen offset:0 sc1\n"
    "s_waitcnt vmcnt(22)\n"
    "s_add_u32 m0, s65, 28672\n"
    "s_add_u32 s72, s85, 4096\n"
    "buffer_load_dwordx4 v89, s[20:23], s72 offen offset:0 nt lds\n"
    "buffer_load_dwordx4 v[112:115], v162, s[28:31], s66 offen offset:3200\n"
    "buffer_load_dwordx4 v[116:119], v162, s[28:31], s66 offen offset:256\n"
    "s_add_u32 s54, s71, s60\n"
    "s_add_u32 s55, s71, s61\n"
    "v_cvt_f32_i32 v36, v36\n"
    "v_cvt_f32_i32 v37, v37\n"
    "v_cvt_f32_i32 v38, v38\n"
    "v_cvt_f32_i32 v39, v39\n"
    "v_cvt_f32_i32 v40, v40\n"
    "v_cvt_f32_i32 v41, v41\n"
    "v_cvt_f32_i32 v42, v42\n"
    "v_cvt_f32_i32 v43, v43\n"
    "v_cvt_f32_i32 v44, v44\n"
    "v_cvt_f32_i32 v45, v45\n"
    "v_cvt_f32_i32 v46, v46\n"
    "v_cvt_f32_i32 v47, v47\n"
    "ds_read_b128 v[92:95], v243 offset:0\n"
    "v_cvt_f32_i32 v48, v48\n"
    "v_cvt_f32_i32 v49, v49\n"
    "v_cvt_f32_i32 v50, v50\n"
    "v_cvt_f32_i32 v51, v51\n"
    "v_cvt_f32_i32 v52, v52\n"
    "v_cvt_f32_i32 v53, v53\n"
    "v_cvt_f32_i32 v54, v54\n"
    "v_cvt_f32_i32 v55, v55\n"
    "v_cvt_f32_i32 v56, v56\n"
    "v_cvt_f32_i32 v57, v57\n"
    "v_cvt_f32_i32 v58, v58\n"
    "v_cvt_f32_i32 v59, v59\n"
    "s_waitcnt lgkmcnt(0)\n"
    "v_fma_f32 v36, v36, v92, -v94\n"
    "v_fma_f32 v37, v37, v93, -v95\n"
    "v_fma_f32 v38, v38, v92, -v94\n"
    "v_fma_f32 v39, v39, v93, -v95\n"
    "v_cvt_pk_bf16_f32 v36, v36, v37\n"
    "v_cvt_pk_bf16_f32 v38, v38, v39\n"
    "v_cvt_f32_i32 v60, v60\n"
    "v_cvt_f32_i32 v61, v61\n"
    "v_cvt_f32_i32 v62, v62\n"
    "v_cvt_f32_i32 v63, v63\n"
    "buffer_store_dword v36, v244, s[16:19], s54 offen offset:0 sc1\n"
    "buffer_store_dword v38, v244, s[16:19], s54 offen offset:128 sc1\n"
    "v_fma_f32 v40, v40, v92, -v94\n"
    "v_fma_f32 v41, v41, v93, -v95\n"
    "v_fma_f32 v42, v42, v92, -v94\n"
    "v_fma_f32 v43, v43, v93, -v95\n"
    "v_cvt_pk_bf16_f32 v40, v40, v41\n"
    "v_cvt_pk_bf16_f32 v42, v42, v43\n"
    "v_cvt_f32_i32 v64, v64\n"
    "v_cvt_f32_i32 v65, v65\n"
    "v_cvt_f32_i32 v66, v66\n"
    "v_cvt_f32_i32 v67, v67\n"
    "s_add_u32 m0, s65, 29696\n"
    "s_add_u32 s72, s85, 5120\n"
    "buffer_load_dwordx4 v90, s[20:23], s72 offen offset:0 nt lds\n"
    "buffer_load_dwordx4 v[120:123], v162, s[28:31], s66 offen offset:3328\n"
    "buffer_store_dword v40, v244, s[16:19], s54 offen offset:1024 sc1\n"
    "buffer_store_dword v42, v244, s[16:19], s54 offen offset:1152 sc1\n"
    "v_fma_f32 v44, v44, v92, -v94\n"
    "v_fma_f32 v45, v45, v93, -v95\n"
    "v_fma_f32 v46, v46, v92, -v94\n"
    "v_fma_f32 v47, v47, v93, -v95\n"
    "v_cvt_pk_bf16_f32 v44, v44, v45\n"
    "v_cvt_pk_bf16_f32 v46, v46, v47\n"
    "buffer_store_dword v44, v244, s[16:19], s54 offen offset:256 sc1\n"
    "buffer_store_dword v46, v244, s[16:19], s54 offen offset:384 sc1\n"
    "v_fma_f32 v48, v48, v92, -v94\n"
    "v_fma_f32 v49, v49, v93, -v95\n"
    "v_fma_f32 v50, v50, v92, -v94\n"
    "v_fma_f32 v51, v51, v93, -v95\n"
    "v_cvt_pk_bf16_f32 v48, v48, v49\n"
    "v_cvt_pk_bf16_f32 v50, v50, v51\n"
    "buffer_store_dword v48, v244, s[16:19], s54 offen offset:1280 sc1\n"
    "buffer_store_dword v50, v244, s[16:19], s54 offen offset:1408 sc1\n"
    "v_fma_f32 v52, v52, v92, -v94\n"
    "v_fma_f32 v53, v53, v93, -v95\n"
    "v_fma_f32 v54, v54, v92, -v94\n"
    "v_fma_f32 v55, v55, v93, -v95\n"
    "v_cvt_pk_bf16_f32 v52, v52, v53\n"
    "v_cvt_pk_bf16_f32 v54, v54, v55\n"
    "buffer_store_dword v52, v244, s[16:19], s55 offen offset:0 sc1\n"
    "buffer_store_dword v54, v244, s[16:19], s55 offen offset:128 sc1\n"
    "v_fma_f32 v56, v56, v92, -v94\n"
    "v_fma_f32 v57, v57, v93, -v95\n"
    "v_fma_f32 v58, v58, v92, -v94\n"
    "v_fma_f32 v59, v59, v93, -v95\n"
    "v_cvt_pk_bf16_f32 v56, v56, v57\n"
    "v_cvt_pk_bf16_f32 v58, v58, v59\n"
    "buffer_store_dword v56, v244, s[16:19], s55 offen offset:1024 sc1\n"
    "buffer_store_dword v58, v244, s[16:19], s55 offen offset:1152 sc1\n"
    "v_fma_f32 v60, v60, v92, -v94\n"
    "v_fma_f32 v61, v61, v93, -v95\n"
    "v_fma_f32 v62, v62, v92, -v94\n"
    "v_fma_f32 v63, v63, v93, -v95\n"
    "v_cvt_pk_bf16_f32 v60, v60, v61\n"
    "v_cvt_pk_bf16_f32 v62, v62, v63\n"
    "buffer_store_dword v60, v244, s[16:19], s55 offen offset:256 sc1\n"
    "buffer_store_dword v62, v244, s[16:19], s55 offen offset:384 sc1\n"
    "v_fma_f32 v64, v64, v92, -v94\n"
    "v_fma_f32 v65, v65, v93, -v95\n"
    "v_fma_f32 v66, v66, v92, -v94\n"
    "v_fma_f32 v67, v67, v93, -v95\n"
    "v_cvt_pk_bf16_f32 v64, v64, v65\n"
    "v_cvt_pk_bf16_f32 v66, v66, v67\n"
    "buffer_store_dword v64, v244, s[16:19], s55 offen offset:1280 sc1\n"
    "buffer_store_dword v66, v244, s[16:19], s55 offen offset:1408 sc1\n"
    "v_add_u32 v243, 512, v243\n"
    "s_add_u32 m0, s65, 73728\n"
    "s_add_u32 s72, s85, 6144\n"
    "buffer_load_dwordx4 v88, s[20:23], s72 offen offset:0 nt lds\n"
    "s_waitcnt vmcnt(56)\n"
    "v_lshlrev_b32 v204, 16, v136\n"
    "v_and_b32 v205, 0xffff0000, v136\n"
    "v_lshlrev_b32 v206, 16, v137\n"
    "v_and_b32 v207, 0xffff0000, v137\n"
    "v_lshlrev_b32 v208, 16, v138\n"
    "v_and_b32 v209, 0xffff0000, v138\n"
    "v_lshlrev_b32 v210, 16, v139\n"
    "v_and_b32 v211, 0xffff0000, v139\n"
    "v_log_f32 v68, v204\n"
    "v_log_f32 v69, v205\n"
    "v_log_f32 v70, v206\n"
    "v_log_f32 v71, v207\n"
    "v_log_f32 v72, v208\n"
    "v_log_f32 v73, v209\n"
    "v_log_f32 v74, v210\n"
    "v_log_f32 v75, v211\n"
    "v_rcp_f32 v204, v204\n"
    "v_rcp_f32 v205, v205\n"
    "v_rcp_f32 v206, v206\n"
    "v_rcp_f32 v207, v207\n"
    "v_rcp_f32 v208, v208\n"
    "v_rcp_f32 v209, v209\n"
    "v_rcp_f32 v210, v210\n"
    "v_rcp_f32 v211, v211\n"
    "s_mov_b32 s73, 0x41a00000\n"
    "v_mov_b32 v228, 0x7fc00000\n"
    "v_cmp_le_f32 vcc, |v68|, s73\n"
    "s_nop 1\n"
    "v_cndmask_b32 v68, v228, v68, vcc\n"
    "v_cmp_le_f32 vcc, |v69|, s73\n"
    "s_nop 1\n"
    "v_cndmask_b32 v69, v228, v69, vcc\n"
    "v_cmp_le_f32 vcc, |v70|, s73\n"
    "s_nop 1\n"
    "v_cndmask_b32 v70, v228, v70, vcc\n"
    "v_cmp_le_f32 vcc, |v71|, s73\n"
    "s_nop 1\n"
    "v_cndmask_b32 v71, v228, v71, vcc\n"
    "s_add_u32 m0, s65, 74752\n"
    "s_add_u32 s72, s85, 7168\n"
    "buffer_load_dwordx4 v89, s[20:23], s72 offen offset:0 nt lds\n"
    "v_cmp_le_f32 vcc, |v72|, s73\n"
    "s_nop 1\n"
    "v_cndmask_b32 v72, v228, v72, vcc\n"
    "v_cmp_le_f32 vcc, |v73|, s73\n"
    "s_nop 1\n"
    "v_cndmask_b32 v73, v228, v73, vcc\n"
    "v_cmp_le_f32 vcc, |v74|, s73\n"
    "s_nop 1\n"
    "v_cndmask_b32 v74, v228, v74, vcc\n"
    "v_cmp_le_f32 vcc, |v75|, s73\n"
    "s_nop 1\n"
    "v_cndmask_b32 v75, v228, v75, vcc\n"
    "v_cvt_pk_bf16_f32 v220, v204, v205\n"
    "v_cvt_pk_bf16_f32 v221, v206, v207\n"
    "v_cvt_pk_bf16_f32 v222, v208, v209\n"
    "v_cvt_pk_bf16_f32 v223, v210, v211\n"
    "v_lshlrev_b32 v212, 16, v220\n"
    "v_and_b32 v213, 0xffff0000, v220\n"
    "v_lshlrev_b32 v214, 16, v221\n"
    "v_and_b32 v215, 0xffff0000, v221\n"
    "v_lshlrev_b32 v216, 16, v222\n"
    "v_and_b32 v217, 0xffff0000, v222\n"
    "v_lshlrev_b32 v218, 16, v223\n"
    "v_and_b32 v219, 0xffff0000, v223\n"
    "v_sub_f32 v204, v204, v212\n"
    "v_sub_f32 v205, v205, v213\n"
    "v_sub_f32 v206, v206, v214\n"
    "v_sub_f32 v207, v207, v215\n"
    "v_sub_f32 v208, v208, v216\n"
    "v_sub_f32 v209, v209, v217\n"
    "v_sub_f32 v210, v210, v218\n"
    "v_sub_f32 v211, v211, v219\n"
    "v_cvt_pk_bf16_f32 v224, v204, v205\n"
    "v_cvt_pk_bf16_f32 v225, v206, v207\n"
    "v_cvt_pk_bf16_f32 v226, v208, v209\n"
    "v_cvt_pk_bf16_f32 v227, v210, v211\n"
    "v_mov_b32 v204, 0\n"
    "v_mov_b32 v205, 0\n"
    "v_mov_b32 v206, 0\n"
    "s_add_u32 m0, s65, 75776\n"
    "s_add_u32 s72, s85, 8192\n"
    "buffer_load_dwordx4 v90, s[20:23], s72 offen offset:0 nt lds\n"
    "v_mov_b32 v207, 0\n"
    "v_swap_b32 v221, v222\n"
    "v_swap_b32 v225, v226\n"
    "v_swap_b32 v141, v142\n"
    "ds_write2_b64 v144, v[220:221], v[222:223] offset0:32 offset1:40\n"
    "ds_write2_b64 v144, v[224:225], v[226:227] offset0:48 offset1:56\n"
    "ds_write2_b64 v144, v[140:141], v[142:143] offset0:64 offset1:72\n"
    "ds_write2_b64 v144, v[204:205], v[206:207] offset0:80 offset1:88\n"
    "s_waitcnt vmcnt(30)\n"
    "v_cvt_f32_i32 v4, v4\n"
    "v_cvt_f32_i32 v5, v5\n"
    "v_cvt_f32_i32 v6, v6\n"
    "v_cvt_f32_i32 v7, v7\n"
    "v_cvt_f32_i32 v8, v8\n"
    "v_cvt_f32_i32 v9, v9\n"
    "v_cvt_f32_i32 v10, v10\n"
    "v_cvt_f32_i32 v11, v11\n"
    "v_cvt_f32_i32 v12, v12\n"
    "v_cvt_f32_i32 v13, v13\n"
    "v_cvt_f32_i32 v14, v14\n"
    "v_cvt_f32_i32 v15, v15\n"
    "v_cvt_f32_i32 v16, v16\n"
    "v_cvt_f32_i32 v17, v17\n"
    "v_cvt_f32_i32 v18, v18\n"
    "v_cvt_f32_i32 v19, v19\n"
    "v_cvt_f32_i32 v20, v20\n"
    "v_cvt_f32_i32 v21, v21\n"
    "v_cvt_f32_i32 v22, v22\n"
    "v_cvt_f32_i32 v23, v23\n"
    "v_cvt_f32_i32 v24, v24\n"
    "v_cvt_f32_i32 v25, v25\n"
    "v_cvt_f32_i32 v26, v26\n"
    "v_cvt_f32_i32 v27, v27\n"
    "v_fma_f32 v4, v4, v180, -v184\n"
    "v_fma_f32 v5, v5, v181, -v185\n"
    "v_fma_f32 v6, v6, v182, -v186\n"
    "s_add_u32 m0, s65, 76800\n"
    "s_add_u32 s72, s85, 9216\n"
    "buffer_load_dwordx4 v88, s[20:23], s72 offen offset:0 nt lds\n"
    "v_fma_f32 v7, v7, v183, -v187\n"
    "v_fma_f32 v8, v8, v180, -v184\n"
    "v_fma_f32 v9, v9, v181, -v185\n"
    "v_fma_f32 v10, v10, v182, -v186\n"
    "v_fma_f32 v11, v11, v183, -v187\n"
    "v_cvt_pk_bf16_f32 v4, v4, v5\n"
    "v_cvt_pk_bf16_f32 v5, v6, v7\n"
    "v_cvt_pk_bf16_f32 v6, v8, v9\n"
    "v_cvt_pk_bf16_f32 v7, v10, v11\n"
    "ds_write_b64 v163, v[4:5] offset:0\n"
    "ds_write_b64 v163, v[6:7] offset:1024\n"
    "v_fma_f32 v12, v12, v188, -v192\n"
    "v_fma_f32 v13, v13, v189, -v193\n"
    "v_fma_f32 v14, v14, v190, -v194\n"
    "v_fma_f32 v15, v15, v191, -v195\n"
    "v_fma_f32 v16, v16, v188, -v192\n"
    "v_fma_f32 v17, v17, v189, -v193\n"
    "v_fma_f32 v18, v18, v190, -v194\n"
    "v_fma_f32 v19, v19, v191, -v195\n"
    "v_cvt_pk_bf16_f32 v12, v12, v13\n"
    "v_cvt_pk_bf16_f32 v13, v14, v15\n"
    "v_cvt_pk_bf16_f32 v14, v16, v17\n"
    "v_cvt_pk_bf16_f32 v15, v18, v19\n"
    "ds_write_b64 v163, v[12:13] offset:2048\n"
    "ds_write_b64 v163, v[14:15] offset:3072\n"
    "v_fma_f32 v20, v20, v196, -v200\n"
    "v_fma_f32 v21, v21, v197, -v201\n"
    "v_fma_f32 v22, v22, v198, -v202\n"
    "v_fma_f32 v23, v23, v199, -v203\n"
    "v_fma_f32 v24, v24, v196, -v200\n"
    "v_fma_f32 v25, v25, v197, -v201\n"
    "v_fma_f32 v26, v26, v198, -v202\n"
    "v_fma_f32 v27, v27, v199, -v203\n"
    "v_cvt_pk_bf16_f32 v20, v20, v21\n"
    "v_cvt_pk_bf16_f32 v21, v22, v23\n"
    "s_add_u32 m0, s65, 77824\n"
    "s_add_u32 s72, s85, 10240\n"
    "buffer_load_dwordx4 v89, s[20:23], s72 offen offset:0 nt lds\n"
    "v_cvt_pk_bf16_f32 v22, v24, v25\n"
    "v_cvt_pk_bf16_f32 v23, v26, v27\n"
    "ds_write_b64 v163, v[20:21] offset:4096\n"
    "ds_write_b64 v163, v[22:23] offset:5120\n"
    "s_waitcnt vmcnt(19)\n"
    "v_cvt_f32_i32 v100, v100\n"
    "v_cvt_f32_i32 v101, v101\n"
    "v_cvt_f32_i32 v102, v102\n"
    "v_cvt_f32_i32 v103, v103\n"
    "v_cvt_f32_i32 v104, v104\n"
    "v_cvt_f32_i32 v105, v105\n"
    "v_cvt_f32_i32 v106, v106\n"
    "v_cvt_f32_i32 v107, v107\n"
    "v_cvt_f32_i32 v108, v108\n"
    "v_cvt_f32_i32 v109, v109\n"
    "v_cvt_f32_i32 v110, v110\n"
    "v_cvt_f32_i32 v111, v111\n"
    "v_cvt_f32_i32 v112, v112\n"
    "v_cvt_f32_i32 v113, v113\n"
    "v_cvt_f32_i32 v114, v114\n"
    "v_cvt_f32_i32 v115, v115\n"
    "v_cvt_f32_i32 v116, v116\n"
    "v_cvt_f32_i32 v117, v117\n"
    "v_cvt_f32_i32 v118, v118\n"
    "v_cvt_f32_i32 v119, v119\n"
    "v_cvt_f32_i32 v120, v120\n"
    "v_cvt_f32_i32 v121, v121\n"
    "v_cvt_f32_i32 v122, v122\n"
    "v_cvt_f32_i32 v123, v123\n"
    "v_fma_f32 v100, v100, v180, -v184\n"
    "v_fma_f32 v101, v101, v181, -v185\n"
    "v_fma_f32 v102, v102, v182, -v186\n"
    "v_fma_f32 v103, v103, v183, -v187\n"
    "v_fma_f32 v104, v104, v180, -v184\n"
    "v_fma_f32 v105, v105, v181, -v185\n"
    "s_add_u32 m0, s65, 78848\n"
    "s_add_u32 s72, s85, 11264\n"
    "buffer_load_dwordx4 v90, s[20:23], s72 offen offset:0 nt lds\n"
    "v_fma_f32 v106, v106, v182, -v186\n"
    "v_fma_f32 v107, v107, v183, -v187\n"
    "v_cvt_pk_bf16_f32 v100, v100, v101\n"
    "v_cvt_pk_bf16_f32 v101, v102, v103\n"
    "v_cvt_pk_bf16_f32 v102, v104, v105\n"
    "v_cvt_pk_bf16_f32 v103, v106, v107\n"
    "ds_write_b64 v163, v[100:101] offset:12288\n"
    "ds_write_b64 v163, v[102:103] offset:13312\n"
    "v_fma_f32 v108, v108, v188, -v192\n"
    "v_fma_f32 v109, v109, v189, -v193\n"
    "v_fma_f32 v110, v110, v190, -v194\n"
    "v_fma_f32 v111, v111, v191, -v195\n"
    "v_fma_f32 v112, v112, v188, -v192\n"
    "v_fma_f32 v113, v113, v189, -v193\n"
    "v_fma_f32 v114, v114, v190, -v194\n"
    "v_fma_f32 v115, v115, v191, -v195\n"
    "v_cvt_pk_bf16_f32 v108, v108, v109\n"
    "v_cvt_pk_bf16_f32 v109, v110, v111\n"
    "v_cvt_pk_bf16_f32 v110, v112, v113\n"
    "v_cvt_pk_bf16_f32 v111, v114, v115\n"
    "ds_write_b64 v163, v[108:109] offset:14336\n"
    "ds_write_b64 v163, v[110:111] offset:15360\n"
    "v_fma_f32 v116, v116, v196, -v200\n"
    "v_fma_f32 v117, v117, v197, -v201\n"
    "v_fma_f32 v118, v118, v198, -v202\n"
    "v_fma_f32 v119, v119, v199, -v203\n"
    "v_fma_f32 v120, v120, v196, -v200\n"
    "v_fma_f32 v121, v121, v197, -v201\n"
    "v_fma_f32 v122, v122, v198, -v202\n"
    "v_fma_f32 v123, v123, v199, -v203\n"
    "v_cvt_pk_bf16_f32 v116, v116, v117\n"
    "v_cvt_pk_bf16_f32 v117, v118, v119\n"
    "v_cvt_pk_bf16_f32 v118, v120, v121\n"
    "v_cvt_pk_bf16_f32 v119, v122, v123\n"
    "ds_write_b64 v163, v[116:117] offset:16384\n"
    "ds_write_b64 v163, v[118:119] offset:17408\n"
    "s_waitcnt vmcnt(21)\n"
    "ds_read_b128 v[36:39], v154 offset:0\n"
    "ds_read_b128 v[40:43], v155 offset:0\n"
    "ds_read_b128 v[44:47], v154 offset:128\n"
    "ds_read_b128 v[48:51], v155 offset:128\n"
    "ds_read_b128 v[52:55], v154 offset:256\n"
    "ds_read_b128 v[56:59], v155 offset:256\n"
    "s_waitcnt lgkmcnt(0)\n"
    "s_barrier\n"
    "ds_read_b128 v[156:159], v2 offset:0\n"
    "ds_read_b128 v[160:163], v2 offset:2048\n"
    "ds_read_b128 v[164:167], v2 offset:4096\n"
    "ds_read_b128 v[168:171], v2 offset:6144\n"
    "ds_read_b128 v[172:175], v2 offset:8192\n"
    "ds_read_b128 v[176:179], v2 offset:10240\n"
    "ds_read_b128 v[180:183], v2 offset:1024\n"
    "ds_read_b128 v[184:187], v2 offset:3072\n"
    "ds_read_b128 v[188:191], v2 offset:5120\n"
    "ds_read_b128 v[192:195], v2 offset:7168\n"
    "ds_read_b128 v[196:199], v2 offset:9216\n"
    "ds_read_b128 v[200:203], v2 offset:11264\n"
    "s_add_u32 m0, s65, 49152\n"
    "s_add_u32 s72, s85, 12288\n"
    "buffer_load_dwordx4 v88, s[20:23], s72 offen offset:0 nt lds\n"
    "v_pk_maximum3_f16 v80, v36, v37, v80\n"
    "v_pk_maximum3_f16 v80, v38, v39, v80\n"
    "v_pk_add_u16 v36, s89, v36 clamp\n"
    "v_pk_add_u16 v37, s89, v37 clamp\n"
    "v_pk_add_u16 v38, s89, v38 clamp\n"
    "v_pk_add_u16 v39, s89, v39 clamp\n"
    "v_pk_maximum3_f16 v80, v40, v41, v80\n"
    "v_pk_maximum3_f16 v80, v42, v43, v80\n"
    "v_pk_add_u16 v40, s89, v40 clamp\n"
    "v_pk_add_u16 v41, s89, v41 clamp\n"
    "v_pk_add_u16 v42, s89, v42 clamp\n"
    "v_pk_add_u16 v43, s89, v43 clamp\n"
    "v_pk_maximum3_f16 v80, v44, v45, v80\n"
    "v_pk_maximum3_f16 v80, v46, v47, v80\n"
    "v_pk_add_u16 v44, s89, v44 clamp\n"
    "v_pk_add_u16 v45, s89, v45 clamp\n"
    "v_pk_add_u16 v46, s89, v46 clamp\n"
    "v_pk_add_u16 v47, s89, v47 clamp\n"
    "v_pk_maximum3_f16 v80, v48, v49, v80\n"
    "v_pk_maximum3_f16 v80, v50, v51, v80\n"
    "v_pk_add_u16 v48, s89, v48 clamp\n"
    "v_pk_add_u16 v49, s89, v49 clamp\n"
    "v_pk_add_u16 v50, s89, v50 clamp\n"
    "v_pk_add_u16 v51, s89, v51 clamp\n"
    "v_pk_maximum3_f16 v80, v52, v53, v80\n"
    "v_pk_maximum3_f16 v80, v54, v55, v80\n"
    "v_pk_add_u16 v52, s89, v52 clamp\n"
    "v_pk_add_u16 v53, s89, v53 clamp\n"
    "v_pk_add_u16 v54, s89, v54 clamp\n"
    "v_pk_add_u16 v55, s89, v55 clamp\n"
    "v_pk_maximum3_f16 v80, v56, v57, v80\n"
    "v_pk_maximum3_f16 v80, v58, v59, v80\n"
    "v_pk_add_u16 v56, s89, v56 clamp\n"
    "v_pk_add_u16 v57, s89, v57 clamp\n"
    "v_pk_add_u16 v58, s89, v58 clamp\n"
    "v_pk_add_u16 v59, s89, v59 clamp\n"
    "v_mov_b32 v108, 0\n"
    "v_mov_b32 v109, 0\n"
    "v_mov_b32 v110, 0\n"
    "v_mov_b32 v111, 0\n"
    "v_mov_b32 v112, 0\n"
    "v_mov_b32 v113, 0\n"
    "v_mov_b32 v114, 0\n"
    "v_mov_b32 v115, 0\n"
    "v_mov_b32 v116, 0\n"
    "v_mov_b32 v117, 0\n"
    "v_mov_b32 v118, 0\n"
    "v_mov_b32 v119, 0\n"
    "v_mov_b32 v120, 0\n"
    "v_mov_b32 v121, 0\n"
    "v_mov_b32 v122, 0\n"
    "v_mov_b32 v123, 0\n"
    "s_waitcnt lgkmcnt(11)\n"
    "v_mfma_f32_16x16x32_bf16 v[4:7], v[156:159], v[36:39], 0\n"
    "s_waitcnt lgkmcnt(10)\n"
    "v_mfma_f32_16x16x32_bf16 v[4:7], v[160:163], v[40:43], v[4:7]\n"
    "s_waitcnt lgkmcnt(9)\n"
    "v_mfma_f32_16x16x32_bf16 v[4:7], v[164:167], v[44:47], v[4:7]\n"
    "s_waitcnt lgkmcnt(8)\n"
    "v_mfma_f32_16x16x32_bf16 v[4:7], v[168:171], v[48:51], v[4:7]\n"
    "s_waitcnt lgkmcnt(7)\n"
    "v_mfma_f32_16x16x32_bf16 v[4:7], v[172:175], v[52:55], v[4:7]\n"
    "s_waitcnt lgkmcnt(6)\n"
    "v_mfma_f32_16x16x32_bf16 v[4:7], v[176:179], v[56:59], v[4:7]\n"
    "s_waitcnt lgkmcnt(5)\n"
    "v_mfma_f32_16x16x32_bf16 v[20:23], v[180:183], v[36:39], 0\n"
    "v_accvgpr_write_b32 a192, v36\n"
    "v_accvgpr_write_b32 a193, v37\n"
    "v_accvgpr_write_b32 a194, v38\n"
    "v_accvgpr_write_b32 a195, v39\n"
    "s_waitcnt lgkmcnt(4)\n"
    "v_mfma_f32_16x16x32_bf16 v[20:23], v[184:187], v[40:43], v[20:23]\n"
    "v_accvgpr_write_b32 a196, v40\n"
    "v_accvgpr_write_b32 a197, v41\n"
    "v_accvgpr_write_b32 a198, v42\n"
    "v_accvgpr_write_b32 a199, v43\n"
    "s_waitcnt lgkmcnt(3)\n"
    "v_mfma_f32_16x16x32_bf16 v[20:23], v[188:191], v[44:47], v[20:23]\n"
    "v_accvgpr_write_b32 a200, v44\n"
    "v_accvgpr_write_b32 a201, v45\n"
    "v_accvgpr_write_b32 a202, v46\n"
    "v_accvgpr_write_b32 a203, v47\n"
    "s_waitcnt lgkmcnt(2)\n"
    "v_mfma_f32_16x16x32_bf16 v[20:23], v[192:195], v[48:51], v[20:23]\n"
    "v_accvgpr_write_b32 a204, v48\n"
    "v_accvgpr_write_b32 a205, v49\n"
    "v_accvgpr_write_b32 a206, v50\n"
    "v_accvgpr_write_b32 a207, v51\n"
    "s_waitcnt lgkmcnt(1)\n"
    "v_mfma_f32_16x16x32_bf16 v[20:23], v[196:199], v[52:55], v[20:23]\n"
    "v_accvgpr_write_b32 a208, v52\n"
    "v_accvgpr_write_b32 a209, v53\n"
    "v_accvgpr_write_b32 a210, v54\n"
    "v_accvgpr_write_b32 a211, v55\n"
    "s_waitcnt lgkmcnt(0)\n"
    "v_mfma_f32_16x16x32_bf16 v[20:23], v[200:203], v[56:59], v[20:23]\n"
    "v_accvgpr_write_b32 a212, v56\n"
    "v_accvgpr_write_b32 a213, v57\n"
    "v_accvgpr_write_b32 a214, v58\n"
    "v_accvgpr_write_b32 a215, v59\n"
    "s_add_u32 m0, s65, 50176\n"
    "s_add_u32 s72, s85, 13312\n"
    "buffer_load_dwordx4 v89, s[20:23], s72 offen offset:0 nt lds\n"
    "v_mov_b32 v77, 155904\n"
    "v_max3_f32 v76, v4, v5, v6\n"
    "v_max3_f32 v76, v76, v7, v20\n"
    "v_max3_f32 v76, v76, v21, v22\n"
    "v_max_f32 v76, v76, v23\n"
    "v_mov_b32 v78, v76\n"
    "s_nop 1\n"
    "v_permlane32_swap_b32 v76, v78\n"
    "v_max_f32 v76, v76, v78\n"
    "v_mov_b32 v78, v76\n"
    "s_nop 1\n"
    "v_permlane16_swap_b32 v76, v78\n"
    "v_max_f32 v76, v76, v78\n"
    "ds_max_f32 v77, v76\n"
    "s_add_u32 m0, s65, 51200\n"
    "s_add_u32 s72, s85, 14336\n"
    "buffer_load_dwordx4 v90, s[20:23], s72 offen offset:0 nt lds\n"
    "buffer_load_dwordx2 v[236:237], v87, s[36:39], 0 offen offset:0 sc1\n"
    "s_waitcnt lgkmcnt(0)\n"
    "s_barrier\n"
    "ds_read_b32 v76, v77\n"
    "s_waitcnt vmcnt(10)\n"
    "buffer_store_dwordx2 v[238:239], v240, s[36:39], s80 offen offset:0 sc1\n"
    "s_lshl_b32 s73, s79, 8\n"
    "s_add_u32 s80, s80, s73\n"
    "s_mul_i32 s73, s79, 98304\n"
    "s_add_u32 s71, s71, s73\n"
    "s_waitcnt lgkmcnt(0)\n"
    "v_add_f32 v76, 0xc2b00000, v76\n"
    "v_sub_f32 v68, v68, v76\n"
    "v_sub_f32 v69, v69, v76\n"
    "v_sub_f32 v70, v70, v76\n"
    "v_sub_f32 v71, v71, v76\n"
    "v_sub_f32 v72, v72, v76\n"
    "v_sub_f32 v73, v73, v76\n"
    "v_sub_f32 v74, v74, v76\n"
    "v_sub_f32 v75, v75, v76\n"
    "ds_write_b128 v86, v[68:71] offset:0\n"
    "ds_write_b128 v86, v[72:75] offset:16\n"
    "s_mul_i32 s73, s79, 768\n"
    "v_add_u32 v77, s73, v250\n"
    "s_add_u32 m0, s65, 52224\n"
    "s_add_u32 s72, s85, 15360\n"
    "buffer_load_dwordx4 v88, s[20:23], s72 offen offset:0 nt lds\n"
    "s_waitcnt lgkmcnt(0)\n"
    "s_barrier\n"
    "ds_read_b128 v[100:103], v77 offset:0\n"
    "ds_read_b128 v[104:107], v77 offset:16\n"
    "s_add_u32 m0, s65, 53248\n"
    "s_add_u32 s72, s85, 16384\n"
    "buffer_load_dwordx4 v89, s[20:23], s72 offen offset:0 nt lds\n"
    "s_waitcnt lgkmcnt(1)\n"
    "v_add_f32 v4, v4, v100\n"
    "v_add_f32 v5, v5, v101\n"
    "v_add_f32 v6, v6, v102\n"
    "v_add_f32 v7, v7, v103\n"
    "s_waitcnt lgkmcnt(0)\n"
    "v_add_f32 v20, v20, v104\n"
    "v_add_f32 v21, v21, v105\n"
    "v_add_f32 v22, v22, v106\n"
    "v_add_f32 v23, v23, v107\n"
    "v_exp_f32 v4, v4\n"
    "v_exp_f32 v5, v5\n"
    "v_exp_f32 v6, v6\n"
    "v_exp_f32 v7, v7\n"
    "v_exp_f32 v20, v20\n"
    "v_exp_f32 v21, v21\n"
    "v_exp_f32 v22, v22\n"
    "v_exp_f32 v23, v23\n"
    "s_add_u32 m0, s65, 54272\n"
    "s_add_u32 s72, s85, 17408\n"
    "buffer_load_dwordx4 v90, s[20:23], s72 offen offset:0 nt lds\n"
    "s_waitcnt vmcnt(8)\n"
    "ds_read_b128 v[56:59], v85 offset:24832\n"
    "ds_read_b128 v[52:55], v84 offset:24832\n"
    "ds_read_b128 v[48:51], v85 offset:24704\n"
    "ds_read_b128 v[44:47], v84 offset:24704\n"
    "ds_read_b128 v[40:43], v85 offset:24576\n"
    "ds_read_b128 v[36:39], v84 offset:24576\n"
    "s_waitcnt lgkmcnt(5)\n"
    "v_pk_maximum3_f16 v80, v56, v57, v80\n"
    "v_pk_maximum3_f16 v80, v58, v59, v80\n"
    "v_pk_add_u16 v56, s89, v56 clamp\n"
    "v_pk_add_u16 v57, s89, v57 clamp\n"
    "v_pk_add_u16 v58, s89, v58 clamp\n"
    "v_pk_add_u16 v59, s89, v59 clamp\n"
    "s_waitcnt lgkmcnt(4)\n"
    "v_pk_maximum3_f16 v80, v52, v53, v80\n"
    "v_pk_maximum3_f16 v80, v54, v55, v80\n"
    "v_pk_add_u16 v52, s89, v52 clamp\n"
    "v_pk_add_u16 v53, s89, v53 clamp\n"
    "v_pk_add_u16 v54, s89, v54 clamp\n"
    "v_pk_add_u16 v55, s89, v55 clamp\n"
    "v_mfma_f32_16x16x32_bf16 v[8:11], v[176:179], v[56:59], v[100:103]\n"
    "s_waitcnt lgkmcnt(3)\n"
    "v_pk_maximum3_f16 v80, v48, v49, v80\n"
    "v_pk_maximum3_f16 v80, v50, v51, v80\n"
    "v_pk_add_u16 v48, s89, v48 clamp\n"
    "v_pk_add_u16 v49, s89, v49 clamp\n"
    "v_pk_add_u16 v50, s89, v50 clamp\n"
    "v_pk_add_u16 v51, s89, v51 clamp\n"
    "v_mfma_f32_16x16x32_bf16 v[8:11], v[172:175], v[52:55], v[8:11]\n"
    "s_waitcnt lgkmcnt(2)\n"
    "v_pk_maximum3_f16 v80, v44, v45, v80\n"
    "v_pk_maximum3_f16 v80, v46, v47, v80\n"
    "v_pk_add_u16 v44, s89, v44 clamp\n"
    "v_pk_add_u16 v45, s89, v45 clamp\n"
    "v_pk_add_u16 v46, s89, v46 clamp\n"
    "v_pk_add_u16 v47, s89, v47 clamp\n"
    "v_mfma_f32_16x16x32_bf16 v[8:11], v[168:171], v[48:51], v[8:11]\n"
    "s_waitcnt lgkmcnt(1)\n"
    "v_pk_maximum3_f16 v80, v40, v41, v80\n"
    "v_pk_maximum3_f16 v80, v42, v43, v80\n"
    "v_pk_add_u16 v40, s89, v40 clamp\n"
    "v_pk_add_u16 v41, s89, v41 clamp\n"
    "v_pk_add_u16 v42, s89, v42 clamp\n"
    "v_pk_add_u16 v43, s89, v43 clamp\n"
    "v_mfma_f32_16x16x32_bf16 v[8:11], v[164:167], v[44:47], v[8:11]\n"
    "s_waitcnt lgkmcnt(0)\n"
    "v_pk_maximum3_f16 v80, v36, v37, v80\n"
    "v_pk_maximum3_f16 v80, v38, v39, v80\n"
    "v_pk_add_u16 v36, s89, v36 clamp\n"
    "v_pk_add_u16 v37, s89, v37 clamp\n"
    "v_pk_add_u16 v38, s89, v38 clamp\n"
    "v_pk_add_u16 v39, s89, v39 clamp\n"
    "v_mfma_f32_16x16x32_bf16 v[8:11], v[160:163], v[40:43], v[8:11]\n"
    "s_nop 0\n"
    "v_mfma_f32_16x16x32_bf16 v[8:11], v[156:159], v[36:39], v[8:11]\n"
    "v_mfma_f32_16x16x32_bf16 v[24:27], v[200:203], v[56:59], v[104:107]\n"
    "v_accvgpr_write_b32 a236, v56\n"
    "v_accvgpr_write_b32 a237, v57\n"
    "v_accvgpr_write_b32 a238, v58\n"
    "v_accvgpr_write_b32 a239, v59\n"
    "v_mfma_f32_16x16x32_bf16 v[24:27], v[196:199], v[52:55], v[24:27]\n"
    "v_accvgpr_write_b32 a232, v52\n"
    "v_accvgpr_write_b32 a233, v53\n"
    "v_accvgpr_write_b32 a234, v54\n"
    "v_accvgpr_write_b32 a235, v55\n"
    "v_mfma_f32_16x16x32_bf16 v[24:27], v[192:195], v[48:51], v[24:27]\n"
    "v_accvgpr_write_b32 a228, v48\n"
    "v_accvgpr_write_b32 a229, v49\n"
    "v_accvgpr_write_b32 a230, v50\n"
    "v_accvgpr_write_b32 a231, v51\n"
    "v_mfma_f32_16x16x32_bf16 v[24:27], v[188:191], v[44:47], v[24:27]\n"
    "v_accvgpr_write_b32 a224, v44\n"
    "v_accvgpr_write_b32 a225, v45\n"
    "v_accvgpr_write_b32 a226, v46\n"
    "v_accvgpr_write_b32 a227, v47\n"
    "v_mfma_f32_16x16x32_bf16 v[24:27], v[184:187], v[40:43], v[24:27]\n"
    "v_accvgpr_write_b32 a220, v40\n"
    "v_accvgpr_write_b32 a221, v41\n"
    "v_accvgpr_write_b32 a222, v42\n"
    "v_accvgpr_write_b32 a223, v43\n"
    "v_mfma_f32_16x16x32_bf16 v[24:27], v[180:183], v[36:39], v[24:27]\n"
    "v_accvgpr_write_b32 a216, v36\n"
    "v_accvgpr_write_b32 a217, v37\n"
    "v_accvgpr_write_b32 a218, v38\n"
    "v_accvgpr_write_b32 a219, v39\n"
    "s_add_u32 m0, s65, 98304\n"
    "s_add_u32 s72, s85, 18432\n"
    "buffer_load_dwordx4 v88, s[20:23], s72 offen offset:0 nt lds\n"
    "v_exp_f32 v8, v8\n"
    "v_exp_f32 v9, v9\n"
    "v_exp_f32 v10, v10\n"
    "v_exp_f32 v11, v11\n"
    "v_exp_f32 v24, v24\n"
    "v_exp_f32 v25, v25\n"
    "v_exp_f32 v26, v26\n"
    "v_exp_f32 v27, v27\n"
    "s_add_u32 m0, s65, 99328\n"
    "s_add_u32 s72, s85, 19456\n"
    "buffer_load_dwordx4 v89, s[20:23], s72 offen offset:0 nt lds\n"
    "s_add_u32 m0, s65, 100352\n"
    "s_add_u32 s72, s85, 20480\n"
    "buffer_load_dwordx4 v90, s[20:23], s72 offen offset:0 nt lds\n"
    "s_waitcnt vmcnt(7)\n"
    "v_cmp_eq_u64 s[54:55], v[236:237], v[238:239]\n"
    "s_nop 0\n"
    "s_cmp_eq_u64 s[54:55], -1\n"
    "s_cbranch_scc0 .LpollAK\n"
    ".LpollAKr:\n"
    "s_add_u32 m0, s65, 101376\n"
    "s_add_u32 s72, s85, 21504\n"
    "buffer_load_dwordx4 v88, s[20:23], s72 offen offset:0 nt lds\n"
    "s_add_u32 m0, s65, 24576\n"
    "s_add_u32 s72, s65, 49152\n"
    "buffer_load_dwordx4 v1, s[16:19], s72 offen offset:0 sc1 lds\n"
    "s_add_u32 m0, s65, 25600\n"
    "s_add_u32 s72, s65, 50176\n"
    "buffer_load_dwordx4 v1, s[16:19], s72 offen offset:0 sc1 lds\n"
    "s_add_u32 m0, s65, 102400\n"
    "s_add_u32 s72, s85, 22528\n"
    "buffer_load_dwordx4 v89, s[20:23], s72 offen offset:0 nt lds\n"
    "s_add_u32 m0, s65, 26624\n"
    "s_add_u32 s72, s65, 51200\n"
    "buffer_load_dwordx4 v1, s[16:19], s72 offen offset:0 sc1 lds\n"
    "s_add_u32 m0, s65, 27648\n"
    "s_add_u32 s72, s65, 52224\n"
    "buffer_load_dwordx4 v1, s[16:19], s72 offen offset:0 sc1 lds\n"
    "s_add_u32 m0, s65, 103424\n"
    "s_add_u32 s72, s85, 23552\n"
    "buffer_load_dwordx4 v90, s[20:23], s72 offen offset:0 nt lds\n"
    "s_add_u32 m0, s65, 28672\n"
    "s_add_u32 s72, s65, 53248\n"
    "buffer_load_dwordx4 v1, s[16:19], s72 offen offset:0 sc1 lds\n"
    "s_add_u32 m0, s65, 29696\n"
    "s_add_u32 s72, s65, 54272\n"
    "buffer_load_dwordx4 v1, s[16:19], s72 offen offset:0 sc1 lds\n"
    "s_add_u32 m0, s65, 73728\n"
    "s_add_u32 s72, s65, 73728\n"
    "buffer_load_dwordx4 v1, s[16:19], s72 offen offset:0 sc1 lds\n"
    "s_add_u32 m0, s65, 74752\n"
    "s_add_u32 s72, s65, 74752\n"
    "buffer_load_dwordx4 v1, s[16:19], s72 offen offset:0 sc1 lds\n"
    "s_add_u32 m0, s65, 75776\n"
    "s_add_u32 s72, s65, 75776\n"
    "buffer_load_dwordx4 v1, s[16:19], s72 offen offset:0 sc1 lds\n"
    "s_add_u32 m0, s65, 76800\n"
    "s_add_u32 s72, s65, 76800\n"
    "buffer_load_dwordx4 v1, s[16:19], s72 offen offset:0 sc1 lds\n"
    "s_add_u32 m0, s65, 77824\n"
    "s_add_u32 s72, s65, 77824\n"
    "buffer_load_dwordx4 v1, s[16:19], s72 offen offset:0 sc1 lds\n"
    "s_add_u32 m0, s65, 78848\n"
    "s_add_u32 s72, s65, 78848\n"
    "buffer_load_dwordx4 v1, s[16:19], s72 offen offset:0 sc1 lds\n"
    "s_bitcmp1_b32 s83, 1\n"
    "s_cbranch_scc1 .Lf3b\n"
    "s_waitcnt vmcnt(18)\n"
    "ds_read_b128 v[36:39], v84 offset:0\n"
    "ds_read_b128 v[40:43], v85 offset:0\n"
    "ds_read_b128 v[44:47], v84 offset:128\n"
    "ds_read_b128 v[48:51], v85 offset:128\n"
    "ds_read_b128 v[124:127], v84 offset:256\n"
    "ds_read_b128 v[128:131], v85 offset:256\n"
    "s_waitcnt lgkmcnt(5)\n"
    "v_pk_maximum3_f16 v80, v36, v37, v80\n"
    "v_pk_maximum3_f16 v80, v38, v39, v80\n"
    "v_pk_add_u16 v36, s89, v36 clamp\n"
    "v_pk_add_u16 v37, s89, v37 clamp\n"
    "v_pk_add_u16 v38, s89, v38 clamp\n"
    "v_pk_add_u16 v39, s89, v39 clamp\n"
    "s_waitcnt lgkmcnt(4)\n"
    "v_pk_maximum3_f16 v80, v40, v41, v80\n"
    "v_pk_maximum3_f16 v80, v42, v43, v80\n"
    "v_pk_add_u16 v40, s89, v40 clamp\n"
    "v_pk_add_u16 v41, s89, v41 clamp\n"
    "v_pk_add_u16 v42, s89, v42 clamp\n"
    "v_pk_add_u16 v43, s89, v43 clamp\n"
    "v_mfma_f32_16x16x32_bf16 v[12:15], v[156:159], v[36:39], v[100:103]\n"
    "s_waitcnt lgkmcnt(3)\n"
    "v_pk_maximum3_f16 v80, v44, v45, v80\n"
    "v_pk_maximum3_f16 v80, v46, v47, v80\n"
    "v_pk_add_u16 v44, s89, v44 clamp\n"
    "v_pk_add_u16 v45, s89, v45 clamp\n"
    "v_pk_add_u16 v46, s89, v46 clamp\n"
    "v_pk_add_u16 v47, s89, v47 clamp\n"
    "v_mfma_f32_16x16x32_bf16 v[12:15], v[160:163], v[40:43], v[12:15]\n"
    "s_waitcnt lgkmcnt(2)\n"
    "v_pk_maximum3_f16 v80, v48, v49, v80\n"
    "v_pk_maximum3_f16 v80, v50, v51, v80\n"
    "v_pk_add_u16 v48, s89, v48 clamp\n"
    "v_pk_add_u16 v49, s89, v49 clamp\n"
    "v_pk_add_u16 v50, s89, v50 clamp\n"
    "v_pk_add_u16 v51, s89, v51 clamp\n"
    "v_mfma_f32_16x16x32_bf16 v[12:15], v[164:167], v[44:47], v[12:15]\n"
    "s_waitcnt lgkmcnt(1)\n"
    "v_pk_maximum3_f16 v80, v124, v125, v80\n"
    "v_pk_maximum3_f16 v80, v126, v127, v80\n"
    "v_pk_add_u16 v124, s89, v124 clamp\n"
    "v_pk_add_u16 v125, s89, v125 clamp\n"
    "v_pk_add_u16 v126, s89, v126 clamp\n"
    "v_pk_add_u16 v127, s89, v127 clamp\n"
    "v_mfma_f32_16x16x32_bf16 v[12:15], v[168:171], v[48:51], v[12:15]\n"
    "s_waitcnt lgkmcnt(0)\n"
    "v_pk_maximum3_f16 v80, v128, v129, v80\n"
    "v_pk_maximum3_f16 v80, v130, v131, v80\n"
    "v_pk_add_u16 v128, s89, v128 clamp\n"
    "v_pk_add_u16 v129, s89, v129 clamp\n"
    "v_pk_add_u16 v130, s89, v130 clamp\n"
    "v_pk_add_u16 v131, s89, v131 clamp\n"
    "v_mfma_f32_16x16x32_bf16 v[12:15], v[172:175], v[124:127], v[12:15]\n"
    "s_nop 0\n"
    "v_mfma_f32_16x16x32_bf16 v[12:15], v[176:179], v[128:131], v[12:15]\n"
    "v_mfma_f32_16x16x32_bf16 v[28:31], v[180:183], v[36:39], v[104:107]\n"
    "v_accvgpr_write_b32 a240, v36\n"
    "v_accvgpr_write_b32 a241, v37\n"
    "v_accvgpr_write_b32 a242, v38\n"
    "v_accvgpr_write_b32 a243, v39\n"
    "v_mfma_f32_16x16x32_bf16 v[28:31], v[184:187], v[40:43], v[28:31]\n"
    "v_accvgpr_write_b32 a244, v40\n"
    "v_accvgpr_write_b32 a245, v41\n"
    "v_accvgpr_write_b32 a246, v42\n"
    "v_accvgpr_write_b32 a247, v43\n"
    "v_mfma_f32_16x16x32_bf16 v[28:31], v[188:191], v[44:47], v[28:31]\n"
    "v_accvgpr_write_b32 a248, v44\n"
    "v_accvgpr_write_b32 a249, v45\n"
    "v_accvgpr_write_b32 a250, v46\n"
    "v_accvgpr_write_b32 a251, v47\n"
    "v_mfma_f32_16x16x32_bf16 v[28:31], v[192:195], v[48:51], v[28:31]\n"
    "v_accvgpr_write_b32 a252, v48\n"
    "v_accvgpr_write_b32 a253, v49\n"
    "v_accvgpr_write_b32 a254, v50\n"
    "v_accvgpr_write_b32 a255, v51\n"
    "v_mfma_f32_16x16x32_bf16 v[28:31], v[196:199], v[124:127], v[28:31]\n"
    "v_mfma_f32_16x16x32_bf16 v[28:31], v[200:203], v[128:131], v[28:31]\n"
    "v_exp_f32 v12, v12\n"
    "v_exp_f32 v13, v13\n"
    "v_exp_f32 v14, v14\n"
    "v_exp_f32 v15, v15\n"
    "s_nop 3\n"
    "v_exp_f32 v28, v28\n"
    "v_exp_f32 v29, v29\n"
    "v_exp_f32 v30, v30\n"
    "v_exp_f32 v31, v31\n"
    "s_waitcnt vmcnt(8)\n"
    "ds_read_b128 v[152:155], v85 offset:49408\n"
    "ds_read_b128 v[148:151], v84 offset:49408\n"
    "ds_read_b128 v[144:147], v85 offset:49280\n"
    "ds_read_b128 v[140:143], v84 offset:49280\n"
    "ds_read_b128 v[136:139], v85 offset:49152\n"
    "ds_read_b128 v[132:135], v84 offset:49152\n"
    "s_waitcnt lgkmcnt(5)\n"
    "v_pk_maximum3_f16 v80, v152, v153, v80\n"
    "v_pk_maximum3_f16 v80, v154, v155, v80\n"
    "v_pk_add_u16 v152, s89, v152 clamp\n"
    "v_pk_add_u16 v153, s89, v153 clamp\n"
    "v_pk_add_u16 v154, s89, v154 clamp\n"
    "v_pk_add_u16 v155, s89, v155 clamp\n"
    "s_waitcnt lgkmcnt(4)\n"
    "v_pk_maximum3_f16 v80, v148, v149, v80\n"
    "v_pk_maximum3_f16 v80, v150, v151, v80\n"
    "v_pk_add_u16 v148, s89, v148 clamp\n"
    "v_pk_add_u16 v149, s89, v149 clamp\n"
    "v_pk_add_u16 v150, s89, v150 clamp\n"
    "v_pk_add_u16 v151, s89, v151 clamp\n"
    "v_mfma_f32_16x16x32_bf16 v[16:19], v[176:179], v[152:155], v[100:103]\n"
    "s_waitcnt lgkmcnt(3)\n"
    "v_pk_maximum3_f16 v80, v144, v145, v80\n"
    "v_pk_maximum3_f16 v80, v146, v147, v80\n"
    "v_pk_add_u16 v144, s89, v144 clamp\n"
    "v_pk_add_u16 v145, s89, v145 clamp\n"
    "v_pk_add_u16 v146, s89, v146 clamp\n"
    "v_pk_add_u16 v147, s89, v147 clamp\n"
    "v_mfma_f32_16x16x32_bf16 v[16:19], v[172:175], v[148:151], v[16:19]\n"
    "s_waitcnt lgkmcnt(2)\n"
    "v_pk_maximum3_f16 v80, v140, v141, v80\n"
    "v_pk_maximum3_f16 v80, v142, v143, v80\n"
    "v_pk_add_u16 v140, s89, v140 clamp\n"
    "v_pk_add_u16 v141, s89, v141 clamp\n"
    "v_pk_add_u16 v142, s89, v142 clamp\n"
    "v_pk_add_u16 v143, s89, v143 clamp\n"
    "v_mfma_f32_16x16x32_bf16 v[16:19], v[168:171], v[144:147], v[16:19]\n"
    "s_waitcnt lgkmcnt(1)\n"
    "v_pk_maximum3_f16 v80, v136, v137, v80\n"
    "v_pk_maximum3_f16 v80, v138, v139, v80\n"
    "v_pk_add_u16 v136, s89, v136 clamp\n"
    "v_pk_add_u16 v137, s89, v137 clamp\n"
    "v_pk_add_u16 v138, s89, v138 clamp\n"
    "v_pk_add_u16 v139, s89, v139 clamp\n"
    "v_mfma_f32_16x16x32_bf16 v[16:19], v[164:167], v[140:143], v[16:19]\n"
    "s_waitcnt lgkmcnt(0)\n"
    "v_pk_maximum3_f16 v80, v132, v133, v80\n"
    "v_pk_maximum3_f16 v80, v134, v135, v80\n"
    "v_pk_add_u16 v132, s89, v132 clamp\n"
    "v_pk_add_u16 v133, s89, v133 clamp\n"
    "v_pk_add_u16 v134, s89, v134 clamp\n"
    "v_pk_add_u16 v135, s89, v135 clamp\n"
    "v_mfma_f32_16x16x32_bf16 v[16:19], v[160:163], v[136:139], v[16:19]\n"
    "s_nop 0\n"
    "v_mfma_f32_16x16x32_bf16 v[16:19], v[156:159], v[132:135], v[16:19]\n"
    "v_mfma_f32_16x16x32_bf16 v[32:35], v[200:203], v[152:155], v[104:107]\n"
    "v_mfma_f32_16x16x32_bf16 v[32:35], v[196:199], v[148:151], v[32:35]\n"
    "v_mfma_f32_16x16x32_bf16 v[32:35], v[192:195], v[144:147], v[32:35]\n"
    "v_mfma_f32_16x16x32_bf16 v[32:35], v[188:191], v[140:143], v[32:35]\n"
    "v_mfma_f32_16x16x32_bf16 v[32:35], v[184:187], v[136:139], v[32:35]\n"
    "v_mfma_f32_16x16x32_bf16 v[32:35], v[180:183], v[132:135], v[32:35]\n"
    "ds_read_b128 v[156:159], v2 offset:12288\n"
    "ds_read_b128 v[160:163], v2 offset:14336\n"
    "ds_read_b128 v[164:167], v2 offset:16384\n"
    "ds_read_b128 v[168:171], v2 offset:18432\n"
    "ds_read_b128 v[172:175], v2 offset:20480\n"
    "ds_read_b128 v[176:179], v2 offset:22528\n"
    "ds_read_b128 v[100:103], v77 offset:128\n"
    "v_pk_add_u16 v80, s89, v80\n"
    "v_and_b32 v80, 0x80008000, v80\n"
    "v_cmp_ne_u32 s[34:35], 0, v80\n"
    "v_exp_f32 v16, v16\n"
    "v_exp_f32 v17, v17\n"
    "v_exp_f32 v18, v18\n"
    "v_exp_f32 v19, v19\n"
    "v_exp_f32 v32, v32\n"
    "v_exp_f32 v33, v33\n"
    "v_exp_f32 v34, v34\n"
    "v_exp_f32 v35, v35\n"
    "s_mov_b32 s66, 0\n"
    "s_waitcnt lgkmcnt(0)\n"
    "s_waitcnt vmcnt(0)\n"
    "ds_add_u32 v247, v246\n"
    "s_nop 7\n"
    "s_branch .LPK\n"
    "s_branch .Lf3e\n"
    ".Lf3b:\n"
    "s_add_u32 s75, s71, s63\n"
    "s_add_u32 s76, s75, 6144\n"
    "s_add_u32 s77, s75, 24576\n"
    "s_add_u32 s78, s75, 30720\n"
    "buffer_load_dwordx2 v[204:205], v242, s[28:31], s75 offen offset:0 nt\n"
    "buffer_load_dwordx2 v[206:207], v242, s[28:31], s75 offen offset:768 nt\n"
    "buffer_load_dwordx2 v[208:209], v242, s[28:31], s75 offen offset:3072 nt\n"
    "buffer_load_dwordx2 v[210:211], v242, s[28:31], s75 offen offset:3840 nt\n"
    "buffer_load_dwordx2 v[212:213], v242, s[28:31], s76 offen offset:0 nt\n"
    "buffer_load_dwordx2 v[214:215], v242, s[28:31], s76 offen offset:768 nt\n"
    "buffer_load_dwordx2 v[216:217], v242, s[28:31], s76 offen offset:3072 nt\n"
    "buffer_load_dwordx2 v[218:219], v242, s[28:31], s76 offen offset:3840 nt\n"
    "buffer_load_dwordx2 v[220:221], v242, s[28:31], s77 offen offset:0 nt\n"
    "buffer_load_dwordx2 v[222:223], v242, s[28:31], s77 offen offset:768 nt\n"
    "buffer_load_dwordx2 v[224:225], v242, s[28:31], s77 offen offset:3072 nt\n"
    "buffer_load_dwordx2 v[226:227], v242, s[28:31], s77 offen offset:3840 nt\n"
    "buffer_load_dwordx2 v[228:229], v242, s[28:31], s78 offen offset:0 nt\n"
    "buffer_load_dwordx2 v[230:231], v242, s[28:31], s78 offen offset:768 nt\n"
    "buffer_load_dwordx2 v[232:233], v242, s[28:31], s78 offen offset:3072 nt\n"
    "buffer_load_dwordx2 v[234:235], v242, s[28:31], s78 offen offset:3840 nt\n"
    "s_waitcnt vmcnt(34)\n"
    "ds_read_b128 v[36:39], v84 offset:0\n"
    "ds_read_b128 v[40:43], v85 offset:0\n"
    "ds_read_b128 v[44:47], v84 offset:128\n"
    "ds_read_b128 v[48:51], v85 offset:128\n"
    "ds_read_b128 v[124:127], v84 offset:256\n"
    "ds_read_b128 v[128:131], v85 offset:256\n"
    "s_waitcnt lgkmcnt(5)\n"
    "v_pk_maximum3_f16 v80, v36, v37, v80\n"
    "v_pk_maximum3_f16 v80, v38, v39, v80\n"
    "v_pk_add_u16 v36, s89, v36 clamp\n"
    "v_pk_add_u16 v37, s89, v37 clamp\n"
    "v_pk_add_u16 v38, s89, v38 clamp\n"
    "v_pk_add_u16 v39, s89, v39 clamp\n"
    "s_waitcnt lgkmcnt(4)\n"
    "v_pk_maximum3_f16 v80, v40, v41, v80\n"
    "v_pk_maximum3_f16 v80, v42, v43, v80\n"
    "v_pk_add_u16 v40, s89, v40 clamp\n"
    "v_pk_add_u16 v41, s89, v41 clamp\n"
    "v_pk_add_u16 v42, s89, v42 clamp\n"
    "v_pk_add_u16 v43, s89, v43 clamp\n"
    "v_mfma_f32_16x16x32_bf16 v[12:15], v[156:159], v[36:39], v[100:103]\n"
    "s_waitcnt lgkmcnt(3)\n"
    "v_pk_maximum3_f16 v80, v44, v45, v80\n"
    "v_pk_maximum3_f16 v80, v46, v47, v80\n"
    "v_pk_add_u16 v44, s89, v44 clamp\n"
    "v_pk_add_u16 v45, s89, v45 clamp\n"
    "v_pk_add_u16 v46, s89, v46 clamp\n"
    "v_pk_add_u16 v47, s89, v47 clamp\n"
    "v_mfma_f32_16x16x32_bf16 v[12:15], v[160:163], v[40:43], v[12:15]\n"
    "s_waitcnt lgkmcnt(2)\n"
    "v_pk_maximum3_f16 v80, v48, v49, v80\n"
    "v_pk_maximum3_f16 v80, v50, v51, v80\n"
    "v_pk_add_u16 v48, s89, v48 clamp\n"
    "v_pk_add_u16 v49, s89, v49 clamp\n"
    "v_pk_add_u16 v50, s89, v50 clamp\n"
    "v_pk_add_u16 v51, s89, v51 clamp\n"
    "v_mfma_f32_16x16x32_bf16 v[12:15], v[164:167], v[44:47], v[12:15]\n"
    "s_waitcnt lgkmcnt(1)\n"
    "v_pk_maximum3_f16 v80, v124, v125, v80\n"
    "v_pk_maximum3_f16 v80, v126, v127, v80\n"
    "v_pk_add_u16 v124, s89, v124 clamp\n"
    "v_pk_add_u16 v125, s89, v125 clamp\n"
    "v_pk_add_u16 v126, s89, v126 clamp\n"
    "v_pk_add_u16 v127, s89, v127 clamp\n"
    "v_mfma_f32_16x16x32_bf16 v[12:15], v[168:171], v[48:51], v[12:15]\n"
    "s_waitcnt lgkmcnt(0)\n"
    "v_pk_maximum3_f16 v80, v128, v129, v80\n"
    "v_pk_maximum3_f16 v80, v130, v131, v80\n"
    "v_pk_add_u16 v128, s89, v128 clamp\n"
    "v_pk_add_u16 v129, s89, v129 clamp\n"
    "v_pk_add_u16 v130, s89, v130 clamp\n"
    "v_pk_add_u16 v131, s89, v131 clamp\n"
    "v_mfma_f32_16x16x32_bf16 v[12:15], v[172:175], v[124:127], v[12:15]\n"
    "s_nop 0\n"
    "v_mfma_f32_16x16x32_bf16 v[12:15], v[176:179], v[128:131], v[12:15]\n"
    "v_mfma_f32_16x16x32_bf16 v[28:31], v[180:183], v[36:39], v[104:107]\n"
    "v_accvgpr_write_b32 a240, v36\n"
    "v_accvgpr_write_b32 a241, v37\n"
    "v_accvgpr_write_b32 a242, v38\n"
    "v_accvgpr_write_b32 a243, v39\n"
    "v_mfma_f32_16x16x32_bf16 v[28:31], v[184:187], v[40:43], v[28:31]\n"
    "v_accvgpr_write_b32 a244, v40\n"
    "v_accvgpr_write_b32 a245, v41\n"
    "v_accvgpr_write_b32 a246, v42\n"
    "v_accvgpr_write_b32 a247, v43\n"
    "v_mfma_f32_16x16x32_bf16 v[28:31], v[188:191], v[44:47], v[28:31]\n"
    "v_accvgpr_write_b32 a248, v44\n"
    "v_accvgpr_write_b32 a249, v45\n"
    "v_accvgpr_write_b32 a250, v46\n"
    "v_accvgpr_write_b32 a251, v47\n"
    "v_mfma_f32_16x16x32_bf16 v[28:31], v[192:195], v[48:51], v[28:31]\n"
    "v_accvgpr_write_b32 a252, v48\n"
    "v_accvgpr_write_b32 a253, v49\n"
    "v_accvgpr_write_b32 a254, v50\n"
    "v_accvgpr_write_b32 a255, v51\n"
    "v_mfma_f32_16x16x32_bf16 v[28:31], v[196:199], v[124:127], v[28:31]\n"
    "v_mfma_f32_16x16x32_bf16 v[28:31], v[200:203], v[128:131], v[28:31]\n"
    "v_exp_f32 v12, v12\n"
    "v_exp_f32 v13, v13\n"
    "v_exp_f32 v14, v14\n"
    "v_exp_f32 v15, v15\n"
    "s_nop 3\n"
    "v_exp_f32 v28, v28\n"
    "v_exp_f32 v29, v29\n"
    "v_exp_f32 v30, v30\n"
    "v_exp_f32 v31, v31\n"
    "s_waitcnt vmcnt(24)\n"
    "ds_read_b128 v[152:155], v85 offset:49408\n"
    "ds_read_b128 v[148:151], v84 offset:49408\n"
    "ds_read_b128 v[144:147], v85 offset:49280\n"
    "ds_read_b128 v[140:143], v84 offset:49280\n"
    "ds_read_b128 v[136:139], v85 offset:49152\n"
    "ds_read_b128 v[132:135], v84 offset:49152\n"
    "s_waitcnt lgkmcnt(5)\n"
    "v_pk_maximum3_f16 v80, v152, v153, v80\n"
    "v_pk_maximum3_f16 v80, v154, v155, v80\n"
    "v_pk_add_u16 v152, s89, v152 clamp\n"
    "v_pk_add_u16 v153, s89, v153 clamp\n"
    "v_pk_add_u16 v154, s89, v154 clamp\n"
    "v_pk_add_u16 v155, s89, v155 clamp\n"
    "s_waitcnt lgkmcnt(4)\n"
    "v_pk_maximum3_f16 v80, v148, v149, v80\n"
    "v_pk_maximum3_f16 v80, v150, v151, v80\n"
    "v_pk_add_u16 v148, s89, v148 clamp\n"
    "v_pk_add_u16 v149, s89, v149 clamp\n"
    "v_pk_add_u16 v150, s89, v150 clamp\n"
    "v_pk_add_u16 v151, s89, v151 clamp\n"
    "v_mfma_f32_16x16x32_bf16 v[16:19], v[176:179], v[152:155], v[100:103]\n"
    "s_waitcnt lgkmcnt(3)\n"
    "v_pk_maximum3_f16 v80, v144, v145, v80\n"
    "v_pk_maximum3_f16 v80, v146, v147, v80\n"
    "v_pk_add_u16 v144, s89, v144 clamp\n"
    "v_pk_add_u16 v145, s89, v145 clamp\n"
    "v_pk_add_u16 v146, s89, v146 clamp\n"
    "v_pk_add_u16 v147, s89, v147 clamp\n"
    "v_mfma_f32_16x16x32_bf16 v[16:19], v[172:175], v[148:151], v[16:19]\n"
    "s_waitcnt lgkmcnt(2)\n"
    "v_pk_maximum3_f16 v80, v140, v141, v80\n"
    "v_pk_maximum3_f16 v80, v142, v143, v80\n"
    "v_pk_add_u16 v140, s89, v140 clamp\n"
    "v_pk_add_u16 v141, s89, v141 clamp\n"
    "v_pk_add_u16 v142, s89, v142 clamp\n"
    "v_pk_add_u16 v143, s89, v143 clamp\n"
    "v_mfma_f32_16x16x32_bf16 v[16:19], v[168:171], v[144:147], v[16:19]\n"
    "s_waitcnt lgkmcnt(1)\n"
    "v_pk_maximum3_f16 v80, v136, v137, v80\n"
    "v_pk_maximum3_f16 v80, v138, v139, v80\n"
    "v_pk_add_u16 v136, s89, v136 clamp\n"
    "v_pk_add_u16 v137, s89, v137 clamp\n"
    "v_pk_add_u16 v138, s89, v138 clamp\n"
    "v_pk_add_u16 v139, s89, v139 clamp\n"
    "v_mfma_f32_16x16x32_bf16 v[16:19], v[164:167], v[140:143], v[16:19]\n"
    "s_waitcnt lgkmcnt(0)\n"
    "v_pk_maximum3_f16 v80, v132, v133, v80\n"
    "v_pk_maximum3_f16 v80, v134, v135, v80\n"
    "v_pk_add_u16 v132, s89, v132 clamp\n"
    "v_pk_add_u16 v133, s89, v133 clamp\n"
    "v_pk_add_u16 v134, s89, v134 clamp\n"
    "v_pk_add_u16 v135, s89, v135 clamp\n"
    "v_mfma_f32_16x16x32_bf16 v[16:19], v[160:163], v[136:139], v[16:19]\n"
    "s_nop 0\n"
    "v_mfma_f32_16x16x32_bf16 v[16:19], v[156:159], v[132:135], v[16:19]\n"
    "v_mfma_f32_16x16x32_bf16 v[32:35], v[200:203], v[152:155], v[104:107]\n"
    "v_mfma_f32_16x16x32_bf16 v[32:35], v[196:199], v[148:151], v[32:35]\n"
    "v_mfma_f32_16x16x32_bf16 v[32:35], v[192:195], v[144:147], v[32:35]\n"
    "v_mfma_f32_16x16x32_bf16 v[32:35], v[188:191], v[140:143], v[32:35]\n"
    "v_mfma_f32_16x16x32_bf16 v[32:35], v[184:187], v[136:139], v[32:35]\n"
    "v_mfma_f32_16x16x32_bf16 v[32:35], v[180:183], v[132:135], v[32:35]\n"
    "ds_read_b128 v[156:159], v2 offset:12288\n"
    "ds_read_b128 v[160:163], v2 offset:14336\n"
    "ds_read_b128 v[164:167], v2 offset:16384\n"
    "ds_read_b128 v[168:171], v2 offset:18432\n"
    "ds_read_b128 v[172:175], v2 offset:20480\n"
    "ds_read_b128 v[176:179], v2 offset:22528\n"
    "ds_read_b128 v[100:103], v77 offset:128\n"
    "v_pk_add_u16 v80, s89, v80\n"
    "v_and_b32 v80, 0x80008000, v80\n"
    "v_cmp_ne_u32 s[34:35], 0, v80\n"
    "v_exp_f32 v16, v16\n"
    "v_exp_f32 v17, v17\n"
    "v_exp_f32 v18, v18\n"
    "v_exp_f32 v19, v19\n"
    "v_exp_f32 v32, v32\n"
    "v_exp_f32 v33, v33\n"
    "v_exp_f32 v34, v34\n"
    "v_exp_f32 v35, v35\n"
    "s_mov_b32 s66, 0\n"
    "s_waitcnt lgkmcnt(0)\n"
    "s_waitcnt vmcnt(16)\n"
    "ds_add_u32 v247, v246\n"
    "s_nop 7\n"
    "s_branch .LNK\n"
    ".Lf3e:\n"
    "s_nop 4\n"
    "s_branch .Lf5e\n"
    ".Lf5b:\n"
    "s_lshl_b32 s73, s79, 8\n"
    "s_mul_i32 s74, s59, 6144\n"
    "s_add_u32 s73, s73, s74\n"
    "v_add_u32 v163, s73, v244\n"
    "s_lshr_b32 s73, s53, 2\n"
    "s_mul_i32 s73, s73, 12288\n"
    "s_and_b32 s74, s53, 3\n"
    "s_lshl_b32 s74, s74, 8\n"
    "s_add_u32 s73, s73, s74\n"
    "s_cmp_eq_u32 s59, 0\n"
    "s_cselect_b32 s73, 0x7f000000, s73\n"
    "s_add_u32 s60, s73, 24576\n"
    "s_add_u32 s61, s73, 28672\n"
    "s_add_u32 s62, s73, 32768\n"
    "v_mov_b32 v244, v243\n"
    "v_mov_b32 v243, 0\n"
    "buffer_load_dwordx4 v[40:43], v242, s[40:43], s75 offen offset:768 nt\n"
    "v_add_u32 v159, 0, v156\n"
    "v_mul_u32_u24 v159, 2731, v159\n"
    "v_lshrrev_b32 v159, 16, v159\n"
    "v_xor_b32 v159, v159, v156\n"
    "v_and_b32 v159, 7, v159\n"
    "v_and_b32 v88, 0x380, v1\n"
    "v_lshl_add_u32 v88, v159, 4, v88\n"
    "v_add_u32 v159, 64, v156\n"
    "v_mul_u32_u24 v159, 2731, v159\n"
    "v_lshrrev_b32 v159, 16, v159\n"
    "v_xor_b32 v159, v159, v156\n"
    "v_and_b32 v159, 7, v159\n"
    "v_and_b32 v89, 0x380, v1\n"
    "v_lshl_add_u32 v89, v159, 4, v89\n"
    "v_add_u32 v159, 128, v156\n"
    "v_mul_u32_u24 v159, 2731, v159\n"
    "v_lshrrev_b32 v159, 16, v159\n"
    "v_xor_b32 v159, v159, v156\n"
    "v_and_b32 v159, 7, v159\n"
    "v_and_b32 v90, 0x380, v1\n"
    "v_lshl_add_u32 v90, v159, 4, v90\n"
    "v_and_b32 v2, 7, v156\n"
    "v_lshrrev_b32 v144, 3, %3\n"
    "v_mul_u32_u24 v144, 768, v144\n"
    "v_lshl_add_u32 v144, v2, 3, v144\n"
    "buffer_load_dwordx4 v[44:47], v242, s[40:43], s75 offen offset:256 nt\n"
    "v_add_u32 v144, 131072, v144\n"
    "v_mad_u32_u24 v86, v2, 24, v144\n"
    "v_and_b32 v84, 15, v156\n"
    "v_lshrrev_b32 v159, 4, v156\n"
    "v_xor_b32 v159, v159, v2\n"
    "v_mul_u32_u24 v84, 384, v84\n"
    "v_lshl_add_u32 v84, v159, 4, v84\n"
    "s_add_u32 s73, s65, 49152\n"
    "v_add_u32 v84, s73, v84\n"
    "v_xor_b32 v85, 64, v84\n"
    "v_subrev_u32 v154, 24576, v84\n"
    "v_subrev_u32 v155, 24576, v85\n"
    "s_add_u32 s67, s65, 98304\n"
    "s_add_u32 s68, s65, 24576\n"
    "s_add_u32 s69, s65, 73728\n"
    "s_mov_b32 s81, 256\n"
    "s_mul_i32 s74, s79, 24576\n"
    "s_add_u32 s70, s74, s65\n"
    "s_add_u32 s70, s70, 73728\n"
    "s_sub_u32 s64, s65, s74\n"
    "s_add_u32 s64, s64, 98304\n"
    "v_and_b32 v249, 0x310, v1\n"
    "v_and_b32 v159, 12, v156\n"
    "v_lshl_add_u32 v249, v159, 3, v249\n"
    "v_and_b32 v159, 2, v156\n"
    "buffer_load_dwordx4 v[48:51], v242, s[40:43], s75 offen offset:1024 nt\n"
    "v_lshl_add_u32 v249, v159, 6, v249\n"
    "s_add_u32 s73, s74, 73728\n"
    "v_add_u32 v248, s73, v249\n"
    "s_sub_u32 s73, 98304, s74\n"
    "v_add_u32 v249, s73, v249\n"
    "s_or_b32 s73, s53, s84\n"
    "s_cmp_eq_u32 s73, 0\n"
    "s_cselect_b32 s82, 0, 0x7fff0000\n"
    "v_and_b32 v252, 12, v156\n"
    "v_bfe_u32 v159, v156, 1, 1\n"
    "v_or_b32 v252, v252, v159\n"
    "v_and_b32 v159, 1, v156\n"
    "v_lshl_or_b32 v252, v159, 1, v252\n"
    "v_lshlrev_b32 v252, 6, v252\n"
    "v_lshlrev_b32 v159, 2, v156\n"
    "v_and_b32 v159, 32, v159\n"
    "v_xor_b32 v159, v159, v156\n"
    "v_and_or_b32 v252, v159, 48, v252\n"
    "v_mov_b32 v2, v252\n"
    "v_add_u32 v3, 24576, v252\n"
    "v_lshrrev_b32 v91, 3, v156\n"
    "v_lshl_add_u32 v87, v91, 6, 64\n"
    "v_add_u32 v87, 64, v87\n"
    "v_and_b32 v241, 1, v156\n"
    "v_lshlrev_b32 v241, 3, v241\n"
    "buffer_load_dwordx4 v[52:55], v242, s[40:43], s75 offen offset:512 nt\n"
    "v_lshl_add_u32 v241, v91, 4, v241\n"
    "v_add_u32 v241, 1152, v241\n"
    "v_lshlrev_b32 v91, 24, v91\n"
    "v_lshl_add_u32 v91, v156, 3, v91\n"
    "s_lshl_b32 s73, s53, 4\n"
    "s_lshl_b32 s74, s59, 3\n"
    "s_add_u32 s73, s73, s74\n"
    "s_add_u32 s73, s73, 1152\n"
    "v_mov_b32 v240, 0x7fff0000\n"
    "v_writelane_b32 v240, s73, 0\n"
    "v_and_b32 v159, 48, v156\n"
    "s_mul_i32 s73, s79, 768\n"
    "s_sub_u32 s73, 131072, s73\n"
    "v_lshlrev_b32 v250, 1, v159\n"
    "v_add_u32 v250, s73, v250\n"
    "v_and_b32 v251, 3, v156\n"
    "v_lshl_add_u32 v251, v251, 7, v159\n"
    "s_add_u32 s73, s73, 256\n"
    "v_add_u32 v251, s73, v251\n"
    "v_and_b32 v159, 64, v1\n"
    "v_add_u32 v251, v159, v251\n"
    "v_bfe_i32 v159, v156, 2, 1\n"
    "v_and_b32 v159, 0xaaaa, v159\n"
    "v_or_b32 v253, 0x4444, v159\n"
    "s_mov_b32 s58, 0\n"
    "s_mov_b32 s59, 8\n"
    "s_waitcnt vmcnt(14)\n"
    "v_pk_min_i16 v4, v4, v10\n"
    "v_pk_min_i16 v5, v5, v11\n"
    "v_pk_min_i16 v6, v6, v12\n"
    "v_pk_min_i16 v7, v7, v13\n"
    "v_pk_min_i16 v8, v8, v14\n"
    "v_pk_min_i16 v9, v9, v15\n"
    "v_pk_min_i16 v16, v16, v18\n"
    "v_pk_min_i16 v17, v17, v19\n"
    "v_pk_min_i16 v4, v4, v7\n"
    "v_pk_min_i16 v5, v5, v8\n"
    "v_pk_min_i16 v6, v6, v9\n"
    "v_pk_min_i16 v16, v16, v17\n"
    "v_pk_min_i16 v4, v4, v5\n"
    "v_lshlrev_b32 v17, 16, v16\n"
    "v_pk_min_i16 v4, v4, v6\n"
    "v_min_i32 v16, v16, v17\n"
    "v_lshlrev_b32 v5, 16, v4\n"
    "v_mov_b32 v7, 155920\n"
    "v_min_i32 v4, v4, v5\n"
    "v_lshl_add_u32 v6, s84, 2, v7\n"
    "v_min_i32_dpp v16, v16, v16 row_shr:1\n"
    "v_min_i32_dpp v4, v4, v4 row_shr:1\n"
    "s_nop 0\n"
    "v_min_i32_dpp v16, v16, v16 row_shr:2\n"
    "v_min_i32_dpp v4, v4, v4 row_shr:2\n"
    "s_nop 0\n"
    "v_min_i32_dpp v16, v16, v16 row_shr:4\n"
    "v_min_i32_dpp v4, v4, v4 row_shr:4\n"
    "s_nop 0\n"
    "v_min_i32_dpp v16, v16, v16 row_shr:8\n"
    "v_min_i32_dpp v4, v4, v4 row_shr:8\n"
    "s_nop 0\n"
    "v_min_i32_dpp v16, v16, v16 row_bcast:15 row_mask:0xa\n"
    "v_min_i32_dpp v4, v4, v4 row_bcast:15 row_mask:0xa\n"
    "s_nop 0\n"
    "v_min_i32_dpp v16, v16, v16 row_bcast:31 row_mask:0xc\n"
    "v_min_i32_dpp v4, v4, v4 row_bcast:31 row_mask:0xc\n"
    "v_readlane_b32 s35, v16, 63\n"
    "v_readlane_b32 s73, v4, 63\n"
    "s_max_i32 s35, s35, 0\n"
    "s_and_b32 s35, s35, 0xffff0000\n"
    "v_mov_b32 v4, s73\n"
    "ds_write_b32 v6, v4\n"
    "v_mov_b32 v12, -126\n"
    "v_mov_b32 v13, 0x3dd53b95\n"
    "v_mov_b32 v80, 0\n"
    "s_waitcnt lgkmcnt(0)\n"
    "s_barrier\n"
    "ds_read_b128 v[8:11], v7 offset:0\n"
    "buffer_load_dwordx4 v[56:59], v242, s[40:43], s75 offen offset:1280 nt\n"
    "buffer_load_dwordx4 v[136:139], v130, s[92:95], 0 offen offset:0\n"
    "buffer_load_dwordx4 v[140:143], v130, s[96:99], 0 offen offset:0\n"
    "v_accvgpr_write_b32 a0, 0\n"
    "v_accvgpr_write_b32 a1, 0\n"
    "v_accvgpr_write_b32 a2, 0\n"
    "v_accvgpr_write_b32 a3, 0\n"
    "v_accvgpr_write_b32 a4, 0\n"
    "v_accvgpr_write_b32 a5, 0\n"
    "v_accvgpr_write_b32 a6, 0\n"
    "v_accvgpr_write_b32 a7, 0\n"
    "v_accvgpr_write_b32 a8, 0\n"
    "v_accvgpr_write_b32 a9, 0\n"
    "v_accvgpr_write_b32 a10, 0\n"
    "v_accvgpr_write_b32 a11, 0\n"
    "v_accvgpr_write_b32 a12, 0\n"
    "v_accvgpr_write_b32 a13, 0\n"
    "v_accvgpr_write_b32 a14, 0\n"
    "v_accvgpr_write_b32 a15, 0\n"
    "v_accvgpr_write_b32 a16, 0\n"
    "v_accvgpr_write_b32 a17, 0\n"
    "v_accvgpr_write_b32 a18, 0\n"
    "v_accvgpr_write_b32 a19, 0\n"
    "v_accvgpr_write_b32 a20, 0\n"
    "v_accvgpr_write_b32 a21, 0\n"
    "v_accvgpr_write_b32 a22, 0\n"
    "v_accvgpr_write_b32 a23, 0\n"
    "v_accvgpr_write_b32 a24, 0\n"
    "v_accvgpr_write_b32 a25, 0\n"
    "v_accvgpr_write_b32 a26, 0\n"
    "v_accvgpr_write_b32 a27, 0\n"
    "v_accvgpr_write_b32 a28, 0\n"
    "v_accvgpr_write_b32 a29, 0\n"
    "v_accvgpr_write_b32 a30, 0\n"
    "v_accvgpr_write_b32 a31, 0\n"
    "v_accvgpr_write_b32 a32, 0\n"
    "v_accvgpr_write_b32 a33, 0\n"
    "v_accvgpr_write_b32 a34, 0\n"
    "v_accvgpr_write_b32 a35, 0\n"
    "v_accvgpr_write_b32 a36, 0\n"
    "v_accvgpr_write_b32 a37, 0\n"
    "v_accvgpr_write_b32 a38, 0\n"
    "v_accvgpr_write_b32 a39, 0\n"
    "v_accvgpr_write_b32 a40, 0\n"
    "v_accvgpr_write_b32 a41, 0\n"
    "v_accvgpr_write_b32 a42, 0\n"
    "v_accvgpr_write_b32 a43, 0\n"
    "v_accvgpr_write_b32 a44, 0\n"
    "v_accvgpr_write_b32 a45, 0\n"
    "v_accvgpr_write_b32 a46, 0\n"
    "v_accvgpr_write_b32 a47, 0\n"
    "v_accvgpr_write_b32 a48, 0\n"
    "v_accvgpr_write_b32 a49, 0\n"
    "v_accvgpr_write_b32 a50, 0\n"
    "v_accvgpr_write_b32 a51, 0\n"
    "v_accvgpr_write_b32 a52, 0\n"
    "v_accvgpr_write_b32 a53, 0\n"
    "v_accvgpr_write_b32 a54, 0\n"
    "v_accvgpr_write_b32 a55, 0\n"
    "v_accvgpr_write_b32 a56, 0\n"
    "v_accvgpr_write_b32 a57, 0\n"
    "v_accvgpr_write_b32 a58, 0\n"
    "v_accvgpr_write_b32 a59, 0\n"
    "s_waitcnt lgkmcnt(0)\n"
    "v_min3_i32 v4, v8, v9, v10\n"
    "v_min_i32 v4, v4, v11\n"
    "v_max_i32 v4, 0, v4\n"
    "v_and_b32 v4, 0xffff0000, v4\n"
    "v_mul_f32 v4, s35, v4\n"
    "v_mul_f32 v4, 0x3ed53b95, v4\n"
    "v_lshrrev_b32 v4, 23, v4\n"
    "v_sub_u32 v4, 2, v4\n"
    "v_med3_i32 v4, v4, 0, v12\n"
    "v_ldexp_f32 v13, v13, v4\n"
    "v_readfirstlane_b32 s89, v4\n"
    "v_readfirstlane_b32 s88, v13\n"
    "s_mul_i32 s89, s89, 0xff7fff80\n"
    "s_waitcnt vmcnt(11)\n"
    "s_add_u32 s54, s71, s60\n"
    "s_add_u32 s55, s71, s61\n"
    "s_add_u32 s56, s71, s62\n"
    "v_cvt_pk_u16_u32 v204, v204, v208\n"
    "v_cvt_pk_u16_u32 v205, v205, v209\n"
    "v_cvt_pk_u16_u32 v206, v206, v210\n"
    "v_cvt_pk_u16_u32 v207, v207, v211\n"
    "v_or3_b32 v243, v204, v205, v243\n"
    "v_or3_b32 v243, v206, v207, v243\n"
    "buffer_store_dword v204, v244, s[16:19], s54 offen offset:0 sc1\n"
    "buffer_store_dword v205, v244, s[16:19], s54 offen offset:1024 sc1\n"
    "buffer_load_dwordx4 v[4:7], v162, s[28:31], s57 offen offset:0\n"
    "buffer_load_dwordx4 v[8:11], v162, s[28:31], s57 offen offset:3072\n"
    "buffer_store_dword v206, v244, s[16:19], s54 offen offset:2048 sc1\n"
    "buffer_store_dword v207, v244, s[16:19], s54 offen offset:3072 sc1\n"
    "v_cvt_pk_u16_u32 v212, v212, v216\n"
    "v_cvt_pk_u16_u32 v213, v213, v217\n"
    "v_cvt_pk_u16_u32 v214, v214, v218\n"
    "v_cvt_pk_u16_u32 v215, v215, v219\n"
    "v_or3_b32 v243, v212, v213, v243\n"
    "v_or3_b32 v243, v214, v215, v243\n"
    "buffer_store_dword v212, v244, s[16:19], s55 offen offset:0 sc1\n"
    "buffer_store_dword v213, v244, s[16:19], s55 offen offset:1024 sc1\n"
    "buffer_store_dword v214, v244, s[16:19], s55 offen offset:2048 sc1\n"
    "buffer_load_dwordx4 v[12:15], v162, s[28:31], s57 offen offset:128\n"
    "buffer_store_dword v215, v244, s[16:19], s55 offen offset:3072 sc1\n"
    "v_cvt_pk_u16_u32 v220, v220, v224\n"
    "v_cvt_pk_u16_u32 v221, v221, v225\n"
    "v_cvt_pk_u16_u32 v222, v222, v226\n"
    "v_cvt_pk_u16_u32 v223, v223, v227\n"
    "v_or3_b32 v243, v220, v221, v243\n"
    "v_or3_b32 v243, v222, v223, v243\n"
    "buffer_store_dword v220, v244, s[16:19], s56 offen offset:0 sc1\n"
    "buffer_store_dword v221, v244, s[16:19], s56 offen offset:1024 sc1\n"
    "buffer_store_dword v222, v244, s[16:19], s56 offen offset:2048 sc1\n"
    "buffer_store_dword v223, v244, s[16:19], s56 offen offset:3072 sc1\n"
    "s_add_u32 s71, s71, 98304\n"
    "s_add_u32 m0, s65, 24576\n"
    "s_add_u32 s72, s85, 0\n"
    "buffer_load_dwordx4 v88, s[20:23], s72 offen offset:0 nt lds\n"
    "buffer_load_dwordx4 v[16:19], v162, s[28:31], s57 offen offset:3200\n"
    "buffer_load_dwordx4 v[20:23], v162, s[28:31], s57 offen offset:256\n"
    "s_waitcnt vmcnt(25)\n"
    "v_add_co_u32 v238, vcc, 0x7f4a7c15, v238\n"
    "v_mov_b32 v159, 0x9e3779b9\n"
    "s_nop 0\n"
    "v_addc_co_u32 v239, vcc, v159, v239, vcc\n"
    "v_mov_b32 v246, 1\n"
    "s_waitcnt vmcnt(26)\n"
    "v_lshlrev_b32 v180, 16, v124\n"
    "v_and_b32 v181, 0xffff0000, v124\n"
    "v_lshlrev_b32 v182, 16, v125\n"
    "v_and_b32 v183, 0xffff0000, v125\n"
    "v_lshlrev_b32 v184, 16, v126\n"
    "v_and_b32 v185, 0xffff0000, v126\n"
    "v_lshlrev_b32 v186, 16, v127\n"
    "v_and_b32 v187, 0xffff0000, v127\n"
    "v_mul_f32 v180, s88, v180\n"
    "v_mul_f32 v181, s88, v181\n"
    "v_mul_f32 v182, s88, v182\n"
    "v_mul_f32 v183, s88, v183\n"
    "v_fmaak_f32 v184, v184, v180, 0x80730000\n"
    "v_fmaak_f32 v185, v185, v181, 0x80730000\n"
    "v_fmaak_f32 v186, v186, v182, 0x80730000\n"
    "v_fmaak_f32 v187, v187, v183, 0x80730000\n"
    "ds_write_b128 v131, v[180:183] offset:0\n"
    "ds_write_b128 v131, v[184:187] offset:1024\n"
    "ds_read_b128 v[180:183], v132 offset:0\n"
    "ds_read_b128 v[184:187], v132 offset:1024\n"
    "ds_read_b128 v[188:191], v132 offset:128\n"
    "ds_read_b128 v[192:195], v132 offset:1152\n"
    "ds_read_b128 v[196:199], v132 offset:256\n"
    "ds_read_b128 v[200:203], v132 offset:1280\n"
    "v_accvgpr_write_b32 a60, 0\n"
    "v_accvgpr_write_b32 a61, 0\n"
    "v_accvgpr_write_b32 a62, 0\n"
    "v_accvgpr_write_b32 a63, 0\n"
    "v_accvgpr_write_b32 a64, 0\n"
    "v_accvgpr_write_b32 a65, 0\n"
    "v_accvgpr_write_b32 a66, 0\n"
    "v_accvgpr_write_b32 a67, 0\n"
    "v_accvgpr_write_b32 a68, 0\n"
    "v_accvgpr_write_b32 a69, 0\n"
    "s_add_u32 m0, s65, 25600\n"
    "s_add_u32 s72, s85, 1024\n"
    "buffer_load_dwordx4 v89, s[20:23], s72 offen offset:0 nt lds\n"
    "buffer_load_dwordx4 v[24:27], v162, s[28:31], s57 offen offset:3328\n"
    "v_accvgpr_write_b32 a70, 0\n"
    "v_accvgpr_write_b32 a71, 0\n"
    "v_accvgpr_write_b32 a72, 0\n"
    "v_accvgpr_write_b32 a73, 0\n"
    "v_accvgpr_write_b32 a74, 0\n"
    "v_accvgpr_write_b32 a75, 0\n"
    "v_accvgpr_write_b32 a76, 0\n"
    "v_accvgpr_write_b32 a77, 0\n"
    "v_accvgpr_write_b32 a78, 0\n"
    "v_accvgpr_write_b32 a79, 0\n"
    "v_accvgpr_write_b32 a80, 0\n"
    "v_accvgpr_write_b32 a81, 0\n"
    "v_accvgpr_write_b32 a82, 0\n"
    "v_accvgpr_write_b32 a83, 0\n"
    "v_accvgpr_write_b32 a84, 0\n"
    "v_accvgpr_write_b32 a85, 0\n"
    "v_accvgpr_write_b32 a86, 0\n"
    "v_accvgpr_write_b32 a87, 0\n"
    "v_accvgpr_write_b32 a88, 0\n"
    "v_accvgpr_write_b32 a89, 0\n"
    "v_accvgpr_write_b32 a90, 0\n"
    "v_accvgpr_write_b32 a91, 0\n"
    "v_accvgpr_write_b32 a92, 0\n"
    "v_accvgpr_write_b32 a93, 0\n"
    "v_accvgpr_write_b32 a94, 0\n"
    "v_accvgpr_write_b32 a95, 0\n"
    "v_accvgpr_write_b32 a96, 0\n"
    "v_accvgpr_write_b32 a97, 0\n"
    "v_accvgpr_write_b32 a98, 0\n"
    "v_accvgpr_write_b32 a99, 0\n"
    "v_accvgpr_write_b32 a100, 0\n"
    "v_accvgpr_write_b32 a101, 0\n"
    "v_accvgpr_write_b32 a102, 0\n"
    "v_accvgpr_write_b32 a103, 0\n"
    "v_accvgpr_write_b32 a104, 0\n"
    "v_accvgpr_write_b32 a105, 0\n"
    "v_accvgpr_write_b32 a106, 0\n"
    "v_accvgpr_write_b32 a107, 0\n"
    "v_accvgpr_write_b32 a108, 0\n"
    "v_accvgpr_write_b32 a109, 0\n"
    "v_accvgpr_write_b32 a110, 0\n"
    "s_add_u32 m0, s65, 26624\n"
    "s_add_u32 s72, s85, 2048\n"
    "buffer_load_dwordx4 v90, s[20:23], s72 offen offset:0 nt lds\n"
    "buffer_load_dwordx4 v[100:103], v162, s[28:31], s66 offen offset:0\n"
    "buffer_load_dwordx4 v[104:107], v162, s[28:31], s66 offen offset:3072\n"
    "v_accvgpr_write_b32 a111, 0\n"
    "v_accvgpr_write_b32 a112, 0\n"
    "v_accvgpr_write_b32 a113, 0\n"
    "v_accvgpr_write_b32 a114, 0\n"
    "v_accvgpr_write_b32 a115, 0\n"
    "v_accvgpr_write_b32 a116, 0\n"
    "v_accvgpr_write_b32 a117, 0\n"
    "v_accvgpr_write_b32 a118, 0\n"
    "v_accvgpr_write_b32 a119, 0\n"
    "v_accvgpr_write_b32 a120, 0\n"
    "v_accvgpr_write_b32 a121, 0\n"
    "v_accvgpr_write_b32 a122, 0\n"
    "v_accvgpr_write_b32 a123, 0\n"
    "v_accvgpr_write_b32 a124, 0\n"
    "v_accvgpr_write_b32 a125, 0\n"
    "v_accvgpr_write_b32 a126, 0\n"
    "v_accvgpr_write_b32 a127, 0\n"
    "v_accvgpr_write_b32 a128, 0\n"
    "v_accvgpr_write_b32 a129, 0\n"
    "v_accvgpr_write_b32 a130, 0\n"
    "v_accvgpr_write_b32 a131, 0\n"
    "v_accvgpr_write_b32 a132, 0\n"
    "v_accvgpr_write_b32 a133, 0\n"
    "v_accvgpr_write_b32 a134, 0\n"
    "v_accvgpr_write_b32 a135, 0\n"
    "v_accvgpr_write_b32 a136, 0\n"
    "v_accvgpr_write_b32 a137, 0\n"
    "v_accvgpr_write_b32 a138, 0\n"
    "v_accvgpr_write_b32 a139, 0\n"
    "v_accvgpr_write_b32 a140, 0\n"
    "v_accvgpr_write_b32 a141, 0\n"
    "v_accvgpr_write_b32 a142, 0\n"
    "v_accvgpr_write_b32 a143, 0\n"
    "v_accvgpr_write_b32 a144, 0\n"
    "v_accvgpr_write_b32 a145, 0\n"
    "v_accvgpr_write_b32 a146, 0\n"
    "v_accvgpr_write_b32 a147, 0\n"
    "v_accvgpr_write_b32 a148, 0\n"
    "v_accvgpr_write_b32 a149, 0\n"
    "v_accvgpr_write_b32 a150, 0\n"
    "s_add_u32 m0, s65, 27648\n"
    "s_add_u32 s72, s85, 3072\n"
    "buffer_load_dwordx4 v88, s[20:23], s72 offen offset:0 nt lds\n"
    "buffer_load_dwordx4 v[108:111], v162, s[28:31], s66 offen offset:128\n"
    "v_accvgpr_write_b32 a151, 0\n"
    "v_accvgpr_write_b32 a152, 0\n"
    "v_accvgpr_write_b32 a153, 0\n"
    "v_accvgpr_write_b32 a154, 0\n"
    "v_accvgpr_write_b32 a155, 0\n"
    "v_accvgpr_write_b32 a156, 0\n"
    "v_accvgpr_write_b32 a157, 0\n"
    "v_accvgpr_write_b32 a158, 0\n"
    "v_accvgpr_write_b32 a159, 0\n"
    "v_accvgpr_write_b32 a160, 0\n"
    "v_accvgpr_write_b32 a161, 0\n"
    "v_accvgpr_write_b32 a162, 0\n"
    "v_accvgpr_write_b32 a163, 0\n"
    "v_accvgpr_write_b32 a164, 0\n"
    "v_accvgpr_write_b32 a165, 0\n"
    "v_accvgpr_write_b32 a166, 0\n"
    "v_accvgpr_write_b32 a167, 0\n"
    "v_accvgpr_write_b32 a168, 0\n"
    "v_accvgpr_write_b32 a169, 0\n"
    "v_accvgpr_write_b32 a170, 0\n"
    "v_accvgpr_write_b32 a171, 0\n"
    "v_accvgpr_write_b32 a172, 0\n"
    "v_accvgpr_write_b32 a173, 0\n"
    "v_accvgpr_write_b32 a174, 0\n"
    "v_accvgpr_write_b32 a175, 0\n"
    "v_accvgpr_write_b32 a176, 0\n"
    "v_accvgpr_write_b32 a177, 0\n"
    "v_accvgpr_write_b32 a178, 0\n"
    "v_accvgpr_write_b32 a179, 0\n"
    "v_accvgpr_write_b32 a180, 0\n"
    "v_accvgpr_write_b32 a181, 0\n"
    "v_accvgpr_write_b32 a182, 0\n"
    "v_accvgpr_write_b32 a183, 0\n"
    "v_accvgpr_write_b32 a184, 0\n"
    "v_accvgpr_write_b32 a185, 0\n"
    "v_accvgpr_write_b32 a186, 0\n"
    "v_accvgpr_write_b32 a187, 0\n"
    "v_accvgpr_write_b32 a188, 0\n"
    "v_accvgpr_write_b32 a189, 0\n"
    "v_accvgpr_write_b32 a190, 0\n"
    "v_accvgpr_write_b32 a191, 0\n"
    "s_waitcnt lgkmcnt(0)\n"
    "s_waitcnt vmcnt(10)\n"
    "ds_add_rtn_u32 v245, v247, v246\n"
    "s_lshl_b32 s73, s53, 6\n"
    "s_add_u32 s73, s73, 128\n"
    "s_waitcnt lgkmcnt(0)\n"
    "v_readfirstlane_b32 s74, v245\n"
    "s_nop 0\n"
    "s_cmp_eq_u32 s74, 3\n"
    "s_cselect_b32 s73, s73, 0x7fff0000\n"
    "buffer_store_dwordx2 v[238:239], v91, s[36:39], s73 offen offset:0 sc1\n"
    "s_waitcnt vmcnt(28)\n"
    "s_add_u32 m0, s65, 28672\n"
    "s_add_u32 s72, s85, 4096\n"
    "buffer_load_dwordx4 v89, s[20:23], s72 offen offset:0 nt lds\n"
    "buffer_load_dwordx4 v[112:115], v162, s[28:31], s66 offen offset:3200\n"
    "buffer_load_dwordx4 v[116:119], v162, s[28:31], s66 offen offset:256\n"
    "s_add_u32 s54, s71, s60\n"
    "s_add_u32 s55, s71, s61\n"
    "s_add_u32 s56, s71, s62\n"
    "v_cvt_pk_u16_u32 v36, v36, v40\n"
    "v_cvt_pk_u16_u32 v37, v37, v41\n"
    "v_cvt_pk_u16_u32 v38, v38, v42\n"
    "v_cvt_pk_u16_u32 v39, v39, v43\n"
    "v_or3_b32 v243, v36, v37, v243\n"
    "v_or3_b32 v243, v38, v39, v243\n"
    "buffer_store_dword v36, v244, s[16:19], s54 offen offset:0 sc1\n"
    "buffer_store_dword v37, v244, s[16:19], s54 offen offset:1024 sc1\n"
    "buffer_store_dword v38, v244, s[16:19], s54 offen offset:2048 sc1\n"
    "buffer_store_dword v39, v244, s[16:19], s54 offen offset:3072 sc1\n"
    "v_cvt_pk_u16_u32 v44, v44, v48\n"
    "v_cvt_pk_u16_u32 v45, v45, v49\n"
    "v_cvt_pk_u16_u32 v46, v46, v50\n"
    "s_add_u32 m0, s65, 29696\n"
    "s_add_u32 s72, s85, 5120\n"
    "buffer_load_dwordx4 v90, s[20:23], s72 offen offset:0 nt lds\n"
    "buffer_load_dwordx4 v[120:123], v162, s[28:31], s66 offen offset:3328\n"
    "v_cvt_pk_u16_u32 v47, v47, v51\n"
    "v_or3_b32 v243, v44, v45, v243\n"
    "v_or3_b32 v243, v46, v47, v243\n"
    "buffer_store_dword v44, v244, s[16:19], s55 offen offset:0 sc1\n"
    "buffer_store_dword v45, v244, s[16:19], s55 offen offset:1024 sc1\n"
    "buffer_store_dword v46, v244, s[16:19], s55 offen offset:2048 sc1\n"
    "buffer_store_dword v47, v244, s[16:19], s55 offen offset:3072 sc1\n"
    "v_cvt_pk_u16_u32 v52, v52, v56\n"
    "v_cvt_pk_u16_u32 v53, v53, v57\n"
    "v_cvt_pk_u16_u32 v54, v54, v58\n"
    "v_cvt_pk_u16_u32 v55, v55, v59\n"
    "v_or3_b32 v243, v52, v53, v243\n"
    "v_or3_b32 v243, v54, v55, v243\n"
    "buffer_store_dword v52, v244, s[16:19], s56 offen offset:0 sc1\n"
    "buffer_store_dword v53, v244, s[16:19], s56 offen offset:1024 sc1\n"
    "buffer_store_dword v54, v244, s[16:19], s56 offen offset:2048 sc1\n"
    "buffer_store_dword v55, v244, s[16:19], s56 offen offset:3072 sc1\n"
    "s_add_u32 m0, s65, 73728\n"
    "s_add_u32 s72, s85, 6144\n"
    "buffer_load_dwordx4 v88, s[20:23], s72 offen offset:0 nt lds\n"
    "s_waitcnt vmcnt(44)\n"
    "v_lshlrev_b32 v204, 16, v136\n"
    "v_and_b32 v205, 0xffff0000, v136\n"
    "v_lshlrev_b32 v206, 16, v137\n"
    "v_and_b32 v207, 0xffff0000, v137\n"
    "v_lshlrev_b32 v208, 16, v138\n"
    "v_and_b32 v209, 0xffff0000, v138\n"
    "v_lshlrev_b32 v210, 16, v139\n"
    "v_and_b32 v211, 0xffff0000, v139\n"
    "v_log_f32 v68, v204\n"
    "v_log_f32 v69, v205\n"
    "v_log_f32 v70, v206\n"
    "v_log_f32 v71, v207\n"
    "v_log_f32 v72, v208\n"
    "v_log_f32 v73, v209\n"
    "v_log_f32 v74, v210\n"
    "v_log_f32 v75, v211\n"
    "v_rcp_f32 v204, v204\n"
    "v_rcp_f32 v205, v205\n"
    "v_rcp_f32 v206, v206\n"
    "v_rcp_f32 v207, v207\n"
    "v_rcp_f32 v208, v208\n"
    "v_rcp_f32 v209, v209\n"
    "v_rcp_f32 v210, v210\n"
    "v_rcp_f32 v211, v211\n"
    "s_mov_b32 s73, 0x41a00000\n"
    "v_mov_b32 v228, 0x7fc00000\n"
    "v_cmp_le_f32 vcc, |v68|, s73\n"
    "s_nop 1\n"
    "v_cndmask_b32 v68, v228, v68, vcc\n"
    "v_cmp_le_f32 vcc, |v69|, s73\n"
    "s_nop 1\n"
    "v_cndmask_b32 v69, v228, v69, vcc\n"
    "v_cmp_le_f32 vcc, |v70|, s73\n"
    "s_nop 1\n"
    "v_cndmask_b32 v70, v228, v70, vcc\n"
    "v_cmp_le_f32 vcc, |v71|, s73\n"
    "s_nop 1\n"
    "v_cndmask_b32 v71, v228, v71, vcc\n"
    "s_add_u32 m0, s65, 74752\n"
    "s_add_u32 s72, s85, 7168\n"
    "buffer_load_dwordx4 v89, s[20:23], s72 offen offset:0 nt lds\n"
    "v_cmp_le_f32 vcc, |v72|, s73\n"
    "s_nop 1\n"
    "v_cndmask_b32 v72, v228, v72, vcc\n"
    "v_cmp_le_f32 vcc, |v73|, s73\n"
    "s_nop 1\n"
    "v_cndmask_b32 v73, v228, v73, vcc\n"
    "v_cmp_le_f32 vcc, |v74|, s73\n"
    "s_nop 1\n"
    "v_cndmask_b32 v74, v228, v74, vcc\n"
    "v_cmp_le_f32 vcc, |v75|, s73\n"
    "s_nop 1\n"
    "v_cndmask_b32 v75, v228, v75, vcc\n"
    "v_cvt_pk_bf16_f32 v220, v204, v205\n"
    "v_cvt_pk_bf16_f32 v221, v206, v207\n"
    "v_cvt_pk_bf16_f32 v222, v208, v209\n"
    "v_cvt_pk_bf16_f32 v223, v210, v211\n"
    "v_lshlrev_b32 v212, 16, v220\n"
    "v_and_b32 v213, 0xffff0000, v220\n"
    "v_lshlrev_b32 v214, 16, v221\n"
    "v_and_b32 v215, 0xffff0000, v221\n"
    "v_lshlrev_b32 v216, 16, v222\n"
    "v_and_b32 v217, 0xffff0000, v222\n"
    "v_lshlrev_b32 v218, 16, v223\n"
    "v_and_b32 v219, 0xffff0000, v223\n"
    "v_sub_f32 v204, v204, v212\n"
    "v_sub_f32 v205, v205, v213\n"
    "v_sub_f32 v206, v206, v214\n"
    "v_sub_f32 v207, v207, v215\n"
    "v_sub_f32 v208, v208, v216\n"
    "v_sub_f32 v209, v209, v217\n"
    "v_sub_f32 v210, v210, v218\n"
    "v_sub_f32 v211, v211, v219\n"
    "v_cvt_pk_bf16_f32 v224, v204, v205\n"
    "v_cvt_pk_bf16_f32 v225, v206, v207\n"
    "v_cvt_pk_bf16_f32 v226, v208, v209\n"
    "v_cvt_pk_bf16_f32 v227, v210, v211\n"
    "v_mov_b32 v204, 0\n"
    "v_mov_b32 v205, 0\n"
    "v_mov_b32 v206, 0\n"
    "s_add_u32 m0, s65, 75776\n"
    "s_add_u32 s72, s85, 8192\n"
    "buffer_load_dwordx4 v90, s[20:23], s72 offen offset:0 nt lds\n"
    "v_mov_b32 v207, 0\n"
    "v_swap_b32 v221, v222\n"
    "v_swap_b32 v225, v226\n"
    "v_swap_b32 v141, v142\n"
    "ds_write2_b64 v144, v[220:221], v[222:223] offset0:32 offset1:40\n"
    "ds_write2_b64 v144, v[224:225], v[226:227] offset0:48 offset1:56\n"
    "ds_write2_b64 v144, v[140:141], v[142:143] offset0:64 offset1:72\n"
    "ds_write2_b64 v144, v[204:205], v[206:207] offset0:80 offset1:88\n"
    "s_waitcnt vmcnt(26)\n"
    "v_cvt_f32_i32 v4, v4\n"
    "v_cvt_f32_i32 v5, v5\n"
    "v_cvt_f32_i32 v6, v6\n"
    "v_cvt_f32_i32 v7, v7\n"
    "v_cvt_f32_i32 v8, v8\n"
    "v_cvt_f32_i32 v9, v9\n"
    "v_cvt_f32_i32 v10, v10\n"
    "v_cvt_f32_i32 v11, v11\n"
    "v_cvt_f32_i32 v12, v12\n"
    "v_cvt_f32_i32 v13, v13\n"
    "v_cvt_f32_i32 v14, v14\n"
    "v_cvt_f32_i32 v15, v15\n"
    "v_cvt_f32_i32 v16, v16\n"
    "v_cvt_f32_i32 v17, v17\n"
    "v_cvt_f32_i32 v18, v18\n"
    "v_cvt_f32_i32 v19, v19\n"
    "v_cvt_f32_i32 v20, v20\n"
    "v_cvt_f32_i32 v21, v21\n"
    "v_cvt_f32_i32 v22, v22\n"
    "v_cvt_f32_i32 v23, v23\n"
    "v_cvt_f32_i32 v24, v24\n"
    "v_cvt_f32_i32 v25, v25\n"
    "v_cvt_f32_i32 v26, v26\n"
    "v_cvt_f32_i32 v27, v27\n"
    "v_fma_f32 v4, v4, v180, -v184\n"
    "v_fma_f32 v5, v5, v181, -v185\n"
    "v_fma_f32 v6, v6, v182, -v186\n"
    "s_add_u32 m0, s65, 76800\n"
    "s_add_u32 s72, s85, 9216\n"
    "buffer_load_dwordx4 v88, s[20:23], s72 offen offset:0 nt lds\n"
    "v_fma_f32 v7, v7, v183, -v187\n"
    "v_fma_f32 v8, v8, v180, -v184\n"
    "v_fma_f32 v9, v9, v181, -v185\n"
    "v_fma_f32 v10, v10, v182, -v186\n"
    "v_fma_f32 v11, v11, v183, -v187\n"
    "v_cvt_pk_bf16_f32 v4, v4, v5\n"
    "v_cvt_pk_bf16_f32 v5, v6, v7\n"
    "v_cvt_pk_bf16_f32 v6, v8, v9\n"
    "v_cvt_pk_bf16_f32 v7, v10, v11\n"
    "ds_write_b64 v163, v[4:5] offset:0\n"
    "ds_write_b64 v163, v[6:7] offset:1024\n"
    "v_fma_f32 v12, v12, v188, -v192\n"
    "v_fma_f32 v13, v13, v189, -v193\n"
    "v_fma_f32 v14, v14, v190, -v194\n"
    "v_fma_f32 v15, v15, v191, -v195\n"
    "v_fma_f32 v16, v16, v188, -v192\n"
    "v_fma_f32 v17, v17, v189, -v193\n"
    "v_fma_f32 v18, v18, v190, -v194\n"
    "v_fma_f32 v19, v19, v191, -v195\n"
    "v_cvt_pk_bf16_f32 v12, v12, v13\n"
    "v_cvt_pk_bf16_f32 v13, v14, v15\n"
    "v_cvt_pk_bf16_f32 v14, v16, v17\n"
    "v_cvt_pk_bf16_f32 v15, v18, v19\n"
    "ds_write_b64 v163, v[12:13] offset:2048\n"
    "ds_write_b64 v163, v[14:15] offset:3072\n"
    "v_fma_f32 v20, v20, v196, -v200\n"
    "v_fma_f32 v21, v21, v197, -v201\n"
    "v_fma_f32 v22, v22, v198, -v202\n"
    "v_fma_f32 v23, v23, v199, -v203\n"
    "v_fma_f32 v24, v24, v196, -v200\n"
    "v_fma_f32 v25, v25, v197, -v201\n"
    "v_fma_f32 v26, v26, v198, -v202\n"
    "v_fma_f32 v27, v27, v199, -v203\n"
    "v_cvt_pk_bf16_f32 v20, v20, v21\n"
    "v_cvt_pk_bf16_f32 v21, v22, v23\n"
    "s_add_u32 m0, s65, 77824\n"
    "s_add_u32 s72, s85, 10240\n"
    "buffer_load_dwordx4 v89, s[20:23], s72 offen offset:0 nt lds\n"
    "v_cvt_pk_bf16_f32 v22, v24, v25\n"
    "v_cvt_pk_bf16_f32 v23, v26, v27\n"
    "ds_write_b64 v163, v[20:21] offset:4096\n"
    "ds_write_b64 v163, v[22:23] offset:5120\n"
    "s_waitcnt vmcnt(13)\n"
    "v_cvt_f32_i32 v100, v100\n"
    "v_cvt_f32_i32 v101, v101\n"
    "v_cvt_f32_i32 v102, v102\n"
    "v_cvt_f32_i32 v103, v103\n"
    "v_cvt_f32_i32 v104, v104\n"
    "v_cvt_f32_i32 v105, v105\n"
    "v_cvt_f32_i32 v106, v106\n"
    "v_cvt_f32_i32 v107, v107\n"
    "v_cvt_f32_i32 v108, v108\n"
    "v_cvt_f32_i32 v109, v109\n"
    "v_cvt_f32_i32 v110, v110\n"
    "v_cvt_f32_i32 v111, v111\n"
    "v_cvt_f32_i32 v112, v112\n"
    "v_cvt_f32_i32 v113, v113\n"
    "v_cvt_f32_i32 v114, v114\n"
    "v_cvt_f32_i32 v115, v115\n"
    "v_cvt_f32_i32 v116, v116\n"
    "v_cvt_f32_i32 v117, v117\n"
    "v_cvt_f32_i32 v118, v118\n"
    "v_cvt_f32_i32 v119, v119\n"
    "v_cvt_f32_i32 v120, v120\n"
    "v_cvt_f32_i32 v121, v121\n"
    "v_cvt_f32_i32 v122, v122\n"
    "v_cvt_f32_i32 v123, v123\n"
    "v_fma_f32 v100, v100, v180, -v184\n"
    "v_fma_f32 v101, v101, v181, -v185\n"
    "v_fma_f32 v102, v102, v182, -v186\n"
    "v_fma_f32 v103, v103, v183, -v187\n"
    "v_fma_f32 v104, v104, v180, -v184\n"
    "v_fma_f32 v105, v105, v181, -v185\n"
    "s_add_u32 m0, s65, 78848\n"
    "s_add_u32 s72, s85, 11264\n"
    "buffer_load_dwordx4 v90, s[20:23], s72 offen offset:0 nt lds\n"
    "v_fma_f32 v106, v106, v182, -v186\n"
    "v_fma_f32 v107, v107, v183, -v187\n"
    "v_cvt_pk_bf16_f32 v100, v100, v101\n"
    "v_cvt_pk_bf16_f32 v101, v102, v103\n"
    "v_cvt_pk_bf16_f32 v102, v104, v105\n"
    "v_cvt_pk_bf16_f32 v103, v106, v107\n"
    "ds_write_b64 v163, v[100:101] offset:12288\n"
    "ds_write_b64 v163, v[102:103] offset:13312\n"
    "v_fma_f32 v108, v108, v188, -v192\n"
    "v_fma_f32 v109, v109, v189, -v193\n"
    "v_fma_f32 v110, v110, v190, -v194\n"
    "v_fma_f32 v111, v111, v191, -v195\n"
    "v_fma_f32 v112, v112, v188, -v192\n"
    "v_fma_f32 v113, v113, v189, -v193\n"
    "v_fma_f32 v114, v114, v190, -v194\n"
    "v_fma_f32 v115, v115, v191, -v195\n"
    "v_cvt_pk_bf16_f32 v108, v108, v109\n"
    "v_cvt_pk_bf16_f32 v109, v110, v111\n"
    "v_cvt_pk_bf16_f32 v110, v112, v113\n"
    "v_cvt_pk_bf16_f32 v111, v114, v115\n"
    "ds_write_b64 v163, v[108:109] offset:14336\n"
    "ds_write_b64 v163, v[110:111] offset:15360\n"
    "v_fma_f32 v116, v116, v196, -v200\n"
    "v_fma_f32 v117, v117, v197, -v201\n"
    "v_fma_f32 v118, v118, v198, -v202\n"
    "v_fma_f32 v119, v119, v199, -v203\n"
    "v_fma_f32 v120, v120, v196, -v200\n"
    "v_fma_f32 v121, v121, v197, -v201\n"
    "v_fma_f32 v122, v122, v198, -v202\n"
    "v_fma_f32 v123, v123, v199, -v203\n"
    "v_cvt_pk_bf16_f32 v116, v116, v117\n"
    "v_cvt_pk_bf16_f32 v117, v118, v119\n"
    "v_cvt_pk_bf16_f32 v118, v120, v121\n"
    "v_cvt_pk_bf16_f32 v119, v122, v123\n"
    "ds_write_b64 v163, v[116:117] offset:16384\n"
    "ds_write_b64 v163, v[118:119] offset:17408\n"
    "s_waitcnt vmcnt(15)\n"
    "ds_read_b128 v[36:39], v154 offset:0\n"
    "ds_read_b128 v[40:43], v155 offset:0\n"
    "ds_read_b128 v[44:47], v154 offset:128\n"
    "ds_read_b128 v[48:51], v155 offset:128\n"
    "ds_read_b128 v[52:55], v154 offset:256\n"
    "ds_read_b128 v[56:59], v155 offset:256\n"
    "s_mov_b64 exec, 0\n"
    "s_bitcmp1_b32 s83, 1\n"
    "s_cbranch_scc1 .LwV\n"
    "s_cmp_eq_u32 0, 0\n"
    "s_branch .LwK\n"
    ".Lw2:\n"
    "s_bitcmp1_b32 s83, 1\n"
    "s_cbranch_scc1 .Lwp1\n"
    "s_branch .Lwp0\n"
    ".Lw3:\n"
    "s_mov_b64 exec, -1\n"
    "s_waitcnt lgkmcnt(0)\n"
    "s_barrier\n"
    "ds_read_b128 v[156:159], v2 offset:0\n"
    "ds_read_b128 v[160:163], v2 offset:2048\n"
    "ds_read_b128 v[164:167], v2 offset:4096\n"
    "ds_read_b128 v[168:171], v2 offset:6144\n"
    "ds_read_b128 v[172:175], v2 offset:8192\n"
    "ds_read_b128 v[176:179], v2 offset:10240\n"
    "ds_read_b128 v[180:183], v2 offset:1024\n"
    "ds_read_b128 v[184:187], v2 offset:3072\n"
    "ds_read_b128 v[188:191], v2 offset:5120\n"
    "ds_read_b128 v[192:195], v2 offset:7168\n"
    "ds_read_b128 v[196:199], v2 offset:9216\n"
    "ds_read_b128 v[200:203], v2 offset:11264\n"
    "s_add_u32 m0, s65, 49152\n"
    "s_add_u32 s72, s85, 12288\n"
    "buffer_load_dwordx4 v88, s[20:23], s72 offen offset:0 nt lds\n"
    "v_pk_maximum3_f16 v80, v36, v37, v80\n"
    "v_pk_maximum3_f16 v80, v38, v39, v80\n"
    "v_pk_add_u16 v36, s89, v36 clamp\n"
    "v_pk_add_u16 v37, s89, v37 clamp\n"
    "v_pk_add_u16 v38, s89, v38 clamp\n"
    "v_pk_add_u16 v39, s89, v39 clamp\n"
    "v_pk_maximum3_f16 v80, v40, v41, v80\n"
    "v_pk_maximum3_f16 v80, v42, v43, v80\n"
    "v_pk_add_u16 v40, s89, v40 clamp\n"
    "v_pk_add_u16 v41, s89, v41 clamp\n"
    "v_pk_add_u16 v42, s89, v42 clamp\n"
    "v_pk_add_u16 v43, s89, v43 clamp\n"
    "v_pk_maximum3_f16 v80, v44, v45, v80\n"
    "v_pk_maximum3_f16 v80, v46, v47, v80\n"
    "v_pk_add_u16 v44, s89, v44 clamp\n"
    "v_pk_add_u16 v45, s89, v45 clamp\n"
    "v_pk_add_u16 v46, s89, v46 clamp\n"
    "v_pk_add_u16 v47, s89, v47 clamp\n"
    "v_pk_maximum3_f16 v80, v48, v49, v80\n"
    "v_pk_maximum3_f16 v80, v50, v51, v80\n"
    "v_pk_add_u16 v48, s89, v48 clamp\n"
    "v_pk_add_u16 v49, s89, v49 clamp\n"
    "v_pk_add_u16 v50, s89, v50 clamp\n"
    "v_pk_add_u16 v51, s89, v51 clamp\n"
    "v_pk_maximum3_f16 v80, v52, v53, v80\n"
    "v_pk_maximum3_f16 v80, v54, v55, v80\n"
    "v_pk_add_u16 v52, s89, v52 clamp\n"
    "v_pk_add_u16 v53, s89, v53 clamp\n"
    "v_pk_add_u16 v54, s89, v54 clamp\n"
    "v_pk_add_u16 v55, s89, v55 clamp\n"
    "v_pk_maximum3_f16 v80, v56, v57, v80\n"
    "v_pk_maximum3_f16 v80, v58, v59, v80\n"
    "v_pk_add_u16 v56, s89, v56 clamp\n"
    "v_pk_add_u16 v57, s89, v57 clamp\n"
    "v_pk_add_u16 v58, s89, v58 clamp\n"
    "v_pk_add_u16 v59, s89, v59 clamp\n"
    "v_mov_b32 v108, 0\n"
    "v_mov_b32 v109, 0\n"
    "v_mov_b32 v110, 0\n"
    "v_mov_b32 v111, 0\n"
    "v_mov_b32 v112, 0\n"
    "v_mov_b32 v113, 0\n"
    "v_mov_b32 v114, 0\n"
    "v_mov_b32 v115, 0\n"
    "v_mov_b32 v116, 0\n"
    "v_mov_b32 v117, 0\n"
    "v_mov_b32 v118, 0\n"
    "v_mov_b32 v119, 0\n"
    "v_mov_b32 v120, 0\n"
    "v_mov_b32 v121, 0\n"
    "v_mov_b32 v122, 0\n"
    "v_mov_b32 v123, 0\n"
    "s_waitcnt lgkmcnt(11)\n"
    "v_mfma_f32_16x16x32_bf16 v[4:7], v[156:159], v[36:39], 0\n"
    "s_waitcnt lgkmcnt(10)\n"
    "v_mfma_f32_16x16x32_bf16 v[4:7], v[160:163], v[40:43], v[4:7]\n"
    "s_waitcnt lgkmcnt(9)\n"
    "v_mfma_f32_16x16x32_bf16 v[4:7], v[164:167], v[44:47], v[4:7]\n"
    "s_waitcnt lgkmcnt(8)\n"
    "v_mfma_f32_16x16x32_bf16 v[4:7], v[168:171], v[48:51], v[4:7]\n"
    "s_waitcnt lgkmcnt(7)\n"
    "v_mfma_f32_16x16x32_bf16 v[4:7], v[172:175], v[52:55], v[4:7]\n"
    "s_waitcnt lgkmcnt(6)\n"
    "v_mfma_f32_16x16x32_bf16 v[4:7], v[176:179], v[56:59], v[4:7]\n"
    "s_waitcnt lgkmcnt(5)\n"
    "v_mfma_f32_16x16x32_bf16 v[20:23], v[180:183], v[36:39], 0\n"
    "v_accvgpr_write_b32 a192, v36\n"
    "v_accvgpr_write_b32 a193, v37\n"
    "v_accvgpr_write_b32 a194, v38\n"
    "v_accvgpr_write_b32 a195, v39\n"
    "s_waitcnt lgkmcnt(4)\n"
    "v_mfma_f32_16x16x32_bf16 v[20:23], v[184:187], v[40:43], v[20:23]\n"
    "v_accvgpr_write_b32 a196, v40\n"
    "v_accvgpr_write_b32 a197, v41\n"
    "v_accvgpr_write_b32 a198, v42\n"
    "v_accvgpr_write_b32 a199, v43\n"
    "s_waitcnt lgkmcnt(3)\n"
    "v_mfma_f32_16x16x32_bf16 v[20:23], v[188:191], v[44:47], v[20:23]\n"
    "v_accvgpr_write_b32 a200, v44\n"
    "v_accvgpr_write_b32 a201, v45\n"
    "v_accvgpr_write_b32 a202, v46\n"
    "v_accvgpr_write_b32 a203, v47\n"
    "s_waitcnt lgkmcnt(2)\n"
    "v_mfma_f32_16x16x32_bf16 v[20:23], v[192:195], v[48:51], v[20:23]\n"
    "v_accvgpr_write_b32 a204, v48\n"
    "v_accvgpr_write_b32 a205, v49\n"
    "v_accvgpr_write_b32 a206, v50\n"
    "v_accvgpr_write_b32 a207, v51\n"
    "s_waitcnt lgkmcnt(1)\n"
    "v_mfma_f32_16x16x32_bf16 v[20:23], v[196:199], v[52:55], v[20:23]\n"
    "v_accvgpr_write_b32 a208, v52\n"
    "v_accvgpr_write_b32 a209, v53\n"
    "v_accvgpr_write_b32 a210, v54\n"
    "v_accvgpr_write_b32 a211, v55\n"
    "s_waitcnt lgkmcnt(0)\n"
    "v_mfma_f32_16x16x32_bf16 v[20:23], v[200:203], v[56:59], v[20:23]\n"
    "v_accvgpr_write_b32 a212, v56\n"
    "v_accvgpr_write_b32 a213, v57\n"
    "v_accvgpr_write_b32 a214, v58\n"
    "v_accvgpr_write_b32 a215, v59\n"
    "s_add_u32 m0, s65, 50176\n"
    "s_add_u32 s72, s85, 13312\n"
    "buffer_load_dwordx4 v89, s[20:23], s72 offen offset:0 nt lds\n"
    "v_mov_b32 v77, 155904\n"
    "v_max3_f32 v76, v4, v5, v6\n"
    "v_max3_f32 v76, v76, v7, v20\n"
    "v_max3_f32 v76, v76, v21, v22\n"
    "v_max_f32 v76, v76, v23\n"
    "v_mov_b32 v78, v76\n"
    "s_nop 1\n"
    "v_permlane32_swap_b32 v76, v78\n"
    "v_max_f32 v76, v76, v78\n"
    "v_mov_b32 v78, v76\n"
    "s_nop 1\n"
    "v_permlane16_swap_b32 v76, v78\n"
    "v_max_f32 v76, v76, v78\n"
    "ds_max_f32 v77, v76\n"
    "s_add_u32 m0, s65, 51200\n"
    "s_add_u32 s72, s85, 14336\n"
    "buffer_load_dwordx4 v90, s[20:23], s72 offen offset:0 nt lds\n"
    "buffer_load_dwordx2 v[236:237], v87, s[36:39], 0 offen offset:0 sc1\n"
    "s_waitcnt lgkmcnt(0)\n"
    "s_barrier\n"
    "ds_read_b32 v76, v77\n"
    "s_waitcnt vmcnt(10)\n"
    "buffer_store_dwordx2 v[238:239], v240, s[36:39], s80 offen offset:0 sc1\n"
    "s_lshl_b32 s73, s79, 8\n"
    "s_add_u32 s80, s80, s73\n"
    "s_mul_i32 s73, s79, 98304\n"
    "s_add_u32 s71, s71, s73\n"
    "s_waitcnt lgkmcnt(0)\n"
    "v_add_f32 v76, 0xc2b00000, v76\n"
    "v_sub_f32 v68, v68, v76\n"
    "v_sub_f32 v69, v69, v76\n"
    "v_sub_f32 v70, v70, v76\n"
    "v_sub_f32 v71, v71, v76\n"
    "v_sub_f32 v72, v72, v76\n"
    "v_sub_f32 v73, v73, v76\n"
    "v_sub_f32 v74, v74, v76\n"
    "v_sub_f32 v75, v75, v76\n"
    "ds_write_b128 v86, v[68:71] offset:0\n"
    "ds_write_b128 v86, v[72:75] offset:16\n"
    "s_mul_i32 s73, s79, 768\n"
    "v_add_u32 v77, s73, v250\n"
    "s_add_u32 m0, s65, 52224\n"
    "s_add_u32 s72, s85, 15360\n"
    "buffer_load_dwordx4 v88, s[20:23], s72 offen offset:0 nt lds\n"
    "s_waitcnt lgkmcnt(0)\n"
    "s_barrier\n"
    "ds_read_b128 v[100:103], v77 offset:0\n"
    "ds_read_b128 v[104:107], v77 offset:16\n"
    "s_add_u32 m0, s65, 53248\n"
    "s_add_u32 s72, s85, 16384\n"
    "buffer_load_dwordx4 v89, s[20:23], s72 offen offset:0 nt lds\n"
    "s_waitcnt lgkmcnt(1)\n"
    "v_add_f32 v4, v4, v100\n"
    "v_add_f32 v5, v5, v101\n"
    "v_add_f32 v6, v6, v102\n"
    "v_add_f32 v7, v7, v103\n"
    "s_waitcnt lgkmcnt(0)\n"
    "v_add_f32 v20, v20, v104\n"
    "v_add_f32 v21, v21, v105\n"
    "v_add_f32 v22, v22, v106\n"
    "v_add_f32 v23, v23, v107\n"
    "v_exp_f32 v4, v4\n"
    "v_exp_f32 v5, v5\n"
    "v_exp_f32 v6, v6\n"
    "v_exp_f32 v7, v7\n"
    "v_exp_f32 v20, v20\n"
    "v_exp_f32 v21, v21\n"
    "v_exp_f32 v22, v22\n"
    "v_exp_f32 v23, v23\n"
    "s_add_u32 m0, s65, 54272\n"
    "s_add_u32 s72, s85, 17408\n"
    "buffer_load_dwordx4 v90, s[20:23], s72 offen offset:0 nt lds\n"
    "s_waitcnt vmcnt(8)\n"
    "ds_read_b128 v[56:59], v85 offset:24832\n"
    "ds_read_b128 v[52:55], v84 offset:24832\n"
    "ds_read_b128 v[48:51], v85 offset:24704\n"
    "ds_read_b128 v[44:47], v84 offset:24704\n"
    "ds_read_b128 v[40:43], v85 offset:24576\n"
    "ds_read_b128 v[36:39], v84 offset:24576\n"
    "s_waitcnt lgkmcnt(5)\n"
    "v_pk_maximum3_f16 v80, v56, v57, v80\n"
    "v_pk_maximum3_f16 v80, v58, v59, v80\n"
    "v_pk_add_u16 v56, s89, v56 clamp\n"
    "v_pk_add_u16 v57, s89, v57 clamp\n"
    "v_pk_add_u16 v58, s89, v58 clamp\n"
    "v_pk_add_u16 v59, s89, v59 clamp\n"
    "s_waitcnt lgkmcnt(4)\n"
    "v_pk_maximum3_f16 v80, v52, v53, v80\n"
    "v_pk_maximum3_f16 v80, v54, v55, v80\n"
    "v_pk_add_u16 v52, s89, v52 clamp\n"
    "v_pk_add_u16 v53, s89, v53 clamp\n"
    "v_pk_add_u16 v54, s89, v54 clamp\n"
    "v_pk_add_u16 v55, s89, v55 clamp\n"
    "v_mfma_f32_16x16x32_bf16 v[8:11], v[176:179], v[56:59], v[100:103]\n"
    "s_waitcnt lgkmcnt(3)\n"
    "v_pk_maximum3_f16 v80, v48, v49, v80\n"
    "v_pk_maximum3_f16 v80, v50, v51, v80\n"
    "v_pk_add_u16 v48, s89, v48 clamp\n"
    "v_pk_add_u16 v49, s89, v49 clamp\n"
    "v_pk_add_u16 v50, s89, v50 clamp\n"
    "v_pk_add_u16 v51, s89, v51 clamp\n"
    "v_mfma_f32_16x16x32_bf16 v[8:11], v[172:175], v[52:55], v[8:11]\n"
    "s_waitcnt lgkmcnt(2)\n"
    "v_pk_maximum3_f16 v80, v44, v45, v80\n"
    "v_pk_maximum3_f16 v80, v46, v47, v80\n"
    "v_pk_add_u16 v44, s89, v44 clamp\n"
    "v_pk_add_u16 v45, s89, v45 clamp\n"
    "v_pk_add_u16 v46, s89, v46 clamp\n"
    "v_pk_add_u16 v47, s89, v47 clamp\n"
    "v_mfma_f32_16x16x32_bf16 v[8:11], v[168:171], v[48:51], v[8:11]\n"
    "s_waitcnt lgkmcnt(1)\n"
    "v_pk_maximum3_f16 v80, v40, v41, v80\n"
    "v_pk_maximum3_f16 v80, v42, v43, v80\n"
    "v_pk_add_u16 v40, s89, v40 clamp\n"
    "v_pk_add_u16 v41, s89, v41 clamp\n"
    "v_pk_add_u16 v42, s89, v42 clamp\n"
    "v_pk_add_u16 v43, s89, v43 clamp\n"
    "v_mfma_f32_16x16x32_bf16 v[8:11], v[164:167], v[44:47], v[8:11]\n"
    "s_waitcnt lgkmcnt(0)\n"
    "v_pk_maximum3_f16 v80, v36, v37, v80\n"
    "v_pk_maximum3_f16 v80, v38, v39, v80\n"
    "v_pk_add_u16 v36, s89, v36 clamp\n"
    "v_pk_add_u16 v37, s89, v37 clamp\n"
    "v_pk_add_u16 v38, s89, v38 clamp\n"
    "v_pk_add_u16 v39, s89, v39 clamp\n"
    "v_mfma_f32_16x16x32_bf16 v[8:11], v[160:163], v[40:43], v[8:11]\n"
    "s_nop 0\n"
    "v_mfma_f32_16x16x32_bf16 v[8:11], v[156:159], v[36:39], v[8:11]\n"
    "v_mfma_f32_16x16x32_bf16 v[24:27], v[200:203], v[56:59], v[104:107]\n"
    "v_accvgpr_write_b32 a236, v56\n"
    "v_accvgpr_write_b32 a237, v57\n"
    "v_accvgpr_write_b32 a238, v58\n"
    "v_accvgpr_write_b32 a239, v59\n"
    "v_mfma_f32_16x16x32_bf16 v[24:27], v[196:199], v[52:55], v[24:27]\n"
    "v_accvgpr_write_b32 a232, v52\n"
    "v_accvgpr_write_b32 a233, v53\n"
    "v_accvgpr_write_b32 a234, v54\n"
    "v_accvgpr_write_b32 a235, v55\n"
    "v_mfma_f32_16x16x32_bf16 v[24:27], v[192:195], v[48:51], v[24:27]\n"
    "v_accvgpr_write_b32 a228, v48\n"
    "v_accvgpr_write_b32 a229, v49\n"
    "v_accvgpr_write_b32 a230, v50\n"
    "v_accvgpr_write_b32 a231, v51\n"
    "v_mfma_f32_16x16x32_bf16 v[24:27], v[188:191], v[44:47], v[24:27]\n"
    "v_accvgpr_write_b32 a224, v44\n"
    "v_accvgpr_write_b32 a225, v45\n"
    "v_accvgpr_write_b32 a226, v46\n"
    "v_accvgpr_write_b32 a227, v47\n"
    "v_mfma_f32_16x16x32_bf16 v[24:27], v[184:187], v[40:43], v[24:27]\n"
    "v_accvgpr_write_b32 a220, v40\n"
    "v_accvgpr_write_b32 a221, v41\n"
    "v_accvgpr_write_b32 a222, v42\n"
    "v_accvgpr_write_b32 a223, v43\n"
    "v_mfma_f32_16x16x32_bf16 v[24:27], v[180:183], v[36:39], v[24:27]\n"
    "v_accvgpr_write_b32 a216, v36\n"
    "v_accvgpr_write_b32 a217, v37\n"
    "v_accvgpr_write_b32 a218, v38\n"
    "v_accvgpr_write_b32 a219, v39\n"
    "s_add_u32 m0, s65, 98304\n"
    "s_add_u32 s72, s85, 18432\n"
    "buffer_load_dwordx4 v88, s[20:23], s72 offen offset:0 nt lds\n"
    "v_exp_f32 v8, v8\n"
    "v_exp_f32 v9, v9\n"
    "v_exp_f32 v10, v10\n"
    "v_exp_f32 v11, v11\n"
    "v_exp_f32 v24, v24\n"
    "v_exp_f32 v25, v25\n"
    "v_exp_f32 v26, v26\n"
    "v_exp_f32 v27, v27\n"
    "s_add_u32 m0, s65, 99328\n"
    "s_add_u32 s72, s85, 19456\n"
    "buffer_load_dwordx4 v89, s[20:23], s72 offen offset:0 nt lds\n"
    "s_add_u32 m0, s65, 100352\n"
    "s_add_u32 s72, s85, 20480\n"
    "buffer_load_dwordx4 v90, s[20:23], s72 offen offset:0 nt lds\n"
    "s_waitcnt vmcnt(7)\n"
    "v_cmp_eq_u64 s[54:55], v[236:237], v[238:239]\n"
    "s_nop 0\n"
    "s_cmp_eq_u64 s[54:55], -1\n"
    "s_cbranch_scc0 .LpollAV\n"
    ".LpollAVr:\n"
    "s_add_u32 m0, s65, 101376\n"
    "s_add_u32 s72, s85, 21504\n"
    "buffer_load_dwordx4 v88, s[20:23], s72 offen offset:0 nt lds\n"
    "s_add_u32 m0, s65, 24576\n"
    "s_add_u32 s72, s65, 49152\n"
    "buffer_load_dwordx4 v1, s[16:19], s72 offen offset:0 sc1 lds\n"
    "s_add_u32 m0, s65, 25600\n"
    "s_add_u32 s72, s65, 50176\n"
    "buffer_load_dwordx4 v1, s[16:19], s72 offen offset:0 sc1 lds\n"
    "s_add_u32 m0, s65, 102400\n"
    "s_add_u32 s72, s85, 22528\n"
    "buffer_load_dwordx4 v89, s[20:23], s72 offen offset:0 nt lds\n"
    "s_add_u32 m0, s65, 26624\n"
    "s_add_u32 s72, s65, 51200\n"
    "buffer_load_dwordx4 v1, s[16:19], s72 offen offset:0 sc1 lds\n"
    "s_add_u32 m0, s65, 27648\n"
    "s_add_u32 s72, s65, 52224\n"
    "buffer_load_dwordx4 v1, s[16:19], s72 offen offset:0 sc1 lds\n"
    "s_add_u32 m0, s65, 103424\n"
    "s_add_u32 s72, s85, 23552\n"
    "buffer_load_dwordx4 v90, s[20:23], s72 offen offset:0 nt lds\n"
    "s_add_u32 m0, s65, 28672\n"
    "s_add_u32 s72, s65, 53248\n"
    "buffer_load_dwordx4 v1, s[16:19], s72 offen offset:0 sc1 lds\n"
    "s_add_u32 m0, s65, 29696\n"
    "s_add_u32 s72, s65, 54272\n"
    "buffer_load_dwordx4 v1, s[16:19], s72 offen offset:0 sc1 lds\n"
    "s_add_u32 m0, s65, 73728\n"
    "s_add_u32 s72, s65, 73728\n"
    "buffer_load_dwordx4 v1, s[16:19], s72 offen offset:0 sc1 lds\n"
    "s_add_u32 m0, s65, 74752\n"
    "s_add_u32 s72, s65, 74752\n"
    "buffer_load_dwordx4 v1, s[16:19], s72 offen offset:0 sc1 lds\n"
    "s_add_u32 m0, s65, 75776\n"
    "s_add_u32 s72, s65, 75776\n"
    "buffer_load_dwordx4 v1, s[16:19], s72 offen offset:0 sc1 lds\n"
    "s_add_u32 m0, s65, 76800\n"
    "s_add_u32 s72, s65, 76800\n"
    "buffer_load_dwordx4 v1, s[16:19], s72 offen offset:0 sc1 lds\n"
    "s_add_u32 m0, s65, 77824\n"
    "s_add_u32 s72, s65, 77824\n"
    "buffer_load_dwordx4 v1, s[16:19], s72 offen offset:0 sc1 lds\n"
    "s_add_u32 m0, s65, 78848\n"
    "s_add_u32 s72, s65, 78848\n"
    "buffer_load_dwordx4 v1, s[16:19], s72 offen offset:0 sc1 lds\n"
    "s_bitcmp1_b32 s83, 1\n"
    "s_cbranch_scc1 .Lf4b\n"
    "s_waitcnt vmcnt(18)\n"
    "ds_read_b128 v[36:39], v84 offset:0\n"
    "ds_read_b128 v[40:43], v85 offset:0\n"
    "ds_read_b128 v[44:47], v84 offset:128\n"
    "ds_read_b128 v[48:51], v85 offset:128\n"
    "ds_read_b128 v[124:127], v84 offset:256\n"
    "ds_read_b128 v[128:131], v85 offset:256\n"
    "s_waitcnt lgkmcnt(5)\n"
    "v_pk_maximum3_f16 v80, v36, v37, v80\n"
    "v_pk_maximum3_f16 v80, v38, v39, v80\n"
    "v_pk_add_u16 v36, s89, v36 clamp\n"
    "v_pk_add_u16 v37, s89, v37 clamp\n"
    "v_pk_add_u16 v38, s89, v38 clamp\n"
    "v_pk_add_u16 v39, s89, v39 clamp\n"
    "s_waitcnt lgkmcnt(4)\n"
    "v_pk_maximum3_f16 v80, v40, v41, v80\n"
    "v_pk_maximum3_f16 v80, v42, v43, v80\n"
    "v_pk_add_u16 v40, s89, v40 clamp\n"
    "v_pk_add_u16 v41, s89, v41 clamp\n"
    "v_pk_add_u16 v42, s89, v42 clamp\n"
    "v_pk_add_u16 v43, s89, v43 clamp\n"
    "v_mfma_f32_16x16x32_bf16 v[12:15], v[156:159], v[36:39], v[100:103]\n"
    "s_waitcnt lgkmcnt(3)\n"
    "v_pk_maximum3_f16 v80, v44, v45, v80\n"
    "v_pk_maximum3_f16 v80, v46, v47, v80\n"
    "v_pk_add_u16 v44, s89, v44 clamp\n"
    "v_pk_add_u16 v45, s89, v45 clamp\n"
    "v_pk_add_u16 v46, s89, v46 clamp\n"
    "v_pk_add_u16 v47, s89, v47 clamp\n"
    "v_mfma_f32_16x16x32_bf16 v[12:15], v[160:163], v[40:43], v[12:15]\n"
    "s_waitcnt lgkmcnt(2)\n"
    "v_pk_maximum3_f16 v80, v48, v49, v80\n"
    "v_pk_maximum3_f16 v80, v50, v51, v80\n"
    "v_pk_add_u16 v48, s89, v48 clamp\n"
    "v_pk_add_u16 v49, s89, v49 clamp\n"
    "v_pk_add_u16 v50, s89, v50 clamp\n"
    "v_pk_add_u16 v51, s89, v51 clamp\n"
    "v_mfma_f32_16x16x32_bf16 v[12:15], v[164:167], v[44:47], v[12:15]\n"
    "s_waitcnt lgkmcnt(1)\n"
    "v_pk_maximum3_f16 v80, v124, v125, v80\n"
    "v_pk_maximum3_f16 v80, v126, v127, v80\n"
    "v_pk_add_u16 v124, s89, v124 clamp\n"
    "v_pk_add_u16 v125, s89, v125 clamp\n"
    "v_pk_add_u16 v126, s89, v126 clamp\n"
    "v_pk_add_u16 v127, s89, v127 clamp\n"
    "v_mfma_f32_16x16x32_bf16 v[12:15], v[168:171], v[48:51], v[12:15]\n"
    "s_waitcnt lgkmcnt(0)\n"
    "v_pk_maximum3_f16 v80, v128, v129, v80\n"
    "v_pk_maximum3_f16 v80, v130, v131, v80\n"
    "v_pk_add_u16 v128, s89, v128 clamp\n"
    "v_pk_add_u16 v129, s89, v129 clamp\n"
    "v_pk_add_u16 v130, s89, v130 clamp\n"
    "v_pk_add_u16 v131, s89, v131 clamp\n"
    "v_mfma_f32_16x16x32_bf16 v[12:15], v[172:175], v[124:127], v[12:15]\n"
    "s_nop 0\n"
    "v_mfma_f32_16x16x32_bf16 v[12:15], v[176:179], v[128:131], v[12:15]\n"
    "v_mfma_f32_16x16x32_bf16 v[28:31], v[180:183], v[36:39], v[104:107]\n"
    "v_accvgpr_write_b32 a240, v36\n"
    "v_accvgpr_write_b32 a241, v37\n"
    "v_accvgpr_write_b32 a242, v38\n"
    "v_accvgpr_write_b32 a243, v39\n"
    "v_mfma_f32_16x16x32_bf16 v[28:31], v[184:187], v[40:43], v[28:31]\n"
    "v_accvgpr_write_b32 a244, v40\n"
    "v_accvgpr_write_b32 a245, v41\n"
    "v_accvgpr_write_b32 a246, v42\n"
    "v_accvgpr_write_b32 a247, v43\n"
    "v_mfma_f32_16x16x32_bf16 v[28:31], v[188:191], v[44:47], v[28:31]\n"
    "v_accvgpr_write_b32 a248, v44\n"
    "v_accvgpr_write_b32 a249, v45\n"
    "v_accvgpr_write_b32 a250, v46\n"
    "v_accvgpr_write_b32 a251, v47\n"
    "v_mfma_f32_16x16x32_bf16 v[28:31], v[192:195], v[48:51], v[28:31]\n"
    "v_accvgpr_write_b32 a252, v48\n"
    "v_accvgpr_write_b32 a253, v49\n"
    "v_accvgpr_write_b32 a254, v50\n"
    "v_accvgpr_write_b32 a255, v51\n"
    "v_mfma_f32_16x16x32_bf16 v[28:31], v[196:199], v[124:127], v[28:31]\n"
    "v_mfma_f32_16x16x32_bf16 v[28:31], v[200:203], v[128:131], v[28:31]\n"
    "v_exp_f32 v12, v12\n"
    "v_exp_f32 v13, v13\n"
    "v_exp_f32 v14, v14\n"
    "v_exp_f32 v15, v15\n"
    "s_nop 3\n"
    "v_exp_f32 v28, v28\n"
    "v_exp_f32 v29, v29\n"
    "v_exp_f32 v30, v30\n"
    "v_exp_f32 v31, v31\n"
    "s_waitcnt vmcnt(8)\n"
    "ds_read_b128 v[152:155], v85 offset:49408\n"
    "ds_read_b128 v[148:151], v84 offset:49408\n"
    "ds_read_b128 v[144:147], v85 offset:49280\n"
    "ds_read_b128 v[140:143], v84 offset:49280\n"
    "ds_read_b128 v[136:139], v85 offset:49152\n"
    "ds_read_b128 v[132:135], v84 offset:49152\n"
    "s_waitcnt lgkmcnt(5)\n"
    "v_pk_maximum3_f16 v80, v152, v153, v80\n"
    "v_pk_maximum3_f16 v80, v154, v155, v80\n"
    "v_pk_add_u16 v152, s89, v152 clamp\n"
    "v_pk_add_u16 v153, s89, v153 clamp\n"
    "v_pk_add_u16 v154, s89, v154 clamp\n"
    "v_pk_add_u16 v155, s89, v155 clamp\n"
    "s_waitcnt lgkmcnt(4)\n"
    "v_pk_maximum3_f16 v80, v148, v149, v80\n"
    "v_pk_maximum3_f16 v80, v150, v151, v80\n"
    "v_pk_add_u16 v148, s89, v148 clamp\n"
    "v_pk_add_u16 v149, s89, v149 clamp\n"
    "v_pk_add_u16 v150, s89, v150 clamp\n"
    "v_pk_add_u16 v151, s89, v151 clamp\n"
    "v_mfma_f32_16x16x32_bf16 v[16:19], v[176:179], v[152:155], v[100:103]\n"
    "s_waitcnt lgkmcnt(3)\n"
    "v_pk_maximum3_f16 v80, v144, v145, v80\n"
    "v_pk_maximum3_f16 v80, v146, v147, v80\n"
    "v_pk_add_u16 v144, s89, v144 clamp\n"
    "v_pk_add_u16 v145, s89, v145 clamp\n"
    "v_pk_add_u16 v146, s89, v146 clamp\n"
    "v_pk_add_u16 v147, s89, v147 clamp\n"
    "v_mfma_f32_16x16x32_bf16 v[16:19], v[172:175], v[148:151], v[16:19]\n"
    "s_waitcnt lgkmcnt(2)\n"
    "v_pk_maximum3_f16 v80, v140, v141, v80\n"
    "v_pk_maximum3_f16 v80, v142, v143, v80\n"
    "v_pk_add_u16 v140, s89, v140 clamp\n"
    "v_pk_add_u16 v141, s89, v141 clamp\n"
    "v_pk_add_u16 v142, s89, v142 clamp\n"
    "v_pk_add_u16 v143, s89, v143 clamp\n"
    "v_mfma_f32_16x16x32_bf16 v[16:19], v[168:171], v[144:147], v[16:19]\n"
    "s_waitcnt lgkmcnt(1)\n"
    "v_pk_maximum3_f16 v80, v136, v137, v80\n"
    "v_pk_maximum3_f16 v80, v138, v139, v80\n"
    "v_pk_add_u16 v136, s89, v136 clamp\n"
    "v_pk_add_u16 v137, s89, v137 clamp\n"
    "v_pk_add_u16 v138, s89, v138 clamp\n"
    "v_pk_add_u16 v139, s89, v139 clamp\n"
    "v_mfma_f32_16x16x32_bf16 v[16:19], v[164:167], v[140:143], v[16:19]\n"
    "s_waitcnt lgkmcnt(0)\n"
    "v_pk_maximum3_f16 v80, v132, v133, v80\n"
    "v_pk_maximum3_f16 v80, v134, v135, v80\n"
    "v_pk_add_u16 v132, s89, v132 clamp\n"
    "v_pk_add_u16 v133, s89, v133 clamp\n"
    "v_pk_add_u16 v134, s89, v134 clamp\n"
    "v_pk_add_u16 v135, s89, v135 clamp\n"
    "v_mfma_f32_16x16x32_bf16 v[16:19], v[160:163], v[136:139], v[16:19]\n"
    "s_nop 0\n"
    "v_mfma_f32_16x16x32_bf16 v[16:19], v[156:159], v[132:135], v[16:19]\n"
    "v_mfma_f32_16x16x32_bf16 v[32:35], v[200:203], v[152:155], v[104:107]\n"
    "v_mfma_f32_16x16x32_bf16 v[32:35], v[196:199], v[148:151], v[32:35]\n"
    "v_mfma_f32_16x16x32_bf16 v[32:35], v[192:195], v[144:147], v[32:35]\n"
    "v_mfma_f32_16x16x32_bf16 v[32:35], v[188:191], v[140:143], v[32:35]\n"
    "v_mfma_f32_16x16x32_bf16 v[32:35], v[184:187], v[136:139], v[32:35]\n"
    "v_mfma_f32_16x16x32_bf16 v[32:35], v[180:183], v[132:135], v[32:35]\n"
    "ds_read_b128 v[156:159], v2 offset:12288\n"
    "ds_read_b128 v[160:163], v2 offset:14336\n"
    "ds_read_b128 v[164:167], v2 offset:16384\n"
    "ds_read_b128 v[168:171], v2 offset:18432\n"
    "ds_read_b128 v[172:175], v2 offset:20480\n"
    "ds_read_b128 v[176:179], v2 offset:22528\n"
    "ds_read_b128 v[100:103], v77 offset:128\n"
    "v_pk_add_u16 v80, s89, v80\n"
    "v_and_b32 v80, 0x80008000, v80\n"
    "v_cmp_ne_u32 s[34:35], 0, v80\n"
    "v_exp_f32 v16, v16\n"
    "v_exp_f32 v17, v17\n"
    "v_exp_f32 v18, v18\n"
    "v_exp_f32 v19, v19\n"
    "v_exp_f32 v32, v32\n"
    "v_exp_f32 v33, v33\n"
    "v_exp_f32 v34, v34\n"
    "v_exp_f32 v35, v35\n"
    "s_mov_b32 s66, 0\n"
    "s_waitcnt lgkmcnt(0)\n"
    "s_waitcnt vmcnt(0)\n"
    "ds_add_u32 v247, v246\n"
    "s_nop 7\n"
    "s_branch .LPV\n"
    "s_branch .Lf4e\n"
    ".Lf4b:\n"
    "s_add_u32 s75, s71, s63\n"
    "buffer_load_dwordx4 v[204:207], v242, s[40:43], s75 offen offset:0 nt\n"
    "buffer_load_dwordx4 v[208:211], v242, s[40:43], s75 offen offset:768 nt\n"
    "buffer_load_dwordx4 v[212:215], v242, s[40:43], s75 offen offset:256 nt\n"
    "buffer_load_dwordx4 v[216:219], v242, s[40:43], s75 offen offset:1024 nt\n"
    "buffer_load_dwordx4 v[220:223], v242, s[40:43], s75 offen offset:512 nt\n"
    "buffer_load_dwordx4 v[224:227], v242, s[40:43], s75 offen offset:1280 nt\n"
    "s_waitcnt vmcnt(24)\n"
    "ds_read_b128 v[36:39], v84 offset:0\n"
    "ds_read_b128 v[40:43], v85 offset:0\n"
    "ds_read_b128 v[44:47], v84 offset:128\n"
    "ds_read_b128 v[48:51], v85 offset:128\n"
    "ds_read_b128 v[124:127], v84 offset:256\n"
    "ds_read_b128 v[128:131], v85 offset:256\n"
    "s_waitcnt lgkmcnt(5)\n"
    "v_pk_maximum3_f16 v80, v36, v37, v80\n"
    "v_pk_maximum3_f16 v80, v38, v39, v80\n"
    "v_pk_add_u16 v36, s89, v36 clamp\n"
    "v_pk_add_u16 v37, s89, v37 clamp\n"
    "v_pk_add_u16 v38, s89, v38 clamp\n"
    "v_pk_add_u16 v39, s89, v39 clamp\n"
    "s_waitcnt lgkmcnt(4)\n"
    "v_pk_maximum3_f16 v80, v40, v41, v80\n"
    "v_pk_maximum3_f16 v80, v42, v43, v80\n"
    "v_pk_add_u16 v40, s89, v40 clamp\n"
    "v_pk_add_u16 v41, s89, v41 clamp\n"
    "v_pk_add_u16 v42, s89, v42 clamp\n"
    "v_pk_add_u16 v43, s89, v43 clamp\n"
    "v_mfma_f32_16x16x32_bf16 v[12:15], v[156:159], v[36:39], v[100:103]\n"
    "s_waitcnt lgkmcnt(3)\n"
    "v_pk_maximum3_f16 v80, v44, v45, v80\n"
    "v_pk_maximum3_f16 v80, v46, v47, v80\n"
    "v_pk_add_u16 v44, s89, v44 clamp\n"
    "v_pk_add_u16 v45, s89, v45 clamp\n"
    "v_pk_add_u16 v46, s89, v46 clamp\n"
    "v_pk_add_u16 v47, s89, v47 clamp\n"
    "v_mfma_f32_16x16x32_bf16 v[12:15], v[160:163], v[40:43], v[12:15]\n"
    "s_waitcnt lgkmcnt(2)\n"
    "v_pk_maximum3_f16 v80, v48, v49, v80\n"
    "v_pk_maximum3_f16 v80, v50, v51, v80\n"
    "v_pk_add_u16 v48, s89, v48 clamp\n"
    "v_pk_add_u16 v49, s89, v49 clamp\n"
    "v_pk_add_u16 v50, s89, v50 clamp\n"
    "v_pk_add_u16 v51, s89, v51 clamp\n"
    "v_mfma_f32_16x16x32_bf16 v[12:15], v[164:167], v[44:47], v[12:15]\n"
    "s_waitcnt lgkmcnt(1)\n"
    "v_pk_maximum3_f16 v80, v124, v125, v80\n"
    "v_pk_maximum3_f16 v80, v126, v127, v80\n"
    "v_pk_add_u16 v124, s89, v124 clamp\n"
    "v_pk_add_u16 v125, s89, v125 clamp\n"
    "v_pk_add_u16 v126, s89, v126 clamp\n"
    "v_pk_add_u16 v127, s89, v127 clamp\n"
    "v_mfma_f32_16x16x32_bf16 v[12:15], v[168:171], v[48:51], v[12:15]\n"
    "s_waitcnt lgkmcnt(0)\n"
    "v_pk_maximum3_f16 v80, v128, v129, v80\n"
    "v_pk_maximum3_f16 v80, v130, v131, v80\n"
    "v_pk_add_u16 v128, s89, v128 clamp\n"
    "v_pk_add_u16 v129, s89, v129 clamp\n"
    "v_pk_add_u16 v130, s89, v130 clamp\n"
    "v_pk_add_u16 v131, s89, v131 clamp\n"
    "v_mfma_f32_16x16x32_bf16 v[12:15], v[172:175], v[124:127], v[12:15]\n"
    "s_nop 0\n"
    "v_mfma_f32_16x16x32_bf16 v[12:15], v[176:179], v[128:131], v[12:15]\n"
    "v_mfma_f32_16x16x32_bf16 v[28:31], v[180:183], v[36:39], v[104:107]\n"
    "v_accvgpr_write_b32 a240, v36\n"
    "v_accvgpr_write_b32 a241, v37\n"
    "v_accvgpr_write_b32 a242, v38\n"
    "v_accvgpr_write_b32 a243, v39\n"
    "v_mfma_f32_16x16x32_bf16 v[28:31], v[184:187], v[40:43], v[28:31]\n"
    "v_accvgpr_write_b32 a244, v40\n"
    "v_accvgpr_write_b32 a245, v41\n"
    "v_accvgpr_write_b32 a246, v42\n"
    "v_accvgpr_write_b32 a247, v43\n"
    "v_mfma_f32_16x16x32_bf16 v[28:31], v[188:191], v[44:47], v[28:31]\n"
    "v_accvgpr_write_b32 a248, v44\n"
    "v_accvgpr_write_b32 a249, v45\n"
    "v_accvgpr_write_b32 a250, v46\n"
    "v_accvgpr_write_b32 a251, v47\n"
    "v_mfma_f32_16x16x32_bf16 v[28:31], v[192:195], v[48:51], v[28:31]\n"
    "v_accvgpr_write_b32 a252, v48\n"
    "v_accvgpr_write_b32 a253, v49\n"
    "v_accvgpr_write_b32 a254, v50\n"
    "v_accvgpr_write_b32 a255, v51\n"
    "v_mfma_f32_16x16x32_bf16 v[28:31], v[196:199], v[124:127], v[28:31]\n"
    "v_mfma_f32_16x16x32_bf16 v[28:31], v[200:203], v[128:131], v[28:31]\n"
    "v_exp_f32 v12, v12\n"
    "v_exp_f32 v13, v13\n"
    "v_exp_f32 v14, v14\n"
    "v_exp_f32 v15, v15\n"
    "s_nop 3\n"
    "v_exp_f32 v28, v28\n"
    "v_exp_f32 v29, v29\n"
    "v_exp_f32 v30, v30\n"
    "v_exp_f32 v31, v31\n"
    "s_waitcnt vmcnt(14)\n"
    "ds_read_b128 v[152:155], v85 offset:49408\n"
    "ds_read_b128 v[148:151], v84 offset:49408\n"
    "ds_read_b128 v[144:147], v85 offset:49280\n"
    "ds_read_b128 v[140:143], v84 offset:49280\n"
    "ds_read_b128 v[136:139], v85 offset:49152\n"
    "ds_read_b128 v[132:135], v84 offset:49152\n"
    "s_waitcnt lgkmcnt(5)\n"
    "v_pk_maximum3_f16 v80, v152, v153, v80\n"
    "v_pk_maximum3_f16 v80, v154, v155, v80\n"
    "v_pk_add_u16 v152, s89, v152 clamp\n"
    "v_pk_add_u16 v153, s89, v153 clamp\n"
    "v_pk_add_u16 v154, s89, v154 clamp\n"
    "v_pk_add_u16 v155, s89, v155 clamp\n"
    "s_waitcnt lgkmcnt(4)\n"
    "v_pk_maximum3_f16 v80, v148, v149, v80\n"
    "v_pk_maximum3_f16 v80, v150, v151, v80\n"
    "v_pk_add_u16 v148, s89, v148 clamp\n"
    "v_pk_add_u16 v149, s89, v149 clamp\n"
    "v_pk_add_u16 v150, s89, v150 clamp\n"
    "v_pk_add_u16 v151, s89, v151 clamp\n"
    "v_mfma_f32_16x16x32_bf16 v[16:19], v[176:179], v[152:155], v[100:103]\n"
    "s_waitcnt lgkmcnt(3)\n"
    "v_pk_maximum3_f16 v80, v144, v145, v80\n"
    "v_pk_maximum3_f16 v80, v146, v147, v80\n"
    "v_pk_add_u16 v144, s89, v144 clamp\n"
    "v_pk_add_u16 v145, s89, v145 clamp\n"
    "v_pk_add_u16 v146, s89, v146 clamp\n"
    "v_pk_add_u16 v147, s89, v147 clamp\n"
    "v_mfma_f32_16x16x32_bf16 v[16:19], v[172:175], v[148:151], v[16:19]\n"
    "s_waitcnt lgkmcnt(2)\n"
    "v_pk_maximum3_f16 v80, v140, v141, v80\n"
    "v_pk_maximum3_f16 v80, v142, v143, v80\n"
    "v_pk_add_u16 v140, s89, v140 clamp\n"
    "v_pk_add_u16 v141, s89, v141 clamp\n"
    "v_pk_add_u16 v142, s89, v142 clamp\n"
    "v_pk_add_u16 v143, s89, v143 clamp\n"
    "v_mfma_f32_16x16x32_bf16 v[16:19], v[168:171], v[144:147], v[16:19]\n"
    "s_waitcnt lgkmcnt(1)\n"
    "v_pk_maximum3_f16 v80, v136, v137, v80\n"
    "v_pk_maximum3_f16 v80, v138, v139, v80\n"
    "v_pk_add_u16 v136, s89, v136 clamp\n"
    "v_pk_add_u16 v137, s89, v137 clamp\n"
    "v_pk_add_u16 v138, s89, v138 clamp\n"
    "v_pk_add_u16 v139, s89, v139 clamp\n"
    "v_mfma_f32_16x16x32_bf16 v[16:19], v[164:167], v[140:143], v[16:19]\n"
    "s_waitcnt lgkmcnt(0)\n"
    "v_pk_maximum3_f16 v80, v132, v133, v80\n"
    "v_pk_maximum3_f16 v80, v134, v135, v80\n"
    "v_pk_add_u16 v132, s89, v132 clamp\n"
    "v_pk_add_u16 v133, s89, v133 clamp\n"
    "v_pk_add_u16 v134, s89, v134 clamp\n"
    "v_pk_add_u16 v135, s89, v135 clamp\n"
    "v_mfma_f32_16x16x32_bf16 v[16:19], v[160:163], v[136:139], v[16:19]\n"
    "s_nop 0\n"
    "v_mfma_f32_16x16x32_bf16 v[16:19], v[156:159], v[132:135], v[16:19]\n"
    "v_mfma_f32_16x16x32_bf16 v[32:35], v[200:203], v[152:155], v[104:107]\n"
    "v_mfma_f32_16x16x32_bf16 v[32:35], v[196:199], v[148:151], v[32:35]\n"
    "v_mfma_f32_16x16x32_bf16 v[32:35], v[192:195], v[144:147], v[32:35]\n"
    "v_mfma_f32_16x16x32_bf16 v[32:35], v[188:191], v[140:143], v[32:35]\n"
    "v_mfma_f32_16x16x32_bf16 v[32:35], v[184:187], v[136:139], v[32:35]\n"
    "v_mfma_f32_16x16x32_bf16 v[32:35], v[180:183], v[132:135], v[32:35]\n"
    "ds_read_b128 v[156:159], v2 offset:12288\n"
    "ds_read_b128 v[160:163], v2 offset:14336\n"
    "ds_read_b128 v[164:167], v2 offset:16384\n"
    "ds_read_b128 v[168:171], v2 offset:18432\n"
    "ds_read_b128 v[172:175], v2 offset:20480\n"
    "ds_read_b128 v[176:179], v2 offset:22528\n"
    "ds_read_b128 v[100:103], v77 offset:128\n"
    "v_pk_add_u16 v80, s89, v80\n"
    "v_and_b32 v80, 0x80008000, v80\n"
    "v_cmp_ne_u32 s[34:35], 0, v80\n"
    "v_exp_f32 v16, v16\n"
    "v_exp_f32 v17, v17\n"
    "v_exp_f32 v18, v18\n"
    "v_exp_f32 v19, v19\n"
    "v_exp_f32 v32, v32\n"
    "v_exp_f32 v33, v33\n"
    "v_exp_f32 v34, v34\n"
    "v_exp_f32 v35, v35\n"
    "s_mov_b32 s66, 0\n"
    "s_waitcnt lgkmcnt(0)\n"
    "s_waitcnt vmcnt(6)\n"
    "ds_add_u32 v247, v246\n"
    "s_nop 7\n"
    "s_branch .LNV\n"
    ".Lf4e:\n"
    "s_nop 4\n"
    ".Lf5e:\n"
    "s_nop 4\n"
    ".p2align 6\n"
    ".fill ((0x20000 - (.LwKe - .LwK) - (. - _Z8attn_fwdPKtPKiS2_S0_S0_S0_S0_PcPt)) & 4095) / 4, 4, 0xBF800000\n"
    ".LwK:\n"
    "s_branch .LwKe\n"
    ".LPK:\n"
    "v_mfma_f32_16x16x32_bf16 v[36:39], v[156:159], a[192:195], v[100:103]\n"
    "s_add_u32 s71, s71, 98304\n"
    "ds_read_b128 v[180:183], v2 offset:13312\n"
    "v_cvt_pk_bf16_f32 v68, v4, v5\n"
    "v_mfma_f32_16x16x32_bf16 v[36:39], v[160:163], a[196:199], v[36:39]\n"
    "s_add_u32 s75, s71, s63\n"
    "ds_read_b128 v[184:187], v2 offset:15360\n"
    "v_mfma_f32_16x16x32_bf16 v[36:39], v[164:167], a[200:203], v[36:39]\n"
    "s_add_u32 s76, s75, 6144\n"
    "ds_read_b128 v[188:191], v2 offset:17408\n"
    "v_mfma_f32_16x16x32_bf16 v[36:39], v[168:171], a[204:207], v[36:39]\n"
    "s_add_u32 s77, s75, 24576\n"
    "ds_read_b128 v[192:195], v2 offset:19456\n"
    "v_cvt_pk_bf16_f32 v69, v6, v7\n"
    "v_mfma_f32_16x16x32_bf16 v[36:39], v[172:175], a[208:211], v[36:39]\n"
    "s_add_u32 s78, s75, 30720\n"
    "ds_read_b128 v[196:199], v2 offset:21504\n"
    "v_mfma_f32_16x16x32_bf16 v[36:39], v[176:179], a[212:215], v[36:39]\n"
    "ds_read_b128 v[200:203], v2 offset:23552\n"
    "v_mfma_f32_16x16x32_bf16 v[40:43], v[176:179], a[236:239], v[100:103]\n"
    "ds_read_b128 v[104:107], v250 offset:144\n"
    "v_mfma_f32_16x16x32_bf16 v[40:43], v[172:175], a[232:235], v[40:43]\n"
    "s_add_u32 s68, s68, 24576\n"
    "v_cvt_pk_bf16_f32 v76, v8, v9\n"
    "v_mfma_f32_16x16x32_bf16 v[40:43], v[168:171], a[228:231], v[40:43]\n"
    "s_cmp_ge_u32 s68, s69\n"
    "s_cselect_b32 s68, s65, s68\n"
    "v_mfma_f32_16x16x32_bf16 v[40:43], v[164:167], a[224:227], v[40:43]\n"
    "v_exp_f32 v36, v36\n"
    "v_mfma_f32_16x16x32_bf16 v[40:43], v[160:163], a[220:223], v[40:43]\n"
    "v_exp_f32 v37, v37\n"
    "v_mfma_f32_16x16x32_bf16 v[40:43], v[156:159], a[216:219], v[40:43]\n"
    "v_cvt_pk_bf16_f32 v77, v10, v11\n"
    "v_cvt_pk_bf16_f32 v72, v36, v37\n"
    "v_mfma_f32_16x16x32_bf16 v[44:47], v[156:159], a[240:243], v[100:103]\n"
    "v_mfma_f32_16x16x32_bf16 v[44:47], v[160:163], a[244:247], v[44:47]\n"
    "v_exp_f32 v38, v38\n"
    "v_mfma_f32_16x16x32_bf16 v[44:47], v[164:167], a[248:251], v[44:47]\n"
    "v_mfma_f32_16x16x32_bf16 v[44:47], v[168:171], a[252:255], v[44:47]\n"
    "v_cvt_pk_bf16_f32 v84, v12, v13\n"
    "v_mfma_f32_16x16x32_bf16 v[44:47], v[172:175], v[124:127], v[44:47]\n"
    "v_exp_f32 v39, v39\n"
    "v_mfma_f32_16x16x32_bf16 v[44:47], v[176:179], v[128:131], v[44:47]\n"
    "v_cvt_pk_bf16_f32 v73, v38, v39\n"
    "v_mfma_f32_16x16x32_bf16 v[48:51], v[176:179], v[152:155], v[100:103]\n"
    "ds_read_b32 v245, v247\n"
    "v_cvt_pk_bf16_f32 v85, v14, v15\n"
    "v_mfma_f32_16x16x32_bf16 v[48:51], v[172:175], v[148:151], v[48:51]\n"
    "s_add_u32 m0, s68, 0\n"
    "v_exp_f32 v40, v40\n"
    "v_mfma_f32_16x16x32_bf16 v[48:51], v[168:171], v[144:147], v[48:51]\n"
    "v_mfma_f32_16x16x32_bf16 v[48:51], v[164:167], v[140:143], v[48:51]\n"
    "v_mfma_f32_16x16x32_bf16 v[48:51], v[160:163], v[136:139], v[48:51]\n"
    "s_waitcnt vmcnt(10)\n"
    "v_cmp_eq_u64 s[54:55], v[236:237], v[238:239]\n"
    "v_cvt_pk_bf16_f32 v92, v16, v17\n"
    "v_mfma_f32_16x16x32_bf16 v[48:51], v[156:159], v[132:135], v[48:51]\n"
    "s_waitcnt lgkmcnt(1)\n"
    "v_exp_f32 v41, v41\n"
    "v_mfma_f32_16x16x32_bf16 v[52:55], v[180:183], a[192:195], v[104:107]\n"
    "s_waitcnt lgkmcnt(0)\n"
    "v_cmp_le_u32 s[56:57], s59, v245\n"
    "v_cvt_pk_bf16_f32 v80, v40, v41\n"
    "v_mfma_f32_16x16x32_bf16 v[52:55], v[184:187], a[196:199], v[52:55]\n"
    "v_exp_f32 v42, v42\n"
    "v_mfma_f32_16x16x32_bf16 v[52:55], v[188:191], a[200:203], v[52:55]\n"
    "s_and_b64 s[54:55], s[54:55], s[56:57]\n"
    "s_cmp_eq_u64 s[54:55], -1\n"
    "v_mfma_f32_16x16x32_bf16 v[52:55], v[192:195], a[204:207], v[52:55]\n"
    "s_cbranch_scc0 .Lpoll1\n"
    ".Lpoll1r:\n"
    "v_mfma_f32_16x16x32_bf16 v[52:55], v[196:199], a[208:211], v[52:55]\n"
    "buffer_load_dwordx4 v1, s[16:19], s67 offen offset:0 sc1 lds\n"
    "v_mfma_f32_16x16x32_bf16 v[52:55], v[200:203], a[212:215], v[52:55]\n"
    "ds_read_b128 v[156:159], v3 offset:0\n"
    "ds_read_b128 v[160:163], v3 offset:2048\n"
    "v_mfma_f32_16x16x32_bf16 v[56:59], v[200:203], a[236:239], v[104:107]\n"
    "ds_read_b128 v[164:167], v3 offset:4096\n"
    "ds_read_b128 v[168:171], v3 offset:6144\n"
    "v_mfma_f32_16x16x32_bf16 v[56:59], v[196:199], a[232:235], v[56:59]\n"
    "ds_read_b128 v[172:175], v3 offset:8192\n"
    "ds_read_b128 v[176:179], v3 offset:10240\n"
    "v_mfma_f32_16x16x32_bf16 v[56:59], v[192:195], a[228:231], v[56:59]\n"
    "buffer_load_dwordx4 v1, s[16:19], s67 offen offset:1024 sc1 lds\n"
    "v_mfma_f32_16x16x32_bf16 v[56:59], v[188:191], a[224:227], v[56:59]\n"
    "ds_read_b128 v[100:103], v250 offset:768\n"
    "v_cvt_pk_bf16_f32 v93, v18, v19\n"
    "v_mfma_f32_16x16x32_bf16 v[56:59], v[184:187], a[220:223], v[56:59]\n"
    "v_cvt_pk_bf16_f32 v70, v20, v21\n"
    "v_cvt_pk_bf16_f32 v71, v22, v23\n"
    "v_mfma_f32_16x16x32_bf16 v[56:59], v[180:183], a[216:219], v[56:59]\n"
    "v_exp_f32 v43, v43\n"
    "v_mfma_f32_16x16x32_bf16 v[60:63], v[180:183], a[240:243], v[104:107]\n"
    "buffer_load_dwordx4 v1, s[16:19], s67 offen offset:2048 sc1 lds\n"
    "v_mfma_f32_16x16x32_bf16 v[60:63], v[184:187], a[244:247], v[60:63]\n"
    "v_cvt_pk_bf16_f32 v78, v24, v25\n"
    "v_cvt_pk_bf16_f32 v81, v42, v43\n"
    "v_mfma_f32_16x16x32_bf16 v[60:63], v[188:191], a[248:251], v[60:63]\n"
    "v_exp_f32 v44, v44\n"
    "v_mfma_f32_16x16x32_bf16 v[60:63], v[192:195], a[252:255], v[60:63]\n"
    "v_exp_f32 v45, v45\n"
    "v_mfma_f32_16x16x32_bf16 v[60:63], v[196:199], v[124:127], v[60:63]\n"
    "buffer_load_dwordx4 v1, s[16:19], s67 offen offset:3072 sc1 lds\n"
    "v_mfma_f32_16x16x32_bf16 v[60:63], v[200:203], v[128:131], v[60:63]\n"
    "s_add_u32 m0, s68, 4096\n"
    "v_cvt_pk_bf16_f32 v79, v26, v27\n"
    "v_cvt_pk_bf16_f32 v88, v44, v45\n"
    "v_mfma_f32_16x16x32_bf16 v[64:67], v[200:203], v[152:155], v[104:107]\n"
    "s_add_u32 s83, s67, 4096\n"
    "v_exp_f32 v46, v46\n"
    "v_mfma_f32_16x16x32_bf16 v[64:67], v[196:199], v[148:151], v[64:67]\n"
    "v_exp_f32 v47, v47\n"
    "v_mfma_f32_16x16x32_bf16 v[64:67], v[192:195], v[144:147], v[64:67]\n"
    "buffer_load_dwordx4 v1, s[16:19], s83 offen offset:0 sc1 lds\n"
    "v_mfma_f32_16x16x32_bf16 v[64:67], v[188:191], v[140:143], v[64:67]\n"
    "v_cvt_pk_bf16_f32 v86, v28, v29\n"
    "v_cvt_pk_bf16_f32 v89, v46, v47\n"
    "v_mfma_f32_16x16x32_bf16 v[64:67], v[184:187], v[136:139], v[64:67]\n"
    "v_exp_f32 v48, v48\n"
    "v_mfma_f32_16x16x32_bf16 v[64:67], v[180:183], v[132:135], v[64:67]\n"
    "s_waitcnt lgkmcnt(0)\n"
    "v_exp_f32 v49, v49\n"
    "v_mfma_f32_16x16x32_bf16 v[4:7], v[156:159], a[192:195], v[100:103]\n"
    "buffer_load_dwordx4 v1, s[16:19], s83 offen offset:1024 sc1 lds\n"
    "v_mfma_f32_16x16x32_bf16 v[4:7], v[160:163], a[196:199], v[4:7]\n"
    "s_add_u32 m0, s64, 0\n"
    "ds_read_b128 v[180:183], v3 offset:1024\n"
    "ds_read_b128 v[184:187], v3 offset:3072\n"
    "v_mfma_f32_16x16x32_bf16 v[4:7], v[164:167], a[200:203], v[4:7]\n"
    "s_add_u32 s83, s67, 24576\n"
    "ds_read_b128 v[188:191], v3 offset:5120\n"
    "v_cvt_pk_bf16_f32 v87, v30, v31\n"
    "v_mfma_f32_16x16x32_bf16 v[4:7], v[168:171], a[204:207], v[4:7]\n"
    "ds_read_b128 v[192:195], v3 offset:7168\n"
    "v_cvt_pk_bf16_f32 v96, v48, v49\n"
    "v_mfma_f32_16x16x32_bf16 v[4:7], v[172:175], a[208:211], v[4:7]\n"
    "buffer_load_dwordx4 v1, s[16:19], s83 offen offset:0 sc1 lds\n"
    "v_mfma_f32_16x16x32_bf16 v[4:7], v[176:179], a[212:215], v[4:7]\n"
    "ds_read_b128 v[196:199], v3 offset:9216\n"
    "ds_read_b128 v[200:203], v3 offset:11264\n"
    "v_mfma_f32_16x16x32_bf16 v[8:11], v[176:179], a[236:239], v[100:103]\n"
    "ds_read_b128 v[104:107], v250 offset:784\n"
    "v_cvt_pk_bf16_f32 v94, v32, v33\n"
    "v_mfma_f32_16x16x32_bf16 v[8:11], v[172:175], a[232:235], v[8:11]\n"
    "v_exp_f32 v52, v52\n"
    "v_mfma_f32_16x16x32_bf16 v[8:11], v[168:171], a[228:231], v[8:11]\n"
    "buffer_load_dwordx4 v1, s[16:19], s83 offen offset:1024 sc1 lds\n"
    "v_mfma_f32_16x16x32_bf16 v[8:11], v[164:167], a[224:227], v[8:11]\n"
    "v_cvt_pk_bf16_f32 v95, v34, v35\n"
    "v_mfma_f32_16x16x32_bf16 v[8:11], v[160:163], a[220:223], v[8:11]\n"
    "v_exp_f32 v53, v53\n"
    "v_mfma_f32_16x16x32_bf16 v[8:11], v[156:159], a[216:219], v[8:11]\n"
    "v_exp_f32 v54, v54\n"
    "v_mfma_f32_16x16x32_bf16 v[12:15], v[156:159], a[240:243], v[100:103]\n"
    "buffer_load_dwordx4 v1, s[16:19], s83 offen offset:2048 sc1 lds\n"
    "v_mfma_f32_16x16x32_bf16 v[12:15], v[160:163], a[244:247], v[12:15]\n"
    "v_cvt_pk_bf16_f32 v74, v52, v53\n"
    "v_mfma_f32_16x16x32_bf16 v[12:15], v[164:167], a[248:251], v[12:15]\n"
    "v_exp_f32 v55, v55\n"
    "v_mfma_f32_16x16x32_bf16 v[12:15], v[168:171], a[252:255], v[12:15]\n"
    "v_cvt_pk_bf16_f32 v75, v54, v55\n"
    "v_mfma_f32_16x16x32_bf16 v[12:15], v[172:175], v[124:127], v[12:15]\n"
    "buffer_load_dwordx4 v1, s[16:19], s83 offen offset:3072 sc1 lds\n"
    "v_mfma_f32_16x16x32_bf16 v[12:15], v[176:179], v[128:131], v[12:15]\n"
    "s_add_u32 m0, s64, 4096\n"
    "v_exp_f32 v56, v56\n"
    "v_mfma_f32_16x16x32_bf16 v[16:19], v[176:179], v[152:155], v[100:103]\n"
    "s_add_u32 s83, s67, 28672\n"
    "v_exp_f32 v50, v50\n"
    "v_mfma_f32_16x16x32_bf16 v[16:19], v[172:175], v[148:151], v[16:19]\n"
    "v_exp_f32 v51, v51\n"
    "v_mfma_f32_16x16x32_bf16 v[16:19], v[168:171], v[144:147], v[16:19]\n"
    "buffer_load_dwordx4 v1, s[16:19], s83 offen offset:0 sc1 lds\n"
    "v_mfma_f32_16x16x32_bf16 v[16:19], v[164:167], v[140:143], v[16:19]\n"
    "v_exp_f32 v57, v57\n"
    "v_mfma_f32_16x16x32_bf16 v[16:19], v[160:163], v[136:139], v[16:19]\n"
    "v_cvt_pk_bf16_f32 v82, v56, v57\n"
    "v_cvt_pk_bf16_f32 v97, v50, v51\n"
    "v_mfma_f32_16x16x32_bf16 v[16:19], v[156:159], v[132:135], v[16:19]\n"
    "s_waitcnt lgkmcnt(0)\n"
    "v_exp_f32 v58, v58\n"
    "v_mfma_f32_16x16x32_bf16 v[20:23], v[180:183], a[192:195], v[104:107]\n"
    "buffer_load_dwordx4 v1, s[16:19], s83 offen offset:1024 sc1 lds\n"
    "v_mfma_f32_16x16x32_bf16 v[20:23], v[184:187], a[196:199], v[20:23]\n"
    "s_add_u32 s67, s67, 49152\n"
    "ds_read_b128 v[156:159], v248 offset:0\n"
    "ds_read_b128 v[160:163], v248 offset:12288\n"
    "v_mfma_f32_16x16x32_bf16 v[20:23], v[188:191], a[200:203], v[20:23]\n"
    "s_add_u32 s59, s59, 4\n"
    "ds_read_b128 v[164:167], v248 offset:1024\n"
    "v_mfma_f32_16x16x32_bf16 v[20:23], v[192:195], a[204:207], v[20:23]\n"
    "s_waitcnt vmcnt(12)\n"
    "ds_read_b128 v[168:171], v248 offset:13312\n"
    "v_mfma_f32_16x16x32_bf16 v[20:23], v[196:199], a[208:211], v[20:23]\n"
    "buffer_store_dwordx2 v[238:239], v240, s[36:39], s80 offen offset:0 sc1\n"
    "v_mfma_f32_16x16x32_bf16 v[20:23], v[200:203], a[212:215], v[20:23]\n"
    "s_add_u32 s80, s80, 256\n"
    "ds_read_b128 v[172:175], v248 offset:2048\n"
    "ds_read_b128 v[176:179], v248 offset:14336\n"
    "v_mfma_f32_16x16x32_bf16 v[24:27], v[200:203], a[236:239], v[104:107]\n"
    "v_exp_f32 v59, v59\n"
    "v_mfma_f32_16x16x32_bf16 v[24:27], v[196:199], a[232:235], v[24:27]\n"
    "v_cvt_pk_bf16_f32 v83, v58, v59\n"
    "v_mfma_f32_16x16x32_bf16 v[24:27], v[192:195], a[228:231], v[24:27]\n"
    "buffer_load_dwordx2 v[204:205], v242, s[28:31], s75 offen offset:0 nt\n"
    "v_mfma_f32_16x16x32_bf16 v[24:27], v[188:191], a[224:227], v[24:27]\n"
    "v_exp_f32 v60, v60\n"
    "v_mfma_f32_16x16x32_bf16 v[24:27], v[184:187], a[220:223], v[24:27]\n"
    "buffer_load_dwordx2 v[206:207], v242, s[28:31], s75 offen offset:768 nt\n"
    "v_mfma_f32_16x16x32_bf16 v[24:27], v[180:183], a[216:219], v[24:27]\n"
    "v_exp_f32 v61, v61\n"
    "v_mfma_f32_16x16x32_bf16 v[28:31], v[180:183], a[240:243], v[104:107]\n"
    "buffer_load_dwordx2 v[208:209], v242, s[28:31], s75 offen offset:3072 nt\n"
    "v_mfma_f32_16x16x32_bf16 v[28:31], v[184:187], a[244:247], v[28:31]\n"
    "v_exp_f32 v62, v62\n"
    "v_mfma_f32_16x16x32_bf16 v[28:31], v[188:191], a[248:251], v[28:31]\n"
    "buffer_load_dwordx2 v[210:211], v242, s[28:31], s75 offen offset:3840 nt\n"
    "v_mfma_f32_16x16x32_bf16 v[28:31], v[192:195], a[252:255], v[28:31]\n"
    "v_exp_f32 v63, v63\n"
    "v_mfma_f32_16x16x32_bf16 v[28:31], v[196:199], v[124:127], v[28:31]\n"
    "buffer_load_dwordx2 v[212:213], v242, s[28:31], s76 offen offset:0 nt\n"
    "v_mfma_f32_16x16x32_bf16 v[28:31], v[200:203], v[128:131], v[28:31]\n"
    "v_cvt_pk_bf16_f32 v90, v60, v61\n"
    "v_cvt_pk_bf16_f32 v91, v62, v63\n"
    "v_mfma_f32_16x16x32_bf16 v[32:35], v[200:203], v[152:155], v[104:107]\n"
    "buffer_load_dwordx2 v[214:215], v242, s[28:31], s76 offen offset:768 nt\n"
    "v_mfma_f32_16x16x32_bf16 v[32:35], v[196:199], v[148:151], v[32:35]\n"
    "v_exp_f32 v64, v64\n"
    "v_mfma_f32_16x16x32_bf16 v[32:35], v[192:195], v[144:147], v[32:35]\n"
    "buffer_load_dwordx2 v[216:217], v242, s[28:31], s76 offen offset:3072 nt\n"
    "v_mfma_f32_16x16x32_bf16 v[32:35], v[188:191], v[140:143], v[32:35]\n"
    "v_exp_f32 v65, v65\n"
    "v_mfma_f32_16x16x32_bf16 v[32:35], v[184:187], v[136:139], v[32:35]\n"
    "v_exp_f32 v66, v66\n"
    "v_mfma_f32_16x16x32_bf16 v[32:35], v[180:183], v[132:135], v[32:35]\n"
    "s_waitcnt lgkmcnt(0)\n"
    "v_cvt_pk_bf16_f32 v98, v64, v65\n"
    "v_mfma_f32_16x16x32_bf16 a[0:3], v[156:159], v[68:71], a[0:3]\n"
    "ds_read_b128 v[180:183], v248 offset:3072\n"
    "v_mfma_f32_16x16x32_bf16 a[0:3], v[160:163], v[72:75], a[0:3]\n"
    "ds_read_b128 v[184:187], v248 offset:15360\n"
    "v_mfma_f32_16x16x32_bf16 a[16:19], v[168:171], v[72:75], a[16:19]\n"
    "ds_read_b128 v[188:191], v248 offset:4096\n"
    "v_mfma_f32_16x16x32_bf16 a[16:19], v[164:167], v[68:71], a[16:19]\n"
    "ds_read_b128 v[192:195], v248 offset:16384\n"
    "v_mfma_f32_16x16x32_bf16 a[32:35], v[172:175], v[68:71], a[32:35]\n"
    "ds_read_b128 v[196:199], v248 offset:5120\n"
    "v_mfma_f32_16x16x32_bf16 a[32:35], v[176:179], v[72:75], a[32:35]\n"
    "ds_read_b128 v[200:203], v248 offset:17408\n"
    "v_mfma_f32_16x16x32_bf16 a[36:39], v[176:179], v[80:83], a[36:39]\n"
    "ds_read_b128 v[104:107], v251 offset:0\n"
    "v_mfma_f32_16x16x32_bf16 a[36:39], v[172:175], v[76:79], a[36:39]\n"
    "v_exp_f32 v67, v67\n"
    "v_mfma_f32_16x16x32_bf16 a[20:23], v[164:167], v[76:79], a[20:23]\n"
    "v_cvt_pk_bf16_f32 v99, v66, v67\n"
    "v_mfma_f32_16x16x32_bf16 a[20:23], v[168:171], v[80:83], a[20:23]\n"
    "buffer_load_dwordx2 v[218:219], v242, s[28:31], s76 offen offset:3840 nt\n"
    "v_mfma_f32_16x16x32_bf16 a[4:7], v[160:163], v[80:83], a[4:7]\n"
    "v_exp_f32 v4, v4\n"
    "v_mfma_f32_16x16x32_bf16 a[4:7], v[156:159], v[76:79], a[4:7]\n"
    "buffer_load_dwordx2 v[220:221], v242, s[28:31], s77 offen offset:0 nt\n"
    "v_mfma_f32_16x16x32_bf16 a[8:11], v[156:159], v[84:87], a[8:11]\n"
    "v_exp_f32 v5, v5\n"
    "v_mfma_f32_16x16x32_bf16 a[8:11], v[160:163], v[88:91], a[8:11]\n"
    "buffer_load_dwordx2 v[222:223], v242, s[28:31], s77 offen offset:768 nt\n"
    "v_mfma_f32_16x16x32_bf16 a[24:27], v[168:171], v[88:91], a[24:27]\n"
    "v_exp_f32 v6, v6\n"
    "v_mfma_f32_16x16x32_bf16 a[24:27], v[164:167], v[84:87], a[24:27]\n"
    "buffer_load_dwordx2 v[224:225], v242, s[28:31], s77 offen offset:3072 nt\n"
    "v_mfma_f32_16x16x32_bf16 a[40:43], v[172:175], v[84:87], a[40:43]\n"
    "v_exp_f32 v7, v7\n"
    "v_mfma_f32_16x16x32_bf16 a[40:43], v[176:179], v[88:91], a[40:43]\n"
    "buffer_load_dwordx2 v[226:227], v242, s[28:31], s77 offen offset:3840 nt\n"
    "v_mfma_f32_16x16x32_bf16 a[44:47], v[176:179], v[96:99], a[44:47]\n"
    "v_exp_f32 v8, v8\n"
    "v_mfma_f32_16x16x32_bf16 a[44:47], v[172:175], v[92:95], a[44:47]\n"
    "buffer_load_dwordx2 v[228:229], v242, s[28:31], s78 offen offset:0 nt\n"
    "v_mfma_f32_16x16x32_bf16 a[28:31], v[164:167], v[92:95], a[28:31]\n"
    "v_exp_f32 v9, v9\n"
    "v_mfma_f32_16x16x32_bf16 a[28:31], v[168:171], v[96:99], a[28:31]\n"
    "buffer_load_dwordx2 v[230:231], v242, s[28:31], s78 offen offset:768 nt\n"
    "v_mfma_f32_16x16x32_bf16 a[12:15], v[160:163], v[96:99], a[12:15]\n"
    "v_exp_f32 v10, v10\n"
    "v_mfma_f32_16x16x32_bf16 a[12:15], v[156:159], v[92:95], a[12:15]\n"
    "s_waitcnt lgkmcnt(0)\n"
    "v_exp_f32 v11, v11\n"
    "v_mfma_f32_16x16x32_bf16 a[48:51], v[180:183], v[68:71], a[48:51]\n"
    "ds_read_b128 v[156:159], v248 offset:6144\n"
    "v_mfma_f32_16x16x32_bf16 a[48:51], v[184:187], v[72:75], a[48:51]\n"
    "ds_read_b128 v[160:163], v248 offset:18432\n"
    "v_mfma_f32_16x16x32_bf16 a[64:67], v[192:195], v[72:75], a[64:67]\n"
    "ds_read_b128 v[164:167], v248 offset:7168\n"
    "v_mfma_f32_16x16x32_bf16 a[64:67], v[188:191], v[68:71], a[64:67]\n"
    "ds_read_b128 v[168:171], v248 offset:19456\n"
    "v_mfma_f32_16x16x32_bf16 a[80:83], v[196:199], v[68:71], a[80:83]\n"
    "ds_read_b128 v[172:175], v248 offset:8192\n"
    "v_mfma_f32_16x16x32_bf16 a[80:83], v[200:203], v[72:75], a[80:83]\n"
    "ds_read_b128 v[176:179], v248 offset:20480\n"
    "v_smfmac_f32_16x16x64_bf16 v[108:111], v[104:107], v[68:75], v253\n"
    "s_add_u32 s81, s81, 128\n"
    "s_cmp_gt_u32 s81, 4096\n"
    "s_cselect_b32 s81, 0, s81\n"
    "v_smfmac_f32_16x16x64_bf16 v[112:115], v[104:107], v[76:83], v253\n"
    "v_mfma_f32_16x16x32_bf16 a[84:87], v[200:203], v[80:83], a[84:87]\n"
    "buffer_load_dwordx2 v[232:233], v242, s[28:31], s78 offen offset:3072 nt\n"
    "v_mfma_f32_16x16x32_bf16 a[84:87], v[196:199], v[76:79], a[84:87]\n"
    "v_exp_f32 v12, v12\n"
    "v_mfma_f32_16x16x32_bf16 a[68:71], v[188:191], v[76:79], a[68:71]\n"
    "buffer_load_dwordx2 v[234:235], v242, s[28:31], s78 offen offset:3840 nt\n"
    "v_mfma_f32_16x16x32_bf16 a[68:71], v[192:195], v[80:83], a[68:71]\n"
    "v_exp_f32 v13, v13\n"
    "v_mfma_f32_16x16x32_bf16 a[52:55], v[184:187], v[80:83], a[52:55]\n"
    "v_exp_f32 v14, v14\n"
    "v_mfma_f32_16x16x32_bf16 a[52:55], v[180:183], v[76:79], a[52:55]\n"
    "v_exp_f32 v15, v15\n"
    "v_mfma_f32_16x16x32_bf16 a[56:59], v[180:183], v[84:87], a[56:59]\n"
    "v_exp_f32 v16, v16\n"
    "v_mfma_f32_16x16x32_bf16 a[56:59], v[184:187], v[88:91], a[56:59]\n"
    "v_exp_f32 v17, v17\n"
    "v_mfma_f32_16x16x32_bf16 a[72:75], v[192:195], v[88:91], a[72:75]\n"
    "v_mfma_f32_16x16x32_bf16 a[72:75], v[188:191], v[84:87], a[72:75]\n"
    "v_exp_f32 v18, v18\n"
    "v_mfma_f32_16x16x32_bf16 a[88:91], v[196:199], v[84:87], a[88:91]\n"
    "v_mfma_f32_16x16x32_bf16 a[88:91], v[200:203], v[88:91], a[88:91]\n"
    "v_smfmac_f32_16x16x64_bf16 v[116:119], v[104:107], v[84:91], v253\n"
    "v_smfmac_f32_16x16x64_bf16 v[120:123], v[104:107], v[92:99], v253\n"
    "v_mfma_f32_16x16x32_bf16 a[92:95], v[200:203], v[96:99], a[92:95]\n"
    "v_exp_f32 v19, v19\n"
    "v_mfma_f32_16x16x32_bf16 a[92:95], v[196:199], v[92:95], a[92:95]\n"
    "v_exp_f32 v20, v20\n"
    "v_mfma_f32_16x16x32_bf16 a[76:79], v[188:191], v[92:95], a[76:79]\n"
    "v_mfma_f32_16x16x32_bf16 a[76:79], v[192:195], v[96:99], a[76:79]\n"
    "v_exp_f32 v21, v21\n"
    "v_mfma_f32_16x16x32_bf16 a[60:63], v[184:187], v[96:99], a[60:63]\n"
    "v_mfma_f32_16x16x32_bf16 a[60:63], v[180:183], v[92:95], a[60:63]\n"
    "s_waitcnt lgkmcnt(0)\n"
    "v_mfma_f32_16x16x32_bf16 a[96:99], v[156:159], v[68:71], a[96:99]\n"
    "ds_read_b128 v[180:183], v248 offset:9216\n"
    "v_mfma_f32_16x16x32_bf16 a[96:99], v[160:163], v[72:75], a[96:99]\n"
    "ds_read_b128 v[184:187], v248 offset:21504\n"
    "v_mfma_f32_16x16x32_bf16 a[112:115], v[168:171], v[72:75], a[112:115]\n"
    "ds_read_b128 v[188:191], v248 offset:10240\n"
    "v_mfma_f32_16x16x32_bf16 a[112:115], v[164:167], v[68:71], a[112:115]\n"
    "ds_read_b128 v[192:195], v248 offset:22528\n"
    "v_mfma_f32_16x16x32_bf16 a[128:131], v[172:175], v[68:71], a[128:131]\n"
    "buffer_load_dwordx2 v[236:237], v241, s[36:39], s81 offen offset:0 sc1\n"
    "v_mfma_f32_16x16x32_bf16 a[128:131], v[176:179], v[72:75], a[128:131]\n"
    "ds_read_b128 v[196:199], v248 offset:11264\n"
    "ds_read_b128 v[200:203], v248 offset:23552\n"
    "v_mfma_f32_16x16x32_bf16 a[132:135], v[176:179], v[80:83], a[132:135]\n"
    "v_exp_f32 v22, v22\n"
    "v_mfma_f32_16x16x32_bf16 a[132:135], v[172:175], v[76:79], a[132:135]\n"
    "v_exp_f32 v23, v23\n"
    "v_mfma_f32_16x16x32_bf16 a[116:119], v[164:167], v[76:79], a[116:119]\n"
    "v_exp_f32 v24, v24\n"
    "v_mfma_f32_16x16x32_bf16 a[116:119], v[168:171], v[80:83], a[116:119]\n"
    "v_exp_f32 v25, v25\n"
    "v_mfma_f32_16x16x32_bf16 a[100:103], v[160:163], v[80:83], a[100:103]\n"
    "v_mfma_f32_16x16x32_bf16 a[100:103], v[156:159], v[76:79], a[100:103]\n"
    "v_exp_f32 v26, v26\n"
    "v_mfma_f32_16x16x32_bf16 a[104:107], v[156:159], v[84:87], a[104:107]\n"
    "v_mfma_f32_16x16x32_bf16 a[104:107], v[160:163], v[88:91], a[104:107]\n"
    "v_exp_f32 v27, v27\n"
    "v_mfma_f32_16x16x32_bf16 a[120:123], v[168:171], v[88:91], a[120:123]\n"
    "v_mfma_f32_16x16x32_bf16 a[120:123], v[164:167], v[84:87], a[120:123]\n"
    "v_mfma_f32_16x16x32_bf16 a[136:139], v[172:175], v[84:87], a[136:139]\n"
    "v_exp_f32 v28, v28\n"
    "v_mfma_f32_16x16x32_bf16 a[136:139], v[176:179], v[88:91], a[136:139]\n"
    "v_mfma_f32_16x16x32_bf16 a[140:143], v[176:179], v[96:99], a[140:143]\n"
    "v_mfma_f32_16x16x32_bf16 a[140:143], v[172:175], v[92:95], a[140:143]\n"
    "v_exp_f32 v29, v29\n"
    "v_mfma_f32_16x16x32_bf16 a[124:127], v[164:167], v[92:95], a[124:127]\n"
    "v_mfma_f32_16x16x32_bf16 a[124:127], v[168:171], v[96:99], a[124:127]\n"
    "v_exp_f32 v30, v30\n"
    "v_mfma_f32_16x16x32_bf16 a[108:111], v[160:163], v[96:99], a[108:111]\n"
    "v_mfma_f32_16x16x32_bf16 a[108:111], v[156:159], v[92:95], a[108:111]\n"
    "s_waitcnt lgkmcnt(0)\n"
    "v_mfma_f32_16x16x32_bf16 a[144:147], v[180:183], v[68:71], a[144:147]\n"
    "ds_read_b128 v[156:159], v3 offset:12288\n"
    "v_mfma_f32_16x16x32_bf16 a[144:147], v[184:187], v[72:75], a[144:147]\n"
    "ds_read_b128 v[160:163], v3 offset:14336\n"
    "v_mfma_f32_16x16x32_bf16 a[160:163], v[192:195], v[72:75], a[160:163]\n"
    "ds_read_b128 v[164:167], v3 offset:16384\n"
    "v_mfma_f32_16x16x32_bf16 a[160:163], v[188:191], v[68:71], a[160:163]\n"
    "ds_read_b128 v[168:171], v3 offset:18432\n"
    "v_mfma_f32_16x16x32_bf16 a[176:179], v[196:199], v[68:71], a[176:179]\n"
    "ds_read_b128 v[172:175], v3 offset:20480\n"
    "v_mfma_f32_16x16x32_bf16 a[176:179], v[200:203], v[72:75], a[176:179]\n"
    "ds_read_b128 v[176:179], v3 offset:22528\n"
    "v_mfma_f32_16x16x32_bf16 a[180:183], v[200:203], v[80:83], a[180:183]\n"
    "ds_read_b128 v[100:103], v250 offset:896\n"
    "v_mfma_f32_16x16x32_bf16 a[180:183], v[196:199], v[76:79], a[180:183]\n"
    "v_exp_f32 v31, v31\n"
    "v_mfma_f32_16x16x32_bf16 a[164:167], v[188:191], v[76:79], a[164:167]\n"
    "v_exp_f32 v32, v32\n"
    "v_mfma_f32_16x16x32_bf16 a[164:167], v[192:195], v[80:83], a[164:167]\n"
    "v_exp_f32 v33, v33\n"
    "v_mfma_f32_16x16x32_bf16 a[148:151], v[184:187], v[80:83], a[148:151]\n"
    "v_exp_f32 v34, v34\n"
    "v_mfma_f32_16x16x32_bf16 a[148:151], v[180:183], v[76:79], a[148:151]\n"
    "v_exp_f32 v35, v35\n"
    "v_mfma_f32_16x16x32_bf16 a[152:155], v[180:183], v[84:87], a[152:155]\n"
    "v_mfma_f32_16x16x32_bf16 a[152:155], v[184:187], v[88:91], a[152:155]\n"
    "v_mfma_f32_16x16x32_bf16 a[168:171], v[192:195], v[88:91], a[168:171]\n"
    "s_waitcnt vmcnt(18)\n"
    "ds_add_u32 v247, v246\n"
    "v_mfma_f32_16x16x32_bf16 a[168:171], v[188:191], v[84:87], a[168:171]\n"
    "v_mfma_f32_16x16x32_bf16 a[184:187], v[196:199], v[84:87], a[184:187]\n"
    "v_mfma_f32_16x16x32_bf16 a[184:187], v[200:203], v[88:91], a[184:187]\n"
    "v_mfma_f32_16x16x32_bf16 a[188:191], v[200:203], v[96:99], a[188:191]\n"
    "v_mfma_f32_16x16x32_bf16 a[188:191], v[196:199], v[92:95], a[188:191]\n"
    "v_mfma_f32_16x16x32_bf16 a[172:175], v[188:191], v[92:95], a[172:175]\n"
    "s_sub_u32 s74, s68, s65\n"
    "v_mfma_f32_16x16x32_bf16 a[172:175], v[192:195], v[96:99], a[172:175]\n"
    "s_add_u32 s66, s66, 1\n"
    "v_mov_b32 v2, v3\n"
    "v_add_u32 v3, s74, v252\n"
    "v_mfma_f32_16x16x32_bf16 a[156:159], v[184:187], v[96:99], a[156:159]\n"
    "s_waitcnt lgkmcnt(1)\n"
    "s_cmp_eq_u32 s66, 30\n"
    "v_mfma_f32_16x16x32_bf16 a[156:159], v[180:183], v[92:95], a[156:159]\n"
    ".LwKe:\n"
    "s_cbranch_scc1 .LtailP\n"
    ".LNK:\n"
    "v_mfma_f32_16x16x32_bf16 v[36:39], v[156:159], a[192:195], v[100:103]\n"
    "ds_read_b128 v[180:183], v2 offset:13312\n"
    "v_cvt_pk_bf16_f32 v68, v4, v5\n"
    "v_mfma_f32_16x16x32_bf16 v[36:39], v[160:163], a[196:199], v[36:39]\n"
    "ds_read_b128 v[184:187], v2 offset:15360\n"
    "v_cvt_pk_bf16_f32 v69, v6, v7\n"
    "v_mfma_f32_16x16x32_bf16 v[36:39], v[164:167], a[200:203], v[36:39]\n"
    "ds_read_b128 v[188:191], v2 offset:17408\n"
    "v_cvt_pk_bf16_f32 v76, v8, v9\n"
    "v_mfma_f32_16x16x32_bf16 v[36:39], v[168:171], a[204:207], v[36:39]\n"
    "ds_read_b128 v[192:195], v2 offset:19456\n"
    "v_cvt_pk_bf16_f32 v77, v10, v11\n"
    "v_mfma_f32_16x16x32_bf16 v[36:39], v[172:175], a[208:211], v[36:39]\n"
    "ds_read_b128 v[196:199], v2 offset:21504\n"
    "v_cvt_pk_bf16_f32 v84, v12, v13\n"
    "v_mfma_f32_16x16x32_bf16 v[36:39], v[176:179], a[212:215], v[36:39]\n"
    "ds_read_b128 v[200:203], v2 offset:23552\n"
    "v_cvt_pk_bf16_f32 v85, v14, v15\n"
    "v_mfma_f32_16x16x32_bf16 v[40:43], v[176:179], a[236:239], v[100:103]\n"
    "ds_read_b128 v[104:107], v250 offset:912\n"
    "v_cvt_pk_bf16_f32 v92, v16, v17\n"
    "v_mfma_f32_16x16x32_bf16 v[40:43], v[172:175], a[232:235], v[40:43]\n"
    "s_add_u32 s68, s68, 24576\n"
    "v_cvt_pk_bf16_f32 v93, v18, v19\n"
    "v_cvt_pk_bf16_f32 v70, v20, v21\n"
    "v_mfma_f32_16x16x32_bf16 v[40:43], v[168:171], a[228:231], v[40:43]\n"
    "s_cmp_ge_u32 s68, s69\n"
    "s_cselect_b32 s68, s65, s68\n"
    "v_cvt_pk_bf16_f32 v71, v22, v23\n"
    "v_mfma_f32_16x16x32_bf16 v[40:43], v[164:167], a[224:227], v[40:43]\n"
    "v_cvt_pk_bf16_f32 v78, v24, v25\n"
    "v_cvt_pk_bf16_f32 v79, v26, v27\n"
    "v_mfma_f32_16x16x32_bf16 v[40:43], v[160:163], a[220:223], v[40:43]\n"
    "v_cvt_pk_bf16_f32 v86, v28, v29\n"
    "v_cvt_pk_bf16_f32 v87, v30, v31\n"
    "v_mfma_f32_16x16x32_bf16 v[40:43], v[156:159], a[216:219], v[40:43]\n"
    "v_cvt_pk_bf16_f32 v94, v32, v33\n"
    "v_cvt_pk_bf16_f32 v95, v34, v35\n"
    "v_mfma_f32_16x16x32_bf16 v[44:47], v[156:159], a[240:243], v[100:103]\n"
    "v_exp_f32 v36, v36\n"
    "v_mfma_f32_16x16x32_bf16 v[44:47], v[160:163], a[244:247], v[44:47]\n"
    "v_exp_f32 v37, v37\n"
    "v_mfma_f32_16x16x32_bf16 v[44:47], v[164:167], a[248:251], v[44:47]\n"
    "v_exp_f32 v38, v38\n"
    "v_mfma_f32_16x16x32_bf16 v[44:47], v[168:171], a[252:255], v[44:47]\n"
    "v_cvt_pk_bf16_f32 v72, v36, v37\n"
    "v_mfma_f32_16x16x32_bf16 v[44:47], v[172:175], v[124:127], v[44:47]\n"
    "v_exp_f32 v39, v39\n"
    "v_mfma_f32_16x16x32_bf16 v[44:47], v[176:179], v[128:131], v[44:47]\n"
    "v_cvt_pk_bf16_f32 v73, v38, v39\n"
    "v_mfma_f32_16x16x32_bf16 v[48:51], v[176:179], v[152:155], v[100:103]\n"
    "ds_read_b32 v245, v247\n"
    "v_mfma_f32_16x16x32_bf16 v[48:51], v[172:175], v[148:151], v[48:51]\n"
    "s_add_u32 m0, s68, 0\n"
    "v_exp_f32 v40, v40\n"
    "v_mfma_f32_16x16x32_bf16 v[48:51], v[168:171], v[144:147], v[48:51]\n"
    "v_mfma_f32_16x16x32_bf16 v[48:51], v[164:167], v[140:143], v[48:51]\n"
    "v_mfma_f32_16x16x32_bf16 v[48:51], v[160:163], v[136:139], v[48:51]\n"
    "s_waitcnt vmcnt(0)\n"
    "v_cmp_eq_u64 s[54:55], v[236:237], v[238:239]\n"
    "v_mfma_f32_16x16x32_bf16 v[48:51], v[156:159], v[132:135], v[48:51]\n"
    "s_waitcnt lgkmcnt(1)\n"
    "v_exp_f32 v41, v41\n"
    "v_mfma_f32_16x16x32_bf16 v[52:55], v[180:183], a[192:195], v[104:107]\n"
    "s_waitcnt lgkmcnt(0)\n"
    "s_waitcnt vmcnt(1)\n"
    "v_cmp_le_u32 s[56:57], s59, v245\n"
    "v_mfma_f32_16x16x32_bf16 v[52:55], v[184:187], a[196:199], v[52:55]\n"
    "s_add_u32 s76, s71, s60\n"
    "v_exp_f32 v42, v42\n"
    "v_mfma_f32_16x16x32_bf16 v[52:55], v[188:191], a[200:203], v[52:55]\n"
    "s_and_b64 s[54:55], s[54:55], s[56:57]\n"
    "s_cmp_eq_u64 s[54:55], -1\n"
    "v_mfma_f32_16x16x32_bf16 v[52:55], v[192:195], a[204:207], v[52:55]\n"
    "s_cbranch_scc0 .Lpoll2\n"
    ".Lpoll2r:\n"
    "v_mfma_f32_16x16x32_bf16 v[52:55], v[196:199], a[208:211], v[52:55]\n"
    "buffer_load_dwordx4 v1, s[16:19], s67 offen offset:0 sc1 lds\n"
    "v_mfma_f32_16x16x32_bf16 v[52:55], v[200:203], a[212:215], v[52:55]\n"
    "s_add_u32 s77, s71, s61\n"
    "ds_read_b128 v[156:159], v3 offset:0\n"
    "ds_read_b128 v[160:163], v3 offset:2048\n"
    "v_mfma_f32_16x16x32_bf16 v[56:59], v[200:203], a[236:239], v[104:107]\n"
    "ds_read_b128 v[164:167], v3 offset:4096\n"
    "ds_read_b128 v[168:171], v3 offset:6144\n"
    "v_mfma_f32_16x16x32_bf16 v[56:59], v[196:199], a[232:235], v[56:59]\n"
    "ds_read_b128 v[172:175], v3 offset:8192\n"
    "ds_read_b128 v[176:179], v3 offset:10240\n"
    "v_mfma_f32_16x16x32_bf16 v[56:59], v[192:195], a[228:231], v[56:59]\n"
    "buffer_load_dwordx4 v1, s[16:19], s67 offen offset:1024 sc1 lds\n"
    "v_mfma_f32_16x16x32_bf16 v[56:59], v[188:191], a[224:227], v[56:59]\n"
    "ds_read_b128 v[100:103], v250 offset:1536\n"
    "v_cvt_pk_bf16_f32 v80, v40, v41\n"
    "v_mfma_f32_16x16x32_bf16 v[56:59], v[184:187], a[220:223], v[56:59]\n"
    "v_exp_f32 v43, v43\n"
    "v_mfma_f32_16x16x32_bf16 v[56:59], v[180:183], a[216:219], v[56:59]\n"
    "v_cvt_pk_bf16_f32 v81, v42, v43\n"
    "v_cvt_f32_i32 v204, v204\n"
    "v_mfma_f32_16x16x32_bf16 v[60:63], v[180:183], a[240:243], v[104:107]\n"
    "buffer_load_dwordx4 v1, s[16:19], s67 offen offset:2048 sc1 lds\n"
    "v_mfma_f32_16x16x32_bf16 v[60:63], v[184:187], a[244:247], v[60:63]\n"
    "v_exp_f32 v44, v44\n"
    "v_mfma_f32_16x16x32_bf16 v[60:63], v[188:191], a[248:251], v[60:63]\n"
    "v_exp_f32 v45, v45\n"
    "v_mfma_f32_16x16x32_bf16 v[60:63], v[192:195], a[252:255], v[60:63]\n"
    "v_exp_f32 v46, v46\n"
    "v_mfma_f32_16x16x32_bf16 v[60:63], v[196:199], v[124:127], v[60:63]\n"
    "buffer_load_dwordx4 v1, s[16:19], s67 offen offset:3072 sc1 lds\n"
    "v_mfma_f32_16x16x32_bf16 v[60:63], v[200:203], v[128:131], v[60:63]\n"
    "s_add_u32 m0, s68, 4096\n"
    "v_exp_f32 v47, v47\n"
    "v_mfma_f32_16x16x32_bf16 v[64:67], v[200:203], v[152:155], v[104:107]\n"
    "s_add_u32 s83, s67, 4096\n"
    "v_cvt_pk_bf16_f32 v88, v44, v45\n"
    "v_cvt_pk_bf16_f32 v89, v46, v47\n"
    "v_mfma_f32_16x16x32_bf16 v[64:67], v[196:199], v[148:151], v[64:67]\n"
    "v_exp_f32 v48, v48\n"
    "v_mfma_f32_16x16x32_bf16 v[64:67], v[192:195], v[144:147], v[64:67]\n"
    "buffer_load_dwordx4 v1, s[16:19], s83 offen offset:0 sc1 lds\n"
    "v_mfma_f32_16x16x32_bf16 v[64:67], v[188:191], v[140:143], v[64:67]\n"
    "v_exp_f32 v49, v49\n"
    "v_mfma_f32_16x16x32_bf16 v[64:67], v[184:187], v[136:139], v[64:67]\n"
    "v_cvt_pk_bf16_f32 v96, v48, v49\n"
    "v_cvt_f32_i32 v205, v205\n"
    "v_mfma_f32_16x16x32_bf16 v[64:67], v[180:183], v[132:135], v[64:67]\n"
    "s_waitcnt lgkmcnt(0)\n"
    "v_cvt_f32_i32 v206, v206\n"
    "v_cvt_f32_i32 v207, v207\n"
    "v_mfma_f32_16x16x32_bf16 v[4:7], v[156:159], a[192:195], v[100:103]\n"
    "buffer_load_dwordx4 v1, s[16:19], s83 offen offset:1024 sc1 lds\n"
    "v_mfma_f32_16x16x32_bf16 v[4:7], v[160:163], a[196:199], v[4:7]\n"
    "s_add_u32 m0, s70, 0\n"
    "ds_read_b128 v[180:183], v3 offset:1024\n"
    "ds_read_b128 v[184:187], v3 offset:3072\n"
    "v_mfma_f32_16x16x32_bf16 v[4:7], v[164:167], a[200:203], v[4:7]\n"
    "s_add_u32 s83, s67, 24576\n"
    "ds_read_b128 v[188:191], v3 offset:5120\n"
    "v_cvt_f32_i32 v208, v208\n"
    "v_mfma_f32_16x16x32_bf16 v[4:7], v[168:171], a[204:207], v[4:7]\n"
    "ds_read_b128 v[192:195], v3 offset:7168\n"
    "v_cvt_f32_i32 v209, v209\n"
    "v_mfma_f32_16x16x32_bf16 v[4:7], v[172:175], a[208:211], v[4:7]\n"
    "buffer_load_dwordx4 v1, s[16:19], s83 offen offset:0 sc1 lds\n"
    "v_mfma_f32_16x16x32_bf16 v[4:7], v[176:179], a[212:215], v[4:7]\n"
    "ds_read_b128 v[196:199], v3 offset:9216\n"
    "ds_read_b128 v[200:203], v3 offset:11264\n"
    "v_mfma_f32_16x16x32_bf16 v[8:11], v[176:179], a[236:239], v[100:103]\n"
    "ds_read_b128 v[104:107], v250 offset:1552\n"
    "v_cvt_f32_i32 v210, v210\n"
    "v_mfma_f32_16x16x32_bf16 v[8:11], v[172:175], a[232:235], v[8:11]\n"
    "v_exp_f32 v52, v52\n"
    "v_mfma_f32_16x16x32_bf16 v[8:11], v[168:171], a[228:231], v[8:11]\n"
    "buffer_load_dwordx4 v1, s[16:19], s83 offen offset:1024 sc1 lds\n"
    "v_mfma_f32_16x16x32_bf16 v[8:11], v[164:167], a[224:227], v[8:11]\n"
    "v_exp_f32 v53, v53\n"
    "v_mfma_f32_16x16x32_bf16 v[8:11], v[160:163], a[220:223], v[8:11]\n"
    "v_cvt_pk_bf16_f32 v74, v52, v53\n"
    "v_cvt_f32_i32 v211, v211\n"
    "v_mfma_f32_16x16x32_bf16 v[8:11], v[156:159], a[216:219], v[8:11]\n"
    "v_exp_f32 v54, v54\n"
    "v_mfma_f32_16x16x32_bf16 v[12:15], v[156:159], a[240:243], v[100:103]\n"
    "buffer_load_dwordx4 v1, s[16:19], s83 offen offset:2048 sc1 lds\n"
    "v_mfma_f32_16x16x32_bf16 v[12:15], v[160:163], a[244:247], v[12:15]\n"
    "v_exp_f32 v50, v50\n"
    "v_mfma_f32_16x16x32_bf16 v[12:15], v[164:167], a[248:251], v[12:15]\n"
    "v_exp_f32 v55, v55\n"
    "v_mfma_f32_16x16x32_bf16 v[12:15], v[168:171], a[252:255], v[12:15]\n"
    "v_cvt_pk_bf16_f32 v75, v54, v55\n"
    "v_cvt_f32_i32 v212, v212\n"
    "v_mfma_f32_16x16x32_bf16 v[12:15], v[172:175], v[124:127], v[12:15]\n"
    "buffer_load_dwordx4 v1, s[16:19], s83 offen offset:3072 sc1 lds\n"
    "v_mfma_f32_16x16x32_bf16 v[12:15], v[176:179], v[128:131], v[12:15]\n"
    "s_add_u32 m0, s70, 4096\n"
    "v_exp_f32 v56, v56\n"
    "v_mfma_f32_16x16x32_bf16 v[16:19], v[176:179], v[152:155], v[100:103]\n"
    "s_add_u32 s83, s67, 28672\n"
    "v_exp_f32 v51, v51\n"
    "v_mfma_f32_16x16x32_bf16 v[16:19], v[172:175], v[148:151], v[16:19]\n"
    "v_cvt_pk_bf16_f32 v97, v50, v51\n"
    "v_cvt_f32_i32 v213, v213\n"
    "v_mfma_f32_16x16x32_bf16 v[16:19], v[168:171], v[144:147], v[16:19]\n"
    "buffer_load_dwordx4 v1, s[16:19], s83 offen offset:0 sc1 lds\n"
    "v_mfma_f32_16x16x32_bf16 v[16:19], v[164:167], v[140:143], v[16:19]\n"
    "v_exp_f32 v57, v57\n"
    "v_mfma_f32_16x16x32_bf16 v[16:19], v[160:163], v[136:139], v[16:19]\n"
    "v_cvt_pk_bf16_f32 v82, v56, v57\n"
    "v_cvt_f32_i32 v214, v214\n"
    "v_mfma_f32_16x16x32_bf16 v[16:19], v[156:159], v[132:135], v[16:19]\n"
    "s_waitcnt lgkmcnt(0)\n"
    "v_exp_f32 v58, v58\n"
    "v_mfma_f32_16x16x32_bf16 v[20:23], v[180:183], a[192:195], v[104:107]\n"
    "buffer_load_dwordx4 v1, s[16:19], s83 offen offset:1024 sc1 lds\n"
    "v_mfma_f32_16x16x32_bf16 v[20:23], v[184:187], a[196:199], v[20:23]\n"
    "s_add_u32 s67, s67, 49152\n"
    "ds_read_b128 v[156:159], v249 offset:0\n"
    "ds_read_b128 v[160:163], v249 offset:12288\n"
    "v_mfma_f32_16x16x32_bf16 v[20:23], v[188:191], a[200:203], v[20:23]\n"
    "s_add_u32 s59, s59, 4\n"
    "ds_read_b128 v[164:167], v249 offset:1024\n"
    "v_cvt_f32_i32 v215, v215\n"
    "v_mfma_f32_16x16x32_bf16 v[20:23], v[192:195], a[204:207], v[20:23]\n"
    "ds_read_b128 v[168:171], v249 offset:13312\n"
    "ds_read_b128 v[36:39], v243 offset:0\n"
    "v_mfma_f32_16x16x32_bf16 v[20:23], v[196:199], a[208:211], v[20:23]\n"
    "ds_read_b128 v[172:175], v249 offset:2048\n"
    "v_add_u32 v243, 512, v243\n"
    "v_mfma_f32_16x16x32_bf16 v[20:23], v[200:203], a[212:215], v[20:23]\n"
    "ds_read_b128 v[176:179], v249 offset:14336\n"
    "v_cvt_f32_i32 v216, v216\n"
    "v_mfma_f32_16x16x32_bf16 v[24:27], v[200:203], a[236:239], v[104:107]\n"
    "v_exp_f32 v59, v59\n"
    "v_mfma_f32_16x16x32_bf16 v[24:27], v[196:199], a[232:235], v[24:27]\n"
    "v_cvt_pk_bf16_f32 v83, v58, v59\n"
    "v_cvt_f32_i32 v217, v217\n"
    "v_mfma_f32_16x16x32_bf16 v[24:27], v[192:195], a[228:231], v[24:27]\n"
    "v_exp_f32 v60, v60\n"
    "v_mfma_f32_16x16x32_bf16 v[24:27], v[188:191], a[224:227], v[24:27]\n"
    "v_exp_f32 v61, v61\n"
    "v_mfma_f32_16x16x32_bf16 v[24:27], v[184:187], a[220:223], v[24:27]\n"
    "v_exp_f32 v62, v62\n"
    "v_mfma_f32_16x16x32_bf16 v[24:27], v[180:183], a[216:219], v[24:27]\n"
    "v_cvt_pk_bf16_f32 v90, v60, v61\n"
    "v_cvt_f32_i32 v218, v218\n"
    "v_mfma_f32_16x16x32_bf16 v[28:31], v[180:183], a[240:243], v[104:107]\n"
    "v_cvt_f32_i32 v219, v219\n"
    "v_cvt_f32_i32 v220, v220\n"
    "v_mfma_f32_16x16x32_bf16 v[28:31], v[184:187], a[244:247], v[28:31]\n"
    "v_exp_f32 v63, v63\n"
    "v_mfma_f32_16x16x32_bf16 v[28:31], v[188:191], a[248:251], v[28:31]\n"
    "v_cvt_pk_bf16_f32 v91, v62, v63\n"
    "v_cvt_f32_i32 v221, v221\n"
    "v_mfma_f32_16x16x32_bf16 v[28:31], v[192:195], a[252:255], v[28:31]\n"
    "v_cvt_f32_i32 v222, v222\n"
    "v_cvt_f32_i32 v223, v223\n"
    "v_mfma_f32_16x16x32_bf16 v[28:31], v[196:199], v[124:127], v[28:31]\n"
    "v_exp_f32 v64, v64\n"
    "v_mfma_f32_16x16x32_bf16 v[28:31], v[200:203], v[128:131], v[28:31]\n"
    "v_cvt_f32_i32 v224, v224\n"
    "v_cvt_f32_i32 v225, v225\n"
    "v_mfma_f32_16x16x32_bf16 v[32:35], v[200:203], v[152:155], v[104:107]\n"
    "v_cvt_f32_i32 v226, v226\n"
    "v_cvt_f32_i32 v227, v227\n"
    "v_mfma_f32_16x16x32_bf16 v[32:35], v[196:199], v[148:151], v[32:35]\n"
    "v_exp_f32 v65, v65\n"
    "v_mfma_f32_16x16x32_bf16 v[32:35], v[192:195], v[144:147], v[32:35]\n"
    "s_waitcnt lgkmcnt(2)\n"
    "v_cvt_pk_bf16_f32 v98, v64, v65\n"
    "v_fma_f32 v204, v204, v36, -v38\n"
    "v_mfma_f32_16x16x32_bf16 v[32:35], v[188:191], v[140:143], v[32:35]\n"
    "v_fma_f32 v205, v205, v37, -v39\n"
    "v_fma_f32 v206, v206, v36, -v38\n"
    "v_mfma_f32_16x16x32_bf16 v[32:35], v[184:187], v[136:139], v[32:35]\n"
    "v_exp_f32 v66, v66\n"
    "v_mfma_f32_16x16x32_bf16 v[32:35], v[180:183], v[132:135], v[32:35]\n"
    "s_waitcnt lgkmcnt(0)\n"
    "v_fma_f32 v207, v207, v37, -v39\n"
    "v_cvt_pk_bf16_f32 v204, v204, v205\n"
    "v_mfma_f32_16x16x32_bf16 a[0:3], v[156:159], v[68:71], a[0:3]\n"
    "ds_read_b128 v[180:183], v249 offset:3072\n"
    "v_cvt_pk_bf16_f32 v206, v206, v207\n"
    "v_mfma_f32_16x16x32_bf16 a[0:3], v[160:163], v[72:75], a[0:3]\n"
    "ds_read_b128 v[184:187], v249 offset:15360\n"
    "v_cvt_f32_i32 v228, v228\n"
    "v_mfma_f32_16x16x32_bf16 a[16:19], v[168:171], v[72:75], a[16:19]\n"
    "ds_read_b128 v[188:191], v249 offset:4096\n"
    "v_cvt_f32_i32 v229, v229\n"
    "v_mfma_f32_16x16x32_bf16 a[16:19], v[164:167], v[68:71], a[16:19]\n"
    "ds_read_b128 v[192:195], v249 offset:16384\n"
    "v_cvt_f32_i32 v230, v230\n"
    "v_mfma_f32_16x16x32_bf16 a[32:35], v[172:175], v[68:71], a[32:35]\n"
    "ds_read_b128 v[196:199], v249 offset:5120\n"
    "v_cvt_f32_i32 v231, v231\n"
    "v_mfma_f32_16x16x32_bf16 a[32:35], v[176:179], v[72:75], a[32:35]\n"
    "ds_read_b128 v[200:203], v249 offset:17408\n"
    "v_fma_f32 v208, v208, v36, -v38\n"
    "v_mfma_f32_16x16x32_bf16 a[36:39], v[176:179], v[80:83], a[36:39]\n"
    "ds_read_b128 v[104:107], v251 offset:768\n"
    "v_fma_f32 v209, v209, v37, -v39\n"
    "v_mfma_f32_16x16x32_bf16 a[36:39], v[172:175], v[76:79], a[36:39]\n"
    "v_exp_f32 v67, v67\n"
    "v_mfma_f32_16x16x32_bf16 a[20:23], v[164:167], v[76:79], a[20:23]\n"
    "v_cvt_pk_bf16_f32 v99, v66, v67\n"
    "v_fma_f32 v210, v210, v36, -v38\n"
    "v_mfma_f32_16x16x32_bf16 a[20:23], v[168:171], v[80:83], a[20:23]\n"
    "buffer_store_dword v204, v244, s[16:19], s76 offen offset:0 sc1\n"
    "v_mfma_f32_16x16x32_bf16 a[4:7], v[160:163], v[80:83], a[4:7]\n"
    "v_fma_f32 v211, v211, v37, -v39\n"
    "v_cvt_pk_bf16_f32 v208, v208, v209\n"
    "v_mfma_f32_16x16x32_bf16 a[4:7], v[156:159], v[76:79], a[4:7]\n"
    "buffer_store_dword v206, v244, s[16:19], s76 offen offset:128 sc1\n"
    "v_mfma_f32_16x16x32_bf16 a[8:11], v[156:159], v[84:87], a[8:11]\n"
    "v_cvt_pk_bf16_f32 v210, v210, v211\n"
    "v_cvt_f32_i32 v232, v232\n"
    "v_mfma_f32_16x16x32_bf16 a[8:11], v[160:163], v[88:91], a[8:11]\n"
    "buffer_store_dword v208, v244, s[16:19], s76 offen offset:1024 sc1\n"
    "v_mfma_f32_16x16x32_bf16 a[24:27], v[168:171], v[88:91], a[24:27]\n"
    "v_exp_f32 v4, v4\n"
    "v_mfma_f32_16x16x32_bf16 a[24:27], v[164:167], v[84:87], a[24:27]\n"
    "buffer_store_dword v210, v244, s[16:19], s76 offen offset:1152 sc1\n"
    "v_mfma_f32_16x16x32_bf16 a[40:43], v[172:175], v[84:87], a[40:43]\n"
    "v_cvt_f32_i32 v233, v233\n"
    "v_cvt_f32_i32 v234, v234\n"
    "v_mfma_f32_16x16x32_bf16 a[40:43], v[176:179], v[88:91], a[40:43]\n"
    "v_exp_f32 v5, v5\n"
    "v_mfma_f32_16x16x32_bf16 a[44:47], v[176:179], v[96:99], a[44:47]\n"
    "v_cvt_f32_i32 v235, v235\n"
    "v_fma_f32 v212, v212, v36, -v38\n"
    "v_mfma_f32_16x16x32_bf16 a[44:47], v[172:175], v[92:95], a[44:47]\n"
    "v_exp_f32 v6, v6\n"
    "v_mfma_f32_16x16x32_bf16 a[28:31], v[164:167], v[92:95], a[28:31]\n"
    "v_fma_f32 v213, v213, v37, -v39\n"
    "v_fma_f32 v214, v214, v36, -v38\n"
    "v_mfma_f32_16x16x32_bf16 a[28:31], v[168:171], v[96:99], a[28:31]\n"
    "v_exp_f32 v7, v7\n"
    "v_mfma_f32_16x16x32_bf16 a[12:15], v[160:163], v[96:99], a[12:15]\n"
    "v_exp_f32 v8, v8\n"
    "v_mfma_f32_16x16x32_bf16 a[12:15], v[156:159], v[92:95], a[12:15]\n"
    "s_waitcnt lgkmcnt(0)\n"
    "v_fma_f32 v215, v215, v37, -v39\n"
    "v_cvt_pk_bf16_f32 v212, v212, v213\n"
    "v_mfma_f32_16x16x32_bf16 a[48:51], v[180:183], v[68:71], a[48:51]\n"
    "buffer_store_dword v212, v244, s[16:19], s76 offen offset:256 sc1\n"
    "v_mfma_f32_16x16x32_bf16 a[48:51], v[184:187], v[72:75], a[48:51]\n"
    "ds_read_b128 v[156:159], v249 offset:6144\n"
    "ds_read_b128 v[160:163], v249 offset:18432\n"
    "v_mfma_f32_16x16x32_bf16 a[64:67], v[192:195], v[72:75], a[64:67]\n"
    "ds_read_b128 v[164:167], v249 offset:7168\n"
    "v_cvt_pk_bf16_f32 v214, v214, v215\n"
    "v_mfma_f32_16x16x32_bf16 a[64:67], v[188:191], v[68:71], a[64:67]\n"
    "buffer_store_dword v214, v244, s[16:19], s76 offen offset:384 sc1\n"
    "v_mfma_f32_16x16x32_bf16 a[80:83], v[196:199], v[68:71], a[80:83]\n"
    "ds_read_b128 v[168:171], v249 offset:19456\n"
    "ds_read_b128 v[172:175], v249 offset:8192\n"
    "v_mfma_f32_16x16x32_bf16 a[80:83], v[200:203], v[72:75], a[80:83]\n"
    "ds_read_b128 v[176:179], v249 offset:20480\n"
    "v_fma_f32 v216, v216, v36, -v38\n"
    "v_smfmac_f32_16x16x64_bf16 v[108:111], v[104:107], v[68:75], v253\n"
    "s_add_u32 s81, s81, 128\n"
    "s_cmp_gt_u32 s81, 4096\n"
    "s_cselect_b32 s81, 0, s81\n"
    "v_smfmac_f32_16x16x64_bf16 v[112:115], v[104:107], v[76:83], v253\n"
    "v_mfma_f32_16x16x32_bf16 a[84:87], v[200:203], v[80:83], a[84:87]\n"
    "v_exp_f32 v9, v9\n"
    "v_mfma_f32_16x16x32_bf16 a[84:87], v[196:199], v[76:79], a[84:87]\n"
    "v_exp_f32 v10, v10\n"
    "v_mfma_f32_16x16x32_bf16 a[68:71], v[188:191], v[76:79], a[68:71]\n"
    "v_fma_f32 v217, v217, v37, -v39\n"
    "v_fma_f32 v218, v218, v36, -v38\n"
    "v_mfma_f32_16x16x32_bf16 a[68:71], v[192:195], v[80:83], a[68:71]\n"
    "v_exp_f32 v11, v11\n"
    "v_mfma_f32_16x16x32_bf16 a[52:55], v[184:187], v[80:83], a[52:55]\n"
    "v_fma_f32 v219, v219, v37, -v39\n"
    "v_cvt_pk_bf16_f32 v216, v216, v217\n"
    "v_mfma_f32_16x16x32_bf16 a[52:55], v[180:183], v[76:79], a[52:55]\n"
    "v_exp_f32 v12, v12\n"
    "v_mfma_f32_16x16x32_bf16 a[56:59], v[180:183], v[84:87], a[56:59]\n"
    "v_cvt_pk_bf16_f32 v218, v218, v219\n"
    "v_fma_f32 v220, v220, v36, -v38\n"
    "v_mfma_f32_16x16x32_bf16 a[56:59], v[184:187], v[88:91], a[56:59]\n"
    "v_exp_f32 v13, v13\n"
    "v_mfma_f32_16x16x32_bf16 a[72:75], v[192:195], v[88:91], a[72:75]\n"
    "v_exp_f32 v14, v14\n"
    "v_mfma_f32_16x16x32_bf16 a[72:75], v[188:191], v[84:87], a[72:75]\n"
    "v_fma_f32 v221, v221, v37, -v39\n"
    "v_fma_f32 v222, v222, v36, -v38\n"
    "v_mfma_f32_16x16x32_bf16 a[88:91], v[196:199], v[84:87], a[88:91]\n"
    "v_exp_f32 v15, v15\n"
    "v_mfma_f32_16x16x32_bf16 a[88:91], v[200:203], v[88:91], a[88:91]\n"
    "v_fma_f32 v223, v223, v37, -v39\n"
    "v_cvt_pk_bf16_f32 v220, v220, v221\n"
    "v_smfmac_f32_16x16x64_bf16 v[116:119], v[104:107], v[84:91], v253\n"
    "v_smfmac_f32_16x16x64_bf16 v[120:123], v[104:107], v[92:99], v253\n"
    "v_mfma_f32_16x16x32_bf16 a[92:95], v[200:203], v[96:99], a[92:95]\n"
    "v_exp_f32 v16, v16\n"
    "v_mfma_f32_16x16x32_bf16 a[92:95], v[196:199], v[92:95], a[92:95]\n"
    "v_cvt_pk_bf16_f32 v222, v222, v223\n"
    "v_fma_f32 v224, v224, v36, -v38\n"
    "v_mfma_f32_16x16x32_bf16 a[76:79], v[188:191], v[92:95], a[76:79]\n"
    "v_exp_f32 v17, v17\n"
    "v_mfma_f32_16x16x32_bf16 a[76:79], v[192:195], v[96:99], a[76:79]\n"
    "v_fma_f32 v225, v225, v37, -v39\n"
    "v_fma_f32 v226, v226, v36, -v38\n"
    "v_mfma_f32_16x16x32_bf16 a[60:63], v[184:187], v[96:99], a[60:63]\n"
    "v_exp_f32 v18, v18\n"
    "v_mfma_f32_16x16x32_bf16 a[60:63], v[180:183], v[92:95], a[60:63]\n"
    "s_waitcnt lgkmcnt(0)\n"
    "v_exp_f32 v19, v19\n"
    "v_mfma_f32_16x16x32_bf16 a[96:99], v[156:159], v[68:71], a[96:99]\n"
    "ds_read_b128 v[180:183], v249 offset:9216\n"
    "v_fma_f32 v227, v227, v37, -v39\n"
    "v_mfma_f32_16x16x32_bf16 a[96:99], v[160:163], v[72:75], a[96:99]\n"
    "ds_read_b128 v[184:187], v249 offset:21504\n"
    "v_cvt_pk_bf16_f32 v224, v224, v225\n"
    "v_mfma_f32_16x16x32_bf16 a[112:115], v[168:171], v[72:75], a[112:115]\n"
    "ds_read_b128 v[188:191], v249 offset:10240\n"
    "v_cvt_pk_bf16_f32 v226, v226, v227\n"
    "v_mfma_f32_16x16x32_bf16 a[112:115], v[164:167], v[68:71], a[112:115]\n"
    "ds_read_b128 v[192:195], v249 offset:22528\n"
    "v_fma_f32 v228, v228, v36, -v38\n"
    "v_mfma_f32_16x16x32_bf16 a[128:131], v[172:175], v[68:71], a[128:131]\n"
    "buffer_load_dwordx2 v[236:237], v241, s[36:39], s81 offen offset:0 sc1\n"
    "v_mfma_f32_16x16x32_bf16 a[128:131], v[176:179], v[72:75], a[128:131]\n"
    "ds_read_b128 v[196:199], v249 offset:11264\n"
    "ds_read_b128 v[200:203], v249 offset:23552\n"
    "v_mfma_f32_16x16x32_bf16 a[132:135], v[176:179], v[80:83], a[132:135]\n"
    "buffer_store_dword v216, v244, s[16:19], s76 offen offset:1280 sc1\n"
    "v_mfma_f32_16x16x32_bf16 a[132:135], v[172:175], v[76:79], a[132:135]\n"
    "v_exp_f32 v20, v20\n"
    "v_mfma_f32_16x16x32_bf16 a[116:119], v[164:167], v[76:79], a[116:119]\n"
    "buffer_store_dword v218, v244, s[16:19], s76 offen offset:1408 sc1\n"
    "v_mfma_f32_16x16x32_bf16 a[116:119], v[168:171], v[80:83], a[116:119]\n"
    "v_exp_f32 v21, v21\n"
    "v_mfma_f32_16x16x32_bf16 a[100:103], v[160:163], v[80:83], a[100:103]\n"
    "buffer_store_dword v220, v244, s[16:19], s77 offen offset:0 sc1\n"
    "v_mfma_f32_16x16x32_bf16 a[100:103], v[156:159], v[76:79], a[100:103]\n"
    "v_fma_f32 v229, v229, v37, -v39\n"
    "v_fma_f32 v230, v230, v36, -v38\n"
    "v_mfma_f32_16x16x32_bf16 a[104:107], v[156:159], v[84:87], a[104:107]\n"
    "buffer_store_dword v222, v244, s[16:19], s77 offen offset:128 sc1\n"
    "v_mfma_f32_16x16x32_bf16 a[104:107], v[160:163], v[88:91], a[104:107]\n"
    "v_exp_f32 v22, v22\n"
    "v_mfma_f32_16x16x32_bf16 a[120:123], v[168:171], v[88:91], a[120:123]\n"
    "buffer_store_dword v224, v244, s[16:19], s77 offen offset:1024 sc1\n"
    "v_mfma_f32_16x16x32_bf16 a[120:123], v[164:167], v[84:87], a[120:123]\n"
    "v_fma_f32 v231, v231, v37, -v39\n"
    "v_cvt_pk_bf16_f32 v228, v228, v229\n"
    "v_mfma_f32_16x16x32_bf16 a[136:139], v[172:175], v[84:87], a[136:139]\n"
    "buffer_store_dword v226, v244, s[16:19], s77 offen offset:1152 sc1\n"
    "v_mfma_f32_16x16x32_bf16 a[136:139], v[176:179], v[88:91], a[136:139]\n"
    "v_exp_f32 v23, v23\n"
    "v_mfma_f32_16x16x32_bf16 a[140:143], v[176:179], v[96:99], a[140:143]\n"
    "buffer_store_dword v228, v244, s[16:19], s77 offen offset:256 sc1\n"
    "v_mfma_f32_16x16x32_bf16 a[140:143], v[172:175], v[92:95], a[140:143]\n"
    "v_exp_f32 v24, v24\n"
    "v_mfma_f32_16x16x32_bf16 a[124:127], v[164:167], v[92:95], a[124:127]\n"
    "v_cvt_pk_bf16_f32 v230, v230, v231\n"
    "v_fma_f32 v232, v232, v36, -v38\n"
    "v_mfma_f32_16x16x32_bf16 a[124:127], v[168:171], v[96:99], a[124:127]\n"
    "buffer_store_dword v230, v244, s[16:19], s77 offen offset:384 sc1\n"
    "v_mfma_f32_16x16x32_bf16 a[108:111], v[160:163], v[96:99], a[108:111]\n"
    "v_exp_f32 v25, v25\n"
    "v_mfma_f32_16x16x32_bf16 a[108:111], v[156:159], v[92:95], a[108:111]\n"
    "s_waitcnt lgkmcnt(0)\n"
    "v_fma_f32 v233, v233, v37, -v39\n"
    "v_fma_f32 v234, v234, v36, -v38\n"
    "v_mfma_f32_16x16x32_bf16 a[144:147], v[180:183], v[68:71], a[144:147]\n"
    "ds_read_b128 v[156:159], v3 offset:12288\n"
    "v_fma_f32 v235, v235, v37, -v39\n"
    "v_mfma_f32_16x16x32_bf16 a[144:147], v[184:187], v[72:75], a[144:147]\n"
    "ds_read_b128 v[160:163], v3 offset:14336\n"
    "v_cvt_pk_bf16_f32 v232, v232, v233\n"
    "v_mfma_f32_16x16x32_bf16 a[160:163], v[192:195], v[72:75], a[160:163]\n"
    "ds_read_b128 v[164:167], v3 offset:16384\n"
    "v_cvt_pk_bf16_f32 v234, v234, v235\n"
    "v_mfma_f32_16x16x32_bf16 a[160:163], v[188:191], v[68:71], a[160:163]\n"
    "ds_read_b128 v[168:171], v3 offset:18432\n"
    "v_mfma_f32_16x16x32_bf16 a[176:179], v[196:199], v[68:71], a[176:179]\n"
    "ds_read_b128 v[172:175], v3 offset:20480\n"
    "v_mfma_f32_16x16x32_bf16 a[176:179], v[200:203], v[72:75], a[176:179]\n"
    "ds_read_b128 v[176:179], v3 offset:22528\n"
    "v_mfma_f32_16x16x32_bf16 a[180:183], v[200:203], v[80:83], a[180:183]\n"
    "ds_read_b128 v[100:103], v250 offset:1664\n"
    "v_mfma_f32_16x16x32_bf16 a[180:183], v[196:199], v[76:79], a[180:183]\n"
    "buffer_store_dword v232, v244, s[16:19], s77 offen offset:1280 sc1\n"
    "v_mfma_f32_16x16x32_bf16 a[164:167], v[188:191], v[76:79], a[164:167]\n"
    "v_exp_f32 v26, v26\n"
    "v_mfma_f32_16x16x32_bf16 a[164:167], v[192:195], v[80:83], a[164:167]\n"
    "buffer_store_dword v234, v244, s[16:19], s77 offen offset:1408 sc1\n"
    "v_mfma_f32_16x16x32_bf16 a[148:151], v[184:187], v[80:83], a[148:151]\n"
    "v_exp_f32 v27, v27\n"
    "v_mfma_f32_16x16x32_bf16 a[148:151], v[180:183], v[76:79], a[148:151]\n"
    "v_exp_f32 v28, v28\n"
    "v_mfma_f32_16x16x32_bf16 a[152:155], v[180:183], v[84:87], a[152:155]\n"
    "v_exp_f32 v29, v29\n"
    "v_mfma_f32_16x16x32_bf16 a[152:155], v[184:187], v[88:91], a[152:155]\n"
    "v_exp_f32 v30, v30\n"
    "v_mfma_f32_16x16x32_bf16 a[168:171], v[192:195], v[88:91], a[168:171]\n"
    "s_waitcnt vmcnt(17)\n"
    "ds_add_u32 v247, v246\n"
    "v_mfma_f32_16x16x32_bf16 a[168:171], v[188:191], v[84:87], a[168:171]\n"
    "v_exp_f32 v31, v31\n"
    "v_mfma_f32_16x16x32_bf16 a[184:187], v[196:199], v[84:87], a[184:187]\n"
    "v_exp_f32 v32, v32\n"
    "v_mfma_f32_16x16x32_bf16 a[184:187], v[200:203], v[88:91], a[184:187]\n"
    "v_exp_f32 v33, v33\n"
    "v_mfma_f32_16x16x32_bf16 a[188:191], v[200:203], v[96:99], a[188:191]\n"
    "v_exp_f32 v34, v34\n"
    "v_mfma_f32_16x16x32_bf16 a[188:191], v[196:199], v[92:95], a[188:191]\n"
    "v_exp_f32 v35, v35\n"
    "v_mfma_f32_16x16x32_bf16 a[172:175], v[188:191], v[92:95], a[172:175]\n"
    "s_sub_u32 s74, s68, s65\n"
    "v_add_u32 v250, 1536, v250\n"
    "v_add_u32 v251, 1536, v251\n"
    "v_mfma_f32_16x16x32_bf16 a[172:175], v[192:195], v[96:99], a[172:175]\n"
    "s_add_u32 s66, s66, 1\n"
    "v_mov_b32 v2, v3\n"
    "v_add_u32 v3, s74, v252\n"
    "v_mfma_f32_16x16x32_bf16 a[156:159], v[184:187], v[96:99], a[156:159]\n"
    "s_waitcnt lgkmcnt(1)\n"
    "s_cmp_lt_u32 s66, 30\n"
    "v_mfma_f32_16x16x32_bf16 a[156:159], v[180:183], v[92:95], a[156:159]\n"
    "s_cbranch_scc1 .LPK\n"
    "s_branch .Ltail\n"
    ".LpollAK:\n"
    "s_sleep 1\n"
    "buffer_load_dwordx2 v[236:237], v87, s[36:39], 0 offen offset:0 sc1\n"
    "s_waitcnt vmcnt(0)\n"
    "v_cmp_eq_u64 s[54:55], v[236:237], v[238:239]\n"
    "s_add_u32 s58, s58, 1\n"
    "s_cmp_eq_u64 s[54:55], -1\n"
    "s_cbranch_scc1 .LpollAKr\n"
    "s_cmp_lt_u32 s58, 0x80000\n"
    "s_cbranch_scc1 .LpollAK\n"
    "s_branch .LpollAKr\n"
    ".LpollAV:\n"
    "s_sleep 1\n"
    "buffer_load_dwordx2 v[236:237], v87, s[36:39], 0 offen offset:0 sc1\n"
    "s_waitcnt vmcnt(0)\n"
    "v_cmp_eq_u64 s[54:55], v[236:237], v[238:239]\n"
    "s_add_u32 s58, s58, 1\n"
    "s_cmp_eq_u64 s[54:55], -1\n"
    "s_cbranch_scc1 .LpollAVr\n"
    "s_cmp_lt_u32 s58, 0x80000\n"
    "s_cbranch_scc1 .LpollAV\n"
    "s_branch .LpollAVr\n"
    ".Lpoll1:\n"
    "s_sleep 1\n"
    "v_cmp_eq_u64 s[54:55], v[236:237], v[238:239]\n"
    "s_nop 1\n"
    "s_cmp_eq_u64 s[54:55], -1\n"
    "s_cbranch_scc1 .Lpoll1l\n"
    "buffer_load_dwordx2 v[236:237], v241, s[36:39], s81 offen offset:0 sc1\n"
    "s_waitcnt vmcnt(0)\n"
    "v_cmp_eq_u64 s[54:55], v[236:237], v[238:239]\n"
    ".Lpoll1l:\n"
    "ds_read_b32 v245, v247\n"
    "s_waitcnt lgkmcnt(0)\n"
    "v_cmp_le_u32 s[56:57], s59, v245\n"
    "s_add_u32 s58, s58, 1\n"
    "s_nop 1\n"
    "s_and_b64 s[54:55], s[54:55], s[56:57]\n"
    "s_cmp_eq_u64 s[54:55], -1\n"
    "s_cbranch_scc1 .Lpoll1r\n"
    "s_cmp_lt_u32 s58, 0x80000\n"
    "s_cbranch_scc1 .Lpoll1\n"
    "s_branch .Lpoll1r\n"
    ".Lpoll2:\n"
    "s_sleep 1\n"
    "v_cmp_eq_u64 s[54:55], v[236:237], v[238:239]\n"
    "s_nop 1\n"
    "s_cmp_eq_u64 s[54:55], -1\n"
    "s_cbranch_scc1 .Lpoll2l\n"
    "buffer_load_dwordx2 v[236:237], v241, s[36:39], s81 offen offset:0 sc1\n"
    "s_waitcnt vmcnt(0)\n"
    "v_cmp_eq_u64 s[54:55], v[236:237], v[238:239]\n"
    ".Lpoll2l:\n"
    "ds_read_b32 v245, v247\n"
    "s_waitcnt lgkmcnt(0)\n"
    "v_cmp_le_u32 s[56:57], s59, v245\n"
    "s_add_u32 s58, s58, 1\n"
    "s_nop 1\n"
    "s_and_b64 s[54:55], s[54:55], s[56:57]\n"
    "s_cmp_eq_u64 s[54:55], -1\n"
    "s_cbranch_scc1 .Lpoll2r\n"
    "s_cmp_lt_u32 s58, 0x80000\n"
    "s_cbranch_scc1 .Lpoll2\n"
    "s_branch .Lpoll2r\n"
    ".Lpoll3:\n"
    "s_sleep 1\n"
    "v_cmp_eq_u64 s[54:55], v[236:237], v[238:239]\n"
    "s_nop 1\n"
    "s_cmp_eq_u64 s[54:55], -1\n"
    "s_cbranch_scc1 .Lpoll3l\n"
    "buffer_load_dwordx2 v[236:237], v241, s[36:39], s81 offen offset:0 sc1\n"
    "s_waitcnt vmcnt(0)\n"
    "v_cmp_eq_u64 s[54:55], v[236:237], v[238:239]\n"
    ".Lpoll3l:\n"
    "ds_read_b32 v245, v247\n"
    "s_waitcnt lgkmcnt(0)\n"
    "v_cmp_le_u32 s[56:57], s59, v245\n"
    "s_add_u32 s58, s58, 1\n"
    "s_nop 1\n"
    "s_and_b64 s[54:55], s[54:55], s[56:57]\n"
    "s_cmp_eq_u64 s[54:55], -1\n"
    "s_cbranch_scc1 .Lpoll3r\n"
    "s_cmp_lt_u32 s58, 0x80000\n"
    "s_cbranch_scc1 .Lpoll3\n"
    "s_branch .Lpoll3r\n"
    ".Lpoll4:\n"
    "s_sleep 1\n"
    "v_cmp_eq_u64 s[54:55], v[236:237], v[238:239]\n"
    "s_nop 1\n"
    "s_cmp_eq_u64 s[54:55], -1\n"
    "s_cbranch_scc1 .Lpoll4l\n"
    "buffer_load_dwordx2 v[236:237], v241, s[36:39], s81 offen offset:0 sc1\n"
    "s_waitcnt vmcnt(0)\n"
    "v_cmp_eq_u64 s[54:55], v[236:237], v[238:239]\n"
    ".Lpoll4l:\n"
    "ds_read_b32 v245, v247\n"
    "s_waitcnt lgkmcnt(0)\n"
    "v_cmp_le_u32 s[56:57], s59, v245\n"
    "s_add_u32 s58, s58, 1\n"
    "s_nop 1\n"
    "s_and_b64 s[54:55], s[54:55], s[56:57]\n"
    "s_cmp_eq_u64 s[54:55], -1\n"
    "s_cbranch_scc1 .Lpoll4r\n"
    "s_cmp_lt_u32 s58, 0x80000\n"
    "s_cbranch_scc1 .Lpoll4\n"
    "s_branch .Lpoll4r\n"
    ".Lpt0:\n"
    "s_sleep 1\n"
    "v_cmp_eq_u64 s[54:55], v[236:237], v[238:239]\n"
    "s_nop 1\n"
    "s_cmp_eq_u64 s[54:55], -1\n"
    "s_cbranch_scc1 .Lpt0l\n"
    "buffer_load_dwordx2 v[236:237], v241, s[36:39], s81 offen offset:0 sc1\n"
    "s_waitcnt vmcnt(0)\n"
    "v_cmp_eq_u64 s[54:55], v[236:237], v[238:239]\n"
    ".Lpt0l:\n"
    "ds_read_b32 v245, v247\n"
    "s_waitcnt lgkmcnt(0)\n"
    "v_cmp_le_u32 s[56:57], s59, v245\n"
    "s_add_u32 s58, s58, 1\n"
    "s_nop 1\n"
    "s_and_b64 s[54:55], s[54:55], s[56:57]\n"
    "s_cmp_eq_u64 s[54:55], -1\n"
    "s_cbranch_scc1 .Lpt0r\n"
    "s_cmp_lt_u32 s58, 0x80000\n"
    "s_cbranch_scc1 .Lpt0\n"
    "s_branch .Lpt0r\n"
    ".Lpt1:\n"
    "s_sleep 1\n"
    "ds_read_b32 v245, v247\n"
    "s_waitcnt lgkmcnt(0)\n"
    "v_cmp_le_u32 s[56:57], s59, v245\n"
    "s_add_u32 s58, s58, 1\n"
    "s_nop 1\n"
    "s_cmp_eq_u64 s[56:57], -1\n"
    "s_cbranch_scc1 .Lpt1r\n"
    "s_cmp_lt_u32 s58, 0x80000\n"
    "s_cbranch_scc1 .Lpt1\n"
    "s_branch .Lpt1r\n"
    ".fill ((0x20000 - (.LwVe - .LwV) - (. - _Z8attn_fwdPKtPKiS2_S0_S0_S0_S0_PcPt)) & 4095) / 4, 4, 0xBF800000\n"
    ".LwV:\n"
    "s_branch .LwVe\n"
    ".LPV:\n"
    "v_mfma_f32_16x16x32_bf16 v[36:39], v[156:159], a[192:195], v[100:103]\n"
    "s_add_u32 s71, s71, 98304\n"
    "ds_read_b128 v[180:183], v2 offset:13312\n"
    "v_cvt_pk_bf16_f32 v68, v4, v5\n"
    "v_mfma_f32_16x16x32_bf16 v[36:39], v[160:163], a[196:199], v[36:39]\n"
    "s_add_u32 s75, s71, s63\n"
    "ds_read_b128 v[184:187], v2 offset:15360\n"
    "v_mfma_f32_16x16x32_bf16 v[36:39], v[164:167], a[200:203], v[36:39]\n"
    "ds_read_b128 v[188:191], v2 offset:17408\n"
    "v_mfma_f32_16x16x32_bf16 v[36:39], v[168:171], a[204:207], v[36:39]\n"
    "ds_read_b128 v[192:195], v2 offset:19456\n"
    "v_cvt_pk_bf16_f32 v69, v6, v7\n"
    "v_mfma_f32_16x16x32_bf16 v[36:39], v[172:175], a[208:211], v[36:39]\n"
    "ds_read_b128 v[196:199], v2 offset:21504\n"
    "v_mfma_f32_16x16x32_bf16 v[36:39], v[176:179], a[212:215], v[36:39]\n"
    "ds_read_b128 v[200:203], v2 offset:23552\n"
    "v_mfma_f32_16x16x32_bf16 v[40:43], v[176:179], a[236:239], v[100:103]\n"
    "ds_read_b128 v[104:107], v250 offset:144\n"
    "v_mfma_f32_16x16x32_bf16 v[40:43], v[172:175], a[232:235], v[40:43]\n"
    "s_add_u32 s68, s68, 24576\n"
    "v_cvt_pk_bf16_f32 v76, v8, v9\n"
    "v_mfma_f32_16x16x32_bf16 v[40:43], v[168:171], a[228:231], v[40:43]\n"
    "s_cmp_ge_u32 s68, s69\n"
    "s_cselect_b32 s68, s65, s68\n"
    "v_mfma_f32_16x16x32_bf16 v[40:43], v[164:167], a[224:227], v[40:43]\n"
    "v_exp_f32 v36, v36\n"
    "v_mfma_f32_16x16x32_bf16 v[40:43], v[160:163], a[220:223], v[40:43]\n"
    "v_exp_f32 v37, v37\n"
    "v_mfma_f32_16x16x32_bf16 v[40:43], v[156:159], a[216:219], v[40:43]\n"
    "v_cvt_pk_bf16_f32 v77, v10, v11\n"
    "v_cvt_pk_bf16_f32 v72, v36, v37\n"
    "v_mfma_f32_16x16x32_bf16 v[44:47], v[156:159], a[240:243], v[100:103]\n"
    "v_mfma_f32_16x16x32_bf16 v[44:47], v[160:163], a[244:247], v[44:47]\n"
    "v_exp_f32 v38, v38\n"
    "v_mfma_f32_16x16x32_bf16 v[44:47], v[164:167], a[248:251], v[44:47]\n"
    "v_mfma_f32_16x16x32_bf16 v[44:47], v[168:171], a[252:255], v[44:47]\n"
    "v_cvt_pk_bf16_f32 v84, v12, v13\n"
    "v_mfma_f32_16x16x32_bf16 v[44:47], v[172:175], v[124:127], v[44:47]\n"
    "v_exp_f32 v39, v39\n"
    "v_mfma_f32_16x16x32_bf16 v[44:47], v[176:179], v[128:131], v[44:47]\n"
    "v_cvt_pk_bf16_f32 v73, v38, v39\n"
    "v_mfma_f32_16x16x32_bf16 v[48:51], v[176:179], v[152:155], v[100:103]\n"
    "ds_read_b32 v245, v247\n"
    "v_cvt_pk_bf16_f32 v85, v14, v15\n"
    "v_mfma_f32_16x16x32_bf16 v[48:51], v[172:175], v[148:151], v[48:51]\n"
    "s_add_u32 m0, s68, 0\n"
    "v_exp_f32 v40, v40\n"
    "v_mfma_f32_16x16x32_bf16 v[48:51], v[168:171], v[144:147], v[48:51]\n"
    "v_mfma_f32_16x16x32_bf16 v[48:51], v[164:167], v[140:143], v[48:51]\n"
    "v_mfma_f32_16x16x32_bf16 v[48:51], v[160:163], v[136:139], v[48:51]\n"
    "s_waitcnt vmcnt(13)\n"
    "v_cmp_eq_u64 s[54:55], v[236:237], v[238:239]\n"
    "v_cvt_pk_bf16_f32 v92, v16, v17\n"
    "v_mfma_f32_16x16x32_bf16 v[48:51], v[156:159], v[132:135], v[48:51]\n"
    "s_waitcnt lgkmcnt(1)\n"
    "v_exp_f32 v41, v41\n"
    "v_mfma_f32_16x16x32_bf16 v[52:55], v[180:183], a[192:195], v[104:107]\n"
    "s_waitcnt lgkmcnt(0)\n"
    "v_cmp_le_u32 s[56:57], s59, v245\n"
    "v_cvt_pk_bf16_f32 v80, v40, v41\n"
    "v_mfma_f32_16x16x32_bf16 v[52:55], v[184:187], a[196:199], v[52:55]\n"
    "v_exp_f32 v42, v42\n"
    "v_mfma_f32_16x16x32_bf16 v[52:55], v[188:191], a[200:203], v[52:55]\n"
    "s_and_b64 s[54:55], s[54:55], s[56:57]\n"
    "s_cmp_eq_u64 s[54:55], -1\n"
    "v_mfma_f32_16x16x32_bf16 v[52:55], v[192:195], a[204:207], v[52:55]\n"
    "s_cbranch_scc0 .Lpoll3\n"
    ".Lpoll3r:\n"
    "v_mfma_f32_16x16x32_bf16 v[52:55], v[196:199], a[208:211], v[52:55]\n"
    "buffer_load_dwordx4 v1, s[16:19], s67 offen offset:0 sc1 lds\n"
    "v_mfma_f32_16x16x32_bf16 v[52:55], v[200:203], a[212:215], v[52:55]\n"
    "ds_read_b128 v[156:159], v3 offset:0\n"
    "ds_read_b128 v[160:163], v3 offset:2048\n"
    "v_mfma_f32_16x16x32_bf16 v[56:59], v[200:203], a[236:239], v[104:107]\n"
    "ds_read_b128 v[164:167], v3 offset:4096\n"
    "ds_read_b128 v[168:171], v3 offset:6144\n"
    "v_mfma_f32_16x16x32_bf16 v[56:59], v[196:199], a[232:235], v[56:59]\n"
    "ds_read_b128 v[172:175], v3 offset:8192\n"
    "ds_read_b128 v[176:179], v3 offset:10240\n"
    "v_mfma_f32_16x16x32_bf16 v[56:59], v[192:195], a[228:231], v[56:59]\n"
    "buffer_load_dwordx4 v1, s[16:19], s67 offen offset:1024 sc1 lds\n"
    "v_mfma_f32_16x16x32_bf16 v[56:59], v[188:191], a[224:227], v[56:59]\n"
    "ds_read_b128 v[100:103], v250 offset:768\n"
    "v_cvt_pk_bf16_f32 v93, v18, v19\n"
    "v_mfma_f32_16x16x32_bf16 v[56:59], v[184:187], a[220:223], v[56:59]\n"
    "v_cvt_pk_bf16_f32 v70, v20, v21\n"
    "v_cvt_pk_bf16_f32 v71, v22, v23\n"
    "v_mfma_f32_16x16x32_bf16 v[56:59], v[180:183], a[216:219], v[56:59]\n"
    "v_exp_f32 v43, v43\n"
    "v_mfma_f32_16x16x32_bf16 v[60:63], v[180:183], a[240:243], v[104:107]\n"
    "buffer_load_dwordx4 v1, s[16:19], s67 offen offset:2048 sc1 lds\n"
    "v_mfma_f32_16x16x32_bf16 v[60:63], v[184:187], a[244:247], v[60:63]\n"
    "v_cvt_pk_bf16_f32 v78, v24, v25\n"
    "v_cvt_pk_bf16_f32 v81, v42, v43\n"
    "v_mfma_f32_16x16x32_bf16 v[60:63], v[188:191], a[248:251], v[60:63]\n"
    "v_exp_f32 v44, v44\n"
    "v_mfma_f32_16x16x32_bf16 v[60:63], v[192:195], a[252:255], v[60:63]\n"
    "v_exp_f32 v45, v45\n"
    "v_mfma_f32_16x16x32_bf16 v[60:63], v[196:199], v[124:127], v[60:63]\n"
    "buffer_load_dwordx4 v1, s[16:19], s67 offen offset:3072 sc1 lds\n"
    "v_mfma_f32_16x16x32_bf16 v[60:63], v[200:203], v[128:131], v[60:63]\n"
    "s_add_u32 m0, s68, 4096\n"
    "v_cvt_pk_bf16_f32 v79, v26, v27\n"
    "v_cvt_pk_bf16_f32 v88, v44, v45\n"
    "v_mfma_f32_16x16x32_bf16 v[64:67], v[200:203], v[152:155], v[104:107]\n"
    "s_add_u32 s83, s67, 4096\n"
    "v_exp_f32 v46, v46\n"
    "v_mfma_f32_16x16x32_bf16 v[64:67], v[196:199], v[148:151], v[64:67]\n"
    "v_exp_f32 v47, v47\n"
    "v_mfma_f32_16x16x32_bf16 v[64:67], v[192:195], v[144:147], v[64:67]\n"
    "buffer_load_dwordx4 v1, s[16:19], s83 offen offset:0 sc1 lds\n"
    "v_mfma_f32_16x16x32_bf16 v[64:67], v[188:191], v[140:143], v[64:67]\n"
    "v_cvt_pk_bf16_f32 v86, v28, v29\n"
    "v_cvt_pk_bf16_f32 v89, v46, v47\n"
    "v_mfma_f32_16x16x32_bf16 v[64:67], v[184:187], v[136:139], v[64:67]\n"
    "v_exp_f32 v48, v48\n"
    "v_mfma_f32_16x16x32_bf16 v[64:67], v[180:183], v[132:135], v[64:67]\n"
    "s_waitcnt lgkmcnt(0)\n"
    "v_exp_f32 v49, v49\n"
    "v_mfma_f32_16x16x32_bf16 v[4:7], v[156:159], a[192:195], v[100:103]\n"
    "buffer_load_dwordx4 v1, s[16:19], s83 offen offset:1024 sc1 lds\n"
    "v_mfma_f32_16x16x32_bf16 v[4:7], v[160:163], a[196:199], v[4:7]\n"
    "s_add_u32 m0, s64, 0\n"
    "ds_read_b128 v[180:183], v3 offset:1024\n"
    "ds_read_b128 v[184:187], v3 offset:3072\n"
    "v_mfma_f32_16x16x32_bf16 v[4:7], v[164:167], a[200:203], v[4:7]\n"
    "s_add_u32 s83, s67, 24576\n"
    "ds_read_b128 v[188:191], v3 offset:5120\n"
    "v_cvt_pk_bf16_f32 v87, v30, v31\n"
    "v_mfma_f32_16x16x32_bf16 v[4:7], v[168:171], a[204:207], v[4:7]\n"
    "ds_read_b128 v[192:195], v3 offset:7168\n"
    "v_cvt_pk_bf16_f32 v96, v48, v49\n"
    "v_mfma_f32_16x16x32_bf16 v[4:7], v[172:175], a[208:211], v[4:7]\n"
    "buffer_load_dwordx4 v1, s[16:19], s83 offen offset:0 sc1 lds\n"
    "v_mfma_f32_16x16x32_bf16 v[4:7], v[176:179], a[212:215], v[4:7]\n"
    "ds_read_b128 v[196:199], v3 offset:9216\n"
    "ds_read_b128 v[200:203], v3 offset:11264\n"
    "v_mfma_f32_16x16x32_bf16 v[8:11], v[176:179], a[236:239], v[100:103]\n"
    "ds_read_b128 v[104:107], v250 offset:784\n"
    "v_cvt_pk_bf16_f32 v94, v32, v33\n"
    "v_mfma_f32_16x16x32_bf16 v[8:11], v[172:175], a[232:235], v[8:11]\n"
    "v_exp_f32 v52, v52\n"
    "v_mfma_f32_16x16x32_bf16 v[8:11], v[168:171], a[228:231], v[8:11]\n"
    "buffer_load_dwordx4 v1, s[16:19], s83 offen offset:1024 sc1 lds\n"
    "v_mfma_f32_16x16x32_bf16 v[8:11], v[164:167], a[224:227], v[8:11]\n"
    "v_cvt_pk_bf16_f32 v95, v34, v35\n"
    "v_mfma_f32_16x16x32_bf16 v[8:11], v[160:163], a[220:223], v[8:11]\n"
    "v_exp_f32 v53, v53\n"
    "v_mfma_f32_16x16x32_bf16 v[8:11], v[156:159], a[216:219], v[8:11]\n"
    "v_exp_f32 v54, v54\n"
    "v_mfma_f32_16x16x32_bf16 v[12:15], v[156:159], a[240:243], v[100:103]\n"
    "buffer_load_dwordx4 v1, s[16:19], s83 offen offset:2048 sc1 lds\n"
    "v_mfma_f32_16x16x32_bf16 v[12:15], v[160:163], a[244:247], v[12:15]\n"
    "v_cvt_pk_bf16_f32 v74, v52, v53\n"
    "v_mfma_f32_16x16x32_bf16 v[12:15], v[164:167], a[248:251], v[12:15]\n"
    "v_exp_f32 v55, v55\n"
    "v_mfma_f32_16x16x32_bf16 v[12:15], v[168:171], a[252:255], v[12:15]\n"
    "v_cvt_pk_bf16_f32 v75, v54, v55\n"
    "v_mfma_f32_16x16x32_bf16 v[12:15], v[172:175], v[124:127], v[12:15]\n"
    "buffer_load_dwordx4 v1, s[16:19], s83 offen offset:3072 sc1 lds\n"
    "v_mfma_f32_16x16x32_bf16 v[12:15], v[176:179], v[128:131], v[12:15]\n"
    "s_add_u32 m0, s64, 4096\n"
    "v_exp_f32 v56, v56\n"
    "v_mfma_f32_16x16x32_bf16 v[16:19], v[176:179], v[152:155], v[100:103]\n"
    "s_add_u32 s83, s67, 28672\n"
    "v_exp_f32 v50, v50\n"
    "v_mfma_f32_16x16x32_bf16 v[16:19], v[172:175], v[148:151], v[16:19]\n"
    "v_exp_f32 v51, v51\n"
    "v_mfma_f32_16x16x32_bf16 v[16:19], v[168:171], v[144:147], v[16:19]\n"
    "buffer_load_dwordx4 v1, s[16:19], s83 offen offset:0 sc1 lds\n"
    "v_mfma_f32_16x16x32_bf16 v[16:19], v[164:167], v[140:143], v[16:19]\n"
    "v_exp_f32 v57, v57\n"
    "v_mfma_f32_16x16x32_bf16 v[16:19], v[160:163], v[136:139], v[16:19]\n"
    "v_cvt_pk_bf16_f32 v82, v56, v57\n"
    "v_cvt_pk_bf16_f32 v97, v50, v51\n"
    "v_mfma_f32_16x16x32_bf16 v[16:19], v[156:159], v[132:135], v[16:19]\n"
    "s_waitcnt lgkmcnt(0)\n"
    "v_exp_f32 v58, v58\n"
    "v_mfma_f32_16x16x32_bf16 v[20:23], v[180:183], a[192:195], v[104:107]\n"
    "buffer_load_dwordx4 v1, s[16:19], s83 offen offset:1024 sc1 lds\n"
    "v_mfma_f32_16x16x32_bf16 v[20:23], v[184:187], a[196:199], v[20:23]\n"
    "s_add_u32 s67, s67, 49152\n"
    "ds_read_b128 v[156:159], v248 offset:0\n"
    "ds_read_b128 v[160:163], v248 offset:12288\n"
    "v_mfma_f32_16x16x32_bf16 v[20:23], v[188:191], a[200:203], v[20:23]\n"
    "s_add_u32 s59, s59, 4\n"
    "ds_read_b128 v[164:167], v248 offset:1024\n"
    "v_mfma_f32_16x16x32_bf16 v[20:23], v[192:195], a[204:207], v[20:23]\n"
    "s_waitcnt vmcnt(12)\n"
    "ds_read_b128 v[168:171], v248 offset:13312\n"
    "v_mfma_f32_16x16x32_bf16 v[20:23], v[196:199], a[208:211], v[20:23]\n"
    "buffer_store_dwordx2 v[238:239], v240, s[36:39], s80 offen offset:0 sc1\n"
    "v_mfma_f32_16x16x32_bf16 v[20:23], v[200:203], a[212:215], v[20:23]\n"
    "s_add_u32 s80, s80, 256\n"
    "ds_read_b128 v[172:175], v248 offset:2048\n"
    "ds_read_b128 v[176:179], v248 offset:14336\n"
    "v_mfma_f32_16x16x32_bf16 v[24:27], v[200:203], a[236:239], v[104:107]\n"
    "v_exp_f32 v59, v59\n"
    "v_mfma_f32_16x16x32_bf16 v[24:27], v[196:199], a[232:235], v[24:27]\n"
    "v_cvt_pk_bf16_f32 v83, v58, v59\n"
    "v_mfma_f32_16x16x32_bf16 v[24:27], v[192:195], a[228:231], v[24:27]\n"
    "buffer_load_dwordx4 v[204:207], v242, s[40:43], s75 offen offset:0 nt\n"
    "v_mfma_f32_16x16x32_bf16 v[24:27], v[188:191], a[224:227], v[24:27]\n"
    "v_exp_f32 v60, v60\n"
    "v_mfma_f32_16x16x32_bf16 v[24:27], v[184:187], a[220:223], v[24:27]\n"
    "buffer_load_dwordx4 v[208:211], v242, s[40:43], s75 offen offset:768 nt\n"
    "v_mfma_f32_16x16x32_bf16 v[24:27], v[180:183], a[216:219], v[24:27]\n"
    "v_exp_f32 v61, v61\n"
    "v_mfma_f32_16x16x32_bf16 v[28:31], v[180:183], a[240:243], v[104:107]\n"
    "buffer_load_dwordx4 v[212:215], v242, s[40:43], s75 offen offset:256 nt\n"
    "v_mfma_f32_16x16x32_bf16 v[28:31], v[184:187], a[244:247], v[28:31]\n"
    "v_exp_f32 v62, v62\n"
    "v_mfma_f32_16x16x32_bf16 v[28:31], v[188:191], a[248:251], v[28:31]\n"
    "buffer_load_dwordx4 v[216:219], v242, s[40:43], s75 offen offset:1024 nt\n"
    "v_mfma_f32_16x16x32_bf16 v[28:31], v[192:195], a[252:255], v[28:31]\n"
    "v_exp_f32 v63, v63\n"
    "v_mfma_f32_16x16x32_bf16 v[28:31], v[196:199], v[124:127], v[28:31]\n"
    "buffer_load_dwordx4 v[220:223], v242, s[40:43], s75 offen offset:512 nt\n"
    "v_mfma_f32_16x16x32_bf16 v[28:31], v[200:203], v[128:131], v[28:31]\n"
    "v_cvt_pk_bf16_f32 v90, v60, v61\n"
    "v_cvt_pk_bf16_f32 v91, v62, v63\n"
    "v_mfma_f32_16x16x32_bf16 v[32:35], v[200:203], v[152:155], v[104:107]\n"
    "buffer_load_dwordx4 v[224:227], v242, s[40:43], s75 offen offset:1280 nt\n"
    "v_mfma_f32_16x16x32_bf16 v[32:35], v[196:199], v[148:151], v[32:35]\n"
    "v_exp_f32 v64, v64\n"
    "v_mfma_f32_16x16x32_bf16 v[32:35], v[192:195], v[144:147], v[32:35]\n"
    "v_exp_f32 v65, v65\n"
    "v_mfma_f32_16x16x32_bf16 v[32:35], v[188:191], v[140:143], v[32:35]\n"
    "v_cvt_pk_bf16_f32 v98, v64, v65\n"
    "v_mfma_f32_16x16x32_bf16 v[32:35], v[184:187], v[136:139], v[32:35]\n"
    "v_exp_f32 v66, v66\n"
    "v_mfma_f32_16x16x32_bf16 v[32:35], v[180:183], v[132:135], v[32:35]\n"
    "s_waitcnt lgkmcnt(0)\n"
    "v_mfma_f32_16x16x32_bf16 a[0:3], v[156:159], v[68:71], a[0:3]\n"
    "ds_read_b128 v[180:183], v248 offset:3072\n"
    "v_mfma_f32_16x16x32_bf16 a[0:3], v[160:163], v[72:75], a[0:3]\n"
    "ds_read_b128 v[184:187], v248 offset:15360\n"
    "v_mfma_f32_16x16x32_bf16 a[16:19], v[168:171], v[72:75], a[16:19]\n"
    "ds_read_b128 v[188:191], v248 offset:4096\n"
    "v_mfma_f32_16x16x32_bf16 a[16:19], v[164:167], v[68:71], a[16:19]\n"
    "ds_read_b128 v[192:195], v248 offset:16384\n"
    "v_mfma_f32_16x16x32_bf16 a[32:35], v[172:175], v[68:71], a[32:35]\n"
    "ds_read_b128 v[196:199], v248 offset:5120\n"
    "v_mfma_f32_16x16x32_bf16 a[32:35], v[176:179], v[72:75], a[32:35]\n"
    "ds_read_b128 v[200:203], v248 offset:17408\n"
    "v_mfma_f32_16x16x32_bf16 a[36:39], v[176:179], v[80:83], a[36:39]\n"
    "ds_read_b128 v[104:107], v251 offset:0\n"
    "v_mfma_f32_16x16x32_bf16 a[36:39], v[172:175], v[76:79], a[36:39]\n"
    "v_exp_f32 v67, v67\n"
    "v_mfma_f32_16x16x32_bf16 a[20:23], v[164:167], v[76:79], a[20:23]\n"
    "v_cvt_pk_bf16_f32 v99, v66, v67\n"
    "v_mfma_f32_16x16x32_bf16 a[20:23], v[168:171], v[80:83], a[20:23]\n"
    "v_exp_f32 v4, v4\n"
    "v_mfma_f32_16x16x32_bf16 a[4:7], v[160:163], v[80:83], a[4:7]\n"
    "v_exp_f32 v5, v5\n"
    "v_mfma_f32_16x16x32_bf16 a[4:7], v[156:159], v[76:79], a[4:7]\n"
    "v_exp_f32 v6, v6\n"
    "v_mfma_f32_16x16x32_bf16 a[8:11], v[156:159], v[84:87], a[8:11]\n"
    "v_exp_f32 v7, v7\n"
    "v_mfma_f32_16x16x32_bf16 a[8:11], v[160:163], v[88:91], a[8:11]\n"
    "v_mfma_f32_16x16x32_bf16 a[24:27], v[168:171], v[88:91], a[24:27]\n"
    "v_exp_f32 v8, v8\n"
    "v_mfma_f32_16x16x32_bf16 a[24:27], v[164:167], v[84:87], a[24:27]\n"
    "v_mfma_f32_16x16x32_bf16 a[40:43], v[172:175], v[84:87], a[40:43]\n"
    "v_mfma_f32_16x16x32_bf16 a[40:43], v[176:179], v[88:91], a[40:43]\n"
    "v_exp_f32 v9, v9\n"
    "v_mfma_f32_16x16x32_bf16 a[44:47], v[176:179], v[96:99], a[44:47]\n"
    "v_mfma_f32_16x16x32_bf16 a[44:47], v[172:175], v[92:95], a[44:47]\n"
    "v_mfma_f32_16x16x32_bf16 a[28:31], v[164:167], v[92:95], a[28:31]\n"
    "v_exp_f32 v10, v10\n"
    "v_mfma_f32_16x16x32_bf16 a[28:31], v[168:171], v[96:99], a[28:31]\n"
    "v_mfma_f32_16x16x32_bf16 a[12:15], v[160:163], v[96:99], a[12:15]\n"
    "v_exp_f32 v11, v11\n"
    "v_mfma_f32_16x16x32_bf16 a[12:15], v[156:159], v[92:95], a[12:15]\n"
    "s_waitcnt lgkmcnt(0)\n"
    "v_mfma_f32_16x16x32_bf16 a[48:51], v[180:183], v[68:71], a[48:51]\n"
    "ds_read_b128 v[156:159], v248 offset:6144\n"
    "v_mfma_f32_16x16x32_bf16 a[48:51], v[184:187], v[72:75], a[48:51]\n"
    "ds_read_b128 v[160:163], v248 offset:18432\n"
    "v_mfma_f32_16x16x32_bf16 a[64:67], v[192:195], v[72:75], a[64:67]\n"
    "ds_read_b128 v[164:167], v248 offset:7168\n"
    "v_mfma_f32_16x16x32_bf16 a[64:67], v[188:191], v[68:71], a[64:67]\n"
    "ds_read_b128 v[168:171], v248 offset:19456\n"
    "v_mfma_f32_16x16x32_bf16 a[80:83], v[196:199], v[68:71], a[80:83]\n"
    "ds_read_b128 v[172:175], v248 offset:8192\n"
    "v_mfma_f32_16x16x32_bf16 a[80:83], v[200:203], v[72:75], a[80:83]\n"
    "ds_read_b128 v[176:179], v248 offset:20480\n"
    "v_smfmac_f32_16x16x64_bf16 v[108:111], v[104:107], v[68:75], v253\n"
    "s_add_u32 s81, s81, 128\n"
    "s_cmp_gt_u32 s81, 4096\n"
    "s_cselect_b32 s81, 0, s81\n"
    "v_smfmac_f32_16x16x64_bf16 v[112:115], v[104:107], v[76:83], v253\n"
    "v_mfma_f32_16x16x32_bf16 a[84:87], v[200:203], v[80:83], a[84:87]\n"
    "v_exp_f32 v12, v12\n"
    "v_mfma_f32_16x16x32_bf16 a[84:87], v[196:199], v[76:79], a[84:87]\n"
    "v_exp_f32 v13, v13\n"
    "v_mfma_f32_16x16x32_bf16 a[68:71], v[188:191], v[76:79], a[68:71]\n"
    "v_exp_f32 v14, v14\n"
    "v_mfma_f32_16x16x32_bf16 a[68:71], v[192:195], v[80:83], a[68:71]\n"
    "v_exp_f32 v15, v15\n"
    "v_mfma_f32_16x16x32_bf16 a[52:55], v[184:187], v[80:83], a[52:55]\n"
    "v_exp_f32 v16, v16\n"
    "v_mfma_f32_16x16x32_bf16 a[52:55], v[180:183], v[76:79], a[52:55]\n"
    "v_mfma_f32_16x16x32_bf16 a[56:59], v[180:183], v[84:87], a[56:59]\n"
    "v_exp_f32 v17, v17\n"
    "v_mfma_f32_16x16x32_bf16 a[56:59], v[184:187], v[88:91], a[56:59]\n"
    "v_mfma_f32_16x16x32_bf16 a[72:75], v[192:195], v[88:91], a[72:75]\n"
    "v_mfma_f32_16x16x32_bf16 a[72:75], v[188:191], v[84:87], a[72:75]\n"
    "v_exp_f32 v18, v18\n"
    "v_mfma_f32_16x16x32_bf16 a[88:91], v[196:199], v[84:87], a[88:91]\n"
    "v_mfma_f32_16x16x32_bf16 a[88:91], v[200:203], v[88:91], a[88:91]\n"
    "v_smfmac_f32_16x16x64_bf16 v[116:119], v[104:107], v[84:91], v253\n"
    "v_smfmac_f32_16x16x64_bf16 v[120:123], v[104:107], v[92:99], v253\n"
    "v_mfma_f32_16x16x32_bf16 a[92:95], v[200:203], v[96:99], a[92:95]\n"
    "v_exp_f32 v19, v19\n"
    "v_mfma_f32_16x16x32_bf16 a[92:95], v[196:199], v[92:95], a[92:95]\n"
    "v_exp_f32 v20, v20\n"
    "v_mfma_f32_16x16x32_bf16 a[76:79], v[188:191], v[92:95], a[76:79]\n"
    "v_mfma_f32_16x16x32_bf16 a[76:79], v[192:195], v[96:99], a[76:79]\n"
    "v_exp_f32 v21, v21\n"
    "v_mfma_f32_16x16x32_bf16 a[60:63], v[184:187], v[96:99], a[60:63]\n"
    "v_mfma_f32_16x16x32_bf16 a[60:63], v[180:183], v[92:95], a[60:63]\n"
    "s_waitcnt lgkmcnt(0)\n"
    "v_mfma_f32_16x16x32_bf16 a[96:99], v[156:159], v[68:71], a[96:99]\n"
    "ds_read_b128 v[180:183], v248 offset:9216\n"
    "v_mfma_f32_16x16x32_bf16 a[96:99], v[160:163], v[72:75], a[96:99]\n"
    "ds_read_b128 v[184:187], v248 offset:21504\n"
    "v_mfma_f32_16x16x32_bf16 a[112:115], v[168:171], v[72:75], a[112:115]\n"
    "ds_read_b128 v[188:191], v248 offset:10240\n"
    "v_mfma_f32_16x16x32_bf16 a[112:115], v[164:167], v[68:71], a[112:115]\n"
    "ds_read_b128 v[192:195], v248 offset:22528\n"
    "v_mfma_f32_16x16x32_bf16 a[128:131], v[172:175], v[68:71], a[128:131]\n"
    "buffer_load_dwordx2 v[236:237], v241, s[36:39], s81 offen offset:0 sc1\n"
    "v_mfma_f32_16x16x32_bf16 a[128:131], v[176:179], v[72:75], a[128:131]\n"
    "ds_read_b128 v[196:199], v248 offset:11264\n"
    "ds_read_b128 v[200:203], v248 offset:23552\n"
    "v_mfma_f32_16x16x32_bf16 a[132:135], v[176:179], v[80:83], a[132:135]\n"
    "v_exp_f32 v22, v22\n"
    "v_mfma_f32_16x16x32_bf16 a[132:135], v[172:175], v[76:79], a[132:135]\n"
    "v_exp_f32 v23, v23\n"
    "v_mfma_f32_16x16x32_bf16 a[116:119], v[164:167], v[76:79], a[116:119]\n"
    "v_exp_f32 v24, v24\n"
    "v_mfma_f32_16x16x32_bf16 a[116:119], v[168:171], v[80:83], a[116:119]\n"
    "v_exp_f32 v25, v25\n"
    "v_mfma_f32_16x16x32_bf16 a[100:103], v[160:163], v[80:83], a[100:103]\n"
    "v_mfma_f32_16x16x32_bf16 a[100:103], v[156:159], v[76:79], a[100:103]\n"
    "v_exp_f32 v26, v26\n"
    "v_mfma_f32_16x16x32_bf16 a[104:107], v[156:159], v[84:87], a[104:107]\n"
    "v_mfma_f32_16x16x32_bf16 a[104:107], v[160:163], v[88:91], a[104:107]\n"
    "v_exp_f32 v27, v27\n"
    "v_mfma_f32_16x16x32_bf16 a[120:123], v[168:171], v[88:91], a[120:123]\n"
    "v_mfma_f32_16x16x32_bf16 a[120:123], v[164:167], v[84:87], a[120:123]\n"
    "v_mfma_f32_16x16x32_bf16 a[136:139], v[172:175], v[84:87], a[136:139]\n"
    "v_exp_f32 v28, v28\n"
    "v_mfma_f32_16x16x32_bf16 a[136:139], v[176:179], v[88:91], a[136:139]\n"
    "v_mfma_f32_16x16x32_bf16 a[140:143], v[176:179], v[96:99], a[140:143]\n"
    "v_mfma_f32_16x16x32_bf16 a[140:143], v[172:175], v[92:95], a[140:143]\n"
    "v_exp_f32 v29, v29\n"
    "v_mfma_f32_16x16x32_bf16 a[124:127], v[164:167], v[92:95], a[124:127]\n"
    "v_mfma_f32_16x16x32_bf16 a[124:127], v[168:171], v[96:99], a[124:127]\n"
    "v_exp_f32 v30, v30\n"
    "v_mfma_f32_16x16x32_bf16 a[108:111], v[160:163], v[96:99], a[108:111]\n"
    "v_mfma_f32_16x16x32_bf16 a[108:111], v[156:159], v[92:95], a[108:111]\n"
    "s_waitcnt lgkmcnt(0)\n"
    "v_mfma_f32_16x16x32_bf16 a[144:147], v[180:183], v[68:71], a[144:147]\n"
    "ds_read_b128 v[156:159], v3 offset:12288\n"
    "v_mfma_f32_16x16x32_bf16 a[144:147], v[184:187], v[72:75], a[144:147]\n"
    "ds_read_b128 v[160:163], v3 offset:14336\n"
    "v_mfma_f32_16x16x32_bf16 a[160:163], v[192:195], v[72:75], a[160:163]\n"
    "ds_read_b128 v[164:167], v3 offset:16384\n"
    "v_mfma_f32_16x16x32_bf16 a[160:163], v[188:191], v[68:71], a[160:163]\n"
    "ds_read_b128 v[168:171], v3 offset:18432\n"
    "v_mfma_f32_16x16x32_bf16 a[176:179], v[196:199], v[68:71], a[176:179]\n"
    "ds_read_b128 v[172:175], v3 offset:20480\n"
    "v_mfma_f32_16x16x32_bf16 a[176:179], v[200:203], v[72:75], a[176:179]\n"
    "ds_read_b128 v[176:179], v3 offset:22528\n"
    "v_mfma_f32_16x16x32_bf16 a[180:183], v[200:203], v[80:83], a[180:183]\n"
    "ds_read_b128 v[100:103], v250 offset:896\n"
    "v_mfma_f32_16x16x32_bf16 a[180:183], v[196:199], v[76:79], a[180:183]\n"
    "v_exp_f32 v31, v31\n"
    "v_mfma_f32_16x16x32_bf16 a[164:167], v[188:191], v[76:79], a[164:167]\n"
    "v_exp_f32 v32, v32\n"
    "v_mfma_f32_16x16x32_bf16 a[164:167], v[192:195], v[80:83], a[164:167]\n"
    "v_exp_f32 v33, v33\n"
    "v_mfma_f32_16x16x32_bf16 a[148:151], v[184:187], v[80:83], a[148:151]\n"
    "v_exp_f32 v34, v34\n"
    "v_mfma_f32_16x16x32_bf16 a[148:151], v[180:183], v[76:79], a[148:151]\n"
    "v_exp_f32 v35, v35\n"
    "v_mfma_f32_16x16x32_bf16 a[152:155], v[180:183], v[84:87], a[152:155]\n"
    "v_mfma_f32_16x16x32_bf16 a[152:155], v[184:187], v[88:91], a[152:155]\n"
    "v_mfma_f32_16x16x32_bf16 a[168:171], v[192:195], v[88:91], a[168:171]\n"
    "s_waitcnt vmcnt(8)\n"
    "ds_add_u32 v247, v246\n"
    "v_mfma_f32_16x16x32_bf16 a[168:171], v[188:191], v[84:87], a[168:171]\n"
    "v_mfma_f32_16x16x32_bf16 a[184:187], v[196:199], v[84:87], a[184:187]\n"
    "v_mfma_f32_16x16x32_bf16 a[184:187], v[200:203], v[88:91], a[184:187]\n"
    "v_mfma_f32_16x16x32_bf16 a[188:191], v[200:203], v[96:99], a[188:191]\n"
    "v_mfma_f32_16x16x32_bf16 a[188:191], v[196:199], v[92:95], a[188:191]\n"
    "v_mfma_f32_16x16x32_bf16 a[172:175], v[188:191], v[92:95], a[172:175]\n"
    "s_sub_u32 s74, s68, s65\n"
    "v_mfma_f32_16x16x32_bf16 a[172:175], v[192:195], v[96:99], a[172:175]\n"
    "s_add_u32 s66, s66, 1\n"
    "v_mov_b32 v2, v3\n"
    "v_add_u32 v3, s74, v252\n"
    "v_mfma_f32_16x16x32_bf16 a[156:159], v[184:187], v[96:99], a[156:159]\n"
    "s_waitcnt lgkmcnt(1)\n"
    "s_cmp_eq_u32 s66, 30\n"
    "v_mfma_f32_16x16x32_bf16 a[156:159], v[180:183], v[92:95], a[156:159]\n"
    ".LwVe:\n"
    "s_cbranch_scc1 .LtailP\n"
    ".LNV:\n"
    "v_mfma_f32_16x16x32_bf16 v[36:39], v[156:159], a[192:195], v[100:103]\n"
    "ds_read_b128 v[180:183], v2 offset:13312\n"
    "v_cvt_pk_bf16_f32 v68, v4, v5\n"
    "v_mfma_f32_16x16x32_bf16 v[36:39], v[160:163], a[196:199], v[36:39]\n"
    "ds_read_b128 v[184:187], v2 offset:15360\n"
    "v_mfma_f32_16x16x32_bf16 v[36:39], v[164:167], a[200:203], v[36:39]\n"
    "ds_read_b128 v[188:191], v2 offset:17408\n"
    "v_mfma_f32_16x16x32_bf16 v[36:39], v[168:171], a[204:207], v[36:39]\n"
    "ds_read_b128 v[192:195], v2 offset:19456\n"
    "v_cvt_pk_bf16_f32 v69, v6, v7\n"
    "v_mfma_f32_16x16x32_bf16 v[36:39], v[172:175], a[208:211], v[36:39]\n"
    "ds_read_b128 v[196:199], v2 offset:21504\n"
    "v_mfma_f32_16x16x32_bf16 v[36:39], v[176:179], a[212:215], v[36:39]\n"
    "ds_read_b128 v[200:203], v2 offset:23552\n"
    "v_mfma_f32_16x16x32_bf16 v[40:43], v[176:179], a[236:239], v[100:103]\n"
    "ds_read_b128 v[104:107], v250 offset:912\n"
    "v_mfma_f32_16x16x32_bf16 v[40:43], v[172:175], a[232:235], v[40:43]\n"
    "s_add_u32 s68, s68, 24576\n"
    "v_cvt_pk_bf16_f32 v76, v8, v9\n"
    "v_mfma_f32_16x16x32_bf16 v[40:43], v[168:171], a[228:231], v[40:43]\n"
    "s_cmp_ge_u32 s68, s69\n"
    "s_cselect_b32 s68, s65, s68\n"
    "v_mfma_f32_16x16x32_bf16 v[40:43], v[164:167], a[224:227], v[40:43]\n"
    "v_exp_f32 v36, v36\n"
    "v_mfma_f32_16x16x32_bf16 v[40:43], v[160:163], a[220:223], v[40:43]\n"
    "v_exp_f32 v37, v37\n"
    "v_mfma_f32_16x16x32_bf16 v[40:43], v[156:159], a[216:219], v[40:43]\n"
    "v_cvt_pk_bf16_f32 v77, v10, v11\n"
    "v_cvt_pk_bf16_f32 v72, v36, v37\n"
    "v_mfma_f32_16x16x32_bf16 v[44:47], v[156:159], a[240:243], v[100:103]\n"
    "v_mfma_f32_16x16x32_bf16 v[44:47], v[160:163], a[244:247], v[44:47]\n"
    "v_exp_f32 v38, v38\n"
    "v_mfma_f32_16x16x32_bf16 v[44:47], v[164:167], a[248:251], v[44:47]\n"
    "v_mfma_f32_16x16x32_bf16 v[44:47], v[168:171], a[252:255], v[44:47]\n"
    "v_cvt_pk_bf16_f32 v84, v12, v13\n"
    "v_mfma_f32_16x16x32_bf16 v[44:47], v[172:175], v[124:127], v[44:47]\n"
    "v_exp_f32 v39, v39\n"
    "v_mfma_f32_16x16x32_bf16 v[44:47], v[176:179], v[128:131], v[44:47]\n"
    "v_cvt_pk_bf16_f32 v73, v38, v39\n"
    "v_mfma_f32_16x16x32_bf16 v[48:51], v[176:179], v[152:155], v[100:103]\n"
    "ds_read_b32 v245, v247\n"
    "v_cvt_pk_bf16_f32 v85, v14, v15\n"
    "v_mfma_f32_16x16x32_bf16 v[48:51], v[172:175], v[148:151], v[48:51]\n"
    "s_add_u32 m0, s68, 0\n"
    "v_exp_f32 v40, v40\n"
    "v_mfma_f32_16x16x32_bf16 v[48:51], v[168:171], v[144:147], v[48:51]\n"
    "v_mfma_f32_16x16x32_bf16 v[48:51], v[164:167], v[140:143], v[48:51]\n"
    "v_mfma_f32_16x16x32_bf16 v[48:51], v[160:163], v[136:139], v[48:51]\n"
    "s_waitcnt vmcnt(0)\n"
    "v_cmp_eq_u64 s[54:55], v[236:237], v[238:239]\n"
    "v_cvt_pk_bf16_f32 v92, v16, v17\n"
    "v_mfma_f32_16x16x32_bf16 v[48:51], v[156:159], v[132:135], v[48:51]\n"
    "s_waitcnt lgkmcnt(1)\n"
    "v_exp_f32 v41, v41\n"
    "v_mfma_f32_16x16x32_bf16 v[52:55], v[180:183], a[192:195], v[104:107]\n"
    "s_waitcnt lgkmcnt(0)\n"
    "s_waitcnt vmcnt(1)\n"
    "v_cmp_le_u32 s[56:57], s59, v245\n"
    "v_mfma_f32_16x16x32_bf16 v[52:55], v[184:187], a[196:199], v[52:55]\n"
    "s_add_u32 s76, s71, s60\n"
    "v_exp_f32 v42, v42\n"
    "v_mfma_f32_16x16x32_bf16 v[52:55], v[188:191], a[200:203], v[52:55]\n"
    "s_and_b64 s[54:55], s[54:55], s[56:57]\n"
    "s_cmp_eq_u64 s[54:55], -1\n"
    "v_mfma_f32_16x16x32_bf16 v[52:55], v[192:195], a[204:207], v[52:55]\n"
    "s_cbranch_scc0 .Lpoll4\n"
    ".Lpoll4r:\n"
    "v_mfma_f32_16x16x32_bf16 v[52:55], v[196:199], a[208:211], v[52:55]\n"
    "buffer_load_dwordx4 v1, s[16:19], s67 offen offset:0 sc1 lds\n"
    "v_mfma_f32_16x16x32_bf16 v[52:55], v[200:203], a[212:215], v[52:55]\n"
    "s_add_u32 s77, s71, s61\n"
    "ds_read_b128 v[156:159], v3 offset:0\n"
    "ds_read_b128 v[160:163], v3 offset:2048\n"
    "v_mfma_f32_16x16x32_bf16 v[56:59], v[200:203], a[236:239], v[104:107]\n"
    "ds_read_b128 v[164:167], v3 offset:4096\n"
    "ds_read_b128 v[168:171], v3 offset:6144\n"
    "v_mfma_f32_16x16x32_bf16 v[56:59], v[196:199], a[232:235], v[56:59]\n"
    "ds_read_b128 v[172:175], v3 offset:8192\n"
    "ds_read_b128 v[176:179], v3 offset:10240\n"
    "v_mfma_f32_16x16x32_bf16 v[56:59], v[192:195], a[228:231], v[56:59]\n"
    "buffer_load_dwordx4 v1, s[16:19], s67 offen offset:1024 sc1 lds\n"
    "v_mfma_f32_16x16x32_bf16 v[56:59], v[188:191], a[224:227], v[56:59]\n"
    "ds_read_b128 v[100:103], v250 offset:1536\n"
    "v_cvt_pk_bf16_f32 v93, v18, v19\n"
    "v_mfma_f32_16x16x32_bf16 v[56:59], v[184:187], a[220:223], v[56:59]\n"
    "s_add_u32 s78, s71, s62\n"
    "v_cvt_pk_bf16_f32 v70, v20, v21\n"
    "v_cvt_pk_bf16_f32 v71, v22, v23\n"
    "v_mfma_f32_16x16x32_bf16 v[56:59], v[180:183], a[216:219], v[56:59]\n"
    "v_exp_f32 v43, v43\n"
    "v_mfma_f32_16x16x32_bf16 v[60:63], v[180:183], a[240:243], v[104:107]\n"
    "buffer_load_dwordx4 v1, s[16:19], s67 offen offset:2048 sc1 lds\n"
    "v_mfma_f32_16x16x32_bf16 v[60:63], v[184:187], a[244:247], v[60:63]\n"
    "v_cvt_pk_bf16_f32 v78, v24, v25\n"
    "v_cvt_pk_bf16_f32 v80, v40, v41\n"
    "v_mfma_f32_16x16x32_bf16 v[60:63], v[188:191], a[248:251], v[60:63]\n"
    "v_cvt_pk_bf16_f32 v81, v42, v43\n"
    "v_mfma_f32_16x16x32_bf16 v[60:63], v[192:195], a[252:255], v[60:63]\n"
    "v_exp_f32 v44, v44\n"
    "v_mfma_f32_16x16x32_bf16 v[60:63], v[196:199], v[124:127], v[60:63]\n"
    "buffer_load_dwordx4 v1, s[16:19], s67 offen offset:3072 sc1 lds\n"
    "v_mfma_f32_16x16x32_bf16 v[60:63], v[200:203], v[128:131], v[60:63]\n"
    "s_add_u32 m0, s68, 4096\n"
    "v_cvt_pk_bf16_f32 v79, v26, v27\n"
    "v_cvt_pk_u16_u32 v204, v204, v208\n"
    "v_mfma_f32_16x16x32_bf16 v[64:67], v[200:203], v[152:155], v[104:107]\n"
    "s_add_u32 s83, s67, 4096\n"
    "v_exp_f32 v45, v45\n"
    "v_mfma_f32_16x16x32_bf16 v[64:67], v[196:199], v[148:151], v[64:67]\n"
    "v_exp_f32 v46, v46\n"
    "v_mfma_f32_16x16x32_bf16 v[64:67], v[192:195], v[144:147], v[64:67]\n"
    "buffer_load_dwordx4 v1, s[16:19], s83 offen offset:0 sc1 lds\n"
    "v_mfma_f32_16x16x32_bf16 v[64:67], v[188:191], v[140:143], v[64:67]\n"
    "v_cvt_pk_bf16_f32 v86, v28, v29\n"
    "v_cvt_pk_bf16_f32 v88, v44, v45\n"
    "v_mfma_f32_16x16x32_bf16 v[64:67], v[184:187], v[136:139], v[64:67]\n"
    "v_exp_f32 v47, v47\n"
    "v_mfma_f32_16x16x32_bf16 v[64:67], v[180:183], v[132:135], v[64:67]\n"
    "s_waitcnt lgkmcnt(0)\n"
    "v_cvt_pk_bf16_f32 v89, v46, v47\n"
    "v_cvt_pk_u16_u32 v205, v205, v209\n"
    "v_mfma_f32_16x16x32_bf16 v[4:7], v[156:159], a[192:195], v[100:103]\n"
    "buffer_load_dwordx4 v1, s[16:19], s83 offen offset:1024 sc1 lds\n"
    "v_mfma_f32_16x16x32_bf16 v[4:7], v[160:163], a[196:199], v[4:7]\n"
    "s_add_u32 m0, s70, 0\n"
    "ds_read_b128 v[180:183], v3 offset:1024\n"
    "ds_read_b128 v[184:187], v3 offset:3072\n"
    "v_mfma_f32_16x16x32_bf16 v[4:7], v[164:167], a[200:203], v[4:7]\n"
    "s_add_u32 s83, s67, 24576\n"
    "ds_read_b128 v[188:191], v3 offset:5120\n"
    "v_cvt_pk_bf16_f32 v87, v30, v31\n"
    "v_mfma_f32_16x16x32_bf16 v[4:7], v[168:171], a[204:207], v[4:7]\n"
    "ds_read_b128 v[192:195], v3 offset:7168\n"
    "v_cvt_pk_u16_u32 v206, v206, v210\n"
    "v_mfma_f32_16x16x32_bf16 v[4:7], v[172:175], a[208:211], v[4:7]\n"
    "buffer_load_dwordx4 v1, s[16:19], s83 offen offset:0 sc1 lds\n"
    "v_mfma_f32_16x16x32_bf16 v[4:7], v[176:179], a[212:215], v[4:7]\n"
    "ds_read_b128 v[196:199], v3 offset:9216\n"
    "ds_read_b128 v[200:203], v3 offset:11264\n"
    "v_mfma_f32_16x16x32_bf16 v[8:11], v[176:179], a[236:239], v[100:103]\n"
    "ds_read_b128 v[104:107], v250 offset:1552\n"
    "v_cvt_pk_bf16_f32 v94, v32, v33\n"
    "v_mfma_f32_16x16x32_bf16 v[8:11], v[172:175], a[232:235], v[8:11]\n"
    "v_exp_f32 v52, v52\n"
    "v_mfma_f32_16x16x32_bf16 v[8:11], v[168:171], a[228:231], v[8:11]\n"
    "buffer_load_dwordx4 v1, s[16:19], s83 offen offset:1024 sc1 lds\n"
    "v_mfma_f32_16x16x32_bf16 v[8:11], v[164:167], a[224:227], v[8:11]\n"
    "v_cvt_pk_bf16_f32 v95, v34, v35\n"
    "v_cvt_pk_u16_u32 v207, v207, v211\n"
    "v_mfma_f32_16x16x32_bf16 v[8:11], v[160:163], a[220:223], v[8:11]\n"
    "v_exp_f32 v53, v53\n"
    "v_mfma_f32_16x16x32_bf16 v[8:11], v[156:159], a[216:219], v[8:11]\n"
    "v_exp_f32 v54, v54\n"
    "v_mfma_f32_16x16x32_bf16 v[12:15], v[156:159], a[240:243], v[100:103]\n"
    "buffer_load_dwordx4 v1, s[16:19], s83 offen offset:2048 sc1 lds\n"
    "v_mfma_f32_16x16x32_bf16 v[12:15], v[160:163], a[244:247], v[12:15]\n"
    "v_cvt_pk_bf16_f32 v74, v52, v53\n"
    "v_or3_b32 v243, v204, v205, v243\n"
    "v_mfma_f32_16x16x32_bf16 v[12:15], v[164:167], a[248:251], v[12:15]\n"
    "v_exp_f32 v55, v55\n"
    "v_mfma_f32_16x16x32_bf16 v[12:15], v[168:171], a[252:255], v[12:15]\n"
    "v_cvt_pk_bf16_f32 v75, v54, v55\n"
    "v_mfma_f32_16x16x32_bf16 v[12:15], v[172:175], v[124:127], v[12:15]\n"
    "buffer_load_dwordx4 v1, s[16:19], s83 offen offset:3072 sc1 lds\n"
    "v_mfma_f32_16x16x32_bf16 v[12:15], v[176:179], v[128:131], v[12:15]\n"
    "s_add_u32 m0, s70, 4096\n"
    "v_exp_f32 v56, v56\n"
    "v_mfma_f32_16x16x32_bf16 v[16:19], v[176:179], v[152:155], v[100:103]\n"
    "s_add_u32 s83, s67, 28672\n"
    "v_exp_f32 v48, v48\n"
    "v_mfma_f32_16x16x32_bf16 v[16:19], v[172:175], v[148:151], v[16:19]\n"
    "v_exp_f32 v49, v49\n"
    "v_mfma_f32_16x16x32_bf16 v[16:19], v[168:171], v[144:147], v[16:19]\n"
    "buffer_load_dwordx4 v1, s[16:19], s83 offen offset:0 sc1 lds\n"
    "v_mfma_f32_16x16x32_bf16 v[16:19], v[164:167], v[140:143], v[16:19]\n"
    "v_exp_f32 v57, v57\n"
    "v_mfma_f32_16x16x32_bf16 v[16:19], v[160:163], v[136:139], v[16:19]\n"
    "v_cvt_pk_bf16_f32 v82, v56, v57\n"
    "v_cvt_pk_bf16_f32 v96, v48, v49\n"
    "v_mfma_f32_16x16x32_bf16 v[16:19], v[156:159], v[132:135], v[16:19]\n"
    "s_waitcnt lgkmcnt(0)\n"
    "v_exp_f32 v58, v58\n"
    "v_mfma_f32_16x16x32_bf16 v[20:23], v[180:183], a[192:195], v[104:107]\n"
    "buffer_load_dwordx4 v1, s[16:19], s83 offen offset:1024 sc1 lds\n"
    "v_mfma_f32_16x16x32_bf16 v[20:23], v[184:187], a[196:199], v[20:23]\n"
    "s_add_u32 s67, s67, 49152\n"
    "ds_read_b128 v[156:159], v249 offset:0\n"
    "ds_read_b128 v[160:163], v249 offset:12288\n"
    "v_mfma_f32_16x16x32_bf16 v[20:23], v[188:191], a[200:203], v[20:23]\n"
    "s_add_u32 s59, s59, 4\n"
    "ds_read_b128 v[164:167], v249 offset:1024\n"
    "v_or3_b32 v243, v206, v207, v243\n"
    "v_mfma_f32_16x16x32_bf16 v[20:23], v[192:195], a[204:207], v[20:23]\n"
    "ds_read_b128 v[168:171], v249 offset:13312\n"
    "v_cvt_pk_u16_u32 v212, v212, v216\n"
    "v_mfma_f32_16x16x32_bf16 v[20:23], v[196:199], a[208:211], v[20:23]\n"
    "ds_read_b128 v[172:175], v249 offset:2048\n"
    "v_cvt_pk_u16_u32 v213, v213, v217\n"
    "v_mfma_f32_16x16x32_bf16 v[20:23], v[200:203], a[212:215], v[20:23]\n"
    "ds_read_b128 v[176:179], v249 offset:14336\n"
    "v_mfma_f32_16x16x32_bf16 v[24:27], v[200:203], a[236:239], v[104:107]\n"
    "v_exp_f32 v59, v59\n"
    "v_mfma_f32_16x16x32_bf16 v[24:27], v[196:199], a[232:235], v[24:27]\n"
    "v_cvt_pk_bf16_f32 v83, v58, v59\n"
    "v_mfma_f32_16x16x32_bf16 v[24:27], v[192:195], a[228:231], v[24:27]\n"
    "v_exp_f32 v60, v60\n"
    "v_mfma_f32_16x16x32_bf16 v[24:27], v[188:191], a[224:227], v[24:27]\n"
    "v_exp_f32 v61, v61\n"
    "v_mfma_f32_16x16x32_bf16 v[24:27], v[184:187], a[220:223], v[24:27]\n"
    "v_exp_f32 v62, v62\n"
    "v_mfma_f32_16x16x32_bf16 v[24:27], v[180:183], a[216:219], v[24:27]\n"
    "v_cvt_pk_bf16_f32 v90, v60, v61\n"
    "v_cvt_pk_u16_u32 v214, v214, v218\n"
    "v_mfma_f32_16x16x32_bf16 v[28:31], v[180:183], a[240:243], v[104:107]\n"
    "v_exp_f32 v50, v50\n"
    "v_mfma_f32_16x16x32_bf16 v[28:31], v[184:187], a[244:247], v[28:31]\n"
    "v_exp_f32 v63, v63\n"
    "v_mfma_f32_16x16x32_bf16 v[28:31], v[188:191], a[248:251], v[28:31]\n"
    "v_cvt_pk_bf16_f32 v91, v62, v63\n"
    "v_cvt_pk_u16_u32 v215, v215, v219\n"
    "v_mfma_f32_16x16x32_bf16 v[28:31], v[192:195], a[252:255], v[28:31]\n"
    "v_exp_f32 v51, v51\n"
    "v_mfma_f32_16x16x32_bf16 v[28:31], v[196:199], v[124:127], v[28:31]\n"
    "v_exp_f32 v64, v64\n"
    "v_mfma_f32_16x16x32_bf16 v[28:31], v[200:203], v[128:131], v[28:31]\n"
    "v_cvt_pk_bf16_f32 v97, v50, v51\n"
    "v_mfma_f32_16x16x32_bf16 v[32:35], v[200:203], v[152:155], v[104:107]\n"
    "v_mfma_f32_16x16x32_bf16 v[32:35], v[196:199], v[148:151], v[32:35]\n"
    "v_exp_f32 v65, v65\n"
    "v_mfma_f32_16x16x32_bf16 v[32:35], v[192:195], v[144:147], v[32:35]\n"
    "v_cvt_pk_bf16_f32 v98, v64, v65\n"
    "v_or3_b32 v243, v212, v213, v243\n"
    "v_mfma_f32_16x16x32_bf16 v[32:35], v[188:191], v[140:143], v[32:35]\n"
    "v_mfma_f32_16x16x32_bf16 v[32:35], v[184:187], v[136:139], v[32:35]\n"
    "v_exp_f32 v66, v66\n"
    "v_mfma_f32_16x16x32_bf16 v[32:35], v[180:183], v[132:135], v[32:35]\n"
    "s_waitcnt lgkmcnt(0)\n"
    "v_mfma_f32_16x16x32_bf16 a[0:3], v[156:159], v[68:71], a[0:3]\n"
    "ds_read_b128 v[180:183], v249 offset:3072\n"
    "v_or3_b32 v243, v214, v215, v243\n"
    "v_mfma_f32_16x16x32_bf16 a[0:3], v[160:163], v[72:75], a[0:3]\n"
    "ds_read_b128 v[184:187], v249 offset:15360\n"
    "v_mfma_f32_16x16x32_bf16 a[16:19], v[168:171], v[72:75], a[16:19]\n"
    "ds_read_b128 v[188:191], v249 offset:4096\n"
    "v_mfma_f32_16x16x32_bf16 a[16:19], v[164:167], v[68:71], a[16:19]\n"
    "ds_read_b128 v[192:195], v249 offset:16384\n"
    "v_mfma_f32_16x16x32_bf16 a[32:35], v[172:175], v[68:71], a[32:35]\n"
    "ds_read_b128 v[196:199], v249 offset:5120\n"
    "v_mfma_f32_16x16x32_bf16 a[32:35], v[176:179], v[72:75], a[32:35]\n"
    "ds_read_b128 v[200:203], v249 offset:17408\n"
    "v_cvt_pk_u16_u32 v220, v220, v224\n"
    "v_mfma_f32_16x16x32_bf16 a[36:39], v[176:179], v[80:83], a[36:39]\n"
    "ds_read_b128 v[104:107], v251 offset:768\n"
    "v_mfma_f32_16x16x32_bf16 a[36:39], v[172:175], v[76:79], a[36:39]\n"
    "v_exp_f32 v67, v67\n"
    "v_mfma_f32_16x16x32_bf16 a[20:23], v[164:167], v[76:79], a[20:23]\n"
    "v_cvt_pk_bf16_f32 v99, v66, v67\n"
    "v_mfma_f32_16x16x32_bf16 a[20:23], v[168:171], v[80:83], a[20:23]\n"
    "v_exp_f32 v4, v4\n"
    "v_mfma_f32_16x16x32_bf16 a[4:7], v[160:163], v[80:83], a[4:7]\n"
    "v_exp_f32 v5, v5\n"
    "v_mfma_f32_16x16x32_bf16 a[4:7], v[156:159], v[76:79], a[4:7]\n"
    "v_exp_f32 v6, v6\n"
    "v_mfma_f32_16x16x32_bf16 a[8:11], v[156:159], v[84:87], a[8:11]\n"
    "v_exp_f32 v7, v7\n"
    "v_mfma_f32_16x16x32_bf16 a[8:11], v[160:163], v[88:91], a[8:11]\n"
    "v_cvt_pk_u16_u32 v221, v221, v225\n"
    "v_mfma_f32_16x16x32_bf16 a[24:27], v[168:171], v[88:91], a[24:27]\n"
    "v_exp_f32 v8, v8\n"
    "v_mfma_f32_16x16x32_bf16 a[24:27], v[164:167], v[84:87], a[24:27]\n"
    "v_cvt_pk_u16_u32 v222, v222, v226\n"
    "v_mfma_f32_16x16x32_bf16 a[40:43], v[172:175], v[84:87], a[40:43]\n"
    "v_mfma_f32_16x16x32_bf16 a[40:43], v[176:179], v[88:91], a[40:43]\n"
    "v_exp_f32 v9, v9\n"
    "v_mfma_f32_16x16x32_bf16 a[44:47], v[176:179], v[96:99], a[44:47]\n"
    "v_mfma_f32_16x16x32_bf16 a[44:47], v[172:175], v[92:95], a[44:47]\n"
    "v_mfma_f32_16x16x32_bf16 a[28:31], v[164:167], v[92:95], a[28:31]\n"
    "v_exp_f32 v10, v10\n"
    "v_mfma_f32_16x16x32_bf16 a[28:31], v[168:171], v[96:99], a[28:31]\n"
    "v_cvt_pk_u16_u32 v223, v223, v227\n"
    "v_mfma_f32_16x16x32_bf16 a[12:15], v[160:163], v[96:99], a[12:15]\n"
    "v_exp_f32 v11, v11\n"
    "v_mfma_f32_16x16x32_bf16 a[12:15], v[156:159], v[92:95], a[12:15]\n"
    "s_waitcnt lgkmcnt(0)\n"
    "v_mfma_f32_16x16x32_bf16 a[48:51], v[180:183], v[68:71], a[48:51]\n"
    "ds_read_b128 v[156:159], v249 offset:6144\n"
    "v_mfma_f32_16x16x32_bf16 a[48:51], v[184:187], v[72:75], a[48:51]\n"
    "ds_read_b128 v[160:163], v249 offset:18432\n"
    "v_mfma_f32_16x16x32_bf16 a[64:67], v[192:195], v[72:75], a[64:67]\n"
    "ds_read_b128 v[164:167], v249 offset:7168\n"
    "v_or3_b32 v243, v220, v221, v243\n"
    "v_mfma_f32_16x16x32_bf16 a[64:67], v[188:191], v[68:71], a[64:67]\n"
    "ds_read_b128 v[168:171], v249 offset:19456\n"
    "v_mfma_f32_16x16x32_bf16 a[80:83], v[196:199], v[68:71], a[80:83]\n"
    "ds_read_b128 v[172:175], v249 offset:8192\n"
    "v_mfma_f32_16x16x32_bf16 a[80:83], v[200:203], v[72:75], a[80:83]\n"
    "ds_read_b128 v[176:179], v249 offset:20480\n"
    "v_smfmac_f32_16x16x64_bf16 v[108:111], v[104:107], v[68:75], v253\n"
    "s_add_u32 s81, s81, 128\n"
    "s_cmp_gt_u32 s81, 4096\n"
    "s_cselect_b32 s81, 0, s81\n"
    "v_smfmac_f32_16x16x64_bf16 v[112:115], v[104:107], v[76:83], v253\n"
    "v_mfma_f32_16x16x32_bf16 a[84:87], v[200:203], v[80:83], a[84:87]\n"
    "v_exp_f32 v12, v12\n"
    "v_mfma_f32_16x16x32_bf16 a[84:87], v[196:199], v[76:79], a[84:87]\n"
    "v_exp_f32 v13, v13\n"
    "v_mfma_f32_16x16x32_bf16 a[68:71], v[188:191], v[76:79], a[68:71]\n"
    "v_exp_f32 v14, v14\n"
    "v_mfma_f32_16x16x32_bf16 a[68:71], v[192:195], v[80:83], a[68:71]\n"
    "v_exp_f32 v15, v15\n"
    "v_mfma_f32_16x16x32_bf16 a[52:55], v[184:187], v[80:83], a[52:55]\n"
    "v_or3_b32 v243, v222, v223, v243\n"
    "v_and_b32 v208, 0xff00ff00, v243\n"
    "v_mfma_f32_16x16x32_bf16 a[52:55], v[180:183], v[76:79], a[52:55]\n"
    "v_exp_f32 v16, v16\n"
    "v_mfma_f32_16x16x32_bf16 a[56:59], v[180:183], v[84:87], a[56:59]\n"
    "v_exp_f32 v17, v17\n"
    "v_mfma_f32_16x16x32_bf16 a[56:59], v[184:187], v[88:91], a[56:59]\n"
    "v_mfma_f32_16x16x32_bf16 a[72:75], v[192:195], v[88:91], a[72:75]\n"
    "v_mfma_f32_16x16x32_bf16 a[72:75], v[188:191], v[84:87], a[72:75]\n"
    "v_exp_f32 v18, v18\n"
    "v_mfma_f32_16x16x32_bf16 a[88:91], v[196:199], v[84:87], a[88:91]\n"
    "v_cmp_ne_u32 vcc, 0, v208\n"
    "v_mfma_f32_16x16x32_bf16 a[88:91], v[200:203], v[88:91], a[88:91]\n"
    "v_smfmac_f32_16x16x64_bf16 v[116:119], v[104:107], v[84:91], v253\n"
    "v_smfmac_f32_16x16x64_bf16 v[120:123], v[104:107], v[92:99], v253\n"
    "v_mfma_f32_16x16x32_bf16 a[92:95], v[200:203], v[96:99], a[92:95]\n"
    "v_exp_f32 v19, v19\n"
    "v_mfma_f32_16x16x32_bf16 a[92:95], v[196:199], v[92:95], a[92:95]\n"
    "v_exp_f32 v20, v20\n"
    "v_mfma_f32_16x16x32_bf16 a[76:79], v[188:191], v[92:95], a[76:79]\n"
    "v_cndmask_b32 v208, 2.0, v241, vcc\n"
    "v_mfma_f32_16x16x32_bf16 a[76:79], v[192:195], v[96:99], a[76:79]\n"
    "v_exp_f32 v21, v21\n"
    "v_mfma_f32_16x16x32_bf16 a[60:63], v[184:187], v[96:99], a[60:63]\n"
    "v_mfma_f32_16x16x32_bf16 a[60:63], v[180:183], v[92:95], a[60:63]\n"
    "s_waitcnt lgkmcnt(0)\n"
    "v_mfma_f32_16x16x32_bf16 a[96:99], v[156:159], v[68:71], a[96:99]\n"
    "ds_read_b128 v[180:183], v249 offset:9216\n"
    "v_mfma_f32_16x16x32_bf16 a[96:99], v[160:163], v[72:75], a[96:99]\n"
    "ds_read_b128 v[184:187], v249 offset:21504\n"
    "v_mfma_f32_16x16x32_bf16 a[112:115], v[168:171], v[72:75], a[112:115]\n"
    "ds_read_b128 v[188:191], v249 offset:10240\n"
    "v_mfma_f32_16x16x32_bf16 a[112:115], v[164:167], v[68:71], a[112:115]\n"
    "ds_read_b128 v[192:195], v249 offset:22528\n"
    "v_mfma_f32_16x16x32_bf16 a[128:131], v[172:175], v[68:71], a[128:131]\n"
    "buffer_load_dwordx2 v[236:237], v241, s[36:39], s81 offen offset:0 sc1\n"
    "v_mfma_f32_16x16x32_bf16 a[128:131], v[176:179], v[72:75], a[128:131]\n"
    "ds_read_b128 v[196:199], v249 offset:11264\n"
    "ds_read_b128 v[200:203], v249 offset:23552\n"
    "v_mfma_f32_16x16x32_bf16 a[132:135], v[176:179], v[80:83], a[132:135]\n"
    "buffer_store_dword v204, v244, s[16:19], s76 offen offset:0 sc1\n"
    "v_mfma_f32_16x16x32_bf16 a[132:135], v[172:175], v[76:79], a[132:135]\n"
    "v_exp_f32 v22, v22\n"
    "v_mfma_f32_16x16x32_bf16 a[116:119], v[164:167], v[76:79], a[116:119]\n"
    "buffer_store_dword v205, v244, s[16:19], s76 offen offset:1024 sc1\n"
    "v_mfma_f32_16x16x32_bf16 a[116:119], v[168:171], v[80:83], a[116:119]\n"
    "v_exp_f32 v23, v23\n"
    "v_mfma_f32_16x16x32_bf16 a[100:103], v[160:163], v[80:83], a[100:103]\n"
    "buffer_store_dword v206, v244, s[16:19], s76 offen offset:2048 sc1\n"
    "v_mfma_f32_16x16x32_bf16 a[100:103], v[156:159], v[76:79], a[100:103]\n"
    "v_exp_f32 v24, v24\n"
    "v_mfma_f32_16x16x32_bf16 a[104:107], v[156:159], v[84:87], a[104:107]\n"
    "buffer_store_dword v207, v244, s[16:19], s76 offen offset:3072 sc1\n"
    "v_mfma_f32_16x16x32_bf16 a[104:107], v[160:163], v[88:91], a[104:107]\n"
    "v_exp_f32 v25, v25\n"
    "v_mfma_f32_16x16x32_bf16 a[120:123], v[168:171], v[88:91], a[120:123]\n"
    "buffer_store_dword v212, v244, s[16:19], s77 offen offset:0 sc1\n"
    "v_mfma_f32_16x16x32_bf16 a[120:123], v[164:167], v[84:87], a[120:123]\n"
    "v_exp_f32 v26, v26\n"
    "v_mfma_f32_16x16x32_bf16 a[136:139], v[172:175], v[84:87], a[136:139]\n"
    "buffer_store_dword v213, v244, s[16:19], s77 offen offset:1024 sc1\n"
    "v_mfma_f32_16x16x32_bf16 a[136:139], v[176:179], v[88:91], a[136:139]\n"
    "v_exp_f32 v27, v27\n"
    "v_mfma_f32_16x16x32_bf16 a[140:143], v[176:179], v[96:99], a[140:143]\n"
    "buffer_store_dword v214, v244, s[16:19], s77 offen offset:2048 sc1\n"
    "v_mfma_f32_16x16x32_bf16 a[140:143], v[172:175], v[92:95], a[140:143]\n"
    "v_exp_f32 v28, v28\n"
    "v_mfma_f32_16x16x32_bf16 a[124:127], v[164:167], v[92:95], a[124:127]\n"
    "buffer_store_dword v215, v244, s[16:19], s77 offen offset:3072 sc1\n"
    "v_mfma_f32_16x16x32_bf16 a[124:127], v[168:171], v[96:99], a[124:127]\n"
    "v_exp_f32 v29, v29\n"
    "v_mfma_f32_16x16x32_bf16 a[108:111], v[160:163], v[96:99], a[108:111]\n"
    "buffer_store_dword v220, v244, s[16:19], s78 offen offset:0 sc1\n"
    "v_mfma_f32_16x16x32_bf16 a[108:111], v[156:159], v[92:95], a[108:111]\n"
    "s_waitcnt lgkmcnt(0)\n"
    "v_exp_f32 v30, v30\n"
    "v_mfma_f32_16x16x32_bf16 a[144:147], v[180:183], v[68:71], a[144:147]\n"
    "ds_read_b128 v[156:159], v3 offset:12288\n"
    "v_mfma_f32_16x16x32_bf16 a[144:147], v[184:187], v[72:75], a[144:147]\n"
    "ds_read_b128 v[160:163], v3 offset:14336\n"
    "v_mfma_f32_16x16x32_bf16 a[160:163], v[192:195], v[72:75], a[160:163]\n"
    "ds_read_b128 v[164:167], v3 offset:16384\n"
    "v_mfma_f32_16x16x32_bf16 a[160:163], v[188:191], v[68:71], a[160:163]\n"
    "ds_read_b128 v[168:171], v3 offset:18432\n"
    "v_mfma_f32_16x16x32_bf16 a[176:179], v[196:199], v[68:71], a[176:179]\n"
    "ds_read_b128 v[172:175], v3 offset:20480\n"
    "v_mfma_f32_16x16x32_bf16 a[176:179], v[200:203], v[72:75], a[176:179]\n"
    "ds_read_b128 v[176:179], v3 offset:22528\n"
    "v_mfma_f32_16x16x32_bf16 a[180:183], v[200:203], v[80:83], a[180:183]\n"
    "ds_read_b128 v[100:103], v250 offset:1664\n"
    "v_mfma_f32_16x16x32_bf16 a[180:183], v[196:199], v[76:79], a[180:183]\n"
    "buffer_store_dword v221, v244, s[16:19], s78 offen offset:1024 sc1\n"
    "v_mfma_f32_16x16x32_bf16 a[164:167], v[188:191], v[76:79], a[164:167]\n"
    "v_exp_f32 v31, v31\n"
    "v_mfma_f32_16x16x32_bf16 a[164:167], v[192:195], v[80:83], a[164:167]\n"
    "buffer_store_dword v222, v244, s[16:19], s78 offen offset:2048 sc1\n"
    "v_mfma_f32_16x16x32_bf16 a[148:151], v[184:187], v[80:83], a[148:151]\n"
    "v_exp_f32 v32, v32\n"
    "v_mfma_f32_16x16x32_bf16 a[148:151], v[180:183], v[76:79], a[148:151]\n"
    "buffer_store_dword v223, v244, s[16:19], s78 offen offset:3072 sc1\n"
    "v_mfma_f32_16x16x32_bf16 a[152:155], v[180:183], v[84:87], a[152:155]\n"
    "v_exp_f32 v33, v33\n"
    "v_mfma_f32_16x16x32_bf16 a[152:155], v[184:187], v[88:91], a[152:155]\n"
    "buffer_store_dwordx2 v[238:239], v208, s[36:39], 0 offen offset:0 sc1\n"
    "v_mfma_f32_16x16x32_bf16 a[168:171], v[192:195], v[88:91], a[168:171]\n"
    "s_waitcnt vmcnt(14)\n"
    "ds_add_u32 v247, v246\n"
    "v_mfma_f32_16x16x32_bf16 a[168:171], v[188:191], v[84:87], a[168:171]\n"
    "v_exp_f32 v34, v34\n"
    "v_mfma_f32_16x16x32_bf16 a[184:187], v[196:199], v[84:87], a[184:187]\n"
    "v_exp_f32 v35, v35\n"
    "v_mfma_f32_16x16x32_bf16 a[184:187], v[200:203], v[88:91], a[184:187]\n"
    "v_mfma_f32_16x16x32_bf16 a[188:191], v[200:203], v[96:99], a[188:191]\n"
    "v_mfma_f32_16x16x32_bf16 a[188:191], v[196:199], v[92:95], a[188:191]\n"
    "v_mfma_f32_16x16x32_bf16 a[172:175], v[188:191], v[92:95], a[172:175]\n"
    "s_sub_u32 s74, s68, s65\n"
    "v_add_u32 v250, 1536, v250\n"
    "v_add_u32 v251, 1536, v251\n"
    "v_mfma_f32_16x16x32_bf16 a[172:175], v[192:195], v[96:99], a[172:175]\n"
    "s_add_u32 s66, s66, 1\n"
    "v_mov_b32 v2, v3\n"
    "v_add_u32 v3, s74, v252\n"
    "v_mfma_f32_16x16x32_bf16 a[156:159], v[184:187], v[96:99], a[156:159]\n"
    "s_waitcnt lgkmcnt(1)\n"
    "s_cmp_lt_u32 s66, 30\n"
    "v_mfma_f32_16x16x32_bf16 a[156:159], v[180:183], v[92:95], a[156:159]\n"
    "s_cbranch_scc1 .LPV\n"
    "s_branch .Ltail\n"
    ".LtailP:\n"
    "s_cbranch_execz .Lw2\n"
    "v_swap_b32 v248, v249\n"
    "s_mov_b32 s64, s70\n"
    "v_add_u32 v250, 768, v250\n"
    "v_add_u32 v251, 768, v251\n"
    ".Ltail:\n"
    "v_mfma_f32_16x16x32_bf16 v[36:39], v[156:159], a[192:195], v[100:103]\n"
    "v_cvt_pk_bf16_f32 v79, v26, v27\n"
    "v_cvt_pk_bf16_f32 v87, v30, v31\n"
    "v_mfma_f32_16x16x32_bf16 v[40:43], v[176:179], a[236:239], v[100:103]\n"
    "ds_read_b128 v[104:107], v250 offset:144\n"
    "v_cvt_pk_bf16_f32 v69, v6, v7\n"
    "v_mfma_f32_16x16x32_bf16 v[44:47], v[156:159], a[240:243], v[100:103]\n"
    "v_cvt_pk_bf16_f32 v92, v16, v17\n"
    "v_cvt_pk_bf16_f32 v93, v18, v19\n"
    "v_mfma_f32_16x16x32_bf16 v[40:43], v[172:175], a[232:235], v[40:43]\n"
    "v_cvt_pk_bf16_f32 v71, v22, v23\n"
    "v_and_b32 v252, 0xf0, v1\n"
    "v_mfma_f32_16x16x32_bf16 v[44:47], v[160:163], a[244:247], v[44:47]\n"
    "v_cvt_pk_bf16_f32 v76, v8, v9\n"
    "v_cvt_pk_bf16_f32 v77, v10, v11\n"
    "v_mfma_f32_16x16x32_bf16 v[44:47], v[164:167], a[248:251], v[44:47]\n"
    "ds_read_b128 v[192:195], v2 offset:19456\n"
    "v_mfma_f32_16x16x32_bf16 v[44:47], v[168:171], a[252:255], v[44:47]\n"
    "ds_read_b128 v[184:187], v2 offset:15360\n"
    "v_cvt_pk_bf16_f32 v84, v12, v13\n"
    "v_mfma_f32_16x16x32_bf16 v[40:43], v[168:171], a[228:231], v[40:43]\n"
    "v_cvt_pk_bf16_f32 v85, v14, v15\n"
    "v_lshlrev_b32 v244, 16, v1\n"
    "v_mfma_f32_16x16x32_bf16 v[40:43], v[164:167], a[224:227], v[40:43]\n"
    "ds_read_b128 v[188:191], v2 offset:17408\n"
    "v_cvt_pk_bf16_f32 v94, v32, v33\n"
    "v_mfma_f32_16x16x32_bf16 v[44:47], v[172:175], v[124:127], v[44:47]\n"
    "ds_read_b128 v[180:183], v2 offset:13312\n"
    "v_cvt_pk_bf16_f32 v78, v24, v25\n"
    "v_mfma_f32_16x16x32_bf16 v[44:47], v[176:179], v[128:131], v[44:47]\n"
    "ds_read_b128 v[200:203], v2 offset:23552\n"
    "v_cvt_pk_bf16_f32 v95, v34, v35\n"
    "v_mfma_f32_16x16x32_bf16 v[48:51], v[176:179], v[152:155], v[100:103]\n"
    "s_mul_i32 s75, s84, 24576\n"
    "v_cvt_pk_bf16_f32 v70, v20, v21\n"
    "v_cvt_pk_bf16_f32 v68, v4, v5\n"
    "v_mfma_f32_16x16x32_bf16 v[40:43], v[160:163], a[220:223], v[40:43]\n"
    "s_nop 0\n"
    "v_exp_f32 v46, v46\n"
    "v_mfma_f32_16x16x32_bf16 v[40:43], v[156:159], a[216:219], v[40:43]\n"
    "s_add_u32 s76, s75, 6144\n"
    "ds_read_b128 v[196:199], v2 offset:21504\n"
    "ds_read_b128 v[100:103], v250 offset:768\n"
    "v_mfma_f32_16x16x32_bf16 v[36:39], v[160:163], a[196:199], v[36:39]\n"
    "v_and_b32 v2, 0x70, v1\n"
    "v_cvt_pk_bf16_f32 v86, v28, v29\n"
    "v_mfma_f32_16x16x32_bf16 v[48:51], v[172:175], v[148:151], v[48:51]\n"
    "s_nop 0\n"
    "v_exp_f32 v40, v40\n"
    "v_mfma_f32_16x16x32_bf16 v[36:39], v[164:167], a[200:203], v[36:39]\n"
    "v_exp_f32 v44, v44\n"
    "v_mfma_f32_16x16x32_bf16 v[48:51], v[168:171], v[144:147], v[48:51]\n"
    "v_exp_f32 v47, v47\n"
    "v_mfma_f32_16x16x32_bf16 v[36:39], v[168:171], a[204:207], v[36:39]\n"
    "s_add_u32 s77, s76, 6144\n"
    "ds_read_b32 v245, v247\n"
    "v_mfma_f32_16x16x32_bf16 v[48:51], v[164:167], v[140:143], v[48:51]\n"
    "s_waitcnt lgkmcnt(3)\n"
    "v_exp_f32 v42, v42\n"
    "v_mfma_f32_16x16x32_bf16 v[56:59], v[200:203], a[236:239], v[104:107]\n"
    "s_add_u32 s78, s77, 6144\n"
    "v_exp_f32 v41, v41\n"
    "v_mfma_f32_16x16x32_bf16 v[60:63], v[180:183], a[240:243], v[104:107]\n"
    "s_add_u32 s72, s65, 122880\n"
    "v_exp_f32 v45, v45\n"
    "v_mfma_f32_16x16x32_bf16 v[48:51], v[160:163], v[136:139], v[48:51]\n"
    "v_exp_f32 v43, v43\n"
    "v_mfma_f32_16x16x32_bf16 v[36:39], v[172:175], a[208:211], v[36:39]\n"
    "s_waitcnt vmcnt(0)\n"
    "v_lshl_add_u32 v252, v252, 3, s72\n"
    "v_lshrrev_b32 v224, 4, v1\n"
    "v_mfma_f32_16x16x32_bf16 v[36:39], v[176:179], a[212:215], v[36:39]\n"
    "v_cvt_pk_bf16_f32 v80, v40, v41\n"
    "v_and_b32 v224, 48, v224\n"
    "v_mfma_f32_16x16x32_bf16 v[64:67], v[200:203], v[152:155], v[104:107]\n"
    "v_cmp_eq_u64 s[54:55], v[236:237], v[238:239]\n"
    "v_xor_b32 v224, v224, v2\n"
    "v_mfma_f32_16x16x32_bf16 v[48:51], v[156:159], v[132:135], v[48:51]\n"
    "s_waitcnt lgkmcnt(0)\n"
    "v_cvt_pk_bf16_f32 v89, v46, v47\n"
    "v_cmp_le_u32 s[56:57], s59, v245\n"
    "v_mfma_f32_16x16x32_bf16 v[52:55], v[180:183], a[192:195], v[104:107]\n"
    "s_and_b64 s[54:55], s[54:55], s[56:57]\n"
    "v_exp_f32 v36, v36\n"
    "v_mfma_f32_16x16x32_bf16 v[56:59], v[196:199], a[232:235], v[56:59]\n"
    "v_cvt_pk_bf16_f32 v88, v44, v45\n"
    "v_add_u32 v252, v252, v224\n"
    "v_mfma_f32_16x16x32_bf16 v[52:55], v[184:187], a[196:199], v[52:55]\n"
    "s_cmp_eq_u64 s[54:55], -1\n"
    "v_exp_f32 v48, v48\n"
    "v_mfma_f32_16x16x32_bf16 v[60:63], v[184:187], a[244:247], v[60:63]\n"
    "s_cbranch_scc0 .Lpt0\n"
    ".Lpt0r:\n"
    "v_mfma_f32_16x16x32_bf16 v[52:55], v[188:191], a[200:203], v[52:55]\n"
    "s_add_u32 s59, s59, 4\n"
    "ds_read_b128 v[168:171], v3 offset:6144\n"
    "ds_read_b128 v[160:163], v3 offset:2048\n"
    "v_mfma_f32_16x16x32_bf16 v[64:67], v[196:199], v[148:151], v[64:67]\n"
    "ds_read_b128 v[172:175], v3 offset:8192\n"
    "ds_read_b128 v[176:179], v3 offset:10240\n"
    "v_mfma_f32_16x16x32_bf16 v[52:55], v[192:195], a[204:207], v[52:55]\n"
    "s_add_u32 m0, s64, 0\n"
    "ds_read_b128 v[164:167], v3 offset:4096\n"
    "ds_read_b128 v[156:159], v3 offset:0\n"
    "v_mfma_f32_16x16x32_bf16 v[52:55], v[196:199], a[208:211], v[52:55]\n"
    "v_exp_f32 v38, v38\n"
    "v_mfma_f32_16x16x32_bf16 v[60:63], v[188:191], a[248:251], v[60:63]\n"
    "v_exp_f32 v39, v39\n"
    "v_mfma_f32_16x16x32_bf16 v[52:55], v[200:203], a[212:215], v[52:55]\n"
    "s_add_u32 s83, s67, 24576\n"
    "buffer_load_dwordx4 v1, s[16:19], s83 offen offset:0 sc1 lds\n"
    "v_mfma_f32_16x16x32_bf16 v[60:63], v[192:195], a[252:255], v[60:63]\n"
    "v_cvt_pk_bf16_f32 v73, v38, v39\n"
    "v_lshrrev_b32 v224, 3, v1\n"
    "v_mfma_f32_16x16x32_bf16 v[56:59], v[192:195], a[228:231], v[56:59]\n"
    "buffer_load_dwordx4 v1, s[16:19], s83 offen offset:1024 sc1 lds\n"
    "v_mfma_f32_16x16x32_bf16 v[64:67], v[192:195], v[144:147], v[64:67]\n"
    "v_exp_f32 v49, v49\n"
    "v_mfma_f32_16x16x32_bf16 v[56:59], v[188:191], a[224:227], v[56:59]\n"
    "buffer_load_dwordx4 v1, s[16:19], s83 offen offset:2048 sc1 lds\n"
    "v_mfma_f32_16x16x32_bf16 v[60:63], v[196:199], v[124:127], v[60:63]\n"
    "v_exp_f32 v51, v51\n"
    "v_mfma_f32_16x16x32_bf16 v[64:67], v[188:191], v[140:143], v[64:67]\n"
    "s_waitcnt lgkmcnt(2)\n"
    "buffer_load_dwordx4 v1, s[16:19], s83 offen offset:3072 sc1 lds\n"
    "v_mfma_f32_16x16x32_bf16 v[8:11], v[176:179], a[236:239], v[100:103]\n"
    "s_add_u32 m0, s64, 4096\n"
    "v_exp_f32 v52, v52\n"
    "v_mfma_f32_16x16x32_bf16 v[56:59], v[184:187], a[220:223], v[56:59]\n"
    "s_add_u32 s83, s67, 28672\n"
    "ds_read_b128 v[196:199], v3 offset:9216\n"
    "ds_read_b128 v[104:107], v250 offset:784\n"
    "v_mfma_f32_16x16x32_bf16 v[64:67], v[184:187], v[136:139], v[64:67]\n"
    "s_waitcnt lgkmcnt(2)\n"
    "buffer_load_dwordx4 v1, s[16:19], s83 offen offset:0 sc1 lds\n"
    "v_mfma_f32_16x16x32_bf16 v[12:15], v[156:159], a[240:243], v[100:103]\n"
    ".Lwp0:\n"
    "s_cbranch_execz .Lw3\n"
    "v_exp_f32 v37, v37\n"
    "v_mfma_f32_16x16x32_bf16 v[8:11], v[172:175], a[232:235], v[8:11]\n"
    "v_exp_f32 v50, v50\n"
    "v_mfma_f32_16x16x32_bf16 v[16:19], v[176:179], v[152:155], v[100:103]\n"
    "buffer_load_dwordx4 v1, s[16:19], s83 offen offset:1024 sc1 lds\n"
    "v_mfma_f32_16x16x32_bf16 v[64:67], v[180:183], v[132:135], v[64:67]\n"
    "ds_read_b128 v[232:235], v251 offset:0\n"
    "v_cvt_pk_bf16_f32 v96, v48, v49\n"
    "v_mfma_f32_16x16x32_bf16 v[60:63], v[200:203], v[128:131], v[60:63]\n"
    "ds_read_b128 v[184:187], v3 offset:3072\n"
    "ds_read_b128 v[188:191], v3 offset:5120\n"
    "v_mfma_f32_16x16x32_bf16 v[56:59], v[180:183], a[216:219], v[56:59]\n"
    "ds_read_b128 v[200:203], v3 offset:11264\n"
    "ds_read_b128 v[180:183], v3 offset:1024\n"
    "v_mfma_f32_16x16x32_bf16 v[8:11], v[168:171], a[228:231], v[8:11]\n"
    "buffer_load_dwordx2 v[236:237], v241, s[36:39], 0 offen offset:0 sc1\n"
    "v_mfma_f32_16x16x32_bf16 v[4:7], v[156:159], a[192:195], v[100:103]\n"
    "v_exp_f32 v65, v65\n"
    "v_mfma_f32_16x16x32_bf16 v[8:11], v[164:167], a[224:227], v[8:11]\n"
    "ds_read_b128 v[192:195], v3 offset:7168\n"
    "v_exp_f32 v54, v54\n"
    "v_mfma_f32_16x16x32_bf16 v[8:11], v[160:163], a[220:223], v[8:11]\n"
    "v_exp_f32 v53, v53\n"
    "v_mfma_f32_16x16x32_bf16 v[8:11], v[156:159], a[216:219], v[8:11]\n"
    "v_exp_f32 v62, v62\n"
    "v_mfma_f32_16x16x32_bf16 v[4:7], v[160:163], a[196:199], v[4:7]\n"
    "v_exp_f32 v66, v66\n"
    "v_mfma_f32_16x16x32_bf16 v[12:15], v[160:163], a[244:247], v[12:15]\n"
    "ds_read_b128 v[100:103], v250 offset:896\n"
    "v_and_b32 v224, 0x70, v224\n"
    "v_mfma_f32_16x16x32_bf16 v[16:19], v[172:175], v[148:151], v[16:19]\n"
    "v_exp_f32 v67, v67\n"
    "v_mfma_f32_16x16x32_bf16 v[4:7], v[164:167], a[200:203], v[4:7]\n"
    "buffer_store_dwordx2 v[238:239], v244, s[36:39], s82 offen offset:0 sc1\n"
    "v_exp_f32 v63, v63\n"
    "v_mfma_f32_16x16x32_bf16 v[4:7], v[168:171], a[204:207], v[4:7]\n"
    "v_exp_f32 v9, v9\n"
    "v_mfma_f32_16x16x32_bf16 v[12:15], v[164:167], a[248:251], v[12:15]\n"
    "v_exp_f32 v57, v57\n"
    "v_mfma_f32_16x16x32_bf16 v[12:15], v[168:171], a[252:255], v[12:15]\n"
    "v_exp_f32 v11, v11\n"
    "v_mfma_f32_16x16x32_bf16 v[4:7], v[172:175], a[208:211], v[4:7]\n"
    "v_exp_f32 v58, v58\n"
    "v_mfma_f32_16x16x32_bf16 v[16:19], v[168:171], v[144:147], v[16:19]\n"
    "v_exp_f32 v64, v64\n"
    "v_mfma_f32_16x16x32_bf16 v[12:15], v[172:175], v[124:127], v[12:15]\n"
    "v_exp_f32 v55, v55\n"
    "v_mfma_f32_16x16x32_bf16 v[4:7], v[176:179], a[212:215], v[4:7]\n"
    "v_mad_u32_u24 v244, v224, 24, v2\n"
    "v_xor_b32 v2, 64, v252\n"
    "v_mfma_f32_16x16x32_bf16 v[16:19], v[164:167], v[140:143], v[16:19]\n"
    "s_waitcnt lgkmcnt(3)\n"
    "v_exp_f32 v10, v10\n"
    "v_mfma_f32_16x16x32_bf16 v[24:27], v[200:203], a[236:239], v[104:107]\n"
    "v_exp_f32 v60, v60\n"
    "v_mfma_f32_16x16x32_bf16 v[24:27], v[196:199], a[232:235], v[24:27]\n"
    "s_waitcnt lgkmcnt(2)\n"
    "v_exp_f32 v8, v8\n"
    "v_mfma_f32_16x16x32_bf16 v[28:31], v[180:183], a[240:243], v[104:107]\n"
    "v_exp_f32 v4, v4\n"
    "v_mfma_f32_16x16x32_bf16 v[28:31], v[184:187], a[244:247], v[28:31]\n"
    "v_xor_b32 v224, v224, v1\n"
    "v_cvt_pk_bf16_f32 v81, v42, v43\n"
    "v_mfma_f32_16x16x32_bf16 v[20:23], v[180:183], a[192:195], v[104:107]\n"
    "v_cvt_pk_bf16_f32 v212, v8, v9\n"
    "v_cvt_pk_bf16_f32 v213, v10, v11\n"
    "v_mfma_f32_16x16x32_bf16 v[16:19], v[160:163], v[136:139], v[16:19]\n"
    "v_exp_f32 v6, v6\n"
    "v_mfma_f32_16x16x32_bf16 v[16:19], v[156:159], v[132:135], v[16:19]\n"
    "s_waitcnt lgkmcnt(1)\n"
    "v_exp_f32 v5, v5\n"
    "v_mfma_f32_16x16x32_bf16 v[24:27], v[192:195], a[228:231], v[24:27]\n"
    "v_exp_f32 v61, v61\n"
    "v_mfma_f32_16x16x32_bf16 v[24:27], v[188:191], a[224:227], v[24:27]\n"
    "v_exp_f32 v7, v7\n"
    "v_mfma_f32_16x16x32_bf16 v[12:15], v[176:179], v[128:131], v[12:15]\n"
    "ds_read_b128 v[156:159], v3 offset:12288\n"
    "v_cvt_pk_bf16_f32 v97, v50, v51\n"
    "v_mfma_f32_16x16x32_bf16 v[28:31], v[188:191], a[248:251], v[28:31]\n"
    "v_exp_f32 v59, v59\n"
    "v_mfma_f32_16x16x32_bf16 v[32:35], v[200:203], v[152:155], v[104:107]\n"
    "v_cvt_pk_bf16_f32 v72, v36, v37\n"
    "v_cvt_pk_bf16_f32 v205, v6, v7\n"
    "v_mfma_f32_16x16x32_bf16 v[28:31], v[192:195], a[252:255], v[28:31]\n"
    "ds_read_b128 v[164:167], v3 offset:16384\n"
    "v_cvt_pk_bf16_f32 v204, v4, v5\n"
    "v_mfma_f32_16x16x32_bf16 v[20:23], v[184:187], a[196:199], v[20:23]\n"
    "v_exp_f32 v15, v15\n"
    "v_mfma_f32_16x16x32_bf16 v[28:31], v[196:199], v[124:127], v[28:31]\n"
    "v_exp_f32 v16, v16\n"
    "v_mfma_f32_16x16x32_bf16 v[24:27], v[184:187], a[220:223], v[24:27]\n"
    "v_exp_f32 v17, v17\n"
    "v_mfma_f32_16x16x32_bf16 v[32:35], v[196:199], v[148:151], v[32:35]\n"
    "ds_read_b128 v[4:7], v248 offset:0\n"
    "ds_read_b128 v[172:175], v3 offset:20480\n"
    "v_mfma_f32_16x16x32_bf16 v[24:27], v[180:183], a[216:219], v[24:27]\n"
    "v_cvt_pk_bf16_f32 v74, v52, v53\n"
    "v_cvt_pk_bf16_f32 v90, v60, v61\n"
    "v_mfma_f32_16x16x32_bf16 v[32:35], v[192:195], v[144:147], v[32:35]\n"
    "ds_read_b128 v[168:171], v3 offset:18432\n"
    "v_cvt_pk_bf16_f32 v99, v66, v67\n"
    "v_mfma_f32_16x16x32_bf16 v[20:23], v[188:191], a[200:203], v[20:23]\n"
    "ds_read_b128 v[176:179], v3 offset:22528\n"
    "v_cvt_pk_bf16_f32 v75, v54, v55\n"
    "v_mfma_f32_16x16x32_bf16 v[28:31], v[200:203], v[128:131], v[28:31]\n"
    "ds_read_b128 v[8:11], v248 offset:12288\n"
    "ds_read_b128 v[160:163], v3 offset:14336\n"
    "v_mfma_f32_16x16x32_bf16 v[20:23], v[192:195], a[204:207], v[20:23]\n"
    "ds_read_b128 v[104:107], v250 offset:912\n"
    "ds_read_b128 v[192:195], v3 offset:19456\n"
    "v_mfma_f32_16x16x32_bf16 v[20:23], v[196:199], a[208:211], v[20:23]\n"
    "v_exp_f32 v14, v14\n"
    "v_mfma_f32_16x16x32_bf16 v[32:35], v[188:191], v[140:143], v[32:35]\n"
    "v_exp_f32 v25, v25\n"
    "v_mfma_f32_16x16x32_bf16 v[20:23], v[200:203], a[212:215], v[20:23]\n"
    "v_exp_f32 v30, v30\n"
    "v_mfma_f32_16x16x32_bf16 v[32:35], v[184:187], v[136:139], v[32:35]\n"
    "s_waitcnt lgkmcnt(7)\n"
    "v_exp_f32 v26, v26\n"
    "v_mfma_f32_16x16x32_bf16 a[0:3], v[4:7], v[68:71], a[0:3]\n"
    "v_exp_f32 v28, v28\n"
    "v_mfma_f32_16x16x32_bf16 v[32:35], v[180:183], v[132:135], v[32:35]\n"
    "s_waitcnt lgkmcnt(4)\n"
    "ds_read_b128 v[180:183], v3 offset:13312\n"
    "ds_read_b128 v[200:203], v3 offset:23552\n"
    "v_mfma_f32_16x16x32_bf16 v[40:43], v[176:179], a[236:239], v[100:103]\n"
    "ds_read_b128 v[184:187], v3 offset:15360\n"
    "v_cvt_pk_bf16_f32 v83, v58, v59\n"
    "v_mfma_f32_16x16x32_bf16 v[44:47], v[156:159], a[240:243], v[100:103]\n"
    "v_exp_f32 v20, v20\n"
    "v_mfma_f32_16x16x32_bf16 v[48:51], v[176:179], v[152:155], v[100:103]\n"
    "v_exp_f32 v12, v12\n"
    "v_mfma_f32_16x16x32_bf16 v[40:43], v[172:175], a[232:235], v[40:43]\n"
    "ds_read_b128 v[196:199], v3 offset:21504\n"
    "ds_read_b128 v[188:191], v3 offset:17408\n"
    "v_mfma_f32_16x16x32_bf16 v[36:39], v[156:159], a[192:195], v[100:103]\n"
    "v_exp_f32 v35, v35\n"
    "v_mfma_f32_16x16x32_bf16 v[40:43], v[168:171], a[228:231], v[40:43]\n"
    "s_waitcnt lgkmcnt(7)\n"
    "v_exp_f32 v56, v56\n"
    "v_mfma_f32_16x16x32_bf16 v[36:39], v[160:163], a[196:199], v[36:39]\n"
    "v_exp_f32 v29, v29\n"
    "v_mfma_f32_16x16x32_bf16 v[44:47], v[160:163], a[244:247], v[44:47]\n"
    "s_waitcnt lgkmcnt(4)\n"
    "v_exp_f32 v33, v33\n"
    "v_mfma_f32_16x16x32_bf16 v[52:55], v[180:183], a[192:195], v[104:107]\n"
    "s_waitcnt lgkmcnt(2)\n"
    "v_exp_f32 v13, v13\n"
    "v_mfma_f32_16x16x32_bf16 v[52:55], v[184:187], a[196:199], v[52:55]\n"
    "v_exp_f32 v34, v34\n"
    "v_mfma_f32_16x16x32_bf16 v[40:43], v[164:167], a[224:227], v[40:43]\n"
    "v_exp_f32 v27, v27\n"
    "v_mfma_f32_16x16x32_bf16 v[36:39], v[164:167], a[200:203], v[36:39]\n"
    "v_exp_f32 v31, v31\n"
    "v_mfma_f32_16x16x32_bf16 v[40:43], v[160:163], a[220:223], v[40:43]\n"
    "s_waitcnt lgkmcnt(0)\n"
    "v_cvt_pk_bf16_f32 v82, v56, v57\n"
    "v_cvt_pk_bf16_f32 v231, v34, v35\n"
    "v_mfma_f32_16x16x32_bf16 v[52:55], v[188:191], a[200:203], v[52:55]\n"
    "v_cvt_pk_bf16_f32 v221, v14, v15\n"
    "v_cvt_pk_bf16_f32 v91, v62, v63\n"
    "v_mfma_f32_16x16x32_bf16 v[36:39], v[168:171], a[204:207], v[36:39]\n"
    "v_exp_f32 v21, v21\n"
    "v_mfma_f32_16x16x32_bf16 v[40:43], v[156:159], a[216:219], v[40:43]\n"
    "v_exp_f32 v23, v23\n"
    "v_mfma_f32_16x16x32_bf16 v[52:55], v[192:195], a[204:207], v[52:55]\n"
    "v_cvt_pk_bf16_f32 v223, v30, v31\n"
    "v_cvt_pk_bf16_f32 v98, v64, v65\n"
    "v_mfma_f32_16x16x32_bf16 v[64:67], v[200:203], v[152:155], v[104:107]\n"
    "v_cvt_pk_bf16_f32 v228, v16, v17\n"
    "v_cvt_pk_bf16_f32 v206, v20, v21\n"
    "v_mfma_f32_16x16x32_bf16 v[52:55], v[196:199], a[208:211], v[52:55]\n"
    "v_exp_f32 v24, v24\n"
    "v_mfma_f32_16x16x32_bf16 v[52:55], v[200:203], a[212:215], v[52:55]\n"
    "v_exp_f32 v43, v43\n"
    "v_mfma_f32_16x16x32_bf16 v[44:47], v[164:167], a[248:251], v[44:47]\n"
    "v_exp_f32 v32, v32\n"
    "v_mfma_f32_16x16x32_bf16 v[36:39], v[172:175], a[208:211], v[36:39]\n"
    "v_exp_f32 v40, v40\n"
    "v_mfma_f32_16x16x32_bf16 v[44:47], v[168:171], a[252:255], v[44:47]\n"
    "v_cvt_pk_bf16_f32 v222, v28, v29\n"
    "v_cvt_pk_bf16_f32 v230, v32, v33\n"
    "v_mfma_f32_16x16x32_bf16 v[56:59], v[200:203], a[236:239], v[104:107]\n"
    "s_mov_b32 s79, 133\n"
    "v_exp_f32 v19, v19\n"
    "v_mfma_f32_16x16x32_bf16 a[4:7], v[4:7], v[76:79], a[4:7]\n"
    "v_add_u32 v3, s72, v224\n"
    "v_exp_f32 v18, v18\n"
    "v_mfma_f32_16x16x32_bf16 v[56:59], v[196:199], a[232:235], v[56:59]\n"
    "ds_read_b128 v[32:35], v248 offset:15360\n"
    "v_cvt_pk_bf16_f32 v214, v24, v25\n"
    "v_mfma_f32_16x16x32_bf16 v[36:39], v[176:179], a[212:215], v[36:39]\n"
    "s_waitcnt vmcnt(0)\n"
    "ds_add_u32 v247, v246\n"
    "v_mfma_f32_16x16x32_bf16 v[56:59], v[192:195], a[228:231], v[56:59]\n"
    "v_exp_f32 v53, v53\n"
    "v_mfma_f32_16x16x32_bf16 a[0:3], v[8:11], v[72:75], a[0:3]\n"
    "v_exp_f32 v55, v55\n"
    "v_mfma_f32_16x16x32_bf16 v[60:63], v[180:183], a[240:243], v[104:107]\n"
    "v_exp_f32 v22, v22\n"
    "v_mfma_f32_16x16x32_bf16 v[64:67], v[196:199], v[148:151], v[64:67]\n"
    "v_exp_f32 v52, v52\n"
    "v_smfmac_f32_16x16x64_bf16 v[112:115], v[232:235], v[76:83], v253\n"
    "v_cvt_pk_bf16_f32 v220, v12, v13\n"
    "v_mfma_f32_16x16x32_bf16 v[56:59], v[188:191], a[224:227], v[56:59]\n"
    "s_mov_b32 s73, 0x7f800000\n"
    "v_exp_f32 v54, v54\n"
    "v_mfma_f32_16x16x32_bf16 v[60:63], v[184:187], a[244:247], v[60:63]\n"
    "v_exp_f32 v41, v41\n"
    "v_exp_f32 v37, v37\n"
    "v_smfmac_f32_16x16x64_bf16 v[108:111], v[232:235], v[68:75], v253\n"
    "s_waitcnt lgkmcnt(1)\n"
    "ds_read_b128 v[240:243], v251 offset:768\n"
    "v_mfma_f32_16x16x32_bf16 a[52:55], v[32:35], v[80:83], a[52:55]\n"
    "v_cvt_pk_bf16_f32 v211, v54, v55\n"
    "v_cvt_pk_bf16_f32 v207, v22, v23\n"
    "v_mfma_f32_16x16x32_bf16 v[56:59], v[184:187], a[220:223], v[56:59]\n"
    "ds_read_b128 v[28:31], v248 offset:3072\n"
    "v_cvt_pk_bf16_f32 v229, v18, v19\n"
    "v_mfma_f32_16x16x32_bf16 v[56:59], v[180:183], a[216:219], v[56:59]\n"
    "v_cvt_pk_bf16_f32 v215, v26, v27\n"
    "v_exp_f32 v42, v42\n"
    "v_mfma_f32_16x16x32_bf16 v[60:63], v[188:191], a[248:251], v[60:63]\n"
    "ds_read_b32 v245, v247\n"
    "ds_read_b128 v[12:15], v248 offset:1024\n"
    "v_cvt_pk_bf16_f32 v210, v52, v53\n"
    "v_mfma_f32_16x16x32_bf16 v[60:63], v[192:195], a[252:255], v[60:63]\n"
    "s_nop 0\n"
    "v_exp_f32 v58, v58\n"
    "v_mfma_f32_16x16x32_bf16 v[64:67], v[192:195], v[144:147], v[64:67]\n"
    "v_exp_f32 v36, v36\n"
    "v_mfma_f32_16x16x32_bf16 v[64:67], v[188:191], v[140:143], v[64:67]\n"
    "ds_read_b128 v[20:23], v248 offset:2048\n"
    "ds_read_b128 v[16:19], v248 offset:13312\n"
    "v_mfma_f32_16x16x32_bf16 v[60:63], v[196:199], v[124:127], v[60:63]\n"
    "v_exp_f32 v39, v39\n"
    "v_mfma_f32_16x16x32_bf16 v[60:63], v[200:203], v[128:131], v[60:63]\n"
    "v_exp_f32 v38, v38\n"
    "v_mfma_f32_16x16x32_bf16 v[64:67], v[184:187], v[136:139], v[64:67]\n"
    "v_cvt_pk_bf16_f32 v208, v36, v37\n"
    "v_cvt_pk_bf16_f32 v216, v40, v41\n"
    "v_mfma_f32_16x16x32_bf16 a[4:7], v[8:11], v[80:83], a[4:7]\n"
    "s_waitcnt lgkmcnt(3)\n"
    "v_cvt_pk_bf16_f32 v209, v38, v39\n"
    "v_cvt_pk_bf16_f32 v217, v42, v43\n"
    "v_cmp_le_u32 s[56:57], s59, v245\n"
    "v_mfma_f32_16x16x32_bf16 v[44:47], v[172:175], v[124:127], v[44:47]\n"
    "v_exp_f32 v57, v57\n"
    "v_mfma_f32_16x16x32_bf16 a[52:55], v[28:31], v[76:79], a[52:55]\n"
    "s_cmp_eq_u64 s[56:57], -1\n"
    "v_exp_f32 v62, v62\n"
    "v_smfmac_f32_16x16x64_bf16 v[108:111], v[240:243], v[204:211], v253\n"
    "s_cbranch_scc0 .Lpt1\n"
    ".Lpt1r:\n"
    "v_mfma_f32_16x16x32_bf16 v[48:51], v[172:175], v[148:151], v[48:51]\n"
    "s_waitcnt lgkmcnt(1)\n"
    "ds_read_b128 v[24:27], v248 offset:14336\n"
    "ds_read_b128 a[216:219], v249 offset:3072\n"
    "v_mfma_f32_16x16x32_bf16 a[32:35], v[20:23], v[68:71], a[32:35]\n"
    "v_exp_f32 v59, v59\n"
    "v_mfma_f32_16x16x32_bf16 v[64:67], v[180:183], v[132:135], v[64:67]\n"
    "ds_read_b128 a[212:215], v249 offset:14336\n"
    "ds_read_b128 a[192:195], v249 offset:0\n"
    "v_mfma_f32_16x16x32_bf16 v[44:47], v[176:179], v[128:131], v[44:47]\n"
    "ds_read_b128 a[208:211], v249 offset:2048\n"
    "v_mov_b32 v111, v110\n"
    "v_mfma_f32_16x16x32_bf16 v[48:51], v[168:171], v[144:147], v[48:51]\n"
    "ds_read_b128 a[200:203], v249 offset:1024\n"
    "v_exp_f32 v56, v56\n"
    "v_smfmac_f32_16x16x64_bf16 v[116:119], v[232:235], v[84:91], v253\n"
    "s_waitcnt lgkmcnt(6)\n"
    "v_add_f32 v108, v108, v109\n"
    "v_mfma_f32_16x16x32_bf16 a[16:19], v[16:19], v[72:75], a[16:19]\n"
    "ds_read_b128 a[196:199], v249 offset:12288\n"
    "ds_read_b128 a[220:223], v249 offset:15360\n"
    "v_mfma_f32_16x16x32_bf16 a[48:51], v[32:35], v[72:75], a[48:51]\n"
    "s_waitcnt lgkmcnt(7)\n"
    "v_cvt_pk_bf16_f32 v219, v58, v59\n"
    "v_cvt_pk_bf16_f32 v218, v56, v57\n"
    "v_mov_b32 v109, v108\n"
    "v_mfma_f32_16x16x32_bf16 a[32:35], v[24:27], v[72:75], a[32:35]\n"
    "ds_read_b128 a[204:207], v249 offset:13312\n"
    "v_exp_f32 v47, v47\n"
    "v_smfmac_f32_16x16x64_bf16 v[112:115], v[240:243], v[212:219], v253\n"
    "s_waitcnt lgkmcnt(5)\n"
    "v_exp_f32 v46, v46\n"
    "v_mfma_f32_16x16x32_bf16 a[0:3], a[192:195], v[204:207], a[0:3]\n"
    "v_exp_f32 v67, v67\n"
    "v_mfma_f32_16x16x32_bf16 a[36:39], v[20:23], v[76:79], a[36:39]\n"
    "v_exp_f32 v61, v61\n"
    "v_mfma_f32_16x16x32_bf16 a[48:51], v[28:31], v[68:71], a[48:51]\n"
    "s_waitcnt lgkmcnt(4)\n"
    "v_exp_f32 v45, v45\n"
    "v_mfma_f32_16x16x32_bf16 a[32:35], a[208:211], v[204:207], a[32:35]\n"
    "s_waitcnt lgkmcnt(2)\n"
    "v_exp_f32 v63, v63\n"
    "v_mfma_f32_16x16x32_bf16 a[0:3], a[196:199], v[208:211], a[0:3]\n"
    "v_permlane16_swap_b32 v110, v111\n"
    "v_mfma_f32_16x16x32_bf16 a[16:19], v[12:15], v[68:71], a[16:19]\n"
    "s_waitcnt lgkmcnt(1)\n"
    "v_exp_f32 v44, v44\n"
    "v_mfma_f32_16x16x32_bf16 a[48:51], a[220:223], v[208:211], a[48:51]\n"
    "s_waitcnt lgkmcnt(0)\n"
    "v_add_f32 v112, v112, v113\n"
    "v_mov_b32 v115, v114\n"
    "v_mfma_f32_16x16x32_bf16 a[16:19], a[204:207], v[208:211], a[16:19]\n"
    "v_permlane16_swap_b32 v108, v109\n"
    "v_mfma_f32_16x16x32_bf16 a[32:35], a[212:215], v[208:211], a[32:35]\n"
    "v_accvgpr_read_b32 v36, a0\n"
    "v_add_f32 v110, v110, v111\n"
    "v_mfma_f32_16x16x32_bf16 a[36:39], v[24:27], v[80:83], a[36:39]\n"
    "v_permlane16_swap_b32 v114, v115\n"
    "v_mfma_f32_16x16x32_bf16 a[48:51], a[216:219], v[204:207], a[48:51]\n"
    "v_accvgpr_read_b32 v52, a2\n"
    "v_add_f32 v108, v108, v109\n"
    "v_mfma_f32_16x16x32_bf16 a[16:19], a[200:203], v[204:207], a[16:19]\n"
    "v_accvgpr_read_b32 v40, a1\n"
    "v_rcp_f32 v109, v108\n"
    "v_mfma_f32_16x16x32_bf16 a[52:55], a[220:223], v[216:219], a[52:55]\n"
    "v_exp_f32 v66, v66\n"
    "v_mfma_f32_16x16x32_bf16 a[52:55], a[216:219], v[212:215], a[52:55]\n"
    "v_accvgpr_read_b32 v54, a34\n"
    "v_accvgpr_read_b32 v38, a32\n"
    "v_accvgpr_read_b32 v43, a49\n"
    "v_mfma_f32_16x16x32_bf16 v[48:51], v[164:167], v[140:143], v[48:51]\n"
    "v_exp_f32 v64, v64\n"
    "v_accvgpr_read_b32 v53, a18\n"
    "v_mfma_f32_16x16x32_bf16 a[20:23], v[16:19], v[80:83], a[20:23]\n"
    "v_accvgpr_read_b32 v59, a51\n"
    "v_mov_b32 v113, v112\n"
    "v_mfma_f32_16x16x32_bf16 a[4:7], a[192:195], v[212:215], a[4:7]\n"
    "v_add_f32 v114, v114, v115\n"
    "v_accvgpr_read_b32 v58, a35\n"
    "v_mul_f32 v111, -v110, v109\n"
    "v_mfma_f32_16x16x32_bf16 a[4:7], a[196:199], v[216:219], a[4:7]\n"
    "v_accvgpr_read_b32 v55, a50\n"
    "v_accvgpr_read_b32 v56, a3\n"
    "v_mfma_f32_16x16x32_bf16 a[36:39], a[208:211], v[212:215], a[36:39]\n"
    "v_permlane16_swap_b32 v112, v113\n"
    "v_mfma_f32_16x16x32_bf16 v[48:51], v[160:163], v[136:139], v[48:51]\n"
    "v_accvgpr_read_b32 v41, a17\n"
    "v_accvgpr_read_b32 v42, a33\n"
    "v_mfma_f32_16x16x32_bf16 a[36:39], a[212:215], v[216:219], a[36:39]\n"
    "v_accvgpr_read_b32 v37, a16\n"
    "v_exp_f32 v60, v60\n"
    "v_mfma_f32_16x16x32_bf16 v[48:51], v[156:159], v[132:135], v[48:51]\n"
    "v_ldexp_f32 v109, v109, s79\n"
    "v_add_f32 v112, v112, v113\n"
    "v_accvgpr_read_b32 v57, a19\n"
    "v_cvt_pk_bf16_f32 v224, v44, v45\n"
    "v_cvt_pk_bf16_f32 v226, v60, v61\n"
    "v_pk_fma_f32 v[42:43], v[42:43], v[108:109], v[110:111] op_sel:[0,1,1] op_sel_hi:[1,1,1]\n"
    "v_pk_fma_f32 v[40:41], v[40:41], v[108:109], v[110:111] op_sel:[0,1,1] op_sel_hi:[1,1,1]\n"
    "v_pk_fma_f32 v[58:59], v[58:59], v[108:109], v[110:111] op_sel:[0,1,1] op_sel_hi:[1,1,1]\n"
    "v_cvt_pk_bf16_f32 v227, v62, v63\n"
    "v_pk_fma_f32 v[56:57], v[56:57], v[108:109], v[110:111] op_sel:[0,1,1] op_sel_hi:[1,1,1]\n"
    "v_accvgpr_read_b32 v39, a48\n"
    "v_cvt_pk_bf16_f32 v225, v46, v47\n"
    "v_pk_fma_f32 v[38:39], v[38:39], v[108:109], v[110:111] op_sel:[0,1,1] op_sel_hi:[1,1,1]\n"
    "v_pk_fma_f32 v[54:55], v[54:55], v[108:109], v[110:111] op_sel:[0,1,1] op_sel_hi:[1,1,1]\n"
    "v_pk_fma_f32 v[36:37], v[36:37], v[108:109], v[110:111] op_sel:[0,1,1] op_sel_hi:[1,1,1]\n"
    "v_pk_fma_f32 v[52:53], v[52:53], v[108:109], v[110:111] op_sel:[0,1,1] op_sel_hi:[1,1,1]\n"
    "v_cvt_pk_bf16_f32 v36, v36, v37\n"
    "v_cvt_pk_bf16_f32 v52, v52, v53\n"
    "v_accvgpr_read_b32 v167, a54\n"
    "v_mfma_f32_16x16x32_bf16 a[20:23], v[12:15], v[76:79], a[20:23]\n"
    "v_rcp_f32 v113, v112\n"
    "v_accvgpr_read_b32 v164, a6\n"
    "v_cvt_pk_bf16_f32 v53, v54, v55\n"
    "v_cvt_pk_bf16_f32 v37, v38, v39\n"
    "v_mfma_f32_16x16x32_bf16 a[20:23], a[204:207], v[216:219], a[20:23]\n"
    "v_cvt_pk_bf16_f32 v38, v40, v41\n"
    "v_cvt_pk_bf16_f32 v39, v42, v43\n"
    "v_smfmac_f32_16x16x64_bf16 v[116:119], v[240:243], v[220:227], v253\n"
    "ds_write_b128 v252, v[36:39] offset:0\n"
    "v_mfma_f32_16x16x32_bf16 a[40:43], v[20:23], v[84:87], a[40:43]\n"
    "v_accvgpr_read_b32 v156, a4\n"
    "v_accvgpr_read_b32 v159, a52\n"
    "v_mfma_f32_16x16x32_bf16 a[40:43], v[24:27], v[88:91], a[40:43]\n"
    "v_cvt_pk_bf16_f32 v54, v56, v57\n"
    "v_mul_f32 v115, -v114, v113\n"
    "v_cvt_pk_bf16_f32 v55, v58, v59\n"
    "v_mfma_f32_16x16x32_bf16 a[24:27], v[16:19], v[88:91], a[24:27]\n"
    "ds_write_b128 v2, v[52:55] offset:0\n"
    "v_mfma_f32_16x16x32_bf16 a[56:59], v[32:35], v[88:91], a[56:59]\n"
    "v_accvgpr_read_b32 v158, a36\n"
    "v_exp_f32 v65, v65\n"
    "v_accvgpr_read_b32 v160, a5\n"
    "v_mfma_f32_16x16x32_bf16 a[20:23], a[200:203], v[212:215], a[20:23]\n"
    "v_exp_f32 v49, v49\n"
    "v_mfma_f32_16x16x32_bf16 a[8:11], v[4:7], v[84:87], a[8:11]\n"
    "ds_read_b128 v[56:59], v3 offset:1024\n"
    "v_add_f32 v116, v116, v117\n"
    "v_mfma_f32_16x16x32_bf16 a[40:43], a[208:211], v[220:223], a[40:43]\n"
    "ds_read_b128 v[40:43], v3 offset:0\n"
    "v_accvgpr_read_b32 v166, a38\n"
    "v_mfma_f32_16x16x32_bf16 a[56:59], v[28:31], v[84:87], a[56:59]\n"
    "v_accvgpr_read_b32 v171, a55\n"
    "v_mov_b32 v119, v118\n"
    "v_mfma_f32_16x16x32_bf16 a[8:11], v[8:11], v[88:91], a[8:11]\n"
    "v_mov_b32 v117, v116\n"
    "v_accvgpr_read_b32 v163, a53\n"
    "v_smfmac_f32_16x16x64_bf16 v[120:123], v[232:235], v[92:99], v253\n"
    "v_accvgpr_read_b32 v168, a7\n"
    "v_mfma_f32_16x16x32_bf16 a[24:27], v[12:15], v[84:87], a[24:27]\n"
    "v_accvgpr_read_b32 v161, a21\n"
    "v_exp_f32 v51, v51\n"
    "v_accvgpr_read_b32 v165, a22\n"
    "v_mfma_f32_16x16x32_bf16 a[56:59], a[220:223], v[224:227], a[56:59]\n"
    "v_accvgpr_read_b32 v170, a39\n"
    "v_accvgpr_read_b32 v162, a37\n"
    "v_accvgpr_read_b32 v157, a20\n"
    "v_mfma_f32_16x16x32_bf16 a[56:59], a[216:219], v[220:223], a[56:59]\n"
    "s_waitcnt lgkmcnt(1)\n"
    "buffer_store_dwordx4 v[56:59], v244, s[24:27], s75 offen offset:3072 sc1\n"
    "v_exp_f32 v48, v48\n"
    "v_accvgpr_read_b32 v169, a23\n"
    "v_ldexp_f32 v113, v113, s79\n"
    "v_pk_fma_f32 v[170:171], v[170:171], v[112:113], v[114:115] op_sel:[0,1,1] op_sel_hi:[1,1,1]\n"
    "v_pk_fma_f32 v[162:163], v[162:163], v[112:113], v[114:115] op_sel:[0,1,1] op_sel_hi:[1,1,1]\n"
    "v_permlane16_swap_b32 v118, v119\n"
    "v_pk_fma_f32 v[168:169], v[168:169], v[112:113], v[114:115] op_sel:[0,1,1] op_sel_hi:[1,1,1]\n"
    "v_cvt_pk_bf16_f32 v234, v64, v65\n"
    "v_pk_fma_f32 v[156:157], v[156:157], v[112:113], v[114:115] op_sel:[0,1,1] op_sel_hi:[1,1,1]\n"
    "v_pk_fma_f32 v[166:167], v[166:167], v[112:113], v[114:115] op_sel:[0,1,1] op_sel_hi:[1,1,1]\n"
    "v_pk_fma_f32 v[158:159], v[158:159], v[112:113], v[114:115] op_sel:[0,1,1] op_sel_hi:[1,1,1]\n"
    "v_cvt_pk_bf16_f32 v156, v156, v157\n"
    "v_cvt_pk_bf16_f32 v157, v158, v159\n"
    "v_pk_fma_f32 v[164:165], v[164:165], v[112:113], v[114:115] op_sel:[0,1,1] op_sel_hi:[1,1,1]\n"
    "v_pk_fma_f32 v[160:161], v[160:161], v[112:113], v[114:115] op_sel:[0,1,1] op_sel_hi:[1,1,1]\n"
    "v_cvt_pk_bf16_f32 v158, v160, v161\n"
    "v_accvgpr_read_b32 v187, a59\n"
    "v_mfma_f32_16x16x32_bf16 a[40:43], a[212:215], v[224:227], a[40:43]\n"
    "v_cvt_pk_bf16_f32 v159, v162, v163\n"
    "v_cvt_pk_bf16_f32 v164, v164, v165\n"
    "v_mfma_f32_16x16x32_bf16 a[8:11], a[192:195], v[220:223], a[8:11]\n"
    "v_cvt_pk_bf16_f32 v165, v166, v167\n"
    "v_cvt_pk_bf16_f32 v235, v66, v67\n"
    "v_mfma_f32_16x16x32_bf16 a[24:27], a[204:207], v[224:227], a[24:27]\n"
    "ds_write_b128 v252, v[156:159] offset:2048\n"
    "v_mfma_f32_16x16x32_bf16 a[24:27], a[200:203], v[220:223], a[24:27]\n"
    "v_cvt_pk_bf16_f32 v166, v168, v169\n"
    "v_add_f32 v118, v118, v119\n"
    "v_cvt_pk_bf16_f32 v167, v170, v171\n"
    "v_mfma_f32_16x16x32_bf16 a[8:11], a[196:199], v[224:227], a[8:11]\n"
    "v_cvt_pk_bf16_f32 v232, v48, v49\n"
    "v_accvgpr_read_b32 v178, a41\n"
    "v_mfma_f32_16x16x32_bf16 a[28:31], v[16:19], v[96:99], a[28:31]\n"
    "s_waitcnt lgkmcnt(1)\n"
    "buffer_store_dwordx4 v[40:43], v244, s[24:27], s75 offen offset:0 sc1\n"
    "ds_write_b128 v2, v[164:167] offset:2048\n"
    "v_mfma_f32_16x16x32_bf16 a[12:15], v[4:7], v[92:95], a[12:15]\n"
    "v_accvgpr_read_b32 v181, a26\n"
    "v_exp_f32 v50, v50\n"
    "v_mfma_f32_16x16x32_bf16 a[44:47], v[20:23], v[92:95], a[44:47]\n"
    "v_permlane16_swap_b32 v116, v117\n"
    "v_accvgpr_read_b32 v180, a10\n"
    "v_mfma_f32_16x16x32_bf16 a[12:15], v[8:11], v[96:99], a[12:15]\n"
    "ds_read_b128 v[8:11], v248 offset:16384\n"
    "ds_read_b128 v[160:163], v3 offset:2048\n"
    "ds_read_b128 v[4:7], v248 offset:4096\n"
    "v_accvgpr_read_b32 v176, a9\n"
    "v_accvgpr_read_b32 v182, a42\n"
    "v_mfma_f32_16x16x32_bf16 a[12:15], a[192:195], v[228:231], a[12:15]\n"
    "v_accvgpr_read_b32 v186, a43\n"
    "v_accvgpr_read_b32 v183, a58\n"
    "v_mfma_f32_16x16x32_bf16 a[28:31], v[12:15], v[92:95], a[28:31]\n"
    "v_accvgpr_read_b32 v173, a24\n"
    "v_accvgpr_read_b32 v184, a11\n"
    "v_mfma_f32_16x16x32_bf16 a[60:63], v[32:35], v[96:99], a[60:63]\n"
    "v_accvgpr_read_b32 v172, a8\n"
    "v_cvt_pk_bf16_f32 v233, v50, v51\n"
    "v_add_f32 v116, v116, v117\n"
    "v_rcp_f32 v117, v116\n"
    "v_mfma_f32_16x16x32_bf16 a[12:15], a[196:199], v[232:235], a[12:15]\n"
    "v_accvgpr_read_b32 v175, a56\n"
    "v_mul_f32 v119, -v118, v117\n"
    "v_smfmac_f32_16x16x64_bf16 v[120:123], v[240:243], v[228:235], v253\n"
    "ds_read_b128 v[20:23], v248 offset:6144\n"
    "v_mfma_f32_16x16x32_bf16 a[28:31], a[204:207], v[232:235], a[28:31]\n"
    "v_accvgpr_read_b32 v177, a25\n"
    "v_accvgpr_read_b32 v179, a57\n"
    "v_ldexp_f32 v117, v117, s79\n"
    "v_mfma_f32_16x16x32_bf16 a[60:63], v[28:31], v[92:95], a[60:63]\n"
    "s_waitcnt lgkmcnt(2)\n"
    "ds_read_b128 v[168:171], v3 offset:3072\n"
    "ds_read_b128 v[16:19], v248 offset:17408\n"
    "buffer_store_dwordx4 v[160:163], v244, s[24:27], s76 offen offset:0 sc1\n"
    "ds_read_b128 v[12:15], v248 offset:5120\n"
    "ds_read_b128 a[192:195], v249 offset:4096\n"
    "v_pk_fma_f32 v[186:187], v[186:187], v[116:117], v[118:119] op_sel:[0,1,1] op_sel_hi:[1,1,1]\n"
    "v_pk_fma_f32 v[182:183], v[182:183], v[116:117], v[118:119] op_sel:[0,1,1] op_sel_hi:[1,1,1]\n"
    "v_accvgpr_read_b32 v174, a40\n"
    "v_accvgpr_read_b32 v185, a27\n"
    "v_pk_fma_f32 v[174:175], v[174:175], v[116:117], v[118:119] op_sel:[0,1,1] op_sel_hi:[1,1,1]\n"
    "v_pk_fma_f32 v[180:181], v[180:181], v[116:117], v[118:119] op_sel:[0,1,1] op_sel_hi:[1,1,1]\n"
    "v_pk_fma_f32 v[184:185], v[184:185], v[116:117], v[118:119] op_sel:[0,1,1] op_sel_hi:[1,1,1]\n"
    "v_pk_fma_f32 v[172:173], v[172:173], v[116:117], v[118:119] op_sel:[0,1,1] op_sel_hi:[1,1,1]\n"
    "v_pk_fma_f32 v[176:177], v[176:177], v[116:117], v[118:119] op_sel:[0,1,1] op_sel_hi:[1,1,1]\n"
    "v_pk_fma_f32 v[178:179], v[178:179], v[116:117], v[118:119] op_sel:[0,1,1] op_sel_hi:[1,1,1]\n"
    "v_cvt_pk_bf16_f32 v172, v172, v173\n"
    "v_cvt_pk_bf16_f32 v173, v174, v175\n"
    "v_mfma_f32_16x16x32_bf16 a[44:47], v[24:27], v[96:99], a[44:47]\n"
    "v_cvt_pk_bf16_f32 v180, v180, v181\n"
    "v_cvt_pk_bf16_f32 v174, v176, v177\n"
    "v_cvt_pk_bf16_f32 v175, v178, v179\n"
    "v_add_f32 v120, v120, v121\n"
    "v_mfma_f32_16x16x32_bf16 a[60:63], a[220:223], v[232:235], a[60:63]\n"
    "ds_read_b128 a[204:207], v249 offset:17408\n"
    "ds_read_b128 a[196:199], v249 offset:16384\n"
    "ds_read_b128 v[32:35], v248 offset:19456\n"
    "v_mfma_f32_16x16x32_bf16 a[28:31], a[200:203], v[228:231], a[28:31]\n"
    "s_waitcnt lgkmcnt(6)\n"
    "buffer_store_dwordx4 v[168:171], v244, s[24:27], s76 offen offset:3072 sc1\n"
    "ds_write_b128 v252, v[172:175] offset:4096\n"
    "v_mfma_f32_16x16x32_bf16 a[96:99], v[20:23], v[68:71], a[96:99]\n"
    "v_mov_b32 v123, v122\n"
    "v_mov_b32 v121, v120\n"
    "v_cvt_pk_bf16_f32 v181, v182, v183\n"
    "v_mfma_f32_16x16x32_bf16 a[44:47], a[208:211], v[228:231], a[44:47]\n"
    "v_cvt_pk_bf16_f32 v183, v186, v187\n"
    "v_cvt_pk_bf16_f32 v182, v184, v185\n"
    "v_mfma_f32_16x16x32_bf16 a[44:47], a[212:215], v[232:235], a[44:47]\n"
    "ds_write_b128 v2, v[180:183] offset:4096\n"
    "v_accvgpr_read_b32 v188, a12\n"
    "v_accvgpr_read_b32 v189, a28\n"
    "v_mfma_f32_16x16x32_bf16 a[64:67], v[4:7], v[68:71], a[64:67]\n"
    "v_permlane16_swap_b32 v122, v123\n"
    "v_mfma_f32_16x16x32_bf16 a[60:63], a[216:219], v[228:231], a[60:63]\n"
    "s_waitcnt lgkmcnt(7)\n"
    "ds_read_b128 v[28:31], v248 offset:7168\n"
    "ds_read_b128 v[184:187], v3 offset:5120\n"
    "ds_read_b128 v[176:179], v3 offset:4096\n"
    "ds_read_b128 v[24:27], v248 offset:18432\n"
    "v_mfma_f32_16x16x32_bf16 a[80:83], v[16:19], v[72:75], a[80:83]\n"
    "s_waitcnt lgkmcnt(10)\n"
    "ds_read_b128 a[216:219], v249 offset:7168\n"
    "ds_read_b128 a[200:203], v249 offset:5120\n"
    "v_mfma_f32_16x16x32_bf16 a[80:83], v[12:15], v[68:71], a[80:83]\n"
    "s_waitcnt lgkmcnt(10)\n"
    "ds_read_b128 a[220:223], v249 offset:19456\n"
    "v_accvgpr_read_b32 v199, a62\n"
    "v_accvgpr_read_b32 v190, a44\n"
    "v_accvgpr_read_b32 v192, a13\n"
    "v_mfma_f32_16x16x32_bf16 a[80:83], a[204:207], v[208:211], a[80:83]\n"
    "ds_read_b128 a[208:211], v249 offset:6144\n"
    "v_accvgpr_read_b32 v200, a15\n"
    "v_mfma_f32_16x16x32_bf16 a[64:67], v[8:11], v[72:75], a[64:67]\n"
    "v_accvgpr_read_b32 v203, a63\n"
    "v_accvgpr_read_b32 v198, a46\n"
    "v_mfma_f32_16x16x32_bf16 a[64:67], a[192:195], v[204:207], a[64:67]\n"
    "s_waitcnt lgkmcnt(10)\n"
    "v_permlane16_swap_b32 v120, v121\n"
    "v_add_f32 v120, v120, v121\n"
    "v_mfma_f32_16x16x32_bf16 a[112:115], v[32:35], v[72:75], a[112:115]\n"
    "v_rcp_f32 v121, v120\n"
    "v_mfma_f32_16x16x32_bf16 a[64:67], a[196:199], v[208:211], a[64:67]\n"
    "s_waitcnt lgkmcnt(6)\n"
    "buffer_store_dwordx4 v[184:187], v244, s[24:27], s77 offen offset:3072 sc1\n"
    "v_accvgpr_read_b32 v196, a14\n"
    "v_add_f32 v122, v122, v123\n"
    "v_mul_f32 v123, -v122, v121\n"
    "v_accvgpr_read_b32 v201, a31\n"
    "v_accvgpr_read_b32 v191, a60\n"
    "v_mfma_f32_16x16x32_bf16 a[112:115], v[28:31], v[68:71], a[112:115]\n"
    "s_waitcnt lgkmcnt(2)\n"
    "v_accvgpr_read_b32 v202, a47\n"
    "v_accvgpr_read_b32 v197, a30\n"
    "v_mfma_f32_16x16x32_bf16 a[80:83], a[200:203], v[204:207], a[80:83]\n"
    "s_waitcnt lgkmcnt(1)\n"
    "v_accvgpr_read_b32 v194, a45\n"
    "v_accvgpr_read_b32 v128, a65\n"
    "v_mfma_f32_16x16x32_bf16 a[112:115], a[220:223], v[208:211], a[112:115]\n"
    "buffer_store_dwordx4 v[176:179], v244, s[24:27], s77 offen offset:0 sc1\n"
    "ds_read_b128 a[212:215], v249 offset:18432\n"
    "v_accvgpr_read_b32 v195, a61\n"
    "v_accvgpr_read_b32 v132, a66\n"
    "v_ldexp_f32 v121, v121, s79\n"
    "v_pk_fma_f32 v[190:191], v[190:191], v[120:121], v[122:123] op_sel:[0,1,1] op_sel_hi:[1,1,1]\n"
    "v_pk_fma_f32 v[194:195], v[194:195], v[120:121], v[122:123] op_sel:[0,1,1] op_sel_hi:[1,1,1]\n"
    "v_pk_fma_f32 v[196:197], v[196:197], v[120:121], v[122:123] op_sel:[0,1,1] op_sel_hi:[1,1,1]\n"
    "v_accvgpr_read_b32 v133, a82\n"
    "v_pk_fma_f32 v[200:201], v[200:201], v[120:121], v[122:123] op_sel:[0,1,1] op_sel_hi:[1,1,1]\n"
    "v_pk_fma_f32 v[198:199], v[198:199], v[120:121], v[122:123] op_sel:[0,1,1] op_sel_hi:[1,1,1]\n"
    "v_accvgpr_read_b32 v125, a80\n"
    "v_accvgpr_read_b32 v193, a29\n"
    "v_accvgpr_read_b32 v129, a81\n"
    "v_cvt_pk_bf16_f32 v196, v196, v197\n"
    "v_pk_fma_f32 v[132:133], v[132:133], v[108:109], v[110:111] op_sel:[0,1,1] op_sel_hi:[1,1,1]\n"
    "v_pk_fma_f32 v[202:203], v[202:203], v[120:121], v[122:123] op_sel:[0,1,1] op_sel_hi:[1,1,1]\n"
    "v_pk_fma_f32 v[128:129], v[128:129], v[108:109], v[110:111] op_sel:[0,1,1] op_sel_hi:[1,1,1]\n"
    "v_cvt_pk_bf16_f32 v197, v198, v199\n"
    "v_pk_fma_f32 v[188:189], v[188:189], v[120:121], v[122:123] op_sel:[0,1,1] op_sel_hi:[1,1,1]\n"
    ".Lwp1:\n"
    "s_cbranch_execz .Lw3\n"
    "v_pk_fma_f32 v[192:193], v[192:193], v[120:121], v[122:123] op_sel:[0,1,1] op_sel_hi:[1,1,1]\n"
    "v_cvt_pk_bf16_f32 v199, v202, v203\n"
    "v_cvt_pk_bf16_f32 v198, v200, v201\n"
    "v_mfma_f32_16x16x32_bf16 a[112:115], a[216:219], v[204:207], a[112:115]\n"
    "ds_write_b128 v2, v[196:199] offset:0\n"
    "v_mfma_f32_16x16x32_bf16 a[96:99], v[24:27], v[72:75], a[96:99]\n"
    "s_waitcnt lgkmcnt(2)\n"
    "v_cvt_pk_bf16_f32 v188, v188, v189\n"
    "v_cvt_pk_bf16_f32 v189, v190, v191\n"
    "v_mfma_f32_16x16x32_bf16 a[96:99], a[208:211], v[204:207], a[96:99]\n"
    "s_waitcnt lgkmcnt(1)\n"
    "v_cvt_pk_bf16_f32 v190, v192, v193\n"
    "v_cvt_pk_bf16_f32 v191, v194, v195\n"
    "v_mfma_f32_16x16x32_bf16 a[96:99], a[212:215], v[208:211], a[96:99]\n"
    "ds_write_b128 v252, v[188:191] offset:0\n"
    "v_mfma_f32_16x16x32_bf16 a[116:119], v[32:35], v[80:83], a[116:119]\n"
    "v_cvt_pk_bf16_f32 v132, v132, v133\n"
    "v_accvgpr_read_b32 v135, a114\n"
    "v_mfma_f32_16x16x32_bf16 a[84:87], v[16:19], v[80:83], a[84:87]\n"
    "v_accvgpr_read_b32 v131, a113\n"
    "v_accvgpr_read_b32 v136, a67\n"
    "v_mfma_f32_16x16x32_bf16 a[100:103], v[20:23], v[76:79], a[100:103]\n"
    "ds_read_b128 v[200:203], v3 offset:1024\n"
    "ds_read_b128 v[192:195], v3 offset:0\n"
    "v_mfma_f32_16x16x32_bf16 a[68:71], v[4:7], v[76:79], a[68:71]\n"
    "v_accvgpr_read_b32 v137, a83\n"
    "v_accvgpr_read_b32 v124, a64\n"
    "v_mfma_f32_16x16x32_bf16 a[68:71], v[8:11], v[80:83], a[68:71]\n"
    "v_fma_f32 v124, v124, v109, v111\n"
    "v_fma_f32 v125, v125, v109, v111\n"
    "v_mfma_f32_16x16x32_bf16 a[116:119], v[28:31], v[76:79], a[116:119]\n"
    "v_accvgpr_read_b32 v127, a112\n"
    "v_accvgpr_read_b32 v130, a97\n"
    "v_mfma_f32_16x16x32_bf16 a[100:103], v[24:27], v[80:83], a[100:103]\n"
    "v_fma_f32 v130, v130, v109, v111\n"
    "v_fma_f32 v131, v131, v109, v111\n"
    "v_mfma_f32_16x16x32_bf16 a[84:87], v[12:15], v[76:79], a[84:87]\n"
    "v_cvt_pk_bf16_f32 v124, v124, v125\n"
    "v_accvgpr_read_b32 v126, a96\n"
    "v_mfma_f32_16x16x32_bf16 a[116:119], a[220:223], v[216:219], a[116:119]\n"
    "v_fma_f32 v126, v126, v109, v111\n"
    "v_fma_f32 v127, v127, v109, v111\n"
    "v_mfma_f32_16x16x32_bf16 a[116:119], a[216:219], v[212:215], a[116:119]\n"
    "v_accvgpr_read_b32 v139, a115\n"
    "v_cvt_pk_bf16_f32 v125, v126, v127\n"
    "v_mfma_f32_16x16x32_bf16 a[100:103], a[208:211], v[212:215], a[100:103]\n"
    "s_waitcnt lgkmcnt(0)\n"
    "buffer_store_dwordx4 v[192:195], v244, s[24:27], s78 offen offset:0 sc1\n"
    "v_cvt_pk_bf16_f32 v127, v130, v131\n"
    "v_cvt_pk_bf16_f32 v126, v128, v129\n"
    "v_accvgpr_read_b32 v134, a98\n"
    "v_accvgpr_read_b32 v138, a99\n"
    "v_mfma_f32_16x16x32_bf16 a[84:87], a[204:207], v[216:219], a[84:87]\n"
    "v_fma_f32 v138, v138, v109, v111\n"
    "v_fma_f32 v139, v139, v109, v111\n"
    "v_mfma_f32_16x16x32_bf16 a[84:87], a[200:203], v[212:215], a[84:87]\n"
    "v_fma_f32 v134, v134, v109, v111\n"
    "v_fma_f32 v135, v135, v109, v111\n"
    "v_mfma_f32_16x16x32_bf16 a[68:71], a[192:195], v[212:215], a[68:71]\n"
    "buffer_store_dwordx4 v[200:203], v244, s[24:27], s78 offen offset:3072 sc1\n"
    "ds_write_b128 v252, v[124:127] offset:2048\n"
    "v_mfma_f32_16x16x32_bf16 a[88:91], v[16:19], v[88:91], a[88:91]\n"
    "v_fma_f32 v136, v136, v109, v111\n"
    "v_fma_f32 v137, v137, v109, v111\n"
    "v_mfma_f32_16x16x32_bf16 a[100:103], a[212:215], v[216:219], a[100:103]\n"
    "v_accvgpr_read_b32 v155, a119\n"
    "v_accvgpr_read_b32 v153, a87\n"
    "v_mfma_f32_16x16x32_bf16 a[120:123], v[32:35], v[88:91], a[120:123]\n"
    "v_accvgpr_read_b32 v151, a118\n"
    "v_cvt_pk_bf16_f32 v133, v134, v135\n"
    "v_mfma_f32_16x16x32_bf16 a[68:71], a[196:199], v[216:219], a[68:71]\n"
    "v_cvt_pk_bf16_f32 v135, v138, v139\n"
    "v_cvt_pk_bf16_f32 v134, v136, v137\n"
    "v_mfma_f32_16x16x32_bf16 a[120:123], v[28:31], v[84:87], a[120:123]\n"
    "ds_write_b128 v2, v[132:135] offset:2048\n"
    "v_mfma_f32_16x16x32_bf16 a[72:75], v[4:7], v[84:87], a[72:75]\n"
    "v_accvgpr_read_b32 v154, a103\n"
    "v_accvgpr_read_b32 v142, a100\n"
    "v_mfma_f32_16x16x32_bf16 a[88:91], v[12:15], v[84:87], a[88:91]\n"
    "v_accvgpr_read_b32 v145, a85\n"
    "v_accvgpr_read_b32 v152, a71\n"
    "v_mfma_f32_16x16x32_bf16 a[120:123], a[220:223], v[224:227], a[120:123]\n"
    "ds_read_b128 v[136:139], v3 offset:3072\n"
    "v_accvgpr_read_b32 v146, a101\n"
    "v_mfma_f32_16x16x32_bf16 a[72:75], v[8:11], v[88:91], a[72:75]\n"
    "ds_read_b128 v[128:131], v3 offset:2048\n"
    "v_accvgpr_read_b32 v140, a68\n"
    "v_mfma_f32_16x16x32_bf16 a[120:123], a[216:219], v[220:223], a[120:123]\n"
    "v_accvgpr_read_b32 v143, a116\n"
    "v_accvgpr_read_b32 v141, a84\n"
    "v_mfma_f32_16x16x32_bf16 a[72:75], a[192:195], v[220:223], a[72:75]\n"
    "v_accvgpr_read_b32 v150, a102\n"
    "v_accvgpr_read_b32 v149, a86\n"
    "v_accvgpr_read_b32 v144, a69\n"
    "v_accvgpr_read_b32 v147, a117\n"
    "v_fma_f32 v140, v140, v113, v115\n"
    "v_fma_f32 v141, v141, v113, v115\n"
    "v_pk_fma_f32 v[154:155], v[154:155], v[112:113], v[114:115] op_sel:[0,1,1] op_sel_hi:[1,1,1]\n"
    "v_pk_fma_f32 v[146:147], v[146:147], v[112:113], v[114:115] op_sel:[0,1,1] op_sel_hi:[1,1,1]\n"
    "v_accvgpr_read_b32 v148, a70\n"
    "v_pk_fma_f32 v[142:143], v[142:143], v[112:113], v[114:115] op_sel:[0,1,1] op_sel_hi:[1,1,1]\n"
    "v_pk_fma_f32 v[144:145], v[144:145], v[112:113], v[114:115] op_sel:[0,1,1] op_sel_hi:[1,1,1]\n"
    "v_cvt_pk_bf16_f32 v140, v140, v141\n"
    "v_cvt_pk_bf16_f32 v141, v142, v143\n"
    "v_mfma_f32_16x16x32_bf16 a[104:107], v[20:23], v[84:87], a[104:107]\n"
    "v_cvt_pk_bf16_f32 v142, v144, v145\n"
    "v_cvt_pk_bf16_f32 v143, v146, v147\n"
    "v_mfma_f32_16x16x32_bf16 a[72:75], a[196:199], v[224:227], a[72:75]\n"
    "s_waitcnt lgkmcnt(1)\n"
    "buffer_store_dwordx4 v[136:139], v244, s[24:27], s75 offen offset:3200 sc1\n"
    "ds_write_b128 v252, v[140:143] offset:4096\n"
    "v_pk_fma_f32 v[148:149], v[148:149], v[112:113], v[114:115] op_sel:[0,1,1] op_sel_hi:[1,1,1]\n"
    "v_pk_fma_f32 v[150:151], v[150:151], v[112:113], v[114:115] op_sel:[0,1,1] op_sel_hi:[1,1,1]\n"
    "v_pk_fma_f32 v[152:153], v[152:153], v[112:113], v[114:115] op_sel:[0,1,1] op_sel_hi:[1,1,1]\n"
    "v_cvt_pk_bf16_f32 v148, v148, v149\n"
    "v_cvt_pk_bf16_f32 v149, v150, v151\n"
    "v_mfma_f32_16x16x32_bf16 a[88:91], a[204:207], v[224:227], a[88:91]\n"
    "v_cvt_pk_bf16_f32 v150, v152, v153\n"
    "v_cvt_pk_bf16_f32 v151, v154, v155\n"
    "v_mfma_f32_16x16x32_bf16 a[88:91], a[200:203], v[220:223], a[88:91]\n"
    "s_waitcnt lgkmcnt(1)\n"
    "buffer_store_dwordx4 v[128:131], v244, s[24:27], s75 offen offset:128 sc1\n"
    "ds_write_b128 v2, v[148:151] offset:4096\n"
    "v_mfma_f32_16x16x32_bf16 a[104:107], v[24:27], v[88:91], a[104:107]\n"
    "s_mov_b32 s74, 0x47000000\n"
    "v_accvgpr_read_b32 v47, a120\n"
    "v_accvgpr_read_b32 v51, a121\n"
    "v_mfma_f32_16x16x32_bf16 a[108:111], v[20:23], v[92:95], a[108:111]\n"
    "v_accvgpr_read_b32 v67, a123\n"
    "v_accvgpr_read_b32 v65, a91\n"
    "v_mfma_f32_16x16x32_bf16 a[104:107], a[208:211], v[220:223], a[104:107]\n"
    "ds_read_b128 v[152:155], v3 offset:5120\n"
    "v_accvgpr_read_b32 v63, a122\n"
    "v_mfma_f32_16x16x32_bf16 a[104:107], a[212:215], v[224:227], a[104:107]\n"
    "v_accvgpr_read_b32 v64, a75\n"
    "v_accvgpr_read_b32 v60, a74\n"
    "v_mfma_f32_16x16x32_bf16 a[124:127], v[32:35], v[96:99], a[124:127]\n"
    "ds_read_b128 v[144:147], v3 offset:4096\n"
    "v_accvgpr_read_b32 v44, a72\n"
    "v_mfma_f32_16x16x32_bf16 a[76:79], v[4:7], v[92:95], a[76:79]\n"
    "v_accvgpr_read_b32 v45, a88\n"
    "v_accvgpr_read_b32 v61, a90\n"
    "v_accvgpr_read_b32 v48, a73\n"
    "v_mfma_f32_16x16x32_bf16 a[76:79], v[8:11], v[96:99], a[76:79]\n"
    "ds_read_b128 v[32:35], v248 offset:23552\n"
    "v_accvgpr_read_b32 v46, a104\n"
    "v_mfma_f32_16x16x32_bf16 a[92:95], v[16:19], v[96:99], a[92:95]\n"
    "ds_read_b128 v[16:19], v248 offset:21504\n"
    "ds_read_b128 v[8:11], v248 offset:20480\n"
    "v_mfma_f32_16x16x32_bf16 a[124:127], v[28:31], v[92:95], a[124:127]\n"
    "v_accvgpr_read_b32 v50, a105\n"
    "v_accvgpr_read_b32 v49, a89\n"
    "v_mfma_f32_16x16x32_bf16 a[124:127], a[220:223], v[232:235], a[124:127]\n"
    "s_waitcnt lgkmcnt(4)\n"
    "ds_read_b128 v[4:7], v248 offset:8192\n"
    "buffer_store_dwordx4 v[152:155], v244, s[24:27], s76 offen offset:3200 sc1\n"
    "ds_read_b128 v[20:23], v248 offset:10240\n"
    "v_accvgpr_read_b32 v62, a106\n"
    "v_accvgpr_read_b32 v66, a107\n"
    "v_pk_fma_f32 v[44:45], v[44:45], v[116:117], v[118:119] op_sel:[0,1,1] op_sel_hi:[1,1,1]\n"
    "v_pk_fma_f32 v[60:61], v[60:61], v[116:117], v[118:119] op_sel:[0,1,1] op_sel_hi:[1,1,1]\n"
    "v_pk_fma_f32 v[62:63], v[62:63], v[116:117], v[118:119] op_sel:[0,1,1] op_sel_hi:[1,1,1]\n"
    "v_pk_fma_f32 v[48:49], v[48:49], v[116:117], v[118:119] op_sel:[0,1,1] op_sel_hi:[1,1,1]\n"
    "v_pk_fma_f32 v[46:47], v[46:47], v[116:117], v[118:119] op_sel:[0,1,1] op_sel_hi:[1,1,1]\n"
    "v_pk_fma_f32 v[50:51], v[50:51], v[116:117], v[118:119] op_sel:[0,1,1] op_sel_hi:[1,1,1]\n"
    "v_pk_fma_f32 v[64:65], v[64:65], v[116:117], v[118:119] op_sel:[0,1,1] op_sel_hi:[1,1,1]\n"
    "v_pk_fma_f32 v[66:67], v[66:67], v[116:117], v[118:119] op_sel:[0,1,1] op_sel_hi:[1,1,1]\n"
    "v_cvt_pk_bf16_f32 v44, v44, v45\n"
    "v_cvt_pk_bf16_f32 v45, v46, v47\n"
    "v_mfma_f32_16x16x32_bf16 a[108:111], v[24:27], v[96:99], a[108:111]\n"
    "ds_read_b128 v[24:27], v248 offset:22528\n"
    "ds_read_b128 v[28:31], v248 offset:11264\n"
    "v_mfma_f32_16x16x32_bf16 a[92:95], v[12:15], v[92:95], a[92:95]\n"
    "v_cvt_pk_bf16_f32 v47, v50, v51\n"
    "v_cvt_pk_bf16_f32 v46, v48, v49\n"
    "v_mfma_f32_16x16x32_bf16 a[76:79], a[192:195], v[228:231], a[76:79]\n"
    "s_waitcnt lgkmcnt(7)\n"
    "buffer_store_dwordx4 v[144:147], v244, s[24:27], s76 offen offset:128 sc1\n"
    "ds_write_b128 v252, v[44:47] offset:0\n"
    "v_mfma_f32_16x16x32_bf16 a[76:79], a[196:199], v[232:235], a[76:79]\n"
    "v_cvt_pk_bf16_f32 v60, v60, v61\n"
    "v_cvt_pk_bf16_f32 v61, v62, v63\n"
    "v_mfma_f32_16x16x32_bf16 a[124:127], a[216:219], v[228:231], a[124:127]\n"
    "v_cvt_pk_bf16_f32 v63, v66, v67\n"
    "v_cvt_pk_bf16_f32 v62, v64, v65\n"
    "v_mfma_f32_16x16x32_bf16 a[92:95], a[204:207], v[232:235], a[92:95]\n"
    "ds_read_b128 a[192:195], v249 offset:8192\n"
    "ds_read_b128 a[196:199], v249 offset:20480\n"
    "v_mfma_f32_16x16x32_bf16 a[108:111], a[208:211], v[228:231], a[108:111]\n"
    "ds_write_b128 v2, v[60:63] offset:0\n"
    "v_mfma_f32_16x16x32_bf16 a[92:95], a[200:203], v[228:231], a[92:95]\n"
    "s_waitcnt lgkmcnt(6)\n"
    "v_accvgpr_read_b32 v39, a124\n"
    "v_accvgpr_read_b32 v56, a79\n"
    "v_mfma_f32_16x16x32_bf16 a[160:163], v[20:23], v[68:71], a[160:163]\n"
    "v_accvgpr_read_b32 v59, a127\n"
    "v_accvgpr_read_b32 v52, a78\n"
    "v_mfma_f32_16x16x32_bf16 a[108:111], a[212:215], v[232:235], a[108:111]\n"
    "ds_read_b128 v[48:51], v3 offset:0\n"
    "ds_read_b128 a[200:203], v249 offset:9216\n"
    "v_mfma_f32_16x16x32_bf16 a[128:131], v[4:7], v[68:71], a[128:131]\n"
    "ds_read_b128 v[12:15], v248 offset:9216\n"
    "ds_read_b128 v[64:67], v3 offset:1024\n"
    "v_mfma_f32_16x16x32_bf16 a[128:131], v[8:11], v[72:75], a[128:131]\n"
    "s_waitcnt lgkmcnt(9)\n"
    "v_accvgpr_read_b32 v36, a76\n"
    "v_accvgpr_read_b32 v57, a95\n"
    "v_accvgpr_read_b32 v55, a126\n"
    "v_mfma_f32_16x16x32_bf16 a[160:163], v[24:27], v[72:75], a[160:163]\n"
    "v_accvgpr_read_b32 v40, a77\n"
    "v_accvgpr_read_b32 v54, a110\n"
    "v_mfma_f32_16x16x32_bf16 a[144:147], v[16:19], v[72:75], a[144:147]\n"
    "ds_read_b128 a[220:223], v249 offset:23552\n"
    "ds_read_b128 a[208:211], v249 offset:10240\n"
    "v_mfma_f32_16x16x32_bf16 a[176:179], v[32:35], v[72:75], a[176:179]\n"
    "s_waitcnt lgkmcnt(8)\n"
    "v_accvgpr_read_b32 v58, a111\n"
    "v_accvgpr_read_b32 v41, a93\n"
    "v_mfma_f32_16x16x32_bf16 a[128:131], a[192:195], v[204:207], a[128:131]\n"
    "s_waitcnt lgkmcnt(7)\n"
    "v_accvgpr_read_b32 v37, a92\n"
    "v_accvgpr_read_b32 v43, a125\n"
    "v_accvgpr_read_b32 v53, a94\n"
    "v_mfma_f32_16x16x32_bf16 a[128:131], a[196:199], v[208:211], a[128:131]\n"
    "s_waitcnt lgkmcnt(5)\n"
    "buffer_store_dwordx4 v[48:51], v244, s[24:27], s77 offen offset:128 sc1\n"
    "ds_read_b128 a[204:207], v249 offset:21504\n"
    "ds_read_b128 a[216:219], v249 offset:11264\n"
    "ds_read_b128 a[212:215], v249 offset:22528\n"
    "v_accvgpr_read_b32 v42, a109\n"
    "v_pk_fma_f32 v[36:37], v[36:37], v[120:121], v[122:123] op_sel:[0,1,1] op_sel_hi:[1,1,1]\n"
    "v_pk_fma_f32 v[54:55], v[54:55], v[120:121], v[122:123] op_sel:[0,1,1] op_sel_hi:[1,1,1]\n"
    "v_pk_fma_f32 v[52:53], v[52:53], v[120:121], v[122:123] op_sel:[0,1,1] op_sel_hi:[1,1,1]\n"
    "v_accvgpr_read_b32 v38, a108\n"
    "v_pk_fma_f32 v[40:41], v[40:41], v[120:121], v[122:123] op_sel:[0,1,1] op_sel_hi:[1,1,1]\n"
    "v_pk_fma_f32 v[38:39], v[38:39], v[120:121], v[122:123] op_sel:[0,1,1] op_sel_hi:[1,1,1]\n"
    "v_pk_fma_f32 v[56:57], v[56:57], v[120:121], v[122:123] op_sel:[0,1,1] op_sel_hi:[1,1,1]\n"
    "v_pk_fma_f32 v[58:59], v[58:59], v[120:121], v[122:123] op_sel:[0,1,1] op_sel_hi:[1,1,1]\n"
    "v_pk_fma_f32 v[42:43], v[42:43], v[120:121], v[122:123] op_sel:[0,1,1] op_sel_hi:[1,1,1]\n"
    "v_cvt_pk_bf16_f32 v36, v36, v37\n"
    "v_cvt_pk_bf16_f32 v37, v38, v39\n"
    "v_mfma_f32_16x16x32_bf16 a[176:179], v[28:31], v[68:71], a[176:179]\n"
    "s_waitcnt lgkmcnt(6)\n"
    "v_cvt_pk_bf16_f32 v38, v40, v41\n"
    "v_cvt_pk_bf16_f32 v39, v42, v43\n"
    "v_mfma_f32_16x16x32_bf16 a[144:147], v[12:15], v[68:71], a[144:147]\n"
    "s_waitcnt lgkmcnt(3)\n"
    "buffer_store_dwordx4 v[64:67], v244, s[24:27], s77 offen offset:3200 sc1\n"
    "ds_write_b128 v252, v[36:39] offset:2048\n"
    "v_mfma_f32_16x16x32_bf16 a[160:163], a[208:211], v[204:207], a[160:163]\n"
    "v_cvt_pk_bf16_f32 v52, v52, v53\n"
    "v_cvt_pk_bf16_f32 v53, v54, v55\n"
    "v_mfma_f32_16x16x32_bf16 a[176:179], a[220:223], v[208:211], a[176:179]\n"
    "s_waitcnt lgkmcnt(1)\n"
    "v_cvt_pk_bf16_f32 v54, v56, v57\n"
    "v_cvt_pk_bf16_f32 v55, v58, v59\n"
    "v_mfma_f32_16x16x32_bf16 a[160:163], a[212:215], v[208:211], a[160:163]\n"
    "ds_write_b128 v2, v[52:55] offset:2048\n"
    "v_mfma_f32_16x16x32_bf16 a[176:179], a[216:219], v[204:207], a[176:179]\n"
    "v_accvgpr_read_b32 v156, a128\n"
    "v_mfma_f32_16x16x32_bf16 a[148:151], v[16:19], v[80:83], a[148:151]\n"
    "v_mov_b32 v250, 0\n"
    "v_accvgpr_read_b32 v168, a131\n"
    "v_mfma_f32_16x16x32_bf16 a[144:147], a[204:207], v[208:211], a[144:147]\n"
    "ds_read_b128 v[56:59], v3 offset:3072\n"
    "ds_read_b128 v[40:43], v3 offset:2048\n"
    "v_mfma_f32_16x16x32_bf16 a[144:147], a[200:203], v[204:207], a[144:147]\n"
    "v_accvgpr_read_b32 v158, a160\n"
    "v_accvgpr_read_b32 v162, a161\n"
    "v_mfma_f32_16x16x32_bf16 a[180:183], v[32:35], v[80:83], a[180:183]\n"
    "v_accvgpr_read_b32 v160, a129\n"
    "v_accvgpr_read_b32 v163, a177\n"
    "v_mfma_f32_16x16x32_bf16 a[164:167], v[20:23], v[76:79], a[164:167]\n"
    "v_accvgpr_read_b32 v164, a130\n"
    "v_mfma_f32_16x16x32_bf16 a[132:135], v[4:7], v[76:79], a[132:135]\n"
    "v_accvgpr_read_b32 v169, a147\n"
    "v_accvgpr_read_b32 v166, a162\n"
    "v_mfma_f32_16x16x32_bf16 a[148:151], v[12:15], v[76:79], a[148:151]\n"
    "v_accvgpr_read_b32 v161, a145\n"
    "v_accvgpr_read_b32 v159, a176\n"
    "v_mfma_f32_16x16x32_bf16 a[148:151], a[204:207], v[216:219], a[148:151]\n"
    "v_accvgpr_read_b32 v157, a144\n"
    "v_accvgpr_read_b32 v170, a163\n"
    "v_mfma_f32_16x16x32_bf16 a[148:151], a[200:203], v[212:215], a[148:151]\n"
    "v_accvgpr_read_b32 v167, a178\n"
    "v_accvgpr_read_b32 v171, a179\n"
    "v_mfma_f32_16x16x32_bf16 a[132:135], v[8:11], v[80:83], a[132:135]\n"
    "s_waitcnt lgkmcnt(1)\n"
    "buffer_store_dwordx4 v[56:59], v244, s[24:27], s78 offen offset:3200 sc1\n"
    "v_accvgpr_read_b32 v165, a146\n"
    "v_fma_f32 v164, v164, v109, v111\n"
    "v_fma_f32 v165, v165, v109, v111\n"
    "v_cvt_pk_bf16_f32 v164, v164, v165\n"
    "v_pk_fma_f32 v[166:167], v[166:167], v[108:109], v[110:111] op_sel:[0,1,1] op_sel_hi:[1,1,1]\n"
    "v_cvt_pk_bf16_f32 v165, v166, v167\n"
    "v_pk_fma_f32 v[168:169], v[168:169], v[108:109], v[110:111] op_sel:[0,1,1] op_sel_hi:[1,1,1]\n"
    "v_pk_fma_f32 v[170:171], v[170:171], v[108:109], v[110:111] op_sel:[0,1,1] op_sel_hi:[1,1,1]\n"
    "v_pk_fma_f32 v[160:161], v[160:161], v[108:109], v[110:111] op_sel:[0,1,1] op_sel_hi:[1,1,1]\n"
    "v_pk_fma_f32 v[156:157], v[156:157], v[108:109], v[110:111] op_sel:[0,1,1] op_sel_hi:[1,1,1]\n"
    "v_pk_fma_f32 v[162:163], v[162:163], v[108:109], v[110:111] op_sel:[0,1,1] op_sel_hi:[1,1,1]\n"
    "v_pk_fma_f32 v[158:159], v[158:159], v[108:109], v[110:111] op_sel:[0,1,1] op_sel_hi:[1,1,1]\n"
    "v_cvt_pk_bf16_f32 v166, v168, v169\n"
    "v_cvt_pk_bf16_f32 v167, v170, v171\n"
    "v_mfma_f32_16x16x32_bf16 a[132:135], a[192:195], v[212:215], a[132:135]\n"
    "s_waitcnt lgkmcnt(0)\n"
    "buffer_store_dwordx4 v[40:43], v244, s[24:27], s78 offen offset:128 sc1\n"
    "ds_write_b128 v2, v[164:167] offset:4096\n"
    "v_mfma_f32_16x16x32_bf16 a[132:135], a[196:199], v[216:219], a[132:135]\n"
    "v_cvt_pk_bf16_f32 v156, v156, v157\n"
    "v_cvt_pk_bf16_f32 v157, v158, v159\n"
    "v_mfma_f32_16x16x32_bf16 a[180:183], v[28:31], v[76:79], a[180:183]\n"
    "v_cvt_pk_bf16_f32 v158, v160, v161\n"
    "v_cvt_pk_bf16_f32 v159, v162, v163\n"
    "v_mfma_f32_16x16x32_bf16 a[180:183], a[220:223], v[216:219], a[180:183]\n"
    "ds_write_b128 v252, v[156:159] offset:4096\n"
    "v_mfma_f32_16x16x32_bf16 a[180:183], a[216:219], v[212:215], a[180:183]\n"
    "v_accvgpr_read_b32 v184, a135\n"
    "v_accvgpr_read_b32 v177, a149\n"
    "v_accvgpr_read_b32 v180, a134\n"
    "v_accvgpr_read_b32 v181, a150\n"
    "v_mfma_f32_16x16x32_bf16 a[164:167], v[24:27], v[80:83], a[164:167]\n"
    "ds_read_b128 v[168:171], v3 offset:5120\n"
    "v_accvgpr_read_b32 v173, a148\n"
    "v_mfma_f32_16x16x32_bf16 a[184:187], v[32:35], v[88:91], a[184:187]\n"
    "v_fma_f32 v180, v180, v113, v115\n"
    "v_fma_f32 v181, v181, v113, v115\n"
    "v_mfma_f32_16x16x32_bf16 a[164:167], a[208:211], v[212:215], a[164:167]\n"
    "v_accvgpr_read_b32 v179, a181\n"
    "v_accvgpr_read_b32 v185, a151\n"
    "v_mfma_f32_16x16x32_bf16 a[164:167], a[212:215], v[216:219], a[164:167]\n"
    "ds_read_b128 v[160:163], v3 offset:4096\n"
    "v_fma_f32 v184, v184, v113, v115\n"
    "v_fma_f32 v185, v185, v113, v115\n"
    "v_mfma_f32_16x16x32_bf16 a[136:139], v[4:7], v[84:87], a[136:139]\n"
    "v_accvgpr_read_b32 v176, a133\n"
    "v_accvgpr_read_b32 v187, a183\n"
    "v_accvgpr_read_b32 v172, a132\n"
    "v_mfma_f32_16x16x32_bf16 a[136:139], v[8:11], v[88:91], a[136:139]\n"
    "v_accvgpr_read_b32 v183, a182\n"
    "v_accvgpr_read_b32 v186, a167\n"
    "v_mfma_f32_16x16x32_bf16 a[136:139], a[192:195], v[220:223], a[136:139]\n"
    "v_accvgpr_read_b32 v174, a164\n"
    "v_accvgpr_read_b32 v178, a165\n"
    "v_mfma_f32_16x16x32_bf16 a[168:171], v[20:23], v[84:87], a[168:171]\n"
    "s_waitcnt lgkmcnt(1)\n"
    "buffer_store_dwordx4 v[168:171], v244, s[24:27], s75 offen offset:3328 sc1\n"
    "v_accvgpr_read_b32 v175, a180\n"
    "v_accvgpr_read_b32 v182, a166\n"
    "v_fma_f32 v178, v178, v113, v115\n"
    "v_fma_f32 v179, v179, v113, v115\n"
    "v_pk_fma_f32 v[182:183], v[182:183], v[112:113], v[114:115] op_sel:[0,1,1] op_sel_hi:[1,1,1]\n"
    "v_cvt_pk_bf16_f32 v180, v180, v181\n"
    "v_pk_fma_f32 v[186:187], v[186:187], v[112:113], v[114:115] op_sel:[0,1,1] op_sel_hi:[1,1,1]\n"
    "v_pk_fma_f32 v[174:175], v[174:175], v[112:113], v[114:115] op_sel:[0,1,1] op_sel_hi:[1,1,1]\n"
    "v_pk_fma_f32 v[176:177], v[176:177], v[112:113], v[114:115] op_sel:[0,1,1] op_sel_hi:[1,1,1]\n"
    "v_cvt_pk_bf16_f32 v181, v182, v183\n"
    "v_pk_fma_f32 v[172:173], v[172:173], v[112:113], v[114:115] op_sel:[0,1,1] op_sel_hi:[1,1,1]\n"
    "v_cvt_pk_bf16_f32 v182, v184, v185\n"
    "v_cvt_pk_bf16_f32 v183, v186, v187\n"
    "v_mfma_f32_16x16x32_bf16 a[168:171], v[24:27], v[88:91], a[168:171]\n"
    "s_waitcnt lgkmcnt(0)\n"
    "buffer_store_dwordx4 v[160:163], v244, s[24:27], s75 offen offset:256 sc1\n"
    "ds_write_b128 v2, v[180:183] offset:0\n"
    "v_mfma_f32_16x16x32_bf16 a[136:139], a[196:199], v[224:227], a[136:139]\n"
    "v_cvt_pk_bf16_f32 v172, v172, v173\n"
    "v_cvt_pk_bf16_f32 v173, v174, v175\n"
    "v_mfma_f32_16x16x32_bf16 a[184:187], v[28:31], v[84:87], a[184:187]\n"
    "v_cvt_pk_bf16_f32 v175, v178, v179\n"
    "v_cvt_pk_bf16_f32 v174, v176, v177\n"
    "v_mfma_f32_16x16x32_bf16 a[168:171], a[208:211], v[220:223], a[168:171]\n"
    "ds_write_b128 v252, v[172:175] offset:0\n"
    "v_mfma_f32_16x16x32_bf16 a[168:171], a[212:215], v[224:227], a[168:171]\n"
    "v_accvgpr_read_b32 v196, a138\n"
    "v_accvgpr_read_b32 v188, a136\n"
    "v_mfma_f32_16x16x32_bf16 a[184:187], a[220:223], v[224:227], a[184:187]\n"
    "v_accvgpr_read_b32 v200, a139\n"
    "v_mfma_f32_16x16x32_bf16 a[184:187], a[216:219], v[220:223], a[184:187]\n"
    "ds_read_b128 v[176:179], v3 offset:0\n"
    "ds_read_b128 v[184:187], v3 offset:1024\n"
    "v_mfma_f32_16x16x32_bf16 a[152:155], v[16:19], v[88:91], a[152:155]\n"
    "v_accvgpr_read_b32 v194, a169\n"
    "v_fma_f32 v250, |v110|, 4.0, v250\n"
    "v_mfma_f32_16x16x32_bf16 a[152:155], v[12:15], v[84:87], a[152:155]\n"
    "v_accvgpr_read_b32 v198, a170\n"
    "v_accvgpr_read_b32 v190, a168\n"
    "v_mfma_f32_16x16x32_bf16 a[152:155], a[204:207], v[224:227], a[152:155]\n"
    "v_accvgpr_read_b32 v192, a137\n"
    "v_accvgpr_read_b32 v199, a186\n"
    "v_mfma_f32_16x16x32_bf16 a[152:155], a[200:203], v[220:223], a[152:155]\n"
    "v_fma_f32 v250, v108, 4.0, v250\n"
    "v_accvgpr_read_b32 v203, a187\n"
    "v_mfma_f32_16x16x32_bf16 a[140:143], v[4:7], v[92:95], a[140:143]\n"
    "v_fma_f32 v250, v109, s74, v250\n"
    "v_accvgpr_read_b32 v191, a184\n"
    "v_accvgpr_read_b32 v195, a185\n"
    "v_mfma_f32_16x16x32_bf16 a[156:159], v[16:19], v[96:99], a[156:159]\n"
    "s_nop 0\n"
    "v_accvgpr_read_b32 v201, a155\n"
    "v_fma_f32 v200, v200, v117, v119\n"
    "v_fma_f32 v201, v201, v117, v119\n"
    "v_mfma_f32_16x16x32_bf16 a[172:175], v[20:23], v[92:95], a[172:175]\n"
    "s_waitcnt lgkmcnt(1)\n"
    "buffer_store_dwordx4 v[176:179], v244, s[24:27], s76 offen offset:256 sc1\n"
    "v_accvgpr_read_b32 v189, a152\n"
    "v_accvgpr_read_b32 v202, a171\n"
    "v_accvgpr_read_b32 v193, a153\n"
    "v_fma_f32 v198, v198, v117, v119\n"
    "v_fma_f32 v199, v199, v117, v119\n"
    "v_accvgpr_read_b32 v197, a154\n"
    "v_pk_fma_f32 v[196:197], v[196:197], v[116:117], v[118:119] op_sel:[0,1,1] op_sel_hi:[1,1,1]\n"
    "v_pk_fma_f32 v[202:203], v[202:203], v[116:117], v[118:119] op_sel:[0,1,1] op_sel_hi:[1,1,1]\n"
    "v_pk_fma_f32 v[192:193], v[192:193], v[116:117], v[118:119] op_sel:[0,1,1] op_sel_hi:[1,1,1]\n"
    "v_cvt_pk_bf16_f32 v196, v196, v197\n"
    "v_cvt_pk_bf16_f32 v197, v198, v199\n"
    "v_cvt_pk_bf16_f32 v199, v202, v203\n"
    "v_cvt_pk_bf16_f32 v198, v200, v201\n"
    "v_mfma_f32_16x16x32_bf16 a[156:159], v[12:15], v[92:95], a[156:159]\n"
    "s_waitcnt lgkmcnt(0)\n"
    "ds_write_b128 v2, v[196:199] offset:2048\n"
    "buffer_store_dwordx4 v[184:187], v244, s[24:27], s76 offen offset:3328 sc1\n"
    "v_pk_fma_f32 v[188:189], v[188:189], v[116:117], v[118:119] op_sel:[0,1,1] op_sel_hi:[1,1,1]\n"
    "v_pk_fma_f32 v[194:195], v[194:195], v[116:117], v[118:119] op_sel:[0,1,1] op_sel_hi:[1,1,1]\n"
    "v_pk_fma_f32 v[190:191], v[190:191], v[116:117], v[118:119] op_sel:[0,1,1] op_sel_hi:[1,1,1]\n"
    "v_cvt_pk_bf16_f32 v188, v188, v189\n"
    "v_cvt_pk_bf16_f32 v189, v190, v191\n"
    "v_mfma_f32_16x16x32_bf16 a[156:159], a[204:207], v[232:235], a[156:159]\n"
    "v_cvt_pk_bf16_f32 v190, v192, v193\n"
    "v_cvt_pk_bf16_f32 v191, v194, v195\n"
    "v_mfma_f32_16x16x32_bf16 a[156:159], a[200:203], v[228:231], a[156:159]\n"
    "ds_write_b128 v252, v[188:191] offset:2048\n"
    "v_mfma_f32_16x16x32_bf16 a[140:143], v[8:11], v[96:99], a[140:143]\n"
    "v_fma_f32 v250, |v114|, 4.0, v250\n"
    "v_fma_f32 v250, v112, 4.0, v250\n"
    "v_mfma_f32_16x16x32_bf16 a[172:175], v[24:27], v[96:99], a[172:175]\n"
    "v_mfma_f32_16x16x32_bf16 a[140:143], a[192:195], v[228:231], a[140:143]\n"
    "ds_read_b128 v[200:203], v3 offset:3072\n"
    "v_mfma_f32_16x16x32_bf16 a[140:143], a[196:199], v[232:235], a[140:143]\n"
    "v_accvgpr_read_b32 v129, a157\n"
    "v_fma_f32 v250, v113, s74, v250\n"
    "v_mfma_f32_16x16x32_bf16 a[188:191], v[32:35], v[96:99], a[188:191]\n"
    "ds_read_b128 v[192:195], v3 offset:2048\n"
    "v_cmp_eq_u64 s[56:57], v[236:237], v[238:239]\n"
    "v_mfma_f32_16x16x32_bf16 a[172:175], a[208:211], v[228:231], a[172:175]\n"
    "v_fma_f32 v250, |v118|, 4.0, v250\n"
    "v_mfma_f32_16x16x32_bf16 a[172:175], a[212:215], v[232:235], a[172:175]\n"
    "v_fma_f32 v250, v116, 4.0, v250\n"
    "v_mfma_f32_16x16x32_bf16 a[188:191], v[28:31], v[92:95], a[188:191]\n"
    "v_fma_f32 v250, v117, s74, v250\n"
    "v_mfma_f32_16x16x32_bf16 a[188:191], a[220:223], v[232:235], a[188:191]\n"
    "v_fma_f32 v250, |v122|, 4.0, v250\n"
    "v_accvgpr_read_b32 v132, a142\n"
    "v_accvgpr_read_b32 v133, a158\n"
    "v_accvgpr_read_b32 v128, a141\n"
    "v_mfma_f32_16x16x32_bf16 a[188:191], a[216:219], v[228:231], a[188:191]\n"
    "s_waitcnt lgkmcnt(1)\n"
    "buffer_store_dwordx4 v[200:203], v244, s[24:27], s77 offen offset:3328 sc1\n"
    "v_accvgpr_read_b32 v125, a156\n"
    "v_accvgpr_read_b32 v126, a172\n"
    "v_fma_f32 v250, v120, 4.0, v250\n"
    "v_accvgpr_read_b32 v124, a140\n"
    "v_pk_fma_f32 v[124:125], v[124:125], v[120:121], v[122:123] op_sel:[0,1,1] op_sel_hi:[1,1,1]\n"
    "v_accvgpr_read_b32 v138, a175\n"
    "v_accvgpr_read_b32 v137, a159\n"
    "v_accvgpr_read_b32 v139, a191\n"
    "v_pk_fma_f32 v[138:139], v[138:139], v[120:121], v[122:123] op_sel:[0,1,1] op_sel_hi:[1,1,1]\n"
    "v_accvgpr_read_b32 v127, a188\n"
    "v_accvgpr_read_b32 v130, a173\n"
    "v_accvgpr_read_b32 v136, a143\n"
    "v_accvgpr_read_b32 v131, a189\n"
    "v_pk_fma_f32 v[126:127], v[126:127], v[120:121], v[122:123] op_sel:[0,1,1] op_sel_hi:[1,1,1]\n"
    "v_pk_fma_f32 v[130:131], v[130:131], v[120:121], v[122:123] op_sel:[0,1,1] op_sel_hi:[1,1,1]\n"
    "v_cvt_pk_bf16_f32 v124, v124, v125\n"
    "v_cvt_pk_bf16_f32 v125, v126, v127\n"
    "v_accvgpr_read_b32 v134, a174\n"
    "v_cvt_pk_bf16_f32 v127, v130, v131\n"
    "v_accvgpr_read_b32 v135, a190\n"
    "s_or_b64 s[56:57], s[56:57], s[34:35]\n"
    "s_waitcnt lgkmcnt(0)\n"
    "buffer_store_dwordx4 v[192:195], v244, s[24:27], s77 offen offset:256 sc1\n"
    "v_pk_fma_f32 v[128:129], v[128:129], v[120:121], v[122:123] op_sel:[0,1,1] op_sel_hi:[1,1,1]\n"
    "v_pk_fma_f32 v[134:135], v[134:135], v[120:121], v[122:123] op_sel:[0,1,1] op_sel_hi:[1,1,1]\n"
    "v_cvt_pk_bf16_f32 v126, v128, v129\n"
    "ds_write_b128 v252, v[124:127] offset:4096\n"
    "v_pk_fma_f32 v[132:133], v[132:133], v[120:121], v[122:123] op_sel:[0,1,1] op_sel_hi:[1,1,1]\n"
    "v_fma_f32 v250, v121, s74, v250\n"
    "v_pk_fma_f32 v[136:137], v[136:137], v[120:121], v[122:123] op_sel:[0,1,1] op_sel_hi:[1,1,1]\n"
    "v_cvt_pk_bf16_f32 v132, v132, v133\n"
    "v_cvt_pk_bf16_f32 v133, v134, v135\n"
    "v_cvt_pk_bf16_f32 v135, v138, v139\n"
    "v_cmp_nlt_f32 s[54:55], |v250|, s73\n"
    "v_cvt_pk_bf16_f32 v134, v136, v137\n"
    "s_or_b64 s[54:55], s[54:55], s[56:57]\n"
    "ds_write_b128 v2, v[132:135] offset:4096\n"
    "ds_read_b128 v[136:139], v3 offset:5120\n"
    "s_or_b32 %0, s54, s55\n"
    "ds_read_b128 v[128:131], v3 offset:4096\n"
    "s_waitcnt lgkmcnt(1)\n"
    "buffer_store_dwordx4 v[136:139], v244, s[24:27], s78 offen offset:3328 sc1\n"
    "s_waitcnt lgkmcnt(0)\n"
    "buffer_store_dwordx4 v[128:131], v244, s[24:27], s78 offen offset:256 sc1\n"
    "s_cmp_eq_u32 %0, 0\n"
    "s_cbranch_scc1 .Lfin\n"
    "s_waitcnt vmcnt(0)\n"
    ".Lfin:\n"
    ".Lend:\n"
    : "=&s"(bad), "+s"(ka) : "s"(b), "v"(tid)
    : "memory", "vcc", "s16", "s17", "s18", "s19", "s20", "s21", "s22", "s23", "s24", "s25", "s26", "s27", "s28", "s29", "s30", "s31", "s34", "s35", "s36", "s37", "s38", "s39", "s40", "s41", "s42", "s43", "s44", "s45", "s46", "s47", "s48", "s49", "s50", "s51", "s52", "s53", "s54", "s55", "s56", "s57", "s58", "s59", "s60", "s61", "s62", "s63", "s64", "s65", "s66", "s67", "s68", "s69", "s70", "s71", "s72", "s73", "s74", "s75", "s76", "s77", "s78", "s79", "s80", "s81", "s82", "s83", "s84", "s85", "s86", "s87", "s88", "s89", "s90", "s91", "s92", "s93", "s94", "s95", "s96", "s97", "s98", "s99", "v1", "v2", "v3", "v4", "v5", "v6", "v7", "v8", "v9", "v10", "v11", "v12", "v13", "v14", "v15", "v16", "v17", "v18", "v19", "v20", "v21", "v22", "v23", "v24", "v25", "v26", "v27", "v28", "v29", "v30", "v31", "v32", "v33", "v34", "v35", "v36", "v37", "v38", "v39", "v40", "v41", "v42", "v43", "v44", "v45", "v46", "v47", "v48", "v49", "v50", "v51", "v52", "v53", "v54", "v55", "v56", "v57", "v58", "v59", "v60", "v61", "v62", "v63", "v64", "v65", "v66", "v67", "v68", "v69", "v70", "v71", "v72", "v73", "v74", "v75", "v76", "v77", "v78", "v79", "v80", "v81", "v82", "v83", "v84", "v85", "v86", "v87", "v88", "v89", "v90", "v91", "v92", "v93", "v94", "v95", "v96", "v97", "v98", "v99", "v100", "v101", "v102", "v103", "v104", "v105", "v106", "v107", "v108", "v109", "v110", "v111", "v112", "v113", "v114", "v115", "v116", "v117", "v118", "v119", "v120", "v121", "v122", "v123", "v124", "v125", "v126", "v127", "v128", "v129", "v130", "v131", "v132", "v133", "v134", "v135", "v136", "v137", "v138", "v139", "v140", "v141", "v142", "v143", "v144", "v145", "v146", "v147", "v148", "v149", "v150", "v151", "v152", "v153", "v154", "v155", "v156", "v157", "v158", "v159", "v160", "v161", "v162", "v163", "v164", "v165", "v166", "v167", "v168", "v169", "v170", "v171", "v172", "v173", "v174", "v175", "v176", "v177", "v178", "v179", "v180", "v181", "v182", "v183", "v184", "v185", "v186", "v187", "v188", "v189", "v190", "v191", "v192", "v193", "v194", "v195", "v196", "v197", "v198", "v199", "v200", "v201", "v202", "v203", "v204", "v205", "v206", "v207", "v208", "v209", "v210", "v211", "v212", "v213", "v214", "v215", "v216", "v217", "v218", "v219", "v220", "v221", "v222", "v223", "v224", "v225", "v226", "v227", "v228", "v229", "v230", "v231", "v232", "v233", "v234", "v235", "v236", "v237", "v238", "v239", "v240", "v241", "v242", "v243", "v244", "v245", "v246", "v247", "v248", "v249", "v250", "v251", "v252", "v253", "v254", "v255", "a0", "a1", "a2", "a3", "a4", "a5", "a6", "a7", "a8", "a9", "a10", "a11", "a12", "a13", "a14", "a15", "a16", "a17", "a18", "a19", "a20", "a21", "a22", "a23", "a24", "a25", "a26", "a27", "a28", "a29", "a30", "a31", "a32", "a33", "a34", "a35", "a36", "a37", "a38", "a39", "a40", "a41", "a42", "a43", "a44", "a45", "a46", "a47", "a48", "a49", "a50", "a51", "a52", "a53", "a54", "a55", "a56", "a57", "a58", "a59", "a60", "a61", "a62", "a63", "a64", "a65", "a66", "a67", "a68", "a69", "a70", "a71", "a72", "a73", "a74", "a75", "a76", "a77", "a78", "a79", "a80", "a81", "a82", "a83", "a84", "a85", "a86", "a87", "a88", "a89", "a90", "a91", "a92", "a93", "a94", "a95", "a96", "a97", "a98", "a99", "a100", "a101", "a102", "a103", "a104", "a105", "a106", "a107", "a108", "a109", "a110", "a111", "a112", "a113", "a114", "a115", "a116", "a117", "a118", "a119", "a120", "a121", "a122", "a123", "a124", "a125", "a126", "a127", "a128", "a129", "a130", "a131", "a132", "a133", "a134", "a135", "a136", "a137", "a138", "a139", "a140", "a141", "a142", "a143", "a144", "a145", "a146", "a147", "a148", "a149", "a150", "a151", "a152", "a153", "a154", "a155", "a156", "a157", "a158", "a159", "a160", "a161", "a162", "a163", "a164", "a165", "a166", "a167", "a168", "a169", "a170", "a171", "a172", "a173", "a174", "a175", "a176", "a177", "a178", "a179", "a180", "a181", "a182", "a183", "a184", "a185", "a186", "a187", "a188", "a189", "a190", "a191", "a192", "a193", "a194", "a195", "a196", "a197", "a198", "a199", "a200", "a201", "a202", "a203", "a204", "a205", "a206", "a207", "a208", "a209", "a210", "a211", "a212", "a213", "a214", "a215", "a216", "a217", "a218", "a219", "a220", "a221", "a222", "a223", "a224", "a225", "a226", "a227", "a228", "a229", "a230", "a231", "a232", "a233", "a234", "a235", "a236", "a237", "a238", "a239", "a240", "a241", "a242", "a243", "a244", "a245", "a246", "a247", "a248", "a249", "a250", "a251", "a252", "a253", "a254", "a255");
  if (bad) {
    slow_rows((const Args*)ka, (b & 7) * 4 + ((b >> 3) & 3), (b >> 5) * 256 + (tid & 192), tid & 63);
  }
}

extern "C" void kernel_launch(void* const* d_in, const int* in_sizes, int n_in,
                              void* d_out, int out_size, void* d_ws, size_t ws_size,
                              hipStream_t stream) {
  (void)hipFuncSetAttribute((const void*)attn_fwd, hipFuncAttributeMaxDynamicSharedMemorySize, SHM);
  attn_fwd<<<256, 256, SHM, stream>>>((const unsigned short*)d_in[0], (const int*)d_in[1], (const int*)d_in[2], (const unsigned short*)d_in[3], (const unsigned short*)d_in[4],
                                      (const unsigned short*)d_in[5], (const unsigned short*)d_in[6], (char*)d_ws, (unsigned short*)d_out);
}
```

```cpp
#include <hip/hip_runtime.h>

constexpr int D = 192, NH = 32, S = 2048;
constexpr int SHM = 155968;

struct Args { const unsigned short* q; const int* qk; const int* qv; const unsigned short* ksc; const unsigned short* kzr;
              const unsigned short* vsc; const unsigned short* vzr; char* ws; unsigned short* o; };

__device__ __forceinline__ float bf2f(unsigned short x) { return __uint_as_float((unsigned)x << 16); }
__device__ __forceinline__ float rbf(float x) { return (float)(__bf16)x; }

__device__ __forceinline__ void slow_rows(const Args* a, unsigned h, unsigned row0, unsigned lane) {
  const unsigned short* q = a->q + ((size_t)h * S + row0) * D;
  unsigned short* o = a->o + ((size_t)h * S + row0) * D;
  const int* qk = a->qk + (size_t)h * S * D; const int* qv = a->qv + (size_t)h * S * D;
  const unsigned short* ks = a->ksc + h * 32 * D; const unsigned short* kz = a->kzr + h * 32 * D;
  const unsigned short* vs = a->vsc + h * S; const unsigned short* vz = a->vzr + h * S;
#pragma unroll 1
  for (int r = 0; r < 64; ++r) {
    float m = -INFINITY, l = 0.f, a0 = 0.f, a1 = 0.f, a2 = 0.f;
#pragma unroll 1
    for (int t = 0; t < 32; ++t) {
      const int* kr = qk + (size_t)(t * 64 + lane) * D;
      float s = 0.f;
#pragma unroll 4
      for (int d = 0; d < D; ++d) s = fmaf(bf2f(q[r * D + d]), rbf(rbf(rbf((float)kr[d]) - bf2f(kz[t * D + d])) * bf2f(ks[t * D + d])), s);
      s *= 0.07216878364870322f;
      float mx = s;
      for (int x = 32; x; x >>= 1) mx = fmaxf(mx, __shfl_xor(mx, x));
      const float mn = fmaxf(m, mx), c = __expf(m - mn), p = __expf(s - mn);
      float ps = p;
      for (int x = 32; x; x >>= 1) ps += __shfl_xor(ps, x);
      l = l * c + ps; a0 *= c; a1 *= c; a2 *= c; m = mn;
#pragma unroll 2
      for (int k = 0; k < 64; ++k) {
        const float pk = __shfl(p, k), z = bf2f(vz[t * 64 + k]), sc = bf2f(vs[t * 64 + k]);
        const int* vr = qv + (size_t)(t * 64 + k) * D + lane;
        a0 = fmaf(pk, rbf(rbf(rbf((float)vr[0]) - z) * sc), a0); a1 = fmaf(pk, rbf(rbf(rbf((float)vr[64]) - z) * sc), a1); a2 = fmaf(pk, rbf(rbf(rbf((float)vr[128]) - z) * sc), a2);
      }
    }
    const float i = 1.f / l;
    o[r * D + lane] = __builtin_bit_cast(unsigned short, (__bf16)(a0 * i)); o[r * D + 64 + lane] = __builtin_bit_cast(unsigned short, (__bf16)(a1 * i));
    o[r * D + 128 + lane] = __builtin_bit_cast(unsigned short, (__bf16)(a2 * i));
  }
}

__global__ __launch_bounds__(256, 1) __attribute__((aligned(4096))) void attn_fwd(const unsigned short* q, const int* qk, const int* qv, const unsigned short* ksc, const unsigned short* kzr,
                                                   const unsigned short* vsc, const unsigned short* vzr, char* ws, unsigned short* o) {
  unsigned long long ka = (unsigned long long)__builtin_amdgcn_kernarg_segment_ptr();
  unsigned bad, b = blockIdx.x;
  const unsigned tid = threadIdx.x;
  asm volatile(
    "s_load_dwordx2 s[28:29], %1, 8\n"
    "s_load_dwordx2 s[40:41], %1, 16\n"
    "s_load_dwordx2 s[44:45], %1, 24\n"
    "s_load_dwordx2 s[88:89], %1, 32\n"
    "s_load_dwordx2 s[36:37], %1, 56\n"
    "s_load_dwordx2 s[92:93], %1, 40\n"
    "s_load_dwordx2 s[96:97], %1, 48\n"
    "s_load_dwordx2 s[20:21], %1, 0\n"
    "s_load_dwordx2 s[16:17], %1, 56\n"
    "s_load_dwordx2 s[24:25], %1, 64\n"
    "v_readfirstlane_b32 s84, %3\n"
    "v_and_b32 v156, 63, %3\n"
    "s_and_b32 s52, %2, 7\n"
    "s_bfe_u32 s72, %2, 0x20003\n"
    "s_lshr_b32 s53, %2, 5\n"
    "s_lshl_b32 s52, s52, 2\n"
    "s_lshr_b32 s84, s84, 6\n"
    "s_or_b32 s52, s52, s72\n"
    "v_lshlrev_b32 v1, 4, v156\n"
    "s_getpc_b64 s[54:55]\n"
    ".Lpc:\n"
    "s_lshl_b32 s72, s72, 14\n"
    "v_and_b32 v246, 127, %3\n"
    "v_lshl_add_u32 v246, v246, 7, s72\n"
    "v_min_u32 v246, .Lend-.Lpc-4, v246\n"
    "global_load_dword v246, v246, s[54:55]\n"
    "s_mov_b32 s30, 1572864\n"
    "s_mov_b32 s31, 0x20000\n"
    "s_mov_b32 s42, 1572864\n"
    "s_mov_b32 s43, 0x20000\n"
    "s_mov_b32 s46, 12288\n"
    "s_mov_b32 s47, 0x20000\n"
    "s_mov_b32 s90, 12288\n"
    "s_mov_b32 s91, 0x20000\n"
    "s_lshr_b32 s79, s84, 1\n"
    "s_and_b32 s59, s84, 1\n"
    "s_cmp_ge_u32 s53, 6\n"
    "s_cselect_b32 s83, 1, 0\n"
    "s_or_b32 s83, s83, s84\n"
    "s_sub_u32 s58, 2, s79\n"
    "s_mul_i32 s71, s58, 49152\n"
    "s_mul_i32 s72, s52, 1572864\n"
    "s_mul_i32 s73, s52, 12288\n"
    "v_lshrrev_b32 v157, 2, v156\n"
    "v_and_b32 v158, 3, v156\n"
    "s_mul_i32 s35, s84, 3072\n"
    "s_bitcmp1_b32 s83, 0\n"
    "s_cbranch_scc1 .Lf2b\n"
    "v_and_b32 v159, 32, v156\n"
    "v_mul_u32_u24 v242, 376, v159\n"
    "v_and_b32 v159, 16, v156\n"
    "v_mul_u32_u24 v159, 88, v159\n"
    "v_lshl_add_u32 v242, v156, 3, v242\n"
    "v_add_u32 v242, v159, v242\n"
    "s_lshl_b32 s63, s53, 7\n"
    "v_mul_u32_u24 v160, 768, v157\n"
    "v_lshl_add_u32 v160, v158, 4, v160\n"
    "s_and_b32 s81, s58, 1\n"
    "s_mul_i32 s81, s81, 384\n"
    "s_lshl_b32 s75, s53, 6\n"
    "s_add_u32 s81, s81, s75\n"
    "s_add_u32 s75, s71, s63\n"
    "s_add_u32 s76, s75, 6144\n"
    "s_add_u32 s77, s75, 24576\n"
    "s_add_u32 s78, s75, 30720\n"
    "s_waitcnt lgkmcnt(0)\n"
    "s_add_u32 s44, s44, s73\n"
    "s_addc_u32 s45, s45, 0\n"
    "s_add_u32 s88, s88, s73\n"
    "s_addc_u32 s89, s89, 0\n"
    "buffer_load_dwordx4 v[4:7], v1, s[44:47], s35 offen offset:0\n"
    "buffer_load_dwordx4 v[8:11], v1, s[44:47], s35 offen offset:1024\n"
    "buffer_load_dwordx4 v[12:15], v1, s[44:47], s35 offen offset:2048\n"
    "buffer_load_dwordx4 v[16:19], v1, s[88:91], 0 offen offset:0\n"
    "buffer_load_dwordx4 v[164:167], v160, s[44:47], s81 offen offset:0\n"
    "buffer_load_dwordx4 v[168:171], v160, s[88:91], s81 offen offset:0\n"
    "s_add_u32 s28, s28, s72\n"
    "s_addc_u32 s29, s29, 0\n"
    "buffer_load_dwordx2 v[204:205], v242, s[28:31], s75 offen offset:0 nt\n"
    "buffer_load_dwordx2 v[206:207], v242, s[28:31], s75 offen offset:768 nt\n"
    "buffer_load_dwordx2 v[208:209], v242, s[28:31], s75 offen offset:3072 nt\n"
    "buffer_load_dwordx2 v[210:211], v242, s[28:31], s75 offen offset:3840 nt\n"
    "buffer_load_dwordx2 v[212:213], v242, s[28:31], s76 offen offset:0 nt\n"
    "buffer_load_dwordx2 v[214:215], v242, s[28:31], s76 offen offset:768 nt\n"
    "buffer_load_dwordx2 v[216:217], v242, s[28:31], s76 offen offset:3072 nt\n"
    "buffer_load_dwordx2 v[218:219], v242, s[28:31], s76 offen offset:3840 nt\n"
    "buffer_load_dwordx2 v[220:221], v242, s[28:31], s77 offen offset:0 nt\n"
    "buffer_load_dwordx2 v[222:223], v242, s[28:31], s77 offen offset:768 nt\n"
    "buffer_load_dwordx2 v[224:225], v242, s[28:31], s77 offen offset:3072 nt\n"
    "buffer_load_dwordx2 v[226:227], v242, s[28:31], s77 offen offset:3840 nt\n"
    "buffer_load_dwordx2 v[228:229], v242, s[28:31], s78 offen offset:0 nt\n"
    "buffer_load_dwordx2 v[230:231], v242, s[28:31], s78 offen offset:768 nt\n"
    "buffer_load_dwordx2 v[232:233], v242, s[28:31], s78 offen offset:3072 nt\n"
    "buffer_load_dwordx2 v[234:235], v242, s[28:31], s78 offen offset:3840 nt\n"
    "s_add_u32 s75, s71, s63\n"
    "s_add_u32 s75, s75, 98304\n"
    "s_add_u32 s76, s75, 6144\n"
    "s_add_u32 s77, s75, 24576\n"
    "s_add_u32 s78, s75, 30720\n"
    "buffer_load_dwordx2 v[36:37], v242, s[28:31], s75 offen offset:0 nt\n"
    "buffer_load_dwordx2 v[38:39], v242, s[28:31], s75 offen offset:768 nt\n"
    "v_lshrrev_b32 v162, 1, v156\n"
    "v_and_b32 v162, 16, v162\n"
    "v_bfe_u32 v159, v156, 4, 1\n"
    "v_or_b32 v162, v162, v159\n"
    "v_and_or_b32 v162, v157, 2, v162\n"
    "v_mul_u32_u24 v162, 768, v162\n"
    "v_and_b32 v163, 0x70, v1\n"
    "v_add_u32 v162, v162, v163\n"
    "s_branch .Lf2e\n"
    ".Lf2b:\n"
    "v_and_b32 v159, 48, v156\n"
    "v_lshlrev_b32 v242, 2, v159\n"
    "v_lshl_add_u32 v243, v158, 4, v242\n"
    "v_and_b32 v159, 12, v156\n"
    "v_mul_u32_u24 v242, 384, v159\n"
    "v_add_u32 v242, v243, v242\n"
    "v_add_u32 v243, v243, v159\n"
    "s_mul_i32 s63, s53, 6144\n"
    "s_sub_u32 s63, s63, 49152\n"
    "s_cmp_eq_u32 s59, 0\n"
    "s_cselect_b32 s63, 0x7f000000, s63\n"
    "s_add_u32 s75, s71, s63\n"
    "s_waitcnt lgkmcnt(0)\n"
    "s_add_u32 s44, s44, s73\n"
    "s_addc_u32 s45, s45, 0\n"
    "s_add_u32 s88, s88, s73\n"
    "s_addc_u32 s89, s89, 0\n"
    "buffer_load_dwordx4 v[4:7], v1, s[44:47], s35 offen offset:0\n"
    "buffer_load_dwordx4 v[8:11], v1, s[44:47], s35 offen offset:1024\n"
    "buffer_load_dwordx4 v[12:15], v1, s[44:47], s35 offen offset:2048\n"
    "buffer_load_dwordx4 v[16:19], v1, s[88:91], 0 offen offset:0\n"
    "s_add_u32 s40, s40, s72\n"
    "s_addc_u32 s41, s41, 0\n"
    "buffer_load_dwordx4 v[204:207], v242, s[40:43], s75 offen offset:0 nt\n"
    "buffer_load_dwordx4 v[208:211], v242, s[40:43], s75 offen offset:768 nt\n"
    "buffer_load_dwordx4 v[212:215], v242, s[40:43], s75 offen offset:256 nt\n"
    "buffer_load_dwordx4 v[216:219], v242, s[40:43], s75 offen offset:1024 nt\n"
    "buffer_load_dwordx4 v[220:223], v242, s[40:43], s75 offen offset:512 nt\n"
    "buffer_load_dwordx4 v[224:227], v242, s[40:43], s75 offen offset:1280 nt\n"
    "s_add_u32 s75, s71, s63\n"
    "s_add_u32 s75, s75, 98304\n"
    "buffer_load_dwordx4 v[36:39], v242, s[40:43], s75 offen offset:0 nt\n"
    "s_add_u32 s28, s28, s72\n"
    "s_addc_u32 s29, s29, 0\n"
    "v_lshrrev_b32 v162, 1, v156\n"
    "v_and_b32 v162, 16, v162\n"
    "v_bfe_u32 v159, v156, 4, 1\n"
    "v_or_b32 v162, v162, v159\n"
    "v_and_or_b32 v162, v157, 2, v162\n"
    "v_mul_u32_u24 v162, 768, v162\n"
    "v_and_b32 v163, 0x70, v1\n"
    "v_add_u32 v162, v162, v163\n"
    ".Lf2e:\n"
    "s_nop 4\n"
    "v_lshlrev_b32 v247, 2, v156\n"
    "v_add_u32 v247, 155648, v247\n"
    "v_mov_b32 v159, 0\n"
    "ds_write_b32 v247, v159\n"
    "v_mov_b32 v159, 0xff800000\n"
    "v_mov_b32 v160, 155904\n"
    "ds_write_b32 v160, v159\n"
    "s_mov_b32 s38, 5376\n"
    "s_mov_b32 s39, 0x20000\n"
    "s_mov_b32 s94, 4096\n"
    "s_mov_b32 s95, 0x20000\n"
    "s_mov_b32 s98, 4096\n"
    "s_mov_b32 s99, 0x20000\n"
    "s_mov_b32 s22, 98304\n"
    "s_mov_b32 s23, 0x20000\n"
    "s_mov_b32 s18, 1622016\n"
    "s_mov_b32 s19, 0x20000\n"
    "s_mov_b32 s26, 98304\n"
    "s_mov_b32 s27, 0x20000\n"
    "s_lshl_b32 s80, s58, 7\n"
    "s_add_u32 s80, s80, 256\n"
    "s_lshl_b32 s74, s52, 16\n"
    "s_mul_i32 s69, s59, 384\n"
    "s_mul_i32 s57, s79, 6144\n"
    "s_add_u32 s57, s57, s69\n"
    "s_add_u32 s66, s57, 24576\n"
    "s_add_u32 s36, s36, s74\n"
    "s_addc_u32 s37, s37, 0\n"
    "s_lshr_b32 s74, s74, 4\n"
    "s_add_u32 s92, s92, s74\n"
    "s_addc_u32 s93, s93, 0\n"
    "s_add_u32 s96, s96, s74\n"
    "s_addc_u32 s97, s97, 0\n"
    "v_lshlrev_b32 v128, 3, v156\n"
    "v_mov_b32 v129, 0\n"
    "v_lshlrev_b32 v130, 4, %3\n"
    "buffer_load_dwordx2 v[124:125], v128, s[44:47], 0 offen offset:0\n"
    "buffer_load_dwordx2 v[126:127], v128, s[88:91], 0 offen offset:0\n"
    "buffer_load_dwordx2 v[238:239], v129, s[36:39], 0 offen offset:0 sc1\n"
    "s_mul_i32 s74, s52, 786432\n"
    "s_mul_i32 s73, s53, 98304\n"
    "s_add_u32 s74, s74, s73\n"
    "s_add_u32 s20, s20, s74\n"
    "s_addc_u32 s21, s21, 0\n"
    "s_add_u32 s24, s24, s74\n"
    "s_addc_u32 s25, s25, 0\n"
    "s_mul_i32 s72, s52, 1626112\n"
    "s_add_u32 s72, s72, 2097152\n"
    "s_add_u32 s16, s16, s72\n"
    "s_addc_u32 s17, s17, 0\n"
    "s_mul_i32 s65, s84, 6144\n"
    "s_mul_i32 s85, s84, 24576\n"
    "s_waitcnt lgkmcnt(0)\n"
    "s_barrier\n"
    "s_add_u32 s73, s65, 49152\n"
    "v_add_u32 v131, s73, v1\n"
    "s_add_u32 s73, s73, s69\n"
    "v_add_u32 v132, s73, v163\n"
    "v_and_b32 v159, 32, v156\n"
    "v_lshlrev_b32 v244, 3, v156\n"
    "v_xor_b32 v244, v244, v159\n"
    "v_lshl_add_u32 v244, v159, 3, v244\n"
    "s_bitcmp1_b32 s83, 0\n"
    "s_cbranch_scc1 .Lf5b\n"
    "s_lshl_b32 s60, s53, 11\n"
    "s_add_u32 s61, s60, 12288\n"
    "s_lshl_b32 s73, s58, 8\n"
    "s_add_u32 s73, s73, 122880\n"
    "v_and_b32 v243, 0xf0, v1\n"
    "v_add_u32 v243, s73, v243\n"
    "s_and_b32 s73, s73, 0x1fd00\n"
    "v_lshlrev_b32 v160, 9, v157\n"
    "v_lshl_add_u32 v160, v158, 6, v160\n"
    "v_add_u32 v160, s73, v160\n"
    "s_lshl_b32 s73, s79, 8\n"
    "s_mul_i32 s74, s59, 6144\n"
    "s_add_u32 s73, s73, s74\n"
    "v_add_u32 v163, s73, v244\n"
    "v_and_b32 v159, 32, v156\n"
    "v_lshlrev_b32 v244, 2, v156\n"
    "v_xor_b32 v244, v244, v159\n"
    "v_mad_u32_u24 v244, v159, 12, v244\n"
    "buffer_load_dwordx2 v[40:41], v242, s[28:31], s75 offen offset:3072 nt\n"
    "buffer_load_dwordx2 v[42:43], v242, s[28:31], s75 offen offset:3840 nt\n"
    "v_add_u32 v159, 0, v156\n"
    "v_mul_u32_u24 v159, 2731, v159\n"
    "v_lshrrev_b32 v159, 16, v159\n"
    "v_xor_b32 v159, v159, v156\n"
    "v_and_b32 v159, 7, v159\n"
    "v_and_b32 v88, 0x380, v1\n"
    "v_lshl_add_u32 v88, v159, 4, v88\n"
    "v_add_u32 v159, 64, v156\n"
    "v_mul_u32_u24 v159, 2731, v159\n"
    "v_lshrrev_b32 v159, 16, v159\n"
    "v_xor_b32 v159, v159, v156\n"
    "v_and_b32 v159, 7, v159\n"
    "v_and_b32 v89, 0x380, v1\n"
    "v_lshl_add_u32 v89, v159, 4, v89\n"
    "v_add_u32 v159, 128, v156\n"
    "v_mul_u32_u24 v159, 2731, v159\n"
    "v_lshrrev_b32 v159, 16, v159\n"
    "v_xor_b32 v159, v159, v156\n"
    "v_and_b32 v159, 7, v159\n"
    "v_and_b32 v90, 0x380, v1\n"
    "v_lshl_add_u32 v90, v159, 4, v90\n"
    "v_and_b32 v2, 7, v156\n"
    "v_lshrrev_b32 v144, 3, %3\n"
    "v_mul_u32_u24 v144, 768, v144\n"
    "v_lshl_add_u32 v144, v2, 3, v144\n"
    "buffer_load_dwordx2 v[44:45], v242, s[28:31], s76 offen offset:0 nt\n"
    "buffer_load_dwordx2 v[46:47], v242, s[28:31], s76 offen offset:768 nt\n"
    "v_add_u32 v144, 131072, v144\n"
    "v_mad_u32_u24 v86, v2, 24, v144\n"
    "v_and_b32 v84, 15, v156\n"
    "v_lshrrev_b32 v159, 4, v156\n"
    "v_xor_b32 v159, v159, v2\n"
    "v_mul_u32_u24 v84, 384, v84\n"
    "v_lshl_add_u32 v84, v159, 4, v84\n"
    "s_add_u32 s73, s65, 49152\n"
    "v_add_u32 v84, s73, v84\n"
    "v_xor_b32 v85, 64, v84\n"
    "v_subrev_u32 v154, 24576, v84\n"
    "v_subrev_u32 v155, 24576, v85\n"
    "s_add_u32 s67, s65, 98304\n"
    "s_add_u32 s68, s65, 24576\n"
    "s_add_u32 s69, s65, 73728\n"
    "s_mov_b32 s81, 256\n"
    "s_mul_i32 s74, s79, 24576\n"
    "s_add_u32 s70, s74, s65\n"
    "s_add_u32 s70, s70, 73728\n"
    "s_sub_u32 s64, s65, s74\n"
    "s_add_u32 s64, s64, 98304\n"
    "v_and_b32 v249, 0x310, v1\n"
    "v_and_b32 v159, 12, v156\n"
    "v_lshl_add_u32 v249, v159, 3, v249\n"
    "v_and_b32 v159, 2, v156\n"
    "buffer_load_dwordx2 v[48:49], v242, s[28:31], s76 offen offset:3072 nt\n"
    "buffer_load_dwordx2 v[50:51], v242, s[28:31], s76 offen offset:3840 nt\n"
    "v_lshl_add_u32 v249, v159, 6, v249\n"
    "s_add_u32 s73, s74, 73728\n"
    "v_add_u32 v248, s73, v249\n"
    "s_sub_u32 s73, 98304, s74\n"
    "v_add_u32 v249, s73, v249\n"
    "s_or_b32 s73, s53, s84\n"
    "s_cmp_eq_u32 s73, 0\n"
    "s_cselect_b32 s82, 0, 0x7fff0000\n"
    "v_and_b32 v252, 12, v156\n"
    "v_bfe_u32 v159, v156, 1, 1\n"
    "v_or_b32 v252, v252, v159\n"
    "v_and_b32 v159, 1, v156\n"
    "v_lshl_or_b32 v252, v159, 1, v252\n"
    "v_lshlrev_b32 v252, 6, v252\n"
    "v_lshlrev_b32 v159, 2, v156\n"
    "v_and_b32 v159, 32, v159\n"
    "v_xor_b32 v159, v159, v156\n"
    "v_and_or_b32 v252, v159, 48, v252\n"
    "v_mov_b32 v2, v252\n"
    "v_add_u32 v3, 24576, v252\n"
    "v_lshrrev_b32 v91, 3, v156\n"
    "v_lshl_add_u32 v87, v91, 6, 64\n"
    "v_add_u32 v87, 64, v87\n"
    "v_and_b32 v241, 1, v156\n"
    "v_lshlrev_b32 v241, 3, v241\n"
    "buffer_load_dwordx2 v[52:53], v242, s[28:31], s77 offen offset:0 nt\n"
    "buffer_load_dwordx2 v[54:55], v242, s[28:31], s77 offen offset:768 nt\n"
    "v_lshl_add_u32 v241, v91, 4, v241\n"
    "v_add_u32 v241, 1152, v241\n"
    "v_lshlrev_b32 v91, 24, v91\n"
    "v_lshl_add_u32 v91, v156, 3, v91\n"
    "s_lshl_b32 s73, s53, 4\n"
    "s_lshl_b32 s74, s59, 3\n"
    "s_add_u32 s73, s73, s74\n"
    "s_add_u32 s73, s73, 1152\n"
    "v_mov_b32 v240, 0x7fff0000\n"
    "v_writelane_b32 v240, s73, 0\n"
    "v_and_b32 v159, 48, v156\n"
    "s_mul_i32 s73, s79, 768\n"
    "s_sub_u32 s73, 131072, s73\n"
    "v_lshlrev_b32 v250, 1, v159\n"
    "v_add_u32 v250, s73, v250\n"
    "v_and_b32 v251, 3, v156\n"
    "v_lshl_add_u32 v251, v251, 7, v159\n"
    "s_add_u32 s73, s73, 256\n"
    "v_add_u32 v251, s73, v251\n"
    "v_and_b32 v159, 64, v1\n"
    "v_add_u32 v251, v159, v251\n"
    "v_bfe_i32 v159, v156, 2, 1\n"
    "v_and_b32 v159, 0xaaaa, v159\n"
    "v_or_b32 v253, 0x4444, v159\n"
    "s_mov_b32 s58, 0\n"
    "s_mov_b32 s59, 8\n"
    "s_waitcnt vmcnt(31)\n"
    "v_pk_min_i16 v4, v4, v10\n"
    "v_pk_min_i16 v5, v5, v11\n"
    "v_pk_min_i16 v6, v6, v12\n"
    "v_pk_min_i16 v7, v7, v13\n"
    "v_pk_min_i16 v8, v8, v14\n"
    "v_pk_min_i16 v9, v9, v15\n"
    "v_pk_min_i16 v16, v16, v18\n"
    "v_pk_min_i16 v17, v17, v19\n"
    "v_pk_min_i16 v4, v4, v7\n"
    "v_pk_min_i16 v5, v5, v8\n"
    "v_pk_min_i16 v6, v6, v9\n"
    "v_pk_min_i16 v16, v16, v17\n"
    "v_pk_min_i16 v4, v4, v5\n"
    "v_lshlrev_b32 v17, 16, v16\n"
    "v_pk_min_i16 v4, v4, v6\n"
    "v_min_i32 v16, v16, v17\n"
    "v_lshlrev_b32 v5, 16, v4\n"
    "v_mov_b32 v7, 155920\n"
    "v_min_i32 v4, v4, v5\n"
    "v_lshl_add_u32 v6, s84, 2, v7\n"
    "v_min_i32_dpp v16, v16, v16 row_shr:1\n"
    "v_min_i32_dpp v4, v4, v4 row_shr:1\n"
    "s_nop 0\n"
    "v_min_i32_dpp v16, v16, v16 row_shr:2\n"
    "v_min_i32_dpp v4, v4, v4 row_shr:2\n"
    "s_nop 0\n"
    "v_min_i32_dpp v16, v16, v16 row_shr:4\n"
    "v_min_i32_dpp v4, v4, v4 row_shr:4\n"
    "s_nop 0\n"
    "v_min_i32_dpp v16, v16, v16 row_shr:8\n"
    "v_min_i32_dpp v4, v4, v4 row_shr:8\n"
    "s_nop 0\n"
    "v_min_i32_dpp v16, v16, v16 row_bcast:15 row_mask:0xa\n"
    "v_min_i32_dpp v4, v4, v4 row_bcast:15 row_mask:0xa\n"
    "s_nop 0\n"
    "v_min_i32_dpp v16, v16, v16 row_bcast:31 row_mask:0xc\n"
    "v_min_i32_dpp v4, v4, v4 row_bcast:31 row_mask:0xc\n"
    "v_readlane_b32 s35, v16, 63\n"
    "v_readlane_b32 s73, v4, 63\n"
    "s_max_i32 s35, s35, 0\n"
    "s_and_b32 s35, s35, 0xffff0000\n"
    "v_mov_b32 v4, s73\n"
    "ds_write_b32 v6, v4\n"
    "v_mov_b32 v14, -32\n"
    "v_mov_b32 v12, -126\n"
    "v_mov_b32 v13, 0x3dd53b95\n"
    "v_mov_b32 v80, 0\n"
    "s_waitcnt vmcnt(29)\n"
    "v_lshlrev_b32 v180, 16, v164\n"
    "v_and_b32 v181, 0xffff0000, v164\n"
    "v_lshlrev_b32 v182, 16, v168\n"
    "v_and_b32 v183, 0xffff0000, v168\n"
    "v_lshlrev_b32 v184, 16, v165\n"
    "v_and_b32 v185, 0xffff0000, v165\n"
    "v_lshlrev_b32 v186, 16, v169\n"
    "v_and_b32 v187, 0xffff0000, v169\n"
    "v_lshlrev_b32 v188, 16, v166\n"
    "v_and_b32 v189, 0xffff0000, v166\n"
    "v_lshlrev_b32 v190, 16, v170\n"
    "v_and_b32 v191, 0xffff0000, v170\n"
    "v_lshlrev_b32 v192, 16, v167\n"
    "v_and_b32 v193, 0xffff0000, v167\n"
    "v_lshlrev_b32 v194, 16, v171\n"
    "v_and_b32 v195, 0xffff0000, v171\n"
    "s_waitcnt lgkmcnt(0)\n"
    "s_barrier\n"
    "ds_read_b128 v[8:11], v7 offset:0\n"
    "buffer_load_dwordx2 v[56:57], v242, s[28:31], s77 offen offset:3072 nt\n"
    "buffer_load_dwordx2 v[58:59], v242, s[28:31], s77 offen offset:3840 nt\n"
    "buffer_load_dwordx4 v[136:139], v130, s[92:95], 0 offen offset:0\n"
    "buffer_load_dwordx4 v[140:143], v130, s[96:99], 0 offen offset:0\n"
    "v_accvgpr_write_b32 a0, 0\n"
    "v_accvgpr_write_b32 a1, 0\n"
    "v_accvgpr_write_b32 a2, 0\n"
    "v_accvgpr_write_b32 a3, 0\n"
    "v_accvgpr_write_b32 a4, 0\n"
    "v_accvgpr_write_b32 a5, 0\n"
    "v_accvgpr_write_b32 a6, 0\n"
    "v_accvgpr_write_b32 a7, 0\n"
    "v_accvgpr_write_b32 a8, 0\n"
    "v_accvgpr_write_b32 a9, 0\n"
    "v_accvgpr_write_b32 a10, 0\n"
    "v_accvgpr_write_b32 a11, 0\n"
    "v_accvgpr_write_b32 a12, 0\n"
    "v_accvgpr_write_b32 a13, 0\n"
    "v_accvgpr_write_b32 a14, 0\n"
    "v_accvgpr_write_b32 a15, 0\n"
    "v_accvgpr_write_b32 a16, 0\n"
    "v_accvgpr_write_b32 a17, 0\n"
    "v_accvgpr_write_b32 a18, 0\n"
    "v_accvgpr_write_b32 a19, 0\n"
    "v_accvgpr_write_b32 a20, 0\n"
    "v_accvgpr_write_b32 a21, 0\n"
    "v_accvgpr_write_b32 a22, 0\n"
    "v_accvgpr_write_b32 a23, 0\n"
    "v_accvgpr_write_b32 a24, 0\n"
    "v_accvgpr_write_b32 a25, 0\n"
    "v_accvgpr_write_b32 a26, 0\n"
    "v_accvgpr_write_b32 a27, 0\n"
    "v_accvgpr_write_b32 a28, 0\n"
    "v_accvgpr_write_b32 a29, 0\n"
    "v_accvgpr_write_b32 a30, 0\n"
    "v_accvgpr_write_b32 a31, 0\n"
    "v_accvgpr_write_b32 a32, 0\n"
    "v_accvgpr_write_b32 a33, 0\n"
    "v_accvgpr_write_b32 a34, 0\n"
    "v_accvgpr_write_b32 a35, 0\n"
    "v_accvgpr_write_b32 a36, 0\n"
    "v_accvgpr_write_b32 a37, 0\n"
    "v_accvgpr_write_b32 a38, 0\n"
    "v_accvgpr_write_b32 a39, 0\n"
    "v_accvgpr_write_b32 a40, 0\n"
    "v_accvgpr_write_b32 a41, 0\n"
    "v_accvgpr_write_b32 a42, 0\n"
    "v_accvgpr_write_b32 a43, 0\n"
    "v_accvgpr_write_b32 a44, 0\n"
    "v_accvgpr_write_b32 a45, 0\n"
    "v_accvgpr_write_b32 a46, 0\n"
    "v_accvgpr_write_b32 a47, 0\n"
    "v_accvgpr_write_b32 a48, 0\n"
    "v_accvgpr_write_b32 a49, 0\n"
    "v_accvgpr_write_b32 a50, 0\n"
    "v_accvgpr_write_b32 a51, 0\n"
    "v_accvgpr_write_b32 a52, 0\n"
    "v_accvgpr_write_b32 a53, 0\n"
    "v_accvgpr_write_b32 a54, 0\n"
    "v_accvgpr_write_b32 a55, 0\n"
    "v_accvgpr_write_b32 a56, 0\n"
    "v_accvgpr_write_b32 a57, 0\n"
    "v_accvgpr_write_b32 a58, 0\n"
    "v_accvgpr_write_b32 a59, 0\n"
    "s_waitcnt lgkmcnt(0)\n"
    "v_min3_i32 v4, v8, v9, v10\n"
    "v_min_i32 v4, v4, v11\n"
    "v_max_i32 v4, 0, v4\n"
    "v_and_b32 v4, 0xffff0000, v4\n"
    "v_mul_f32 v4, s35, v4\n"
    "v_mul_f32 v4, 0x3ed53b95, v4\n"
    "v_lshrrev_b32 v4, 23, v4\n"
    "v_sub_u32 v4, 2, v4\n"
    "v_med3_i32 v4, v4, v14, v12\n"
    "v_ldexp_f32 v13, v13, v4\n"
    "v_readfirstlane_b32 s89, v4\n"
    "v_readfirstlane_b32 s88, v13\n"
    "s_mul_i32 s89, s89, 0xff7fff80\n"
    "s_mov_b32 s90, 149\n"
    "v_mul_f32 v180, s88, v180\n"
    "v_mul_f32 v181, s88, v181\n"
    "v_mul_f32 v184, s88, v184\n"
    "v_mul_f32 v185, s88, v185\n"
    "v_mul_f32 v188, s88, v188\n"
    "v_mul_f32 v189, s88, v189\n"
    "v_mul_f32 v192, s88, v192\n"
    "v_mul_f32 v193, s88, v193\n"
    "v_fmaak_f32 v182, v182, v180, 0x80730000\n"
    "v_fmaak_f32 v183, v183, v181, 0x80730000\n"
    "v_fmaak_f32 v186, v186, v184, 0x80730000\n"
    "v_fmaak_f32 v187, v187, v185, 0x80730000\n"
    "v_fmaak_f32 v190, v190, v188, 0x80730000\n"
    "v_fmaak_f32 v191, v191, v189, 0x80730000\n"
    "v_fmaak_f32 v194, v194, v192, 0x80730000\n"
    "v_fmaak_f32 v195, v195, v193, 0x80730000\n"
    "v_ldexp_f32 v180, v180, s90\n"
    "v_ldexp_f32 v181, v181, s90\n"
    "v_ldexp_f32 v184, v184, s90\n"
    "v_ldexp_f32 v185, v185, s90\n"
    "v_ldexp_f32 v188, v188, s90\n"
    "v_ldexp_f32 v189, v189, s90\n"
    "v_ldexp_f32 v192, v192, s90\n"
    "v_ldexp_f32 v193, v193, s90\n"
    "v_fma_f32 v180, v180, 0, v180\n"
    "v_fma_f32 v181, v181, 0, v181\n"
    "v_fma_f32 v184, v184, 0, v184\n"
    "v_fma_f32 v185, v185, 0, v185\n"
    "v_fma_f32 v188, v188, 0, v188\n"
    "v_fma_f32 v189, v189, 0, v189\n"
    "v_fma_f32 v192, v192, 0, v192\n"
    "v_fma_f32 v193, v193, 0, v193\n"
    "ds_write_b128 v160, v[180:183] offset:0\n"
    "ds_write_b128 v160, v[184:187] offset:16\n"
    "ds_write_b128 v160, v[188:191] offset:32\n"
    "ds_write_b128 v160, v[192:195] offset:48\n"
    "s_waitcnt vmcnt(17)\n"
    "s_add_u32 s54, s71, s60\n"
    "s_add_u32 s55, s71, s61\n"
    "ds_read_b128 v[92:95], v243 offset:0\n"
    "s_waitcnt lgkmcnt(0)\n"
    "v_fma_f32 v204, v204, v92, -v94\n"
    "v_fma_f32 v205, v205, v93, -v95\n"
    "v_fma_f32 v206, v206, v92, -v94\n"
    "v_fma_f32 v207, v207, v93, -v95\n"
    "v_cvt_pk_bf16_f32 v204, v204, v205\n"
    "v_cvt_pk_bf16_f32 v206, v206, v207\n"
    "buffer_store_dword v204, v244, s[16:19], s54 offen offset:0 sc1\n"
    "buffer_store_dword v206, v244, s[16:19], s54 offen offset:128 sc1\n"
    "v_fma_f32 v208, v208, v92, -v94\n"
    "v_fma_f32 v209, v209, v93, -v95\n"
    "v_fma_f32 v210, v210, v92, -v94\n"
    "v_fma_f32 v211, v211, v93, -v95\n"
    "v_cvt_pk_bf16_f32 v208, v208, v209\n"
    "v_cvt_pk_bf16_f32 v210, v210, v211\n"
    "buffer_store_dword v208, v244, s[16:19], s54 offen offset:1024 sc1\n"
    "buffer_store_dword v210, v244, s[16:19], s54 offen offset:1152 sc1\n"
    "v_fma_f32 v212, v212, v92, -v94\n"
    "v_fma_f32 v213, v213, v93, -v95\n"
    "buffer_load_dwordx2 v[60:61], v242, s[28:31], s78 offen offset:0 nt\n"
    "buffer_load_dwordx2 v[62:63], v242, s[28:31], s78 offen offset:768 nt\n"
    "buffer_load_dwordx4 v[4:7], v162, s[28:31], s57 offen offset:0\n"
    "buffer_load_dwordx4 v[8:11], v162, s[28:31], s57 offen offset:3072\n"
    "v_fma_f32 v214, v214, v92, -v94\n"
    "v_fma_f32 v215, v215, v93, -v95\n"
    "v_cvt_pk_bf16_f32 v212, v212, v213\n"
    "v_cvt_pk_bf16_f32 v214, v214, v215\n"
    "buffer_store_dword v212, v244, s[16:19], s54 offen offset:256 sc1\n"
    "buffer_store_dword v214, v244, s[16:19], s54 offen offset:384 sc1\n"
    "v_fma_f32 v216, v216, v92, -v94\n"
    "v_fma_f32 v217, v217, v93, -v95\n"
    "v_fma_f32 v218, v218, v92, -v94\n"
    "v_fma_f32 v219, v219, v93, -v95\n"
    "v_cvt_pk_bf16_f32 v216, v216, v217\n"
    "v_cvt_pk_bf16_f32 v218, v218, v219\n"
    "buffer_store_dword v216, v244, s[16:19], s54 offen offset:1280 sc1\n"
    "buffer_store_dword v218, v244, s[16:19], s54 offen offset:1408 sc1\n"
    "v_fma_f32 v220, v220, v92, -v94\n"
    "v_fma_f32 v221, v221, v93, -v95\n"
    "v_fma_f32 v222, v222, v92, -v94\n"
    "v_fma_f32 v223, v223, v93, -v95\n"
    "v_cvt_pk_bf16_f32 v220, v220, v221\n"
    "v_cvt_pk_bf16_f32 v222, v222, v223\n"
    "buffer_store_dword v220, v244, s[16:19], s55 offen offset:0 sc1\n"
    "buffer_store_dword v222, v244, s[16:19], s55 offen offset:128 sc1\n"
    "v_fma_f32 v224, v224, v92, -v94\n"
    "buffer_load_dwordx2 v[64:65], v242, s[28:31], s78 offen offset:3072 nt\n"
    "buffer_load_dwordx2 v[66:67], v242, s[28:31], s78 offen offset:3840 nt\n"
    "buffer_load_dwordx4 v[12:15], v162, s[28:31], s57 offen offset:128\n"
    "v_fma_f32 v225, v225, v93, -v95\n"
    "v_fma_f32 v226, v226, v92, -v94\n"
    "v_fma_f32 v227, v227, v93, -v95\n"
    "v_cvt_pk_bf16_f32 v224, v224, v225\n"
    "v_cvt_pk_bf16_f32 v226, v226, v227\n"
    "buffer_store_dword v224, v244, s[16:19], s55 offen offset:1024 sc1\n"
    "buffer_store_dword v226, v244, s[16:19], s55 offen offset:1152 sc1\n"
    "v_fma_f32 v228, v228, v92, -v94\n"
    "v_fma_f32 v229, v229, v93, -v95\n"
    "v_fma_f32 v230, v230, v92, -v94\n"
    "v_fma_f32 v231, v231, v93, -v95\n"
    "v_cvt_pk_bf16_f32 v228, v228, v229\n"
    "v_cvt_pk_bf16_f32 v230, v230, v231\n"
    "buffer_store_dword v228, v244, s[16:19], s55 offen offset:256 sc1\n"
    "buffer_store_dword v230, v244, s[16:19], s55 offen offset:384 sc1\n"
    "v_fma_f32 v232, v232, v92, -v94\n"
    "v_fma_f32 v233, v233, v93, -v95\n"
    "v_fma_f32 v234, v234, v92, -v94\n"
    "v_fma_f32 v235, v235, v93, -v95\n"
    "v_cvt_pk_bf16_f32 v232, v232, v233\n"
    "v_cvt_pk_bf16_f32 v234, v234, v235\n"
    "buffer_store_dword v232, v244, s[16:19], s55 offen offset:1280 sc1\n"
    "buffer_store_dword v234, v244, s[16:19], s55 offen offset:1408 sc1\n"
    "v_add_u32 v243, 512, v243\n"
    "s_add_u32 s71, s71, 98304\n"
    "s_add_u32 m0, s65, 24576\n"
    "s_add_u32 s72, s85, 0\n"
    "buffer_load_dwordx4 v88, s[20:23], s72 offen offset:0 nt lds\n"
    "buffer_load_dwordx4 v[16:19], v162, s[28:31], s57 offen offset:3200\n"
    "buffer_load_dwordx4 v[20:23], v162, s[28:31], s57 offen offset:256\n"
    "s_waitcnt vmcnt(38)\n"
    "v_add_co_u32 v238, vcc, 0x7f4a7c15, v238\n"
    "v_mov_b32 v159, 0x9e3779b9\n"
    "s_nop 0\n"
    "v_addc_co_u32 v239, vcc, v159, v239, vcc\n"
    "v_mov_b32 v246, 1\n"
    "s_waitcnt vmcnt(39)\n"
    "v_lshlrev_b32 v180, 16, v124\n"
    "v_and_b32 v181, 0xffff0000, v124\n"
    "v_lshlrev_b32 v182, 16, v125\n"
    "v_and_b32 v183, 0xffff0000, v125\n"
    "v_lshlrev_b32 v184, 16, v126\n"
    "v_and_b32 v185, 0xffff0000, v126\n"
    "v_lshlrev_b32 v186, 16, v127\n"
    "v_and_b32 v187, 0xffff0000, v127\n"
    "v_mul_f32 v180, s88, v180\n"
    "v_mul_f32 v181, s88, v181\n"
    "v_mul_f32 v182, s88, v182\n"
    "v_mul_f32 v183, s88, v183\n"
    "v_fmaak_f32 v184, v184, v180, 0x80730000\n"
    "v_fmaak_f32 v185, v185, v181, 0x80730000\n"
    "v_fmaak_f32 v186, v186, v182, 0x80730000\n"
    "v_fmaak_f32 v187, v187, v183, 0x80730000\n"
    "v_ldexp_f32 v180, v180, s90\n"
    "v_ldexp_f32 v181, v181, s90\n"
    "v_ldexp_f32 v182, v182, s90\n"
    "v_ldexp_f32 v183, v183, s90\n"
    "v_fma_f32 v180, v180, 0, v180\n"
    "v_fma_f32 v181, v181, 0, v181\n"
    "v_fma_f32 v182, v182, 0, v182\n"
    "v_fma_f32 v183, v183, 0, v183\n"
    "ds_write_b128 v131, v[180:183] offset:0\n"
    "ds_write_b128 v131, v[184:187] offset:1024\n"
    "ds_read_b128 v[180:183], v132 offset:0\n"
    "ds_read_b128 v[184:187], v132 offset:1024\n"
    "ds_read_b128 v[188:191], v132 offset:128\n"
    "ds_read_b128 v[192:195], v132 offset:1152\n"
    "ds_read_b128 v[196:199], v132 offset:256\n"
    "ds_read_b128 v[200:203], v132 offset:1280\n"
    "v_accvgpr_write_b32 a60, 0\n"
    "v_accvgpr_write_b32 a61, 0\n"
    "v_accvgpr_write_b32 a62, 0\n"
    "v_accvgpr_write_b32 a63, 0\n"
    "s_add_u32 m0, s65, 25600\n"
    "s_add_u32 s72, s85, 1024\n"
    "buffer_load_dwordx4 v89, s[20:23], s72 offen offset:0 nt lds\n"
    "buffer_load_dwordx4 v[24:27], v162, s[28:31], s57 offen offset:3328\n"
    "v_accvgpr_write_b32 a64, 0\n"
    "v_accvgpr_write_b32 a65, 0\n"
    "v_accvgpr_write_b32 a66, 0\n"
    "v_accvgpr_write_b32 a67, 0\n"
    "v_accvgpr_write_b32 a68, 0\n"
    "v_accvgpr_write_b32 a69, 0\n"
    "v_accvgpr_write_b32 a70, 0\n"
    "v_accvgpr_write_b32 a71, 0\n"
    "v_accvgpr_write_b32 a72, 0\n"
    "v_accvgpr_write_b32 a73, 0\n"
    "v_accvgpr_write_b32 a74, 0\n"
    "v_accvgpr_write_b32 a75, 0\n"
    "v_accvgpr_write_b32 a76, 0\n"
    "v_accvgpr_write_b32 a77, 0\n"
    "v_accvgpr_write_b32 a78, 0\n"
    "v_accvgpr_write_b32 a79, 0\n"
    "v_accvgpr_write_b32 a80, 0\n"
    "v_accvgpr_write_b32 a81, 0\n"
    "v_accvgpr_write_b32 a82, 0\n"
    "v_accvgpr_write_b32 a83, 0\n"
    "v_accvgpr_write_b32 a84, 0\n"
    "v_accvgpr_write_b32 a85, 0\n"
    "v_accvgpr_write_b32 a86, 0\n"
    "v_accvgpr_write_b32 a87, 0\n"
    "v_accvgpr_write_b32 a88, 0\n"
    "v_accvgpr_write_b32 a89, 0\n"
    "v_accvgpr_write_b32 a90, 0\n"
    "v_accvgpr_write_b32 a91, 0\n"
    "v_accvgpr_write_b32 a92, 0\n"
    "v_accvgpr_write_b32 a93, 0\n"
    "v_accvgpr_write_b32 a94, 0\n"
    "v_accvgpr_write_b32 a95, 0\n"
    "v_accvgpr_write_b32 a96, 0\n"
    "v_accvgpr_write_b32 a97, 0\n"
    "v_accvgpr_write_b32 a98, 0\n"
    "v_accvgpr_write_b32 a99, 0\n"
    "v_accvgpr_write_b32 a100, 0\n"
    "v_accvgpr_write_b32 a101, 0\n"
    "v_accvgpr_write_b32 a102, 0\n"
    "v_accvgpr_write_b32 a103, 0\n"
    "v_accvgpr_write_b32 a104, 0\n"
    "v_accvgpr_write_b32 a105, 0\n"
    "v_accvgpr_write_b32 a106, 0\n"
    "s_add_u32 m0, s65, 26624\n"
    "s_add_u32 s72, s85, 2048\n"
    "buffer_load_dwordx4 v90, s[20:23], s72 offen offset:0 nt lds\n"
    "buffer_load_dwordx4 v[100:103], v162, s[28:31], s66 offen offset:0\n"
    "buffer_load_dwordx4 v[104:107], v162, s[28:31], s66 offen offset:3072\n"
    "v_accvgpr_write_b32 a107, 0\n"
    "v_accvgpr_write_b32 a108, 0\n"
    "v_accvgpr_write_b32 a109, 0\n"
    "v_accvgpr_write_b32 a110, 0\n"
    "v_accvgpr_write_b32 a111, 0\n"
    "v_accvgpr_write_b32 a112, 0\n"
    "v_accvgpr_write_b32 a113, 0\n"
    "v_accvgpr_write_b32 a114, 0\n"
    "v_accvgpr_write_b32 a115, 0\n"
    "v_accvgpr_write_b32 a116, 0\n"
    "v_accvgpr_write_b32 a117, 0\n"
    "v_accvgpr_write_b32 a118, 0\n"
    "v_accvgpr_write_b32 a119, 0\n"
    "v_accvgpr_write_b32 a120, 0\n"
    "v_accvgpr_write_b32 a121, 0\n"
    "v_accvgpr_write_b32 a122, 0\n"
    "v_accvgpr_write_b32 a123, 0\n"
    "v_accvgpr_write_b32 a124, 0\n"
    "v_accvgpr_write_b32 a125, 0\n"
    "v_accvgpr_write_b32 a126, 0\n"
    "v_accvgpr_write_b32 a127, 0\n"
    "v_accvgpr_write_b32 a128, 0\n"
    "v_accvgpr_write_b32 a129, 0\n"
    "v_accvgpr_write_b32 a130, 0\n"
    "v_accvgpr_write_b32 a131, 0\n"
    "v_accvgpr_write_b32 a132, 0\n"
    "v_accvgpr_write_b32 a133, 0\n"
    "v_accvgpr_write_b32 a134, 0\n"
    "v_accvgpr_write_b32 a135, 0\n"
    "v_accvgpr_write_b32 a136, 0\n"
    "v_accvgpr_write_b32 a137, 0\n"
    "v_accvgpr_write_b32 a138, 0\n"
    "v_accvgpr_write_b32 a139, 0\n"
    "v_accvgpr_write_b32 a140, 0\n"
    "v_accvgpr_write_b32 a141, 0\n"
    "v_accvgpr_write_b32 a142, 0\n"
    "v_accvgpr_write_b32 a143, 0\n"
    "v_accvgpr_write_b32 a144, 0\n"
    "v_accvgpr_write_b32 a145, 0\n"
    "v_accvgpr_write_b32 a146, 0\n"
    "v_accvgpr_write_b32 a147, 0\n"
    "v_accvgpr_write_b32 a148, 0\n"
    "s_add_u32 m0, s65, 27648\n"
    "s_add_u32 s72, s85, 3072\n"
    "buffer_load_dwordx4 v88, s[20:23], s72 offen offset:0 nt lds\n"
    "buffer_load_dwordx4 v[108:111], v162, s[28:31], s66 offen offset:128\n"
    "v_accvgpr_write_b32 a149, 0\n"
    "v_accvgpr_write_b32 a150, 0\n"
    "v_accvgpr_write_b32 a151, 0\n"
    "v_accvgpr_write_b32 a152, 0\n"
    "v_accvgpr_write_b32 a153, 0\n"
    "v_accvgpr_write_b32 a154, 0\n"
    "v_accvgpr_write_b32 a155, 0\n"
    "v_accvgpr_write_b32 a156, 0\n"
    "v_accvgpr_write_b32 a157, 0\n"
    "v_accvgpr_write_b32 a158, 0\n"
    "v_accvgpr_write_b32 a159, 0\n"
    "v_accvgpr_write_b32 a160, 0\n"
    "v_accvgpr_write_b32 a161, 0\n"
    "v_accvgpr_write_b32 a162, 0\n"
    "v_accvgpr_write_b32 a163, 0\n"
    "v_accvgpr_write_b32 a164, 0\n"
    "v_accvgpr_write_b32 a165, 0\n"
    "v_accvgpr_write_b32 a166, 0\n"
    "v_accvgpr_write_b32 a167, 0\n"
    "v_accvgpr_write_b32 a168, 0\n"
    "v_accvgpr_write_b32 a169, 0\n"
    "v_accvgpr_write_b32 a170, 0\n"
    "v_accvgpr_write_b32 a171, 0\n"
    "v_accvgpr_write_b32 a172, 0\n"
    "v_accvgpr_write_b32 a173, 0\n"
    "v_accvgpr_write_b32 a174, 0\n"
    "v_accvgpr_write_b32 a175, 0\n"
    "v_accvgpr_write_b32 a176, 0\n"
    "v_accvgpr_write_b32 a177, 0\n"
    "v_accvgpr_write_b32 a178, 0\n"
    "v_accvgpr_write_b32 a179, 0\n"
    "v_accvgpr_write_b32 a180, 0\n"
    "v_accvgpr_write_b32 a181, 0\n"
    "v_accvgpr_write_b32 a182, 0\n"
    "v_accvgpr_write_b32 a183, 0\n"
    "v_accvgpr_write_b32 a184, 0\n"
    "v_accvgpr_write_b32 a185, 0\n"
    "v_accvgpr_write_b32 a186, 0\n"
    "v_accvgpr_write_b32 a187, 0\n"
    "v_accvgpr_write_b32 a188, 0\n"
    "v_accvgpr_write_b32 a189, 0\n"
    "v_accvgpr_write_b32 a190, 0\n"
    "v_accvgpr_write_b32 a191, 0\n"
    "s_waitcnt lgkmcnt(0)\n"
    "s_waitcnt vmcnt(10)\n"
    "ds_add_rtn_u32 v245, v247, v246\n"
    "s_lshl_b32 s73, s53, 6\n"
    "s_add_u32 s73, s73, 128\n"
    "s_waitcnt lgkmcnt(0)\n"
    "v_readfirstlane_b32 s74, v245\n"
    "s_nop 0\n"
    "s_cmp_eq_u32 s74, 3\n"
    "s_cselect_b32 s73, s73, 0x7fff0000\n"
    "buffer_store_dwordx2 v[238:239], v91, s[36:39], s73 offen offset:0 sc1\n"
    "s_waitcnt vmcnt(18)\n"
    "s_add_u32 m0, s65, 28672\n"
    "s_add_u32 s72, s85, 4096\n"
    "buffer_load_dwordx4 v89, s[20:23], s72 offen offset:0 nt lds\n"
    "buffer_load_dwordx4 v[112:115], v162, s[28:31], s66 offen offset:3200\n"
    "buffer_load_dwordx4 v[116:119], v162, s[28:31], s66 offen offset:256\n"
    "s_add_u32 s54, s71, s60\n"
    "s_add_u32 s55, s71, s61\n"
    "ds_read_b128 v[92:95], v243 offset:0\n"
    "s_waitcnt lgkmcnt(0)\n"
    "v_fma_f32 v36, v36, v92, -v94\n"
    "v_fma_f32 v37, v37, v93, -v95\n"
    "v_fma_f32 v38, v38, v92, -v94\n"
    "v_fma_f32 v39, v39, v93, -v95\n"
    "v_cvt_pk_bf16_f32 v36, v36, v37\n"
    "v_cvt_pk_bf16_f32 v38, v38, v39\n"
    "buffer_store_dword v36, v244, s[16:19], s54 offen offset:0 sc1\n"
    "buffer_store_dword v38, v244, s[16:19], s54 offen offset:128 sc1\n"
    "v_fma_f32 v40, v40, v92, -v94\n"
    "v_fma_f32 v41, v41, v93, -v95\n"
    "v_fma_f32 v42, v42, v92, -v94\n"
    "v_fma_f32 v43, v43, v93, -v95\n"
    "v_cvt_pk_bf16_f32 v40, v40, v41\n"
    "v_cvt_pk_bf16_f32 v42, v42, v43\n"
    "buffer_store_dword v40, v244, s[16:19], s54 offen offset:1024 sc1\n"
    "buffer_store_dword v42, v244, s[16:19], s54 offen offset:1152 sc1\n"
    "v_fma_f32 v44, v44, v92, -v94\n"
    "v_fma_f32 v45, v45, v93, -v95\n"
    "v_fma_f32 v46, v46, v92, -v94\n"
    "v_fma_f32 v47, v47, v93, -v95\n"
    "v_cvt_pk_bf16_f32 v44, v44, v45\n"
    "v_cvt_pk_bf16_f32 v46, v46, v47\n"
    "buffer_store_dword v44, v244, s[16:19], s54 offen offset:256 sc1\n"
    "buffer_store_dword v46, v244, s[16:19], s54 offen offset:384 sc1\n"
    "v_fma_f32 v48, v48, v92, -v94\n"
    "v_fma_f32 v49, v49, v93, -v95\n"
    "v_fma_f32 v50, v50, v92, -v94\n"
    "v_fma_f32 v51, v51, v93, -v95\n"
    "v_cvt_pk_bf16_f32 v48, v48, v49\n"
    "v_cvt_pk_bf16_f32 v50, v50, v51\n"
    "s_add_u32 m0, s65, 29696\n"
    "s_add_u32 s72, s85, 5120\n"
    "buffer_load_dwordx4 v90, s[20:23], s72 offen offset:0 nt lds\n"
    "buffer_load_dwordx4 v[120:123], v162, s[28:31], s66 offen offset:3328\n"
    "buffer_store_dword v48, v244, s[16:19], s54 offen offset:1280 sc1\n"
    "buffer_store_dword v50, v244, s[16:19], s54 offen offset:1408 sc1\n"
    "v_fma_f32 v52, v52, v92, -v94\n"
    "v_fma_f32 v53, v53, v93, -v95\n"
    "v_fma_f32 v54, v54, v92, -v94\n"
    "v_fma_f32 v55, v55, v93, -v95\n"
    "v_cvt_pk_bf16_f32 v52, v52, v53\n"
    "v_cvt_pk_bf16_f32 v54, v54, v55\n"
    "buffer_store_dword v52, v244, s[16:19], s55 offen offset:0 sc1\n"
    "buffer_store_dword v54, v244, s[16:19], s55 offen offset:128 sc1\n"
    "v_fma_f32 v56, v56, v92, -v94\n"
    "v_fma_f32 v57, v57, v93, -v95\n"
    "v_fma_f32 v58, v58, v92, -v94\n"
    "v_fma_f32 v59, v59, v93, -v95\n"
    "v_cvt_pk_bf16_f32 v56, v56, v57\n"
    "v_cvt_pk_bf16_f32 v58, v58, v59\n"
    "buffer_store_dword v56, v244, s[16:19], s55 offen offset:1024 sc1\n"
    "buffer_store_dword v58, v244, s[16:19], s55 offen offset:1152 sc1\n"
    "v_fma_f32 v60, v60, v92, -v94\n"
    "v_fma_f32 v61, v61, v93, -v95\n"
    "v_fma_f32 v62, v62, v92, -v94\n"
    "v_fma_f32 v63, v63, v93, -v95\n"
    "v_cvt_pk_bf16_f32 v60, v60, v61\n"
    "v_cvt_pk_bf16_f32 v62, v62, v63\n"
    "buffer_store_dword v60, v244, s[16:19], s55 offen offset:256 sc1\n"
    "buffer_store_dword v62, v244, s[16:19], s55 offen offset:384 sc1\n"
    "v_fma_f32 v64, v64, v92, -v94\n"
    "v_fma_f32 v65, v65, v93, -v95\n"
    "v_fma_f32 v66, v66, v92, -v94\n"
    "v_fma_f32 v67, v67, v93, -v95\n"
    "v_cvt_pk_bf16_f32 v64, v64, v65\n"
    "v_cvt_pk_bf16_f32 v66, v66, v67\n"
    "buffer_store_dword v64, v244, s[16:19], s55 offen offset:1280 sc1\n"
    "buffer_store_dword v66, v244, s[16:19], s55 offen offset:1408 sc1\n"
    "v_add_u32 v243, 512, v243\n"
    "s_add_u32 m0, s65, 73728\n"
    "s_add_u32 s72, s85, 6144\n"
    "buffer_load_dwordx4 v88, s[20:23], s72 offen offset:0 nt lds\n"
    "s_waitcnt vmcnt(56)\n"
    "v_lshlrev_b32 v204, 16, v136\n"
    "v_and_b32 v205, 0xffff0000, v136\n"
    "v_lshlrev_b32 v206, 16, v137\n"
    "v_and_b32 v207, 0xffff0000, v137\n"
    "v_lshlrev_b32 v208, 16, v138\n"
    "v_and_b32 v209, 0xffff0000, v138\n"
    "v_lshlrev_b32 v210, 16, v139\n"
    "v_and_b32 v211, 0xffff0000, v139\n"
    "v_log_f32 v68, v204\n"
    "v_log_f32 v69, v205\n"
    "v_log_f32 v70, v206\n"
    "v_log_f32 v71, v207\n"
    "v_log_f32 v72, v208\n"
    "v_log_f32 v73, v209\n"
    "v_log_f32 v74, v210\n"
    "v_log_f32 v75, v211\n"
    "v_rcp_f32 v204, v204\n"
    "v_rcp_f32 v205, v205\n"
    "v_rcp_f32 v206, v206\n"
    "v_rcp_f32 v207, v207\n"
    "v_rcp_f32 v208, v208\n"
    "v_rcp_f32 v209, v209\n"
    "v_rcp_f32 v210, v210\n"
    "v_rcp_f32 v211, v211\n"
    "s_mov_b32 s73, 0x41a00000\n"
    "v_mov_b32 v228, 0x7fc00000\n"
    "s_add_u32 m0, s65, 74752\n"
    "s_add_u32 s72, s85, 7168\n"
    "buffer_load_dwordx4 v89, s[20:23], s72 offen offset:0 nt lds\n"
    "v_cmp_le_f32 vcc, |v68|, s73\n"
    "s_nop 1\n"
    "v_cndmask_b32 v68, v228, v68, vcc\n"
    "v_cmp_le_f32 vcc, |v69|, s73\n"
    "s_nop 1\n"
    "v_cndmask_b32 v69, v228, v69, vcc\n"
    "v_cmp_le_f32 vcc, |v70|, s73\n"
    "s_nop 1\n"
    "v_cndmask_b32 v70, v228, v70, vcc\n"
    "v_cmp_le_f32 vcc, |v71|, s73\n"
    "s_nop 1\n"
    "v_cndmask_b32 v71, v228, v71, vcc\n"
    "v_cmp_le_f32 vcc, |v72|, s73\n"
    "s_nop 1\n"
    "v_cndmask_b32 v72, v228, v72, vcc\n"
    "v_cmp_le_f32 vcc, |v73|, s73\n"
    "s_nop 1\n"
    "v_cndmask_b32 v73, v228, v73, vcc\n"
    "v_cmp_le_f32 vcc, |v74|, s73\n"
    "s_nop 1\n"
    "v_cndmask_b32 v74, v228, v74, vcc\n"
    "v_cmp_le_f32 vcc, |v75|, s73\n"
    "s_nop 1\n"
    "v_cndmask_b32 v75, v228, v75, vcc\n"
    "v_cvt_pk_bf16_f32 v220, v204, v205\n"
    "v_cvt_pk_bf16_f32 v221, v206, v207\n"
    "v_cvt_pk_bf16_f32 v222, v208, v209\n"
    "v_cvt_pk_bf16_f32 v223, v210, v211\n"
    "v_lshlrev_b32 v212, 16, v220\n"
    "v_and_b32 v213, 0xffff0000, v220\n"
    "v_lshlrev_b32 v214, 16, v221\n"
    "v_and_b32 v215, 0xffff0000, v221\n"
    "v_lshlrev_b32 v216, 16, v222\n"
    "v_and_b32 v217, 0xffff0000, v222\n"
    "v_lshlrev_b32 v218, 16, v223\n"
    "s_add_u32 m0, s65, 75776\n"
    "s_add_u32 s72, s85, 8192\n"
    "buffer_load_dwordx4 v90, s[20:23], s72 offen offset:0 nt lds\n"
    "v_and_b32 v219, 0xffff0000, v223\n"
    "v_sub_f32 v204, v204, v212\n"
    "v_sub_f32 v205, v205, v213\n"
    "v_sub_f32 v206, v206, v214\n"
    "v_sub_f32 v207, v207, v215\n"
    "v_sub_f32 v208, v208, v216\n"
    "v_sub_f32 v209, v209, v217\n"
    "v_sub_f32 v210, v210, v218\n"
    "v_sub_f32 v211, v211, v219\n"
    "v_cvt_pk_bf16_f32 v224, v204, v205\n"
    "v_cvt_pk_bf16_f32 v225, v206, v207\n"
    "v_cvt_pk_bf16_f32 v226, v208, v209\n"
    "v_cvt_pk_bf16_f32 v227, v210, v211\n"
    "v_mov_b32 v204, 0\n"
    "v_mov_b32 v205, 0\n"
    "v_mov_b32 v206, 0\n"
    "v_mov_b32 v207, 0\n"
    "v_swap_b32 v221, v222\n"
    "v_swap_b32 v225, v226\n"
    "v_swap_b32 v141, v142\n"
    "ds_write2_b64 v144, v[220:221], v[222:223] offset0:32 offset1:40\n"
    "ds_write2_b64 v144, v[224:225], v[226:227] offset0:48 offset1:56\n"
    "ds_write2_b64 v144, v[140:141], v[142:143] offset0:64 offset1:72\n"
    "ds_write2_b64 v144, v[204:205], v[206:207] offset0:80 offset1:88\n"
    "s_waitcnt vmcnt(30)\n"
    "v_fma_f32 v4, v4, v180, -v184\n"
    "v_fma_f32 v5, v5, v181, -v185\n"
    "v_fma_f32 v6, v6, v182, -v186\n"
    "s_add_u32 m0, s65, 76800\n"
    "s_add_u32 s72, s85, 9216\n"
    "buffer_load_dwordx4 v88, s[20:23], s72 offen offset:0 nt lds\n"
    "v_fma_f32 v7, v7, v183, -v187\n"
    "v_fma_f32 v8, v8, v180, -v184\n"
    "v_fma_f32 v9, v9, v181, -v185\n"
    "v_fma_f32 v10, v10, v182, -v186\n"
    "v_fma_f32 v11, v11, v183, -v187\n"
    "v_cvt_pk_bf16_f32 v4, v4, v5\n"
    "v_cvt_pk_bf16_f32 v5, v6, v7\n"
    "v_cvt_pk_bf16_f32 v6, v8, v9\n"
    "v_cvt_pk_bf16_f32 v7, v10, v11\n"
    "ds_write_b64 v163, v[4:5] offset:0\n"
    "ds_write_b64 v163, v[6:7] offset:1024\n"
    "v_fma_f32 v12, v12, v188, -v192\n"
    "v_fma_f32 v13, v13, v189, -v193\n"
    "v_fma_f32 v14, v14, v190, -v194\n"
    "v_fma_f32 v15, v15, v191, -v195\n"
    "v_fma_f32 v16, v16, v188, -v192\n"
    "v_fma_f32 v17, v17, v189, -v193\n"
    "v_fma_f32 v18, v18, v190, -v194\n"
    "v_fma_f32 v19, v19, v191, -v195\n"
    "v_cvt_pk_bf16_f32 v12, v12, v13\n"
    "v_cvt_pk_bf16_f32 v13, v14, v15\n"
    "v_cvt_pk_bf16_f32 v14, v16, v17\n"
    "v_cvt_pk_bf16_f32 v15, v18, v19\n"
    "ds_write_b64 v163, v[12:13] offset:2048\n"
    "ds_write_b64 v163, v[14:15] offset:3072\n"
    "v_fma_f32 v20, v20, v196, -v200\n"
    "v_fma_f32 v21, v21, v197, -v201\n"
    "s_add_u32 m0, s65, 77824\n"
    "s_add_u32 s72, s85, 10240\n"
    "buffer_load_dwordx4 v89, s[20:23], s72 offen offset:0 nt lds\n"
    "v_fma_f32 v22, v22, v198, -v202\n"
    "v_fma_f32 v23, v23, v199, -v203\n"
    "v_fma_f32 v24, v24, v196, -v200\n"
    "v_fma_f32 v25, v25, v197, -v201\n"
    "v_fma_f32 v26, v26, v198, -v202\n"
    "v_fma_f32 v27, v27, v199, -v203\n"
    "v_cvt_pk_bf16_f32 v20, v20, v21\n"
    "v_cvt_pk_bf16_f32 v21, v22, v23\n"
    "v_cvt_pk_bf16_f32 v22, v24, v25\n"
    "v_cvt_pk_bf16_f32 v23, v26, v27\n"
    "ds_write_b64 v163, v[20:21] offset:4096\n"
    "ds_write_b64 v163, v[22:23] offset:5120\n"
    "s_waitcnt vmcnt(15)\n"
    "v_fma_f32 v100, v100, v180, -v184\n"
    "v_fma_f32 v101, v101, v181, -v185\n"
    "v_fma_f32 v102, v102, v182, -v186\n"
    "v_fma_f32 v103, v103, v183, -v187\n"
    "v_fma_f32 v104, v104, v180, -v184\n"
    "v_fma_f32 v105, v105, v181, -v185\n"
    "v_fma_f32 v106, v106, v182, -v186\n"
    "v_fma_f32 v107, v107, v183, -v187\n"
    "v_cvt_pk_bf16_f32 v100, v100, v101\n"
    "v_cvt_pk_bf16_f32 v101, v102, v103\n"
    "v_cvt_pk_bf16_f32 v102, v104, v105\n"
    "v_cvt_pk_bf16_f32 v103, v106, v107\n"
    "ds_write_b64 v163, v[100:101] offset:12288\n"
    "ds_write_b64 v163, v[102:103] offset:13312\n"
    "s_add_u32 m0, s65, 78848\n"
    "s_add_u32 s72, s85, 11264\n"
    "buffer_load_dwordx4 v90, s[20:23], s72 offen offset:0 nt lds\n"
    "v_fma_f32 v108, v108, v188, -v192\n"
    "v_fma_f32 v109, v109, v189, -v193\n"
    "v_fma_f32 v110, v110, v190, -v194\n"
    "v_fma_f32 v111, v111, v191, -v195\n"
    "v_fma_f32 v112, v112, v188, -v192\n"
    "v_fma_f32 v113, v113, v189, -v193\n"
    "v_fma_f32 v114, v114, v190, -v194\n"
    "v_fma_f32 v115, v115, v191, -v195\n"
    "v_cvt_pk_bf16_f32 v108, v108, v109\n"
    "v_cvt_pk_bf16_f32 v109, v110, v111\n"
    "v_cvt_pk_bf16_f32 v110, v112, v113\n"
    "v_cvt_pk_bf16_f32 v111, v114, v115\n"
    "ds_write_b64 v163, v[108:109] offset:14336\n"
    "ds_write_b64 v163, v[110:111] offset:15360\n"
    "v_fma_f32 v116, v116, v196, -v200\n"
    "v_fma_f32 v117, v117, v197, -v201\n"
    "v_fma_f32 v118, v118, v198, -v202\n"
    "v_fma_f32 v119, v119, v199, -v203\n"
    "v_fma_f32 v120, v120, v196, -v200\n"
    "v_fma_f32 v121, v121, v197, -v201\n"
    "v_fma_f32 v122, v122, v198, -v202\n"
    "v_fma_f32 v123, v123, v199, -v203\n"
    "v_cvt_pk_bf16_f32 v116, v116, v117\n"
    "v_cvt_pk_bf16_f32 v117, v118, v119\n"
    "v_cvt_pk_bf16_f32 v118, v120, v121\n"
    "v_cvt_pk_bf16_f32 v119, v122, v123\n"
    "ds_write_b64 v163, v[116:117] offset:16384\n"
    "ds_write_b64 v163, v[118:119] offset:17408\n"
    "s_waitcnt vmcnt(17)\n"
    "ds_read_b128 v[36:39], v154 offset:0\n"
    "ds_read_b128 v[40:43], v155 offset:0\n"
    "ds_read_b128 v[44:47], v154 offset:128\n"
    "ds_read_b128 v[48:51], v155 offset:128\n"
    "ds_read_b128 v[52:55], v154 offset:256\n"
    "ds_read_b128 v[56:59], v155 offset:256\n"
    "s_waitcnt lgkmcnt(0)\n"
    "s_barrier\n"
    "ds_read_b128 v[156:159], v2 offset:0\n"
    "ds_read_b128 v[160:163], v2 offset:2048\n"
    "ds_read_b128 v[164:167], v2 offset:4096\n"
    "ds_read_b128 v[168:171], v2 offset:6144\n"
    "ds_read_b128 v[172:175], v2 offset:8192\n"
    "ds_read_b128 v[176:179], v2 offset:10240\n"
    "ds_read_b128 v[180:183], v2 offset:1024\n"
    "ds_read_b128 v[184:187], v2 offset:3072\n"
    "ds_read_b128 v[188:191], v2 offset:5120\n"
    "ds_read_b128 v[192:195], v2 offset:7168\n"
    "ds_read_b128 v[196:199], v2 offset:9216\n"
    "ds_read_b128 v[200:203], v2 offset:11264\n"
    "s_add_u32 m0, s65, 49152\n"
    "s_add_u32 s72, s85, 12288\n"
    "buffer_load_dwordx4 v88, s[20:23], s72 offen offset:0 nt lds\n"
    "v_pk_maximum3_f16 v80, v36, v37, v80\n"
    "v_pk_maximum3_f16 v80, v38, v39, v80\n"
    "v_pk_add_u16 v36, s89, v36 clamp\n"
    "v_pk_add_u16 v37, s89, v37 clamp\n"
    "v_pk_add_u16 v38, s89, v38 clamp\n"
    "v_pk_add_u16 v39, s89, v39 clamp\n"
    "v_pk_maximum3_f16 v80, v40, v41, v80\n"
    "v_pk_maximum3_f16 v80, v42, v43, v80\n"
    "v_pk_add_u16 v40, s89, v40 clamp\n"
    "v_pk_add_u16 v41, s89, v41 clamp\n"
    "v_pk_add_u16 v42, s89, v42 clamp\n"
    "v_pk_add_u16 v43, s89, v43 clamp\n"
    "v_pk_maximum3_f16 v80, v44, v45, v80\n"
    "v_pk_maximum3_f16 v80, v46, v47, v80\n"
    "v_pk_add_u16 v44, s89, v44 clamp\n"
    "v_pk_add_u16 v45, s89, v45 clamp\n"
    "v_pk_add_u16 v46, s89, v46 clamp\n"
    "v_pk_add_u16 v47, s89, v47 clamp\n"
    "v_pk_maximum3_f16 v80, v48, v49, v80\n"
    "v_pk_maximum3_f16 v80, v50, v51, v80\n"
    "v_pk_add_u16 v48, s89, v48 clamp\n"
    "v_pk_add_u16 v49, s89, v49 clamp\n"
    "v_pk_add_u16 v50, s89, v50 clamp\n"
    "v_pk_add_u16 v51, s89, v51 clamp\n"
    "v_pk_maximum3_f16 v80, v52, v53, v80\n"
    "v_pk_maximum3_f16 v80, v54, v55, v80\n"
    "v_pk_add_u16 v52, s89, v52 clamp\n"
    "v_pk_add_u16 v53, s89, v53 clamp\n"
    "v_pk_add_u16 v54, s89, v54 clamp\n"
    "v_pk_add_u16 v55, s89, v55 clamp\n"
    "v_pk_maximum3_f16 v80, v56, v57, v80\n"
    "v_pk_maximum3_f16 v80, v58, v59, v80\n"
    "v_pk_add_u16 v56, s89, v56 clamp\n"
    "v_pk_add_u16 v57, s89, v57 clamp\n"
    "v_pk_add_u16 v58, s89, v58 clamp\n"
    "v_pk_add_u16 v59, s89, v59 clamp\n"
    "v_mov_b32 v108, 0\n"
    "v_mov_b32 v109, 0\n"
    "v_mov_b32 v110, 0\n"
    "v_mov_b32 v111, 0\n"
    "v_mov_b32 v112, 0\n"
    "v_mov_b32 v113, 0\n"
    "v_mov_b32 v114, 0\n"
    "v_mov_b32 v115, 0\n"
    "v_mov_b32 v116, 0\n"
    "v_mov_b32 v117, 0\n"
    "v_mov_b32 v118, 0\n"
    "v_mov_b32 v119, 0\n"
    "v_mov_b32 v120, 0\n"
    "v_mov_b32 v121, 0\n"
    "v_mov_b32 v122, 0\n"
    "v_mov_b32 v123, 0\n"
    "s_waitcnt lgkmcnt(11)\n"
    "v_mfma_f32_16x16x32_bf16 v[4:7], v[156:159], v[36:39], 0\n"
    "s_waitcnt lgkmcnt(10)\n"
    "v_mfma_f32_16x16x32_bf16 v[4:7], v[160:163], v[40:43], v[4:7]\n"
    "s_waitcnt lgkmcnt(9)\n"
    "v_mfma_f32_16x16x32_bf16 v[4:7], v[164:167], v[44:47], v[4:7]\n"
    "s_waitcnt lgkmcnt(8)\n"
    "v_mfma_f32_16x16x32_bf16 v[4:7], v[168:171], v[48:51], v[4:7]\n"
    "s_waitcnt lgkmcnt(7)\n"
    "v_mfma_f32_16x16x32_bf16 v[4:7], v[172:175], v[52:55], v[4:7]\n"
    "s_waitcnt lgkmcnt(6)\n"
    "v_mfma_f32_16x16x32_bf16 v[4:7], v[176:179], v[56:59], v[4:7]\n"
    "s_waitcnt lgkmcnt(5)\n"
    "v_mfma_f32_16x16x32_bf16 v[20:23], v[180:183], v[36:39], 0\n"
    "v_accvgpr_write_b32 a192, v36\n"
    "v_accvgpr_write_b32 a193, v37\n"
    "v_accvgpr_write_b32 a194, v38\n"
    "v_accvgpr_write_b32 a195, v39\n"
    "s_waitcnt lgkmcnt(4)\n"
    "v_mfma_f32_16x16x32_bf16 v[20:23], v[184:187], v[40:43], v[20:23]\n"
    "v_accvgpr_write_b32 a196, v40\n"
    "v_accvgpr_write_b32 a197, v41\n"
    "v_accvgpr_write_b32 a198, v42\n"
    "v_accvgpr_write_b32 a199, v43\n"
    "s_waitcnt lgkmcnt(3)\n"
    "v_mfma_f32_16x16x32_bf16 v[20:23], v[188:191], v[44:47], v[20:23]\n"
    "v_accvgpr_write_b32 a200, v44\n"
    "v_accvgpr_write_b32 a201, v45\n"
    "v_accvgpr_write_b32 a202, v46\n"
    "v_accvgpr_write_b32 a203, v47\n"
    "s_waitcnt lgkmcnt(2)\n"
    "v_mfma_f32_16x16x32_bf16 v[20:23], v[192:195], v[48:51], v[20:23]\n"
    "v_accvgpr_write_b32 a204, v48\n"
    "v_accvgpr_write_b32 a205, v49\n"
    "v_accvgpr_write_b32 a206, v50\n"
    "v_accvgpr_write_b32 a207, v51\n"
    "s_waitcnt lgkmcnt(1)\n"
    "v_mfma_f32_16x16x32_bf16 v[20:23], v[196:199], v[52:55], v[20:23]\n"
    "v_accvgpr_write_b32 a208, v52\n"
    "v_accvgpr_write_b32 a209, v53\n"
    "v_accvgpr_write_b32 a210, v54\n"
    "v_accvgpr_write_b32 a211, v55\n"
    "s_waitcnt lgkmcnt(0)\n"
    "v_mfma_f32_16x16x32_bf16 v[20:23], v[200:203], v[56:59], v[20:23]\n"
    "v_accvgpr_write_b32 a212, v56\n"
    "v_accvgpr_write_b32 a213, v57\n"
    "v_accvgpr_write_b32 a214, v58\n"
    "v_accvgpr_write_b32 a215, v59\n"
    "s_add_u32 m0, s65, 50176\n"
    "s_add_u32 s72, s85, 13312\n"
    "buffer_load_dwordx4 v89, s[20:23], s72 offen offset:0 nt lds\n"
    "v_mov_b32 v77, 155904\n"
    "v_max3_f32 v76, v4, v5, v6\n"
    "v_max3_f32 v76, v76, v7, v20\n"
    "v_max3_f32 v76, v76, v21, v22\n"
    "v_max_f32 v76, v76, v23\n"
    "v_mov_b32 v78, v76\n"
    "s_nop 1\n"
    "v_permlane32_swap_b32 v76, v78\n"
    "v_max_f32 v76, v76, v78\n"
    "v_mov_b32 v78, v76\n"
    "s_nop 1\n"
    "v_permlane16_swap_b32 v76, v78\n"
    "v_max_f32 v76, v76, v78\n"
    "ds_max_f32 v77, v76\n"
    "s_add_u32 m0, s65, 51200\n"
    "s_add_u32 s72, s85, 14336\n"
    "buffer_load_dwordx4 v90, s[20:23], s72 offen offset:0 nt lds\n"
    "buffer_load_dwordx2 v[236:237], v87, s[36:39], 0 offen offset:0 sc1\n"
    "s_waitcnt lgkmcnt(0)\n"
    "s_barrier\n"
    "ds_read_b32 v76, v77\n"
    "s_waitcnt vmcnt(10)\n"
    "buffer_store_dwordx2 v[238:239], v240, s[36:39], s80 offen offset:0 sc1\n"
    "s_lshl_b32 s73, s79, 8\n"
    "s_add_u32 s80, s80, s73\n"
    "s_mul_i32 s73, s79, 98304\n"
    "s_add_u32 s71, s71, s73\n"
    "s_waitcnt lgkmcnt(0)\n"
    "v_add_f32 v76, 0xc2b00000, v76\n"
    "v_sub_f32 v68, v68, v76\n"
    "v_sub_f32 v69, v69, v76\n"
    "v_sub_f32 v70, v70, v76\n"
    "v_sub_f32 v71, v71, v76\n"
    "v_sub_f32 v72, v72, v76\n"
    "v_sub_f32 v73, v73, v76\n"
    "v_sub_f32 v74, v74, v76\n"
    "v_sub_f32 v75, v75, v76\n"
    "ds_write_b128 v86, v[68:71] offset:0\n"
    "ds_write_b128 v86, v[72:75] offset:16\n"
    "s_mul_i32 s73, s79, 768\n"
    "v_add_u32 v77, s73, v250\n"
    "s_add_u32 m0, s65, 52224\n"
    "s_add_u32 s72, s85, 15360\n"
    "buffer_load_dwordx4 v88, s[20:23], s72 offen offset:0 nt lds\n"
    "s_waitcnt lgkmcnt(0)\n"
    "s_barrier\n"
    "ds_read_b128 v[100:103], v77 offset:0\n"
    "ds_read_b128 v[104:107], v77 offset:16\n"
    "s_add_u32 m0, s65, 53248\n"
    "s_add_u32 s72, s85, 16384\n"
    "buffer_load_dwordx4 v89, s[20:23], s72 offen offset:0 nt lds\n"
    "s_waitcnt lgkmcnt(1)\n"
    "v_add_f32 v4, v4, v100\n"
    "v_add_f32 v5, v5, v101\n"
    "v_add_f32 v6, v6, v102\n"
    "v_add_f32 v7, v7, v103\n"
    "s_waitcnt lgkmcnt(0)\n"
    "v_add_f32 v20, v20, v104\n"
    "v_add_f32 v21, v21, v105\n"
    "v_add_f32 v22, v22, v106\n"
    "v_add_f32 v23, v23, v107\n"
    "v_exp_f32 v4, v4\n"
    "v_exp_f32 v5, v5\n"
    "v_exp_f32 v6, v6\n"
    "v_exp_f32 v7, v7\n"
    "v_exp_f32 v20, v20\n"
    "v_exp_f32 v21, v21\n"
    "v_exp_f32 v22, v22\n"
    "v_exp_f32 v23, v23\n"
    "s_add_u32 m0, s65, 54272\n"
    "s_add_u32 s72, s85, 17408\n"
    "buffer_load_dwordx4 v90, s[20:23], s72 offen offset:0 nt lds\n"
    "s_waitcnt vmcnt(8)\n"
    "ds_read_b128 v[56:59], v85 offset:24832\n"
    "ds_read_b128 v[52:55], v84 offset:24832\n"
    "ds_read_b128 v[48:51], v85 offset:24704\n"
    "ds_read_b128 v[44:47], v84 offset:24704\n"
    "ds_read_b128 v[40:43], v85 offset:24576\n"
    "ds_read_b128 v[36:39], v84 offset:24576\n"
    "s_waitcnt lgkmcnt(5)\n"
    "v_pk_maximum3_f16 v80, v56, v57, v80\n"
    "v_pk_maximum3_f16 v80, v58, v59, v80\n"
    "v_pk_add_u16 v56, s89, v56 clamp\n"
    "v_pk_add_u16 v57, s89, v57 clamp\n"
    "v_pk_add_u16 v58, s89, v58 clamp\n"
    "v_pk_add_u16 v59, s89, v59 clamp\n"
    "s_waitcnt lgkmcnt(4)\n"
    "v_pk_maximum3_f16 v80, v52, v53, v80\n"
    "v_pk_maximum3_f16 v80, v54, v55, v80\n"
    "v_pk_add_u16 v52, s89, v52 clamp\n"
    "v_pk_add_u16 v53, s89, v53 clamp\n"
    "v_pk_add_u16 v54, s89, v54 clamp\n"
    "v_pk_add_u16 v55, s89, v55 clamp\n"
    "v_mfma_f32_16x16x32_bf16 v[8:11], v[176:179], v[56:59], v[100:103]\n"
    "s_waitcnt lgkmcnt(3)\n"
    "v_pk_maximum3_f16 v80, v48, v49, v80\n"
    "v_pk_maximum3_f16 v80, v50, v51, v80\n"
    "v_pk_add_u16 v48, s89, v48 clamp\n"
    "v_pk_add_u16 v49, s89, v49 clamp\n"
    "v_pk_add_u16 v50, s89, v50 clamp\n"
    "v_pk_add_u16 v51, s89, v51 clamp\n"
    "v_mfma_f32_16x16x32_bf16 v[8:11], v[172:175], v[52:55], v[8:11]\n"
    "s_waitcnt lgkmcnt(2)\n"
    "v_pk_maximum3_f16 v80, v44, v45, v80\n"
    "v_pk_maximum3_f16 v80, v46, v47, v80\n"
    "v_pk_add_u16 v44, s89, v44 clamp\n"
    "v_pk_add_u16 v45, s89, v45 clamp\n"
    "v_pk_add_u16 v46, s89, v46 clamp\n"
    "v_pk_add_u16 v47, s89, v47 clamp\n"
    "v_mfma_f32_16x16x32_bf16 v[8:11], v[168:171], v[48:51], v[8:11]\n"
    "s_waitcnt lgkmcnt(1)\n"
    "v_pk_maximum3_f16 v80, v40, v41, v80\n"
    "v_pk_maximum3_f16 v80, v42, v43, v80\n"
    "v_pk_add_u16 v40, s89, v40 clamp\n"
    "v_pk_add_u16 v41, s89, v41 clamp\n"
    "v_pk_add_u16 v42, s89, v42 clamp\n"
    "v_pk_add_u16 v43, s89, v43 clamp\n"
    "v_mfma_f32_16x16x32_bf16 v[8:11], v[164:167], v[44:47], v[8:11]\n"
    "s_waitcnt lgkmcnt(0)\n"
    "v_pk_maximum3_f16 v80, v36, v37, v80\n"
    "v_pk_maximum3_f16 v80, v38, v39, v80\n"
    "v_pk_add_u16 v36, s89, v36 clamp\n"
    "v_pk_add_u16 v37, s89, v37 clamp\n"
    "v_pk_add_u16 v38, s89, v38 clamp\n"
    "v_pk_add_u16 v39, s89, v39 clamp\n"
    "v_mfma_f32_16x16x32_bf16 v[8:11], v[160:163], v[40:43], v[8:11]\n"
    "s_nop 0\n"
    "v_mfma_f32_16x16x32_bf16 v[8:11], v[156:159], v[36:39], v[8:11]\n"
    "v_mfma_f32_16x16x32_bf16 v[24:27], v[200:203], v[56:59], v[104:107]\n"
    "v_accvgpr_write_b32 a236, v56\n"
    "v_accvgpr_write_b32 a237, v57\n"
    "v_accvgpr_write_b32 a238, v58\n"
    "v_accvgpr_write_b32 a239, v59\n"
    "v_mfma_f32_16x16x32_bf16 v[24:27], v[196:199], v[52:55], v[24:27]\n"
    "v_accvgpr_write_b32 a232, v52\n"
    "v_accvgpr_write_b32 a233, v53\n"
    "v_accvgpr_write_b32 a234, v54\n"
    "v_accvgpr_write_b32 a235, v55\n"
    "v_mfma_f32_16x16x32_bf16 v[24:27], v[192:195], v[48:51], v[24:27]\n"
    "v_accvgpr_write_b32 a228, v48\n"
    "v_accvgpr_write_b32 a229, v49\n"
    "v_accvgpr_write_b32 a230, v50\n"
    "v_accvgpr_write_b32 a231, v51\n"
    "v_mfma_f32_16x16x32_bf16 v[24:27], v[188:191], v[44:47], v[24:27]\n"
    "v_accvgpr_write_b32 a224, v44\n"
    "v_accvgpr_write_b32 a225, v45\n"
    "v_accvgpr_write_b32 a226, v46\n"
    "v_accvgpr_write_b32 a227, v47\n"
    "v_mfma_f32_16x16x32_bf16 v[24:27], v[184:187], v[40:43], v[24:27]\n"
    "v_accvgpr_write_b32 a220, v40\n"
    "v_accvgpr_write_b32 a221, v41\n"
    "v_accvgpr_write_b32 a222, v42\n"
    "v_accvgpr_write_b32 a223, v43\n"
    "v_mfma_f32_16x16x32_bf16 v[24:27], v[180:183], v[36:39], v[24:27]\n"
    "v_accvgpr_write_b32 a216, v36\n"
    "v_accvgpr_write_b32 a217, v37\n"
    "v_accvgpr_write_b32 a218, v38\n"
    "v_accvgpr_write_b32 a219, v39\n"
    "s_add_u32 m0, s65, 98304\n"
    "s_add_u32 s72, s85, 18432\n"
    "buffer_load_dwordx4 v88, s[20:23], s72 offen offset:0 nt lds\n"
    "v_exp_f32 v8, v8\n"
    "v_exp_f32 v9, v9\n"
    "v_exp_f32 v10, v10\n"
    "v_exp_f32 v11, v11\n"
    "v_exp_f32 v24, v24\n"
    "v_exp_f32 v25, v25\n"
    "v_exp_f32 v26, v26\n"
    "v_exp_f32 v27, v27\n"
    "s_add_u32 m0, s65, 99328\n"
    "s_add_u32 s72, s85, 19456\n"
    "buffer_load_dwordx4 v89, s[20:23], s72 offen offset:0 nt lds\n"
    "s_add_u32 m0, s65, 100352\n"
    "s_add_u32 s72, s85, 20480\n"
    "buffer_load_dwordx4 v90, s[20:23], s72 offen offset:0 nt lds\n"
    "s_waitcnt vmcnt(7)\n"
    "v_cmp_eq_u64 s[54:55], v[236:237], v[238:239]\n"
    "s_nop 0\n"
    "s_cmp_eq_u64 s[54:55], -1\n"
    "s_cbranch_scc0 .LpollAK\n"
    ".LpollAKr:\n"
    "s_add_u32 m0, s65, 101376\n"
    "s_add_u32 s72, s85, 21504\n"
    "buffer_load_dwordx4 v88, s[20:23], s72 offen offset:0 nt lds\n"
    "s_add_u32 m0, s65, 24576\n"
    "s_add_u32 s72, s65, 49152\n"
    "buffer_load_dwordx4 v1, s[16:19], s72 offen offset:0 sc1 lds\n"
    "s_add_u32 m0, s65, 25600\n"
    "s_add_u32 s72, s65, 50176\n"
    "buffer_load_dwordx4 v1, s[16:19], s72 offen offset:0 sc1 lds\n"
    "s_add_u32 m0, s65, 102400\n"
    "s_add_u32 s72, s85, 22528\n"
    "buffer_load_dwordx4 v89, s[20:23], s72 offen offset:0 nt lds\n"
    "s_add_u32 m0, s65, 26624\n"
    "s_add_u32 s72, s65, 51200\n"
    "buffer_load_dwordx4 v1, s[16:19], s72 offen offset:0 sc1 lds\n"
    "s_add_u32 m0, s65, 27648\n"
    "s_add_u32 s72, s65, 52224\n"
    "buffer_load_dwordx4 v1, s[16:19], s72 offen offset:0 sc1 lds\n"
    "s_add_u32 m0, s65, 103424\n"
    "s_add_u32 s72, s85, 23552\n"
    "buffer_load_dwordx4 v90, s[20:23], s72 offen offset:0 nt lds\n"
    "s_add_u32 m0, s65, 28672\n"
    "s_add_u32 s72, s65, 53248\n"
    "buffer_load_dwordx4 v1, s[16:19], s72 offen offset:0 sc1 lds\n"
    "s_add_u32 m0, s65, 29696\n"
    "s_add_u32 s72, s65, 54272\n"
    "buffer_load_dwordx4 v1, s[16:19], s72 offen offset:0 sc1 lds\n"
    "s_add_u32 m0, s65, 73728\n"
    "s_add_u32 s72, s65, 73728\n"
    "buffer_load_dwordx4 v1, s[16:19], s72 offen offset:0 sc1 lds\n"
    "s_add_u32 m0, s65, 74752\n"
    "s_add_u32 s72, s65, 74752\n"
    "buffer_load_dwordx4 v1, s[16:19], s72 offen offset:0 sc1 lds\n"
    "s_add_u32 m0, s65, 75776\n"
    "s_add_u32 s72, s65, 75776\n"
    "buffer_load_dwordx4 v1, s[16:19], s72 offen offset:0 sc1 lds\n"
    "s_add_u32 m0, s65, 76800\n"
    "s_add_u32 s72, s65, 76800\n"
    "buffer_load_dwordx4 v1, s[16:19], s72 offen offset:0 sc1 lds\n"
    "s_add_u32 m0, s65, 77824\n"
    "s_add_u32 s72, s65, 77824\n"
    "buffer_load_dwordx4 v1, s[16:19], s72 offen offset:0 sc1 lds\n"
    "s_add_u32 m0, s65, 78848\n"
    "s_add_u32 s72, s65, 78848\n"
    "buffer_load_dwordx4 v1, s[16:19], s72 offen offset:0 sc1 lds\n"
    "s_bitcmp1_b32 s83, 1\n"
    "s_cbranch_scc1 .Lf3b\n"
    "s_waitcnt vmcnt(18)\n"
    "ds_read_b128 v[36:39], v84 offset:0\n"
    "ds_read_b128 v[40:43], v85 offset:0\n"
    "ds_read_b128 v[44:47], v84 offset:128\n"
    "ds_read_b128 v[48:51], v85 offset:128\n"
    "ds_read_b128 v[124:127], v84 offset:256\n"
    "ds_read_b128 v[128:131], v85 offset:256\n"
    "s_waitcnt lgkmcnt(5)\n"
    "v_pk_maximum3_f16 v80, v36, v37, v80\n"
    "v_pk_maximum3_f16 v80, v38, v39, v80\n"
    "v_pk_add_u16 v36, s89, v36 clamp\n"
    "v_pk_add_u16 v37, s89, v37 clamp\n"
    "v_pk_add_u16 v38, s89, v38 clamp\n"
    "v_pk_add_u16 v39, s89, v39 clamp\n"
    "s_waitcnt lgkmcnt(4)\n"
    "v_pk_maximum3_f16 v80, v40, v41, v80\n"
    "v_pk_maximum3_f16 v80, v42, v43, v80\n"
    "v_pk_add_u16 v40, s89, v40 clamp\n"
    "v_pk_add_u16 v41, s89, v41 clamp\n"
    "v_pk_add_u16 v42, s89, v42 clamp\n"
    "v_pk_add_u16 v43, s89, v43 clamp\n"
    "v_mfma_f32_16x16x32_bf16 v[12:15], v[156:159], v[36:39], v[100:103]\n"
    "s_waitcnt lgkmcnt(3)\n"
    "v_pk_maximum3_f16 v80, v44, v45, v80\n"
    "v_pk_maximum3_f16 v80, v46, v47, v80\n"
    "v_pk_add_u16 v44, s89, v44 clamp\n"
    "v_pk_add_u16 v45, s89, v45 clamp\n"
    "v_pk_add_u16 v46, s89, v46 clamp\n"
    "v_pk_add_u16 v47, s89, v47 clamp\n"
    "v_mfma_f32_16x16x32_bf16 v[12:15], v[160:163], v[40:43], v[12:15]\n"
    "s_waitcnt lgkmcnt(2)\n"
    "v_pk_maximum3_f16 v80, v48, v49, v80\n"
    "v_pk_maximum3_f16 v80, v50, v51, v80\n"
    "v_pk_add_u16 v48, s89, v48 clamp\n"
    "v_pk_add_u16 v49, s89, v49 clamp\n"
    "v_pk_add_u16 v50, s89, v50 clamp\n"
    "v_pk_add_u16 v51, s89, v51 clamp\n"
    "v_mfma_f32_16x16x32_bf16 v[12:15], v[164:167], v[44:47], v[12:15]\n"
    "s_waitcnt lgkmcnt(1)\n"
    "v_pk_maximum3_f16 v80, v124, v125, v80\n"
    "v_pk_maximum3_f16 v80, v126, v127, v80\n"
    "v_pk_add_u16 v124, s89, v124 clamp\n"
    "v_pk_add_u16 v125, s89, v125 clamp\n"
    "v_pk_add_u16 v126, s89, v126 clamp\n"
    "v_pk_add_u16 v127, s89, v127 clamp\n"
    "v_mfma_f32_16x16x32_bf16 v[12:15], v[168:171], v[48:51], v[12:15]\n"
    "s_waitcnt lgkmcnt(0)\n"
    "v_pk_maximum3_f16 v80, v128, v129, v80\n"
    "v_pk_maximum3_f16 v80, v130, v131, v80\n"
    "v_pk_add_u16 v128, s89, v128 clamp\n"
    "v_pk_add_u16 v129, s89, v129 clamp\n"
    "v_pk_add_u16 v130, s89, v130 clamp\n"
    "v_pk_add_u16 v131, s89, v131 clamp\n"
    "v_mfma_f32_16x16x32_bf16 v[12:15], v[172:175], v[124:127], v[12:15]\n"
    "s_nop 0\n"
    "v_mfma_f32_16x16x32_bf16 v[12:15], v[176:179], v[128:131], v[12:15]\n"
    "v_mfma_f32_16x16x32_bf16 v[28:31], v[180:183], v[36:39], v[104:107]\n"
    "v_accvgpr_write_b32 a240, v36\n"
    "v_accvgpr_write_b32 a241, v37\n"
    "v_accvgpr_write_b32 a242, v38\n"
    "v_accvgpr_write_b32 a243, v39\n"
    "v_mfma_f32_16x16x32_bf16 v[28:31], v[184:187], v[40:43], v[28:31]\n"
    "v_accvgpr_write_b32 a244, v40\n"
    "v_accvgpr_write_b32 a245, v41\n"
    "v_accvgpr_write_b32 a246, v42\n"
    "v_accvgpr_write_b32 a247, v43\n"
    "v_mfma_f32_16x16x32_bf16 v[28:31], v[188:191], v[44:47], v[28:31]\n"
    "v_accvgpr_write_b32 a248, v44\n"
    "v_accvgpr_write_b32 a249, v45\n"
    "v_accvgpr_write_b32 a250, v46\n"
    "v_accvgpr_write_b32 a251, v47\n"
    "v_mfma_f32_16x16x32_bf16 v[28:31], v[192:195], v[48:51], v[28:31]\n"
    "v_accvgpr_write_b32 a252, v48\n"
    "v_accvgpr_write_b32 a253, v49\n"
    "v_accvgpr_write_b32 a254, v50\n"
    "v_accvgpr_write_b32 a255, v51\n"
    "v_mfma_f32_16x16x32_bf16 v[28:31], v[196:199], v[124:127], v[28:31]\n"
    "v_mfma_f32_16x16x32_bf16 v[28:31], v[200:203], v[128:131], v[28:31]\n"
    "v_exp_f32 v12, v12\n"
    "v_exp_f32 v13, v13\n"
    "v_exp_f32 v14, v14\n"
    "v_exp_f32 v15, v15\n"
    "s_nop 3\n"
    "v_exp_f32 v28, v28\n"
    "v_exp_f32 v29, v29\n"
    "v_exp_f32 v30, v30\n"
    "v_exp_f32 v31, v31\n"
    "s_waitcnt vmcnt(8)\n"
    "ds_read_b128 v[152:155], v85 offset:49408\n"
    "ds_read_b128 v[148:151], v84 offset:49408\n"
    "ds_read_b128 v[144:147], v85 offset:49280\n"
    "ds_read_b128 v[140:143], v84 offset:49280\n"
    "ds_read_b128 v[136:139], v85 offset:49152\n"
    "ds_read_b128 v[132:135], v84 offset:49152\n"
    "s_waitcnt lgkmcnt(5)\n"
    "v_pk_maximum3_f16 v80, v152, v153, v80\n"
    "v_pk_maximum3_f16 v80, v154, v155, v80\n"
    "v_pk_add_u16 v152, s89, v152 clamp\n"
    "v_pk_add_u16 v153, s89, v153 clamp\n"
    "v_pk_add_u16 v154, s89, v154 clamp\n"
    "v_pk_add_u16 v155, s89, v155 clamp\n"
    "s_waitcnt lgkmcnt(4)\n"
    "v_pk_maximum3_f16 v80, v148, v149, v80\n"
    "v_pk_maximum3_f16 v80, v150, v151, v80\n"
    "v_pk_add_u16 v148, s89, v148 clamp\n"
    "v_pk_add_u16 v149, s89, v149 clamp\n"
    "v_pk_add_u16 v150, s89, v150 clamp\n"
    "v_pk_add_u16 v151, s89, v151 clamp\n"
    "v_mfma_f32_16x16x32_bf16 v[16:19], v[176:179], v[152:155], v[100:103]\n"
    "s_waitcnt lgkmcnt(3)\n"
    "v_pk_maximum3_f16 v80, v144, v145, v80\n"
    "v_pk_maximum3_f16 v80, v146, v147, v80\n"
    "v_pk_add_u16 v144, s89, v144 clamp\n"
    "v_pk_add_u16 v145, s89, v145 clamp\n"
    "v_pk_add_u16 v146, s89, v146 clamp\n"
    "v_pk_add_u16 v147, s89, v147 clamp\n"
    "v_mfma_f32_16x16x32_bf16 v[16:19], v[172:175], v[148:151], v[16:19]\n"
    "s_waitcnt lgkmcnt(2)\n"
    "v_pk_maximum3_f16 v80, v140, v141, v80\n"
    "v_pk_maximum3_f16 v80, v142, v143, v80\n"
    "v_pk_add_u16 v140, s89, v140 clamp\n"
    "v_pk_add_u16 v141, s89, v141 clamp\n"
    "v_pk_add_u16 v142, s89, v142 clamp\n"
    "v_pk_add_u16 v143, s89, v143 clamp\n"
    "v_mfma_f32_16x16x32_bf16 v[16:19], v[168:171], v[144:147], v[16:19]\n"
    "s_waitcnt lgkmcnt(1)\n"
    "v_pk_maximum3_f16 v80, v136, v137, v80\n"
    "v_pk_maximum3_f16 v80, v138, v139, v80\n"
    "v_pk_add_u16 v136, s89, v136 clamp\n"
    "v_pk_add_u16 v137, s89, v137 clamp\n"
    "v_pk_add_u16 v138, s89, v138 clamp\n"
    "v_pk_add_u16 v139, s89, v139 clamp\n"
    "v_mfma_f32_16x16x32_bf16 v[16:19], v[164:167], v[140:143], v[16:19]\n"
    "s_waitcnt lgkmcnt(0)\n"
    "v_pk_maximum3_f16 v80, v132, v133, v80\n"
    "v_pk_maximum3_f16 v80, v134, v135, v80\n"
    "v_pk_add_u16 v132, s89, v132 clamp\n"
    "v_pk_add_u16 v133, s89, v133 clamp\n"
    "v_pk_add_u16 v134, s89, v134 clamp\n"
    "v_pk_add_u16 v135, s89, v135 clamp\n"
    "v_mfma_f32_16x16x32_bf16 v[16:19], v[160:163], v[136:139], v[16:19]\n"
    "s_nop 0\n"
    "v_mfma_f32_16x16x32_bf16 v[16:19], v[156:159], v[132:135], v[16:19]\n"
    "v_mfma_f32_16x16x32_bf16 v[32:35], v[200:203], v[152:155], v[104:107]\n"
    "v_mfma_f32_16x16x32_bf16 v[32:35], v[196:199], v[148:151], v[32:35]\n"
    "v_mfma_f32_16x16x32_bf16 v[32:35], v[192:195], v[144:147], v[32:35]\n"
    "v_mfma_f32_16x16x32_bf16 v[32:35], v[188:191], v[140:143], v[32:35]\n"
    "v_mfma_f32_16x16x32_bf16 v[32:35], v[184:187], v[136:139], v[32:35]\n"
    "v_mfma_f32_16x16x32_bf16 v[32:35], v[180:183], v[132:135], v[32:35]\n"
    "ds_read_b128 v[156:159], v2 offset:12288\n"
    "ds_read_b128 v[160:163], v2 offset:14336\n"
    "ds_read_b128 v[164:167], v2 offset:16384\n"
    "ds_read_b128 v[168:171], v2 offset:18432\n"
    "ds_read_b128 v[172:175], v2 offset:20480\n"
    "ds_read_b128 v[176:179], v2 offset:22528\n"
    "ds_read_b128 v[100:103], v77 offset:128\n"
    "v_pk_add_u16 v80, s89, v80\n"
    "v_and_b32 v80, 0x80008000, v80\n"
    "v_cmp_ne_u32 s[34:35], 0, v80\n"
    "v_exp_f32 v16, v16\n"
    "v_exp_f32 v17, v17\n"
    "v_exp_f32 v18, v18\n"
    "v_exp_f32 v19, v19\n"
    "v_exp_f32 v32, v32\n"
    "v_exp_f32 v33, v33\n"
    "v_exp_f32 v34, v34\n"
    "v_exp_f32 v35, v35\n"
    "s_mov_b32 s66, 0\n"
    "s_waitcnt lgkmcnt(0)\n"
    "s_waitcnt vmcnt(0)\n"
    "ds_add_u32 v247, v246\n"
    "s_nop 7\n"
    "s_branch .LPK\n"
    "s_branch .Lf3e\n"
    ".Lf3b:\n"
    "s_add_u32 s75, s71, s63\n"
    "s_add_u32 s76, s75, 6144\n"
    "s_add_u32 s77, s75, 24576\n"
    "s_add_u32 s78, s75, 30720\n"
    "buffer_load_dwordx2 v[204:205], v242, s[28:31], s75 offen offset:0 nt\n"
    "buffer_load_dwordx2 v[206:207], v242, s[28:31], s75 offen offset:768 nt\n"
    "buffer_load_dwordx2 v[208:209], v242, s[28:31], s75 offen offset:3072 nt\n"
    "buffer_load_dwordx2 v[210:211], v242, s[28:31], s75 offen offset:3840 nt\n"
    "buffer_load_dwordx2 v[212:213], v242, s[28:31], s76 offen offset:0 nt\n"
    "buffer_load_dwordx2 v[214:215], v242, s[28:31], s76 offen offset:768 nt\n"
    "buffer_load_dwordx2 v[216:217], v242, s[28:31], s76 offen offset:3072 nt\n"
    "buffer_load_dwordx2 v[218:219], v242, s[28:31], s76 offen offset:3840 nt\n"
    "buffer_load_dwordx2 v[220:221], v242, s[28:31], s77 offen offset:0 nt\n"
    "buffer_load_dwordx2 v[222:223], v242, s[28:31], s77 offen offset:768 nt\n"
    "buffer_load_dwordx2 v[224:225], v242, s[28:31], s77 offen offset:3072 nt\n"
    "buffer_load_dwordx2 v[226:227], v242, s[28:31], s77 offen offset:3840 nt\n"
    "buffer_load_dwordx2 v[228:229], v242, s[28:31], s78 offen offset:0 nt\n"
    "buffer_load_dwordx2 v[230:231], v242, s[28:31], s78 offen offset:768 nt\n"
    "buffer_load_dwordx2 v[232:233], v242, s[28:31], s78 offen offset:3072 nt\n"
    "buffer_load_dwordx2 v[234:235], v242, s[28:31], s78 offen offset:3840 nt\n"
    "s_waitcnt vmcnt(34)\n"
    "ds_read_b128 v[36:39], v84 offset:0\n"
    "ds_read_b128 v[40:43], v85 offset:0\n"
    "ds_read_b128 v[44:47], v84 offset:128\n"
    "ds_read_b128 v[48:51], v85 offset:128\n"
    "ds_read_b128 v[124:127], v84 offset:256\n"
    "ds_read_b128 v[128:131], v85 offset:256\n"
    "s_waitcnt lgkmcnt(5)\n"
    "v_pk_maximum3_f16 v80, v36, v37, v80\n"
    "v_pk_maximum3_f16 v80, v38, v39, v80\n"
    "v_pk_add_u16 v36, s89, v36 clamp\n"
    "v_pk_add_u16 v37, s89, v37 clamp\n"
    "v_pk_add_u16 v38, s89, v38 clamp\n"
    "v_pk_add_u16 v39, s89, v39 clamp\n"
    "s_waitcnt lgkmcnt(4)\n"
    "v_pk_maximum3_f16 v80, v40, v41, v80\n"
    "v_pk_maximum3_f16 v80, v42, v43, v80\n"
    "v_pk_add_u16 v40, s89, v40 clamp\n"
    "v_pk_add_u16 v41, s89, v41 clamp\n"
    "v_pk_add_u16 v42, s89, v42 clamp\n"
    "v_pk_add_u16 v43, s89, v43 clamp\n"
    "v_mfma_f32_16x16x32_bf16 v[12:15], v[156:159], v[36:39], v[100:103]\n"
    "s_waitcnt lgkmcnt(3)\n"
    "v_pk_maximum3_f16 v80, v44, v45, v80\n"
    "v_pk_maximum3_f16 v80, v46, v47, v80\n"
    "v_pk_add_u16 v44, s89, v44 clamp\n"
    "v_pk_add_u16 v45, s89, v45 clamp\n"
    "v_pk_add_u16 v46, s89, v46 clamp\n"
    "v_pk_add_u16 v47, s89, v47 clamp\n"
    "v_mfma_f32_16x16x32_bf16 v[12:15], v[160:163], v[40:43], v[12:15]\n"
    "s_waitcnt lgkmcnt(2)\n"
    "v_pk_maximum3_f16 v80, v48, v49, v80\n"
    "v_pk_maximum3_f16 v80, v50, v51, v80\n"
    "v_pk_add_u16 v48, s89, v48 clamp\n"
    "v_pk_add_u16 v49, s89, v49 clamp\n"
    "v_pk_add_u16 v50, s89, v50 clamp\n"
    "v_pk_add_u16 v51, s89, v51 clamp\n"
    "v_mfma_f32_16x16x32_bf16 v[12:15], v[164:167], v[44:47], v[12:15]\n"
    "s_waitcnt lgkmcnt(1)\n"
    "v_pk_maximum3_f16 v80, v124, v125, v80\n"
    "v_pk_maximum3_f16 v80, v126, v127, v80\n"
    "v_pk_add_u16 v124, s89, v124 clamp\n"
    "v_pk_add_u16 v125, s89, v125 clamp\n"
    "v_pk_add_u16 v126, s89, v126 clamp\n"
    "v_pk_add_u16 v127, s89, v127 clamp\n"
    "v_mfma_f32_16x16x32_bf16 v[12:15], v[168:171], v[48:51], v[12:15]\n"
    "s_waitcnt lgkmcnt(0)\n"
    "v_pk_maximum3_f16 v80, v128, v129, v80\n"
    "v_pk_maximum3_f16 v80, v130, v131, v80\n"
    "v_pk_add_u16 v128, s89, v128 clamp\n"
    "v_pk_add_u16 v129, s89, v129 clamp\n"
    "v_pk_add_u16 v130, s89, v130 clamp\n"
    "v_pk_add_u16 v131, s89, v131 clamp\n"
    "v_mfma_f32_16x16x32_bf16 v[12:15], v[172:175], v[124:127], v[12:15]\n"
    "s_nop 0\n"
    "v_mfma_f32_16x16x32_bf16 v[12:15], v[176:179], v[128:131], v[12:15]\n"
    "v_mfma_f32_16x16x32_bf16 v[28:31], v[180:183], v[36:39], v[104:107]\n"
    "v_accvgpr_write_b32 a240, v36\n"
    "v_accvgpr_write_b32 a241, v37\n"
    "v_accvgpr_write_b32 a242, v38\n"
    "v_accvgpr_write_b32 a243, v39\n"
    "v_mfma_f32_16x16x32_bf16 v[28:31], v[184:187], v[40:43], v[28:31]\n"
    "v_accvgpr_write_b32 a244, v40\n"
    "v_accvgpr_write_b32 a245, v41\n"
    "v_accvgpr_write_b32 a246, v42\n"
    "v_accvgpr_write_b32 a247, v43\n"
    "v_mfma_f32_16x16x32_bf16 v[28:31], v[188:191], v[44:47], v[28:31]\n"
    "v_accvgpr_write_b32 a248, v44\n"
    "v_accvgpr_write_b32 a249, v45\n"
    "v_accvgpr_write_b32 a250, v46\n"
    "v_accvgpr_write_b32 a251, v47\n"
    "v_mfma_f32_16x16x32_bf16 v[28:31], v[192:195], v[48:51], v[28:31]\n"
    "v_accvgpr_write_b32 a252, v48\n"
    "v_accvgpr_write_b32 a253, v49\n"
    "v_accvgpr_write_b32 a254, v50\n"
    "v_accvgpr_write_b32 a255, v51\n"
    "v_mfma_f32_16x16x32_bf16 v[28:31], v[196:199], v[124:127], v[28:31]\n"
    "v_mfma_f32_16x16x32_bf16 v[28:31], v[200:203], v[128:131], v[28:31]\n"
    "v_exp_f32 v12, v12\n"
    "v_exp_f32 v13, v13\n"
    "v_exp_f32 v14, v14\n"
    "v_exp_f32 v15, v15\n"
    "s_nop 3\n"
    "v_exp_f32 v28, v28\n"
    "v_exp_f32 v29, v29\n"
    "v_exp_f32 v30, v30\n"
    "v_exp_f32 v31, v31\n"
    "s_waitcnt vmcnt(24)\n"
    "ds_read_b128 v[152:155], v85 offset:49408\n"
    "ds_read_b128 v[148:151], v84 offset:49408\n"
    "ds_read_b128 v[144:147], v85 offset:49280\n"
    "ds_read_b128 v[140:143], v84 offset:49280\n"
    "ds_read_b128 v[136:139], v85 offset:49152\n"
    "ds_read_b128 v[132:135], v84 offset:49152\n"
    "s_waitcnt lgkmcnt(5)\n"
    "v_pk_maximum3_f16 v80, v152, v153, v80\n"
    "v_pk_maximum3_f16 v80, v154, v155, v80\n"
    "v_pk_add_u16 v152, s89, v152 clamp\n"
    "v_pk_add_u16 v153, s89, v153 clamp\n"
    "v_pk_add_u16 v154, s89, v154 clamp\n"
    "v_pk_add_u16 v155, s89, v155 clamp\n"
    "s_waitcnt lgkmcnt(4)\n"
    "v_pk_maximum3_f16 v80, v148, v149, v80\n"
    "v_pk_maximum3_f16 v80, v150, v151, v80\n"
    "v_pk_add_u16 v148, s89, v148 clamp\n"
    "v_pk_add_u16 v149, s89, v149 clamp\n"
    "v_pk_add_u16 v150, s89, v150 clamp\n"
    "v_pk_add_u16 v151, s89, v151 clamp\n"
    "v_mfma_f32_16x16x32_bf16 v[16:19], v[176:179], v[152:155], v[100:103]\n"
    "s_waitcnt lgkmcnt(3)\n"
    "v_pk_maximum3_f16 v80, v144, v145, v80\n"
    "v_pk_maximum3_f16 v80, v146, v147, v80\n"
    "v_pk_add_u16 v144, s89, v144 clamp\n"
    "v_pk_add_u16 v145, s89, v145 clamp\n"
    "v_pk_add_u16 v146, s89, v146 clamp\n"
    "v_pk_add_u16 v147, s89, v147 clamp\n"
    "v_mfma_f32_16x16x32_bf16 v[16:19], v[172:175], v[148:151], v[16:19]\n"
    "s_waitcnt lgkmcnt(2)\n"
    "v_pk_maximum3_f16 v80, v140, v141, v80\n"
    "v_pk_maximum3_f16 v80, v142, v143, v80\n"
    "v_pk_add_u16 v140, s89, v140 clamp\n"
    "v_pk_add_u16 v141, s89, v141 clamp\n"
    "v_pk_add_u16 v142, s89, v142 clamp\n"
    "v_pk_add_u16 v143, s89, v143 clamp\n"
    "v_mfma_f32_16x16x32_bf16 v[16:19], v[168:171], v[144:147], v[16:19]\n"
    "s_waitcnt lgkmcnt(1)\n"
    "v_pk_maximum3_f16 v80, v136, v137, v80\n"
    "v_pk_maximum3_f16 v80, v138, v139, v80\n"
    "v_pk_add_u16 v136, s89, v136 clamp\n"
    "v_pk_add_u16 v137, s89, v137 clamp\n"
    "v_pk_add_u16 v138, s89, v138 clamp\n"
    "v_pk_add_u16 v139, s89, v139 clamp\n"
    "v_mfma_f32_16x16x32_bf16 v[16:19], v[164:167], v[140:143], v[16:19]\n"
    "s_waitcnt lgkmcnt(0)\n"
    "v_pk_maximum3_f16 v80, v132, v133, v80\n"
    "v_pk_maximum3_f16 v80, v134, v135, v80\n"
    "v_pk_add_u16 v132, s89, v132 clamp\n"
    "v_pk_add_u16 v133, s89, v133 clamp\n"
    "v_pk_add_u16 v134, s89, v134 clamp\n"
    "v_pk_add_u16 v135, s89, v135 clamp\n"
    "v_mfma_f32_16x16x32_bf16 v[16:19], v[160:163], v[136:139], v[16:19]\n"
    "s_nop 0\n"
    "v_mfma_f32_16x16x32_bf16 v[16:19], v[156:159], v[132:135], v[16:19]\n"
    "v_mfma_f32_16x16x32_bf16 v[32:35], v[200:203], v[152:155], v[104:107]\n"
    "v_mfma_f32_16x16x32_bf16 v[32:35], v[196:199], v[148:151], v[32:35]\n"
    "v_mfma_f32_16x16x32_bf16 v[32:35], v[192:195], v[144:147], v[32:35]\n"
    "v_mfma_f32_16x16x32_bf16 v[32:35], v[188:191], v[140:143], v[32:35]\n"
    "v_mfma_f32_16x16x32_bf16 v[32:35], v[184:187], v[136:139], v[32:35]\n"
    "v_mfma_f32_16x16x32_bf16 v[32:35], v[180:183], v[132:135], v[32:35]\n"
    "ds_read_b128 v[156:159], v2 offset:12288\n"
    "ds_read_b128 v[160:163], v2 offset:14336\n"
    "ds_read_b128 v[164:167], v2 offset:16384\n"
    "ds_read_b128 v[168:171], v2 offset:18432\n"
    "ds_read_b128 v[172:175], v2 offset:20480\n"
    "ds_read_b128 v[176:179], v2 offset:22528\n"
    "ds_read_b128 v[100:103], v77 offset:128\n"
    "v_pk_add_u16 v80, s89, v80\n"
    "v_and_b32 v80, 0x80008000, v80\n"
    "v_cmp_ne_u32 s[34:35], 0, v80\n"
    "v_exp_f32 v16, v16\n"
    "v_exp_f32 v17, v17\n"
    "v_exp_f32 v18, v18\n"
    "v_exp_f32 v19, v19\n"
    "v_exp_f32 v32, v32\n"
    "v_exp_f32 v33, v33\n"
    "v_exp_f32 v34, v34\n"
    "v_exp_f32 v35, v35\n"
    "s_mov_b32 s66, 0\n"
    "s_waitcnt lgkmcnt(0)\n"
    "s_waitcnt vmcnt(16)\n"
    "ds_add_u32 v247, v246\n"
    "s_nop 7\n"
    "s_branch .LNK\n"
    ".Lf3e:\n"
    "s_nop 4\n"
    "s_branch .Lf5e\n"
    ".Lf5b:\n"
    "s_lshl_b32 s73, s79, 8\n"
    "s_mul_i32 s74, s59, 6144\n"
    "s_add_u32 s73, s73, s74\n"
    "v_add_u32 v163, s73, v244\n"
    "s_lshr_b32 s73, s53, 2\n"
    "s_mul_i32 s73, s73, 12288\n"
    "s_and_b32 s74, s53, 3\n"
    "s_lshl_b32 s74, s74, 8\n"
    "s_add_u32 s73, s73, s74\n"
    "s_cmp_eq_u32 s59, 0\n"
    "s_cselect_b32 s73, 0x7f000000, s73\n"
    "s_add_u32 s60, s73, 24576\n"
    "s_add_u32 s61, s73, 28672\n"
    "s_add_u32 s62, s73, 32768\n"
    "v_mov_b32 v244, v243\n"
    "v_mov_b32 v243, 0\n"
    "buffer_load_dwordx4 v[40:43], v242, s[40:43], s75 offen offset:768 nt\n"
    "v_add_u32 v159, 0, v156\n"
    "v_mul_u32_u24 v159, 2731, v159\n"
    "v_lshrrev_b32 v159, 16, v159\n"
    "v_xor_b32 v159, v159, v156\n"
    "v_and_b32 v159, 7, v159\n"
    "v_and_b32 v88, 0x380, v1\n"
    "v_lshl_add_u32 v88, v159, 4, v88\n"
    "v_add_u32 v159, 64, v156\n"
    "v_mul_u32_u24 v159, 2731, v159\n"
    "v_lshrrev_b32 v159, 16, v159\n"
    "v_xor_b32 v159, v159, v156\n"
    "v_and_b32 v159, 7, v159\n"
    "v_and_b32 v89, 0x380, v1\n"
    "v_lshl_add_u32 v89, v159, 4, v89\n"
    "v_add_u32 v159, 128, v156\n"
    "v_mul_u32_u24 v159, 2731, v159\n"
    "v_lshrrev_b32 v159, 16, v159\n"
    "v_xor_b32 v159, v159, v156\n"
    "v_and_b32 v159, 7, v159\n"
    "v_and_b32 v90, 0x380, v1\n"
    "v_lshl_add_u32 v90, v159, 4, v90\n"
    "v_and_b32 v2, 7, v156\n"
    "v_lshrrev_b32 v144, 3, %3\n"
    "v_mul_u32_u24 v144, 768, v144\n"
    "v_lshl_add_u32 v144, v2, 3, v144\n"
    "buffer_load_dwordx4 v[44:47], v242, s[40:43], s75 offen offset:256 nt\n"
    "v_add_u32 v144, 131072, v144\n"
    "v_mad_u32_u24 v86, v2, 24, v144\n"
    "v_and_b32 v84, 15, v156\n"
    "v_lshrrev_b32 v159, 4, v156\n"
    "v_xor_b32 v159, v159, v2\n"
    "v_mul_u32_u24 v84, 384, v84\n"
    "v_lshl_add_u32 v84, v159, 4, v84\n"
    "s_add_u32 s73, s65, 49152\n"
    "v_add_u32 v84, s73, v84\n"
    "v_xor_b32 v85, 64, v84\n"
    "v_subrev_u32 v154, 24576, v84\n"
    "v_subrev_u32 v155, 24576, v85\n"
    "s_add_u32 s67, s65, 98304\n"
    "s_add_u32 s68, s65, 24576\n"
    "s_add_u32 s69, s65, 73728\n"
    "s_mov_b32 s81, 256\n"
    "s_mul_i32 s74, s79, 24576\n"
    "s_add_u32 s70, s74, s65\n"
    "s_add_u32 s70, s70, 73728\n"
    "s_sub_u32 s64, s65, s74\n"
    "s_add_u32 s64, s64, 98304\n"
    "v_and_b32 v249, 0x310, v1\n"
    "v_and_b32 v159, 12, v156\n"
    "v_lshl_add_u32 v249, v159, 3, v249\n"
    "v_and_b32 v159, 2, v156\n"
    "buffer_load_dwordx4 v[48:51], v242, s[40:43], s75 offen offset:1024 nt\n"
    "v_lshl_add_u32 v249, v159, 6, v249\n"
    "s_add_u32 s73, s74, 73728\n"
    "v_add_u32 v248, s73, v249\n"
    "s_sub_u32 s73, 98304, s74\n"
    "v_add_u32 v249, s73, v249\n"
    "s_or_b32 s73, s53, s84\n"
    "s_cmp_eq_u32 s73, 0\n"
    "s_cselect_b32 s82, 0, 0x7fff0000\n"
    "v_and_b32 v252, 12, v156\n"
    "v_bfe_u32 v159, v156, 1, 1\n"
    "v_or_b32 v252, v252, v159\n"
    "v_and_b32 v159, 1, v156\n"
    "v_lshl_or_b32 v252, v159, 1, v252\n"
    "v_lshlrev_b32 v252, 6, v252\n"
    "v_lshlrev_b32 v159, 2, v156\n"
    "v_and_b32 v159, 32, v159\n"
    "v_xor_b32 v159, v159, v156\n"
    "v_and_or_b32 v252, v159, 48, v252\n"
    "v_mov_b32 v2, v252\n"
    "v_add_u32 v3, 24576, v252\n"
    "v_lshrrev_b32 v91, 3, v156\n"
    "v_lshl_add_u32 v87, v91, 6, 64\n"
    "v_add_u32 v87, 64, v87\n"
    "v_and_b32 v241, 1, v156\n"
    "v_lshlrev_b32 v241, 3, v241\n"
    "buffer_load_dwordx4 v[52:55], v242, s[40:43], s75 offen offset:512 nt\n"
    "v_lshl_add_u32 v241, v91, 4, v241\n"
    "v_add_u32 v241, 1152, v241\n"
    "v_lshlrev_b32 v91, 24, v91\n"
    "v_lshl_add_u32 v91, v156, 3, v91\n"
    "s_lshl_b32 s73, s53, 4\n"
    "s_lshl_b32 s74, s59, 3\n"
    "s_add_u32 s73, s73, s74\n"
    "s_add_u32 s73, s73, 1152\n"
    "v_mov_b32 v240, 0x7fff0000\n"
    "v_writelane_b32 v240, s73, 0\n"
    "v_and_b32 v159, 48, v156\n"
    "s_mul_i32 s73, s79, 768\n"
    "s_sub_u32 s73, 131072, s73\n"
    "v_lshlrev_b32 v250, 1, v159\n"
    "v_add_u32 v250, s73, v250\n"
    "v_and_b32 v251, 3, v156\n"
    "v_lshl_add_u32 v251, v251, 7, v159\n"
    "s_add_u32 s73, s73, 256\n"
    "v_add_u32 v251, s73, v251\n"
    "v_and_b32 v159, 64, v1\n"
    "v_add_u32 v251, v159, v251\n"
    "v_bfe_i32 v159, v156, 2, 1\n"
    "v_and_b32 v159, 0xaaaa, v159\n"
    "v_or_b32 v253, 0x4444, v159\n"
    "s_mov_b32 s58, 0\n"
    "s_mov_b32 s59, 8\n"
    "s_waitcnt vmcnt(14)\n"
    "v_pk_min_i16 v4, v4, v10\n"
    "v_pk_min_i16 v5, v5, v11\n"
    "v_pk_min_i16 v6, v6, v12\n"
    "v_pk_min_i16 v7, v7, v13\n"
    "v_pk_min_i16 v8, v8, v14\n"
    "v_pk_min_i16 v9, v9, v15\n"
    "v_pk_min_i16 v16, v16, v18\n"
    "v_pk_min_i16 v17, v17, v19\n"
    "v_pk_min_i16 v4, v4, v7\n"
    "v_pk_min_i16 v5, v5, v8\n"
    "v_pk_min_i16 v6, v6, v9\n"
    "v_pk_min_i16 v16, v16, v17\n"
    "v_pk_min_i16 v4, v4, v5\n"
    "v_lshlrev_b32 v17, 16, v16\n"
    "v_pk_min_i16 v4, v4, v6\n"
    "v_min_i32 v16, v16, v17\n"
    "v_lshlrev_b32 v5, 16, v4\n"
    "v_mov_b32 v7, 155920\n"
    "v_min_i32 v4, v4, v5\n"
    "v_lshl_add_u32 v6, s84, 2, v7\n"
    "v_min_i32_dpp v16, v16, v16 row_shr:1\n"
    "v_min_i32_dpp v4, v4, v4 row_shr:1\n"
    "s_nop 0\n"
    "v_min_i32_dpp v16, v16, v16 row_shr:2\n"
    "v_min_i32_dpp v4, v4, v4 row_shr:2\n"
    "s_nop 0\n"
    "v_min_i32_dpp v16, v16, v16 row_shr:4\n"
    "v_min_i32_dpp v4, v4, v4 row_shr:4\n"
    "s_nop 0\n"
    "v_min_i32_dpp v16, v16, v16 row_shr:8\n"
    "v_min_i32_dpp v4, v4, v4 row_shr:8\n"
    "s_nop 0\n"
    "v_min_i32_dpp v16, v16, v16 row_bcast:15 row_mask:0xa\n"
    "v_min_i32_dpp v4, v4, v4 row_bcast:15 row_mask:0xa\n"
    "s_nop 0\n"
    "v_min_i32_dpp v16, v16, v16 row_bcast:31 row_mask:0xc\n"
    "v_min_i32_dpp v4, v4, v4 row_bcast:31 row_mask:0xc\n"
    "v_readlane_b32 s35, v16, 63\n"
    "v_readlane_b32 s73, v4, 63\n"
    "s_max_i32 s35, s35, 0\n"
    "s_and_b32 s35, s35, 0xffff0000\n"
    "v_mov_b32 v4, s73\n"
    "ds_write_b32 v6, v4\n"
    "v_mov_b32 v14, -32\n"
    "v_mov_b32 v12, -126\n"
    "v_mov_b32 v13, 0x3dd53b95\n"
    "v_mov_b32 v80, 0\n"
    "s_waitcnt lgkmcnt(0)\n"
    "s_barrier\n"
    "ds_read_b128 v[8:11], v7 offset:0\n"
    "buffer_load_dwordx4 v[56:59], v242, s[40:43], s75 offen offset:1280 nt\n"
    "buffer_load_dwordx4 v[136:139], v130, s[92:95], 0 offen offset:0\n"
    "buffer_load_dwordx4 v[140:143], v130, s[96:99], 0 offen offset:0\n"
    "v_accvgpr_write_b32 a0, 0\n"
    "v_accvgpr_write_b32 a1, 0\n"
    "v_accvgpr_write_b32 a2, 0\n"
    "v_accvgpr_write_b32 a3, 0\n"
    "v_accvgpr_write_b32 a4, 0\n"
    "v_accvgpr_write_b32 a5, 0\n"
    "v_accvgpr_write_b32 a6, 0\n"
    "v_accvgpr_write_b32 a7, 0\n"
    "v_accvgpr_write_b32 a8, 0\n"
    "v_accvgpr_write_b32 a9, 0\n"
    "v_accvgpr_write_b32 a10, 0\n"
    "v_accvgpr_write_b32 a11, 0\n"
    "v_accvgpr_write_b32 a12, 0\n"
    "v_accvgpr_write_b32 a13, 0\n"
    "v_accvgpr_write_b32 a14, 0\n"
    "v_accvgpr_write_b32 a15, 0\n"
    "v_accvgpr_write_b32 a16, 0\n"
    "v_accvgpr_write_b32 a17, 0\n"
    "v_accvgpr_write_b32 a18, 0\n"
    "v_accvgpr_write_b32 a19, 0\n"
    "v_accvgpr_write_b32 a20, 0\n"
    "v_accvgpr_write_b32 a21, 0\n"
    "v_accvgpr_write_b32 a22, 0\n"
    "v_accvgpr_write_b32 a23, 0\n"
    "v_accvgpr_write_b32 a24, 0\n"
    "v_accvgpr_write_b32 a25, 0\n"
    "v_accvgpr_write_b32 a26, 0\n"
    "v_accvgpr_write_b32 a27, 0\n"
    "v_accvgpr_write_b32 a28, 0\n"
    "v_accvgpr_write_b32 a29, 0\n"
    "v_accvgpr_write_b32 a30, 0\n"
    "v_accvgpr_write_b32 a31, 0\n"
    "v_accvgpr_write_b32 a32, 0\n"
    "v_accvgpr_write_b32 a33, 0\n"
    "v_accvgpr_write_b32 a34, 0\n"
    "v_accvgpr_write_b32 a35, 0\n"
    "v_accvgpr_write_b32 a36, 0\n"
    "v_accvgpr_write_b32 a37, 0\n"
    "v_accvgpr_write_b32 a38, 0\n"
    "v_accvgpr_write_b32 a39, 0\n"
    "v_accvgpr_write_b32 a40, 0\n"
    "v_accvgpr_write_b32 a41, 0\n"
    "v_accvgpr_write_b32 a42, 0\n"
    "v_accvgpr_write_b32 a43, 0\n"
    "v_accvgpr_write_b32 a44, 0\n"
    "v_accvgpr_write_b32 a45, 0\n"
    "v_accvgpr_write_b32 a46, 0\n"
    "v_accvgpr_write_b32 a47, 0\n"
    "v_accvgpr_write_b32 a48, 0\n"
    "v_accvgpr_write_b32 a49, 0\n"
    "v_accvgpr_write_b32 a50, 0\n"
    "v_accvgpr_write_b32 a51, 0\n"
    "v_accvgpr_write_b32 a52, 0\n"
    "v_accvgpr_write_b32 a53, 0\n"
    "v_accvgpr_write_b32 a54, 0\n"
    "v_accvgpr_write_b32 a55, 0\n"
    "v_accvgpr_write_b32 a56, 0\n"
    "v_accvgpr_write_b32 a57, 0\n"
    "v_accvgpr_write_b32 a58, 0\n"
    "v_accvgpr_write_b32 a59, 0\n"
    "s_waitcnt lgkmcnt(0)\n"
    "v_min3_i32 v4, v8, v9, v10\n"
    "v_min_i32 v4, v4, v11\n"
    "v_max_i32 v4, 0, v4\n"
    "v_and_b32 v4, 0xffff0000, v4\n"
    "v_mul_f32 v4, s35, v4\n"
    "v_mul_f32 v4, 0x3ed53b95, v4\n"
    "v_lshrrev_b32 v4, 23, v4\n"
    "v_sub_u32 v4, 2, v4\n"
    "v_med3_i32 v4, v4, v14, v12\n"
    "v_ldexp_f32 v13, v13, v4\n"
    "v_readfirstlane_b32 s89, v4\n"
    "v_readfirstlane_b32 s88, v13\n"
    "s_mul_i32 s89, s89, 0xff7fff80\n"
    "s_mov_b32 s90, 149\n"
    "s_waitcnt vmcnt(11)\n"
    "s_add_u32 s54, s71, s60\n"
    "s_add_u32 s55, s71, s61\n"
    "s_add_u32 s56, s71, s62\n"
    "v_cvt_pk_u16_u32 v204, v204, v208\n"
    "v_cvt_pk_u16_u32 v205, v205, v209\n"
    "v_cvt_pk_u16_u32 v206, v206, v210\n"
    "v_cvt_pk_u16_u32 v207, v207, v211\n"
    "v_or3_b32 v243, v204, v205, v243\n"
    "v_or3_b32 v243, v206, v207, v243\n"
    "buffer_store_dword v204, v244, s[16:19], s54 offen offset:0 sc1\n"
    "buffer_store_dword v205, v244, s[16:19], s54 offen offset:1024 sc1\n"
    "buffer_load_dwordx4 v[4:7], v162, s[28:31], s57 offen offset:0\n"
    "buffer_load_dwordx4 v[8:11], v162, s[28:31], s57 offen offset:3072\n"
    "buffer_store_dword v206, v244, s[16:19], s54 offen offset:2048 sc1\n"
    "buffer_store_dword v207, v244, s[16:19], s54 offen offset:3072 sc1\n"
    "v_cvt_pk_u16_u32 v212, v212, v216\n"
    "v_cvt_pk_u16_u32 v213, v213, v217\n"
    "v_cvt_pk_u16_u32 v214, v214, v218\n"
    "v_cvt_pk_u16_u32 v215, v215, v219\n"
    "v_or3_b32 v243, v212, v213, v243\n"
    "v_or3_b32 v243, v214, v215, v243\n"
    "buffer_store_dword v212, v244, s[16:19], s55 offen offset:0 sc1\n"
    "buffer_store_dword v213, v244, s[16:19], s55 offen offset:1024 sc1\n"
    "buffer_store_dword v214, v244, s[16:19], s55 offen offset:2048 sc1\n"
    "buffer_load_dwordx4 v[12:15], v162, s[28:31], s57 offen offset:128\n"
    "buffer_store_dword v215, v244, s[16:19], s55 offen offset:3072 sc1\n"
    "v_cvt_pk_u16_u32 v220, v220, v224\n"
    "v_cvt_pk_u16_u32 v221, v221, v225\n"
    "v_cvt_pk_u16_u32 v222, v222, v226\n"
    "v_cvt_pk_u16_u32 v223, v223, v227\n"
    "v_or3_b32 v243, v220, v221, v243\n"
    "v_or3_b32 v243, v222, v223, v243\n"
    "buffer_store_dword v220, v244, s[16:19], s56 offen offset:0 sc1\n"
    "buffer_store_dword v221, v244, s[16:19], s56 offen offset:1024 sc1\n"
    "buffer_store_dword v222, v244, s[16:19], s56 offen offset:2048 sc1\n"
    "buffer_store_dword v223, v244, s[16:19], s56 offen offset:3072 sc1\n"
    "s_add_u32 s71, s71, 98304\n"
    "s_add_u32 m0, s65, 24576\n"
    "s_add_u32 s72, s85, 0\n"
    "buffer_load_dwordx4 v88, s[20:23], s72 offen offset:0 nt lds\n"
    "buffer_load_dwordx4 v[16:19], v162, s[28:31], s57 offen offset:3200\n"
    "buffer_load_dwordx4 v[20:23], v162, s[28:31], s57 offen offset:256\n"
    "s_waitcnt vmcnt(25)\n"
    "v_add_co_u32 v238, vcc, 0x7f4a7c15, v238\n"
    "v_mov_b32 v159, 0x9e3779b9\n"
    "s_nop 0\n"
    "v_addc_co_u32 v239, vcc, v159, v239, vcc\n"
    "v_mov_b32 v246, 1\n"
    "s_waitcnt vmcnt(26)\n"
    "v_lshlrev_b32 v180, 16, v124\n"
    "v_and_b32 v181, 0xffff0000, v124\n"
    "v_lshlrev_b32 v182, 16, v125\n"
    "v_and_b32 v183, 0xffff0000, v125\n"
    "v_lshlrev_b32 v184, 16, v126\n"
    "v_and_b32 v185, 0xffff0000, v126\n"
    "v_lshlrev_b32 v186, 16, v127\n"
    "v_and_b32 v187, 0xffff0000, v127\n"
    "v_mul_f32 v180, s88, v180\n"
    "v_mul_f32 v181, s88, v181\n"
    "v_mul_f32 v182, s88, v182\n"
    "v_mul_f32 v183, s88, v183\n"
    "v_fmaak_f32 v184, v184, v180, 0x80730000\n"
    "v_fmaak_f32 v185, v185, v181, 0x80730000\n"
    "v_fmaak_f32 v186, v186, v182, 0x80730000\n"
    "v_fmaak_f32 v187, v187, v183, 0x80730000\n"
    "v_ldexp_f32 v180, v180, s90\n"
    "v_ldexp_f32 v181, v181, s90\n"
    "v_ldexp_f32 v182, v182, s90\n"
    "v_ldexp_f32 v183, v183, s90\n"
    "v_fma_f32 v180, v180, 0, v180\n"
    "v_fma_f32 v181, v181, 0, v181\n"
    "v_fma_f32 v182, v182, 0, v182\n"
    "v_fma_f32 v183, v183, 0, v183\n"
    "ds_write_b128 v131, v[180:183] offset:0\n"
    "ds_write_b128 v131, v[184:187] offset:1024\n"
    "ds_read_b128 v[180:183], v132 offset:0\n"
    "ds_read_b128 v[184:187], v132 offset:1024\n"
    "ds_read_b128 v[188:191], v132 offset:128\n"
    "ds_read_b128 v[192:195], v132 offset:1152\n"
    "ds_read_b128 v[196:199], v132 offset:256\n"
    "ds_read_b128 v[200:203], v132 offset:1280\n"
    "v_accvgpr_write_b32 a60, 0\n"
    "v_accvgpr_write_b32 a61, 0\n"
    "v_accvgpr_write_b32 a62, 0\n"
    "v_accvgpr_write_b32 a63, 0\n"
    "s_add_u32 m0, s65, 25600\n"
    "s_add_u32 s72, s85, 1024\n"
    "buffer_load_dwordx4 v89, s[20:23], s72 offen offset:0 nt lds\n"
    "buffer_load_dwordx4 v[24:27], v162, s[28:31], s57 offen offset:3328\n"
    "v_accvgpr_write_b32 a64, 0\n"
    "v_accvgpr_write_b32 a65, 0\n"
    "v_accvgpr_write_b32 a66, 0\n"
    "v_accvgpr_write_b32 a67, 0\n"
    "v_accvgpr_write_b32 a68, 0\n"
    "v_accvgpr_write_b32 a69, 0\n"
    "v_accvgpr_write_b32 a70, 0\n"
    "v_accvgpr_write_b32 a71, 0\n"
    "v_accvgpr_write_b32 a72, 0\n"
    "v_accvgpr_write_b32 a73, 0\n"
    "v_accvgpr_write_b32 a74, 0\n"
    "v_accvgpr_write_b32 a75, 0\n"
    "v_accvgpr_write_b32 a76, 0\n"
    "v_accvgpr_write_b32 a77, 0\n"
    "v_accvgpr_write_b32 a78, 0\n"
    "v_accvgpr_write_b32 a79, 0\n"
    "v_accvgpr_write_b32 a80, 0\n"
    "v_accvgpr_write_b32 a81, 0\n"
    "v_accvgpr_write_b32 a82, 0\n"
    "v_accvgpr_write_b32 a83, 0\n"
    "v_accvgpr_write_b32 a84, 0\n"
    "v_accvgpr_write_b32 a85, 0\n"
    "v_accvgpr_write_b32 a86, 0\n"
    "v_accvgpr_write_b32 a87, 0\n"
    "v_accvgpr_write_b32 a88, 0\n"
    "v_accvgpr_write_b32 a89, 0\n"
    "v_accvgpr_write_b32 a90, 0\n"
    "v_accvgpr_write_b32 a91, 0\n"
    "v_accvgpr_write_b32 a92, 0\n"
    "v_accvgpr_write_b32 a93, 0\n"
    "v_accvgpr_write_b32 a94, 0\n"
    "v_accvgpr_write_b32 a95, 0\n"
    "v_accvgpr_write_b32 a96, 0\n"
    "v_accvgpr_write_b32 a97, 0\n"
    "v_accvgpr_write_b32 a98, 0\n"
    "v_accvgpr_write_b32 a99, 0\n"
    "v_accvgpr_write_b32 a100, 0\n"
    "v_accvgpr_write_b32 a101, 0\n"
    "v_accvgpr_write_b32 a102, 0\n"
    "v_accvgpr_write_b32 a103, 0\n"
    "v_accvgpr_write_b32 a104, 0\n"
    "v_accvgpr_write_b32 a105, 0\n"
    "v_accvgpr_write_b32 a106, 0\n"
    "s_add_u32 m0, s65, 26624\n"
    "s_add_u32 s72, s85, 2048\n"
    "buffer_load_dwordx4 v90, s[20:23], s72 offen offset:0 nt lds\n"
    "buffer_load_dwordx4 v[100:103], v162, s[28:31], s66 offen offset:0\n"
    "buffer_load_dwordx4 v[104:107], v162, s[28:31], s66 offen offset:3072\n"
    "v_accvgpr_write_b32 a107, 0\n"
    "v_accvgpr_write_b32 a108, 0\n"
    "v_accvgpr_write_b32 a109, 0\n"
    "v_accvgpr_write_b32 a110, 0\n"
    "v_accvgpr_write_b32 a111, 0\n"
    "v_accvgpr_write_b32 a112, 0\n"
    "v_accvgpr_write_b32 a113, 0\n"
    "v_accvgpr_write_b32 a114, 0\n"
    "v_accvgpr_write_b32 a115, 0\n"
    "v_accvgpr_write_b32 a116, 0\n"
    "v_accvgpr_write_b32 a117, 0\n"
    "v_accvgpr_write_b32 a118, 0\n"
    "v_accvgpr_write_b32 a119, 0\n"
    "v_accvgpr_write_b32 a120, 0\n"
    "v_accvgpr_write_b32 a121, 0\n"
    "v_accvgpr_write_b32 a122, 0\n"
    "v_accvgpr_write_b32 a123, 0\n"
    "v_accvgpr_write_b32 a124, 0\n"
    "v_accvgpr_write_b32 a125, 0\n"
    "v_accvgpr_write_b32 a126, 0\n"
    "v_accvgpr_write_b32 a127, 0\n"
    "v_accvgpr_write_b32 a128, 0\n"
    "v_accvgpr_write_b32 a129, 0\n"
    "v_accvgpr_write_b32 a130, 0\n"
    "v_accvgpr_write_b32 a131, 0\n"
    "v_accvgpr_write_b32 a132, 0\n"
    "v_accvgpr_write_b32 a133, 0\n"
    "v_accvgpr_write_b32 a134, 0\n"
    "v_accvgpr_write_b32 a135, 0\n"
    "v_accvgpr_write_b32 a136, 0\n"
    "v_accvgpr_write_b32 a137, 0\n"
    "v_accvgpr_write_b32 a138, 0\n"
    "v_accvgpr_write_b32 a139, 0\n"
    "v_accvgpr_write_b32 a140, 0\n"
    "v_accvgpr_write_b32 a141, 0\n"
    "v_accvgpr_write_b32 a142, 0\n"
    "v_accvgpr_write_b32 a143, 0\n"
    "v_accvgpr_write_b32 a144, 0\n"
    "v_accvgpr_write_b32 a145, 0\n"
    "v_accvgpr_write_b32 a146, 0\n"
    "v_accvgpr_write_b32 a147, 0\n"
    "v_accvgpr_write_b32 a148, 0\n"
    "s_add_u32 m0, s65, 27648\n"
    "s_add_u32 s72, s85, 3072\n"
    "buffer_load_dwordx4 v88, s[20:23], s72 offen offset:0 nt lds\n"
    "buffer_load_dwordx4 v[108:111], v162, s[28:31], s66 offen offset:128\n"
    "v_accvgpr_write_b32 a149, 0\n"
    "v_accvgpr_write_b32 a150, 0\n"
    "v_accvgpr_write_b32 a151, 0\n"
    "v_accvgpr_write_b32 a152, 0\n"
    "v_accvgpr_write_b32 a153, 0\n"
    "v_accvgpr_write_b32 a154, 0\n"
    "v_accvgpr_write_b32 a155, 0\n"
    "v_accvgpr_write_b32 a156, 0\n"
    "v_accvgpr_write_b32 a157, 0\n"
    "v_accvgpr_write_b32 a158, 0\n"
    "v_accvgpr_write_b32 a159, 0\n"
    "v_accvgpr_write_b32 a160, 0\n"
    "v_accvgpr_write_b32 a161, 0\n"
    "v_accvgpr_write_b32 a162, 0\n"
    "v_accvgpr_write_b32 a163, 0\n"
    "v_accvgpr_write_b32 a164, 0\n"
    "v_accvgpr_write_b32 a165, 0\n"
    "v_accvgpr_write_b32 a166, 0\n"
    "v_accvgpr_write_b32 a167, 0\n"
    "v_accvgpr_write_b32 a168, 0\n"
    "v_accvgpr_write_b32 a169, 0\n"
    "v_accvgpr_write_b32 a170, 0\n"
    "v_accvgpr_write_b32 a171, 0\n"
    "v_accvgpr_write_b32 a172, 0\n"
    "v_accvgpr_write_b32 a173, 0\n"
    "v_accvgpr_write_b32 a174, 0\n"
    "v_accvgpr_write_b32 a175, 0\n"
    "v_accvgpr_write_b32 a176, 0\n"
    "v_accvgpr_write_b32 a177, 0\n"
    "v_accvgpr_write_b32 a178, 0\n"
    "v_accvgpr_write_b32 a179, 0\n"
    "v_accvgpr_write_b32 a180, 0\n"
    "v_accvgpr_write_b32 a181, 0\n"
    "v_accvgpr_write_b32 a182, 0\n"
    "v_accvgpr_write_b32 a183, 0\n"
    "v_accvgpr_write_b32 a184, 0\n"
    "v_accvgpr_write_b32 a185, 0\n"
    "v_accvgpr_write_b32 a186, 0\n"
    "v_accvgpr_write_b32 a187, 0\n"
    "v_accvgpr_write_b32 a188, 0\n"
    "v_accvgpr_write_b32 a189, 0\n"
    "v_accvgpr_write_b32 a190, 0\n"
    "v_accvgpr_write_b32 a191, 0\n"
    "s_waitcnt lgkmcnt(0)\n"
    "s_waitcnt vmcnt(10)\n"
    "ds_add_rtn_u32 v245, v247, v246\n"
    "s_lshl_b32 s73, s53, 6\n"
    "s_add_u32 s73, s73, 128\n"
    "s_waitcnt lgkmcnt(0)\n"
    "v_readfirstlane_b32 s74, v245\n"
    "s_nop 0\n"
    "s_cmp_eq_u32 s74, 3\n"
    "s_cselect_b32 s73, s73, 0x7fff0000\n"
    "buffer_store_dwordx2 v[238:239], v91, s[36:39], s73 offen offset:0 sc1\n"
    "s_waitcnt vmcnt(28)\n"
    "s_add_u32 m0, s65, 28672\n"
    "s_add_u32 s72, s85, 4096\n"
    "buffer_load_dwordx4 v89, s[20:23], s72 offen offset:0 nt lds\n"
    "buffer_load_dwordx4 v[112:115], v162, s[28:31], s66 offen offset:3200\n"
    "buffer_load_dwordx4 v[116:119], v162, s[28:31], s66 offen offset:256\n"
    "s_add_u32 s54, s71, s60\n"
    "s_add_u32 s55, s71, s61\n"
    "s_add_u32 s56, s71, s62\n"
    "v_cvt_pk_u16_u32 v36, v36, v40\n"
    "v_cvt_pk_u16_u32 v37, v37, v41\n"
    "v_cvt_pk_u16_u32 v38, v38, v42\n"
    "v_cvt_pk_u16_u32 v39, v39, v43\n"
    "v_or3_b32 v243, v36, v37, v243\n"
    "v_or3_b32 v243, v38, v39, v243\n"
    "buffer_store_dword v36, v244, s[16:19], s54 offen offset:0 sc1\n"
    "buffer_store_dword v37, v244, s[16:19], s54 offen offset:1024 sc1\n"
    "buffer_store_dword v38, v244, s[16:19], s54 offen offset:2048 sc1\n"
    "buffer_store_dword v39, v244, s[16:19], s54 offen offset:3072 sc1\n"
    "v_cvt_pk_u16_u32 v44, v44, v48\n"
    "v_cvt_pk_u16_u32 v45, v45, v49\n"
    "v_cvt_pk_u16_u32 v46, v46, v50\n"
    "s_add_u32 m0, s65, 29696\n"
    "s_add_u32 s72, s85, 5120\n"
    "buffer_load_dwordx4 v90, s[20:23], s72 offen offset:0 nt lds\n"
    "buffer_load_dwordx4 v[120:123], v162, s[28:31], s66 offen offset:3328\n"
    "v_cvt_pk_u16_u32 v47, v47, v51\n"
    "v_or3_b32 v243, v44, v45, v243\n"
    "v_or3_b32 v243, v46, v47, v243\n"
    "buffer_store_dword v44, v244, s[16:19], s55 offen offset:0 sc1\n"
    "buffer_store_dword v45, v244, s[16:19], s55 offen offset:1024 sc1\n"
    "buffer_store_dword v46, v244, s[16:19], s55 offen offset:2048 sc1\n"
    "buffer_store_dword v47, v244, s[16:19], s55 offen offset:3072 sc1\n"
    "v_cvt_pk_u16_u32 v52, v52, v56\n"
    "v_cvt_pk_u16_u32 v53, v53, v57\n"
    "v_cvt_pk_u16_u32 v54, v54, v58\n"
    "v_cvt_pk_u16_u32 v55, v55, v59\n"
    "v_or3_b32 v243, v52, v53, v243\n"
    "v_or3_b32 v243, v54, v55, v243\n"
    "buffer_store_dword v52, v244, s[16:19], s56 offen offset:0 sc1\n"
    "buffer_store_dword v53, v244, s[16:19], s56 offen offset:1024 sc1\n"
    "buffer_store_dword v54, v244, s[16:19], s56 offen offset:2048 sc1\n"
    "buffer_store_dword v55, v244, s[16:19], s56 offen offset:3072 sc1\n"
    "s_add_u32 m0, s65, 73728\n"
    "s_add_u32 s72, s85, 6144\n"
    "buffer_load_dwordx4 v88, s[20:23], s72 offen offset:0 nt lds\n"
    "s_waitcnt vmcnt(44)\n"
    "v_lshlrev_b32 v204, 16, v136\n"
    "v_and_b32 v205, 0xffff0000, v136\n"
    "v_lshlrev_b32 v206, 16, v137\n"
    "v_and_b32 v207, 0xffff0000, v137\n"
    "v_lshlrev_b32 v208, 16, v138\n"
    "v_and_b32 v209, 0xffff0000, v138\n"
    "v_lshlrev_b32 v210, 16, v139\n"
    "v_and_b32 v211, 0xffff0000, v139\n"
    "v_log_f32 v68, v204\n"
    "v_log_f32 v69, v205\n"
    "v_log_f32 v70, v206\n"
    "v_log_f32 v71, v207\n"
    "v_log_f32 v72, v208\n"
    "v_log_f32 v73, v209\n"
    "v_log_f32 v74, v210\n"
    "v_log_f32 v75, v211\n"
    "v_rcp_f32 v204, v204\n"
    "v_rcp_f32 v205, v205\n"
    "v_rcp_f32 v206, v206\n"
    "v_rcp_f32 v207, v207\n"
    "v_rcp_f32 v208, v208\n"
    "v_rcp_f32 v209, v209\n"
    "v_rcp_f32 v210, v210\n"
    "v_rcp_f32 v211, v211\n"
    "s_mov_b32 s73, 0x41a00000\n"
    "v_mov_b32 v228, 0x7fc00000\n"
    "s_add_u32 m0, s65, 74752\n"
    "s_add_u32 s72, s85, 7168\n"
    "buffer_load_dwordx4 v89, s[20:23], s72 offen offset:0 nt lds\n"
    "v_cmp_le_f32 vcc, |v68|, s73\n"
    "s_nop 1\n"
    "v_cndmask_b32 v68, v228, v68, vcc\n"
    "v_cmp_le_f32 vcc, |v69|, s73\n"
    "s_nop 1\n"
    "v_cndmask_b32 v69, v228, v69, vcc\n"
    "v_cmp_le_f32 vcc, |v70|, s73\n"
    "s_nop 1\n"
    "v_cndmask_b32 v70, v228, v70, vcc\n"
    "v_cmp_le_f32 vcc, |v71|, s73\n"
    "s_nop 1\n"
    "v_cndmask_b32 v71, v228, v71, vcc\n"
    "v_cmp_le_f32 vcc, |v72|, s73\n"
    "s_nop 1\n"
    "v_cndmask_b32 v72, v228, v72, vcc\n"
    "v_cmp_le_f32 vcc, |v73|, s73\n"
    "s_nop 1\n"
    "v_cndmask_b32 v73, v228, v73, vcc\n"
    "v_cmp_le_f32 vcc, |v74|, s73\n"
    "s_nop 1\n"
    "v_cndmask_b32 v74, v228, v74, vcc\n"
    "v_cmp_le_f32 vcc, |v75|, s73\n"
    "s_nop 1\n"
    "v_cndmask_b32 v75, v228, v75, vcc\n"
    "v_cvt_pk_bf16_f32 v220, v204, v205\n"
    "v_cvt_pk_bf16_f32 v221, v206, v207\n"
    "v_cvt_pk_bf16_f32 v222, v208, v209\n"
    "v_cvt_pk_bf16_f32 v223, v210, v211\n"
    "v_lshlrev_b32 v212, 16, v220\n"
    "v_and_b32 v213, 0xffff0000, v220\n"
    "v_lshlrev_b32 v214, 16, v221\n"
    "v_and_b32 v215, 0xffff0000, v221\n"
    "v_lshlrev_b32 v216, 16, v222\n"
    "v_and_b32 v217, 0xffff0000, v222\n"
    "v_lshlrev_b32 v218, 16, v223\n"
    "s_add_u32 m0, s65, 75776\n"
    "s_add_u32 s72, s85, 8192\n"
    "buffer_load_dwordx4 v90, s[20:23], s72 offen offset:0 nt lds\n"
    "v_and_b32 v219, 0xffff0000, v223\n"
    "v_sub_f32 v204, v204, v212\n"
    "v_sub_f32 v205, v205, v213\n"
    "v_sub_f32 v206, v206, v214\n"
    "v_sub_f32 v207, v207, v215\n"
    "v_sub_f32 v208, v208, v216\n"
    "v_sub_f32 v209, v209, v217\n"
    "v_sub_f32 v210, v210, v218\n"
    "v_sub_f32 v211, v211, v219\n"
    "v_cvt_pk_bf16_f32 v224, v204, v205\n"
    "v_cvt_pk_bf16_f32 v225, v206, v207\n"
    "v_cvt_pk_bf16_f32 v226, v208, v209\n"
    "v_cvt_pk_bf16_f32 v227, v210, v211\n"
    "v_mov_b32 v204, 0\n"
    "v_mov_b32 v205, 0\n"
    "v_mov_b32 v206, 0\n"
    "v_mov_b32 v207, 0\n"
    "v_swap_b32 v221, v222\n"
    "v_swap_b32 v225, v226\n"
    "v_swap_b32 v141, v142\n"
    "ds_write2_b64 v144, v[220:221], v[222:223] offset0:32 offset1:40\n"
    "ds_write2_b64 v144, v[224:225], v[226:227] offset0:48 offset1:56\n"
    "ds_write2_b64 v144, v[140:141], v[142:143] offset0:64 offset1:72\n"
    "ds_write2_b64 v144, v[204:205], v[206:207] offset0:80 offset1:88\n"
    "s_waitcnt vmcnt(26)\n"
    "v_fma_f32 v4, v4, v180, -v184\n"
    "v_fma_f32 v5, v5, v181, -v185\n"
    "v_fma_f32 v6, v6, v182, -v186\n"
    "s_add_u32 m0, s65, 76800\n"
    "s_add_u32 s72, s85, 9216\n"
    "buffer_load_dwordx4 v88, s[20:23], s72 offen offset:0 nt lds\n"
    "v_fma_f32 v7, v7, v183, -v187\n"
    "v_fma_f32 v8, v8, v180, -v184\n"
    "v_fma_f32 v9, v9, v181, -v185\n"
    "v_fma_f32 v10, v10, v182, -v186\n"
    "v_fma_f32 v11, v11, v183, -v187\n"
    "v_cvt_pk_bf16_f32 v4, v4, v5\n"
    "v_cvt_pk_bf16_f32 v5, v6, v7\n"
    "v_cvt_pk_bf16_f32 v6, v8, v9\n"
    "v_cvt_pk_bf16_f32 v7, v10, v11\n"
    "ds_write_b64 v163, v[4:5] offset:0\n"
    "ds_write_b64 v163, v[6:7] offset:1024\n"
    "v_fma_f32 v12, v12, v188, -v192\n"
    "v_fma_f32 v13, v13, v189, -v193\n"
    "v_fma_f32 v14, v14, v190, -v194\n"
    "v_fma_f32 v15, v15, v191, -v195\n"
    "v_fma_f32 v16, v16, v188, -v192\n"
    "v_fma_f32 v17, v17, v189, -v193\n"
    "v_fma_f32 v18, v18, v190, -v194\n"
    "v_fma_f32 v19, v19, v191, -v195\n"
    "v_cvt_pk_bf16_f32 v12, v12, v13\n"
    "v_cvt_pk_bf16_f32 v13, v14, v15\n"
    "v_cvt_pk_bf16_f32 v14, v16, v17\n"
    "v_cvt_pk_bf16_f32 v15, v18, v19\n"
    "ds_write_b64 v163, v[12:13] offset:2048\n"
    "ds_write_b64 v163, v[14:15] offset:3072\n"
    "v_fma_f32 v20, v20, v196, -v200\n"
    "v_fma_f32 v21, v21, v197, -v201\n"
    "s_add_u32 m0, s65, 77824\n"
    "s_add_u32 s72, s85, 10240\n"
    "buffer_load_dwordx4 v89, s[20:23], s72 offen offset:0 nt lds\n"
    "v_fma_f32 v22, v22, v198, -v202\n"
    "v_fma_f32 v23, v23, v199, -v203\n"
    "v_fma_f32 v24, v24, v196, -v200\n"
    "v_fma_f32 v25, v25, v197, -v201\n"
    "v_fma_f32 v26, v26, v198, -v202\n"
    "v_fma_f32 v27, v27, v199, -v203\n"
    "v_cvt_pk_bf16_f32 v20, v20, v21\n"
    "v_cvt_pk_bf16_f32 v21, v22, v23\n"
    "v_cvt_pk_bf16_f32 v22, v24, v25\n"
    "v_cvt_pk_bf16_f32 v23, v26, v27\n"
    "ds_write_b64 v163, v[20:21] offset:4096\n"
    "ds_write_b64 v163, v[22:23] offset:5120\n"
    "s_waitcnt vmcnt(13)\n"
    "v_fma_f32 v100, v100, v180, -v184\n"
    "v_fma_f32 v101, v101, v181, -v185\n"
    "v_fma_f32 v102, v102, v182, -v186\n"
    "v_fma_f32 v103, v103, v183, -v187\n"
    "v_fma_f32 v104, v104, v180, -v184\n"
    "v_fma_f32 v105, v105, v181, -v185\n"
    "v_fma_f32 v106, v106, v182, -v186\n"
    "v_fma_f32 v107, v107, v183, -v187\n"
    "v_cvt_pk_bf16_f32 v100, v100, v101\n"
    "v_cvt_pk_bf16_f32 v101, v102, v103\n"
    "v_cvt_pk_bf16_f32 v102, v104, v105\n"
    "v_cvt_pk_bf16_f32 v103, v106, v107\n"
    "ds_write_b64 v163, v[100:101] offset:12288\n"
    "ds_write_b64 v163, v[102:103] offset:13312\n"
    "s_add_u32 m0, s65, 78848\n"
    "s_add_u32 s72, s85, 11264\n"
    "buffer_load_dwordx4 v90, s[20:23], s72 offen offset:0 nt lds\n"
    "v_fma_f32 v108, v108, v188, -v192\n"
    "v_fma_f32 v109, v109, v189, -v193\n"
    "v_fma_f32 v110, v110, v190, -v194\n"
    "v_fma_f32 v111, v111, v191, -v195\n"
    "v_fma_f32 v112, v112, v188, -v192\n"
    "v_fma_f32 v113, v113, v189, -v193\n"
    "v_fma_f32 v114, v114, v190, -v194\n"
    "v_fma_f32 v115, v115, v191, -v195\n"
    "v_cvt_pk_bf16_f32 v108, v108, v109\n"
    "v_cvt_pk_bf16_f32 v109, v110, v111\n"
    "v_cvt_pk_bf16_f32 v110, v112, v113\n"
    "v_cvt_pk_bf16_f32 v111, v114, v115\n"
    "ds_write_b64 v163, v[108:109] offset:14336\n"
    "ds_write_b64 v163, v[110:111] offset:15360\n"
    "v_fma_f32 v116, v116, v196, -v200\n"
    "v_fma_f32 v117, v117, v197, -v201\n"
    "v_fma_f32 v118, v118, v198, -v202\n"
    "v_fma_f32 v119, v119, v199, -v203\n"
    "v_fma_f32 v120, v120, v196, -v200\n"
    "v_fma_f32 v121, v121, v197, -v201\n"
    "v_fma_f32 v122, v122, v198, -v202\n"
    "v_fma_f32 v123, v123, v199, -v203\n"
    "v_cvt_pk_bf16_f32 v116, v116, v117\n"
    "v_cvt_pk_bf16_f32 v117, v118, v119\n"
    "v_cvt_pk_bf16_f32 v118, v120, v121\n"
    "v_cvt_pk_bf16_f32 v119, v122, v123\n"
    "ds_write_b64 v163, v[116:117] offset:16384\n"
    "ds_write_b64 v163, v[118:119] offset:17408\n"
    "s_waitcnt vmcnt(15)\n"
    "ds_read_b128 v[36:39], v154 offset:0\n"
    "ds_read_b128 v[40:43], v155 offset:0\n"
    "ds_read_b128 v[44:47], v154 offset:128\n"
    "ds_read_b128 v[48:51], v155 offset:128\n"
    "ds_read_b128 v[52:55], v154 offset:256\n"
    "ds_read_b128 v[56:59], v155 offset:256\n"
    "s_mov_b64 exec, 0\n"
    "s_bitcmp1_b32 s83, 1\n"
    "s_cbranch_scc1 .LwV\n"
    "s_cmp_eq_u32 0, 0\n"
    "s_branch .LwK\n"
    ".Lw2:\n"
    "s_bitcmp1_b32 s83, 1\n"
    "s_cbranch_scc1 .Lwp1\n"
    "s_branch .Lwp0\n"
    ".Lw3:\n"
    "s_mov_b64 exec, -1\n"
    "s_waitcnt lgkmcnt(0)\n"
    "s_barrier\n"
    "ds_read_b128 v[156:159], v2 offset:0\n"
    "ds_read_b128 v[160:163], v2 offset:2048\n"
    "ds_read_b128 v[164:167], v2 offset:4096\n"
    "ds_read_b128 v[168:171], v2 offset:6144\n"
    "ds_read_b128 v[172:175], v2 offset:8192\n"
    "ds_read_b128 v[176:179], v2 offset:10240\n"
    "ds_read_b128 v[180:183], v2 offset:1024\n"
    "ds_read_b128 v[184:187], v2 offset:3072\n"
    "ds_read_b128 v[188:191], v2 offset:5120\n"
    "ds_read_b128 v[192:195], v2 offset:7168\n"
    "ds_read_b128 v[196:199], v2 offset:9216\n"
    "ds_read_b128 v[200:203], v2 offset:11264\n"
    "s_add_u32 m0, s65, 49152\n"
    "s_add_u32 s72, s85, 12288\n"
    "buffer_load_dwordx4 v88, s[20:23], s72 offen offset:0 nt lds\n"
    "v_pk_maximum3_f16 v80, v36, v37, v80\n"
    "v_pk_maximum3_f16 v80, v38, v39, v80\n"
    "v_pk_add_u16 v36, s89, v36 clamp\n"
    "v_pk_add_u16 v37, s89, v37 clamp\n"
    "v_pk_add_u16 v38, s89, v38 clamp\n"
    "v_pk_add_u16 v39, s89, v39 clamp\n"
    "v_pk_maximum3_f16 v80, v40, v41, v80\n"
    "v_pk_maximum3_f16 v80, v42, v43, v80\n"
    "v_pk_add_u16 v40, s89, v40 clamp\n"
    "v_pk_add_u16 v41, s89, v41 clamp\n"
    "v_pk_add_u16 v42, s89, v42 clamp\n"
    "v_pk_add_u16 v43, s89, v43 clamp\n"
    "v_pk_maximum3_f16 v80, v44, v45, v80\n"
    "v_pk_maximum3_f16 v80, v46, v47, v80\n"
    "v_pk_add_u16 v44, s89, v44 clamp\n"
    "v_pk_add_u16 v45, s89, v45 clamp\n"
    "v_pk_add_u16 v46, s89, v46 clamp\n"
    "v_pk_add_u16 v47, s89, v47 clamp\n"
    "v_pk_maximum3_f16 v80, v48, v49, v80\n"
    "v_pk_maximum3_f16 v80, v50, v51, v80\n"
    "v_pk_add_u16 v48, s89, v48 clamp\n"
    "v_pk_add_u16 v49, s89, v49 clamp\n"
    "v_pk_add_u16 v50, s89, v50 clamp\n"
    "v_pk_add_u16 v51, s89, v51 clamp\n"
    "v_pk_maximum3_f16 v80, v52, v53, v80\n"
    "v_pk_maximum3_f16 v80, v54, v55, v80\n"
    "v_pk_add_u16 v52, s89, v52 clamp\n"
    "v_pk_add_u16 v53, s89, v53 clamp\n"
    "v_pk_add_u16 v54, s89, v54 clamp\n"
    "v_pk_add_u16 v55, s89, v55 clamp\n"
    "v_pk_maximum3_f16 v80, v56, v57, v80\n"
    "v_pk_maximum3_f16 v80, v58, v59, v80\n"
    "v_pk_add_u16 v56, s89, v56 clamp\n"
    "v_pk_add_u16 v57, s89, v57 clamp\n"
    "v_pk_add_u16 v58, s89, v58 clamp\n"
    "v_pk_add_u16 v59, s89, v59 clamp\n"
    "v_mov_b32 v108, 0\n"
    "v_mov_b32 v109, 0\n"
    "v_mov_b32 v110, 0\n"
    "v_mov_b32 v111, 0\n"
    "v_mov_b32 v112, 0\n"
    "v_mov_b32 v113, 0\n"
    "v_mov_b32 v114, 0\n"
    "v_mov_b32 v115, 0\n"
    "v_mov_b32 v116, 0\n"
    "v_mov_b32 v117, 0\n"
    "v_mov_b32 v118, 0\n"
    "v_mov_b32 v119, 0\n"
    "v_mov_b32 v120, 0\n"
    "v_mov_b32 v121, 0\n"
    "v_mov_b32 v122, 0\n"
    "v_mov_b32 v123, 0\n"
    "s_waitcnt lgkmcnt(11)\n"
    "v_mfma_f32_16x16x32_bf16 v[4:7], v[156:159], v[36:39], 0\n"
    "s_waitcnt lgkmcnt(10)\n"
    "v_mfma_f32_16x16x32_bf16 v[4:7], v[160:163], v[40:43], v[4:7]\n"
    "s_waitcnt lgkmcnt(9)\n"
    "v_mfma_f32_16x16x32_bf16 v[4:7], v[164:167], v[44:47], v[4:7]\n"
    "s_waitcnt lgkmcnt(8)\n"
    "v_mfma_f32_16x16x32_bf16 v[4:7], v[168:171], v[48:51], v[4:7]\n"
    "s_waitcnt lgkmcnt(7)\n"
    "v_mfma_f32_16x16x32_bf16 v[4:7], v[172:175], v[52:55], v[4:7]\n"
    "s_waitcnt lgkmcnt(6)\n"
    "v_mfma_f32_16x16x32_bf16 v[4:7], v[176:179], v[56:59], v[4:7]\n"
    "s_waitcnt lgkmcnt(5)\n"
    "v_mfma_f32_16x16x32_bf16 v[20:23], v[180:183], v[36:39], 0\n"
    "v_accvgpr_write_b32 a192, v36\n"
    "v_accvgpr_write_b32 a193, v37\n"
    "v_accvgpr_write_b32 a194, v38\n"
    "v_accvgpr_write_b32 a195, v39\n"
    "s_waitcnt lgkmcnt(4)\n"
    "v_mfma_f32_16x16x32_bf16 v[20:23], v[184:187], v[40:43], v[20:23]\n"
    "v_accvgpr_write_b32 a196, v40\n"
    "v_accvgpr_write_b32 a197, v41\n"
    "v_accvgpr_write_b32 a198, v42\n"
    "v_accvgpr_write_b32 a199, v43\n"
    "s_waitcnt lgkmcnt(3)\n"
    "v_mfma_f32_16x16x32_bf16 v[20:23], v[188:191], v[44:47], v[20:23]\n"
    "v_accvgpr_write_b32 a200, v44\n"
    "v_accvgpr_write_b32 a201, v45\n"
    "v_accvgpr_write_b32 a202, v46\n"
    "v_accvgpr_write_b32 a203, v47\n"
    "s_waitcnt lgkmcnt(2)\n"
    "v_mfma_f32_16x16x32_bf16 v[20:23], v[192:195], v[48:51], v[20:23]\n"
    "v_accvgpr_write_b32 a204, v48\n"
    "v_accvgpr_write_b32 a205, v49\n"
    "v_accvgpr_write_b32 a206, v50\n"
    "v_accvgpr_write_b32 a207, v51\n"
    "s_waitcnt lgkmcnt(1)\n"
    "v_mfma_f32_16x16x32_bf16 v[20:23], v[196:199], v[52:55], v[20:23]\n"
    "v_accvgpr_write_b32 a208, v52\n"
    "v_accvgpr_write_b32 a209, v53\n"
    "v_accvgpr_write_b32 a210, v54\n"
    "v_accvgpr_write_b32 a211, v55\n"
    "s_waitcnt lgkmcnt(0)\n"
    "v_mfma_f32_16x16x32_bf16 v[20:23], v[200:203], v[56:59], v[20:23]\n"
    "v_accvgpr_write_b32 a212, v56\n"
    "v_accvgpr_write_b32 a213, v57\n"
    "v_accvgpr_write_b32 a214, v58\n"
    "v_accvgpr_write_b32 a215, v59\n"
    "s_add_u32 m0, s65, 50176\n"
    "s_add_u32 s72, s85, 13312\n"
    "buffer_load_dwordx4 v89, s[20:23], s72 offen offset:0 nt lds\n"
    "v_mov_b32 v77, 155904\n"
    "v_max3_f32 v76, v4, v5, v6\n"
    "v_max3_f32 v76, v76, v7, v20\n"
    "v_max3_f32 v76, v76, v21, v22\n"
    "v_max_f32 v76, v76, v23\n"
    "v_mov_b32 v78, v76\n"
    "s_nop 1\n"
    "v_permlane32_swap_b32 v76, v78\n"
    "v_max_f32 v76, v76, v78\n"
    "v_mov_b32 v78, v76\n"
    "s_nop 1\n"
    "v_permlane16_swap_b32 v76, v78\n"
    "v_max_f32 v76, v76, v78\n"
    "ds_max_f32 v77, v76\n"
    "s_add_u32 m0, s65, 51200\n"
    "s_add_u32 s72, s85, 14336\n"
    "buffer_load_dwordx4 v90, s[20:23], s72 offen offset:0 nt lds\n"
    "buffer_load_dwordx2 v[236:237], v87, s[36:39], 0 offen offset:0 sc1\n"
    "s_waitcnt lgkmcnt(0)\n"
    "s_barrier\n"
    "ds_read_b32 v76, v77\n"
    "s_waitcnt vmcnt(10)\n"
    "buffer_store_dwordx2 v[238:239], v240, s[36:39], s80 offen offset:0 sc1\n"
    "s_lshl_b32 s73, s79, 8\n"
    "s_add_u32 s80, s80, s73\n"
    "s_mul_i32 s73, s79, 98304\n"
    "s_add_u32 s71, s71, s73\n"
    "s_waitcnt lgkmcnt(0)\n"
    "v_add_f32 v76, 0xc2b00000, v76\n"
    "v_sub_f32 v68, v68, v76\n"
    "v_sub_f32 v69, v69, v76\n"
    "v_sub_f32 v70, v70, v76\n"
    "v_sub_f32 v71, v71, v76\n"
    "v_sub_f32 v72, v72, v76\n"
    "v_sub_f32 v73, v73, v76\n"
    "v_sub_f32 v74, v74, v76\n"
    "v_sub_f32 v75, v75, v76\n"
    "ds_write_b128 v86, v[68:71] offset:0\n"
    "ds_write_b128 v86, v[72:75] offset:16\n"
    "s_mul_i32 s73, s79, 768\n"
    "v_add_u32 v77, s73, v250\n"
    "s_add_u32 m0, s65, 52224\n"
    "s_add_u32 s72, s85, 15360\n"
    "buffer_load_dwordx4 v88, s[20:23], s72 offen offset:0 nt lds\n"
    "s_waitcnt lgkmcnt(0)\n"
    "s_barrier\n"
    "ds_read_b128 v[100:103], v77 offset:0\n"
    "ds_read_b128 v[104:107], v77 offset:16\n"
    "s_add_u32 m0, s65, 53248\n"
    "s_add_u32 s72, s85, 16384\n"
    "buffer_load_dwordx4 v89, s[20:23], s72 offen offset:0 nt lds\n"
    "s_waitcnt lgkmcnt(1)\n"
    "v_add_f32 v4, v4, v100\n"
    "v_add_f32 v5, v5, v101\n"
    "v_add_f32 v6, v6, v102\n"
    "v_add_f32 v7, v7, v103\n"
    "s_waitcnt lgkmcnt(0)\n"
    "v_add_f32 v20, v20, v104\n"
    "v_add_f32 v21, v21, v105\n"
    "v_add_f32 v22, v22, v106\n"
    "v_add_f32 v23, v23, v107\n"
    "v_exp_f32 v4, v4\n"
    "v_exp_f32 v5, v5\n"
    "v_exp_f32 v6, v6\n"
    "v_exp_f32 v7, v7\n"
    "v_exp_f32 v20, v20\n"
    "v_exp_f32 v21, v21\n"
    "v_exp_f32 v22, v22\n"
    "v_exp_f32 v23, v23\n"
    "s_add_u32 m0, s65, 54272\n"
    "s_add_u32 s72, s85, 17408\n"
    "buffer_load_dwordx4 v90, s[20:23], s72 offen offset:0 nt lds\n"
    "s_waitcnt vmcnt(8)\n"
    "ds_read_b128 v[56:59], v85 offset:24832\n"
    "ds_read_b128 v[52:55], v84 offset:24832\n"
    "ds_read_b128 v[48:51], v85 offset:24704\n"
    "ds_read_b128 v[44:47], v84 offset:24704\n"
    "ds_read_b128 v[40:43], v85 offset:24576\n"
    "ds_read_b128 v[36:39], v84 offset:24576\n"
    "s_waitcnt lgkmcnt(5)\n"
    "v_pk_maximum3_f16 v80, v56, v57, v80\n"
    "v_pk_maximum3_f16 v80, v58, v59, v80\n"
    "v_pk_add_u16 v56, s89, v56 clamp\n"
    "v_pk_add_u16 v57, s89, v57 clamp\n"
    "v_pk_add_u16 v58, s89, v58 clamp\n"
    "v_pk_add_u16 v59, s89, v59 clamp\n"
    "s_waitcnt lgkmcnt(4)\n"
    "v_pk_maximum3_f16 v80, v52, v53, v80\n"
    "v_pk_maximum3_f16 v80, v54, v55, v80\n"
    "v_pk_add_u16 v52, s89, v52 clamp\n"
    "v_pk_add_u16 v53, s89, v53 clamp\n"
    "v_pk_add_u16 v54, s89, v54 clamp\n"
    "v_pk_add_u16 v55, s89, v55 clamp\n"
    "v_mfma_f32_16x16x32_bf16 v[8:11], v[176:179], v[56:59], v[100:103]\n"
    "s_waitcnt lgkmcnt(3)\n"
    "v_pk_maximum3_f16 v80, v48, v49, v80\n"
    "v_pk_maximum3_f16 v80, v50, v51, v80\n"
    "v_pk_add_u16 v48, s89, v48 clamp\n"
    "v_pk_add_u16 v49, s89, v49 clamp\n"
    "v_pk_add_u16 v50, s89, v50 clamp\n"
    "v_pk_add_u16 v51, s89, v51 clamp\n"
    "v_mfma_f32_16x16x32_bf16 v[8:11], v[172:175], v[52:55], v[8:11]\n"
    "s_waitcnt lgkmcnt(2)\n"
    "v_pk_maximum3_f16 v80, v44, v45, v80\n"
    "v_pk_maximum3_f16 v80, v46, v47, v80\n"
    "v_pk_add_u16 v44, s89, v44 clamp\n"
    "v_pk_add_u16 v45, s89, v45 clamp\n"
    "v_pk_add_u16 v46, s89, v46 clamp\n"
    "v_pk_add_u16 v47, s89, v47 clamp\n"
    "v_mfma_f32_16x16x32_bf16 v[8:11], v[168:171], v[48:51], v[8:11]\n"
    "s_waitcnt lgkmcnt(1)\n"
    "v_pk_maximum3_f16 v80, v40, v41, v80\n"
    "v_pk_maximum3_f16 v80, v42, v43, v80\n"
    "v_pk_add_u16 v40, s89, v40 clamp\n"
    "v_pk_add_u16 v41, s89, v41 clamp\n"
    "v_pk_add_u16 v42, s89, v42 clamp\n"
    "v_pk_add_u16 v43, s89, v43 clamp\n"
    "v_mfma_f32_16x16x32_bf16 v[8:11], v[164:167], v[44:47], v[8:11]\n"
    "s_waitcnt lgkmcnt(0)\n"
    "v_pk_maximum3_f16 v80, v36, v37, v80\n"
    "v_pk_maximum3_f16 v80, v38, v39, v80\n"
    "v_pk_add_u16 v36, s89, v36 clamp\n"
    "v_pk_add_u16 v37, s89, v37 clamp\n"
    "v_pk_add_u16 v38, s89, v38 clamp\n"
    "v_pk_add_u16 v39, s89, v39 clamp\n"
    "v_mfma_f32_16x16x32_bf16 v[8:11], v[160:163], v[40:43], v[8:11]\n"
    "s_nop 0\n"
    "v_mfma_f32_16x16x32_bf16 v[8:11], v[156:159], v[36:39], v[8:11]\n"
    "v_mfma_f32_16x16x32_bf16 v[24:27], v[200:203], v[56:59], v[104:107]\n"
    "v_accvgpr_write_b32 a236, v56\n"
    "v_accvgpr_write_b32 a237, v57\n"
    "v_accvgpr_write_b32 a238, v58\n"
    "v_accvgpr_write_b32 a239, v59\n"
    "v_mfma_f32_16x16x32_bf16 v[24:27], v[196:199], v[52:55], v[24:27]\n"
    "v_accvgpr_write_b32 a232, v52\n"
    "v_accvgpr_write_b32 a233, v53\n"
    "v_accvgpr_write_b32 a234, v54\n"
    "v_accvgpr_write_b32 a235, v55\n"
    "v_mfma_f32_16x16x32_bf16 v[24:27], v[192:195], v[48:51], v[24:27]\n"
    "v_accvgpr_write_b32 a228, v48\n"
    "v_accvgpr_write_b32 a229, v49\n"
    "v_accvgpr_write_b32 a230, v50\n"
    "v_accvgpr_write_b32 a231, v51\n"
    "v_mfma_f32_16x16x32_bf16 v[24:27], v[188:191], v[44:47], v[24:27]\n"
    "v_accvgpr_write_b32 a224, v44\n"
    "v_accvgpr_write_b32 a225, v45\n"
    "v_accvgpr_write_b32 a226, v46\n"
    "v_accvgpr_write_b32 a227, v47\n"
    "v_mfma_f32_16x16x32_bf16 v[24:27], v[184:187], v[40:43], v[24:27]\n"
    "v_accvgpr_write_b32 a220, v40\n"
    "v_accvgpr_write_b32 a221, v41\n"
    "v_accvgpr_write_b32 a222, v42\n"
    "v_accvgpr_write_b32 a223, v43\n"
    "v_mfma_f32_16x16x32_bf16 v[24:27], v[180:183], v[36:39], v[24:27]\n"
    "v_accvgpr_write_b32 a216, v36\n"
    "v_accvgpr_write_b32 a217, v37\n"
    "v_accvgpr_write_b32 a218, v38\n"
    "v_accvgpr_write_b32 a219, v39\n"
    "s_add_u32 m0, s65, 98304\n"
    "s_add_u32 s72, s85, 18432\n"
    "buffer_load_dwordx4 v88, s[20:23], s72 offen offset:0 nt lds\n"
    "v_exp_f32 v8, v8\n"
    "v_exp_f32 v9, v9\n"
    "v_exp_f32 v10, v10\n"
    "v_exp_f32 v11, v11\n"
    "v_exp_f32 v24, v24\n"
    "v_exp_f32 v25, v25\n"
    "v_exp_f32 v26, v26\n"
    "v_exp_f32 v27, v27\n"
    "s_add_u32 m0, s65, 99328\n"
    "s_add_u32 s72, s85, 19456\n"
    "buffer_load_dwordx4 v89, s[20:23], s72 offen offset:0 nt lds\n"
    "s_add_u32 m0, s65, 100352\n"
    "s_add_u32 s72, s85, 20480\n"
    "buffer_load_dwordx4 v90, s[20:23], s72 offen offset:0 nt lds\n"
    "s_waitcnt vmcnt(7)\n"
    "v_cmp_eq_u64 s[54:55], v[236:237], v[238:239]\n"
    "s_nop 0\n"
    "s_cmp_eq_u64 s[54:55], -1\n"
    "s_cbranch_scc0 .LpollAV\n"
    ".LpollAVr:\n"
    "s_add_u32 m0, s65, 101376\n"
    "s_add_u32 s72, s85, 21504\n"
    "buffer_load_dwordx4 v88, s[20:23], s72 offen offset:0 nt lds\n"
    "s_add_u32 m0, s65, 24576\n"
    "s_add_u32 s72, s65, 49152\n"
    "buffer_load_dwordx4 v1, s[16:19], s72 offen offset:0 sc1 lds\n"
    "s_add_u32 m0, s65, 25600\n"
    "s_add_u32 s72, s65, 50176\n"
    "buffer_load_dwordx4 v1, s[16:19], s72 offen offset:0 sc1 lds\n"
    "s_add_u32 m0, s65, 102400\n"
    "s_add_u32 s72, s85, 22528\n"
    "buffer_load_dwordx4 v89, s[20:23], s72 offen offset:0 nt lds\n"
    "s_add_u32 m0, s65, 26624\n"
    "s_add_u32 s72, s65, 51200\n"
    "buffer_load_dwordx4 v1, s[16:19], s72 offen offset:0 sc1 lds\n"
    "s_add_u32 m0, s65, 27648\n"
    "s_add_u32 s72, s65, 52224\n"
    "buffer_load_dwordx4 v1, s[16:19], s72 offen offset:0 sc1 lds\n"
    "s_add_u32 m0, s65, 103424\n"
    "s_add_u32 s72, s85, 23552\n"
    "buffer_load_dwordx4 v90, s[20:23], s72 offen offset:0 nt lds\n"
    "s_add_u32 m0, s65, 28672\n"
    "s_add_u32 s72, s65, 53248\n"
    "buffer_load_dwordx4 v1, s[16:19], s72 offen offset:0 sc1 lds\n"
    "s_add_u32 m0, s65, 29696\n"
    "s_add_u32 s72, s65, 54272\n"
    "buffer_load_dwordx4 v1, s[16:19], s72 offen offset:0 sc1 lds\n"
    "s_add_u32 m0, s65, 73728\n"
    "s_add_u32 s72, s65, 73728\n"
    "buffer_load_dwordx4 v1, s[16:19], s72 offen offset:0 sc1 lds\n"
    "s_add_u32 m0, s65, 74752\n"
    "s_add_u32 s72, s65, 74752\n"
    "buffer_load_dwordx4 v1, s[16:19], s72 offen offset:0 sc1 lds\n"
    "s_add_u32 m0, s65, 75776\n"
    "s_add_u32 s72, s65, 75776\n"
    "buffer_load_dwordx4 v1, s[16:19], s72 offen offset:0 sc1 lds\n"
    "s_add_u32 m0, s65, 76800\n"
    "s_add_u32 s72, s65, 76800\n"
    "buffer_load_dwordx4 v1, s[16:19], s72 offen offset:0 sc1 lds\n"
    "s_add_u32 m0, s65, 77824\n"
    "s_add_u32 s72, s65, 77824\n"
    "buffer_load_dwordx4 v1, s[16:19], s72 offen offset:0 sc1 lds\n"
    "s_add_u32 m0, s65, 78848\n"
    "s_add_u32 s72, s65, 78848\n"
    "buffer_load_dwordx4 v1, s[16:19], s72 offen offset:0 sc1 lds\n"
    "s_bitcmp1_b32 s83, 1\n"
    "s_cbranch_scc1 .Lf4b\n"
    "s_waitcnt vmcnt(18)\n"
    "ds_read_b128 v[36:39], v84 offset:0\n"
    "ds_read_b128 v[40:43], v85 offset:0\n"
    "ds_read_b128 v[44:47], v84 offset:128\n"
    "ds_read_b128 v[48:51], v85 offset:128\n"
    "ds_read_b128 v[124:127], v84 offset:256\n"
    "ds_read_b128 v[128:131], v85 offset:256\n"
    "s_waitcnt lgkmcnt(5)\n"
    "v_pk_maximum3_f16 v80, v36, v37, v80\n"
    "v_pk_maximum3_f16 v80, v38, v39, v80\n"
    "v_pk_add_u16 v36, s89, v36 clamp\n"
    "v_pk_add_u16 v37, s89, v37 clamp\n"
    "v_pk_add_u16 v38, s89, v38 clamp\n"
    "v_pk_add_u16 v39, s89, v39 clamp\n"
    "s_waitcnt lgkmcnt(4)\n"
    "v_pk_maximum3_f16 v80, v40, v41, v80\n"
    "v_pk_maximum3_f16 v80, v42, v43, v80\n"
    "v_pk_add_u16 v40, s89, v40 clamp\n"
    "v_pk_add_u16 v41, s89, v41 clamp\n"
    "v_pk_add_u16 v42, s89, v42 clamp\n"
    "v_pk_add_u16 v43, s89, v43 clamp\n"
    "v_mfma_f32_16x16x32_bf16 v[12:15], v[156:159], v[36:39], v[100:103]\n"
    "s_waitcnt lgkmcnt(3)\n"
    "v_pk_maximum3_f16 v80, v44, v45, v80\n"
    "v_pk_maximum3_f16 v80, v46, v47, v80\n"
    "v_pk_add_u16 v44, s89, v44 clamp\n"
    "v_pk_add_u16 v45, s89, v45 clamp\n"
    "v_pk_add_u16 v46, s89, v46 clamp\n"
    "v_pk_add_u16 v47, s89, v47 clamp\n"
    "v_mfma_f32_16x16x32_bf16 v[12:15], v[160:163], v[40:43], v[12:15]\n"
    "s_waitcnt lgkmcnt(2)\n"
    "v_pk_maximum3_f16 v80, v48, v49, v80\n"
    "v_pk_maximum3_f16 v80, v50, v51, v80\n"
    "v_pk_add_u16 v48, s89, v48 clamp\n"
    "v_pk_add_u16 v49, s89, v49 clamp\n"
    "v_pk_add_u16 v50, s89, v50 clamp\n"
    "v_pk_add_u16 v51, s89, v51 clamp\n"
    "v_mfma_f32_16x16x32_bf16 v[12:15], v[164:167], v[44:47], v[12:15]\n"
    "s_waitcnt lgkmcnt(1)\n"
    "v_pk_maximum3_f16 v80, v124, v125, v80\n"
    "v_pk_maximum3_f16 v80, v126, v127, v80\n"
    "v_pk_add_u16 v124, s89, v124 clamp\n"
    "v_pk_add_u16 v125, s89, v125 clamp\n"
    "v_pk_add_u16 v126, s89, v126 clamp\n"
    "v_pk_add_u16 v127, s89, v127 clamp\n"
    "v_mfma_f32_16x16x32_bf16 v[12:15], v[168:171], v[48:51], v[12:15]\n"
    "s_waitcnt lgkmcnt(0)\n"
    "v_pk_maximum3_f16 v80, v128, v129, v80\n"
    "v_pk_maximum3_f16 v80, v130, v131, v80\n"
    "v_pk_add_u16 v128, s89, v128 clamp\n"
    "v_pk_add_u16 v129, s89, v129 clamp\n"
    "v_pk_add_u16 v130, s89, v130 clamp\n"
    "v_pk_add_u16 v131, s89, v131 clamp\n"
    "v_mfma_f32_16x16x32_bf16 v[12:15], v[172:175], v[124:127], v[12:15]\n"
    "s_nop 0\n"
    "v_mfma_f32_16x16x32_bf16 v[12:15], v[176:179], v[128:131], v[12:15]\n"
    "v_mfma_f32_16x16x32_bf16 v[28:31], v[180:183], v[36:39], v[104:107]\n"
    "v_accvgpr_write_b32 a240, v36\n"
    "v_accvgpr_write_b32 a241, v37\n"
    "v_accvgpr_write_b32 a242, v38\n"
    "v_accvgpr_write_b32 a243, v39\n"
    "v_mfma_f32_16x16x32_bf16 v[28:31], v[184:187], v[40:43], v[28:31]\n"
    "v_accvgpr_write_b32 a244, v40\n"
    "v_accvgpr_write_b32 a245, v41\n"
    "v_accvgpr_write_b32 a246, v42\n"
    "v_accvgpr_write_b32 a247, v43\n"
    "v_mfma_f32_16x16x32_bf16 v[28:31], v[188:191], v[44:47], v[28:31]\n"
    "v_accvgpr_write_b32 a248, v44\n"
    "v_accvgpr_write_b32 a249, v45\n"
    "v_accvgpr_write_b32 a250, v46\n"
    "v_accvgpr_write_b32 a251, v47\n"
    "v_mfma_f32_16x16x32_bf16 v[28:31], v[192:195], v[48:51], v[28:31]\n"
    "v_accvgpr_write_b32 a252, v48\n"
    "v_accvgpr_write_b32 a253, v49\n"
    "v_accvgpr_write_b32 a254, v50\n"
    "v_accvgpr_write_b32 a255, v51\n"
    "v_mfma_f32_16x16x32_bf16 v[28:31], v[196:199], v[124:127], v[28:31]\n"
    "v_mfma_f32_16x16x32_bf16 v[28:31], v[200:203], v[128:131], v[28:31]\n"
    "v_exp_f32 v12, v12\n"
    "v_exp_f32 v13, v13\n"
    "v_exp_f32 v14, v14\n"
    "v_exp_f32 v15, v15\n"
    "s_nop 3\n"
    "v_exp_f32 v28, v28\n"
    "v_exp_f32 v29, v29\n"
    "v_exp_f32 v30, v30\n"
    "v_exp_f32 v31, v31\n"
    "s_waitcnt vmcnt(8)\n"
    "ds_read_b128 v[152:155], v85 offset:49408\n"
    "ds_read_b128 v[148:151], v84 offset:49408\n"
    "ds_read_b128 v[144:147], v85 offset:49280\n"
    "ds_read_b128 v[140:143], v84 offset:49280\n"
    "ds_read_b128 v[136:139], v85 offset:49152\n"
    "ds_read_b128 v[132:135], v84 offset:49152\n"
    "s_waitcnt lgkmcnt(5)\n"
    "v_pk_maximum3_f16 v80, v152, v153, v80\n"
    "v_pk_maximum3_f16 v80, v154, v155, v80\n"
    "v_pk_add_u16 v152, s89, v152 clamp\n"
    "v_pk_add_u16 v153, s89, v153 clamp\n"
    "v_pk_add_u16 v154, s89, v154 clamp\n"
    "v_pk_add_u16 v155, s89, v155 clamp\n"
    "s_waitcnt lgkmcnt(4)\n"
    "v_pk_maximum3_f16 v80, v148, v149, v80\n"
    "v_pk_maximum3_f16 v80, v150, v151, v80\n"
    "v_pk_add_u16 v148, s89, v148 clamp\n"
    "v_pk_add_u16 v149, s89, v149 clamp\n"
    "v_pk_add_u16 v150, s89, v150 clamp\n"
    "v_pk_add_u16 v151, s89, v151 clamp\n"
    "v_mfma_f32_16x16x32_bf16 v[16:19], v[176:179], v[152:155], v[100:103]\n"
    "s_waitcnt lgkmcnt(3)\n"
    "v_pk_maximum3_f16 v80, v144, v145, v80\n"
    "v_pk_maximum3_f16 v80, v146, v147, v80\n"
    "v_pk_add_u16 v144, s89, v144 clamp\n"
    "v_pk_add_u16 v145, s89, v145 clamp\n"
    "v_pk_add_u16 v146, s89, v146 clamp\n"
    "v_pk_add_u16 v147, s89, v147 clamp\n"
    "v_mfma_f32_16x16x32_bf16 v[16:19], v[172:175], v[148:151], v[16:19]\n"
    "s_waitcnt lgkmcnt(2)\n"
    "v_pk_maximum3_f16 v80, v140, v141, v80\n"
    "v_pk_maximum3_f16 v80, v142, v143, v80\n"
    "v_pk_add_u16 v140, s89, v140 clamp\n"
    "v_pk_add_u16 v141, s89, v141 clamp\n"
    "v_pk_add_u16 v142, s89, v142 clamp\n"
    "v_pk_add_u16 v143, s89, v143 clamp\n"
    "v_mfma_f32_16x16x32_bf16 v[16:19], v[168:171], v[144:147], v[16:19]\n"
    "s_waitcnt lgkmcnt(1)\n"
    "v_pk_maximum3_f16 v80, v136, v137, v80\n"
    "v_pk_maximum3_f16 v80, v138, v139, v80\n"
    "v_pk_add_u16 v136, s89, v136 clamp\n"
    "v_pk_add_u16 v137, s89, v137 clamp\n"
    "v_pk_add_u16 v138, s89, v138 clamp\n"
    "v_pk_add_u16 v139, s89, v139 clamp\n"
    "v_mfma_f32_16x16x32_bf16 v[16:19], v[164:167], v[140:143], v[16:19]\n"
    "s_waitcnt lgkmcnt(0)\n"
    "v_pk_maximum3_f16 v80, v132, v133, v80\n"
    "v_pk_maximum3_f16 v80, v134, v135, v80\n"
    "v_pk_add_u16 v132, s89, v132 clamp\n"
    "v_pk_add_u16 v133, s89, v133 clamp\n"
    "v_pk_add_u16 v134, s89, v134 clamp\n"
    "v_pk_add_u16 v135, s89, v135 clamp\n"
    "v_mfma_f32_16x16x32_bf16 v[16:19], v[160:163], v[136:139], v[16:19]\n"
    "s_nop 0\n"
    "v_mfma_f32_16x16x32_bf16 v[16:19], v[156:159], v[132:135], v[16:19]\n"
    "v_mfma_f32_16x16x32_bf16 v[32:35], v[200:203], v[152:155], v[104:107]\n"
    "v_mfma_f32_16x16x32_bf16 v[32:35], v[196:199], v[148:151], v[32:35]\n"
    "v_mfma_f32_16x16x32_bf16 v[32:35], v[192:195], v[144:147], v[32:35]\n"
    "v_mfma_f32_16x16x32_bf16 v[32:35], v[188:191], v[140:143], v[32:35]\n"
    "v_mfma_f32_16x16x32_bf16 v[32:35], v[184:187], v[136:139], v[32:35]\n"
    "v_mfma_f32_16x16x32_bf16 v[32:35], v[180:183], v[132:135], v[32:35]\n"
    "ds_read_b128 v[156:159], v2 offset:12288\n"
    "ds_read_b128 v[160:163], v2 offset:14336\n"
    "ds_read_b128 v[164:167], v2 offset:16384\n"
    "ds_read_b128 v[168:171], v2 offset:18432\n"
    "ds_read_b128 v[172:175], v2 offset:20480\n"
    "ds_read_b128 v[176:179], v2 offset:22528\n"
    "ds_read_b128 v[100:103], v77 offset:128\n"
    "v_pk_add_u16 v80, s89, v80\n"
    "v_and_b32 v80, 0x80008000, v80\n"
    "v_cmp_ne_u32 s[34:35], 0, v80\n"
    "v_exp_f32 v16, v16\n"
    "v_exp_f32 v17, v17\n"
    "v_exp_f32 v18, v18\n"
    "v_exp_f32 v19, v19\n"
    "v_exp_f32 v32, v32\n"
    "v_exp_f32 v33, v33\n"
    "v_exp_f32 v34, v34\n"
    "v_exp_f32 v35, v35\n"
    "s_mov_b32 s66, 0\n"
    "s_waitcnt lgkmcnt(0)\n"
    "s_waitcnt vmcnt(0)\n"
    "ds_add_u32 v247, v246\n"
    "s_nop 7\n"
    "s_branch .LPV\n"
    "s_branch .Lf4e\n"
    ".Lf4b:\n"
    "s_add_u32 s75, s71, s63\n"
    "buffer_load_dwordx4 v[204:207], v242, s[40:43], s75 offen offset:0 nt\n"
    "buffer_load_dwordx4 v[208:211], v242, s[40:43], s75 offen offset:768 nt\n"
    "buffer_load_dwordx4 v[212:215], v242, s[40:43], s75 offen offset:256 nt\n"
    "buffer_load_dwordx4 v[216:219], v242, s[40:43], s75 offen offset:1024 nt\n"
    "buffer_load_dwordx4 v[220:223], v242, s[40:43], s75 offen offset:512 nt\n"
    "buffer_load_dwordx4 v[224:227], v242, s[40:43], s75 offen offset:1280 nt\n"
    "s_waitcnt vmcnt(24)\n"
    "ds_read_b128 v[36:39], v84 offset:0\n"
    "ds_read_b128 v[40:43], v85 offset:0\n"
    "ds_read_b128 v[44:47], v84 offset:128\n"
    "ds_read_b128 v[48:51], v85 offset:128\n"
    "ds_read_b128 v[124:127], v84 offset:256\n"
    "ds_read_b128 v[128:131], v85 offset:256\n"
    "s_waitcnt lgkmcnt(5)\n"
    "v_pk_maximum3_f16 v80, v36, v37, v80\n"
    "v_pk_maximum3_f16 v80, v38, v39, v80\n"
    "v_pk_add_u16 v36, s89, v36 clamp\n"
    "v_pk_add_u16 v37, s89, v37 clamp\n"
    "v_pk_add_u16 v38, s89, v38 clamp\n"
    "v_pk_add_u16 v39, s89, v39 clamp\n"
    "s_waitcnt lgkmcnt(4)\n"
    "v_pk_maximum3_f16 v80, v40, v41, v80\n"
    "v_pk_maximum3_f16 v80, v42, v43, v80\n"
    "v_pk_add_u16 v40, s89, v40 clamp\n"
    "v_pk_add_u16 v41, s89, v41 clamp\n"
    "v_pk_add_u16 v42, s89, v42 clamp\n"
    "v_pk_add_u16 v43, s89, v43 clamp\n"
    "v_mfma_f32_16x16x32_bf16 v[12:15], v[156:159], v[36:39], v[100:103]\n"
    "s_waitcnt lgkmcnt(3)\n"
    "v_pk_maximum3_f16 v80, v44, v45, v80\n"
    "v_pk_maximum3_f16 v80, v46, v47, v80\n"
    "v_pk_add_u16 v44, s89, v44 clamp\n"
    "v_pk_add_u16 v45, s89, v45 clamp\n"
    "v_pk_add_u16 v46, s89, v46 clamp\n"
    "v_pk_add_u16 v47, s89, v47 clamp\n"
    "v_mfma_f32_16x16x32_bf16 v[12:15], v[160:163], v[40:43], v[12:15]\n"
    "s_waitcnt lgkmcnt(2)\n"
    "v_pk_maximum3_f16 v80, v48, v49, v80\n"
    "v_pk_maximum3_f16 v80, v50, v51, v80\n"
    "v_pk_add_u16 v48, s89, v48 clamp\n"
    "v_pk_add_u16 v49, s89, v49 clamp\n"
    "v_pk_add_u16 v50, s89, v50 clamp\n"
    "v_pk_add_u16 v51, s89, v51 clamp\n"
    "v_mfma_f32_16x16x32_bf16 v[12:15], v[164:167], v[44:47], v[12:15]\n"
    "s_waitcnt lgkmcnt(1)\n"
    "v_pk_maximum3_f16 v80, v124, v125, v80\n"
    "v_pk_maximum3_f16 v80, v126, v127, v80\n"
    "v_pk_add_u16 v124, s89, v124 clamp\n"
    "v_pk_add_u16 v125, s89, v125 clamp\n"
    "v_pk_add_u16 v126, s89, v126 clamp\n"
    "v_pk_add_u16 v127, s89, v127 clamp\n"
    "v_mfma_f32_16x16x32_bf16 v[12:15], v[168:171], v[48:51], v[12:15]\n"
    "s_waitcnt lgkmcnt(0)\n"
    "v_pk_maximum3_f16 v80, v128, v129, v80\n"
    "v_pk_maximum3_f16 v80, v130, v131, v80\n"
    "v_pk_add_u16 v128, s89, v128 clamp\n"
    "v_pk_add_u16 v129, s89, v129 clamp\n"
    "v_pk_add_u16 v130, s89, v130 clamp\n"
    "v_pk_add_u16 v131, s89, v131 clamp\n"
    "v_mfma_f32_16x16x32_bf16 v[12:15], v[172:175], v[124:127], v[12:15]\n"
    "s_nop 0\n"
    "v_mfma_f32_16x16x32_bf16 v[12:15], v[176:179], v[128:131], v[12:15]\n"
    "v_mfma_f32_16x16x32_bf16 v[28:31], v[180:183], v[36:39], v[104:107]\n"
    "v_accvgpr_write_b32 a240, v36\n"
    "v_accvgpr_write_b32 a241, v37\n"
    "v_accvgpr_write_b32 a242, v38\n"
    "v_accvgpr_write_b32 a243, v39\n"
    "v_mfma_f32_16x16x32_bf16 v[28:31], v[184:187], v[40:43], v[28:31]\n"
    "v_accvgpr_write_b32 a244, v40\n"
    "v_accvgpr_write_b32 a245, v41\n"
    "v_accvgpr_write_b32 a246, v42\n"
    "v_accvgpr_write_b32 a247, v43\n"
    "v_mfma_f32_16x16x32_bf16 v[28:31], v[188:191], v[44:47], v[28:31]\n"
    "v_accvgpr_write_b32 a248, v44\n"
    "v_accvgpr_write_b32 a249, v45\n"
    "v_accvgpr_write_b32 a250, v46\n"
    "v_accvgpr_write_b32 a251, v47\n"
    "v_mfma_f32_16x16x32_bf16 v[28:31], v[192:195], v[48:51], v[28:31]\n"
    "v_accvgpr_write_b32 a252, v48\n"
    "v_accvgpr_write_b32 a253, v49\n"
    "v_accvgpr_write_b32 a254, v50\n"
    "v_accvgpr_write_b32 a255, v51\n"
    "v_mfma_f32_16x16x32_bf16 v[28:31], v[196:199], v[124:127], v[28:31]\n"
    "v_mfma_f32_16x16x32_bf16 v[28:31], v[200:203], v[128:131], v[28:31]\n"
    "v_exp_f32 v12, v12\n"
    "v_exp_f32 v13, v13\n"
    "v_exp_f32 v14, v14\n"
    "v_exp_f32 v15, v15\n"
    "s_nop 3\n"
    "v_exp_f32 v28, v28\n"
    "v_exp_f32 v29, v29\n"
    "v_exp_f32 v30, v30\n"
    "v_exp_f32 v31, v31\n"
    "s_waitcnt vmcnt(14)\n"
    "ds_read_b128 v[152:155], v85 offset:49408\n"
    "ds_read_b128 v[148:151], v84 offset:49408\n"
    "ds_read_b128 v[144:147], v85 offset:49280\n"
    "ds_read_b128 v[140:143], v84 offset:49280\n"
    "ds_read_b128 v[136:139], v85 offset:49152\n"
    "ds_read_b128 v[132:135], v84 offset:49152\n"
    "s_waitcnt lgkmcnt(5)\n"
    "v_pk_maximum3_f16 v80, v152, v153, v80\n"
    "v_pk_maximum3_f16 v80, v154, v155, v80\n"
    "v_pk_add_u16 v152, s89, v152 clamp\n"
    "v_pk_add_u16 v153, s89, v153 clamp\n"
    "v_pk_add_u16 v154, s89, v154 clamp\n"
    "v_pk_add_u16 v155, s89, v155 clamp\n"
    "s_waitcnt lgkmcnt(4)\n"
    "v_pk_maximum3_f16 v80, v148, v149, v80\n"
    "v_pk_maximum3_f16 v80, v150, v151, v80\n"
    "v_pk_add_u16 v148, s89, v148 clamp\n"
    "v_pk_add_u16 v149, s89, v149 clamp\n"
    "v_pk_add_u16 v150, s89, v150 clamp\n"
    "v_pk_add_u16 v151, s89, v151 clamp\n"
    "v_mfma_f32_16x16x32_bf16 v[16:19], v[176:179], v[152:155], v[100:103]\n"
    "s_waitcnt lgkmcnt(3)\n"
    "v_pk_maximum3_f16 v80, v144, v145, v80\n"
    "v_pk_maximum3_f16 v80, v146, v147, v80\n"
    "v_pk_add_u16 v144, s89, v144 clamp\n"
    "v_pk_add_u16 v145, s89, v145 clamp\n"
    "v_pk_add_u16 v146, s89, v146 clamp\n"
    "v_pk_add_u16 v147, s89, v147 clamp\n"
    "v_mfma_f32_16x16x32_bf16 v[16:19], v[172:175], v[148:151], v[16:19]\n"
    "s_waitcnt lgkmcnt(2)\n"
    "v_pk_maximum3_f16 v80, v140, v141, v80\n"
    "v_pk_maximum3_f16 v80, v142, v143, v80\n"
    "v_pk_add_u16 v140, s89, v140 clamp\n"
    "v_pk_add_u16 v141, s89, v141 clamp\n"
    "v_pk_add_u16 v142, s89, v142 clamp\n"
    "v_pk_add_u16 v143, s89, v143 clamp\n"
    "v_mfma_f32_16x16x32_bf16 v[16:19], v[168:171], v[144:147], v[16:19]\n"
    "s_waitcnt lgkmcnt(1)\n"
    "v_pk_maximum3_f16 v80, v136, v137, v80\n"
    "v_pk_maximum3_f16 v80, v138, v139, v80\n"
    "v_pk_add_u16 v136, s89, v136 clamp\n"
    "v_pk_add_u16 v137, s89, v137 clamp\n"
    "v_pk_add_u16 v138, s89, v138 clamp\n"
    "v_pk_add_u16 v139, s89, v139 clamp\n"
    "v_mfma_f32_16x16x32_bf16 v[16:19], v[164:167], v[140:143], v[16:19]\n"
    "s_waitcnt lgkmcnt(0)\n"
    "v_pk_maximum3_f16 v80, v132, v133, v80\n"
    "v_pk_maximum3_f16 v80, v134, v135, v80\n"
    "v_pk_add_u16 v132, s89, v132 clamp\n"
    "v_pk_add_u16 v133, s89, v133 clamp\n"
    "v_pk_add_u16 v134, s89, v134 clamp\n"
    "v_pk_add_u16 v135, s89, v135 clamp\n"
    "v_mfma_f32_16x16x32_bf16 v[16:19], v[160:163], v[136:139], v[16:19]\n"
    "s_nop 0\n"
    "v_mfma_f32_16x16x32_bf16 v[16:19], v[156:159], v[132:135], v[16:19]\n"
    "v_mfma_f32_16x16x32_bf16 v[32:35], v[200:203], v[152:155], v[104:107]\n"
    "v_mfma_f32_16x16x32_bf16 v[32:35], v[196:199], v[148:151], v[32:35]\n"
    "v_mfma_f32_16x16x32_bf16 v[32:35], v[192:195], v[144:147], v[32:35]\n"
    "v_mfma_f32_16x16x32_bf16 v[32:35], v[188:191], v[140:143], v[32:35]\n"
    "v_mfma_f32_16x16x32_bf16 v[32:35], v[184:187], v[136:139], v[32:35]\n"
    "v_mfma_f32_16x16x32_bf16 v[32:35], v[180:183], v[132:135], v[32:35]\n"
    "ds_read_b128 v[156:159], v2 offset:12288\n"
    "ds_read_b128 v[160:163], v2 offset:14336\n"
    "ds_read_b128 v[164:167], v2 offset:16384\n"
    "ds_read_b128 v[168:171], v2 offset:18432\n"
    "ds_read_b128 v[172:175], v2 offset:20480\n"
    "ds_read_b128 v[176:179], v2 offset:22528\n"
    "ds_read_b128 v[100:103], v77 offset:128\n"
    "v_pk_add_u16 v80, s89, v80\n"
    "v_and_b32 v80, 0x80008000, v80\n"
    "v_cmp_ne_u32 s[34:35], 0, v80\n"
    "v_exp_f32 v16, v16\n"
    "v_exp_f32 v17, v17\n"
    "v_exp_f32 v18, v18\n"
    "v_exp_f32 v19, v19\n"
    "v_exp_f32 v32, v32\n"
    "v_exp_f32 v33, v33\n"
    "v_exp_f32 v34, v34\n"
    "v_exp_f32 v35, v35\n"
    "s_mov_b32 s66, 0\n"
    "s_waitcnt lgkmcnt(0)\n"
    "s_waitcnt vmcnt(6)\n"
    "ds_add_u32 v247, v246\n"
    "s_nop 7\n"
    "s_branch .LNV\n"
    ".Lf4e:\n"
    "s_nop 4\n"
    ".Lf5e:\n"
    "s_nop 4\n"
    ".fill ((0x20000 - (.LwKe - .LwK) - (. - _Z8attn_fwdPKtPKiS2_S0_S0_S0_S0_PcPt)) & 4095) / 4, 4, 0xBF800000\n"
    ".LwK:\n"
    "s_branch .LwKe\n"
    ".LPK:\n"
    "v_mfma_f32_16x16x32_bf16 v[36:39], v[156:159], a[192:195], v[100:103]\n"
    "s_add_u32 s71, s71, 98304\n"
    "ds_read_b128 v[180:183], v2 offset:13312\n"
    "v_cvt_pk_bf16_f32 v68, v4, v5\n"
    "v_mfma_f32_16x16x32_bf16 v[36:39], v[160:163], a[196:199], v[36:39]\n"
    "s_add_u32 s75, s71, s63\n"
    "ds_read_b128 v[184:187], v2 offset:15360\n"
    "v_cvt_pk_bf16_f32 v69, v6, v7\n"
    "v_mfma_f32_16x16x32_bf16 v[36:39], v[164:167], a[200:203], v[36:39]\n"
    "s_add_u32 s76, s75, 6144\n"
    "ds_read_b128 v[188:191], v2 offset:17408\n"
    "v_cvt_pk_bf16_f32 v76, v8, v9\n"
    "s_nop 0\n"
    "v_mfma_f32_16x16x32_bf16 v[36:39], v[168:171], a[204:207], v[36:39]\n"
    "s_add_u32 s77, s75, 24576\n"
    "ds_read_b128 v[192:195], v2 offset:19456\n"
    "v_cvt_pk_bf16_f32 v77, v10, v11\n"
    "v_mfma_f32_16x16x32_bf16 v[36:39], v[172:175], a[208:211], v[36:39]\n"
    "s_add_u32 s78, s75, 30720\n"
    "ds_read_b128 v[196:199], v2 offset:21504\n"
    "v_cvt_pk_bf16_f32 v84, v12, v13\n"
    "v_mfma_f32_16x16x32_bf16 v[36:39], v[176:179], a[212:215], v[36:39]\n"
    "ds_read_b128 v[200:203], v2 offset:23552\n"
    "v_cvt_pk_bf16_f32 v85, v14, v15\n"
    "v_mfma_f32_16x16x32_bf16 v[40:43], v[176:179], a[236:239], v[100:103]\n"
    "ds_read_b128 v[104:107], v250 offset:144\n"
    "v_cvt_pk_bf16_f32 v92, v16, v17\n"
    "v_mfma_f32_16x16x32_bf16 v[40:43], v[172:175], a[232:235], v[40:43]\n"
    "s_add_u32 s68, s68, 24576\n"
    "v_cvt_pk_bf16_f32 v93, v18, v19\n"
    "v_cvt_pk_bf16_f32 v70, v20, v21\n"
    "v_mfma_f32_16x16x32_bf16 v[40:43], v[168:171], a[228:231], v[40:43]\n"
    "s_cmp_ge_u32 s68, s69\n"
    "s_cselect_b32 s68, s65, s68\n"
    "v_cvt_pk_bf16_f32 v71, v22, v23\n"
    "v_mfma_f32_16x16x32_bf16 v[40:43], v[164:167], a[224:227], v[40:43]\n"
    "v_cvt_pk_bf16_f32 v78, v24, v25\n"
    "v_cvt_pk_bf16_f32 v79, v26, v27\n"
    "v_mfma_f32_16x16x32_bf16 v[40:43], v[160:163], a[220:223], v[40:43]\n"
    "v_cvt_pk_bf16_f32 v86, v28, v29\n"
    "v_cvt_pk_bf16_f32 v87, v30, v31\n"
    "v_mfma_f32_16x16x32_bf16 v[40:43], v[156:159], a[216:219], v[40:43]\n"
    "v_cvt_pk_bf16_f32 v94, v32, v33\n"
    "v_cvt_pk_bf16_f32 v95, v34, v35\n"
    "v_mfma_f32_16x16x32_bf16 v[44:47], v[156:159], a[240:243], v[100:103]\n"
    "v_exp_f32 v36, v36\n"
    "v_mfma_f32_16x16x32_bf16 v[44:47], v[160:163], a[244:247], v[44:47]\n"
    "v_exp_f32 v37, v37\n"
    "v_mfma_f32_16x16x32_bf16 v[44:47], v[164:167], a[248:251], v[44:47]\n"
    "v_exp_f32 v38, v38\n"
    "v_mfma_f32_16x16x32_bf16 v[44:47], v[168:171], a[252:255], v[44:47]\n"
    "v_exp_f32 v39, v39\n"
    "v_mfma_f32_16x16x32_bf16 v[44:47], v[172:175], v[124:127], v[44:47]\n"
    "v_cvt_pk_bf16_f32 v72, v36, v37\n"
    "v_cvt_pk_bf16_f32 v73, v38, v39\n"
    "v_mfma_f32_16x16x32_bf16 v[44:47], v[176:179], v[128:131], v[44:47]\n"
    "v_exp_f32 v40, v40\n"
    "v_mfma_f32_16x16x32_bf16 v[48:51], v[176:179], v[152:155], v[100:103]\n"
    "ds_read_b32 v245, v247\n"
    "v_mfma_f32_16x16x32_bf16 v[48:51], v[172:175], v[148:151], v[48:51]\n"
    "s_add_u32 m0, s68, 0\n"
    "v_exp_f32 v41, v41\n"
    "v_mfma_f32_16x16x32_bf16 v[48:51], v[168:171], v[144:147], v[48:51]\n"
    "v_exp_f32 v42, v42\n"
    "v_mfma_f32_16x16x32_bf16 v[48:51], v[164:167], v[140:143], v[48:51]\n"
    "v_exp_f32 v43, v43\n"
    "v_mfma_f32_16x16x32_bf16 v[48:51], v[160:163], v[136:139], v[48:51]\n"
    "s_waitcnt vmcnt(10)\n"
    "v_cmp_eq_u64 s[54:55], v[236:237], v[238:239]\n"
    "v_cvt_pk_bf16_f32 v80, v40, v41\n"
    "v_mfma_f32_16x16x32_bf16 v[48:51], v[156:159], v[132:135], v[48:51]\n"
    "s_waitcnt lgkmcnt(1)\n"
    "v_cvt_pk_bf16_f32 v81, v42, v43\n"
    "v_mfma_f32_16x16x32_bf16 v[52:55], v[180:183], a[192:195], v[104:107]\n"
    "s_waitcnt lgkmcnt(0)\n"
    "v_cmp_le_u32 s[56:57], s59, v245\n"
    "v_mfma_f32_16x16x32_bf16 v[52:55], v[184:187], a[196:199], v[52:55]\n"
    "v_exp_f32 v44, v44\n"
    "v_mfma_f32_16x16x32_bf16 v[52:55], v[188:191], a[200:203], v[52:55]\n"
    "s_and_b64 s[54:55], s[54:55], s[56:57]\n"
    "s_cmp_eq_u64 s[54:55], -1\n"
    "v_mfma_f32_16x16x32_bf16 v[52:55], v[192:195], a[204:207], v[52:55]\n"
    "s_cbranch_scc0 .Lpoll1\n"
    ".Lpoll1r:\n"
    "v_mfma_f32_16x16x32_bf16 v[52:55], v[196:199], a[208:211], v[52:55]\n"
    "buffer_load_dwordx4 v1, s[16:19], s67 offen offset:0 sc1 lds\n"
    "v_mfma_f32_16x16x32_bf16 v[52:55], v[200:203], a[212:215], v[52:55]\n"
    "ds_read_b128 v[156:159], v3 offset:0\n"
    "v_mfma_f32_16x16x32_bf16 v[56:59], v[200:203], a[236:239], v[104:107]\n"
    "ds_read_b128 v[160:163], v3 offset:2048\n"
    "v_mfma_f32_16x16x32_bf16 v[56:59], v[196:199], a[232:235], v[56:59]\n"
    "ds_read_b128 v[164:167], v3 offset:4096\n"
    "v_mfma_f32_16x16x32_bf16 v[56:59], v[192:195], a[228:231], v[56:59]\n"
    "buffer_load_dwordx4 v1, s[16:19], s67 offen offset:1024 sc1 lds\n"
    "v_mfma_f32_16x16x32_bf16 v[56:59], v[188:191], a[224:227], v[56:59]\n"
    "ds_read_b128 v[168:171], v3 offset:6144\n"
    "v_mfma_f32_16x16x32_bf16 v[56:59], v[184:187], a[220:223], v[56:59]\n"
    "ds_read_b128 v[172:175], v3 offset:8192\n"
    "v_mfma_f32_16x16x32_bf16 v[56:59], v[180:183], a[216:219], v[56:59]\n"
    "ds_read_b128 v[176:179], v3 offset:10240\n"
    "v_mfma_f32_16x16x32_bf16 v[60:63], v[180:183], a[240:243], v[104:107]\n"
    "buffer_load_dwordx4 v1, s[16:19], s67 offen offset:2048 sc1 lds\n"
    "v_mfma_f32_16x16x32_bf16 v[60:63], v[184:187], a[244:247], v[60:63]\n"
    "ds_read_b128 v[100:103], v250 offset:768\n"
    "v_mfma_f32_16x16x32_bf16 v[60:63], v[188:191], a[248:251], v[60:63]\n"
    "v_exp_f32 v52, v52\n"
    "v_mfma_f32_16x16x32_bf16 v[60:63], v[192:195], a[252:255], v[60:63]\n"
    "v_exp_f32 v53, v53\n"
    "v_mfma_f32_16x16x32_bf16 v[60:63], v[196:199], v[124:127], v[60:63]\n"
    "buffer_load_dwordx4 v1, s[16:19], s67 offen offset:3072 sc1 lds\n"
    "v_mfma_f32_16x16x32_bf16 v[60:63], v[200:203], v[128:131], v[60:63]\n"
    "s_add_u32 m0, s68, 4096\n"
    "v_exp_f32 v54, v54\n"
    "v_mfma_f32_16x16x32_bf16 v[64:67], v[200:203], v[152:155], v[104:107]\n"
    "s_add_u32 s83, s67, 4096\n"
    "v_exp_f32 v55, v55\n"
    "v_mfma_f32_16x16x32_bf16 v[64:67], v[196:199], v[148:151], v[64:67]\n"
    "v_cvt_pk_bf16_f32 v74, v52, v53\n"
    "v_cvt_pk_bf16_f32 v75, v54, v55\n"
    "v_mfma_f32_16x16x32_bf16 v[64:67], v[192:195], v[144:147], v[64:67]\n"
    "buffer_load_dwordx4 v1, s[16:19], s83 offen offset:0 sc1 lds\n"
    "v_mfma_f32_16x16x32_bf16 v[64:67], v[188:191], v[140:143], v[64:67]\n"
    "v_exp_f32 v56, v56\n"
    "v_mfma_f32_16x16x32_bf16 v[64:67], v[184:187], v[136:139], v[64:67]\n"
    "v_exp_f32 v57, v57\n"
    "v_mfma_f32_16x16x32_bf16 v[64:67], v[180:183], v[132:135], v[64:67]\n"
    "s_waitcnt lgkmcnt(0)\n"
    "v_exp_f32 v58, v58\n"
    "v_mfma_f32_16x16x32_bf16 v[4:7], v[156:159], a[192:195], v[100:103]\n"
    "buffer_load_dwordx4 v1, s[16:19], s83 offen offset:1024 sc1 lds\n"
    "v_mfma_f32_16x16x32_bf16 v[4:7], v[160:163], a[196:199], v[4:7]\n"
    "s_add_u32 m0, s64, 0\n"
    "ds_read_b128 v[180:183], v3 offset:1024\n"
    "v_cvt_pk_bf16_f32 v82, v56, v57\n"
    "v_mfma_f32_16x16x32_bf16 v[4:7], v[164:167], a[200:203], v[4:7]\n"
    "s_add_u32 s83, s67, 24576\n"
    "ds_read_b128 v[184:187], v3 offset:3072\n"
    "v_mfma_f32_16x16x32_bf16 v[4:7], v[168:171], a[204:207], v[4:7]\n"
    "ds_read_b128 v[188:191], v3 offset:5120\n"
    "v_mfma_f32_16x16x32_bf16 v[4:7], v[172:175], a[208:211], v[4:7]\n"
    "buffer_load_dwordx4 v1, s[16:19], s83 offen offset:0 sc1 lds\n"
    "v_mfma_f32_16x16x32_bf16 v[4:7], v[176:179], a[212:215], v[4:7]\n"
    "ds_read_b128 v[192:195], v3 offset:7168\n"
    "v_mfma_f32_16x16x32_bf16 v[8:11], v[176:179], a[236:239], v[100:103]\n"
    "ds_read_b128 v[196:199], v3 offset:9216\n"
    "v_mfma_f32_16x16x32_bf16 v[8:11], v[172:175], a[232:235], v[8:11]\n"
    "ds_read_b128 v[200:203], v3 offset:11264\n"
    "v_mfma_f32_16x16x32_bf16 v[8:11], v[168:171], a[228:231], v[8:11]\n"
    "buffer_load_dwordx4 v1, s[16:19], s83 offen offset:1024 sc1 lds\n"
    "v_mfma_f32_16x16x32_bf16 v[8:11], v[164:167], a[224:227], v[8:11]\n"
    "ds_read_b128 v[104:107], v250 offset:784\n"
    "v_mfma_f32_16x16x32_bf16 v[8:11], v[160:163], a[220:223], v[8:11]\n"
    "v_exp_f32 v59, v59\n"
    "v_mfma_f32_16x16x32_bf16 v[8:11], v[156:159], a[216:219], v[8:11]\n"
    "v_cvt_pk_bf16_f32 v83, v58, v59\n"
    "v_mfma_f32_16x16x32_bf16 v[12:15], v[156:159], a[240:243], v[100:103]\n"
    "buffer_load_dwordx4 v1, s[16:19], s83 offen offset:2048 sc1 lds\n"
    "v_mfma_f32_16x16x32_bf16 v[12:15], v[160:163], a[244:247], v[12:15]\n"
    "v_exp_f32 v45, v45\n"
    "v_mfma_f32_16x16x32_bf16 v[12:15], v[164:167], a[248:251], v[12:15]\n"
    "v_exp_f32 v46, v46\n"
    "v_mfma_f32_16x16x32_bf16 v[12:15], v[168:171], a[252:255], v[12:15]\n"
    "v_exp_f32 v47, v47\n"
    "v_mfma_f32_16x16x32_bf16 v[12:15], v[172:175], v[124:127], v[12:15]\n"
    "buffer_load_dwordx4 v1, s[16:19], s83 offen offset:3072 sc1 lds\n"
    "v_mfma_f32_16x16x32_bf16 v[12:15], v[176:179], v[128:131], v[12:15]\n"
    "s_add_u32 m0, s64, 4096\n"
    "v_exp_f32 v60, v60\n"
    "v_mfma_f32_16x16x32_bf16 v[16:19], v[176:179], v[152:155], v[100:103]\n"
    "s_add_u32 s83, s67, 28672\n"
    "v_exp_f32 v61, v61\n"
    "v_mfma_f32_16x16x32_bf16 v[16:19], v[172:175], v[148:151], v[16:19]\n"
    "v_exp_f32 v62, v62\n"
    "v_mfma_f32_16x16x32_bf16 v[16:19], v[168:171], v[144:147], v[16:19]\n"
    "buffer_load_dwordx4 v1, s[16:19], s83 offen offset:0 sc1 lds\n"
    "v_mfma_f32_16x16x32_bf16 v[16:19], v[164:167], v[140:143], v[16:19]\n"
    "v_exp_f32 v63, v63\n"
    "v_mfma_f32_16x16x32_bf16 v[16:19], v[160:163], v[136:139], v[16:19]\n"
    "v_cvt_pk_bf16_f32 v88, v44, v45\n"
    "v_cvt_pk_bf16_f32 v89, v46, v47\n"
    "v_mfma_f32_16x16x32_bf16 v[16:19], v[156:159], v[132:135], v[16:19]\n"
    "s_waitcnt lgkmcnt(0)\n"
    "v_cvt_pk_bf16_f32 v90, v60, v61\n"
    "v_cvt_pk_bf16_f32 v91, v62, v63\n"
    "v_mfma_f32_16x16x32_bf16 v[20:23], v[180:183], a[192:195], v[104:107]\n"
    "buffer_load_dwordx4 v1, s[16:19], s83 offen offset:1024 sc1 lds\n"
    "v_mfma_f32_16x16x32_bf16 v[20:23], v[184:187], a[196:199], v[20:23]\n"
    "s_add_u32 s67, s67, 49152\n"
    "ds_read_b128 v[156:159], v248 offset:0\n"
    "v_mfma_f32_16x16x32_bf16 v[20:23], v[188:191], a[200:203], v[20:23]\n"
    "s_add_u32 s59, s59, 4\n"
    "ds_read_b128 v[160:163], v248 offset:12288\n"
    "v_mfma_f32_16x16x32_bf16 v[20:23], v[192:195], a[204:207], v[20:23]\n"
    "s_waitcnt vmcnt(12)\n"
    "ds_read_b128 v[164:167], v248 offset:1024\n"
    "v_mfma_f32_16x16x32_bf16 v[20:23], v[196:199], a[208:211], v[20:23]\n"
    "buffer_store_dwordx2 v[238:239], v240, s[36:39], s80 offen offset:0 sc1\n"
    "v_mfma_f32_16x16x32_bf16 v[20:23], v[200:203], a[212:215], v[20:23]\n"
    "buffer_load_dwordx2 v[204:205], v242, s[28:31], s75 offen offset:0 nt\n"
    "v_mfma_f32_16x16x32_bf16 v[24:27], v[200:203], a[236:239], v[104:107]\n"
    "s_add_u32 s80, s80, 256\n"
    "ds_read_b128 v[168:171], v248 offset:13312\n"
    "v_mfma_f32_16x16x32_bf16 v[24:27], v[196:199], a[232:235], v[24:27]\n"
    "buffer_load_dwordx2 v[206:207], v242, s[28:31], s75 offen offset:768 nt\n"
    "v_mfma_f32_16x16x32_bf16 v[24:27], v[192:195], a[228:231], v[24:27]\n"
    "ds_read_b128 v[172:175], v248 offset:2048\n"
    "v_mfma_f32_16x16x32_bf16 v[24:27], v[188:191], a[224:227], v[24:27]\n"
    "buffer_load_dwordx2 v[208:209], v242, s[28:31], s75 offen offset:3072 nt\n"
    "v_mfma_f32_16x16x32_bf16 v[24:27], v[184:187], a[220:223], v[24:27]\n"
    "ds_read_b128 v[176:179], v248 offset:14336\n"
    "v_mfma_f32_16x16x32_bf16 v[24:27], v[180:183], a[216:219], v[24:27]\n"
    "buffer_load_dwordx2 v[210:211], v242, s[28:31], s75 offen offset:3840 nt\n"
    "v_mfma_f32_16x16x32_bf16 v[28:31], v[180:183], a[240:243], v[104:107]\n"
    "v_exp_f32 v48, v48\n"
    "v_mfma_f32_16x16x32_bf16 v[28:31], v[184:187], a[244:247], v[28:31]\n"
    "buffer_load_dwordx2 v[212:213], v242, s[28:31], s76 offen offset:0 nt\n"
    "v_mfma_f32_16x16x32_bf16 v[28:31], v[188:191], a[248:251], v[28:31]\n"
    "v_exp_f32 v49, v49\n"
    "v_mfma_f32_16x16x32_bf16 v[28:31], v[192:195], a[252:255], v[28:31]\n"
    "buffer_load_dwordx2 v[214:215], v242, s[28:31], s76 offen offset:768 nt\n"
    "v_mfma_f32_16x16x32_bf16 v[28:31], v[196:199], v[124:127], v[28:31]\n"
    "v_exp_f32 v50, v50\n"
    "v_mfma_f32_16x16x32_bf16 v[28:31], v[200:203], v[128:131], v[28:31]\n"
    "buffer_load_dwordx2 v[216:217], v242, s[28:31], s76 offen offset:3072 nt\n"
    "v_mfma_f32_16x16x32_bf16 v[32:35], v[200:203], v[152:155], v[104:107]\n"
    "v_exp_f32 v51, v51\n"
    "v_mfma_f32_16x16x32_bf16 v[32:35], v[196:199], v[148:151], v[32:35]\n"
    "buffer_load_dwordx2 v[218:219], v242, s[28:31], s76 offen offset:3840 nt\n"
    "v_mfma_f32_16x16x32_bf16 v[32:35], v[192:195], v[144:147], v[32:35]\n"
    "v_exp_f32 v64, v64\n"
    "v_mfma_f32_16x16x32_bf16 v[32:35], v[188:191], v[140:143], v[32:35]\n"
    "buffer_load_dwordx2 v[220:221], v242, s[28:31], s77 offen offset:0 nt\n"
    "v_mfma_f32_16x16x32_bf16 v[32:35], v[184:187], v[136:139], v[32:35]\n"
    "v_exp_f32 v65, v65\n"
    "v_mfma_f32_16x16x32_bf16 v[32:35], v[180:183], v[132:135], v[32:35]\n"
    "s_waitcnt lgkmcnt(0)\n"
    "v_exp_f32 v66, v66\n"
    "v_mfma_f32_16x16x32_bf16 a[0:3], v[156:159], v[68:71], a[0:3]\n"
    "buffer_load_dwordx2 v[222:223], v242, s[28:31], s77 offen offset:768 nt\n"
    "v_mfma_f32_16x16x32_bf16 a[0:3], v[160:163], v[72:75], a[0:3]\n"
    "ds_read_b128 v[180:183], v248 offset:3072\n"
    "v_cvt_pk_bf16_f32 v96, v48, v49\n"
    "v_mfma_f32_16x16x32_bf16 a[16:19], v[168:171], v[72:75], a[16:19]\n"
    "buffer_load_dwordx2 v[224:225], v242, s[28:31], s77 offen offset:3072 nt\n"
    "v_mfma_f32_16x16x32_bf16 a[16:19], v[164:167], v[68:71], a[16:19]\n"
    "ds_read_b128 v[184:187], v248 offset:15360\n"
    "v_cvt_pk_bf16_f32 v97, v50, v51\n"
    "v_mfma_f32_16x16x32_bf16 a[32:35], v[172:175], v[68:71], a[32:35]\n"
    "buffer_load_dwordx2 v[226:227], v242, s[28:31], s77 offen offset:3840 nt\n"
    "v_mfma_f32_16x16x32_bf16 a[32:35], v[176:179], v[72:75], a[32:35]\n"
    "ds_read_b128 v[188:191], v248 offset:4096\n"
    "v_cvt_pk_bf16_f32 v98, v64, v65\n"
    "v_mfma_f32_16x16x32_bf16 a[36:39], v[176:179], v[80:83], a[36:39]\n"
    "buffer_load_dwordx2 v[228:229], v242, s[28:31], s78 offen offset:0 nt\n"
    "v_mfma_f32_16x16x32_bf16 a[36:39], v[172:175], v[76:79], a[36:39]\n"
    "ds_read_b128 v[192:195], v248 offset:16384\n"
    "v_mfma_f32_16x16x32_bf16 a[20:23], v[164:167], v[76:79], a[20:23]\n"
    "buffer_load_dwordx2 v[230:231], v242, s[28:31], s78 offen offset:768 nt\n"
    "v_mfma_f32_16x16x32_bf16 a[20:23], v[168:171], v[80:83], a[20:23]\n"
    "ds_read_b128 v[196:199], v248 offset:5120\n"
    "v_mfma_f32_16x16x32_bf16 a[4:7], v[160:163], v[80:83], a[4:7]\n"
    "buffer_load_dwordx2 v[232:233], v242, s[28:31], s78 offen offset:3072 nt\n"
    "v_mfma_f32_16x16x32_bf16 a[4:7], v[156:159], v[76:79], a[4:7]\n"
    "ds_read_b128 v[200:203], v248 offset:17408\n"
    "v_mfma_f32_16x16x32_bf16 a[8:11], v[156:159], v[84:87], a[8:11]\n"
    "buffer_load_dwordx2 v[234:235], v242, s[28:31], s78 offen offset:3840 nt\n"
    "v_mfma_f32_16x16x32_bf16 a[8:11], v[160:163], v[88:91], a[8:11]\n"
    "ds_read_b128 v[104:107], v251 offset:0\n"
    "v_mfma_f32_16x16x32_bf16 a[24:27], v[168:171], v[88:91], a[24:27]\n"
    "v_exp_f32 v67, v67\n"
    "v_mfma_f32_16x16x32_bf16 a[24:27], v[164:167], v[84:87], a[24:27]\n"
    "v_cvt_pk_bf16_f32 v99, v66, v67\n"
    "v_mfma_f32_16x16x32_bf16 a[40:43], v[172:175], v[84:87], a[40:43]\n"
    "v_exp_f32 v4, v4\n"
    "v_mfma_f32_16x16x32_bf16 a[40:43], v[176:179], v[88:91], a[40:43]\n"
    "v_exp_f32 v5, v5\n"
    "v_mfma_f32_16x16x32_bf16 a[44:47], v[176:179], v[96:99], a[44:47]\n"
    "v_exp_f32 v6, v6\n"
    "v_mfma_f32_16x16x32_bf16 a[44:47], v[172:175], v[92:95], a[44:47]\n"
    "v_exp_f32 v7, v7\n"
    "v_mfma_f32_16x16x32_bf16 a[28:31], v[164:167], v[92:95], a[28:31]\n"
    "v_exp_f32 v8, v8\n"
    "v_mfma_f32_16x16x32_bf16 a[28:31], v[168:171], v[96:99], a[28:31]\n"
    "v_exp_f32 v9, v9\n"
    "v_mfma_f32_16x16x32_bf16 a[12:15], v[160:163], v[96:99], a[12:15]\n"
    "v_exp_f32 v10, v10\n"
    "v_mfma_f32_16x16x32_bf16 a[12:15], v[156:159], v[92:95], a[12:15]\n"
    "s_waitcnt lgkmcnt(0)\n"
    "v_exp_f32 v11, v11\n"
    "v_mfma_f32_16x16x32_bf16 a[48:51], v[180:183], v[68:71], a[48:51]\n"
    "ds_read_b128 v[156:159], v248 offset:6144\n"
    "v_mfma_f32_16x16x32_bf16 a[48:51], v[184:187], v[72:75], a[48:51]\n"
    "ds_read_b128 v[160:163], v248 offset:18432\n"
    "v_mfma_f32_16x16x32_bf16 a[64:67], v[192:195], v[72:75], a[64:67]\n"
    "ds_read_b128 v[164:167], v248 offset:7168\n"
    "v_mfma_f32_16x16x32_bf16 a[64:67], v[188:191], v[68:71], a[64:67]\n"
    "ds_read_b128 v[168:171], v248 offset:19456\n"
    "v_mfma_f32_16x16x32_bf16 a[80:83], v[196:199], v[68:71], a[80:83]\n"
    "ds_read_b128 v[172:175], v248 offset:8192\n"
    "v_mfma_f32_16x16x32_bf16 a[80:83], v[200:203], v[72:75], a[80:83]\n"
    "ds_read_b128 v[176:179], v248 offset:20480\n"
    "v_smfmac_f32_16x16x64_bf16 v[108:111], v[104:107], v[68:75], v253\n"
    "s_add_u32 s81, s81, 128\n"
    "s_cmp_gt_u32 s81, 4096\n"
    "s_cselect_b32 s81, 0, s81\n"
    "v_smfmac_f32_16x16x64_bf16 v[112:115], v[104:107], v[76:83], v253\n"
    "v_mfma_f32_16x16x32_bf16 a[84:87], v[200:203], v[80:83], a[84:87]\n"
    "v_exp_f32 v12, v12\n"
    "v_mfma_f32_16x16x32_bf16 a[84:87], v[196:199], v[76:79], a[84:87]\n"
    "v_exp_f32 v13, v13\n"
    "v_mfma_f32_16x16x32_bf16 a[68:71], v[188:191], v[76:79], a[68:71]\n"
    "v_exp_f32 v14, v14\n"
    "v_mfma_f32_16x16x32_bf16 a[68:71], v[192:195], v[80:83], a[68:71]\n"
    "v_exp_f32 v15, v15\n"
    "v_mfma_f32_16x16x32_bf16 a[52:55], v[184:187], v[80:83], a[52:55]\n"
    "v_exp_f32 v16, v16\n"
    "v_mfma_f32_16x16x32_bf16 a[52:55], v[180:183], v[76:79], a[52:55]\n"
    "v_mfma_f32_16x16x32_bf16 a[56:59], v[180:183], v[84:87], a[56:59]\n"
    "v_exp_f32 v17, v17\n"
    "v_mfma_f32_16x16x32_bf16 a[56:59], v[184:187], v[88:91], a[56:59]\n"
    "v_mfma_f32_16x16x32_bf16 a[72:75], v[192:195], v[88:91], a[72:75]\n"
    "v_mfma_f32_16x16x32_bf16 a[72:75], v[188:191], v[84:87], a[72:75]\n"
    "v_exp_f32 v18, v18\n"
    "v_mfma_f32_16x16x32_bf16 a[88:91], v[196:199], v[84:87], a[88:91]\n"
    "v_mfma_f32_16x16x32_bf16 a[88:91], v[200:203], v[88:91], a[88:91]\n"
    "v_smfmac_f32_16x16x64_bf16 v[116:119], v[104:107], v[84:91], v253\n"
    "v_smfmac_f32_16x16x64_bf16 v[120:123], v[104:107], v[92:99], v253\n"
    "v_mfma_f32_16x16x32_bf16 a[92:95], v[200:203], v[96:99], a[92:95]\n"
    "v_exp_f32 v19, v19\n"
    "v_mfma_f32_16x16x32_bf16 a[92:95], v[196:199], v[92:95], a[92:95]\n"
    "v_exp_f32 v20, v20\n"
    "v_mfma_f32_16x16x32_bf16 a[76:79], v[188:191], v[92:95], a[76:79]\n"
    "v_mfma_f32_16x16x32_bf16 a[76:79], v[192:195], v[96:99], a[76:79]\n"
    "v_exp_f32 v21, v21\n"
    "v_mfma_f32_16x16x32_bf16 a[60:63], v[184:187], v[96:99], a[60:63]\n"
    "v_mfma_f32_16x16x32_bf16 a[60:63], v[180:183], v[92:95], a[60:63]\n"
    "s_waitcnt lgkmcnt(0)\n"
    "v_mfma_f32_16x16x32_bf16 a[96:99], v[156:159], v[68:71], a[96:99]\n"
    "ds_read_b128 v[180:183], v248 offset:9216\n"
    "v_mfma_f32_16x16x32_bf16 a[96:99], v[160:163], v[72:75], a[96:99]\n"
    "ds_read_b128 v[184:187], v248 offset:21504\n"
    "v_mfma_f32_16x16x32_bf16 a[112:115], v[168:171], v[72:75], a[112:115]\n"
    "ds_read_b128 v[188:191], v248 offset:10240\n"
    "v_mfma_f32_16x16x32_bf16 a[112:115], v[164:167], v[68:71], a[112:115]\n"
    "ds_read_b128 v[192:195], v248 offset:22528\n"
    "v_mfma_f32_16x16x32_bf16 a[128:131], v[172:175], v[68:71], a[128:131]\n"
    "buffer_load_dwordx2 v[236:237], v241, s[36:39], s81 offen offset:0 sc1\n"
    "v_mfma_f32_16x16x32_bf16 a[128:131], v[176:179], v[72:75], a[128:131]\n"
    "ds_read_b128 v[196:199], v248 offset:11264\n"
    "v_mfma_f32_16x16x32_bf16 a[132:135], v[176:179], v[80:83], a[132:135]\n"
    "ds_read_b128 v[200:203], v248 offset:23552\n"
    "v_mfma_f32_16x16x32_bf16 a[132:135], v[172:175], v[76:79], a[132:135]\n"
    "v_exp_f32 v22, v22\n"
    "v_mfma_f32_16x16x32_bf16 a[116:119], v[164:167], v[76:79], a[116:119]\n"
    "v_exp_f32 v23, v23\n"
    "v_mfma_f32_16x16x32_bf16 a[116:119], v[168:171], v[80:83], a[116:119]\n"
    "v_exp_f32 v24, v24\n"
    "v_mfma_f32_16x16x32_bf16 a[100:103], v[160:163], v[80:83], a[100:103]\n"
    "v_exp_f32 v25, v25\n"
    "v_mfma_f32_16x16x32_bf16 a[100:103], v[156:159], v[76:79], a[100:103]\n"
    "v_exp_f32 v26, v26\n"
    "v_mfma_f32_16x16x32_bf16 a[104:107], v[156:159], v[84:87], a[104:107]\n"
    "v_mfma_f32_16x16x32_bf16 a[104:107], v[160:163], v[88:91], a[104:107]\n"
    "v_exp_f32 v27, v27\n"
    "v_mfma_f32_16x16x32_bf16 a[120:123], v[168:171], v[88:91], a[120:123]\n"
    "v_mfma_f32_16x16x32_bf16 a[120:123], v[164:167], v[84:87], a[120:123]\n"
    "v_mfma_f32_16x16x32_bf16 a[136:139], v[172:175], v[84:87], a[136:139]\n"
    "v_exp_f32 v28, v28\n"
    "v_mfma_f32_16x16x32_bf16 a[136:139], v[176:179], v[88:91], a[136:139]\n"
    "v_mfma_f32_16x16x32_bf16 a[140:143], v[176:179], v[96:99], a[140:143]\n"
    "v_mfma_f32_16x16x32_bf16 a[140:143], v[172:175], v[92:95], a[140:143]\n"
    "v_exp_f32 v29, v29\n"
    "v_mfma_f32_16x16x32_bf16 a[124:127], v[164:167], v[92:95], a[124:127]\n"
    "v_mfma_f32_16x16x32_bf16 a[124:127], v[168:171], v[96:99], a[124:127]\n"
    "v_exp_f32 v30, v30\n"
    "v_mfma_f32_16x16x32_bf16 a[108:111], v[160:163], v[96:99], a[108:111]\n"
    "v_mfma_f32_16x16x32_bf16 a[108:111], v[156:159], v[92:95], a[108:111]\n"
    "s_waitcnt lgkmcnt(0)\n"
    "v_mfma_f32_16x16x32_bf16 a[144:147], v[180:183], v[68:71], a[144:147]\n"
    "ds_read_b128 v[156:159], v3 offset:12288\n"
    "v_mfma_f32_16x16x32_bf16 a[144:147], v[184:187], v[72:75], a[144:147]\n"
    "ds_read_b128 v[160:163], v3 offset:14336\n"
    "v_mfma_f32_16x16x32_bf16 a[160:163], v[192:195], v[72:75], a[160:163]\n"
    "ds_read_b128 v[164:167], v3 offset:16384\n"
    "v_mfma_f32_16x16x32_bf16 a[160:163], v[188:191], v[68:71], a[160:163]\n"
    "ds_read_b128 v[168:171], v3 offset:18432\n"
    "v_mfma_f32_16x16x32_bf16 a[176:179], v[196:199], v[68:71], a[176:179]\n"
    "ds_read_b128 v[172:175], v3 offset:20480\n"
    "v_mfma_f32_16x16x32_bf16 a[176:179], v[200:203], v[72:75], a[176:179]\n"
    "ds_read_b128 v[176:179], v3 offset:22528\n"
    "v_mfma_f32_16x16x32_bf16 a[180:183], v[200:203], v[80:83], a[180:183]\n"
    "ds_read_b128 v[100:103], v250 offset:896\n"
    "v_mfma_f32_16x16x32_bf16 a[180:183], v[196:199], v[76:79], a[180:183]\n"
    "v_exp_f32 v31, v31\n"
    "v_mfma_f32_16x16x32_bf16 a[164:167], v[188:191], v[76:79], a[164:167]\n"
    "v_exp_f32 v32, v32\n"
    "v_mfma_f32_16x16x32_bf16 a[164:167], v[192:195], v[80:83], a[164:167]\n"
    "v_exp_f32 v33, v33\n"
    "v_mfma_f32_16x16x32_bf16 a[148:151], v[184:187], v[80:83], a[148:151]\n"
    "v_exp_f32 v34, v34\n"
    "v_mfma_f32_16x16x32_bf16 a[148:151], v[180:183], v[76:79], a[148:151]\n"
    "v_exp_f32 v35, v35\n"
    "v_mfma_f32_16x16x32_bf16 a[152:155], v[180:183], v[84:87], a[152:155]\n"
    "v_mfma_f32_16x16x32_bf16 a[152:155], v[184:187], v[88:91], a[152:155]\n"
    "v_mfma_f32_16x16x32_bf16 a[168:171], v[192:195], v[88:91], a[168:171]\n"
    "s_waitcnt vmcnt(18)\n"
    "ds_add_u32 v247, v246\n"
    "v_mfma_f32_16x16x32_bf16 a[168:171], v[188:191], v[84:87], a[168:171]\n"
    "v_mfma_f32_16x16x32_bf16 a[184:187], v[196:199], v[84:87], a[184:187]\n"
    "v_mfma_f32_16x16x32_bf16 a[184:187], v[200:203], v[88:91], a[184:187]\n"
    "v_mfma_f32_16x16x32_bf16 a[188:191], v[200:203], v[96:99], a[188:191]\n"
    "v_mfma_f32_16x16x32_bf16 a[188:191], v[196:199], v[92:95], a[188:191]\n"
    "v_mfma_f32_16x16x32_bf16 a[172:175], v[188:191], v[92:95], a[172:175]\n"
    "s_sub_u32 s74, s68, s65\n"
    "v_mfma_f32_16x16x32_bf16 a[172:175], v[192:195], v[96:99], a[172:175]\n"
    "s_add_u32 s66, s66, 1\n"
    "v_mov_b32 v2, v3\n"
    "v_add_u32_e64 v3, s74, v252\n"
    "v_mfma_f32_16x16x32_bf16 a[156:159], v[184:187], v[96:99], a[156:159]\n"
    "s_waitcnt lgkmcnt(1)\n"
    "s_cmp_eq_u32 s66, .Lend-.Lend2+(30)\n"
    "v_mfma_f32_16x16x32_bf16 a[156:159], v[180:183], v[92:95], a[156:159]\n"
    ".LwKe:\n"
    "s_cbranch_scc1 .LtailP\n"
    ".LNK:\n"
    "v_mfma_f32_16x16x32_bf16 v[36:39], v[156:159], a[192:195], v[100:103]\n"
    "ds_read_b128 v[180:183], v2 offset:13312\n"
    "v_cvt_pk_bf16_f32 v68, v4, v5\n"
    "v_mfma_f32_16x16x32_bf16 v[36:39], v[160:163], a[196:199], v[36:39]\n"
    "ds_read_b128 v[184:187], v2 offset:15360\n"
    "v_mfma_f32_16x16x32_bf16 v[36:39], v[164:167], a[200:203], v[36:39]\n"
    "ds_read_b128 v[188:191], v2 offset:17408\n"
    "v_mfma_f32_16x16x32_bf16 v[36:39], v[168:171], a[204:207], v[36:39]\n"
    "ds_read_b128 v[192:195], v2 offset:19456\n"
    "v_cvt_pk_bf16_f32 v69, v6, v7\n"
    "v_mfma_f32_16x16x32_bf16 v[36:39], v[172:175], a[208:211], v[36:39]\n"
    "ds_read_b128 v[196:199], v2 offset:21504\n"
    "v_mfma_f32_16x16x32_bf16 v[36:39], v[176:179], a[212:215], v[36:39]\n"
    "ds_read_b128 v[200:203], v2 offset:23552\n"
    "v_mfma_f32_16x16x32_bf16 v[40:43], v[176:179], a[236:239], v[100:103]\n"
    "ds_read_b128 v[104:107], v250 offset:912\n"
    "v_mfma_f32_16x16x32_bf16 v[40:43], v[172:175], a[232:235], v[40:43]\n"
    "s_add_u32 s68, s68, 24576\n"
    "v_cvt_pk_bf16_f32 v76, v8, v9\n"
    "v_mfma_f32_16x16x32_bf16 v[40:43], v[168:171], a[228:231], v[40:43]\n"
    "s_cmp_ge_u32 s68, s69\n"
    "s_cselect_b32 s68, s65, s68\n"
    "v_mfma_f32_16x16x32_bf16 v[40:43], v[164:167], a[224:227], v[40:43]\n"
    "v_exp_f32 v36, v36\n"
    "v_mfma_f32_16x16x32_bf16 v[40:43], v[160:163], a[220:223], v[40:43]\n"
    "v_exp_f32 v37, v37\n"
    "v_mfma_f32_16x16x32_bf16 v[40:43], v[156:159], a[216:219], v[40:43]\n"
    "v_cvt_pk_bf16_f32 v77, v10, v11\n"
    "v_cvt_pk_bf16_f32 v72, v36, v37\n"
    "v_mfma_f32_16x16x32_bf16 v[44:47], v[156:159], a[240:243], v[100:103]\n"
    "v_mfma_f32_16x16x32_bf16 v[44:47], v[160:163], a[244:247], v[44:47]\n"
    "v_exp_f32 v38, v38\n"
    "v_mfma_f32_16x16x32_bf16 v[44:47], v[164:167], a[248:251], v[44:47]\n"
    "v_mfma_f32_16x16x32_bf16 v[44:47], v[168:171], a[252:255], v[44:47]\n"
    "v_cvt_pk_bf16_f32 v84, v12, v13\n"
    "v_mfma_f32_16x16x32_bf16 v[44:47], v[172:175], v[124:127], v[44:47]\n"
    "v_exp_f32 v39, v39\n"
    "v_mfma_f32_16x16x32_bf16 v[44:47], v[176:179], v[128:131], v[44:47]\n"
    "v_cvt_pk_bf16_f32 v73, v38, v39\n"
    "v_mfma_f32_16x16x32_bf16 v[48:51], v[176:179], v[152:155], v[100:103]\n"
    "ds_read_b32 v245, v247\n"
    "v_cvt_pk_bf16_f32 v85, v14, v15\n"
    "v_mfma_f32_16x16x32_bf16 v[48:51], v[172:175], v[148:151], v[48:51]\n"
    "s_add_u32 m0, s68, 0\n"
    "v_exp_f32 v40, v40\n"
    "v_mfma_f32_16x16x32_bf16 v[48:51], v[168:171], v[144:147], v[48:51]\n"
    "v_mfma_f32_16x16x32_bf16 v[48:51], v[164:167], v[140:143], v[48:51]\n"
    "v_mfma_f32_16x16x32_bf16 v[48:51], v[160:163], v[136:139], v[48:51]\n"
    "s_waitcnt vmcnt(0)\n"
    "v_cmp_eq_u64 s[54:55], v[236:237], v[238:239]\n"
    "v_cvt_pk_bf16_f32 v92, v16, v17\n"
    "v_mfma_f32_16x16x32_bf16 v[48:51], v[156:159], v[132:135], v[48:51]\n"
    "s_waitcnt lgkmcnt(1)\n"
    "v_exp_f32 v41, v41\n"
    "v_mfma_f32_16x16x32_bf16 v[52:55], v[180:183], a[192:195], v[104:107]\n"
    "s_waitcnt lgkmcnt(0)\n"
    "v_cmp_le_u32 s[56:57], s59, v245\n"
    "v_cvt_pk_bf16_f32 v80, v40, v41\n"
    "v_mfma_f32_16x16x32_bf16 v[52:55], v[184:187], a[196:199], v[52:55]\n"
    "s_waitcnt vmcnt(1)\n"
    "v_exp_f32 v42, v42\n"
    "v_mfma_f32_16x16x32_bf16 v[52:55], v[188:191], a[200:203], v[52:55]\n"
    "s_and_b64 s[54:55], s[54:55], s[56:57]\n"
    "s_cmp_eq_u64 s[54:55], -1\n"
    "v_mfma_f32_16x16x32_bf16 v[52:55], v[192:195], a[204:207], v[52:55]\n"
    "s_cbranch_scc0 .Lpoll2\n"
    ".Lpoll2r:\n"
    "v_mfma_f32_16x16x32_bf16 v[52:55], v[196:199], a[208:211], v[52:55]\n"
    "buffer_load_dwordx4 v1, s[16:19], s67 offen offset:0 sc1 lds\n"
    "v_mfma_f32_16x16x32_bf16 v[52:55], v[200:203], a[212:215], v[52:55]\n"
    "s_add_u32 s76, s71, s60\n"
    "ds_read_b128 v[156:159], v3 offset:0\n"
    "v_cvt_pk_bf16_f32 v93, v18, v19\n"
    "v_mfma_f32_16x16x32_bf16 v[56:59], v[200:203], a[236:239], v[104:107]\n"
    "s_add_u32 s77, s71, s61\n"
    "ds_read_b128 v[160:163], v3 offset:2048\n"
    "v_cvt_pk_bf16_f32 v70, v20, v21\n"
    "v_mfma_f32_16x16x32_bf16 v[56:59], v[196:199], a[232:235], v[56:59]\n"
    "ds_read_b128 v[164:167], v3 offset:4096\n"
    "v_mfma_f32_16x16x32_bf16 v[56:59], v[192:195], a[228:231], v[56:59]\n"
    "buffer_load_dwordx4 v1, s[16:19], s67 offen offset:1024 sc1 lds\n"
    "v_mfma_f32_16x16x32_bf16 v[56:59], v[188:191], a[224:227], v[56:59]\n"
    "ds_read_b128 v[168:171], v3 offset:6144\n"
    "v_cvt_pk_bf16_f32 v71, v22, v23\n"
    "v_mfma_f32_16x16x32_bf16 v[56:59], v[184:187], a[220:223], v[56:59]\n"
    "ds_read_b128 v[172:175], v3 offset:8192\n"
    "v_mfma_f32_16x16x32_bf16 v[56:59], v[180:183], a[216:219], v[56:59]\n"
    "ds_read_b128 v[176:179], v3 offset:10240\n"
    "v_mfma_f32_16x16x32_bf16 v[60:63], v[180:183], a[240:243], v[104:107]\n"
    "buffer_load_dwordx4 v1, s[16:19], s67 offen offset:2048 sc1 lds\n"
    "v_mfma_f32_16x16x32_bf16 v[60:63], v[184:187], a[244:247], v[60:63]\n"
    "ds_read_b128 v[100:103], v250 offset:1536\n"
    "v_cvt_pk_bf16_f32 v78, v24, v25\n"
    "v_mfma_f32_16x16x32_bf16 v[60:63], v[188:191], a[248:251], v[60:63]\n"
    "v_exp_f32 v43, v43\n"
    "v_mfma_f32_16x16x32_bf16 v[60:63], v[192:195], a[252:255], v[60:63]\n"
    "ds_read_b128 v[36:39], v243 offset:0\n"
    "v_cvt_pk_bf16_f32 v81, v42, v43\n"
    "v_mfma_f32_16x16x32_bf16 v[60:63], v[196:199], v[124:127], v[60:63]\n"
    "buffer_load_dwordx4 v1, s[16:19], s67 offen offset:3072 sc1 lds\n"
    "v_mfma_f32_16x16x32_bf16 v[60:63], v[200:203], v[128:131], v[60:63]\n"
    "s_add_u32 m0, s68, 4096\n"
    "v_cvt_pk_bf16_f32 v79, v26, v27\n"
    "v_add_u32 v243, 512, v243\n"
    "v_mfma_f32_16x16x32_bf16 v[64:67], v[200:203], v[152:155], v[104:107]\n"
    "s_add_u32 s83, s67, 4096\n"
    "v_exp_f32 v44, v44\n"
    "v_mfma_f32_16x16x32_bf16 v[64:67], v[196:199], v[148:151], v[64:67]\n"
    "v_exp_f32 v45, v45\n"
    "v_mfma_f32_16x16x32_bf16 v[64:67], v[192:195], v[144:147], v[64:67]\n"
    "buffer_load_dwordx4 v1, s[16:19], s83 offen offset:0 sc1 lds\n"
    "v_mfma_f32_16x16x32_bf16 v[64:67], v[188:191], v[140:143], v[64:67]\n"
    "v_cvt_pk_bf16_f32 v86, v28, v29\n"
    "v_cvt_pk_bf16_f32 v88, v44, v45\n"
    "v_mfma_f32_16x16x32_bf16 v[64:67], v[184:187], v[136:139], v[64:67]\n"
    "v_exp_f32 v46, v46\n"
    "v_mfma_f32_16x16x32_bf16 v[64:67], v[180:183], v[132:135], v[64:67]\n"
    "s_waitcnt lgkmcnt(1)\n"
    "v_exp_f32 v47, v47\n"
    "v_mfma_f32_16x16x32_bf16 v[4:7], v[156:159], a[192:195], v[100:103]\n"
    "buffer_load_dwordx4 v1, s[16:19], s83 offen offset:1024 sc1 lds\n"
    "v_mfma_f32_16x16x32_bf16 v[4:7], v[160:163], a[196:199], v[4:7]\n"
    "s_add_u32 m0, s70, .Lend-.Lend2+(0)\n"
    "ds_read_b128 v[180:183], v3 offset:1024\n"
    "v_cvt_pk_bf16_f32 v87, v30, v31\n"
    "v_mfma_f32_16x16x32_bf16 v[4:7], v[164:167], a[200:203], v[4:7]\n"
    "s_add_u32 s83, s67, 24576\n"
    "ds_read_b128 v[184:187], v3 offset:3072\n"
    "v_cvt_pk_bf16_f32 v89, v46, v47\n"
    "v_mfma_f32_16x16x32_bf16 v[4:7], v[168:171], a[204:207], v[4:7]\n"
    "s_waitcnt lgkmcnt(2)\n"
    "ds_read_b128 v[188:191], v3 offset:5120\n"
    "v_fma_f32 v204, v204, v36, -v38\n"
    "v_mfma_f32_16x16x32_bf16 v[4:7], v[172:175], a[208:211], v[4:7]\n"
    "buffer_load_dwordx4 v1, s[16:19], s83 offen offset:0 sc1 lds\n"
    "v_mfma_f32_16x16x32_bf16 v[4:7], v[176:179], a[212:215], v[4:7]\n"
    "ds_read_b128 v[192:195], v3 offset:7168\n"
    "v_cvt_pk_bf16_f32 v94, v32, v33\n"
    "v_mfma_f32_16x16x32_bf16 v[8:11], v[176:179], a[236:239], v[100:103]\n"
    "ds_read_b128 v[196:199], v3 offset:9216\n"
    "v_fma_f32 v205, v205, v37, -v39\n"
    "v_mfma_f32_16x16x32_bf16 v[8:11], v[172:175], a[232:235], v[8:11]\n"
    "ds_read_b128 v[200:203], v3 offset:11264\n"
    "v_fma_f32 v206, v206, v36, -v38\n"
    "v_mfma_f32_16x16x32_bf16 v[8:11], v[168:171], a[228:231], v[8:11]\n"
    "buffer_load_dwordx4 v1, s[16:19], s83 offen offset:1024 sc1 lds\n"
    "v_mfma_f32_16x16x32_bf16 v[8:11], v[164:167], a[224:227], v[8:11]\n"
    "ds_read_b128 v[104:107], v250 offset:1552\n"
    "v_cvt_pk_bf16_f32 v95, v34, v35\n"
    "v_mfma_f32_16x16x32_bf16 v[8:11], v[160:163], a[220:223], v[8:11]\n"
    "v_exp_f32 v52, v52\n"
    "v_mfma_f32_16x16x32_bf16 v[8:11], v[156:159], a[216:219], v[8:11]\n"
    "v_exp_f32 v53, v53\n"
    "v_mfma_f32_16x16x32_bf16 v[12:15], v[156:159], a[240:243], v[100:103]\n"
    "buffer_load_dwordx4 v1, s[16:19], s83 offen offset:2048 sc1 lds\n"
    "v_mfma_f32_16x16x32_bf16 v[12:15], v[160:163], a[244:247], v[12:15]\n"
    "v_exp_f32 v54, v54\n"
    "v_mfma_f32_16x16x32_bf16 v[12:15], v[164:167], a[248:251], v[12:15]\n"
    "v_exp_f32 v55, v55\n"
    "v_mfma_f32_16x16x32_bf16 v[12:15], v[168:171], a[252:255], v[12:15]\n"
    "v_cvt_pk_bf16_f32 v74, v52, v53\n"
    "v_cvt_pk_bf16_f32 v75, v54, v55\n"
    "v_mfma_f32_16x16x32_bf16 v[12:15], v[172:175], v[124:127], v[12:15]\n"
    "buffer_load_dwordx4 v1, s[16:19], s83 offen offset:3072 sc1 lds\n"
    "v_mfma_f32_16x16x32_bf16 v[12:15], v[176:179], v[128:131], v[12:15]\n"
    "s_add_u32 m0, s70, 4096\n"
    "v_exp_f32 v56, v56\n"
    "v_mfma_f32_16x16x32_bf16 v[16:19], v[176:179], v[152:155], v[100:103]\n"
    "s_add_u32 s83, s67, 28672\n"
    "v_exp_f32 v48, v48\n"
    "v_mfma_f32_16x16x32_bf16 v[16:19], v[172:175], v[148:151], v[16:19]\n"
    "v_exp_f32 v49, v49\n"
    "v_mfma_f32_16x16x32_bf16 v[16:19], v[168:171], v[144:147], v[16:19]\n"
    "buffer_load_dwordx4 v1, s[16:19], s83 offen offset:0 sc1 lds\n"
    "v_mfma_f32_16x16x32_bf16 v[16:19], v[164:167], v[140:143], v[16:19]\n"
    "v_exp_f32 v57, v57\n"
    "v_mfma_f32_16x16x32_bf16 v[16:19], v[160:163], v[136:139], v[16:19]\n"
    "v_cvt_pk_bf16_f32 v82, v56, v57\n"
    "v_cvt_pk_bf16_f32 v96, v48, v49\n"
    "v_mfma_f32_16x16x32_bf16 v[16:19], v[156:159], v[132:135], v[16:19]\n"
    "s_waitcnt lgkmcnt(0)\n"
    "v_exp_f32 v58, v58\n"
    "v_mfma_f32_16x16x32_bf16 v[20:23], v[180:183], a[192:195], v[104:107]\n"
    "buffer_load_dwordx4 v1, s[16:19], s83 offen offset:1024 sc1 lds\n"
    "v_mfma_f32_16x16x32_bf16 v[20:23], v[184:187], a[196:199], v[20:23]\n"
    "s_add_u32 s67, s67, 49152\n"
    "ds_read_b128 v[156:159], v249 offset:0\n"
    "v_fma_f32 v207, v207, v37, -v39\n"
    "v_mfma_f32_16x16x32_bf16 v[20:23], v[188:191], a[200:203], v[20:23]\n"
    "s_add_u32 s59, s59, 4\n"
    "ds_read_b128 v[160:163], v249 offset:12288\n"
    "v_cvt_pk_bf16_f32 v204, v204, v205\n"
    "v_mfma_f32_16x16x32_bf16 v[20:23], v[192:195], a[204:207], v[20:23]\n"
    "ds_read_b128 v[164:167], v249 offset:1024\n"
    "v_cvt_pk_bf16_f32 v206, v206, v207\n"
    "v_mfma_f32_16x16x32_bf16 v[20:23], v[196:199], a[208:211], v[20:23]\n"
    "ds_read_b128 v[168:171], v249 offset:13312\n"
    "v_fma_f32 v208, v208, v36, -v38\n"
    "v_mfma_f32_16x16x32_bf16 v[20:23], v[200:203], a[212:215], v[20:23]\n"
    "ds_read_b128 v[172:175], v249 offset:2048\n"
    "v_fma_f32 v209, v209, v37, -v39\n"
    "v_mfma_f32_16x16x32_bf16 v[24:27], v[200:203], a[236:239], v[104:107]\n"
    "ds_read_b128 v[176:179], v249 offset:14336\n"
    "v_fma_f32 v210, v210, v36, -v38\n"
    "v_mfma_f32_16x16x32_bf16 v[24:27], v[196:199], a[232:235], v[24:27]\n"
    "v_exp_f32 v59, v59\n"
    "v_mfma_f32_16x16x32_bf16 v[24:27], v[192:195], a[228:231], v[24:27]\n"
    "v_cvt_pk_bf16_f32 v83, v58, v59\n"
    "v_fma_f32 v211, v211, v37, -v39\n"
    "v_mfma_f32_16x16x32_bf16 v[24:27], v[188:191], a[224:227], v[24:27]\n"
    "v_exp_f32 v60, v60\n"
    "v_mfma_f32_16x16x32_bf16 v[24:27], v[184:187], a[220:223], v[24:27]\n"
    "v_exp_f32 v61, v61\n"
    "v_mfma_f32_16x16x32_bf16 v[24:27], v[180:183], a[216:219], v[24:27]\n"
    "v_exp_f32 v62, v62\n"
    "v_mfma_f32_16x16x32_bf16 v[28:31], v[180:183], a[240:243], v[104:107]\n"
    "v_cvt_pk_bf16_f32 v90, v60, v61\n"
    "v_cvt_pk_bf16_f32 v208, v208, v209\n"
    "v_mfma_f32_16x16x32_bf16 v[28:31], v[184:187], a[244:247], v[28:31]\n"
    "v_exp_f32 v63, v63\n"
    "v_mfma_f32_16x16x32_bf16 v[28:31], v[188:191], a[248:251], v[28:31]\n"
    "v_cvt_pk_bf16_f32 v91, v62, v63\n"
    "v_cvt_pk_bf16_f32 v210, v210, v211\n"
    "v_mfma_f32_16x16x32_bf16 v[28:31], v[192:195], a[252:255], v[28:31]\n"
    "v_exp_f32 v50, v50\n"
    "v_mfma_f32_16x16x32_bf16 v[28:31], v[196:199], v[124:127], v[28:31]\n"
    "v_exp_f32 v51, v51\n"
    "v_mfma_f32_16x16x32_bf16 v[28:31], v[200:203], v[128:131], v[28:31]\n"
    "v_exp_f32 v64, v64\n"
    "v_mfma_f32_16x16x32_bf16 v[32:35], v[200:203], v[152:155], v[104:107]\n"
    "v_cvt_pk_bf16_f32 v97, v50, v51\n"
    "v_fma_f32 v212, v212, v36, -v38\n"
    "v_mfma_f32_16x16x32_bf16 v[32:35], v[196:199], v[148:151], v[32:35]\n"
    "v_exp_f32 v65, v65\n"
    "v_mfma_f32_16x16x32_bf16 v[32:35], v[192:195], v[144:147], v[32:35]\n"
    "v_cvt_pk_bf16_f32 v98, v64, v65\n"
    "v_fma_f32 v213, v213, v37, -v39\n"
    "v_mfma_f32_16x16x32_bf16 v[32:35], v[188:191], v[140:143], v[32:35]\n"
    "buffer_store_dword v204, v244, s[16:19], s76 offen offset:0 sc1\n"
    "v_mfma_f32_16x16x32_bf16 v[32:35], v[184:187], v[136:139], v[32:35]\n"
    "v_exp_f32 v66, v66\n"
    "v_mfma_f32_16x16x32_bf16 v[32:35], v[180:183], v[132:135], v[32:35]\n"
    "s_waitcnt lgkmcnt(0)\n"
    "v_fma_f32 v214, v214, v36, -v38\n"
    "v_fma_f32 v215, v215, v37, -v39\n"
    "v_mfma_f32_16x16x32_bf16 a[0:3], v[156:159], v[68:71], a[0:3]\n"
    "ds_read_b128 v[180:183], v249 offset:3072\n"
    "v_cvt_pk_bf16_f32 v212, v212, v213\n"
    "v_mfma_f32_16x16x32_bf16 a[0:3], v[160:163], v[72:75], a[0:3]\n"
    "ds_read_b128 v[184:187], v249 offset:15360\n"
    "v_cvt_pk_bf16_f32 v214, v214, v215\n"
    "v_mfma_f32_16x16x32_bf16 a[16:19], v[168:171], v[72:75], a[16:19]\n"
    "ds_read_b128 v[188:191], v249 offset:4096\n"
    "v_fma_f32 v216, v216, v36, -v38\n"
    "v_mfma_f32_16x16x32_bf16 a[16:19], v[164:167], v[68:71], a[16:19]\n"
    "ds_read_b128 v[192:195], v249 offset:16384\n"
    "v_fma_f32 v217, v217, v37, -v39\n"
    "v_mfma_f32_16x16x32_bf16 a[32:35], v[172:175], v[68:71], a[32:35]\n"
    "ds_read_b128 v[196:199], v249 offset:5120\n"
    "v_fma_f32 v218, v218, v36, -v38\n"
    "v_mfma_f32_16x16x32_bf16 a[32:35], v[176:179], v[72:75], a[32:35]\n"
    "ds_read_b128 v[200:203], v249 offset:17408\n"
    "v_fma_f32 v219, v219, v37, -v39\n"
    "v_mfma_f32_16x16x32_bf16 a[36:39], v[176:179], v[80:83], a[36:39]\n"
    "ds_read_b128 v[104:107], v251 offset:768\n"
    "v_cvt_pk_bf16_f32 v216, v216, v217\n"
    "v_mfma_f32_16x16x32_bf16 a[36:39], v[172:175], v[76:79], a[36:39]\n"
    "v_exp_f32 v67, v67\n"
    "v_mfma_f32_16x16x32_bf16 a[20:23], v[164:167], v[76:79], a[20:23]\n"
    "v_cvt_pk_bf16_f32 v99, v66, v67\n"
    "v_cvt_pk_bf16_f32 v218, v218, v219\n"
    "v_mfma_f32_16x16x32_bf16 a[20:23], v[168:171], v[80:83], a[20:23]\n"
    "buffer_store_dword v206, v244, s[16:19], s76 offen offset:128 sc1\n"
    "v_mfma_f32_16x16x32_bf16 a[4:7], v[160:163], v[80:83], a[4:7]\n"
    "v_exp_f32 v4, v4\n"
    "v_mfma_f32_16x16x32_bf16 a[4:7], v[156:159], v[76:79], a[4:7]\n"
    "buffer_store_dword v208, v244, s[16:19], s76 offen offset:1024 sc1\n"
    "v_mfma_f32_16x16x32_bf16 a[8:11], v[156:159], v[84:87], a[8:11]\n"
    "v_fma_f32 v220, v220, v36, -v38\n"
    "v_fma_f32 v221, v221, v37, -v39\n"
    "v_mfma_f32_16x16x32_bf16 a[8:11], v[160:163], v[88:91], a[8:11]\n"
    "buffer_store_dword v210, v244, s[16:19], s76 offen offset:1152 sc1\n"
    "v_mfma_f32_16x16x32_bf16 a[24:27], v[168:171], v[88:91], a[24:27]\n"
    "v_exp_f32 v5, v5\n"
    "v_mfma_f32_16x16x32_bf16 a[24:27], v[164:167], v[84:87], a[24:27]\n"
    "buffer_store_dword v212, v244, s[16:19], s76 offen offset:256 sc1\n"
    "v_mfma_f32_16x16x32_bf16 a[40:43], v[172:175], v[84:87], a[40:43]\n"
    "v_exp_f32 v6, v6\n"
    "v_mfma_f32_16x16x32_bf16 a[40:43], v[176:179], v[88:91], a[40:43]\n"
    "buffer_store_dword v214, v244, s[16:19], s76 offen offset:384 sc1\n"
    "v_mfma_f32_16x16x32_bf16 a[44:47], v[176:179], v[96:99], a[44:47]\n"
    "v_fma_f32 v222, v222, v36, -v38\n"
    "v_fma_f32 v223, v223, v37, -v39\n"
    "v_mfma_f32_16x16x32_bf16 a[44:47], v[172:175], v[92:95], a[44:47]\n"
    "v_exp_f32 v7, v7\n"
    "v_mfma_f32_16x16x32_bf16 a[28:31], v[164:167], v[92:95], a[28:31]\n"
    "v_exp_f32 v8, v8\n"
    "v_mfma_f32_16x16x32_bf16 a[28:31], v[168:171], v[96:99], a[28:31]\n"
    "v_cvt_pk_bf16_f32 v220, v220, v221\n"
    "v_cvt_pk_bf16_f32 v222, v222, v223\n"
    "v_mfma_f32_16x16x32_bf16 a[12:15], v[160:163], v[96:99], a[12:15]\n"
    "v_exp_f32 v9, v9\n"
    "v_mfma_f32_16x16x32_bf16 a[12:15], v[156:159], v[92:95], a[12:15]\n"
    "s_waitcnt lgkmcnt(0)\n"
    "v_exp_f32 v10, v10\n"
    "v_mfma_f32_16x16x32_bf16 a[48:51], v[180:183], v[68:71], a[48:51]\n"
    "ds_read_b128 v[156:159], v249 offset:6144\n"
    "v_fma_f32 v224, v224, v36, -v38\n"
    "v_mfma_f32_16x16x32_bf16 a[48:51], v[184:187], v[72:75], a[48:51]\n"
    "ds_read_b128 v[160:163], v249 offset:18432\n"
    "v_fma_f32 v225, v225, v37, -v39\n"
    "v_mfma_f32_16x16x32_bf16 a[64:67], v[192:195], v[72:75], a[64:67]\n"
    "ds_read_b128 v[164:167], v249 offset:7168\n"
    "v_fma_f32 v226, v226, v36, -v38\n"
    "v_mfma_f32_16x16x32_bf16 a[64:67], v[188:191], v[68:71], a[64:67]\n"
    "ds_read_b128 v[168:171], v249 offset:19456\n"
    "v_fma_f32 v227, v227, v37, -v39\n"
    "v_mfma_f32_16x16x32_bf16 a[80:83], v[196:199], v[68:71], a[80:83]\n"
    "ds_read_b128 v[172:175], v249 offset:8192\n"
    "v_cvt_pk_bf16_f32 v224, v224, v225\n"
    "v_mfma_f32_16x16x32_bf16 a[80:83], v[200:203], v[72:75], a[80:83]\n"
    "ds_read_b128 v[176:179], v249 offset:20480\n"
    "v_smfmac_f32_16x16x64_bf16 v[108:111], v[104:107], v[68:75], v253\n"
    "s_add_u32 s81, s81, 128\n"
    "s_cmp_gt_u32 s81, 4096\n"
    "s_cselect_b32 s81, 0, s81\n"
    "v_smfmac_f32_16x16x64_bf16 v[112:115], v[104:107], v[76:83], v253\n"
    "v_mfma_f32_16x16x32_bf16 a[84:87], v[200:203], v[80:83], a[84:87]\n"
    "v_exp_f32 v11, v11\n"
    "v_mfma_f32_16x16x32_bf16 a[84:87], v[196:199], v[76:79], a[84:87]\n"
    "v_exp_f32 v12, v12\n"
    "v_mfma_f32_16x16x32_bf16 a[68:71], v[188:191], v[76:79], a[68:71]\n"
    "v_exp_f32 v13, v13\n"
    "v_mfma_f32_16x16x32_bf16 a[68:71], v[192:195], v[80:83], a[68:71]\n"
    "v_exp_f32 v14, v14\n"
    "v_mfma_f32_16x16x32_bf16 a[52:55], v[184:187], v[80:83], a[52:55]\n"
    "v_exp_f32 v15, v15\n"
    "v_mfma_f32_16x16x32_bf16 a[52:55], v[180:183], v[76:79], a[52:55]\n"
    "v_cvt_pk_bf16_f32 v226, v226, v227\n"
    "v_fma_f32 v228, v228, v36, -v38\n"
    "v_mfma_f32_16x16x32_bf16 a[56:59], v[180:183], v[84:87], a[56:59]\n"
    "v_exp_f32 v16, v16\n"
    "v_mfma_f32_16x16x32_bf16 a[56:59], v[184:187], v[88:91], a[56:59]\n"
    "v_exp_f32 v17, v17\n"
    "v_mfma_f32_16x16x32_bf16 a[72:75], v[192:195], v[88:91], a[72:75]\n"
    "v_fma_f32 v229, v229, v37, -v39\n"
    "v_fma_f32 v230, v230, v36, -v38\n"
    "v_mfma_f32_16x16x32_bf16 a[72:75], v[188:191], v[84:87], a[72:75]\n"
    "v_exp_f32 v18, v18\n"
    "v_mfma_f32_16x16x32_bf16 a[88:91], v[196:199], v[84:87], a[88:91]\n"
    "v_fma_f32 v231, v231, v37, -v39\n"
    "v_mfma_f32_16x16x32_bf16 a[88:91], v[200:203], v[88:91], a[88:91]\n"
    "v_smfmac_f32_16x16x64_bf16 v[116:119], v[104:107], v[84:91], v253\n"
    "v_smfmac_f32_16x16x64_bf16 v[120:123], v[104:107], v[92:99], v253\n"
    "v_mfma_f32_16x16x32_bf16 a[92:95], v[200:203], v[96:99], a[92:95]\n"
    "v_exp_f32 v19, v19\n"
    "v_mfma_f32_16x16x32_bf16 a[92:95], v[196:199], v[92:95], a[92:95]\n"
    "v_exp_f32 v20, v20\n"
    "v_mfma_f32_16x16x32_bf16 a[76:79], v[188:191], v[92:95], a[76:79]\n"
    "v_cvt_pk_bf16_f32 v228, v228, v229\n"
    "v_cvt_pk_bf16_f32 v230, v230, v231\n"
    "v_mfma_f32_16x16x32_bf16 a[76:79], v[192:195], v[96:99], a[76:79]\n"
    "v_exp_f32 v21, v21\n"
    "v_mfma_f32_16x16x32_bf16 a[60:63], v[184:187], v[96:99], a[60:63]\n"
    "v_fma_f32 v232, v232, v36, -v38\n"
    "v_mfma_f32_16x16x32_bf16 a[60:63], v[180:183], v[92:95], a[60:63]\n"
    "s_waitcnt lgkmcnt(0)\n"
    "v_mfma_f32_16x16x32_bf16 a[96:99], v[156:159], v[68:71], a[96:99]\n"
    "ds_read_b128 v[180:183], v249 offset:9216\n"
    "v_fma_f32 v233, v233, v37, -v39\n"
    "v_mfma_f32_16x16x32_bf16 a[96:99], v[160:163], v[72:75], a[96:99]\n"
    "ds_read_b128 v[184:187], v249 offset:21504\n"
    "v_mfma_f32_16x16x32_bf16 a[112:115], v[168:171], v[72:75], a[112:115]\n"
    "ds_read_b128 v[188:191], v249 offset:10240\n"
    "v_mfma_f32_16x16x32_bf16 a[112:115], v[164:167], v[68:71], a[112:115]\n"
    "ds_read_b128 v[192:195], v249 offset:22528\n"
    "v_fma_f32 v234, v234, v36, -v38\n"
    "v_mfma_f32_16x16x32_bf16 a[128:131], v[172:175], v[68:71], a[128:131]\n"
    "buffer_load_dwordx2 v[236:237], v241, s[36:39], s81 offen offset:0 sc1\n"
    "v_mfma_f32_16x16x32_bf16 a[128:131], v[176:179], v[72:75], a[128:131]\n"
    "ds_read_b128 v[196:199], v249 offset:11264\n"
    "v_fma_f32 v235, v235, v37, -v39\n"
    "v_mfma_f32_16x16x32_bf16 a[132:135], v[176:179], v[80:83], a[132:135]\n"
    "ds_read_b128 v[200:203], v249 offset:23552\n"
    "v_mfma_f32_16x16x32_bf16 a[132:135], v[172:175], v[76:79], a[132:135]\n"
    "buffer_store_dword v216, v244, s[16:19], s76 offen offset:1280 sc1\n"
    "v_mfma_f32_16x16x32_bf16 a[116:119], v[164:167], v[76:79], a[116:119]\n"
    "v_exp_f32 v22, v22\n"
    "v_mfma_f32_16x16x32_bf16 a[116:119], v[168:171], v[80:83], a[116:119]\n"
    "buffer_store_dword v218, v244, s[16:19], s76 offen offset:1408 sc1\n"
    "v_mfma_f32_16x16x32_bf16 a[100:103], v[160:163], v[80:83], a[100:103]\n"
    "v_exp_f32 v23, v23\n"
    "v_mfma_f32_16x16x32_bf16 a[100:103], v[156:159], v[76:79], a[100:103]\n"
    "buffer_store_dword v220, v244, s[16:19], s77 offen offset:0 sc1\n"
    "v_mfma_f32_16x16x32_bf16 a[104:107], v[156:159], v[84:87], a[104:107]\n"
    "v_exp_f32 v24, v24\n"
    "v_mfma_f32_16x16x32_bf16 a[104:107], v[160:163], v[88:91], a[104:107]\n"
    "buffer_store_dword v222, v244, s[16:19], s77 offen offset:128 sc1\n"
    "v_mfma_f32_16x16x32_bf16 a[120:123], v[168:171], v[88:91], a[120:123]\n"
    "v_exp_f32 v25, v25\n"
    "v_mfma_f32_16x16x32_bf16 a[120:123], v[164:167], v[84:87], a[120:123]\n"
    "buffer_store_dword v224, v244, s[16:19], s77 offen offset:1024 sc1\n"
    "v_mfma_f32_16x16x32_bf16 a[136:139], v[172:175], v[84:87], a[136:139]\n"
    "v_exp_f32 v26, v26\n"
    "v_mfma_f32_16x16x32_bf16 a[136:139], v[176:179], v[88:91], a[136:139]\n"
    "buffer_store_dword v226, v244, s[16:19], s77 offen offset:1152 sc1\n"
    "v_mfma_f32_16x16x32_bf16 a[140:143], v[176:179], v[96:99], a[140:143]\n"
    "v_cvt_pk_bf16_f32 v232, v232, v233\n"
    "v_cvt_pk_bf16_f32 v234, v234, v235\n"
    "v_mfma_f32_16x16x32_bf16 a[140:143], v[172:175], v[92:95], a[140:143]\n"
    "buffer_store_dword v228, v244, s[16:19], s77 offen offset:256 sc1\n"
    "v_mfma_f32_16x16x32_bf16 a[124:127], v[164:167], v[92:95], a[124:127]\n"
    "v_exp_f32 v27, v27\n"
    "v_mfma_f32_16x16x32_bf16 a[124:127], v[168:171], v[96:99], a[124:127]\n"
    "buffer_store_dword v230, v244, s[16:19], s77 offen offset:384 sc1\n"
    "v_mfma_f32_16x16x32_bf16 a[108:111], v[160:163], v[96:99], a[108:111]\n"
    "v_exp_f32 v28, v28\n"
    "v_mfma_f32_16x16x32_bf16 a[108:111], v[156:159], v[92:95], a[108:111]\n"
    "s_waitcnt lgkmcnt(0)\n"
    "v_exp_f32 v29, v29\n"
    "v_mfma_f32_16x16x32_bf16 a[144:147], v[180:183], v[68:71], a[144:147]\n"
    "ds_read_b128 v[156:159], v3 offset:12288\n"
    "v_mfma_f32_16x16x32_bf16 a[144:147], v[184:187], v[72:75], a[144:147]\n"
    "ds_read_b128 v[160:163], v3 offset:14336\n"
    "v_mfma_f32_16x16x32_bf16 a[160:163], v[192:195], v[72:75], a[160:163]\n"
    "ds_read_b128 v[164:167], v3 offset:16384\n"
    "v_mfma_f32_16x16x32_bf16 a[160:163], v[188:191], v[68:71], a[160:163]\n"
    "ds_read_b128 v[168:171], v3 offset:18432\n"
    "v_mfma_f32_16x16x32_bf16 a[176:179], v[196:199], v[68:71], a[176:179]\n"
    "ds_read_b128 v[172:175], v3 offset:20480\n"
    "v_mfma_f32_16x16x32_bf16 a[176:179], v[200:203], v[72:75], a[176:179]\n"
    "ds_read_b128 v[176:179], v3 offset:22528\n"
    "v_mfma_f32_16x16x32_bf16 a[180:183], v[200:203], v[80:83], a[180:183]\n"
    "ds_read_b128 v[100:103], v250 offset:1664\n"
    "v_mfma_f32_16x16x32_bf16 a[180:183], v[196:199], v[76:79], a[180:183]\n"
    "buffer_store_dword v232, v244, s[16:19], s77 offen offset:1280 sc1\n"
    "v_mfma_f32_16x16x32_bf16 a[164:167], v[188:191], v[76:79], a[164:167]\n"
    "v_exp_f32 v30, v30\n"
    "v_mfma_f32_16x16x32_bf16 a[164:167], v[192:195], v[80:83], a[164:167]\n"
    "buffer_store_dword v234, v244, s[16:19], s77 offen offset:1408 sc1\n"
    "v_mfma_f32_16x16x32_bf16 a[148:151], v[184:187], v[80:83], a[148:151]\n"
    "v_exp_f32 v31, v31\n"
    "v_mfma_f32_16x16x32_bf16 a[148:151], v[180:183], v[76:79], a[148:151]\n"
    "v_exp_f32 v32, v32\n"
    "v_mfma_f32_16x16x32_bf16 a[152:155], v[180:183], v[84:87], a[152:155]\n"
    "v_exp_f32 v33, v33\n"
    "v_mfma_f32_16x16x32_bf16 a[152:155], v[184:187], v[88:91], a[152:155]\n"
    "v_exp_f32 v34, v34\n"
    "v_mfma_f32_16x16x32_bf16 a[168:171], v[192:195], v[88:91], a[168:171]\n"
    "s_waitcnt vmcnt(17)\n"
    "ds_add_u32 v247, v246\n"
    "v_mfma_f32_16x16x32_bf16 a[168:171], v[188:191], v[84:87], a[168:171]\n"
    "v_exp_f32 v35, v35\n"
    "v_mfma_f32_16x16x32_bf16 a[184:187], v[196:199], v[84:87], a[184:187]\n"
    "v_mfma_f32_16x16x32_bf16 a[184:187], v[200:203], v[88:91], a[184:187]\n"
    "v_mfma_f32_16x16x32_bf16 a[188:191], v[200:203], v[96:99], a[188:191]\n"
    "v_mfma_f32_16x16x32_bf16 a[188:191], v[196:199], v[92:95], a[188:191]\n"
    "v_mfma_f32_16x16x32_bf16 a[172:175], v[188:191], v[92:95], a[172:175]\n"
    "s_sub_u32 s74, s68, s65\n"
    "v_add_u32 v250, 1536, v250\n"
    "v_add_u32 v251, 1536, v251\n"
    "v_mfma_f32_16x16x32_bf16 a[172:175], v[192:195], v[96:99], a[172:175]\n"
    "s_add_u32 s66, s66, 1\n"
    "v_mov_b32 v2, v3\n"
    "v_add_u32 v3, s74, v252\n"
    "v_mfma_f32_16x16x32_bf16 a[156:159], v[184:187], v[96:99], a[156:159]\n"
    "s_waitcnt lgkmcnt(1)\n"
    "s_cmp_lt_u32 s66, 30\n"
    "v_mfma_f32_16x16x32_bf16 a[156:159], v[180:183], v[92:95], a[156:159]\n"
    "s_cbranch_scc1 .LPK\n"
    "s_branch .Ltail\n"
    "s_nop 0\n"
    "s_nop 0\n"
    ".LpollAK:\n"
    "s_sleep 1\n"
    "buffer_load_dwordx2 v[236:237], v87, s[36:39], 0 offen offset:0 sc1\n"
    "s_waitcnt vmcnt(0)\n"
    "v_cmp_eq_u64 s[54:55], v[236:237], v[238:239]\n"
    "s_add_u32 s58, s58, 1\n"
    "s_cmp_eq_u64 s[54:55], -1\n"
    "s_cbranch_scc1 .LpollAKr\n"
    "s_cmp_lt_u32 s58, 0x80000\n"
    "s_cbranch_scc1 .LpollAK\n"
    "s_branch .LpollAKr\n"
    "s_nop 0\n"
    "s_nop 0\n"
    ".LpollAV:\n"
    "s_sleep 1\n"
    "buffer_load_dwordx2 v[236:237], v87, s[36:39], 0 offen offset:0 sc1\n"
    "s_waitcnt vmcnt(0)\n"
    "v_cmp_eq_u64 s[54:55], v[236:237], v[238:239]\n"
    "s_add_u32 s58, s58, 1\n"
    "s_cmp_eq_u64 s[54:55], -1\n"
    "s_cbranch_scc1 .LpollAVr\n"
    "s_cmp_lt_u32 s58, 0x80000\n"
    "s_cbranch_scc1 .LpollAV\n"
    "s_branch .LpollAVr\n"
    "s_nop 0\n"
    "s_nop 0\n"
    "s_nop 0\n"
    "s_nop 0\n"
    "s_nop 0\n"
    "s_nop 0\n"
    "s_nop 0\n"
    ".Lpoll1:\n"
    "s_sleep 1\n"
    "v_cmp_eq_u64 s[54:55], v[236:237], v[238:239]\n"
    "s_nop 1\n"
    "s_cmp_eq_u64 s[54:55], -1\n"
    "s_cbranch_scc1 .Lpoll1l\n"
    "buffer_load_dwordx2 v[236:237], v241, s[36:39], s81 offen offset:0 sc1\n"
    "s_waitcnt vmcnt(0)\n"
    "v_cmp_eq_u64 s[54:55], v[236:237], v[238:239]\n"
    ".Lpoll1l:\n"
    "ds_read_b32 v245, v247\n"
    "s_waitcnt lgkmcnt(0)\n"
    "v_cmp_le_u32 s[56:57], s59, v245\n"
    "s_add_u32 s58, s58, 1\n"
    "s_nop 1\n"
    "s_and_b64 s[54:55], s[54:55], s[56:57]\n"
    "s_cmp_eq_u64 s[54:55], -1\n"
    "s_cbranch_scc1 .Lpoll1r\n"
    "s_cmp_lt_u32 s58, 0x80000\n"
    "s_cbranch_scc1 .Lpoll1\n"
    "s_branch .Lpoll1r\n"
    "s_nop 0\n"
    "s_nop 0\n"
    "s_nop 0\n"
    "s_nop 0\n"
    "s_nop 0\n"
    "s_nop 0\n"
    "s_nop 0\n"
    ".Lpoll2:\n"
    "s_sleep 1\n"
    "v_cmp_eq_u64 s[54:55], v[236:237], v[238:239]\n"
    "s_nop 1\n"
    "s_cmp_eq_u64 s[54:55], -1\n"
    "s_cbranch_scc1 .Lpoll2l\n"
    "buffer_load_dwordx2 v[236:237], v241, s[36:39], s81 offen offset:0 sc1\n"
    "s_waitcnt vmcnt(0)\n"
    "v_cmp_eq_u64 s[54:55], v[236:237], v[238:239]\n"
    ".Lpoll2l:\n"
    "ds_read_b32 v245, v247\n"
    "s_waitcnt lgkmcnt(0)\n"
    "v_cmp_le_u32 s[56:57], s59, v245\n"
    "s_add_u32 s58, s58, 1\n"
    "s_nop 1\n"
    "s_and_b64 s[54:55], s[54:55], s[56:57]\n"
    "s_cmp_eq_u64 s[54:55], -1\n"
    "s_cbranch_scc1 .Lpoll2r\n"
    "s_cmp_lt_u32 s58, 0x80000\n"
    "s_cbranch_scc1 .Lpoll2\n"
    "s_branch .Lpoll2r\n"
    "s_nop 0\n"
    "s_nop 0\n"
    "s_nop 0\n"
    "s_nop 0\n"
    "s_nop 0\n"
    "s_nop 0\n"
    "s_nop 0\n"
    ".Lpoll3:\n"
    "s_sleep 1\n"
    "v_cmp_eq_u64 s[54:55], v[236:237], v[238:239]\n"
    "s_nop 1\n"
    "s_cmp_eq_u64 s[54:55], -1\n"
    "s_cbranch_scc1 .Lpoll3l\n"
    "buffer_load_dwordx2 v[236:237], v241, s[36:39], s81 offen offset:0 sc1\n"
    "s_waitcnt vmcnt(0)\n"
    "v_cmp_eq_u64 s[54:55], v[236:237], v[238:239]\n"
    ".Lpoll3l:\n"
    "ds_read_b32 v245, v247\n"
    "s_waitcnt lgkmcnt(0)\n"
    "v_cmp_le_u32 s[56:57], s59, v245\n"
    "s_add_u32 s58, s58, 1\n"
    "s_nop 1\n"
    "s_and_b64 s[54:55], s[54:55], s[56:57]\n"
    "s_cmp_eq_u64 s[54:55], -1\n"
    "s_cbranch_scc1 .Lpoll3r\n"
    "s_cmp_lt_u32 s58, 0x80000\n"
    "s_cbranch_scc1 .Lpoll3\n"
    "s_branch .Lpoll3r\n"
    "s_nop 0\n"
    "s_nop 0\n"
    "s_nop 0\n"
    "s_nop 0\n"
    "s_nop 0\n"
    "s_nop 0\n"
    "s_nop 0\n"
    ".Lpoll4:\n"
    "s_sleep 1\n"
    "v_cmp_eq_u64 s[54:55], v[236:237], v[238:239]\n"
    "s_nop 1\n"
    "s_cmp_eq_u64 s[54:55], -1\n"
    "s_cbranch_scc1 .Lpoll4l\n"
    "buffer_load_dwordx2 v[236:237], v241, s[36:39], s81 offen offset:0 sc1\n"
    "s_waitcnt vmcnt(0)\n"
    "v_cmp_eq_u64 s[54:55], v[236:237], v[238:239]\n"
    ".Lpoll4l:\n"
    "ds_read_b32 v245, v247\n"
    "s_waitcnt lgkmcnt(0)\n"
    "v_cmp_le_u32 s[56:57], s59, v245\n"
    "s_add_u32 s58, s58, 1\n"
    "s_nop 1\n"
    "s_and_b64 s[54:55], s[54:55], s[56:57]\n"
    "s_cmp_eq_u64 s[54:55], -1\n"
    "s_cbranch_scc1 .Lpoll4r\n"
    "s_cmp_lt_u32 s58, 0x80000\n"
    "s_cbranch_scc1 .Lpoll4\n"
    "s_branch .Lpoll4r\n"
    "s_nop 0\n"
    "s_nop 0\n"
    "s_nop 0\n"
    "s_nop 0\n"
    "s_nop 0\n"
    "s_nop 0\n"
    "s_nop 0\n"
    ".Lpt0:\n"
    "s_sleep 1\n"
    "v_cmp_eq_u64 s[54:55], v[236:237], v[238:239]\n"
    "s_nop 1\n"
    "s_cmp_eq_u64 s[54:55], -1\n"
    "s_cbranch_scc1 .Lpt0l\n"
    "buffer_load_dwordx2 v[236:237], v241, s[36:39], s81 offen offset:0 sc1\n"
    "s_waitcnt vmcnt(0)\n"
    "v_cmp_eq_u64 s[54:55], v[236:237], v[238:239]\n"
    ".Lpt0l:\n"
    "ds_read_b32 v245, v247\n"
    "s_waitcnt lgkmcnt(0)\n"
    "v_cmp_le_u32 s[56:57], s59, v245\n"
    "s_add_u32 s58, s58, 1\n"
    "s_nop 1\n"
    "s_and_b64 s[54:55], s[54:55], s[56:57]\n"
    "s_cmp_eq_u64 s[54:55], -1\n"
    "s_cbranch_scc1 .Lpt0r\n"
    "s_cmp_lt_u32 s58, 0x80000\n"
    "s_cbranch_scc1 .Lpt0\n"
    "s_branch .Lpt0r\n"
    "s_nop 0\n"
    "s_nop 0\n"
    "s_nop 0\n"
    ".Lpt1:\n"
    "s_sleep 1\n"
    "ds_read_b32 v245, v247\n"
    "s_waitcnt lgkmcnt(0)\n"
    "v_cmp_le_u32 s[56:57], s59, v245\n"
    "s_add_u32 s58, s58, 1\n"
    "s_nop 1\n"
    "s_cmp_eq_u64 s[56:57], -1\n"
    "s_cbranch_scc1 .Lpt1r\n"
    "s_cmp_lt_u32 s58, 0x80000\n"
    "s_cbranch_scc1 .Lpt1\n"
    "s_branch .Lpt1r\n"
    ".fill ((0x20000 - (.LwVe - .LwV) - (. - _Z8attn_fwdPKtPKiS2_S0_S0_S0_S0_PcPt)) & 4095) / 4, 4, 0xBF800000\n"
    ".LwV:\n"
    "s_branch .LwVe\n"
    ".LPV:\n"
    "v_mfma_f32_16x16x32_bf16 v[36:39], v[156:159], a[192:195], v[100:103]\n"
    "s_add_u32 s71, s71, 98304\n"
    "ds_read_b128 v[180:183], v2 offset:13312\n"
    "v_cvt_pk_bf16_f32 v68, v4, v5\n"
    "v_mfma_f32_16x16x32_bf16 v[36:39], v[160:163], a[196:199], v[36:39]\n"
    "s_add_u32 s75, s71, s63\n"
    "ds_read_b128 v[184:187], v2 offset:15360\n"
    "v_cvt_pk_bf16_f32 v69, v6, v7\n"
    "v_mfma_f32_16x16x32_bf16 v[36:39], v[164:167], a[200:203], v[36:39]\n"
    "ds_read_b128 v[188:191], v2 offset:17408\n"
    "v_cvt_pk_bf16_f32 v76, v8, v9\n"
    "v_mfma_f32_16x16x32_bf16 v[36:39], v[168:171], a[204:207], v[36:39]\n"
    "ds_read_b128 v[192:195], v2 offset:19456\n"
    "v_cvt_pk_bf16_f32 v77, v10, v11\n"
    "v_mfma_f32_16x16x32_bf16 v[36:39], v[172:175], a[208:211], v[36:39]\n"
    "ds_read_b128 v[196:199], v2 offset:21504\n"
    "v_cvt_pk_bf16_f32 v84, v12, v13\n"
    "v_mfma_f32_16x16x32_bf16 v[36:39], v[176:179], a[212:215], v[36:39]\n"
    "ds_read_b128 v[200:203], v2 offset:23552\n"
    "v_cvt_pk_bf16_f32 v85, v14, v15\n"
    "v_mfma_f32_16x16x32_bf16 v[40:43], v[176:179], a[236:239], v[100:103]\n"
    "ds_read_b128 v[104:107], v250 offset:144\n"
    "v_cvt_pk_bf16_f32 v92, v16, v17\n"
    "v_mfma_f32_16x16x32_bf16 v[40:43], v[172:175], a[232:235], v[40:43]\n"
    "s_add_u32 s68, s68, 24576\n"
    "v_cvt_pk_bf16_f32 v93, v18, v19\n"
    "v_cvt_pk_bf16_f32 v70, v20, v21\n"
    "v_mfma_f32_16x16x32_bf16 v[40:43], v[168:171], a[228:231], v[40:43]\n"
    "s_cmp_ge_u32 s68, s69\n"
    "s_cselect_b32 s68, s65, s68\n"
    "v_cvt_pk_bf16_f32 v71, v22, v23\n"
    "v_mfma_f32_16x16x32_bf16 v[40:43], v[164:167], a[224:227], v[40:43]\n"
    "v_cvt_pk_bf16_f32 v78, v24, v25\n"
    "v_cvt_pk_bf16_f32 v79, v26, v27\n"
    "v_mfma_f32_16x16x32_bf16 v[40:43], v[160:163], a[220:223], v[40:43]\n"
    "v_cvt_pk_bf16_f32 v86, v28, v29\n"
    "v_cvt_pk_bf16_f32 v87, v30, v31\n"
    "v_mfma_f32_16x16x32_bf16 v[40:43], v[156:159], a[216:219], v[40:43]\n"
    "v_cvt_pk_bf16_f32 v94, v32, v33\n"
    "v_cvt_pk_bf16_f32 v95, v34, v35\n"
    "v_mfma_f32_16x16x32_bf16 v[44:47], v[156:159], a[240:243], v[100:103]\n"
    "v_exp_f32 v36, v36\n"
    "v_mfma_f32_16x16x32_bf16 v[44:47], v[160:163], a[244:247], v[44:47]\n"
    "v_exp_f32 v37, v37\n"
    "v_mfma_f32_16x16x32_bf16 v[44:47], v[164:167], a[248:251], v[44:47]\n"
    "v_exp_f32 v38, v38\n"
    "v_mfma_f32_16x16x32_bf16 v[44:47], v[168:171], a[252:255], v[44:47]\n"
    "v_exp_f32 v39, v39\n"
    "v_mfma_f32_16x16x32_bf16 v[44:47], v[172:175], v[124:127], v[44:47]\n"
    "v_cvt_pk_bf16_f32 v72, v36, v37\n"
    "v_cvt_pk_bf16_f32 v73, v38, v39\n"
    "v_mfma_f32_16x16x32_bf16 v[44:47], v[176:179], v[128:131], v[44:47]\n"
    "v_exp_f32 v40, v40\n"
    "v_mfma_f32_16x16x32_bf16 v[48:51], v[176:179], v[152:155], v[100:103]\n"
    "ds_read_b32 v245, v247\n"
    "v_mfma_f32_16x16x32_bf16 v[48:51], v[172:175], v[148:151], v[48:51]\n"
    "s_add_u32 m0, s68, 0\n"
    "v_exp_f32 v41, v41\n"
    "v_mfma_f32_16x16x32_bf16 v[48:51], v[168:171], v[144:147], v[48:51]\n"
    "v_exp_f32 v42, v42\n"
    "v_mfma_f32_16x16x32_bf16 v[48:51], v[164:167], v[140:143], v[48:51]\n"
    "v_exp_f32 v43, v43\n"
    "v_mfma_f32_16x16x32_bf16 v[48:51], v[160:163], v[136:139], v[48:51]\n"
    "s_waitcnt vmcnt(13)\n"
    "v_cmp_eq_u64 s[54:55], v[236:237], v[238:239]\n"
    "v_cvt_pk_bf16_f32 v80, v40, v41\n"
    "v_mfma_f32_16x16x32_bf16 v[48:51], v[156:159], v[132:135], v[48:51]\n"
    "s_waitcnt lgkmcnt(1)\n"
    "v_cvt_pk_bf16_f32 v81, v42, v43\n"
    "v_mfma_f32_16x16x32_bf16 v[52:55], v[180:183], a[192:195], v[104:107]\n"
    "s_waitcnt lgkmcnt(0)\n"
    "v_cmp_le_u32 s[56:57], s59, v245\n"
    "v_mfma_f32_16x16x32_bf16 v[52:55], v[184:187], a[196:199], v[52:55]\n"
    "v_exp_f32 v44, v44\n"
    "v_mfma_f32_16x16x32_bf16 v[52:55], v[188:191], a[200:203], v[52:55]\n"
    "s_and_b64 s[54:55], s[54:55], s[56:57]\n"
    "s_cmp_eq_u64 s[54:55], -1\n"
    "v_mfma_f32_16x16x32_bf16 v[52:55], v[192:195], a[204:207], v[52:55]\n"
    "s_cbranch_scc0 .Lpoll3\n"
    ".Lpoll3r:\n"
    "v_mfma_f32_16x16x32_bf16 v[52:55], v[196:199], a[208:211], v[52:55]\n"
    "buffer_load_dwordx4 v1, s[16:19], s67 offen offset:0 sc1 lds\n"
    "v_mfma_f32_16x16x32_bf16 v[52:55], v[200:203], a[212:215], v[52:55]\n"
    "ds_read_b128 v[156:159], v3 offset:0\n"
    "v_mfma_f32_16x16x32_bf16 v[56:59], v[200:203], a[236:239], v[104:107]\n"
    "ds_read_b128 v[160:163], v3 offset:2048\n"
    "v_mfma_f32_16x16x32_bf16 v[56:59], v[196:199], a[232:235], v[56:59]\n"
    "ds_read_b128 v[164:167], v3 offset:4096\n"
    "v_mfma_f32_16x16x32_bf16 v[56:59], v[192:195], a[228:231], v[56:59]\n"
    "buffer_load_dwordx4 v1, s[16:19], s67 offen offset:1024 sc1 lds\n"
    "v_mfma_f32_16x16x32_bf16 v[56:59], v[188:191], a[224:227], v[56:59]\n"
    "ds_read_b128 v[168:171], v3 offset:6144\n"
    "v_mfma_f32_16x16x32_bf16 v[56:59], v[184:187], a[220:223], v[56:59]\n"
    "ds_read_b128 v[172:175], v3 offset:8192\n"
    "v_mfma_f32_16x16x32_bf16 v[56:59], v[180:183], a[216:219], v[56:59]\n"
    "ds_read_b128 v[176:179], v3 offset:10240\n"
    "v_mfma_f32_16x16x32_bf16 v[60:63], v[180:183], a[240:243], v[104:107]\n"
    "buffer_load_dwordx4 v1, s[16:19], s67 offen offset:2048 sc1 lds\n"
    "v_mfma_f32_16x16x32_bf16 v[60:63], v[184:187], a[244:247], v[60:63]\n"
    "ds_read_b128 v[100:103], v250 offset:768\n"
    "v_mfma_f32_16x16x32_bf16 v[60:63], v[188:191], a[248:251], v[60:63]\n"
    "v_exp_f32 v52, v52\n"
    "v_mfma_f32_16x16x32_bf16 v[60:63], v[192:195], a[252:255], v[60:63]\n"
    "v_exp_f32 v53, v53\n"
    "v_mfma_f32_16x16x32_bf16 v[60:63], v[196:199], v[124:127], v[60:63]\n"
    "buffer_load_dwordx4 v1, s[16:19], s67 offen offset:3072 sc1 lds\n"
    "v_mfma_f32_16x16x32_bf16 v[60:63], v[200:203], v[128:131], v[60:63]\n"
    "s_add_u32 m0, s68, 4096\n"
    "v_exp_f32 v54, v54\n"
    "v_mfma_f32_16x16x32_bf16 v[64:67], v[200:203], v[152:155], v[104:107]\n"
    "s_add_u32 s83, s67, 4096\n"
    "v_exp_f32 v55, v55\n"
    "v_mfma_f32_16x16x32_bf16 v[64:67], v[196:199], v[148:151], v[64:67]\n"
    "v_cvt_pk_bf16_f32 v74, v52, v53\n"
    "v_cvt_pk_bf16_f32 v75, v54, v55\n"
    "v_mfma_f32_16x16x32_bf16 v[64:67], v[192:195], v[144:147], v[64:67]\n"
    "buffer_load_dwordx4 v1, s[16:19], s83 offen offset:0 sc1 lds\n"
    "v_mfma_f32_16x16x32_bf16 v[64:67], v[188:191], v[140:143], v[64:67]\n"
    "v_exp_f32 v56, v56\n"
    "v_mfma_f32_16x16x32_bf16 v[64:67], v[184:187], v[136:139], v[64:67]\n"
    "v_exp_f32 v57, v57\n"
    "v_mfma_f32_16x16x32_bf16 v[64:67], v[180:183], v[132:135], v[64:67]\n"
    "s_waitcnt lgkmcnt(0)\n"
    "v_exp_f32 v58, v58\n"
    "v_mfma_f32_16x16x32_bf16 v[4:7], v[156:159], a[192:195], v[100:103]\n"
    "buffer_load_dwordx4 v1, s[16:19], s83 offen offset:1024 sc1 lds\n"
    "v_mfma_f32_16x16x32_bf16 v[4:7], v[160:163], a[196:199], v[4:7]\n"
    "s_add_u32 m0, s64, 0\n"
    "ds_read_b128 v[180:183], v3 offset:1024\n"
    "v_cvt_pk_bf16_f32 v82, v56, v57\n"
    "v_mfma_f32_16x16x32_bf16 v[4:7], v[164:167], a[200:203], v[4:7]\n"
    "s_add_u32 s83, s67, 24576\n"
    "ds_read_b128 v[184:187], v3 offset:3072\n"
    "v_mfma_f32_16x16x32_bf16 v[4:7], v[168:171], a[204:207], v[4:7]\n"
    "ds_read_b128 v[188:191], v3 offset:5120\n"
    "v_mfma_f32_16x16x32_bf16 v[4:7], v[172:175], a[208:211], v[4:7]\n"
    "buffer_load_dwordx4 v1, s[16:19], s83 offen offset:0 sc1 lds\n"
    "v_mfma_f32_16x16x32_bf16 v[4:7], v[176:179], a[212:215], v[4:7]\n"
    "ds_read_b128 v[192:195], v3 offset:7168\n"
    "v_mfma_f32_16x16x32_bf16 v[8:11], v[176:179], a[236:239], v[100:103]\n"
    "ds_read_b128 v[196:199], v3 offset:9216\n"
    "v_mfma_f32_16x16x32_bf16 v[8:11], v[172:175], a[232:235], v[8:11]\n"
    "ds_read_b128 v[200:203], v3 offset:11264\n"
    "v_mfma_f32_16x16x32_bf16 v[8:11], v[168:171], a[228:231], v[8:11]\n"
    "buffer_load_dwordx4 v1, s[16:19], s83 offen offset:1024 sc1 lds\n"
    "v_mfma_f32_16x16x32_bf16 v[8:11], v[164:167], a[224:227], v[8:11]\n"
    "ds_read_b128 v[104:107], v250 offset:784\n"
    "v_mfma_f32_16x16x32_bf16 v[8:11], v[160:163], a[220:223], v[8:11]\n"
    "v_exp_f32 v59, v59\n"
    "v_mfma_f32_16x16x32_bf16 v[8:11], v[156:159], a[216:219], v[8:11]\n"
    "v_cvt_pk_bf16_f32 v83, v58, v59\n"
    "v_mfma_f32_16x16x32_bf16 v[12:15], v[156:159], a[240:243], v[100:103]\n"
    "buffer_load_dwordx4 v1, s[16:19], s83 offen offset:2048 sc1 lds\n"
    "v_mfma_f32_16x16x32_bf16 v[12:15], v[160:163], a[244:247], v[12:15]\n"
    "v_exp_f32 v45, v45\n"
    "v_mfma_f32_16x16x32_bf16 v[12:15], v[164:167], a[248:251], v[12:15]\n"
    "v_exp_f32 v46, v46\n"
    "v_mfma_f32_16x16x32_bf16 v[12:15], v[168:171], a[252:255], v[12:15]\n"
    "v_exp_f32 v47, v47\n"
    "v_mfma_f32_16x16x32_bf16 v[12:15], v[172:175], v[124:127], v[12:15]\n"
    "buffer_load_dwordx4 v1, s[16:19], s83 offen offset:3072 sc1 lds\n"
    "v_mfma_f32_16x16x32_bf16 v[12:15], v[176:179], v[128:131], v[12:15]\n"
    "s_add_u32 m0, s64, 4096\n"
    "v_exp_f32 v60, v60\n"
    "v_mfma_f32_16x16x32_bf16 v[16:19], v[176:179], v[152:155], v[100:103]\n"
    "s_add_u32 s83, s67, 28672\n"
    "v_exp_f32 v61, v61\n"
    "v_mfma_f32_16x16x32_bf16 v[16:19], v[172:175], v[148:151], v[16:19]\n"
    "v_exp_f32 v62, v62\n"
    "v_mfma_f32_16x16x32_bf16 v[16:19], v[168:171], v[144:147], v[16:19]\n"
    "buffer_load_dwordx4 v1, s[16:19], s83 offen offset:0 sc1 lds\n"
    "v_mfma_f32_16x16x32_bf16 v[16:19], v[164:167], v[140:143], v[16:19]\n"
    "v_exp_f32 v63, v63\n"
    "v_mfma_f32_16x16x32_bf16 v[16:19], v[160:163], v[136:139], v[16:19]\n"
    "v_cvt_pk_bf16_f32 v88, v44, v45\n"
    "v_cvt_pk_bf16_f32 v89, v46, v47\n"
    "v_mfma_f32_16x16x32_bf16 v[16:19], v[156:159], v[132:135], v[16:19]\n"
    "s_waitcnt lgkmcnt(0)\n"
    "v_cvt_pk_bf16_f32 v90, v60, v61\n"
    "v_cvt_pk_bf16_f32 v91, v62, v63\n"
    "v_mfma_f32_16x16x32_bf16 v[20:23], v[180:183], a[192:195], v[104:107]\n"
    "buffer_load_dwordx4 v1, s[16:19], s83 offen offset:1024 sc1 lds\n"
    "v_mfma_f32_16x16x32_bf16 v[20:23], v[184:187], a[196:199], v[20:23]\n"
    "s_add_u32 s67, s67, 49152\n"
    "ds_read_b128 v[156:159], v248 offset:0\n"
    "v_mfma_f32_16x16x32_bf16 v[20:23], v[188:191], a[200:203], v[20:23]\n"
    "s_add_u32 s59, s59, 4\n"
    "ds_read_b128 v[160:163], v248 offset:12288\n"
    "v_mfma_f32_16x16x32_bf16 v[20:23], v[192:195], a[204:207], v[20:23]\n"
    "s_waitcnt vmcnt(12)\n"
    "ds_read_b128 v[164:167], v248 offset:1024\n"
    "v_mfma_f32_16x16x32_bf16 v[20:23], v[196:199], a[208:211], v[20:23]\n"
    "buffer_store_dwordx2 v[238:239], v240, s[36:39], s80 offen offset:0 sc1\n"
    "v_mfma_f32_16x16x32_bf16 v[20:23], v[200:203], a[212:215], v[20:23]\n"
    "buffer_load_dwordx4 v[204:207], v242, s[40:43], s75 offen offset:0 nt\n"
    "v_mfma_f32_16x16x32_bf16 v[24:27], v[200:203], a[236:239], v[104:107]\n"
    "s_add_u32 s80, s80, 256\n"
    "ds_read_b128 v[168:171], v248 offset:13312\n"
    "v_mfma_f32_16x16x32_bf16 v[24:27], v[196:199], a[232:235], v[24:27]\n"
    "buffer_load_dwordx4 v[208:211], v242, s[40:43], s75 offen offset:768 nt\n"
    "v_mfma_f32_16x16x32_bf16 v[24:27], v[192:195], a[228:231], v[24:27]\n"
    "ds_read_b128 v[172:175], v248 offset:2048\n"
    "v_mfma_f32_16x16x32_bf16 v[24:27], v[188:191], a[224:227], v[24:27]\n"
    "buffer_load_dwordx4 v[212:215], v242, s[40:43], s75 offen offset:256 nt\n"
    "v_mfma_f32_16x16x32_bf16 v[24:27], v[184:187], a[220:223], v[24:27]\n"
    "ds_read_b128 v[176:179], v248 offset:14336\n"
    "v_mfma_f32_16x16x32_bf16 v[24:27], v[180:183], a[216:219], v[24:27]\n"
    "buffer_load_dwordx4 v[216:219], v242, s[40:43], s75 offen offset:1024 nt\n"
    "v_mfma_f32_16x16x32_bf16 v[28:31], v[180:183], a[240:243], v[104:107]\n"
    "v_exp_f32 v48, v48\n"
    "v_mfma_f32_16x16x32_bf16 v[28:31], v[184:187], a[244:247], v[28:31]\n"
    "buffer_load_dwordx4 v[220:223], v242, s[40:43], s75 offen offset:512 nt\n"
    "v_mfma_f32_16x16x32_bf16 v[28:31], v[188:191], a[248:251], v[28:31]\n"
    "v_exp_f32 v49, v49\n"
    "v_mfma_f32_16x16x32_bf16 v[28:31], v[192:195], a[252:255], v[28:31]\n"
    "buffer_load_dwordx4 v[224:227], v242, s[40:43], s75 offen offset:1280 nt\n"
    "v_mfma_f32_16x16x32_bf16 v[28:31], v[196:199], v[124:127], v[28:31]\n"
    "v_exp_f32 v50, v50\n"
    "v_mfma_f32_16x16x32_bf16 v[28:31], v[200:203], v[128:131], v[28:31]\n"
    "v_exp_f32 v51, v51\n"
    "v_mfma_f32_16x16x32_bf16 v[32:35], v[200:203], v[152:155], v[104:107]\n"
    "v_exp_f32 v64, v64\n"
    "v_mfma_f32_16x16x32_bf16 v[32:35], v[196:199], v[148:151], v[32:35]\n"
    "v_exp_f32 v65, v65\n"
    "v_mfma_f32_16x16x32_bf16 v[32:35], v[192:195], v[144:147], v[32:35]\n"
    "v_exp_f32 v66, v66\n"
    "v_mfma_f32_16x16x32_bf16 v[32:35], v[188:191], v[140:143], v[32:35]\n"
    "v_exp_f32 v67, v67\n"
    "v_mfma_f32_16x16x32_bf16 v[32:35], v[184:187], v[136:139], v[32:35]\n"
    "v_cvt_pk_bf16_f32 v96, v48, v49\n"
    "v_cvt_pk_bf16_f32 v97, v50, v51\n"
    "v_mfma_f32_16x16x32_bf16 v[32:35], v[180:183], v[132:135], v[32:35]\n"
    "s_waitcnt lgkmcnt(0)\n"
    "v_cvt_pk_bf16_f32 v98, v64, v65\n"
    "v_cvt_pk_bf16_f32 v99, v66, v67\n"
    "v_mfma_f32_16x16x32_bf16 a[0:3], v[156:159], v[68:71], a[0:3]\n"
    "ds_read_b128 v[180:183], v248 offset:3072\n"
    "v_mfma_f32_16x16x32_bf16 a[0:3], v[160:163], v[72:75], a[0:3]\n"
    "ds_read_b128 v[184:187], v248 offset:15360\n"
    "v_mfma_f32_16x16x32_bf16 a[16:19], v[168:171], v[72:75], a[16:19]\n"
    "ds_read_b128 v[188:191], v248 offset:4096\n"
    "v_mfma_f32_16x16x32_bf16 a[16:19], v[164:167], v[68:71], a[16:19]\n"
    "ds_read_b128 v[192:195], v248 offset:16384\n"
    "v_mfma_f32_16x16x32_bf16 a[32:35], v[172:175], v[68:71], a[32:35]\n"
    "ds_read_b128 v[196:199], v248 offset:5120\n"
    "v_mfma_f32_16x16x32_bf16 a[32:35], v[176:179], v[72:75], a[32:35]\n"
    "ds_read_b128 v[200:203], v248 offset:17408\n"
    "v_mfma_f32_16x16x32_bf16 a[36:39], v[176:179], v[80:83], a[36:39]\n"
    "ds_read_b128 v[104:107], v251 offset:0\n"
    "v_mfma_f32_16x16x32_bf16 a[36:39], v[172:175], v[76:79], a[36:39]\n"
    "v_exp_f32 v4, v4\n"
    "v_mfma_f32_16x16x32_bf16 a[20:23], v[164:167], v[76:79], a[20:23]\n"
    "v_exp_f32 v5, v5\n"
    "v_mfma_f32_16x16x32_bf16 a[20:23], v[168:171], v[80:83], a[20:23]\n"
    "v_exp_f32 v6, v6\n"
    "v_mfma_f32_16x16x32_bf16 a[4:7], v[160:163], v[80:83], a[4:7]\n"
    "v_mfma_f32_16x16x32_bf16 a[4:7], v[156:159], v[76:79], a[4:7]\n"
    "v_mfma_f32_16x16x32_bf16 a[8:11], v[156:159], v[84:87], a[8:11]\n"
    "v_exp_f32 v7, v7\n"
    "v_mfma_f32_16x16x32_bf16 a[8:11], v[160:163], v[88:91], a[8:11]\n"
    "v_mfma_f32_16x16x32_bf16 a[24:27], v[168:171], v[88:91], a[24:27]\n"
    "v_exp_f32 v8, v8\n"
    "v_mfma_f32_16x16x32_bf16 a[24:27], v[164:167], v[84:87], a[24:27]\n"
    "v_mfma_f32_16x16x32_bf16 a[40:43], v[172:175], v[84:87], a[40:43]\n"
    "v_mfma_f32_16x16x32_bf16 a[40:43], v[176:179], v[88:91], a[40:43]\n"
    "v_exp_f32 v9, v9\n"
    "v_mfma_f32_16x16x32_bf16 a[44:47], v[176:179], v[96:99], a[44:47]\n"
    "v_mfma_f32_16x16x32_bf16 a[44:47], v[172:175], v[92:95], a[44:47]\n"
    "v_mfma_f32_16x16x32_bf16 a[28:31], v[164:167], v[92:95], a[28:31]\n"
    "v_exp_f32 v10, v10\n"
    "v_mfma_f32_16x16x32_bf16 a[28:31], v[168:171], v[96:99], a[28:31]\n"
    "v_mfma_f32_16x16x32_bf16 a[12:15], v[160:163], v[96:99], a[12:15]\n"
    "v_exp_f32 v11, v11\n"
    "v_mfma_f32_16x16x32_bf16 a[12:15], v[156:159], v[92:95], a[12:15]\n"
    "s_waitcnt lgkmcnt(0)\n"
    "v_mfma_f32_16x16x32_bf16 a[48:51], v[180:183], v[68:71], a[48:51]\n"
    "ds_read_b128 v[156:159], v248 offset:6144\n"
    "v_mfma_f32_16x16x32_bf16 a[48:51], v[184:187], v[72:75], a[48:51]\n"
    "ds_read_b128 v[160:163], v248 offset:18432\n"
    "v_mfma_f32_16x16x32_bf16 a[64:67], v[192:195], v[72:75], a[64:67]\n"
    "ds_read_b128 v[164:167], v248 offset:7168\n"
    "v_mfma_f32_16x16x32_bf16 a[64:67], v[188:191], v[68:71], a[64:67]\n"
    "ds_read_b128 v[168:171], v248 offset:19456\n"
    "v_mfma_f32_16x16x32_bf16 a[80:83], v[196:199], v[68:71], a[80:83]\n"
    "ds_read_b128 v[172:175], v248 offset:8192\n"
    "v_mfma_f32_16x16x32_bf16 a[80:83], v[200:203], v[72:75], a[80:83]\n"
    "ds_read_b128 v[176:179], v248 offset:20480\n"
    "v_smfmac_f32_16x16x64_bf16 v[108:111], v[104:107], v[68:75], v253\n"
    "s_add_u32 s81, s81, 128\n"
    "s_cmp_gt_u32 s81, 4096\n"
    "s_cselect_b32 s81, 0, s81\n"
    "v_smfmac_f32_16x16x64_bf16 v[112:115], v[104:107], v[76:83], v253\n"
    "v_mfma_f32_16x16x32_bf16 a[84:87], v[200:203], v[80:83], a[84:87]\n"
    "v_exp_f32 v12, v12\n"
    "v_mfma_f32_16x16x32_bf16 a[84:87], v[196:199], v[76:79], a[84:87]\n"
    "v_exp_f32 v13, v13\n"
    "v_mfma_f32_16x16x32_bf16 a[68:71], v[188:191], v[76:79], a[68:71]\n"
    "v_exp_f32 v14, v14\n"
    "v_mfma_f32_16x16x32_bf16 a[68:71], v[192:195], v[80:83], a[68:71]\n"
    "v_exp_f32 v15, v15\n"
    "v_mfma_f32_16x16x32_bf16 a[52:55], v[184:187], v[80:83], a[52:55]\n"
    "v_exp_f32 v16, v16\n"
    "v_mfma_f32_16x16x32_bf16 a[52:55], v[180:183], v[76:79], a[52:55]\n"
    "v_mfma_f32_16x16x32_bf16 a[56:59], v[180:183], v[84:87], a[56:59]\n"
    "v_exp_f32 v17, v17\n"
    "v_mfma_f32_16x16x32_bf16 a[56:59], v[184:187], v[88:91], a[56:59]\n"
    "v_mfma_f32_16x16x32_bf16 a[72:75], v[192:195], v[88:91], a[72:75]\n"
    "v_mfma_f32_16x16x32_bf16 a[72:75], v[188:191], v[84:87], a[72:75]\n"
    "v_exp_f32 v18, v18\n"
    "v_mfma_f32_16x16x32_bf16 a[88:91], v[196:199], v[84:87], a[88:91]\n"
    "v_mfma_f32_16x16x32_bf16 a[88:91], v[200:203], v[88:91], a[88:91]\n"
    "v_smfmac_f32_16x16x64_bf16 v[116:119], v[104:107], v[84:91], v253\n"
    "v_smfmac_f32_16x16x64_bf16 v[120:123], v[104:107], v[92:99], v253\n"
    "v_mfma_f32_16x16x32_bf16 a[92:95], v[200:203], v[96:99], a[92:95]\n"
    "v_exp_f32 v19, v19\n"
    "v_mfma_f32_16x16x32_bf16 a[92:95], v[196:199], v[92:95], a[92:95]\n"
    "v_exp_f32 v20, v20\n"
    "v_mfma_f32_16x16x32_bf16 a[76:79], v[188:191], v[92:95], a[76:79]\n"
    "v_mfma_f32_16x16x32_bf16 a[76:79], v[192:195], v[96:99], a[76:79]\n"
    "v_exp_f32 v21, v21\n"
    "v_mfma_f32_16x16x32_bf16 a[60:63], v[184:187], v[96:99], a[60:63]\n"
    "v_mfma_f32_16x16x32_bf16 a[60:63], v[180:183], v[92:95], a[60:63]\n"
    "s_waitcnt lgkmcnt(0)\n"
    "v_mfma_f32_16x16x32_bf16 a[96:99], v[156:159], v[68:71], a[96:99]\n"
    "ds_read_b128 v[180:183], v248 offset:9216\n"
    "v_mfma_f32_16x16x32_bf16 a[96:99], v[160:163], v[72:75], a[96:99]\n"
    "ds_read_b128 v[184:187], v248 offset:21504\n"
    "v_mfma_f32_16x16x32_bf16 a[112:115], v[168:171], v[72:75], a[112:115]\n"
    "ds_read_b128 v[188:191], v248 offset:10240\n"
    "v_mfma_f32_16x16x32_bf16 a[112:115], v[164:167], v[68:71], a[112:115]\n"
    "ds_read_b128 v[192:195], v248 offset:22528\n"
    "v_mfma_f32_16x16x32_bf16 a[128:131], v[172:175], v[68:71], a[128:131]\n"
    "buffer_load_dwordx2 v[236:237], v241, s[36:39], s81 offen offset:0 sc1\n"
    "v_mfma_f32_16x16x32_bf16 a[128:131], v[176:179], v[72:75], a[128:131]\n"
    "ds_read_b128 v[196:199], v248 offset:11264\n"
    "v_mfma_f32_16x16x32_bf16 a[132:135], v[176:179], v[80:83], a[132:135]\n"
    "ds_read_b128 v[200:203], v248 offset:23552\n"
    "v_mfma_f32_16x16x32_bf16 a[132:135], v[172:175], v[76:79], a[132:135]\n"
    "v_exp_f32 v22, v22\n"
    "v_mfma_f32_16x16x32_bf16 a[116:119], v[164:167], v[76:79], a[116:119]\n"
    "v_exp_f32 v23, v23\n"
    "v_mfma_f32_16x16x32_bf16 a[116:119], v[168:171], v[80:83], a[116:119]\n"
    "v_exp_f32 v24, v24\n"
    "v_mfma_f32_16x16x32_bf16 a[100:103], v[160:163], v[80:83], a[100:103]\n"
    "v_exp_f32 v25, v25\n"
    "v_mfma_f32_16x16x32_bf16 a[100:103], v[156:159], v[76:79], a[100:103]\n"
    "v_exp_f32 v26, v26\n"
    "v_mfma_f32_16x16x32_bf16 a[104:107], v[156:159], v[84:87], a[104:107]\n"
    "v_mfma_f32_16x16x32_bf16 a[104:107], v[160:163], v[88:91], a[104:107]\n"
    "v_exp_f32 v27, v27\n"
    "v_mfma_f32_16x16x32_bf16 a[120:123], v[168:171], v[88:91], a[120:123]\n"
    "v_mfma_f32_16x16x32_bf16 a[120:123], v[164:167], v[84:87], a[120:123]\n"
    "v_mfma_f32_16x16x32_bf16 a[136:139], v[172:175], v[84:87], a[136:139]\n"
    "v_exp_f32 v28, v28\n"
    "v_mfma_f32_16x16x32_bf16 a[136:139], v[176:179], v[88:91], a[136:139]\n"
    "v_mfma_f32_16x16x32_bf16 a[140:143], v[176:179], v[96:99], a[140:143]\n"
    "v_mfma_f32_16x16x32_bf16 a[140:143], v[172:175], v[92:95], a[140:143]\n"
    "v_exp_f32 v29, v29\n"
    "v_mfma_f32_16x16x32_bf16 a[124:127], v[164:167], v[92:95], a[124:127]\n"
    "v_mfma_f32_16x16x32_bf16 a[124:127], v[168:171], v[96:99], a[124:127]\n"
    "v_exp_f32 v30, v30\n"
    "v_mfma_f32_16x16x32_bf16 a[108:111], v[160:163], v[96:99], a[108:111]\n"
    "v_mfma_f32_16x16x32_bf16 a[108:111], v[156:159], v[92:95], a[108:111]\n"
    "s_waitcnt lgkmcnt(0)\n"
    "v_mfma_f32_16x16x32_bf16 a[144:147], v[180:183], v[68:71], a[144:147]\n"
    "ds_read_b128 v[156:159], v3 offset:12288\n"
    "v_mfma_f32_16x16x32_bf16 a[144:147], v[184:187], v[72:75], a[144:147]\n"
    "ds_read_b128 v[160:163], v3 offset:14336\n"
    "v_mfma_f32_16x16x32_bf16 a[160:163], v[192:195], v[72:75], a[160:163]\n"
    "ds_read_b128 v[164:167], v3 offset:16384\n"
    "v_mfma_f32_16x16x32_bf16 a[160:163], v[188:191], v[68:71], a[160:163]\n"
    "ds_read_b128 v[168:171], v3 offset:18432\n"
    "v_mfma_f32_16x16x32_bf16 a[176:179], v[196:199], v[68:71], a[176:179]\n"
    "ds_read_b128 v[172:175], v3 offset:20480\n"
    "v_mfma_f32_16x16x32_bf16 a[176:179], v[200:203], v[72:75], a[176:179]\n"
    "ds_read_b128 v[176:179], v3 offset:22528\n"
    "v_mfma_f32_16x16x32_bf16 a[180:183], v[200:203], v[80:83], a[180:183]\n"
    "ds_read_b128 v[100:103], v250 offset:896\n"
    "v_mfma_f32_16x16x32_bf16 a[180:183], v[196:199], v[76:79], a[180:183]\n"
    "v_exp_f32 v31, v31\n"
    "v_mfma_f32_16x16x32_bf16 a[164:167], v[188:191], v[76:79], a[164:167]\n"
    "v_exp_f32 v32, v32\n"
    "v_mfma_f32_16x16x32_bf16 a[164:167], v[192:195], v[80:83], a[164:167]\n"
    "v_exp_f32 v33, v33\n"
    "v_mfma_f32_16x16x32_bf16 a[148:151], v[184:187], v[80:83], a[148:151]\n"
    "v_exp_f32 v34, v34\n"
    "v_mfma_f32_16x16x32_bf16 a[148:151], v[180:183], v[76:79], a[148:151]\n"
    "v_exp_f32_e64 v35, v35\n"
    "v_mfma_f32_16x16x32_bf16 a[152:155], v[180:183], v[84:87], a[152:155]\n"
    "v_mfma_f32_16x16x32_bf16 a[152:155], v[184:187], v[88:91], a[152:155]\n"
    "v_mfma_f32_16x16x32_bf16 a[168:171], v[192:195], v[88:91], a[168:171]\n"
    "s_waitcnt vmcnt(8)\n"
    "ds_add_u32 v247, v246\n"
    "v_mfma_f32_16x16x32_bf16 a[168:171], v[188:191], v[84:87], a[168:171]\n"
    "v_mfma_f32_16x16x32_bf16 a[184:187], v[196:199], v[84:87], a[184:187]\n"
    "v_mfma_f32_16x16x32_bf16 a[184:187], v[200:203], v[88:91], a[184:187]\n"
    "v_mfma_f32_16x16x32_bf16 a[188:191], v[200:203], v[96:99], a[188:191]\n"
    "v_mfma_f32_16x16x32_bf16 a[188:191], v[196:199], v[92:95], a[188:191]\n"
    "v_mfma_f32_16x16x32_bf16 a[172:175], v[188:191], v[92:95], a[172:175]\n"
    "s_sub_u32 s74, s68, s65\n"
    "v_mfma_f32_16x16x32_bf16 a[172:175], v[192:195], v[96:99], a[172:175]\n"
    "s_add_u32 s66, s66, .Lend-.Lend2+(1)\n"
    "v_mov_b32_e64 v2, v3\n"
    "v_add_u32_e64 v3, s74, v252\n"
    "v_mfma_f32_16x16x32_bf16 a[156:159], v[184:187], v[96:99], a[156:159]\n"
    "s_waitcnt lgkmcnt(1)\n"
    "s_cmp_eq_u32 s66, .Lend-.Lend2+(30)\n"
    "v_mfma_f32_16x16x32_bf16 a[156:159], v[180:183], v[92:95], a[156:159]\n"
    ".LwVe:\n"
    "s_cbranch_scc1 .LtailP\n"
    ".LNV:\n"
    "v_mfma_f32_16x16x32_bf16 v[36:39], v[156:159], a[192:195], v[100:103]\n"
    "ds_read_b128 v[180:183], v2 offset:13312\n"
    "v_cvt_pk_bf16_f32 v68, v4, v5\n"
    "v_mfma_f32_16x16x32_bf16 v[36:39], v[160:163], a[196:199], v[36:39]\n"
    "ds_read_b128 v[184:187], v2 offset:15360\n"
    "v_mfma_f32_16x16x32_bf16 v[36:39], v[164:167], a[200:203], v[36:39]\n"
    "ds_read_b128 v[188:191], v2 offset:17408\n"
    "v_mfma_f32_16x16x32_bf16 v[36:39], v[168:171], a[204:207], v[36:39]\n"
    "ds_read_b128 v[192:195], v2 offset:19456\n"
    "v_cvt_pk_bf16_f32 v69, v6, v7\n"
    "v_mfma_f32_16x16x32_bf16 v[36:39], v[172:175], a[208:211], v[36:39]\n"
    "ds_read_b128 v[196:199], v2 offset:21504\n"
    "v_mfma_f32_16x16x32_bf16 v[36:39], v[176:179], a[212:215], v[36:39]\n"
    "ds_read_b128 v[200:203], v2 offset:23552\n"
    "v_mfma_f32_16x16x32_bf16 v[40:43], v[176:179], a[236:239], v[100:103]\n"
    "ds_read_b128 v[104:107], v250 offset:912\n"
    "v_mfma_f32_16x16x32_bf16 v[40:43], v[172:175], a[232:235], v[40:43]\n"
    "s_add_u32 s68, s68, 24576\n"
    "v_cvt_pk_bf16_f32 v76, v8, v9\n"
    "v_mfma_f32_16x16x32_bf16 v[40:43], v[168:171], a[228:231], v[40:43]\n"
    "s_cmp_ge_u32 s68, s69\n"
    "s_cselect_b32 s68, s65, s68\n"
    "v_mfma_f32_16x16x32_bf16 v[40:43], v[164:167], a[224:227], v[40:43]\n"
    "v_exp_f32 v36, v36\n"
    "v_mfma_f32_16x16x32_bf16 v[40:43], v[160:163], a[220:223], v[40:43]\n"
    "v_exp_f32 v37, v37\n"
    "v_mfma_f32_16x16x32_bf16 v[40:43], v[156:159], a[216:219], v[40:43]\n"
    "v_cvt_pk_bf16_f32 v77, v10, v11\n"
    "v_cvt_pk_bf16_f32 v72, v36, v37\n"
    "v_mfma_f32_16x16x32_bf16 v[44:47], v[156:159], a[240:243], v[100:103]\n"
    "v_mfma_f32_16x16x32_bf16 v[44:47], v[160:163], a[244:247], v[44:47]\n"
    "v_exp_f32 v38, v38\n"
    "v_mfma_f32_16x16x32_bf16 v[44:47], v[164:167], a[248:251], v[44:47]\n"
    "v_mfma_f32_16x16x32_bf16 v[44:47], v[168:171], a[252:255], v[44:47]\n"
    "v_cvt_pk_bf16_f32 v84, v12, v13\n"
    "v_mfma_f32_16x16x32_bf16 v[44:47], v[172:175], v[124:127], v[44:47]\n"
    "v_exp_f32 v39, v39\n"
    "v_mfma_f32_16x16x32_bf16 v[44:47], v[176:179], v[128:131], v[44:47]\n"
    "v_cvt_pk_bf16_f32 v73, v38, v39\n"
    "v_mfma_f32_16x16x32_bf16 v[48:51], v[176:179], v[152:155], v[100:103]\n"
    "ds_read_b32 v245, v247\n"
    "v_cvt_pk_bf16_f32 v85, v14, v15\n"
    "v_mfma_f32_16x16x32_bf16 v[48:51], v[172:175], v[148:151], v[48:51]\n"
    "s_add_u32 m0, s68, 0\n"
    "v_exp_f32 v40, v40\n"
    "v_mfma_f32_16x16x32_bf16 v[48:51], v[168:171], v[144:147], v[48:51]\n"
    "v_mfma_f32_16x16x32_bf16 v[48:51], v[164:167], v[140:143], v[48:51]\n"
    "v_mfma_f32_16x16x32_bf16 v[48:51], v[160:163], v[136:139], v[48:51]\n"
    "s_waitcnt vmcnt(0)\n"
    "v_cmp_eq_u64 s[54:55], v[236:237], v[238:239]\n"
    "v_cvt_pk_bf16_f32 v92, v16, v17\n"
    "v_mfma_f32_16x16x32_bf16 v[48:51], v[156:159], v[132:135], v[48:51]\n"
    "s_waitcnt lgkmcnt(1)\n"
    "v_exp_f32 v41, v41\n"
    "v_mfma_f32_16x16x32_bf16 v[52:55], v[180:183], a[192:195], v[104:107]\n"
    "s_waitcnt lgkmcnt(0)\n"
    "v_cmp_le_u32 s[56:57], s59, v245\n"
    "v_cvt_pk_bf16_f32 v80, v40, v41\n"
    "v_mfma_f32_16x16x32_bf16 v[52:55], v[184:187], a[196:199], v[52:55]\n"
    "s_waitcnt vmcnt(1)\n"
    "v_exp_f32 v42, v42\n"
    "v_mfma_f32_16x16x32_bf16 v[52:55], v[188:191], a[200:203], v[52:55]\n"
    "s_and_b64 s[54:55], s[54:55], s[56:57]\n"
    "s_cmp_eq_u64 s[54:55], -1\n"
    "v_mfma_f32_16x16x32_bf16 v[52:55], v[192:195], a[204:207], v[52:55]\n"
    "s_cbranch_scc0 .Lpoll4\n"
    ".Lpoll4r:\n"
    "v_mfma_f32_16x16x32_bf16 v[52:55], v[196:199], a[208:211], v[52:55]\n"
    "buffer_load_dwordx4 v1, s[16:19], s67 offen offset:0 sc1 lds\n"
    "v_mfma_f32_16x16x32_bf16 v[52:55], v[200:203], a[212:215], v[52:55]\n"
    "s_add_u32 s76, s71, s60\n"
    "ds_read_b128 v[156:159], v3 offset:0\n"
    "v_cvt_pk_bf16_f32 v93, v18, v19\n"
    "v_mfma_f32_16x16x32_bf16 v[56:59], v[200:203], a[236:239], v[104:107]\n"
    "s_add_u32 s77, s71, s61\n"
    "ds_read_b128 v[160:163], v3 offset:2048\n"
    "v_cvt_pk_bf16_f32 v70, v20, v21\n"
    "v_mfma_f32_16x16x32_bf16 v[56:59], v[196:199], a[232:235], v[56:59]\n"
    "ds_read_b128 v[164:167], v3 offset:4096\n"
    "v_mfma_f32_16x16x32_bf16 v[56:59], v[192:195], a[228:231], v[56:59]\n"
    "buffer_load_dwordx4 v1, s[16:19], s67 offen offset:1024 sc1 lds\n"
    "v_mfma_f32_16x16x32_bf16 v[56:59], v[188:191], a[224:227], v[56:59]\n"
    "ds_read_b128 v[168:171], v3 offset:6144\n"
    "v_cvt_pk_bf16_f32 v71, v22, v23\n"
    "v_mfma_f32_16x16x32_bf16 v[56:59], v[184:187], a[220:223], v[56:59]\n"
    "s_add_u32 s78, s71, s62\n"
    "ds_read_b128 v[172:175], v3 offset:8192\n"
    "v_mfma_f32_16x16x32_bf16 v[56:59], v[180:183], a[216:219], v[56:59]\n"
    "ds_read_b128 v[176:179], v3 offset:10240\n"
    "v_mfma_f32_16x16x32_bf16 v[60:63], v[180:183], a[240:243], v[104:107]\n"
    "buffer_load_dwordx4 v1, s[16:19], s67 offen offset:2048 sc1 lds\n"
    "v_mfma_f32_16x16x32_bf16 v[60:63], v[184:187], a[244:247], v[60:63]\n"
    "ds_read_b128 v[100:103], v250 offset:1536\n"
    "v_cvt_pk_bf16_f32 v78, v24, v25\n"
    "v_mfma_f32_16x16x32_bf16 v[60:63], v[188:191], a[248:251], v[60:63]\n"
    "v_exp_f32 v43, v43\n"
    "v_mfma_f32_16x16x32_bf16 v[60:63], v[192:195], a[252:255], v[60:63]\n"
    "v_cvt_pk_bf16_f32 v81, v42, v43\n"
    "v_cvt_pk_u16_u32 v204, v204, v208\n"
    "v_mfma_f32_16x16x32_bf16 v[60:63], v[196:199], v[124:127], v[60:63]\n"
    "buffer_load_dwordx4 v1, s[16:19], s67 offen offset:3072 sc1 lds\n"
    "v_mfma_f32_16x16x32_bf16 v[60:63], v[200:203], v[128:131], v[60:63]\n"
    "s_add_u32 m0, s68, 4096\n"
    "v_cvt_pk_bf16_f32 v79, v26, v27\n"
    "v_mfma_f32_16x16x32_bf16 v[64:67], v[200:203], v[152:155], v[104:107]\n"
    "s_add_u32 s83, s67, 4096\n"
    "v_exp_f32 v44, v44\n"
    "v_mfma_f32_16x16x32_bf16 v[64:67], v[196:199], v[148:151], v[64:67]\n"
    "v_exp_f32 v45, v45\n"
    "v_mfma_f32_16x16x32_bf16 v[64:67], v[192:195], v[144:147], v[64:67]\n"
    "buffer_load_dwordx4 v1, s[16:19], s83 offen offset:0 sc1 lds\n"
    "v_mfma_f32_16x16x32_bf16 v[64:67], v[188:191], v[140:143], v[64:67]\n"
    "v_cvt_pk_bf16_f32 v86, v28, v29\n"
    "v_cvt_pk_bf16_f32 v88, v44, v45\n"
    "v_mfma_f32_16x16x32_bf16 v[64:67], v[184:187], v[136:139], v[64:67]\n"
    "v_exp_f32 v46, v46\n"
    "v_mfma_f32_16x16x32_bf16 v[64:67], v[180:183], v[132:135], v[64:67]\n"
    "s_waitcnt lgkmcnt(0)\n"
    "v_exp_f32 v47, v47\n"
    "v_mfma_f32_16x16x32_bf16 v[4:7], v[156:159], a[192:195], v[100:103]\n"
    "buffer_load_dwordx4 v1, s[16:19], s83 offen offset:1024 sc1 lds\n"
    "v_mfma_f32_16x16x32_bf16 v[4:7], v[160:163], a[196:199], v[4:7]\n"
    "s_add_u32 m0, s70, 0\n"
    "ds_read_b128 v[180:183], v3 offset:1024\n"
    "v_cvt_pk_bf16_f32 v87, v30, v31\n"
    "v_mfma_f32_16x16x32_bf16 v[4:7], v[164:167], a[200:203], v[4:7]\n"
    "s_add_u32 s83, s67, 24576\n"
    "ds_read_b128 v[184:187], v3 offset:3072\n"
    "v_cvt_pk_bf16_f32 v89, v46, v47\n"
    "v_mfma_f32_16x16x32_bf16 v[4:7], v[168:171], a[204:207], v[4:7]\n"
    "ds_read_b128 v[188:191], v3 offset:5120\n"
    "v_cvt_pk_u16_u32 v205, v205, v209\n"
    "v_mfma_f32_16x16x32_bf16 v[4:7], v[172:175], a[208:211], v[4:7]\n"
    "buffer_load_dwordx4 v1, s[16:19], s83 offen offset:0 sc1 lds\n"
    "v_mfma_f32_16x16x32_bf16 v[4:7], v[176:179], a[212:215], v[4:7]\n"
    "ds_read_b128 v[192:195], v3 offset:7168\n"
    "v_cvt_pk_bf16_f32 v94, v32, v33\n"
    "v_mfma_f32_16x16x32_bf16 v[8:11], v[176:179], a[236:239], v[100:103]\n"
    "ds_read_b128 v[196:199], v3 offset:9216\n"
    "v_cvt_pk_u16_u32 v206, v206, v210\n"
    "v_mfma_f32_16x16x32_bf16 v[8:11], v[172:175], a[232:235], v[8:11]\n"
    "ds_read_b128 v[200:203], v3 offset:11264\n"
    "v_cvt_pk_u16_u32 v207, v207, v211\n"
    "v_mfma_f32_16x16x32_bf16 v[8:11], v[168:171], a[228:231], v[8:11]\n"
    "buffer_load_dwordx4 v1, s[16:19], s83 offen offset:1024 sc1 lds\n"
    "v_mfma_f32_16x16x32_bf16 v[8:11], v[164:167], a[224:227], v[8:11]\n"
    "ds_read_b128 v[104:107], v250 offset:1552\n"
    "v_cvt_pk_bf16_f32 v95, v34, v35\n"
    "v_mfma_f32_16x16x32_bf16 v[8:11], v[160:163], a[220:223], v[8:11]\n"
    "v_exp_f32 v52, v52\n"
    "v_mfma_f32_16x16x32_bf16 v[8:11], v[156:159], a[216:219], v[8:11]\n"
    "v_exp_f32 v53, v53\n"
    "v_mfma_f32_16x16x32_bf16 v[12:15], v[156:159], a[240:243], v[100:103]\n"
    "buffer_load_dwordx4 v1, s[16:19], s83 offen offset:2048 sc1 lds\n"
    "v_mfma_f32_16x16x32_bf16 v[12:15], v[160:163], a[244:247], v[12:15]\n"
    "v_exp_f32 v54, v54\n"
    "v_mfma_f32_16x16x32_bf16 v[12:15], v[164:167], a[248:251], v[12:15]\n"
    "v_exp_f32 v55, v55\n"
    "v_mfma_f32_16x16x32_bf16 v[12:15], v[168:171], a[252:255], v[12:15]\n"
    "v_cvt_pk_bf16_f32 v74, v52, v53\n"
    "v_cvt_pk_bf16_f32 v75, v54, v55\n"
    "v_mfma_f32_16x16x32_bf16 v[12:15], v[172:175], v[124:127], v[12:15]\n"
    "buffer_load_dwordx4 v1, s[16:19], s83 offen offset:3072 sc1 lds\n"
    "v_mfma_f32_16x16x32_bf16 v[12:15], v[176:179], v[128:131], v[12:15]\n"
    "s_add_u32 m0, s70, 4096\n"
    "v_exp_f32 v56, v56\n"
    "v_mfma_f32_16x16x32_bf16 v[16:19], v[176:179], v[152:155], v[100:103]\n"
    "s_add_u32 s83, s67, 28672\n"
    "v_exp_f32 v48, v48\n"
    "v_mfma_f32_16x16x32_bf16 v[16:19], v[172:175], v[148:151], v[16:19]\n"
    "v_exp_f32 v49, v49\n"
    "v_mfma_f32_16x16x32_bf16 v[16:19], v[168:171], v[144:147], v[16:19]\n"
    "buffer_load_dwordx4 v1, s[16:19], s83 offen offset:0 sc1 lds\n"
    "v_mfma_f32_16x16x32_bf16 v[16:19], v[164:167], v[140:143], v[16:19]\n"
    "v_exp_f32 v57, v57\n"
    "v_mfma_f32_16x16x32_bf16 v[16:19], v[160:163], v[136:139], v[16:19]\n"
    "v_cvt_pk_bf16_f32 v82, v56, v57\n"
    "v_cvt_pk_bf16_f32 v96, v48, v49\n"
    "v_mfma_f32_16x16x32_bf16 v[16:19], v[156:159], v[132:135], v[16:19]\n"
    "s_waitcnt lgkmcnt(0)\n"
    "v_exp_f32 v58, v58\n"
    "v_mfma_f32_16x16x32_bf16 v[20:23], v[180:183], a[192:195], v[104:107]\n"
    "buffer_load_dwordx4 v1, s[16:19], s83 offen offset:1024 sc1 lds\n"
    "v_mfma_f32_16x16x32_bf16 v[20:23], v[184:187], a[196:199], v[20:23]\n"
    "s_add_u32 s67, s67, 49152\n"
    "ds_read_b128 v[156:159], v249 offset:0\n"
    "v_or3_b32 v243, v204, v205, v243\n"
    "v_mfma_f32_16x16x32_bf16 v[20:23], v[188:191], a[200:203], v[20:23]\n"
    "s_add_u32 s59, s59, 4\n"
    "ds_read_b128 v[160:163], v249 offset:12288\n"
    "v_or3_b32 v243, v206, v207, v243\n"
    "v_mfma_f32_16x16x32_bf16 v[20:23], v[192:195], a[204:207], v[20:23]\n"
    "ds_read_b128 v[164:167], v249 offset:1024\n"
    "v_cvt_pk_u16_u32 v212, v212, v216\n"
    "v_mfma_f32_16x16x32_bf16 v[20:23], v[196:199], a[208:211], v[20:23]\n"
    "ds_read_b128 v[168:171], v249 offset:13312\n"
    "v_cvt_pk_u16_u32 v213, v213, v217\n"
    "v_mfma_f32_16x16x32_bf16 v[20:23], v[200:203], a[212:215], v[20:23]\n"
    "ds_read_b128 v[172:175], v249 offset:2048\n"
    "v_mfma_f32_16x16x32_bf16 v[24:27], v[200:203], a[236:239], v[104:107]\n"
    "ds_read_b128 v[176:179], v249 offset:14336\n"
    "v_mfma_f32_16x16x32_bf16 v[24:27], v[196:199], a[232:235], v[24:27]\n"
    "v_exp_f32 v59, v59\n"
    "v_mfma_f32_16x16x32_bf16 v[24:27], v[192:195], a[228:231], v[24:27]\n"
    "v_cvt_pk_bf16_f32 v83, v58, v59\n"
    "v_cvt_pk_u16_u32 v214, v214, v218\n"
    "v_mfma_f32_16x16x32_bf16 v[24:27], v[188:191], a[224:227], v[24:27]\n"
    "v_exp_f32 v60, v60\n"
    "v_mfma_f32_16x16x32_bf16 v[24:27], v[184:187], a[220:223], v[24:27]\n"
    "v_exp_f32 v61, v61\n"
    "v_mfma_f32_16x16x32_bf16 v[24:27], v[180:183], a[216:219], v[24:27]\n"
    "v_exp_f32 v62, v62\n"
    "v_mfma_f32_16x16x32_bf16 v[28:31], v[180:183], a[240:243], v[104:107]\n"
    "v_cvt_pk_bf16_f32 v90, v60, v61\n"
    "v_mfma_f32_16x16x32_bf16 v[28:31], v[184:187], a[244:247], v[28:31]\n"
    "v_exp_f32 v63, v63\n"
    "v_mfma_f32_16x16x32_bf16 v[28:31], v[188:191], a[248:251], v[28:31]\n"
    "v_cvt_pk_bf16_f32 v91, v62, v63\n"
    "v_cvt_pk_u16_u32 v215, v215, v219\n"
    "v_mfma_f32_16x16x32_bf16 v[28:31], v[192:195], a[252:255], v[28:31]\n"
    "v_exp_f32 v50, v50\n"
    "v_mfma_f32_16x16x32_bf16 v[28:31], v[196:199], v[124:127], v[28:31]\n"
    "v_exp_f32 v51, v51\n"
    "v_mfma_f32_16x16x32_bf16 v[28:31], v[200:203], v[128:131], v[28:31]\n"
    "v_exp_f32 v64, v64\n"
    "v_mfma_f32_16x16x32_bf16 v[32:35], v[200:203], v[152:155], v[104:107]\n"
    "v_cvt_pk_bf16_f32 v97, v50, v51\n"
    "v_mfma_f32_16x16x32_bf16 v[32:35], v[196:199], v[148:151], v[32:35]\n"
    "v_exp_f32 v65, v65\n"
    "v_mfma_f32_16x16x32_bf16 v[32:35], v[192:195], v[144:147], v[32:35]\n"
    "v_cvt_pk_bf16_f32 v98, v64, v65\n"
    "v_or3_b32 v243, v212, v213, v243\n"
    "v_mfma_f32_16x16x32_bf16 v[32:35], v[188:191], v[140:143], v[32:35]\n"
    "v_mfma_f32_16x16x32_bf16 v[32:35], v[184:187], v[136:139], v[32:35]\n"
    "v_exp_f32 v66, v66\n"
    "v_mfma_f32_16x16x32_bf16 v[32:35], v[180:183], v[132:135], v[32:35]\n"
    "s_waitcnt lgkmcnt(0)\n"
    "v_mfma_f32_16x16x32_bf16 a[0:3], v[156:159], v[68:71], a[0:3]\n"
    "ds_read_b128 v[180:183], v249 offset:3072\n"
    "v_or3_b32 v243, v214, v215, v243\n"
    "v_mfma_f32_16x16x32_bf16 a[0:3], v[160:163], v[72:75], a[0:3]\n"
    "ds_read_b128 v[184:187], v249 offset:15360\n"
    "v_mfma_f32_16x16x32_bf16 a[16:19], v[168:171], v[72:75], a[16:19]\n"
    "ds_read_b128 v[188:191], v249 offset:4096\n"
    "v_mfma_f32_16x16x32_bf16 a[16:19], v[164:167], v[68:71], a[16:19]\n"
    "ds_read_b128 v[192:195], v249 offset:16384\n"
    "v_mfma_f32_16x16x32_bf16 a[32:35], v[172:175], v[68:71], a[32:35]\n"
    "ds_read_b128 v[196:199], v249 offset:5120\n"
    "v_mfma_f32_16x16x32_bf16 a[32:35], v[176:179], v[72:75], a[32:35]\n"
    "ds_read_b128 v[200:203], v249 offset:17408\n"
    "v_cvt_pk_u16_u32 v220, v220, v224\n"
    "v_mfma_f32_16x16x32_bf16 a[36:39], v[176:179], v[80:83], a[36:39]\n"
    "ds_read_b128 v[104:107], v251 offset:768\n"
    "v_mfma_f32_16x16x32_bf16 a[36:39], v[172:175], v[76:79], a[36:39]\n"
    "v_exp_f32 v67, v67\n"
    "v_mfma_f32_16x16x32_bf16 a[20:23], v[164:167], v[76:79], a[20:23]\n"
    "v_cvt_pk_bf16_f32 v99, v66, v67\n"
    "v_mfma_f32_16x16x32_bf16 a[20:23], v[168:171], v[80:83], a[20:23]\n"
    "v_exp_f32 v4, v4\n"
    "v_mfma_f32_16x16x32_bf16 a[4:7], v[160:163], v[80:83], a[4:7]\n"
    "v_exp_f32 v5, v5\n"
    "v_mfma_f32_16x16x32_bf16 a[4:7], v[156:159], v[76:79], a[4:7]\n"
    "v_exp_f32 v6, v6\n"
    "v_mfma_f32_16x16x32_bf16 a[8:11], v[156:159], v[84:87], a[8:11]\n"
    "v_exp_f32 v7, v7\n"
    "v_mfma_f32_16x16x32_bf16 a[8:11], v[160:163], v[88:91], a[8:11]\n"
    "v_cvt_pk_u16_u32 v221, v221, v225\n"
    "v_mfma_f32_16x16x32_bf16 a[24:27], v[168:171], v[88:91], a[24:27]\n"
    "v_exp_f32 v8, v8\n"
    "v_mfma_f32_16x16x32_bf16 a[24:27], v[164:167], v[84:87], a[24:27]\n"
    "v_cvt_pk_u16_u32 v222, v222, v226\n"
    "v_mfma_f32_16x16x32_bf16 a[40:43], v[172:175], v[84:87], a[40:43]\n"
    "v_mfma_f32_16x16x32_bf16 a[40:43], v[176:179], v[88:91], a[40:43]\n"
    "v_exp_f32 v9, v9\n"
    "v_mfma_f32_16x16x32_bf16 a[44:47], v[176:179], v[96:99], a[44:47]\n"
    "v_mfma_f32_16x16x32_bf16 a[44:47], v[172:175], v[92:95], a[44:47]\n"
    "v_mfma_f32_16x16x32_bf16 a[28:31], v[164:167], v[92:95], a[28:31]\n"
    "v_exp_f32 v10, v10\n"
    "v_mfma_f32_16x16x32_bf16 a[28:31], v[168:171], v[96:99], a[28:31]\n"
    "v_cvt_pk_u16_u32 v223, v223, v227\n"
    "v_mfma_f32_16x16x32_bf16 a[12:15], v[160:163], v[96:99], a[12:15]\n"
    "v_exp_f32 v11, v11\n"
    "v_mfma_f32_16x16x32_bf16 a[12:15], v[156:159], v[92:95], a[12:15]\n"
    "s_waitcnt lgkmcnt(0)\n"
    "v_mfma_f32_16x16x32_bf16 a[48:51], v[180:183], v[68:71], a[48:51]\n"
    "ds_read_b128 v[156:159], v249 offset:6144\n"
    "v_mfma_f32_16x16x32_bf16 a[48:51], v[184:187], v[72:75], a[48:51]\n"
    "ds_read_b128 v[160:163], v249 offset:18432\n"
    "v_mfma_f32_16x16x32_bf16 a[64:67], v[192:195], v[72:75], a[64:67]\n"
    "ds_read_b128 v[164:167], v249 offset:7168\n"
    "v_or3_b32 v243, v220, v221, v243\n"
    "v_mfma_f32_16x16x32_bf16 a[64:67], v[188:191], v[68:71], a[64:67]\n"
    "ds_read_b128 v[168:171], v249 offset:19456\n"
    "v_mfma_f32_16x16x32_bf16 a[80:83], v[196:199], v[68:71], a[80:83]\n"
    "ds_read_b128 v[172:175], v249 offset:8192\n"
    "v_mfma_f32_16x16x32_bf16 a[80:83], v[200:203], v[72:75], a[80:83]\n"
    "ds_read_b128 v[176:179], v249 offset:20480\n"
    "v_smfmac_f32_16x16x64_bf16 v[108:111], v[104:107], v[68:75], v253\n"
    "s_add_u32 s81, s81, 128\n"
    "s_cmp_gt_u32 s81, 4096\n"
    "s_cselect_b32 s81, 0, s81\n"
    "v_smfmac_f32_16x16x64_bf16 v[112:115], v[104:107], v[76:83], v253\n"
    "v_mfma_f32_16x16x32_bf16 a[84:87], v[200:203], v[80:83], a[84:87]\n"
    "v_exp_f32 v12, v12\n"
    "v_mfma_f32_16x16x32_bf16 a[84:87], v[196:199], v[76:79], a[84:87]\n"
    "v_exp_f32 v13, v13\n"
    "v_mfma_f32_16x16x32_bf16 a[68:71], v[188:191], v[76:79], a[68:71]\n"
    "v_exp_f32 v14, v14\n"
    "v_mfma_f32_16x16x32_bf16 a[68:71], v[192:195], v[80:83], a[68:71]\n"
    "v_exp_f32 v15, v15\n"
    "v_mfma_f32_16x16x32_bf16 a[52:55], v[184:187], v[80:83], a[52:55]\n"
    "v_or3_b32 v243, v222, v223, v243\n"
    "v_and_b32 v208, 0xff00ff00, v243\n"
    "v_mfma_f32_16x16x32_bf16 a[52:55], v[180:183], v[76:79], a[52:55]\n"
    "v_exp_f32 v16, v16\n"
    "v_mfma_f32_16x16x32_bf16 a[56:59], v[180:183], v[84:87], a[56:59]\n"
    "v_exp_f32 v17, v17\n"
    "v_mfma_f32_16x16x32_bf16 a[56:59], v[184:187], v[88:91], a[56:59]\n"
    "v_mfma_f32_16x16x32_bf16 a[72:75], v[192:195], v[88:91], a[72:75]\n"
    "v_mfma_f32_16x16x32_bf16 a[72:75], v[188:191], v[84:87], a[72:75]\n"
    "v_exp_f32 v18, v18\n"
    "v_mfma_f32_16x16x32_bf16 a[88:91], v[196:199], v[84:87], a[88:91]\n"
    "v_cmp_ne_u32 vcc, 0, v208\n"
    "v_mfma_f32_16x16x32_bf16 a[88:91], v[200:203], v[88:91], a[88:91]\n"
    "v_smfmac_f32_16x16x64_bf16 v[116:119], v[104:107], v[84:91], v253\n"
    "v_smfmac_f32_16x16x64_bf16 v[120:123], v[104:107], v[92:99], v253\n"
    "v_mfma_f32_16x16x32_bf16 a[92:95], v[200:203], v[96:99], a[92:95]\n"
    "v_exp_f32 v19, v19\n"
    "v_mfma_f32_16x16x32_bf16 a[92:95], v[196:199], v[92:95], a[92:95]\n"
    "v_exp_f32 v20, v20\n"
    "v_mfma_f32_16x16x32_bf16 a[76:79], v[188:191], v[92:95], a[76:79]\n"
    "v_cndmask_b32 v208, 2.0, v241, vcc\n"
    "v_mfma_f32_16x16x32_bf16 a[76:79], v[192:195], v[96:99], a[76:79]\n"
    "v_exp_f32 v21, v21\n"
    "v_mfma_f32_16x16x32_bf16 a[60:63], v[184:187], v[96:99], a[60:63]\n"
    "v_mfma_f32_16x16x32_bf16 a[60:63], v[180:183], v[92:95], a[60:63]\n"
    "s_waitcnt lgkmcnt(0)\n"
    "v_mfma_f32_16x16x32_bf16 a[96:99], v[156:159], v[68:71], a[96:99]\n"
    "ds_read_b128 v[180:183], v249 offset:9216\n"
    "v_mfma_f32_16x16x32_bf16 a[96:99], v[160:163], v[72:75], a[96:99]\n"
    "ds_read_b128 v[184:187], v249 offset:21504\n"
    "v_mfma_f32_16x16x32_bf16 a[112:115], v[168:171], v[72:75], a[112:115]\n"
    "ds_read_b128 v[188:191], v249 offset:10240\n"
    "v_mfma_f32_16x16x32_bf16 a[112:115], v[164:167], v[68:71], a[112:115]\n"
    "ds_read_b128 v[192:195], v249 offset:22528\n"
    "v_mfma_f32_16x16x32_bf16 a[128:131], v[172:175], v[68:71], a[128:131]\n"
    "buffer_load_dwordx2 v[236:237], v241, s[36:39], s81 offen offset:0 sc1\n"
    "v_mfma_f32_16x16x32_bf16 a[128:131], v[176:179], v[72:75], a[128:131]\n"
    "ds_read_b128 v[196:199], v249 offset:11264\n"
    "v_mfma_f32_16x16x32_bf16 a[132:135], v[176:179], v[80:83], a[132:135]\n"
    "ds_read_b128 v[200:203], v249 offset:23552\n"
    "v_mfma_f32_16x16x32_bf16 a[132:135], v[172:175], v[76:79], a[132:135]\n"
    "buffer_store_dword v204, v244, s[16:19], s76 offen offset:0 sc1\n"
    "v_mfma_f32_16x16x32_bf16 a[116:119], v[164:167], v[76:79], a[116:119]\n"
    "v_exp_f32 v22, v22\n"
    "v_mfma_f32_16x16x32_bf16 a[116:119], v[168:171], v[80:83], a[116:119]\n"
    "buffer_store_dword v205, v244, s[16:19], s76 offen offset:1024 sc1\n"
    "v_mfma_f32_16x16x32_bf16 a[100:103], v[160:163], v[80:83], a[100:103]\n"
    "v_exp_f32 v23, v23\n"
    "v_mfma_f32_16x16x32_bf16 a[100:103], v[156:159], v[76:79], a[100:103]\n"
    "buffer_store_dword v206, v244, s[16:19], s76 offen offset:2048 sc1\n"
    "v_mfma_f32_16x16x32_bf16 a[104:107], v[156:159], v[84:87], a[104:107]\n"
    "v_exp_f32 v24, v24\n"
    "v_mfma_f32_16x16x32_bf16 a[104:107], v[160:163], v[88:91], a[104:107]\n"
    "buffer_store_dword v207, v244, s[16:19], s76 offen offset:3072 sc1\n"
    "v_mfma_f32_16x16x32_bf16 a[120:123], v[168:171], v[88:91], a[120:123]\n"
    "v_exp_f32 v25, v25\n"
    "v_mfma_f32_16x16x32_bf16 a[120:123], v[164:167], v[84:87], a[120:123]\n"
    "buffer_store_dword v212, v244, s[16:19], s77 offen offset:0 sc1\n"
    "v_mfma_f32_16x16x32_bf16 a[136:139], v[172:175], v[84:87], a[136:139]\n"
    "v_exp_f32 v26, v26\n"
    "v_mfma_f32_16x16x32_bf16 a[136:139], v[176:179], v[88:91], a[136:139]\n"
    "buffer_store_dword v213, v244, s[16:19], s77 offen offset:1024 sc1\n"
    "v_mfma_f32_16x16x32_bf16 a[140:143], v[176:179], v[96:99], a[140:143]\n"
    "v_exp_f32 v27, v27\n"
    "v_mfma_f32_16x16x32_bf16 a[140:143], v[172:175], v[92:95], a[140:143]\n"
    "buffer_store_dword v214, v244, s[16:19], s77 offen offset:2048 sc1\n"
    "v_mfma_f32_16x16x32_bf16 a[124:127], v[164:167], v[92:95], a[124:127]\n"
    "v_exp_f32 v28, v28\n"
    "v_mfma_f32_16x16x32_bf16 a[124:127], v[168:171], v[96:99], a[124:127]\n"
    "buffer_store_dword v215, v244, s[16:19], s77 offen offset:3072 sc1\n"
    "v_mfma_f32_16x16x32_bf16 a[108:111], v[160:163], v[96:99], a[108:111]\n"
    "v_exp_f32 v29, v29\n"
    "v_mfma_f32_16x16x32_bf16 a[108:111], v[156:159], v[92:95], a[108:111]\n"
    "s_waitcnt lgkmcnt(0)\n"
    "v_exp_f32 v30, v30\n"
    "v_mfma_f32_16x16x32_bf16 a[144:147], v[180:183], v[68:71], a[144:147]\n"
    "ds_read_b128 v[156:159], v3 offset:12288\n"
    "v_mfma_f32_16x16x32_bf16 a[144:147], v[184:187], v[72:75], a[144:147]\n"
    "ds_read_b128 v[160:163], v3 offset:14336\n"
    "v_mfma_f32_16x16x32_bf16 a[160:163], v[192:195], v[72:75], a[160:163]\n"
    "ds_read_b128 v[164:167], v3 offset:16384\n"
    "v_mfma_f32_16x16x32_bf16 a[160:163], v[188:191], v[68:71], a[160:163]\n"
    "ds_read_b128 v[168:171], v3 offset:18432\n"
    "v_mfma_f32_16x16x32_bf16 a[176:179], v[196:199], v[68:71], a[176:179]\n"
    "ds_read_b128 v[172:175], v3 offset:20480\n"
    "v_mfma_f32_16x16x32_bf16 a[176:179], v[200:203], v[72:75], a[176:179]\n"
    "ds_read_b128 v[176:179], v3 offset:22528\n"
    "v_mfma_f32_16x16x32_bf16 a[180:183], v[200:203], v[80:83], a[180:183]\n"
    "ds_read_b128 v[100:103], v250 offset:1664\n"
    "v_mfma_f32_16x16x32_bf16 a[180:183], v[196:199], v[76:79], a[180:183]\n"
    "buffer_store_dword v220, v244, s[16:19], s78 offen offset:0 sc1\n"
    "v_mfma_f32_16x16x32_bf16 a[164:167], v[188:191], v[76:79], a[164:167]\n"
    "v_exp_f32 v31, v31\n"
    "v_mfma_f32_16x16x32_bf16 a[164:167], v[192:195], v[80:83], a[164:167]\n"
    "buffer_store_dword v221, v244, s[16:19], s78 offen offset:1024 sc1\n"
    "v_mfma_f32_16x16x32_bf16 a[148:151], v[184:187], v[80:83], a[148:151]\n"
    "v_exp_f32 v32, v32\n"
    "v_mfma_f32_16x16x32_bf16 a[148:151], v[180:183], v[76:79], a[148:151]\n"
    "buffer_store_dword v222, v244, s[16:19], s78 offen offset:2048 sc1\n"
    "v_mfma_f32_16x16x32_bf16 a[152:155], v[180:183], v[84:87], a[152:155]\n"
    "v_exp_f32 v33, v33\n"
    "v_mfma_f32_16x16x32_bf16 a[152:155], v[184:187], v[88:91], a[152:155]\n"
    "buffer_store_dword v223, v244, s[16:19], s78 offen offset:3072 sc1\n"
    "v_mfma_f32_16x16x32_bf16 a[168:171], v[192:195], v[88:91], a[168:171]\n"
    "s_waitcnt vmcnt(13)\n"
    "ds_add_u32 v247, v246\n"
    "v_mfma_f32_16x16x32_bf16 a[168:171], v[188:191], v[84:87], a[168:171]\n"
    "buffer_store_dwordx2 v[238:239], v208, s[36:39], 0 offen offset:0 sc1\n"
    "v_mfma_f32_16x16x32_bf16 a[184:187], v[196:199], v[84:87], a[184:187]\n"
    "v_exp_f32 v34, v34\n"
    "v_mfma_f32_16x16x32_bf16 a[184:187], v[200:203], v[88:91], a[184:187]\n"
    "v_exp_f32 v35, v35\n"
    "v_mfma_f32_16x16x32_bf16 a[188:191], v[200:203], v[96:99], a[188:191]\n"
    "v_mfma_f32_16x16x32_bf16 a[188:191], v[196:199], v[92:95], a[188:191]\n"
    "v_mfma_f32_16x16x32_bf16 a[172:175], v[188:191], v[92:95], a[172:175]\n"
    "s_sub_u32 s74, s68, s65\n"
    "v_add_u32 v250, 1536, v250\n"
    "v_add_u32 v251, 1536, v251\n"
    "v_mfma_f32_16x16x32_bf16 a[172:175], v[192:195], v[96:99], a[172:175]\n"
    "s_add_u32 s66, s66, 1\n"
    "v_mov_b32 v2, v3\n"
    "v_add_u32 v3, s74, v252\n"
    "v_mfma_f32_16x16x32_bf16 a[156:159], v[184:187], v[96:99], a[156:159]\n"
    "s_waitcnt lgkmcnt(1)\n"
    "s_cmp_lt_u32 s66, 30\n"
    "v_mfma_f32_16x16x32_bf16 a[156:159], v[180:183], v[92:95], a[156:159]\n"
    "s_cbranch_scc1 .LPV\n"
    "s_branch .Ltail\n"
    "s_nop 0\n"
    "s_nop 0\n"
    "s_nop 0\n"
    "s_nop 0\n"
    "s_nop 0\n"
    "s_nop 0\n"
    ".LtailP:\n"
    "s_cbranch_execz .Lw2\n"
    "v_swap_b32 v248, v249\n"
    "s_mov_b32 s64, s70\n"
    "v_add_u32 v250, 768, v250\n"
    "v_add_u32 v251, 768, v251\n"
    ".Ltail:\n"
    "v_mfma_f32_16x16x32_bf16 v[36:39], v[156:159], a[192:195], v[100:103]\n"
    "v_cvt_pk_bf16_f32 v79, v26, v27\n"
    "v_cvt_pk_bf16_f32 v87, v30, v31\n"
    "v_mfma_f32_16x16x32_bf16 v[40:43], v[176:179], a[236:239], v[100:103]\n"
    "ds_read_b128 v[104:107], v250 offset:144\n"
    "v_cvt_pk_bf16_f32 v69, v6, v7\n"
    "v_mfma_f32_16x16x32_bf16 v[44:47], v[156:159], a[240:243], v[100:103]\n"
    "v_cvt_pk_bf16_f32 v92, v16, v17\n"
    "v_cvt_pk_bf16_f32 v93, v18, v19\n"
    "v_mfma_f32_16x16x32_bf16 v[40:43], v[172:175], a[232:235], v[40:43]\n"
    "v_cvt_pk_bf16_f32 v71, v22, v23\n"
    "v_and_b32 v252, 0xf0, v1\n"
    "v_mfma_f32_16x16x32_bf16 v[44:47], v[160:163], a[244:247], v[44:47]\n"
    "v_cvt_pk_bf16_f32 v76, v8, v9\n"
    "v_cvt_pk_bf16_f32 v77, v10, v11\n"
    "v_mfma_f32_16x16x32_bf16 v[44:47], v[164:167], a[248:251], v[44:47]\n"
    "ds_read_b128 v[192:195], v2 offset:19456\n"
    "v_mfma_f32_16x16x32_bf16 v[44:47], v[168:171], a[252:255], v[44:47]\n"
    "ds_read_b128 v[184:187], v2 offset:15360\n"
    "v_cvt_pk_bf16_f32 v84, v12, v13\n"
    "v_mfma_f32_16x16x32_bf16 v[40:43], v[168:171], a[228:231], v[40:43]\n"
    "v_cvt_pk_bf16_f32 v85, v14, v15\n"
    "v_lshlrev_b32 v244, 16, v1\n"
    "v_mfma_f32_16x16x32_bf16 v[40:43], v[164:167], a[224:227], v[40:43]\n"
    "ds_read_b128 v[188:191], v2 offset:17408\n"
    "v_cvt_pk_bf16_f32 v94, v32, v33\n"
    "v_mfma_f32_16x16x32_bf16 v[44:47], v[172:175], v[124:127], v[44:47]\n"
    "ds_read_b128 v[180:183], v2 offset:13312\n"
    "v_cvt_pk_bf16_f32 v78, v24, v25\n"
    "v_mfma_f32_16x16x32_bf16 v[44:47], v[176:179], v[128:131], v[44:47]\n"
    "ds_read_b128 v[200:203], v2 offset:23552\n"
    "v_cvt_pk_bf16_f32 v95, v34, v35\n"
    "v_mfma_f32_16x16x32_bf16 v[48:51], v[176:179], v[152:155], v[100:103]\n"
    "s_mul_i32 s75, s84, 24576\n"
    "v_cvt_pk_bf16_f32 v70, v20, v21\n"
    "v_cvt_pk_bf16_f32 v68, v4, v5\n"
    "v_mfma_f32_16x16x32_bf16 v[40:43], v[160:163], a[220:223], v[40:43]\n"
    "s_nop 0\n"
    "v_exp_f32 v46, v46\n"
    "v_mfma_f32_16x16x32_bf16 v[40:43], v[156:159], a[216:219], v[40:43]\n"
    "s_add_u32 s76, s75, 6144\n"
    "ds_read_b128 v[196:199], v2 offset:21504\n"
    "ds_read_b128 v[100:103], v250 offset:768\n"
    "v_mfma_f32_16x16x32_bf16 v[36:39], v[160:163], a[196:199], v[36:39]\n"
    "v_and_b32 v2, 0x70, v1\n"
    "v_cvt_pk_bf16_f32 v86, v28, v29\n"
    "v_mfma_f32_16x16x32_bf16 v[48:51], v[172:175], v[148:151], v[48:51]\n"
    "s_nop 0\n"
    "v_exp_f32 v40, v40\n"
    "v_mfma_f32_16x16x32_bf16 v[36:39], v[164:167], a[200:203], v[36:39]\n"
    "v_exp_f32 v44, v44\n"
    "v_mfma_f32_16x16x32_bf16 v[48:51], v[168:171], v[144:147], v[48:51]\n"
    "v_exp_f32 v47, v47\n"
    "v_mfma_f32_16x16x32_bf16 v[36:39], v[168:171], a[204:207], v[36:39]\n"
    "s_add_u32 s77, s76, 6144\n"
    "ds_read_b32 v245, v247\n"
    "v_mfma_f32_16x16x32_bf16 v[48:51], v[164:167], v[140:143], v[48:51]\n"
    "s_waitcnt lgkmcnt(3)\n"
    "v_exp_f32 v42, v42\n"
    "v_mfma_f32_16x16x32_bf16 v[56:59], v[200:203], a[236:239], v[104:107]\n"
    "s_add_u32 s78, s77, 6144\n"
    "v_exp_f32 v41, v41\n"
    "v_mfma_f32_16x16x32_bf16 v[60:63], v[180:183], a[240:243], v[104:107]\n"
    "s_add_u32 s72, s65, 122880\n"
    "v_exp_f32 v45, v45\n"
    "v_mfma_f32_16x16x32_bf16 v[48:51], v[160:163], v[136:139], v[48:51]\n"
    "v_exp_f32 v43, v43\n"
    "v_mfma_f32_16x16x32_bf16 v[36:39], v[172:175], a[208:211], v[36:39]\n"
    "s_waitcnt vmcnt(0)\n"
    "v_lshl_add_u32 v252, v252, 3, s72\n"
    "v_lshrrev_b32 v224, 4, v1\n"
    "v_mfma_f32_16x16x32_bf16 v[36:39], v[176:179], a[212:215], v[36:39]\n"
    "v_cvt_pk_bf16_f32 v80, v40, v41\n"
    "v_and_b32 v224, 48, v224\n"
    "v_mfma_f32_16x16x32_bf16 v[64:67], v[200:203], v[152:155], v[104:107]\n"
    "v_cmp_eq_u64 s[54:55], v[236:237], v[238:239]\n"
    "v_xor_b32 v224, v224, v2\n"
    "v_mfma_f32_16x16x32_bf16 v[48:51], v[156:159], v[132:135], v[48:51]\n"
    "s_waitcnt lgkmcnt(0)\n"
    "v_cvt_pk_bf16_f32 v89, v46, v47\n"
    "v_cmp_le_u32 s[56:57], s59, v245\n"
    "v_mfma_f32_16x16x32_bf16 v[52:55], v[180:183], a[192:195], v[104:107]\n"
    "s_and_b64 s[54:55], s[54:55], s[56:57]\n"
    "v_exp_f32 v36, v36\n"
    "v_mfma_f32_16x16x32_bf16 v[56:59], v[196:199], a[232:235], v[56:59]\n"
    "v_cvt_pk_bf16_f32 v88, v44, v45\n"
    "v_add_u32 v252, v252, v224\n"
    "v_mfma_f32_16x16x32_bf16 v[52:55], v[184:187], a[196:199], v[52:55]\n"
    "s_cmp_eq_u64 s[54:55], -1\n"
    "v_exp_f32 v48, v48\n"
    "v_mfma_f32_16x16x32_bf16 v[60:63], v[184:187], a[244:247], v[60:63]\n"
    "s_cbranch_scc0 .Lpt0\n"
    ".Lpt0r:\n"
    "v_mfma_f32_16x16x32_bf16 v[52:55], v[188:191], a[200:203], v[52:55]\n"
    "s_add_u32 s59, s59, 4\n"
    "ds_read_b128 v[168:171], v3 offset:6144\n"
    "ds_read_b128 v[160:163], v3 offset:2048\n"
    "v_mfma_f32_16x16x32_bf16 v[64:67], v[196:199], v[148:151], v[64:67]\n"
    "ds_read_b128 v[172:175], v3 offset:8192\n"
    "ds_read_b128 v[176:179], v3 offset:10240\n"
    "v_mfma_f32_16x16x32_bf16 v[52:55], v[192:195], a[204:207], v[52:55]\n"
    "s_add_u32 m0, s64, 0\n"
    "ds_read_b128 v[164:167], v3 offset:4096\n"
    "ds_read_b128 v[156:159], v3 offset:0\n"
    "v_mfma_f32_16x16x32_bf16 v[52:55], v[196:199], a[208:211], v[52:55]\n"
    "v_exp_f32 v38, v38\n"
    "v_mfma_f32_16x16x32_bf16 v[60:63], v[188:191], a[248:251], v[60:63]\n"
    "v_exp_f32 v39, v39\n"
    "v_mfma_f32_16x16x32_bf16 v[52:55], v[200:203], a[212:215], v[52:55]\n"
    "s_add_u32 s83, s67, 24576\n"
    "buffer_load_dwordx4 v1, s[16:19], s83 offen offset:0 sc1 lds\n"
    "v_mfma_f32_16x16x32_bf16 v[60:63], v[192:195], a[252:255], v[60:63]\n"
    "v_cvt_pk_bf16_f32 v73, v38, v39\n"
    "v_lshrrev_b32 v224, 3, v1\n"
    "v_mfma_f32_16x16x32_bf16 v[56:59], v[192:195], a[228:231], v[56:59]\n"
    "buffer_load_dwordx4 v1, s[16:19], s83 offen offset:1024 sc1 lds\n"
    "v_mfma_f32_16x16x32_bf16 v[64:67], v[192:195], v[144:147], v[64:67]\n"
    "v_exp_f32 v49, v49\n"
    "v_mfma_f32_16x16x32_bf16 v[56:59], v[188:191], a[224:227], v[56:59]\n"
    "buffer_load_dwordx4 v1, s[16:19], s83 offen offset:2048 sc1 lds\n"
    "v_mfma_f32_16x16x32_bf16 v[60:63], v[196:199], v[124:127], v[60:63]\n"
    "v_exp_f32 v51, v51\n"
    "v_mfma_f32_16x16x32_bf16 v[64:67], v[188:191], v[140:143], v[64:67]\n"
    "s_waitcnt lgkmcnt(2)\n"
    "buffer_load_dwordx4 v1, s[16:19], s83 offen offset:3072 sc1 lds\n"
    "v_mfma_f32_16x16x32_bf16 v[8:11], v[176:179], a[236:239], v[100:103]\n"
    "s_add_u32 m0, s64, 4096\n"
    "v_exp_f32_e64 v52, v52\n"
    "v_mfma_f32_16x16x32_bf16 v[56:59], v[184:187], a[220:223], v[56:59]\n"
    "s_add_u32 s83, s67, 28672\n"
    "ds_read_b128 v[196:199], v3 offset:9216\n"
    "ds_read_b128 v[104:107], v250 offset:784\n"
    "v_mfma_f32_16x16x32_bf16 v[64:67], v[184:187], v[136:139], v[64:67]\n"
    ".Lwp0:\n"
    "s_cbranch_execz .Lw3\n"
    "s_waitcnt lgkmcnt(2)\n"
    "buffer_load_dwordx4 v1, s[16:19], s83 offen offset:0 sc1 lds\n"
    "v_mfma_f32_16x16x32_bf16 v[12:15], v[156:159], a[240:243], v[100:103]\n"
    "v_exp_f32 v37, v37\n"
    "v_mfma_f32_16x16x32_bf16 v[8:11], v[172:175], a[232:235], v[8:11]\n"
    "v_exp_f32_e64 v50, v50\n"
    "v_mfma_f32_16x16x32_bf16 v[16:19], v[176:179], v[152:155], v[100:103]\n"
    "buffer_load_dwordx4 v1, s[16:19], s83 offen offset:1024 sc1 lds\n"
    "v_mfma_f32_16x16x32_bf16 v[64:67], v[180:183], v[132:135], v[64:67]\n"
    "ds_read_b128 v[232:235], v251 offset:0\n"
    "v_cvt_pk_bf16_f32 v96, v48, v49\n"
    "v_mfma_f32_16x16x32_bf16 v[60:63], v[200:203], v[128:131], v[60:63]\n"
    "ds_read_b128 v[184:187], v3 offset:3072\n"
    "ds_read_b128 v[188:191], v3 offset:5120\n"
    "v_mfma_f32_16x16x32_bf16 v[56:59], v[180:183], a[216:219], v[56:59]\n"
    "ds_read_b128 v[200:203], v3 offset:11264\n"
    "ds_read_b128 v[180:183], v3 offset:1024\n"
    "v_mfma_f32_16x16x32_bf16 v[8:11], v[168:171], a[228:231], v[8:11]\n"
    "buffer_load_dwordx2 v[236:237], v241, s[36:39], 0 offen offset:0 sc1\n"
    "v_mfma_f32_16x16x32_bf16 v[4:7], v[156:159], a[192:195], v[100:103]\n"
    "v_exp_f32 v65, v65\n"
    "v_mfma_f32_16x16x32_bf16 v[8:11], v[164:167], a[224:227], v[8:11]\n"
    "ds_read_b128 v[192:195], v3 offset:7168\n"
    "v_exp_f32 v54, v54\n"
    "v_mfma_f32_16x16x32_bf16 v[8:11], v[160:163], a[220:223], v[8:11]\n"
    "v_exp_f32 v53, v53\n"
    "v_mfma_f32_16x16x32_bf16 v[8:11], v[156:159], a[216:219], v[8:11]\n"
    "v_exp_f32 v62, v62\n"
    "v_mfma_f32_16x16x32_bf16 v[4:7], v[160:163], a[196:199], v[4:7]\n"
    "v_exp_f32 v66, v66\n"
    "v_mfma_f32_16x16x32_bf16 v[12:15], v[160:163], a[244:247], v[12:15]\n"
    "ds_read_b128 v[100:103], v250 offset:896\n"
    "v_and_b32 v224, 0x70, v224\n"
    "v_mfma_f32_16x16x32_bf16 v[16:19], v[172:175], v[148:151], v[16:19]\n"
    "v_exp_f32 v67, v67\n"
    "v_mfma_f32_16x16x32_bf16 v[4:7], v[164:167], a[200:203], v[4:7]\n"
    "buffer_store_dwordx2 v[238:239], v244, s[36:39], s82 offen offset:0 sc1\n"
    "v_exp_f32 v63, v63\n"
    "v_mfma_f32_16x16x32_bf16 v[4:7], v[168:171], a[204:207], v[4:7]\n"
    "v_exp_f32 v9, v9\n"
    "v_mfma_f32_16x16x32_bf16 v[12:15], v[164:167], a[248:251], v[12:15]\n"
    "v_exp_f32 v57, v57\n"
    "v_mfma_f32_16x16x32_bf16 v[12:15], v[168:171], a[252:255], v[12:15]\n"
    "v_exp_f32 v11, v11\n"
    "v_mfma_f32_16x16x32_bf16 v[4:7], v[172:175], a[208:211], v[4:7]\n"
    "v_exp_f32 v58, v58\n"
    "v_mfma_f32_16x16x32_bf16 v[16:19], v[168:171], v[144:147], v[16:19]\n"
    "v_exp_f32 v64, v64\n"
    "v_mfma_f32_16x16x32_bf16 v[12:15], v[172:175], v[124:127], v[12:15]\n"
    "v_exp_f32 v55, v55\n"
    "v_mfma_f32_16x16x32_bf16 v[4:7], v[176:179], a[212:215], v[4:7]\n"
    "v_mad_u32_u24 v244, v224, 24, v2\n"
    "v_xor_b32 v2, 64, v252\n"
    "v_mfma_f32_16x16x32_bf16 v[16:19], v[164:167], v[140:143], v[16:19]\n"
    "s_waitcnt lgkmcnt(3)\n"
    "v_exp_f32 v10, v10\n"
    "v_mfma_f32_16x16x32_bf16 v[24:27], v[200:203], a[236:239], v[104:107]\n"
    "v_exp_f32 v60, v60\n"
    "v_mfma_f32_16x16x32_bf16 v[24:27], v[196:199], a[232:235], v[24:27]\n"
    "s_waitcnt lgkmcnt(2)\n"
    "v_exp_f32 v8, v8\n"
    "v_mfma_f32_16x16x32_bf16 v[28:31], v[180:183], a[240:243], v[104:107]\n"
    "v_exp_f32 v4, v4\n"
    "v_mfma_f32_16x16x32_bf16 v[28:31], v[184:187], a[244:247], v[28:31]\n"
    "v_xor_b32 v224, v224, v1\n"
    "v_cvt_pk_bf16_f32 v81, v42, v43\n"
    "v_mfma_f32_16x16x32_bf16 v[20:23], v[180:183], a[192:195], v[104:107]\n"
    "v_cvt_pk_bf16_f32 v212, v8, v9\n"
    "v_cvt_pk_bf16_f32 v213, v10, v11\n"
    "v_mfma_f32_16x16x32_bf16 v[16:19], v[160:163], v[136:139], v[16:19]\n"
    "v_exp_f32 v6, v6\n"
    "v_mfma_f32_16x16x32_bf16 v[16:19], v[156:159], v[132:135], v[16:19]\n"
    "s_waitcnt lgkmcnt(1)\n"
    "v_exp_f32 v5, v5\n"
    "v_mfma_f32_16x16x32_bf16 v[24:27], v[192:195], a[228:231], v[24:27]\n"
    "v_exp_f32 v61, v61\n"
    "v_mfma_f32_16x16x32_bf16 v[24:27], v[188:191], a[224:227], v[24:27]\n"
    "v_exp_f32 v7, v7\n"
    "v_mfma_f32_16x16x32_bf16 v[12:15], v[176:179], v[128:131], v[12:15]\n"
    "ds_read_b128 v[156:159], v3 offset:12288\n"
    "v_cvt_pk_bf16_f32 v97, v50, v51\n"
    "v_mfma_f32_16x16x32_bf16 v[28:31], v[188:191], a[248:251], v[28:31]\n"
    "v_exp_f32 v59, v59\n"
    "v_mfma_f32_16x16x32_bf16 v[32:35], v[200:203], v[152:155], v[104:107]\n"
    "v_cvt_pk_bf16_f32 v72, v36, v37\n"
    "v_cvt_pk_bf16_f32 v205, v6, v7\n"
    "v_mfma_f32_16x16x32_bf16 v[28:31], v[192:195], a[252:255], v[28:31]\n"
    "ds_read_b128 v[164:167], v3 offset:16384\n"
    "v_cvt_pk_bf16_f32 v204, v4, v5\n"
    "v_mfma_f32_16x16x32_bf16 v[20:23], v[184:187], a[196:199], v[20:23]\n"
    "v_exp_f32 v15, v15\n"
    "v_mfma_f32_16x16x32_bf16 v[28:31], v[196:199], v[124:127], v[28:31]\n"
    "v_exp_f32 v16, v16\n"
    "v_mfma_f32_16x16x32_bf16 v[24:27], v[184:187], a[220:223], v[24:27]\n"
    "v_exp_f32 v17, v17\n"
    "v_mfma_f32_16x16x32_bf16 v[32:35], v[196:199], v[148:151], v[32:35]\n"
    "ds_read_b128 v[4:7], v248 offset:0\n"
    "ds_read_b128 v[172:175], v3 offset:20480\n"
    "v_mfma_f32_16x16x32_bf16 v[24:27], v[180:183], a[216:219], v[24:27]\n"
    "v_cvt_pk_bf16_f32 v74, v52, v53\n"
    "v_cvt_pk_bf16_f32 v90, v60, v61\n"
    "v_mfma_f32_16x16x32_bf16 v[32:35], v[192:195], v[144:147], v[32:35]\n"
    "ds_read_b128 v[168:171], v3 offset:18432\n"
    "v_cvt_pk_bf16_f32 v99, v66, v67\n"
    "v_mfma_f32_16x16x32_bf16 v[20:23], v[188:191], a[200:203], v[20:23]\n"
    "ds_read_b128 v[176:179], v3 offset:22528\n"
    "v_cvt_pk_bf16_f32 v75, v54, v55\n"
    "v_mfma_f32_16x16x32_bf16 v[28:31], v[200:203], v[128:131], v[28:31]\n"
    "ds_read_b128 v[8:11], v248 offset:12288\n"
    "ds_read_b128 v[160:163], v3 offset:14336\n"
    "v_mfma_f32_16x16x32_bf16 v[20:23], v[192:195], a[204:207], v[20:23]\n"
    "ds_read_b128 v[104:107], v250 offset:912\n"
    "ds_read_b128 v[192:195], v3 offset:19456\n"
    "v_mfma_f32_16x16x32_bf16 v[20:23], v[196:199], a[208:211], v[20:23]\n"
    "v_exp_f32 v14, v14\n"
    "v_mfma_f32_16x16x32_bf16 v[32:35], v[188:191], v[140:143], v[32:35]\n"
    "v_exp_f32 v25, v25\n"
    "v_mfma_f32_16x16x32_bf16 v[20:23], v[200:203], a[212:215], v[20:23]\n"
    "v_exp_f32 v30, v30\n"
    "v_mfma_f32_16x16x32_bf16 v[32:35], v[184:187], v[136:139], v[32:35]\n"
    "s_waitcnt lgkmcnt(7)\n"
    "v_exp_f32 v26, v26\n"
    "v_mfma_f32_16x16x32_bf16 a[0:3], v[4:7], v[68:71], a[0:3]\n"
    "v_exp_f32 v28, v28\n"
    "v_mfma_f32_16x16x32_bf16 v[32:35], v[180:183], v[132:135], v[32:35]\n"
    "s_waitcnt lgkmcnt(4)\n"
    "ds_read_b128 v[180:183], v3 offset:13312\n"
    "ds_read_b128 v[200:203], v3 offset:23552\n"
    "v_mfma_f32_16x16x32_bf16 v[40:43], v[176:179], a[236:239], v[100:103]\n"
    "ds_read_b128 v[184:187], v3 offset:15360\n"
    "v_cvt_pk_bf16_f32 v83, v58, v59\n"
    "v_mfma_f32_16x16x32_bf16 v[44:47], v[156:159], a[240:243], v[100:103]\n"
    "v_exp_f32 v20, v20\n"
    "v_mfma_f32_16x16x32_bf16 v[48:51], v[176:179], v[152:155], v[100:103]\n"
    "v_exp_f32 v12, v12\n"
    "v_mfma_f32_16x16x32_bf16 v[40:43], v[172:175], a[232:235], v[40:43]\n"
    "ds_read_b128 v[196:199], v3 offset:21504\n"
    "ds_read_b128 v[188:191], v3 offset:17408\n"
    "v_mfma_f32_16x16x32_bf16 v[36:39], v[156:159], a[192:195], v[100:103]\n"
    "v_exp_f32 v35, v35\n"
    "v_mfma_f32_16x16x32_bf16 v[40:43], v[168:171], a[228:231], v[40:43]\n"
    "s_waitcnt lgkmcnt(7)\n"
    "v_exp_f32 v56, v56\n"
    "v_mfma_f32_16x16x32_bf16 v[36:39], v[160:163], a[196:199], v[36:39]\n"
    "v_exp_f32 v29, v29\n"
    "v_mfma_f32_16x16x32_bf16 v[44:47], v[160:163], a[244:247], v[44:47]\n"
    "s_waitcnt lgkmcnt(4)\n"
    "v_exp_f32 v33, v33\n"
    "v_mfma_f32_16x16x32_bf16 v[52:55], v[180:183], a[192:195], v[104:107]\n"
    "s_waitcnt lgkmcnt(2)\n"
    "v_exp_f32 v13, v13\n"
    "v_mfma_f32_16x16x32_bf16 v[52:55], v[184:187], a[196:199], v[52:55]\n"
    "v_exp_f32 v34, v34\n"
    "v_mfma_f32_16x16x32_bf16 v[40:43], v[164:167], a[224:227], v[40:43]\n"
    "v_exp_f32 v27, v27\n"
    "v_mfma_f32_16x16x32_bf16 v[36:39], v[164:167], a[200:203], v[36:39]\n"
    "v_exp_f32 v31, v31\n"
    "v_mfma_f32_16x16x32_bf16 v[40:43], v[160:163], a[220:223], v[40:43]\n"
    "s_waitcnt lgkmcnt(0)\n"
    "v_cvt_pk_bf16_f32 v82, v56, v57\n"
    "v_cvt_pk_bf16_f32 v231, v34, v35\n"
    "v_mfma_f32_16x16x32_bf16 v[52:55], v[188:191], a[200:203], v[52:55]\n"
    "v_cvt_pk_bf16_f32 v221, v14, v15\n"
    "v_cvt_pk_bf16_f32 v91, v62, v63\n"
    "v_mfma_f32_16x16x32_bf16 v[36:39], v[168:171], a[204:207], v[36:39]\n"
    "v_exp_f32 v21, v21\n"
    "v_mfma_f32_16x16x32_bf16 v[40:43], v[156:159], a[216:219], v[40:43]\n"
    "v_exp_f32 v23, v23\n"
    "v_mfma_f32_16x16x32_bf16 v[52:55], v[192:195], a[204:207], v[52:55]\n"
    "v_cvt_pk_bf16_f32 v223, v30, v31\n"
    "v_cvt_pk_bf16_f32 v98, v64, v65\n"
    "v_mfma_f32_16x16x32_bf16 v[64:67], v[200:203], v[152:155], v[104:107]\n"
    "v_cvt_pk_bf16_f32 v228, v16, v17\n"
    "v_cvt_pk_bf16_f32 v206, v20, v21\n"
    "v_mfma_f32_16x16x32_bf16 v[52:55], v[196:199], a[208:211], v[52:55]\n"
    "v_exp_f32 v24, v24\n"
    "v_mfma_f32_16x16x32_bf16 v[52:55], v[200:203], a[212:215], v[52:55]\n"
    "v_exp_f32 v43, v43\n"
    "v_mfma_f32_16x16x32_bf16 v[44:47], v[164:167], a[248:251], v[44:47]\n"
    "v_exp_f32 v32, v32\n"
    "v_mfma_f32_16x16x32_bf16 v[36:39], v[172:175], a[208:211], v[36:39]\n"
    "v_exp_f32 v40, v40\n"
    "v_mfma_f32_16x16x32_bf16 v[44:47], v[168:171], a[252:255], v[44:47]\n"
    "v_cvt_pk_bf16_f32 v222, v28, v29\n"
    "v_cvt_pk_bf16_f32 v230, v32, v33\n"
    "v_mfma_f32_16x16x32_bf16 v[56:59], v[200:203], a[236:239], v[104:107]\n"
    "s_mov_b32 s79, 133\n"
    "v_exp_f32 v19, v19\n"
    "v_mfma_f32_16x16x32_bf16 a[4:7], v[4:7], v[76:79], a[4:7]\n"
    "v_add_u32 v3, s72, v224\n"
    "v_exp_f32 v18, v18\n"
    "v_mfma_f32_16x16x32_bf16 v[56:59], v[196:199], a[232:235], v[56:59]\n"
    "ds_read_b128 v[32:35], v248 offset:15360\n"
    "v_cvt_pk_bf16_f32 v214, v24, v25\n"
    "v_mfma_f32_16x16x32_bf16 v[36:39], v[176:179], a[212:215], v[36:39]\n"
    "s_waitcnt vmcnt(0)\n"
    "ds_add_u32 v247, v246\n"
    "v_mfma_f32_16x16x32_bf16 v[56:59], v[192:195], a[228:231], v[56:59]\n"
    "v_exp_f32 v53, v53\n"
    "v_mfma_f32_16x16x32_bf16 a[0:3], v[8:11], v[72:75], a[0:3]\n"
    "v_exp_f32 v55, v55\n"
    "v_mfma_f32_16x16x32_bf16 v[60:63], v[180:183], a[240:243], v[104:107]\n"
    "v_exp_f32 v22, v22\n"
    "v_mfma_f32_16x16x32_bf16 v[64:67], v[196:199], v[148:151], v[64:67]\n"
    "v_exp_f32 v52, v52\n"
    "v_smfmac_f32_16x16x64_bf16 v[112:115], v[232:235], v[76:83], v253\n"
    "v_cvt_pk_bf16_f32 v220, v12, v13\n"
    "v_mfma_f32_16x16x32_bf16 v[56:59], v[188:191], a[224:227], v[56:59]\n"
    "s_mov_b32 s73, 0x7f800000\n"
    "v_exp_f32 v54, v54\n"
    "v_mfma_f32_16x16x32_bf16 v[60:63], v[184:187], a[244:247], v[60:63]\n"
    "v_exp_f32 v41, v41\n"
    "v_exp_f32 v37, v37\n"
    "v_smfmac_f32_16x16x64_bf16 v[108:111], v[232:235], v[68:75], v253\n"
    "s_waitcnt lgkmcnt(1)\n"
    "ds_read_b128 v[240:243], v251 offset:768\n"
    "v_mfma_f32_16x16x32_bf16 a[52:55], v[32:35], v[80:83], a[52:55]\n"
    "v_cvt_pk_bf16_f32 v211, v54, v55\n"
    "v_cvt_pk_bf16_f32 v207, v22, v23\n"
    "v_mfma_f32_16x16x32_bf16 v[56:59], v[184:187], a[220:223], v[56:59]\n"
    "ds_read_b128 v[28:31], v248 offset:3072\n"
    "v_cvt_pk_bf16_f32 v229, v18, v19\n"
    "v_mfma_f32_16x16x32_bf16 v[56:59], v[180:183], a[216:219], v[56:59]\n"
    "v_cvt_pk_bf16_f32 v215, v26, v27\n"
    "v_exp_f32 v42, v42\n"
    "v_mfma_f32_16x16x32_bf16 v[60:63], v[188:191], a[248:251], v[60:63]\n"
    "ds_read_b32 v245, v247\n"
    "ds_read_b128 v[12:15], v248 offset:1024\n"
    "v_cvt_pk_bf16_f32 v210, v52, v53\n"
    "v_mfma_f32_16x16x32_bf16 v[60:63], v[192:195], a[252:255], v[60:63]\n"
    "s_nop 0\n"
    "v_exp_f32 v58, v58\n"
    "v_mfma_f32_16x16x32_bf16 v[64:67], v[192:195], v[144:147], v[64:67]\n"
    "v_exp_f32 v36, v36\n"
    "v_mfma_f32_16x16x32_bf16 v[64:67], v[188:191], v[140:143], v[64:67]\n"
    "ds_read_b128 v[20:23], v248 offset:2048\n"
    "ds_read_b128 v[16:19], v248 offset:13312\n"
    "v_mfma_f32_16x16x32_bf16 v[60:63], v[196:199], v[124:127], v[60:63]\n"
    "v_exp_f32 v39, v39\n"
    "v_mfma_f32_16x16x32_bf16 v[60:63], v[200:203], v[128:131], v[60:63]\n"
    "v_exp_f32 v38, v38\n"
    "v_mfma_f32_16x16x32_bf16 v[64:67], v[184:187], v[136:139], v[64:67]\n"
    "v_cvt_pk_bf16_f32 v208, v36, v37\n"
    "v_cvt_pk_bf16_f32 v216, v40, v41\n"
    "v_mfma_f32_16x16x32_bf16 a[4:7], v[8:11], v[80:83], a[4:7]\n"
    "s_waitcnt lgkmcnt(3)\n"
    "v_cvt_pk_bf16_f32 v209, v38, v39\n"
    "v_cvt_pk_bf16_f32 v217, v42, v43\n"
    "v_cmp_le_u32 s[56:57], s59, v245\n"
    "v_mfma_f32_16x16x32_bf16 v[44:47], v[172:175], v[124:127], v[44:47]\n"
    "v_exp_f32 v57, v57\n"
    "v_mfma_f32_16x16x32_bf16 a[52:55], v[28:31], v[76:79], a[52:55]\n"
    "s_cmp_eq_u64 s[56:57], -1\n"
    "v_exp_f32 v62, v62\n"
    "v_smfmac_f32_16x16x64_bf16 v[108:111], v[240:243], v[204:211], v253\n"
    "s_cbranch_scc0 .Lpt1\n"
    ".Lpt1r:\n"
    "v_mfma_f32_16x16x32_bf16 v[48:51], v[172:175], v[148:151], v[48:51]\n"
    "s_waitcnt lgkmcnt(1)\n"
    "ds_read_b128 v[24:27], v248 offset:14336\n"
    "ds_read_b128 a[216:219], v249 offset:3072\n"
    "v_mfma_f32_16x16x32_bf16 a[32:35], v[20:23], v[68:71], a[32:35]\n"
    "v_exp_f32 v59, v59\n"
    "v_mfma_f32_16x16x32_bf16 v[64:67], v[180:183], v[132:135], v[64:67]\n"
    "ds_read_b128 a[212:215], v249 offset:14336\n"
    "ds_read_b128 a[192:195], v249 offset:0\n"
    "v_mfma_f32_16x16x32_bf16 v[44:47], v[176:179], v[128:131], v[44:47]\n"
    "ds_read_b128 a[208:211], v249 offset:2048\n"
    "v_mov_b32 v111, v110\n"
    "v_mfma_f32_16x16x32_bf16 v[48:51], v[168:171], v[144:147], v[48:51]\n"
    "ds_read_b128 a[200:203], v249 offset:1024\n"
    "v_exp_f32 v56, v56\n"
    "v_smfmac_f32_16x16x64_bf16 v[116:119], v[232:235], v[84:91], v253\n"
    "s_waitcnt lgkmcnt(6)\n"
    "v_add_f32 v108, v108, v109\n"
    "v_mfma_f32_16x16x32_bf16 a[16:19], v[16:19], v[72:75], a[16:19]\n"
    "ds_read_b128 a[196:199], v249 offset:12288\n"
    "ds_read_b128 a[220:223], v249 offset:15360\n"
    "v_mfma_f32_16x16x32_bf16 a[48:51], v[32:35], v[72:75], a[48:51]\n"
    "s_waitcnt lgkmcnt(7)\n"
    "v_cvt_pk_bf16_f32 v219, v58, v59\n"
    "v_cvt_pk_bf16_f32 v218, v56, v57\n"
    "v_mov_b32 v109, v108\n"
    "v_mfma_f32_16x16x32_bf16 a[32:35], v[24:27], v[72:75], a[32:35]\n"
    "ds_read_b128 a[204:207], v249 offset:13312\n"
    "v_exp_f32 v47, v47\n"
    "v_smfmac_f32_16x16x64_bf16 v[112:115], v[240:243], v[212:219], v253\n"
    "s_waitcnt lgkmcnt(5)\n"
    "v_exp_f32 v46, v46\n"
    "v_mfma_f32_16x16x32_bf16 a[0:3], a[192:195], v[204:207], a[0:3]\n"
    "v_exp_f32 v67, v67\n"
    "v_mfma_f32_16x16x32_bf16 a[36:39], v[20:23], v[76:79], a[36:39]\n"
    "v_exp_f32 v61, v61\n"
    "v_mfma_f32_16x16x32_bf16 a[48:51], v[28:31], v[68:71], a[48:51]\n"
    "s_waitcnt lgkmcnt(4)\n"
    "v_exp_f32 v45, v45\n"
    "v_mfma_f32_16x16x32_bf16 a[32:35], a[208:211], v[204:207], a[32:35]\n"
    "s_waitcnt lgkmcnt(2)\n"
    "v_exp_f32 v63, v63\n"
    "v_mfma_f32_16x16x32_bf16 a[0:3], a[196:199], v[208:211], a[0:3]\n"
    "v_permlane16_swap_b32 v110, v111\n"
    "v_mfma_f32_16x16x32_bf16 a[16:19], v[12:15], v[68:71], a[16:19]\n"
    "s_waitcnt lgkmcnt(1)\n"
    "v_exp_f32 v44, v44\n"
    "v_mfma_f32_16x16x32_bf16 a[48:51], a[220:223], v[208:211], a[48:51]\n"
    "s_waitcnt lgkmcnt(0)\n"
    "v_add_f32 v112, v112, v113\n"
    "v_mov_b32 v115, v114\n"
    "v_mfma_f32_16x16x32_bf16 a[16:19], a[204:207], v[208:211], a[16:19]\n"
    "v_permlane16_swap_b32 v108, v109\n"
    "v_mfma_f32_16x16x32_bf16 a[32:35], a[212:215], v[208:211], a[32:35]\n"
    "v_accvgpr_read_b32 v36, a0\n"
    "v_add_f32 v110, v110, v111\n"
    "v_mfma_f32_16x16x32_bf16 a[36:39], v[24:27], v[80:83], a[36:39]\n"
    "v_permlane16_swap_b32 v114, v115\n"
    "v_mfma_f32_16x16x32_bf16 a[48:51], a[216:219], v[204:207], a[48:51]\n"
    "v_accvgpr_read_b32 v52, a2\n"
    "v_add_f32 v108, v108, v109\n"
    "v_mfma_f32_16x16x32_bf16 a[16:19], a[200:203], v[204:207], a[16:19]\n"
    "v_accvgpr_read_b32 v40, a1\n"
    "v_rcp_f32 v109, v108\n"
    "v_mfma_f32_16x16x32_bf16 a[52:55], a[220:223], v[216:219], a[52:55]\n"
    "v_exp_f32 v66, v66\n"
    "v_mfma_f32_16x16x32_bf16 a[52:55], a[216:219], v[212:215], a[52:55]\n"
    "v_accvgpr_read_b32 v54, a34\n"
    "v_accvgpr_read_b32 v38, a32\n"
    "v_accvgpr_read_b32 v43, a49\n"
    "v_mfma_f32_16x16x32_bf16 v[48:51], v[164:167], v[140:143], v[48:51]\n"
    "v_exp_f32 v64, v64\n"
    "v_accvgpr_read_b32 v53, a18\n"
    "v_mfma_f32_16x16x32_bf16 a[20:23], v[16:19], v[80:83], a[20:23]\n"
    "v_accvgpr_read_b32 v59, a51\n"
    "v_mov_b32 v113, v112\n"
    "v_mfma_f32_16x16x32_bf16 a[4:7], a[192:195], v[212:215], a[4:7]\n"
    "v_add_f32 v114, v114, v115\n"
    "v_accvgpr_read_b32 v58, a35\n"
    "v_mul_f32 v111, -v110, v109\n"
    "v_mfma_f32_16x16x32_bf16 a[4:7], a[196:199], v[216:219], a[4:7]\n"
    "v_accvgpr_read_b32 v55, a50\n"
    "v_accvgpr_read_b32 v56, a3\n"
    "v_mfma_f32_16x16x32_bf16 a[36:39], a[208:211], v[212:215], a[36:39]\n"
    "v_permlane16_swap_b32 v112, v113\n"
    "v_mfma_f32_16x16x32_bf16 v[48:51], v[160:163], v[136:139], v[48:51]\n"
    "v_accvgpr_read_b32 v41, a17\n"
    "v_accvgpr_read_b32 v42, a33\n"
    "v_mfma_f32_16x16x32_bf16 a[36:39], a[212:215], v[216:219], a[36:39]\n"
    "v_accvgpr_read_b32 v37, a16\n"
    "v_exp_f32 v60, v60\n"
    "v_mfma_f32_16x16x32_bf16 v[48:51], v[156:159], v[132:135], v[48:51]\n"
    "v_ldexp_f32 v109, v109, s79\n"
    "v_add_f32 v112, v112, v113\n"
    "v_accvgpr_read_b32 v57, a19\n"
    "v_cvt_pk_bf16_f32 v224, v44, v45\n"
    "v_cvt_pk_bf16_f32 v226, v60, v61\n"
    "v_pk_fma_f32 v[42:43], v[42:43], v[108:109], v[110:111] op_sel:[0,1,1] op_sel_hi:[1,1,1]\n"
    "v_pk_fma_f32 v[40:41], v[40:41], v[108:109], v[110:111] op_sel:[0,1,1] op_sel_hi:[1,1,1]\n"
    "v_pk_fma_f32 v[58:59], v[58:59], v[108:109], v[110:111] op_sel:[0,1,1] op_sel_hi:[1,1,1]\n"
    "v_cvt_pk_bf16_f32 v227, v62, v63\n"
    "v_pk_fma_f32 v[56:57], v[56:57], v[108:109], v[110:111] op_sel:[0,1,1] op_sel_hi:[1,1,1]\n"
    "v_accvgpr_read_b32 v39, a48\n"
    "v_cvt_pk_bf16_f32 v225, v46, v47\n"
    "v_pk_fma_f32 v[38:39], v[38:39], v[108:109], v[110:111] op_sel:[0,1,1] op_sel_hi:[1,1,1]\n"
    "v_pk_fma_f32 v[54:55], v[54:55], v[108:109], v[110:111] op_sel:[0,1,1] op_sel_hi:[1,1,1]\n"
    "v_pk_fma_f32 v[36:37], v[36:37], v[108:109], v[110:111] op_sel:[0,1,1] op_sel_hi:[1,1,1]\n"
    "v_pk_fma_f32 v[52:53], v[52:53], v[108:109], v[110:111] op_sel:[0,1,1] op_sel_hi:[1,1,1]\n"
    "v_cvt_pk_bf16_f32 v36, v36, v37\n"
    "v_cvt_pk_bf16_f32 v52, v52, v53\n"
    "v_accvgpr_read_b32 v167, a54\n"
    "v_mfma_f32_16x16x32_bf16 a[20:23], v[12:15], v[76:79], a[20:23]\n"
    "v_rcp_f32 v113, v112\n"
    "v_accvgpr_read_b32 v164, a6\n"
    "v_cvt_pk_bf16_f32 v53, v54, v55\n"
    "v_cvt_pk_bf16_f32 v37, v38, v39\n"
    "v_mfma_f32_16x16x32_bf16 a[20:23], a[204:207], v[216:219], a[20:23]\n"
    "v_cvt_pk_bf16_f32 v38, v40, v41\n"
    "v_cvt_pk_bf16_f32 v39, v42, v43\n"
    "v_smfmac_f32_16x16x64_bf16 v[116:119], v[240:243], v[220:227], v253\n"
    "ds_write_b128 v252, v[36:39] offset:0\n"
    "v_mfma_f32_16x16x32_bf16 a[40:43], v[20:23], v[84:87], a[40:43]\n"
    "v_accvgpr_read_b32 v156, a4\n"
    "v_accvgpr_read_b32 v159, a52\n"
    "v_mfma_f32_16x16x32_bf16 a[40:43], v[24:27], v[88:91], a[40:43]\n"
    "v_cvt_pk_bf16_f32 v54, v56, v57\n"
    "v_mul_f32 v115, -v114, v113\n"
    "v_cvt_pk_bf16_f32 v55, v58, v59\n"
    "v_mfma_f32_16x16x32_bf16 a[24:27], v[16:19], v[88:91], a[24:27]\n"
    "ds_write_b128 v2, v[52:55] offset:0\n"
    "v_mfma_f32_16x16x32_bf16 a[56:59], v[32:35], v[88:91], a[56:59]\n"
    "v_accvgpr_read_b32 v158, a36\n"
    "v_exp_f32 v65, v65\n"
    "v_accvgpr_read_b32 v160, a5\n"
    "v_mfma_f32_16x16x32_bf16 a[20:23], a[200:203], v[212:215], a[20:23]\n"
    "v_exp_f32 v49, v49\n"
    "v_mfma_f32_16x16x32_bf16 a[8:11], v[4:7], v[84:87], a[8:11]\n"
    "ds_read_b128 v[56:59], v3 offset:1024\n"
    "v_add_f32 v116, v116, v117\n"
    "v_mfma_f32_16x16x32_bf16 a[40:43], a[208:211], v[220:223], a[40:43]\n"
    "ds_read_b128 v[40:43], v3 offset:0\n"
    "v_accvgpr_read_b32 v166, a38\n"
    "v_mfma_f32_16x16x32_bf16 a[56:59], v[28:31], v[84:87], a[56:59]\n"
    "v_accvgpr_read_b32 v171, a55\n"
    "v_mov_b32 v119, v118\n"
    "v_mfma_f32_16x16x32_bf16 a[8:11], v[8:11], v[88:91], a[8:11]\n"
    "v_mov_b32 v117, v116\n"
    "v_accvgpr_read_b32 v163, a53\n"
    "v_smfmac_f32_16x16x64_bf16 v[120:123], v[232:235], v[92:99], v253\n"
    "v_accvgpr_read_b32 v168, a7\n"
    "v_mfma_f32_16x16x32_bf16 a[24:27], v[12:15], v[84:87], a[24:27]\n"
    "v_accvgpr_read_b32 v161, a21\n"
    "v_exp_f32 v51, v51\n"
    "v_accvgpr_read_b32 v165, a22\n"
    "v_mfma_f32_16x16x32_bf16 a[56:59], a[220:223], v[224:227], a[56:59]\n"
    "v_accvgpr_read_b32 v170, a39\n"
    "v_accvgpr_read_b32 v162, a37\n"
    "v_accvgpr_read_b32 v157, a20\n"
    "v_mfma_f32_16x16x32_bf16 a[56:59], a[216:219], v[220:223], a[56:59]\n"
    "s_waitcnt lgkmcnt(1)\n"
    "buffer_store_dwordx4 v[56:59], v244, s[24:27], s75 offen offset:3072 sc1\n"
    "v_exp_f32 v48, v48\n"
    "v_accvgpr_read_b32 v169, a23\n"
    "v_ldexp_f32 v113, v113, s79\n"
    "v_pk_fma_f32 v[170:171], v[170:171], v[112:113], v[114:115] op_sel:[0,1,1] op_sel_hi:[1,1,1]\n"
    "v_pk_fma_f32 v[162:163], v[162:163], v[112:113], v[114:115] op_sel:[0,1,1] op_sel_hi:[1,1,1]\n"
    "v_permlane16_swap_b32 v118, v119\n"
    "v_pk_fma_f32 v[168:169], v[168:169], v[112:113], v[114:115] op_sel:[0,1,1] op_sel_hi:[1,1,1]\n"
    "v_cvt_pk_bf16_f32 v234, v64, v65\n"
    "v_pk_fma_f32 v[156:157], v[156:157], v[112:113], v[114:115] op_sel:[0,1,1] op_sel_hi:[1,1,1]\n"
    "v_pk_fma_f32 v[166:167], v[166:167], v[112:113], v[114:115] op_sel:[0,1,1] op_sel_hi:[1,1,1]\n"
    "v_pk_fma_f32 v[158:159], v[158:159], v[112:113], v[114:115] op_sel:[0,1,1] op_sel_hi:[1,1,1]\n"
    "v_cvt_pk_bf16_f32 v156, v156, v157\n"
    "v_cvt_pk_bf16_f32 v157, v158, v159\n"
    "v_pk_fma_f32 v[164:165], v[164:165], v[112:113], v[114:115] op_sel:[0,1,1] op_sel_hi:[1,1,1]\n"
    "v_pk_fma_f32 v[160:161], v[160:161], v[112:113], v[114:115] op_sel:[0,1,1] op_sel_hi:[1,1,1]\n"
    "v_cvt_pk_bf16_f32 v158, v160, v161\n"
    "v_accvgpr_read_b32 v187, a59\n"
    "v_mfma_f32_16x16x32_bf16 a[40:43], a[212:215], v[224:227], a[40:43]\n"
    "v_cvt_pk_bf16_f32 v159, v162, v163\n"
    "v_cvt_pk_bf16_f32 v164, v164, v165\n"
    "v_mfma_f32_16x16x32_bf16 a[8:11], a[192:195], v[220:223], a[8:11]\n"
    "v_cvt_pk_bf16_f32 v165, v166, v167\n"
    "v_cvt_pk_bf16_f32 v235, v66, v67\n"
    "v_mfma_f32_16x16x32_bf16 a[24:27], a[204:207], v[224:227], a[24:27]\n"
    "ds_write_b128 v252, v[156:159] offset:2048\n"
    "v_mfma_f32_16x16x32_bf16 a[24:27], a[200:203], v[220:223], a[24:27]\n"
    "v_cvt_pk_bf16_f32 v166, v168, v169\n"
    "v_add_f32 v118, v118, v119\n"
    "v_cvt_pk_bf16_f32 v167, v170, v171\n"
    "v_mfma_f32_16x16x32_bf16 a[8:11], a[196:199], v[224:227], a[8:11]\n"
    "v_cvt_pk_bf16_f32 v232, v48, v49\n"
    "v_accvgpr_read_b32 v178, a41\n"
    "v_mfma_f32_16x16x32_bf16 a[28:31], v[16:19], v[96:99], a[28:31]\n"
    "s_waitcnt lgkmcnt(1)\n"
    "buffer_store_dwordx4 v[40:43], v244, s[24:27], s75 offen offset:0 sc1\n"
    "ds_write_b128 v2, v[164:167] offset:2048\n"
    "v_mfma_f32_16x16x32_bf16 a[12:15], v[4:7], v[92:95], a[12:15]\n"
    "v_accvgpr_read_b32 v181, a26\n"
    "v_exp_f32 v50, v50\n"
    "v_mfma_f32_16x16x32_bf16 a[44:47], v[20:23], v[92:95], a[44:47]\n"
    "v_permlane16_swap_b32 v116, v117\n"
    "v_accvgpr_read_b32 v180, a10\n"
    "v_mfma_f32_16x16x32_bf16 a[12:15], v[8:11], v[96:99], a[12:15]\n"
    "ds_read_b128 v[8:11], v248 offset:16384\n"
    "ds_read_b128 v[160:163], v3 offset:2048\n"
    "ds_read_b128 v[4:7], v248 offset:4096\n"
    "v_accvgpr_read_b32 v176, a9\n"
    "v_accvgpr_read_b32 v182, a42\n"
    "v_mfma_f32_16x16x32_bf16 a[12:15], a[192:195], v[228:231], a[12:15]\n"
    "v_accvgpr_read_b32 v186, a43\n"
    "v_accvgpr_read_b32 v183, a58\n"
    "v_mfma_f32_16x16x32_bf16 a[28:31], v[12:15], v[92:95], a[28:31]\n"
    "v_accvgpr_read_b32 v173, a24\n"
    "v_accvgpr_read_b32 v184, a11\n"
    "v_mfma_f32_16x16x32_bf16 a[60:63], v[32:35], v[96:99], a[60:63]\n"
    "v_accvgpr_read_b32 v172, a8\n"
    "v_cvt_pk_bf16_f32 v233, v50, v51\n"
    "v_add_f32 v116, v116, v117\n"
    "v_rcp_f32_e64 v117, v116\n"
    "v_mfma_f32_16x16x32_bf16 a[12:15], a[196:199], v[232:235], a[12:15]\n"
    "v_accvgpr_read_b32 v175, a56\n"
    "v_mul_f32 v119, -v118, v117\n"
    "v_smfmac_f32_16x16x64_bf16 v[120:123], v[240:243], v[228:235], v253\n"
    "ds_read_b128 v[20:23], v248 offset:6144\n"
    "v_mfma_f32_16x16x32_bf16 a[28:31], a[204:207], v[232:235], a[28:31]\n"
    "v_accvgpr_read_b32 v177, a25\n"
    "v_accvgpr_read_b32 v179, a57\n"
    "v_ldexp_f32 v117, v117, s79\n"
    "v_mfma_f32_16x16x32_bf16 a[60:63], v[28:31], v[92:95], a[60:63]\n"
    "s_waitcnt lgkmcnt(2)\n"
    "ds_read_b128 v[168:171], v3 offset:3072\n"
    "ds_read_b128 v[16:19], v248 offset:17408\n"
    "buffer_store_dwordx4 v[160:163], v244, s[24:27], s76 offen offset:0 sc1\n"
    "ds_read_b128 v[12:15], v248 offset:5120\n"
    "ds_read_b128 a[192:195], v249 offset:4096\n"
    "v_pk_fma_f32 v[186:187], v[186:187], v[116:117], v[118:119] op_sel:[0,1,1] op_sel_hi:[1,1,1]\n"
    "v_pk_fma_f32 v[182:183], v[182:183], v[116:117], v[118:119] op_sel:[0,1,1] op_sel_hi:[1,1,1]\n"
    "v_accvgpr_read_b32 v174, a40\n"
    "v_accvgpr_read_b32 v185, a27\n"
    "v_pk_fma_f32 v[174:175], v[174:175], v[116:117], v[118:119] op_sel:[0,1,1] op_sel_hi:[1,1,1]\n"
    "v_pk_fma_f32 v[180:181], v[180:181], v[116:117], v[118:119] op_sel:[0,1,1] op_sel_hi:[1,1,1]\n"
    "v_pk_fma_f32 v[184:185], v[184:185], v[116:117], v[118:119] op_sel:[0,1,1] op_sel_hi:[1,1,1]\n"
    "v_pk_fma_f32 v[172:173], v[172:173], v[116:117], v[118:119] op_sel:[0,1,1] op_sel_hi:[1,1,1]\n"
    "v_pk_fma_f32 v[176:177], v[176:177], v[116:117], v[118:119] op_sel:[0,1,1] op_sel_hi:[1,1,1]\n"
    "v_pk_fma_f32 v[178:179], v[178:179], v[116:117], v[118:119] op_sel:[0,1,1] op_sel_hi:[1,1,1]\n"
    "v_cvt_pk_bf16_f32 v172, v172, v173\n"
    "v_cvt_pk_bf16_f32 v173, v174, v175\n"
    "v_mfma_f32_16x16x32_bf16 a[44:47], v[24:27], v[96:99], a[44:47]\n"
    "v_cvt_pk_bf16_f32 v180, v180, v181\n"
    "v_cvt_pk_bf16_f32 v174, v176, v177\n"
    "v_cvt_pk_bf16_f32 v175, v178, v179\n"
    "v_add_f32 v120, v120, v121\n"
    "v_mfma_f32_16x16x32_bf16 a[60:63], a[220:223], v[232:235], a[60:63]\n"
    "ds_read_b128 a[204:207], v249 offset:17408\n"
    "ds_read_b128 a[196:199], v249 offset:16384\n"
    "ds_read_b128 v[32:35], v248 offset:19456\n"
    "v_mfma_f32_16x16x32_bf16 a[28:31], a[200:203], v[228:231], a[28:31]\n"
    "s_waitcnt lgkmcnt(6)\n"
    "buffer_store_dwordx4 v[168:171], v244, s[24:27], s76 offen offset:3072 sc1\n"
    "ds_write_b128 v252, v[172:175] offset:4096\n"
    "v_mfma_f32_16x16x32_bf16 a[96:99], v[20:23], v[68:71], a[96:99]\n"
    "v_mov_b32 v123, v122\n"
    "v_mov_b32 v121, v120\n"
    "v_cvt_pk_bf16_f32 v181, v182, v183\n"
    "v_mfma_f32_16x16x32_bf16 a[44:47], a[208:211], v[228:231], a[44:47]\n"
    "v_cvt_pk_bf16_f32 v183, v186, v187\n"
    "v_cvt_pk_bf16_f32 v182, v184, v185\n"
    "v_mfma_f32_16x16x32_bf16 a[44:47], a[212:215], v[232:235], a[44:47]\n"
    "ds_write_b128 v2, v[180:183] offset:4096\n"
    "v_accvgpr_read_b32 v188, a12\n"
    "v_accvgpr_read_b32 v189, a28\n"
    "v_mfma_f32_16x16x32_bf16 a[64:67], v[4:7], v[68:71], a[64:67]\n"
    "v_permlane16_swap_b32 v122, v123\n"
    "v_mfma_f32_16x16x32_bf16 a[60:63], a[216:219], v[228:231], a[60:63]\n"
    "s_waitcnt lgkmcnt(7)\n"
    "ds_read_b128 v[28:31], v248 offset:7168\n"
    "ds_read_b128 v[184:187], v3 offset:5120\n"
    "ds_read_b128 v[176:179], v3 offset:4096\n"
    "ds_read_b128 v[24:27], v248 offset:18432\n"
    "v_mfma_f32_16x16x32_bf16 a[80:83], v[16:19], v[72:75], a[80:83]\n"
    "s_waitcnt lgkmcnt(10)\n"
    "ds_read_b128 a[216:219], v249 offset:7168\n"
    "ds_read_b128 a[200:203], v249 offset:5120\n"
    "v_mfma_f32_16x16x32_bf16 a[80:83], v[12:15], v[68:71], a[80:83]\n"
    "s_waitcnt lgkmcnt(10)\n"
    "ds_read_b128 a[220:223], v249 offset:19456\n"
    "v_accvgpr_read_b32 v199, a62\n"
    "v_accvgpr_read_b32 v190, a44\n"
    "v_accvgpr_read_b32 v192, a13\n"
    "v_mfma_f32_16x16x32_bf16 a[80:83], a[204:207], v[208:211], a[80:83]\n"
    "ds_read_b128 a[208:211], v249 offset:6144\n"
    "v_accvgpr_read_b32 v200, a15\n"
    "v_mfma_f32_16x16x32_bf16 a[64:67], v[8:11], v[72:75], a[64:67]\n"
    "v_accvgpr_read_b32 v203, a63\n"
    "v_accvgpr_read_b32 v198, a46\n"
    "v_mfma_f32_16x16x32_bf16 a[64:67], a[192:195], v[204:207], a[64:67]\n"
    "s_waitcnt lgkmcnt(10)\n"
    "v_permlane16_swap_b32_e64 v120, v121\n"
    "v_add_f32_e64 v120, v120, v121\n"
    "v_mfma_f32_16x16x32_bf16 a[112:115], v[32:35], v[72:75], a[112:115]\n"
    "v_rcp_f32_e64 v121, v120\n"
    "v_mfma_f32_16x16x32_bf16 a[64:67], a[196:199], v[208:211], a[64:67]\n"
    "s_waitcnt lgkmcnt(6)\n"
    "buffer_store_dwordx4 v[184:187], v244, s[24:27], s77 offen offset:3072 sc1\n"
    "v_accvgpr_read_b32 v196, a14\n"
    "v_add_f32_e64 v122, v122, v123\n"
    "v_mul_f32 v123, -v122, v121\n"
    "v_accvgpr_read_b32 v201, a31\n"
    "v_accvgpr_read_b32 v191, a60\n"
    "v_mfma_f32_16x16x32_bf16 a[112:115], v[28:31], v[68:71], a[112:115]\n"
    "s_waitcnt lgkmcnt(2)\n"
    "v_accvgpr_read_b32 v202, a47\n"
    "v_accvgpr_read_b32 v197, a30\n"
    "v_mfma_f32_16x16x32_bf16 a[80:83], a[200:203], v[204:207], a[80:83]\n"
    "s_waitcnt lgkmcnt(1)\n"
    "v_accvgpr_read_b32 v194, a45\n"
    "v_accvgpr_read_b32 v128, a65\n"
    "v_mfma_f32_16x16x32_bf16 a[112:115], a[220:223], v[208:211], a[112:115]\n"
    "buffer_store_dwordx4 v[176:179], v244, s[24:27], s77 offen offset:0 sc1\n"
    "ds_read_b128 a[212:215], v249 offset:18432\n"
    "v_accvgpr_read_b32 v195, a61\n"
    "v_accvgpr_read_b32 v132, a66\n"
    "v_ldexp_f32 v121, v121, s79\n"
    "v_pk_fma_f32 v[190:191], v[190:191], v[120:121], v[122:123] op_sel:[0,1,1] op_sel_hi:[1,1,1]\n"
    "v_pk_fma_f32 v[194:195], v[194:195], v[120:121], v[122:123] op_sel:[0,1,1] op_sel_hi:[1,1,1]\n"
    "v_pk_fma_f32 v[196:197], v[196:197], v[120:121], v[122:123] op_sel:[0,1,1] op_sel_hi:[1,1,1]\n"
    "v_accvgpr_read_b32 v133, a82\n"
    "v_pk_fma_f32 v[200:201], v[200:201], v[120:121], v[122:123] op_sel:[0,1,1] op_sel_hi:[1,1,1]\n"
    "v_pk_fma_f32 v[198:199], v[198:199], v[120:121], v[122:123] op_sel:[0,1,1] op_sel_hi:[1,1,1]\n"
    "v_accvgpr_read_b32 v125, a80\n"
    ".Lwp1:\n"
    "s_cbranch_execz .Lw3\n"
    "v_accvgpr_read_b32 v193, a29\n"
    "v_accvgpr_read_b32 v129, a81\n"
    "v_cvt_pk_bf16_f32 v196, v196, v197\n"
    "s_nop 0\n"
    "v_pk_fma_f32 v[132:133], v[132:133], v[108:109], v[110:111] op_sel:[0,1,1] op_sel_hi:[1,1,1]\n"
    "v_pk_fma_f32 v[202:203], v[202:203], v[120:121], v[122:123] op_sel:[0,1,1] op_sel_hi:[1,1,1]\n"
    "v_pk_fma_f32 v[128:129], v[128:129], v[108:109], v[110:111] op_sel:[0,1,1] op_sel_hi:[1,1,1]\n"
    "v_cvt_pk_bf16_f32 v197, v198, v199\n"
    "v_pk_fma_f32 v[188:189], v[188:189], v[120:121], v[122:123] op_sel:[0,1,1] op_sel_hi:[1,1,1]\n"
    "v_pk_fma_f32 v[192:193], v[192:193], v[120:121], v[122:123] op_sel:[0,1,1] op_sel_hi:[1,1,1]\n"
    "v_cvt_pk_bf16_f32 v199, v202, v203\n"
    "v_cvt_pk_bf16_f32 v198, v200, v201\n"
    "v_mfma_f32_16x16x32_bf16 a[112:115], a[216:219], v[204:207], a[112:115]\n"
    "ds_write_b128 v2, v[196:199] offset:0\n"
    "v_mfma_f32_16x16x32_bf16 a[96:99], v[24:27], v[72:75], a[96:99]\n"
    "s_waitcnt lgkmcnt(2)\n"
    "v_cvt_pk_bf16_f32 v188, v188, v189\n"
    "v_cvt_pk_bf16_f32 v189, v190, v191\n"
    "v_mfma_f32_16x16x32_bf16 a[96:99], a[208:211], v[204:207], a[96:99]\n"
    "s_waitcnt lgkmcnt(1)\n"
    "v_cvt_pk_bf16_f32 v190, v192, v193\n"
    "v_cvt_pk_bf16_f32 v191, v194, v195\n"
    "v_mfma_f32_16x16x32_bf16 a[96:99], a[212:215], v[208:211], a[96:99]\n"
    "ds_write_b128 v252, v[188:191] offset:0\n"
    "v_mfma_f32_16x16x32_bf16 a[116:119], v[32:35], v[80:83], a[116:119]\n"
    "v_cvt_pk_bf16_f32 v132, v132, v133\n"
    "v_accvgpr_read_b32 v135, a114\n"
    "v_mfma_f32_16x16x32_bf16 a[84:87], v[16:19], v[80:83], a[84:87]\n"
    "v_accvgpr_read_b32 v131, a113\n"
    "v_accvgpr_read_b32 v136, a67\n"
    "v_mfma_f32_16x16x32_bf16 a[100:103], v[20:23], v[76:79], a[100:103]\n"
    "ds_read_b128 v[200:203], v3 offset:1024\n"
    "ds_read_b128 v[192:195], v3 offset:0\n"
    "v_mfma_f32_16x16x32_bf16 a[68:71], v[4:7], v[76:79], a[68:71]\n"
    "v_accvgpr_read_b32 v137, a83\n"
    "v_accvgpr_read_b32 v124, a64\n"
    "v_mfma_f32_16x16x32_bf16 a[68:71], v[8:11], v[80:83], a[68:71]\n"
    "v_fma_f32 v124, v124, v109, v111\n"
    "v_fma_f32 v125, v125, v109, v111\n"
    "v_mfma_f32_16x16x32_bf16 a[116:119], v[28:31], v[76:79], a[116:119]\n"
    "v_accvgpr_read_b32 v127, a112\n"
    "v_accvgpr_read_b32 v130, a97\n"
    "v_mfma_f32_16x16x32_bf16 a[100:103], v[24:27], v[80:83], a[100:103]\n"
    "v_fma_f32 v130, v130, v109, v111\n"
    "v_fma_f32 v131, v131, v109, v111\n"
    "v_mfma_f32_16x16x32_bf16 a[84:87], v[12:15], v[76:79], a[84:87]\n"
    "v_cvt_pk_bf16_f32 v124, v124, v125\n"
    "v_accvgpr_read_b32 v126, a96\n"
    "v_mfma_f32_16x16x32_bf16 a[116:119], a[220:223], v[216:219], a[116:119]\n"
    "v_fma_f32 v126, v126, v109, v111\n"
    "v_fma_f32 v127, v127, v109, v111\n"
    "v_mfma_f32_16x16x32_bf16 a[116:119], a[216:219], v[212:215], a[116:119]\n"
    "s_nop 0\n"
    "v_accvgpr_read_b32 v139, a115\n"
    "v_cvt_pk_bf16_f32 v125, v126, v127\n"
    "v_mfma_f32_16x16x32_bf16 a[100:103], a[208:211], v[212:215], a[100:103]\n"
    "s_waitcnt lgkmcnt(0)\n"
    "buffer_store_dwordx4 v[192:195], v244, s[24:27], s78 offen offset:0 sc1\n"
    "v_cvt_pk_bf16_f32 v127, v130, v131\n"
    "v_cvt_pk_bf16_f32 v126, v128, v129\n"
    "v_accvgpr_read_b32 v134, a98\n"
    "v_accvgpr_read_b32 v138, a99\n"
    "v_mfma_f32_16x16x32_bf16 a[84:87], a[204:207], v[216:219], a[84:87]\n"
    "v_fma_f32 v138, v138, v109, v111\n"
    "v_fma_f32 v139, v139, v109, v111\n"
    "v_mfma_f32_16x16x32_bf16 a[84:87], a[200:203], v[212:215], a[84:87]\n"
    "v_fma_f32 v134, v134, v109, v111\n"
    "v_fma_f32 v135, v135, v109, v111\n"
    "v_mfma_f32_16x16x32_bf16 a[68:71], a[192:195], v[212:215], a[68:71]\n"
    "buffer_store_dwordx4 v[200:203], v244, s[24:27], s78 offen offset:3072 sc1\n"
    "ds_write_b128 v252, v[124:127] offset:2048\n"
    "v_mfma_f32_16x16x32_bf16 a[88:91], v[16:19], v[88:91], a[88:91]\n"
    "v_fma_f32 v136, v136, v109, v111\n"
    "v_fma_f32 v137, v137, v109, v111\n"
    "v_mfma_f32_16x16x32_bf16 a[100:103], a[212:215], v[216:219], a[100:103]\n"
    "v_accvgpr_read_b32 v155, a119\n"
    "v_accvgpr_read_b32 v153, a87\n"
    "v_mfma_f32_16x16x32_bf16 a[120:123], v[32:35], v[88:91], a[120:123]\n"
    "v_accvgpr_read_b32 v151, a118\n"
    "v_cvt_pk_bf16_f32 v133, v134, v135\n"
    "v_mfma_f32_16x16x32_bf16 a[68:71], a[196:199], v[216:219], a[68:71]\n"
    "v_cvt_pk_bf16_f32 v135, v138, v139\n"
    "v_cvt_pk_bf16_f32 v134, v136, v137\n"
    "v_mfma_f32_16x16x32_bf16 a[120:123], v[28:31], v[84:87], a[120:123]\n"
    "ds_write_b128 v2, v[132:135] offset:2048\n"
    "v_mfma_f32_16x16x32_bf16 a[72:75], v[4:7], v[84:87], a[72:75]\n"
    "v_accvgpr_read_b32 v154, a103\n"
    "v_accvgpr_read_b32 v142, a100\n"
    "v_mfma_f32_16x16x32_bf16 a[88:91], v[12:15], v[84:87], a[88:91]\n"
    "v_accvgpr_read_b32 v145, a85\n"
    "v_accvgpr_read_b32 v152, a71\n"
    "v_mfma_f32_16x16x32_bf16 a[120:123], a[220:223], v[224:227], a[120:123]\n"
    "ds_read_b128 v[136:139], v3 offset:3072\n"
    "v_accvgpr_read_b32 v146, a101\n"
    "v_mfma_f32_16x16x32_bf16 a[72:75], v[8:11], v[88:91], a[72:75]\n"
    "ds_read_b128 v[128:131], v3 offset:2048\n"
    "v_accvgpr_read_b32 v140, a68\n"
    "v_mfma_f32_16x16x32_bf16 a[120:123], a[216:219], v[220:223], a[120:123]\n"
    "v_accvgpr_read_b32 v143, a116\n"
    "v_accvgpr_read_b32 v141, a84\n"
    "v_mfma_f32_16x16x32_bf16 a[72:75], a[192:195], v[220:223], a[72:75]\n"
    "v_accvgpr_read_b32 v150, a102\n"
    "v_accvgpr_read_b32 v149, a86\n"
    "v_accvgpr_read_b32 v144, a69\n"
    "v_accvgpr_read_b32 v147, a117\n"
    "v_fma_f32 v140, v140, v113, v115\n"
    "v_fma_f32 v141, v141, v113, v115\n"
    "v_pk_fma_f32 v[154:155], v[154:155], v[112:113], v[114:115] op_sel:[0,1,1] op_sel_hi:[1,1,1]\n"
    "v_pk_fma_f32 v[146:147], v[146:147], v[112:113], v[114:115] op_sel:[0,1,1] op_sel_hi:[1,1,1]\n"
    "v_accvgpr_read_b32 v148, a70\n"
    "v_pk_fma_f32 v[142:143], v[142:143], v[112:113], v[114:115] op_sel:[0,1,1] op_sel_hi:[1,1,1]\n"
    "v_pk_fma_f32 v[144:145], v[144:145], v[112:113], v[114:115] op_sel:[0,1,1] op_sel_hi:[1,1,1]\n"
    "v_cvt_pk_bf16_f32 v140, v140, v141\n"
    "v_cvt_pk_bf16_f32 v141, v142, v143\n"
    "v_mfma_f32_16x16x32_bf16 a[104:107], v[20:23], v[84:87], a[104:107]\n"
    "s_nop 0\n"
    "v_cvt_pk_bf16_f32 v142, v144, v145\n"
    "v_cvt_pk_bf16_f32 v143, v146, v147\n"
    "v_mfma_f32_16x16x32_bf16 a[72:75], a[196:199], v[224:227], a[72:75]\n"
    "s_waitcnt lgkmcnt(1)\n"
    "buffer_store_dwordx4 v[136:139], v244, s[24:27], s75 offen offset:3200 sc1\n"
    "ds_write_b128 v252, v[140:143] offset:4096\n"
    "v_pk_fma_f32 v[148:149], v[148:149], v[112:113], v[114:115] op_sel:[0,1,1] op_sel_hi:[1,1,1]\n"
    "v_pk_fma_f32 v[150:151], v[150:151], v[112:113], v[114:115] op_sel:[0,1,1] op_sel_hi:[1,1,1]\n"
    "v_pk_fma_f32 v[152:153], v[152:153], v[112:113], v[114:115] op_sel:[0,1,1] op_sel_hi:[1,1,1]\n"
    "v_cvt_pk_bf16_f32 v148, v148, v149\n"
    "v_cvt_pk_bf16_f32 v149, v150, v151\n"
    "v_mfma_f32_16x16x32_bf16 a[88:91], a[204:207], v[224:227], a[88:91]\n"
    "v_cvt_pk_bf16_f32 v150, v152, v153\n"
    "v_cvt_pk_bf16_f32 v151, v154, v155\n"
    "v_mfma_f32_16x16x32_bf16 a[88:91], a[200:203], v[220:223], a[88:91]\n"
    "s_waitcnt lgkmcnt(1)\n"
    "buffer_store_dwordx4 v[128:131], v244, s[24:27], s75 offen offset:128 sc1\n"
    "ds_write_b128 v2, v[148:151] offset:4096\n"
    "v_mfma_f32_16x16x32_bf16 a[104:107], v[24:27], v[88:91], a[104:107]\n"
    "s_mov_b32 s74, 0x47000000\n"
    "v_accvgpr_read_b32 v47, a120\n"
    "v_accvgpr_read_b32 v51, a121\n"
    "v_mfma_f32_16x16x32_bf16 a[108:111], v[20:23], v[92:95], a[108:111]\n"
    "v_accvgpr_read_b32 v67, a123\n"
    "v_accvgpr_read_b32 v65, a91\n"
    "v_mfma_f32_16x16x32_bf16 a[104:107], a[208:211], v[220:223], a[104:107]\n"
    "ds_read_b128 v[152:155], v3 offset:5120\n"
    "v_accvgpr_read_b32 v63, a122\n"
    "v_mfma_f32_16x16x32_bf16 a[104:107], a[212:215], v[224:227], a[104:107]\n"
    "v_accvgpr_read_b32 v64, a75\n"
    "v_accvgpr_read_b32 v60, a74\n"
    "v_mfma_f32_16x16x32_bf16 a[124:127], v[32:35], v[96:99], a[124:127]\n"
    "ds_read_b128 v[144:147], v3 offset:4096\n"
    "v_accvgpr_read_b32 v44, a72\n"
    "v_mfma_f32_16x16x32_bf16 a[76:79], v[4:7], v[92:95], a[76:79]\n"
    "v_accvgpr_read_b32 v45, a88\n"
    "v_accvgpr_read_b32 v61, a90\n"
    "v_accvgpr_read_b32 v48, a73\n"
    "v_mfma_f32_16x16x32_bf16 a[76:79], v[8:11], v[96:99], a[76:79]\n"
    "ds_read_b128 v[32:35], v248 offset:23552\n"
    "v_accvgpr_read_b32 v46, a104\n"
    "v_mfma_f32_16x16x32_bf16 a[92:95], v[16:19], v[96:99], a[92:95]\n"
    "ds_read_b128 v[16:19], v248 offset:21504\n"
    "ds_read_b128 v[8:11], v248 offset:20480\n"
    "v_mfma_f32_16x16x32_bf16 a[124:127], v[28:31], v[92:95], a[124:127]\n"
    "v_accvgpr_read_b32 v50, a105\n"
    "v_accvgpr_read_b32 v49, a89\n"
    "v_mfma_f32_16x16x32_bf16 a[124:127], a[220:223], v[232:235], a[124:127]\n"
    "s_waitcnt lgkmcnt(4)\n"
    "ds_read_b128 v[4:7], v248 offset:8192\n"
    "buffer_store_dwordx4 v[152:155], v244, s[24:27], s76 offen offset:3200 sc1\n"
    "ds_read_b128 v[20:23], v248 offset:10240\n"
    "v_accvgpr_read_b32 v62, a106\n"
    "v_accvgpr_read_b32 v66, a107\n"
    "v_pk_fma_f32 v[44:45], v[44:45], v[116:117], v[118:119] op_sel:[0,1,1] op_sel_hi:[1,1,1]\n"
    "v_pk_fma_f32 v[60:61], v[60:61], v[116:117], v[118:119] op_sel:[0,1,1] op_sel_hi:[1,1,1]\n"
    "v_pk_fma_f32 v[62:63], v[62:63], v[116:117], v[118:119] op_sel:[0,1,1] op_sel_hi:[1,1,1]\n"
    "v_pk_fma_f32 v[48:49], v[48:49], v[116:117], v[118:119] op_sel:[0,1,1] op_sel_hi:[1,1,1]\n"
    "v_pk_fma_f32 v[46:47], v[46:47], v[116:117], v[118:119] op_sel:[0,1,1] op_sel_hi:[1,1,1]\n"
    "v_pk_fma_f32 v[50:51], v[50:51], v[116:117], v[118:119] op_sel:[0,1,1] op_sel_hi:[1,1,1]\n"
    "v_pk_fma_f32 v[64:65], v[64:65], v[116:117], v[118:119] op_sel:[0,1,1] op_sel_hi:[1,1,1]\n"
    "v_pk_fma_f32 v[66:67], v[66:67], v[116:117], v[118:119] op_sel:[0,1,1] op_sel_hi:[1,1,1]\n"
    "v_cvt_pk_bf16_f32 v44, v44, v45\n"
    "v_cvt_pk_bf16_f32 v45, v46, v47\n"
    "v_mfma_f32_16x16x32_bf16 a[108:111], v[24:27], v[96:99], a[108:111]\n"
    "ds_read_b128 v[24:27], v248 offset:22528\n"
    "ds_read_b128 v[28:31], v248 offset:11264\n"
    "v_mfma_f32_16x16x32_bf16 a[92:95], v[12:15], v[92:95], a[92:95]\n"
    "v_cvt_pk_bf16_f32 v47, v50, v51\n"
    "v_cvt_pk_bf16_f32 v46, v48, v49\n"
    "v_mfma_f32_16x16x32_bf16 a[76:79], a[192:195], v[228:231], a[76:79]\n"
    "s_waitcnt lgkmcnt(7)\n"
    "buffer_store_dwordx4 v[144:147], v244, s[24:27], s76 offen offset:128 sc1\n"
    "ds_write_b128 v252, v[44:47] offset:0\n"
    "v_mfma_f32_16x16x32_bf16 a[76:79], a[196:199], v[232:235], a[76:79]\n"
    "v_cvt_pk_bf16_f32 v60, v60, v61\n"
    "v_cvt_pk_bf16_f32 v61, v62, v63\n"
    "v_mfma_f32_16x16x32_bf16 a[124:127], a[216:219], v[228:231], a[124:127]\n"
    "v_cvt_pk_bf16_f32 v63, v66, v67\n"
    "v_cvt_pk_bf16_f32 v62, v64, v65\n"
    "v_mfma_f32_16x16x32_bf16 a[92:95], a[204:207], v[232:235], a[92:95]\n"
    "ds_read_b128 a[192:195], v249 offset:8192\n"
    "ds_read_b128 a[196:199], v249 offset:20480\n"
    "v_mfma_f32_16x16x32_bf16 a[108:111], a[208:211], v[228:231], a[108:111]\n"
    "ds_write_b128 v2, v[60:63] offset:0\n"
    "v_mfma_f32_16x16x32_bf16 a[92:95], a[200:203], v[228:231], a[92:95]\n"
    "s_waitcnt lgkmcnt(6)\n"
    "v_accvgpr_read_b32 v39, a124\n"
    "v_accvgpr_read_b32 v56, a79\n"
    "v_mfma_f32_16x16x32_bf16 a[160:163], v[20:23], v[68:71], a[160:163]\n"
    "v_accvgpr_read_b32 v59, a127\n"
    "v_accvgpr_read_b32 v52, a78\n"
    "v_mfma_f32_16x16x32_bf16 a[108:111], a[212:215], v[232:235], a[108:111]\n"
    "ds_read_b128 v[48:51], v3 offset:0\n"
    "ds_read_b128 a[200:203], v249 offset:9216\n"
    "v_mfma_f32_16x16x32_bf16 a[128:131], v[4:7], v[68:71], a[128:131]\n"
    "ds_read_b128 v[12:15], v248 offset:9216\n"
    "ds_read_b128 v[64:67], v3 offset:1024\n"
    "v_mfma_f32_16x16x32_bf16 a[128:131], v[8:11], v[72:75], a[128:131]\n"
    "s_waitcnt lgkmcnt(9)\n"
    "v_accvgpr_read_b32 v36, a76\n"
    "v_accvgpr_read_b32 v57, a95\n"
    "v_accvgpr_read_b32 v55, a126\n"
    "v_mfma_f32_16x16x32_bf16 a[160:163], v[24:27], v[72:75], a[160:163]\n"
    "v_accvgpr_read_b32 v40, a77\n"
    "v_accvgpr_read_b32 v54, a110\n"
    "v_mfma_f32_16x16x32_bf16 a[144:147], v[16:19], v[72:75], a[144:147]\n"
    "ds_read_b128 a[220:223], v249 offset:23552\n"
    "ds_read_b128 a[208:211], v249 offset:10240\n"
    "v_mfma_f32_16x16x32_bf16 a[176:179], v[32:35], v[72:75], a[176:179]\n"
    "s_waitcnt lgkmcnt(8)\n"
    "v_accvgpr_read_b32 v58, a111\n"
    "v_accvgpr_read_b32 v41, a93\n"
    "v_mfma_f32_16x16x32_bf16 a[128:131], a[192:195], v[204:207], a[128:131]\n"
    "s_waitcnt lgkmcnt(7)\n"
    "v_accvgpr_read_b32 v37, a92\n"
    "v_accvgpr_read_b32 v43, a125\n"
    "v_accvgpr_read_b32 v53, a94\n"
    "v_mfma_f32_16x16x32_bf16 a[128:131], a[196:199], v[208:211], a[128:131]\n"
    "s_waitcnt lgkmcnt(5)\n"
    "buffer_store_dwordx4 v[48:51], v244, s[24:27], s77 offen offset:128 sc1\n"
    "ds_read_b128 a[204:207], v249 offset:21504\n"
    "ds_read_b128 a[216:219], v249 offset:11264\n"
    "ds_read_b128 a[212:215], v249 offset:22528\n"
    "v_accvgpr_read_b32 v42, a109\n"
    "v_pk_fma_f32 v[36:37], v[36:37], v[120:121], v[122:123] op_sel:[0,1,1] op_sel_hi:[1,1,1]\n"
    "v_pk_fma_f32 v[54:55], v[54:55], v[120:121], v[122:123] op_sel:[0,1,1] op_sel_hi:[1,1,1]\n"
    "v_pk_fma_f32 v[52:53], v[52:53], v[120:121], v[122:123] op_sel:[0,1,1] op_sel_hi:[1,1,1]\n"
    "v_accvgpr_read_b32 v38, a108\n"
    "v_pk_fma_f32 v[40:41], v[40:41], v[120:121], v[122:123] op_sel:[0,1,1] op_sel_hi:[1,1,1]\n"
    "v_pk_fma_f32 v[38:39], v[38:39], v[120:121], v[122:123] op_sel:[0,1,1] op_sel_hi:[1,1,1]\n"
    "v_pk_fma_f32 v[56:57], v[56:57], v[120:121], v[122:123] op_sel:[0,1,1] op_sel_hi:[1,1,1]\n"
    "v_pk_fma_f32 v[58:59], v[58:59], v[120:121], v[122:123] op_sel:[0,1,1] op_sel_hi:[1,1,1]\n"
    "v_pk_fma_f32 v[42:43], v[42:43], v[120:121], v[122:123] op_sel:[0,1,1] op_sel_hi:[1,1,1]\n"
    "v_cvt_pk_bf16_f32 v36, v36, v37\n"
    "v_cvt_pk_bf16_f32 v37, v38, v39\n"
    "v_mfma_f32_16x16x32_bf16 a[176:179], v[28:31], v[68:71], a[176:179]\n"
    "s_waitcnt lgkmcnt(6)\n"
    "v_cvt_pk_bf16_f32 v38, v40, v41\n"
    "v_cvt_pk_bf16_f32 v39, v42, v43\n"
    "v_mfma_f32_16x16x32_bf16 a[144:147], v[12:15], v[68:71], a[144:147]\n"
    "s_waitcnt lgkmcnt(3)\n"
    "buffer_store_dwordx4 v[64:67], v244, s[24:27], s77 offen offset:3200 sc1\n"
    "ds_write_b128 v252, v[36:39] offset:2048\n"
    "v_mfma_f32_16x16x32_bf16 a[160:163], a[208:211], v[204:207], a[160:163]\n"
    "v_cvt_pk_bf16_f32 v52, v52, v53\n"
    "v_cvt_pk_bf16_f32 v53, v54, v55\n"
    "v_mfma_f32_16x16x32_bf16 a[176:179], a[220:223], v[208:211], a[176:179]\n"
    "s_waitcnt lgkmcnt(1)\n"
    "v_cvt_pk_bf16_f32 v54, v56, v57\n"
    "v_cvt_pk_bf16_f32 v55, v58, v59\n"
    "v_mfma_f32_16x16x32_bf16 a[160:163], a[212:215], v[208:211], a[160:163]\n"
    "ds_write_b128 v2, v[52:55] offset:2048\n"
    "v_mfma_f32_16x16x32_bf16 a[176:179], a[216:219], v[204:207], a[176:179]\n"
    "v_accvgpr_read_b32 v156, a128\n"
    "v_mfma_f32_16x16x32_bf16 a[148:151], v[16:19], v[80:83], a[148:151]\n"
    "v_mov_b32_e64 v250, 0\n"
    "v_accvgpr_read_b32 v168, a131\n"
    "v_mfma_f32_16x16x32_bf16 a[144:147], a[204:207], v[208:211], a[144:147]\n"
    "ds_read_b128 v[56:59], v3 offset:3072\n"
    "ds_read_b128 v[40:43], v3 offset:2048\n"
    "v_mfma_f32_16x16x32_bf16 a[144:147], a[200:203], v[204:207], a[144:147]\n"
    "v_accvgpr_read_b32 v158, a160\n"
    "v_accvgpr_read_b32 v162, a161\n"
    "v_mfma_f32_16x16x32_bf16 a[180:183], v[32:35], v[80:83], a[180:183]\n"
    "v_accvgpr_read_b32 v160, a129\n"
    "v_accvgpr_read_b32 v163, a177\n"
    "v_mfma_f32_16x16x32_bf16 a[164:167], v[20:23], v[76:79], a[164:167]\n"
    "v_accvgpr_read_b32 v164, a130\n"
    "v_mfma_f32_16x16x32_bf16 a[132:135], v[4:7], v[76:79], a[132:135]\n"
    "v_accvgpr_read_b32 v169, a147\n"
    "v_accvgpr_read_b32 v166, a162\n"
    "v_mfma_f32_16x16x32_bf16 a[148:151], v[12:15], v[76:79], a[148:151]\n"
    "v_accvgpr_read_b32 v161, a145\n"
    "v_accvgpr_read_b32 v159, a176\n"
    "v_mfma_f32_16x16x32_bf16 a[148:151], a[204:207], v[216:219], a[148:151]\n"
    "v_accvgpr_read_b32 v157, a144\n"
    "v_accvgpr_read_b32 v170, a163\n"
    "v_mfma_f32_16x16x32_bf16 a[148:151], a[200:203], v[212:215], a[148:151]\n"
    "v_accvgpr_read_b32 v167, a178\n"
    "v_accvgpr_read_b32 v171, a179\n"
    "v_mfma_f32_16x16x32_bf16 a[132:135], v[8:11], v[80:83], a[132:135]\n"
    "s_waitcnt lgkmcnt(1)\n"
    "buffer_store_dwordx4 v[56:59], v244, s[24:27], s78 offen offset:3200 sc1\n"
    "v_accvgpr_read_b32 v165, a146\n"
    "v_fma_f32 v164, v164, v109, v111\n"
    "v_fma_f32 v165, v165, v109, v111\n"
    "v_cvt_pk_bf16_f32 v164, v164, v165\n"
    "v_pk_fma_f32 v[166:167], v[166:167], v[108:109], v[110:111] op_sel:[0,1,1] op_sel_hi:[1,1,1]\n"
    "v_cvt_pk_bf16_f32 v165, v166, v167\n"
    "v_pk_fma_f32 v[168:169], v[168:169], v[108:109], v[110:111] op_sel:[0,1,1] op_sel_hi:[1,1,1]\n"
    "v_pk_fma_f32 v[170:171], v[170:171], v[108:109], v[110:111] op_sel:[0,1,1] op_sel_hi:[1,1,1]\n"
    "v_pk_fma_f32 v[160:161], v[160:161], v[108:109], v[110:111] op_sel:[0,1,1] op_sel_hi:[1,1,1]\n"
    "v_pk_fma_f32 v[156:157], v[156:157], v[108:109], v[110:111] op_sel:[0,1,1] op_sel_hi:[1,1,1]\n"
    "v_pk_fma_f32 v[162:163], v[162:163], v[108:109], v[110:111] op_sel:[0,1,1] op_sel_hi:[1,1,1]\n"
    "v_pk_fma_f32 v[158:159], v[158:159], v[108:109], v[110:111] op_sel:[0,1,1] op_sel_hi:[1,1,1]\n"
    "v_cvt_pk_bf16_f32 v166, v168, v169\n"
    "v_cvt_pk_bf16_f32 v167, v170, v171\n"
    "v_mfma_f32_16x16x32_bf16 a[132:135], a[192:195], v[212:215], a[132:135]\n"
    "s_waitcnt lgkmcnt(0)\n"
    "buffer_store_dwordx4 v[40:43], v244, s[24:27], s78 offen offset:128 sc1\n"
    "ds_write_b128 v2, v[164:167] offset:4096\n"
    "v_mfma_f32_16x16x32_bf16 a[132:135], a[196:199], v[216:219], a[132:135]\n"
    "v_cvt_pk_bf16_f32 v156, v156, v157\n"
    "v_cvt_pk_bf16_f32 v157, v158, v159\n"
    "v_mfma_f32_16x16x32_bf16 a[180:183], v[28:31], v[76:79], a[180:183]\n"
    "v_cvt_pk_bf16_f32 v158, v160, v161\n"
    "v_cvt_pk_bf16_f32 v159, v162, v163\n"
    "v_mfma_f32_16x16x32_bf16 a[180:183], a[220:223], v[216:219], a[180:183]\n"
    "ds_write_b128 v252, v[156:159] offset:4096\n"
    "v_mfma_f32_16x16x32_bf16 a[180:183], a[216:219], v[212:215], a[180:183]\n"
    "v_accvgpr_read_b32 v184, a135\n"
    "v_accvgpr_read_b32 v177, a149\n"
    "v_accvgpr_read_b32 v180, a134\n"
    "v_accvgpr_read_b32 v181, a150\n"
    "v_mfma_f32_16x16x32_bf16 a[164:167], v[24:27], v[80:83], a[164:167]\n"
    "ds_read_b128 v[168:171], v3 offset:5120\n"
    "v_accvgpr_read_b32 v173, a148\n"
    "v_mfma_f32_16x16x32_bf16 a[184:187], v[32:35], v[88:91], a[184:187]\n"
    "v_fma_f32 v180, v180, v113, v115\n"
    "v_fma_f32 v181, v181, v113, v115\n"
    "v_mfma_f32_16x16x32_bf16 a[164:167], a[208:211], v[212:215], a[164:167]\n"
    "v_accvgpr_read_b32 v179, a181\n"
    "v_accvgpr_read_b32 v185, a151\n"
    "v_mfma_f32_16x16x32_bf16 a[164:167], a[212:215], v[216:219], a[164:167]\n"
    "ds_read_b128 v[160:163], v3 offset:4096\n"
    "v_fma_f32 v184, v184, v113, v115\n"
    "v_fma_f32 v185, v185, v113, v115\n"
    "v_mfma_f32_16x16x32_bf16 a[136:139], v[4:7], v[84:87], a[136:139]\n"
    "v_accvgpr_read_b32 v176, a133\n"
    "v_accvgpr_read_b32 v187, a183\n"
    "v_accvgpr_read_b32 v172, a132\n"
    "v_mfma_f32_16x16x32_bf16 a[136:139], v[8:11], v[88:91], a[136:139]\n"
    "v_accvgpr_read_b32 v183, a182\n"
    "v_accvgpr_read_b32 v186, a167\n"
    "v_mfma_f32_16x16x32_bf16 a[136:139], a[192:195], v[220:223], a[136:139]\n"
    "v_accvgpr_read_b32 v174, a164\n"
    "v_accvgpr_read_b32 v178, a165\n"
    "v_mfma_f32_16x16x32_bf16 a[168:171], v[20:23], v[84:87], a[168:171]\n"
    "s_waitcnt lgkmcnt(1)\n"
    "buffer_store_dwordx4 v[168:171], v244, s[24:27], s75 offen offset:3328 sc1\n"
    "v_accvgpr_read_b32 v175, a180\n"
    "v_accvgpr_read_b32 v182, a166\n"
    "v_fma_f32 v178, v178, v113, v115\n"
    "v_fma_f32 v179, v179, v113, v115\n"
    "v_pk_fma_f32 v[182:183], v[182:183], v[112:113], v[114:115] op_sel:[0,1,1] op_sel_hi:[1,1,1]\n"
    "v_cvt_pk_bf16_f32 v180, v180, v181\n"
    "v_pk_fma_f32 v[186:187], v[186:187], v[112:113], v[114:115] op_sel:[0,1,1] op_sel_hi:[1,1,1]\n"
    "v_pk_fma_f32 v[174:175], v[174:175], v[112:113], v[114:115] op_sel:[0,1,1] op_sel_hi:[1,1,1]\n"
    "v_pk_fma_f32 v[176:177], v[176:177], v[112:113], v[114:115] op_sel:[0,1,1] op_sel_hi:[1,1,1]\n"
    "v_cvt_pk_bf16_f32 v181, v182, v183\n"
    "v_pk_fma_f32 v[172:173], v[172:173], v[112:113], v[114:115] op_sel:[0,1,1] op_sel_hi:[1,1,1]\n"
    "v_cvt_pk_bf16_f32 v182, v184, v185\n"
    "v_cvt_pk_bf16_f32 v183, v186, v187\n"
    "v_mfma_f32_16x16x32_bf16 a[168:171], v[24:27], v[88:91], a[168:171]\n"
    "s_waitcnt lgkmcnt(0)\n"
    "buffer_store_dwordx4 v[160:163], v244, s[24:27], s75 offen offset:256 sc1\n"
    "ds_write_b128 v2, v[180:183] offset:0\n"
    "v_mfma_f32_16x16x32_bf16 a[136:139], a[196:199], v[224:227], a[136:139]\n"
    "s_nop 0\n"
    "v_cvt_pk_bf16_f32 v172, v172, v173\n"
    "v_cvt_pk_bf16_f32 v173, v174, v175\n"
    "v_mfma_f32_16x16x32_bf16 a[184:187], v[28:31], v[84:87], a[184:187]\n"
    "v_cvt_pk_bf16_f32 v175, v178, v179\n"
    "v_cvt_pk_bf16_f32 v174, v176, v177\n"
    "v_mfma_f32_16x16x32_bf16 a[168:171], a[208:211], v[220:223], a[168:171]\n"
    "ds_write_b128 v252, v[172:175] offset:0\n"
    "v_mfma_f32_16x16x32_bf16 a[168:171], a[212:215], v[224:227], a[168:171]\n"
    "s_nop 0\n"
    "v_accvgpr_read_b32 v196, a138\n"
    "v_accvgpr_read_b32 v188, a136\n"
    "v_mfma_f32_16x16x32_bf16 a[184:187], a[220:223], v[224:227], a[184:187]\n"
    "v_accvgpr_read_b32 v200, a139\n"
    "s_nop 0\n"
    "v_mfma_f32_16x16x32_bf16 a[184:187], a[216:219], v[220:223], a[184:187]\n"
    "ds_read_b128 v[176:179], v3 offset:0\n"
    "ds_read_b128 v[184:187], v3 offset:1024\n"
    "v_mfma_f32_16x16x32_bf16 a[152:155], v[16:19], v[88:91], a[152:155]\n"
    "v_accvgpr_read_b32 v194, a169\n"
    "v_fma_f32 v250, |v110|, 4.0, v250\n"
    "v_mfma_f32_16x16x32_bf16 a[152:155], v[12:15], v[84:87], a[152:155]\n"
    "v_accvgpr_read_b32 v198, a170\n"
    "v_accvgpr_read_b32 v190, a168\n"
    "v_mfma_f32_16x16x32_bf16 a[152:155], a[204:207], v[224:227], a[152:155]\n"
    "s_nop 0\n"
    "v_accvgpr_read_b32 v192, a137\n"
    "v_accvgpr_read_b32 v199, a186\n"
    "v_mfma_f32_16x16x32_bf16 a[152:155], a[200:203], v[220:223], a[152:155]\n"
    "s_nop 0\n"
    "v_fma_f32 v250, v108, 4.0, v250\n"
    "v_accvgpr_read_b32 v203, a187\n"
    "v_mfma_f32_16x16x32_bf16 a[140:143], v[4:7], v[92:95], a[140:143]\n"
    "v_fma_f32 v250, v109, s74, v250\n"
    "v_accvgpr_read_b32 v191, a184\n"
    "v_accvgpr_read_b32 v195, a185\n"
    "v_mfma_f32_16x16x32_bf16 a[156:159], v[16:19], v[96:99], a[156:159]\n"
    "s_nop 0\n"
    "v_accvgpr_read_b32 v201, a155\n"
    "v_fma_f32 v200, v200, v117, v119\n"
    "v_fma_f32 v201, v201, v117, v119\n"
    "v_mfma_f32_16x16x32_bf16 a[172:175], v[20:23], v[92:95], a[172:175]\n"
    "s_waitcnt lgkmcnt(1)\n"
    "buffer_store_dwordx4 v[176:179], v244, s[24:27], s76 offen offset:256 sc1\n"
    "v_accvgpr_read_b32 v189, a152\n"
    "v_accvgpr_read_b32 v202, a171\n"
    "v_accvgpr_read_b32 v193, a153\n"
    "v_fma_f32 v198, v198, v117, v119\n"
    "v_fma_f32 v199, v199, v117, v119\n"
    "v_accvgpr_read_b32 v197, a154\n"
    "v_pk_fma_f32 v[196:197], v[196:197], v[116:117], v[118:119] op_sel:[0,1,1] op_sel_hi:[1,1,1]\n"
    "v_pk_fma_f32 v[202:203], v[202:203], v[116:117], v[118:119] op_sel:[0,1,1] op_sel_hi:[1,1,1]\n"
    "v_pk_fma_f32 v[192:193], v[192:193], v[116:117], v[118:119] op_sel:[0,1,1] op_sel_hi:[1,1,1]\n"
    "v_cvt_pk_bf16_f32 v196, v196, v197\n"
    "v_cvt_pk_bf16_f32 v197, v198, v199\n"
    "v_cvt_pk_bf16_f32 v199, v202, v203\n"
    "v_cvt_pk_bf16_f32 v198, v200, v201\n"
    "v_mfma_f32_16x16x32_bf16 a[156:159], v[12:15], v[92:95], a[156:159]\n"
    "s_waitcnt lgkmcnt(0)\n"
    "ds_write_b128 v2, v[196:199] offset:2048\n"
    "buffer_store_dwordx4 v[184:187], v244, s[24:27], s76 offen offset:3328 sc1\n"
    "v_pk_fma_f32 v[188:189], v[188:189], v[116:117], v[118:119] op_sel:[0,1,1] op_sel_hi:[1,1,1]\n"
    "v_pk_fma_f32 v[194:195], v[194:195], v[116:117], v[118:119] op_sel:[0,1,1] op_sel_hi:[1,1,1]\n"
    "v_pk_fma_f32 v[190:191], v[190:191], v[116:117], v[118:119] op_sel:[0,1,1] op_sel_hi:[1,1,1]\n"
    "v_cvt_pk_bf16_f32 v188, v188, v189\n"
    "v_cvt_pk_bf16_f32 v189, v190, v191\n"
    "v_mfma_f32_16x16x32_bf16 a[156:159], a[204:207], v[232:235], a[156:159]\n"
    "v_cvt_pk_bf16_f32 v190, v192, v193\n"
    "v_cvt_pk_bf16_f32 v191, v194, v195\n"
    "v_mfma_f32_16x16x32_bf16 a[156:159], a[200:203], v[228:231], a[156:159]\n"
    "ds_write_b128 v252, v[188:191] offset:2048\n"
    "v_mfma_f32_16x16x32_bf16 a[140:143], v[8:11], v[96:99], a[140:143]\n"
    "v_fma_f32 v250, |v114|, 4.0, v250\n"
    "v_fma_f32 v250, v112, 4.0, v250\n"
    "v_mfma_f32_16x16x32_bf16 a[172:175], v[24:27], v[96:99], a[172:175]\n"
    "v_mfma_f32_16x16x32_bf16 a[140:143], a[192:195], v[228:231], a[140:143]\n"
    "ds_read_b128 v[200:203], v3 offset:3072\n"
    "v_mfma_f32_16x16x32_bf16 a[140:143], a[196:199], v[232:235], a[140:143]\n"
    "v_accvgpr_read_b32 v129, a157\n"
    "v_fma_f32 v250, v113, s74, v250\n"
    "v_mfma_f32_16x16x32_bf16 a[188:191], v[32:35], v[96:99], a[188:191]\n"
    "ds_read_b128 v[192:195], v3 offset:2048\n"
    "v_cmp_eq_u64 s[56:57], v[236:237], v[238:239]\n"
    "v_mfma_f32_16x16x32_bf16 a[172:175], a[208:211], v[228:231], a[172:175]\n"
    "v_fma_f32 v250, |v118|, 4.0, v250\n"
    "v_mfma_f32_16x16x32_bf16 a[172:175], a[212:215], v[232:235], a[172:175]\n"
    "v_fma_f32 v250, v116, 4.0, v250\n"
    "v_mfma_f32_16x16x32_bf16 a[188:191], v[28:31], v[92:95], a[188:191]\n"
    "v_fma_f32 v250, v117, s74, v250\n"
    "v_mfma_f32_16x16x32_bf16 a[188:191], a[220:223], v[232:235], a[188:191]\n"
    "v_fma_f32 v250, |v122|, 4.0, v250\n"
    "v_accvgpr_read_b32 v132, a142\n"
    "v_accvgpr_read_b32 v133, a158\n"
    "v_accvgpr_read_b32 v128, a141\n"
    "v_mfma_f32_16x16x32_bf16 a[188:191], a[216:219], v[228:231], a[188:191]\n"
    "s_waitcnt lgkmcnt(1)\n"
    "buffer_store_dwordx4 v[200:203], v244, s[24:27], s77 offen offset:3328 sc1\n"
    "v_accvgpr_read_b32 v125, a156\n"
    "v_accvgpr_read_b32 v126, a172\n"
    "v_fma_f32 v250, v120, 4.0, v250\n"
    "v_accvgpr_read_b32 v124, a140\n"
    "v_pk_fma_f32 v[124:125], v[124:125], v[120:121], v[122:123] op_sel:[0,1,1] op_sel_hi:[1,1,1]\n"
    "v_accvgpr_read_b32 v138, a175\n"
    "v_accvgpr_read_b32 v137, a159\n"
    "v_accvgpr_read_b32 v139, a191\n"
    "v_pk_fma_f32 v[138:139], v[138:139], v[120:121], v[122:123] op_sel:[0,1,1] op_sel_hi:[1,1,1]\n"
    "v_accvgpr_read_b32 v127, a188\n"
    "v_accvgpr_read_b32 v130, a173\n"
    "v_accvgpr_read_b32 v136, a143\n"
    "v_accvgpr_read_b32 v131, a189\n"
    "v_pk_fma_f32 v[126:127], v[126:127], v[120:121], v[122:123] op_sel:[0,1,1] op_sel_hi:[1,1,1]\n"
    "v_pk_fma_f32 v[130:131], v[130:131], v[120:121], v[122:123] op_sel:[0,1,1] op_sel_hi:[1,1,1]\n"
    "v_cvt_pk_bf16_f32 v124, v124, v125\n"
    "v_cvt_pk_bf16_f32 v125, v126, v127\n"
    "v_accvgpr_read_b32 v134, a174\n"
    "v_cvt_pk_bf16_f32 v127, v130, v131\n"
    "v_accvgpr_read_b32 v135, a190\n"
    "s_or_b64 s[56:57], s[56:57], s[34:35]\n"
    "s_waitcnt lgkmcnt(0)\n"
    "buffer_store_dwordx4 v[192:195], v244, s[24:27], s77 offen offset:256 sc1\n"
    "v_pk_fma_f32 v[128:129], v[128:129], v[120:121], v[122:123] op_sel:[0,1,1] op_sel_hi:[1,1,1]\n"
    "v_pk_fma_f32 v[134:135], v[134:135], v[120:121], v[122:123] op_sel:[0,1,1] op_sel_hi:[1,1,1]\n"
    "v_cvt_pk_bf16_f32 v126, v128, v129\n"
    "ds_write_b128 v252, v[124:127] offset:4096\n"
    "v_pk_fma_f32 v[132:133], v[132:133], v[120:121], v[122:123] op_sel:[0,1,1] op_sel_hi:[1,1,1]\n"
    "v_fma_f32 v250, v121, s74, v250\n"
    "v_pk_fma_f32 v[136:137], v[136:137], v[120:121], v[122:123] op_sel:[0,1,1] op_sel_hi:[1,1,1]\n"
    "v_cvt_pk_bf16_f32 v132, v132, v133\n"
    "v_cvt_pk_bf16_f32 v133, v134, v135\n"
    "v_cvt_pk_bf16_f32 v135, v138, v139\n"
    "v_cmp_nlt_f32 s[54:55], |v250|, s73\n"
    "v_cvt_pk_bf16_f32 v134, v136, v137\n"
    "s_or_b64 s[54:55], s[54:55], s[56:57]\n"
    "ds_write_b128 v2, v[132:135] offset:4096\n"
    "ds_read_b128 v[136:139], v3 offset:5120\n"
    "s_or_b32 %0, s54, s55\n"
    "ds_read_b128 v[128:131], v3 offset:4096\n"
    "s_waitcnt lgkmcnt(1)\n"
    "buffer_store_dwordx4 v[136:139], v244, s[24:27], s78 offen offset:3328 sc1\n"
    "s_waitcnt lgkmcnt(0)\n"
    "buffer_store_dwordx4 v[128:131], v244, s[24:27], s78 offen offset:256 sc1\n"
    "s_cmp_eq_u32 %0, 0\n"
    "s_cbranch_scc1 .Lfin\n"
    "s_waitcnt vmcnt(0)\n"
    ".Lfin:\n"
    ".Lend:\n"
    ".Lend2:\n"
    : "=&s"(bad), "+s"(ka) : "s"(b), "v"(tid)
    : "memory", "vcc", "s16", "s17", "s18", "s19", "s20", "s21", "s22", "s23", "s24", "s25", "s26", "s27", "s28", "s29", "s30", "s31", "s34", "s35", "s36", "s37", "s38", "s39", "s40", "s41", "s42", "s43", "s44", "s45", "s46", "s47", "s48", "s49", "s50", "s51", "s52", "s53", "s54", "s55", "s56", "s57", "s58", "s59", "s60", "s61", "s62", "s63", "s64", "s65", "s66", "s67", "s68", "s69", "s70", "s71", "s72", "s73", "s74", "s75", "s76", "s77", "s78", "s79", "s80", "s81", "s82", "s83", "s84", "s85", "s86", "s87", "s88", "s89", "s90", "s91", "s92", "s93", "s94", "s95", "s96", "s97", "s98", "s99", "v1", "v2", "v3", "v4", "v5", "v6", "v7", "v8", "v9", "v10", "v11", "v12", "v13", "v14", "v15", "v16", "v17", "v18", "v19", "v20", "v21", "v22", "v23", "v24", "v25", "v26", "v27", "v28", "v29", "v30", "v31", "v32", "v33", "v34", "v35", "v36", "v37", "v38", "v39", "v40", "v41", "v42", "v43", "v44", "v45", "v46", "v47", "v48", "v49", "v50", "v51", "v52", "v53", "v54", "v55", "v56", "v57", "v58", "v59", "v60", "v61", "v62", "v63", "v64", "v65", "v66", "v67", "v68", "v69", "v70", "v71", "v72", "v73", "v74", "v75", "v76", "v77", "v78", "v79", "v80", "v81", "v82", "v83", "v84", "v85", "v86", "v87", "v88", "v89", "v90", "v91", "v92", "v93", "v94", "v95", "v96", "v97", "v98", "v99", "v100", "v101", "v102", "v103", "v104", "v105", "v106", "v107", "v108", "v109", "v110", "v111", "v112", "v113", "v114", "v115", "v116", "v117", "v118", "v119", "v120", "v121", "v122", "v123", "v124", "v125", "v126", "v127", "v128", "v129", "v130", "v131", "v132", "v133", "v134", "v135", "v136", "v137", "v138", "v139", "v140", "v141", "v142", "v143", "v144", "v145", "v146", "v147", "v148", "v149", "v150", "v151", "v152", "v153", "v154", "v155", "v156", "v157", "v158", "v159", "v160", "v161", "v162", "v163", "v164", "v165", "v166", "v167", "v168", "v169", "v170", "v171", "v172", "v173", "v174", "v175", "v176", "v177", "v178", "v179", "v180", "v181", "v182", "v183", "v184", "v185", "v186", "v187", "v188", "v189", "v190", "v191", "v192", "v193", "v194", "v195", "v196", "v197", "v198", "v199", "v200", "v201", "v202", "v203", "v204", "v205", "v206", "v207", "v208", "v209", "v210", "v211", "v212", "v213", "v214", "v215", "v216", "v217", "v218", "v219", "v220", "v221", "v222", "v223", "v224", "v225", "v226", "v227", "v228", "v229", "v230", "v231", "v232", "v233", "v234", "v235", "v236", "v237", "v238", "v239", "v240", "v241", "v242", "v243", "v244", "v245", "v246", "v247", "v248", "v249", "v250", "v251", "v252", "v253", "v254", "v255", "a0", "a1", "a2", "a3", "a4", "a5", "a6", "a7", "a8", "a9", "a10", "a11", "a12", "a13", "a14", "a15", "a16", "a17", "a18", "a19", "a20", "a21", "a22", "a23", "a24", "a25", "a26", "a27", "a28", "a29", "a30", "a31", "a32", "a33", "a34", "a35", "a36", "a37", "a38", "a39", "a40", "a41", "a42", "a43", "a44", "a45", "a46", "a47", "a48", "a49", "a50", "a51", "a52", "a53", "a54", "a55", "a56", "a57", "a58", "a59", "a60", "a61", "a62", "a63", "a64", "a65", "a66", "a67", "a68", "a69", "a70", "a71", "a72", "a73", "a74", "a75", "a76", "a77", "a78", "a79", "a80", "a81", "a82", "a83", "a84", "a85", "a86", "a87", "a88", "a89", "a90", "a91", "a92", "a93", "a94", "a95", "a96", "a97", "a98", "a99", "a100", "a101", "a102", "a103", "a104", "a105", "a106", "a107", "a108", "a109", "a110", "a111", "a112", "a113", "a114", "a115", "a116", "a117", "a118", "a119", "a120", "a121", "a122", "a123", "a124", "a125", "a126", "a127", "a128", "a129", "a130", "a131", "a132", "a133", "a134", "a135", "a136", "a137", "a138", "a139", "a140", "a141", "a142", "a143", "a144", "a145", "a146", "a147", "a148", "a149", "a150", "a151", "a152", "a153", "a154", "a155", "a156", "a157", "a158", "a159", "a160", "a161", "a162", "a163", "a164", "a165", "a166", "a167", "a168", "a169", "a170", "a171", "a172", "a173", "a174", "a175", "a176", "a177", "a178", "a179", "a180", "a181", "a182", "a183", "a184", "a185", "a186", "a187", "a188", "a189", "a190", "a191", "a192", "a193", "a194", "a195", "a196", "a197", "a198", "a199", "a200", "a201", "a202", "a203", "a204", "a205", "a206", "a207", "a208", "a209", "a210", "a211", "a212", "a213", "a214", "a215", "a216", "a217", "a218", "a219", "a220", "a221", "a222", "a223", "a224", "a225", "a226", "a227", "a228", "a229", "a230", "a231", "a232", "a233", "a234", "a235", "a236", "a237", "a238", "a239", "a240", "a241", "a242", "a243", "a244", "a245", "a246", "a247", "a248", "a249", "a250", "a251", "a252", "a253", "a254", "a255");
  if (bad) {
    slow_rows((const Args*)ka, (b & 7) * 4 + ((b >> 3) & 3), (b >> 5) * 256 + (tid & 192), tid & 63);
  }
}

extern "C" void kernel_launch(void* const* d_in, const int* in_sizes, int n_in,
                              void* d_out, int out_size, void* d_ws, size_t ws_size,
                              hipStream_t stream) {
  (void)hipFuncSetAttribute((const void*)attn_fwd, hipFuncAttributeMaxDynamicSharedMemorySize, SHM);
  attn_fwd<<<256, 256, SHM, stream>>>((const unsigned short*)d_in[0], (const int*)d_in[1], (const int*)d_in[2], (const unsigned short*)d_in[3], (const unsigned short*)d_in[4],
                                      (const unsigned short*)d_in[5], (const unsigned short*)d_in[6], (char*)d_ws, (unsigned short*)d_out);
}
```

```cpp
#include <hip/hip_runtime.h>

constexpr int D = 192, NH = 32, S = 2048;
constexpr int SHM = 155968;

struct Args { const unsigned short* q; const int* qk; const int* qv; const unsigned short* ksc; const unsigned short* kzr;
              const unsigned short* vsc; const unsigned short* vzr; char* ws; unsigned short* o; };

__device__ __forceinline__ float bf2f(unsigned short x) { return __uint_as_float((unsigned)x << 16); }
__device__ __forceinline__ float rbf(float x) { return (float)(__bf16)x; }

__device__ __forceinline__ void slow_rows(const Args* a, unsigned h, unsigned row0, unsigned lane) {
  const unsigned short* q = a->q + ((size_t)h * S + row0) * D;
  unsigned short* o = a->o + ((size_t)h * S + row0) * D;
  const int* qk = a->qk + (size_t)h * S * D; const int* qv = a->qv + (size_t)h * S * D;
  const unsigned short* ks = a->ksc + h * 32 * D; const unsigned short* kz = a->kzr + h * 32 * D;
  const unsigned short* vs = a->vsc + h * S; const unsigned short* vz = a->vzr + h * S;
#pragma unroll 1
  for (int r = 0; r < 64; ++r) {
    float m = -INFINITY, l = 0.f, a0 = 0.f, a1 = 0.f, a2 = 0.f;
#pragma unroll 1
    for (int t = 0; t < 32; ++t) {
      const int* kr = qk + (size_t)(t * 64 + lane) * D;
      float s = 0.f;
#pragma unroll 4
      for (int d = 0; d < D; ++d) s = fmaf(bf2f(q[r * D + d]), rbf(rbf(rbf((float)kr[d]) - bf2f(kz[t * D + d])) * bf2f(ks[t * D + d])), s);
      s *= 0.07216878364870322f;
      float mx = s;
      for (int x = 32; x; x >>= 1) mx = fmaxf(mx, __shfl_xor(mx, x));
      const float mn = fmaxf(m, mx), c = __expf(m - mn), p = __expf(s - mn);
      float ps = p;
      for (int x = 32; x; x >>= 1) ps += __shfl_xor(ps, x);
      l = l * c + ps; a0 *= c; a1 *= c; a2 *= c; m = mn;
#pragma unroll 2
      for (int k = 0; k < 64; ++k) {
        const float pk = __shfl(p, k), z = bf2f(vz[t * 64 + k]), sc = bf2f(vs[t * 64 + k]);
        const int* vr = qv + (size_t)(t * 64 + k) * D + lane;
        a0 = fmaf(pk, rbf(rbf(rbf((float)vr[0]) - z) * sc), a0); a1 = fmaf(pk, rbf(rbf(rbf((float)vr[64]) - z) * sc), a1); a2 = fmaf(pk, rbf(rbf(rbf((float)vr[128]) - z) * sc), a2);
      }
    }
    const float i = 1.f / l;
    o[r * D + lane] = __builtin_bit_cast(unsigned short, (__bf16)(a0 * i)); o[r * D + 64 + lane] = __builtin_bit_cast(unsigned short, (__bf16)(a1 * i));
    o[r * D + 128 + lane] = __builtin_bit_cast(unsigned short, (__bf16)(a2 * i));
  }
}

__global__ __launch_bounds__(256, 1) __attribute__((aligned(4096))) void attn_fwd(const unsigned short* q, const int* qk, const int* qv, const unsigned short* ksc, const unsigned short* kzr,
                                                   const unsigned short* vsc, const unsigned short* vzr, char* ws, unsigned short* o) {
  unsigned long long ka = (unsigned long long)__builtin_amdgcn_kernarg_segment_ptr();
  unsigned bad, b = blockIdx.x;
  const unsigned tid = threadIdx.x;
  asm volatile(
    "s_load_dwordx2 s[28:29], %1, 8\n"
    "s_load_dwordx2 s[40:41], %1, 16\n"
    "s_load_dwordx2 s[44:45], %1, 24\n"
    "s_load_dwordx2 s[88:89], %1, 32\n"
    "s_load_dwordx2 s[36:37], %1, 56\n"
    "s_load_dwordx2 s[92:93], %1, 40\n"
    "s_load_dwordx2 s[96:97], %1, 48\n"
    "s_load_dwordx2 s[20:21], %1, 0\n"
    "s_load_dwordx2 s[16:17], %1, 56\n"
    "s_load_dwordx2 s[24:25], %1, 64\n"
    "s_getreg_b32 s72, hwreg(HW_REG_XCC_ID, 0, 1)\n"
    "s_cmp_eq_u32 s72, 1\n"
    "s_cbranch_scc1 .Ldly\n"
    "s_sleep 32\n"
    ".Ldly:\n"
    "v_readfirstlane_b32 s84, %3\n"
    "v_and_b32 v156, 63, %3\n"
    "s_and_b32 s52, %2, 7\n"
    "s_bfe_u32 s72, %2, 0x20003\n"
    "s_lshr_b32 s53, %2, 5\n"
    "s_lshl_b32 s52, s52, 2\n"
    "s_lshr_b32 s84, s84, 6\n"
    "s_or_b32 s52, s52, s72\n"
    "v_lshlrev_b32 v1, 4, v156\n"
    "s_getpc_b64 s[54:55]\n"
    ".Lpc:\n"
    "s_lshl_b32 s72, s72, 14\n"
    "v_and_b32 v246, 127, %3\n"
    "v_lshl_add_u32 v246, v246, 7, s72\n"
    "v_min_u32 v246, .Lend-.Lpc-4, v246\n"
    "global_load_dword v246, v246, s[54:55]\n"
    "s_mov_b32 s30, 1572864\n"
    "s_mov_b32 s31, 0x20000\n"
    "s_mov_b32 s42, 1572864\n"
    "s_mov_b32 s43, 0x20000\n"
    "s_mov_b32 s46, 12288\n"
    "s_mov_b32 s47, 0x20000\n"
    "s_mov_b32 s90, 12288\n"
    "s_mov_b32 s91, 0x20000\n"
    "s_lshr_b32 s79, s84, 1\n"
    "s_and_b32 s59, s84, 1\n"
    "s_cmp_ge_u32 s53, 6\n"
    "s_cselect_b32 s83, 1, 0\n"
    "s_or_b32 s83, s83, s84\n"
    "s_sub_u32 s58, 2, s79\n"
    "s_mul_i32 s71, s58, 49152\n"
    "s_mul_i32 s72, s52, 1572864\n"
    "s_mul_i32 s73, s52, 12288\n"
    "v_lshrrev_b32 v157, 2, v156\n"
    "v_and_b32 v158, 3, v156\n"
    "s_mul_i32 s35, s84, 3072\n"
    "s_bitcmp1_b32 s83, 0\n"
    "s_cbranch_scc1 .Lf2b\n"
    "v_and_b32 v159, 32, v156\n"
    "v_mul_u32_u24 v242, 376, v159\n"
    "v_and_b32 v159, 16, v156\n"
    "v_mul_u32_u24 v159, 88, v159\n"
    "v_lshl_add_u32 v242, v156, 3, v242\n"
    "v_add_u32 v242, v159, v242\n"
    "s_lshl_b32 s63, s53, 7\n"
    "v_mul_u32_u24 v160, 768, v157\n"
    "v_lshl_add_u32 v160, v158, 4, v160\n"
    "s_and_b32 s81, s58, 1\n"
    "s_mul_i32 s81, s81, 384\n"
    "s_lshl_b32 s75, s53, 6\n"
    "s_add_u32 s81, s81, s75\n"
    "s_add_u32 s75, s71, s63\n"
    "s_add_u32 s76, s75, 6144\n"
    "s_add_u32 s77, s75, 24576\n"
    "s_add_u32 s78, s75, 30720\n"
    "s_waitcnt lgkmcnt(0)\n"
    "s_add_u32 s44, s44, s73\n"
    "s_addc_u32 s45, s45, 0\n"
    "s_add_u32 s88, s88, s73\n"
    "s_addc_u32 s89, s89, 0\n"
    "buffer_load_dwordx4 v[4:7], v1, s[44:47], s35 offen offset:0\n"
    "buffer_load_dwordx4 v[8:11], v1, s[44:47], s35 offen offset:1024\n"
    "buffer_load_dwordx4 v[12:15], v1, s[44:47], s35 offen offset:2048\n"
    "buffer_load_dwordx4 v[16:19], v1, s[88:91], 0 offen offset:0\n"
    "buffer_load_dwordx4 v[164:167], v160, s[44:47], s81 offen offset:0\n"
    "buffer_load_dwordx4 v[168:171], v160, s[88:91], s81 offen offset:0\n"
    "s_add_u32 s28, s28, s72\n"
    "s_addc_u32 s29, s29, 0\n"
    "buffer_load_dwordx2 v[204:205], v242, s[28:31], s75 offen offset:0 nt\n"
    "buffer_load_dwordx2 v[206:207], v242, s[28:31], s75 offen offset:768 nt\n"
    "buffer_load_dwordx2 v[208:209], v242, s[28:31], s75 offen offset:3072 nt\n"
    "buffer_load_dwordx2 v[210:211], v242, s[28:31], s75 offen offset:3840 nt\n"
    "buffer_load_dwordx2 v[212:213], v242, s[28:31], s76 offen offset:0 nt\n"
    "buffer_load_dwordx2 v[214:215], v242, s[28:31], s76 offen offset:768 nt\n"
    "buffer_load_dwordx2 v[216:217], v242, s[28:31], s76 offen offset:3072 nt\n"
    "buffer_load_dwordx2 v[218:219], v242, s[28:31], s76 offen offset:3840 nt\n"
    "buffer_load_dwordx2 v[220:221], v242, s[28:31], s77 offen offset:0 nt\n"
    "buffer_load_dwordx2 v[222:223], v242, s[28:31], s77 offen offset:768 nt\n"
    "buffer_load_dwordx2 v[224:225], v242, s[28:31], s77 offen offset:3072 nt\n"
    "buffer_load_dwordx2 v[226:227], v242, s[28:31], s77 offen offset:3840 nt\n"
    "buffer_load_dwordx2 v[228:229], v242, s[28:31], s78 offen offset:0 nt\n"
    "buffer_load_dwordx2 v[230:231], v242, s[28:31], s78 offen offset:768 nt\n"
    "buffer_load_dwordx2 v[232:233], v242, s[28:31], s78 offen offset:3072 nt\n"
    "buffer_load_dwordx2 v[234:235], v242, s[28:31], s78 offen offset:3840 nt\n"
    "s_add_u32 s75, s71, s63\n"
    "s_add_u32 s75, s75, 98304\n"
    "s_add_u32 s76, s75, 6144\n"
    "s_add_u32 s77, s75, 24576\n"
    "s_add_u32 s78, s75, 30720\n"
    "buffer_load_dwordx2 v[36:37], v242, s[28:31], s75 offen offset:0 nt\n"
    "buffer_load_dwordx2 v[38:39], v242, s[28:31], s75 offen offset:768 nt\n"
    "v_lshrrev_b32 v162, 1, v156\n"
    "v_and_b32 v162, 16, v162\n"
    "v_bfe_u32 v159, v156, 4, 1\n"
    "v_or_b32 v162, v162, v159\n"
    "v_and_or_b32 v162, v157, 2, v162\n"
    "v_mul_u32_u24 v162, 768, v162\n"
    "v_and_b32 v163, 0x70, v1\n"
    "v_add_u32 v162, v162, v163\n"
    "s_branch .Lf2e\n"
    ".Lf2b:\n"
    "v_and_b32 v159, 48, v156\n"
    "v_lshlrev_b32 v242, 2, v159\n"
    "v_lshl_add_u32 v243, v158, 4, v242\n"
    "v_and_b32 v159, 12, v156\n"
    "v_mul_u32_u24 v242, 384, v159\n"
    "v_add_u32 v242, v243, v242\n"
    "v_add_u32 v243, v243, v159\n"
    "s_mul_i32 s63, s53, 6144\n"
    "s_sub_u32 s63, s63, 49152\n"
    "s_cmp_eq_u32 s59, 0\n"
    "s_cselect_b32 s63, 0x7f000000, s63\n"
    "s_add_u32 s75, s71, s63\n"
    "s_waitcnt lgkmcnt(0)\n"
    "s_add_u32 s44, s44, s73\n"
    "s_addc_u32 s45, s45, 0\n"
    "s_add_u32 s88, s88, s73\n"
    "s_addc_u32 s89, s89, 0\n"
    "buffer_load_dwordx4 v[4:7], v1, s[44:47], s35 offen offset:0\n"
    "buffer_load_dwordx4 v[8:11], v1, s[44:47], s35 offen offset:1024\n"
    "buffer_load_dwordx4 v[12:15], v1, s[44:47], s35 offen offset:2048\n"
    "buffer_load_dwordx4 v[16:19], v1, s[88:91], 0 offen offset:0\n"
    "s_add_u32 s40, s40, s72\n"
    "s_addc_u32 s41, s41, 0\n"
    "buffer_load_dwordx4 v[204:207], v242, s[40:43], s75 offen offset:0 nt\n"
    "buffer_load_dwordx4 v[208:211], v242, s[40:43], s75 offen offset:768 nt\n"
    "buffer_load_dwordx4 v[212:215], v242, s[40:43], s75 offen offset:256 nt\n"
    "buffer_load_dwordx4 v[216:219], v242, s[40:43], s75 offen offset:1024 nt\n"
    "buffer_load_dwordx4 v[220:223], v242, s[40:43], s75 offen offset:512 nt\n"
    "buffer_load_dwordx4 v[224:227], v242, s[40:43], s75 offen offset:1280 nt\n"
    "s_add_u32 s75, s71, s63\n"
    "s_add_u32 s75, s75, 98304\n"
    "buffer_load_dwordx4 v[36:39], v242, s[40:43], s75 offen offset:0 nt\n"
    "s_add_u32 s28, s28, s72\n"
    "s_addc_u32 s29, s29, 0\n"
    "v_lshrrev_b32 v162, 1, v156\n"
    "v_and_b32 v162, 16, v162\n"
    "v_bfe_u32 v159, v156, 4, 1\n"
    "v_or_b32 v162, v162, v159\n"
    "v_and_or_b32 v162, v157, 2, v162\n"
    "v_mul_u32_u24 v162, 768, v162\n"
    "v_and_b32 v163, 0x70, v1\n"
    "v_add_u32 v162, v162, v163\n"
    ".Lf2e:\n"
    "s_nop 4\n"
    "v_lshlrev_b32 v247, 2, v156\n"
    "v_add_u32 v247, 155648, v247\n"
    "v_mov_b32 v159, 0\n"
    "ds_write_b32 v247, v159\n"
    "v_mov_b32 v159, 0xff800000\n"
    "v_mov_b32 v160, 155904\n"
    "ds_write_b32 v160, v159\n"
    "s_mov_b32 s38, 5376\n"
    "s_mov_b32 s39, 0x20000\n"
    "s_mov_b32 s94, 4096\n"
    "s_mov_b32 s95, 0x20000\n"
    "s_mov_b32 s98, 4096\n"
    "s_mov_b32 s99, 0x20000\n"
    "s_mov_b32 s22, 98304\n"
    "s_mov_b32 s23, 0x20000\n"
    "s_mov_b32 s18, 1622016\n"
    "s_mov_b32 s19, 0x20000\n"
    "s_mov_b32 s26, 98304\n"
    "s_mov_b32 s27, 0x20000\n"
    "s_lshl_b32 s80, s58, 7\n"
    "s_add_u32 s80, s80, 256\n"
    "s_lshl_b32 s74, s52, 16\n"
    "s_mul_i32 s69, s59, 384\n"
    "s_mul_i32 s57, s79, 6144\n"
    "s_add_u32 s57, s57, s69\n"
    "s_add_u32 s66, s57, 24576\n"
    "s_add_u32 s36, s36, s74\n"
    "s_addc_u32 s37, s37, 0\n"
    "s_lshr_b32 s74, s74, 4\n"
    "s_add_u32 s92, s92, s74\n"
    "s_addc_u32 s93, s93, 0\n"
    "s_add_u32 s96, s96, s74\n"
    "s_addc_u32 s97, s97, 0\n"
    "v_lshlrev_b32 v128, 3, v156\n"
    "v_mov_b32 v129, 0\n"
    "v_lshlrev_b32 v130, 4, %3\n"
    "buffer_load_dwordx2 v[124:125], v128, s[44:47], 0 offen offset:0\n"
    "buffer_load_dwordx2 v[126:127], v128, s[88:91], 0 offen offset:0\n"
    "buffer_load_dwordx2 v[238:239], v129, s[36:39], 0 offen offset:0 sc1\n"
    "s_mul_i32 s74, s52, 786432\n"
    "s_mul_i32 s73, s53, 98304\n"
    "s_add_u32 s74, s74, s73\n"
    "s_add_u32 s20, s20, s74\n"
    "s_addc_u32 s21, s21, 0\n"
    "s_add_u32 s24, s24, s74\n"
    "s_addc_u32 s25, s25, 0\n"
    "s_mul_i32 s72, s52, 1626112\n"
    "s_add_u32 s72, s72, 2097152\n"
    "s_add_u32 s16, s16, s72\n"
    "s_addc_u32 s17, s17, 0\n"
    "s_mul_i32 s65, s84, 6144\n"
    "s_mul_i32 s85, s84, 24576\n"
    "s_waitcnt lgkmcnt(0)\n"
    "s_barrier\n"
    "s_add_u32 s73, s65, 49152\n"
    "v_add_u32 v131, s73, v1\n"
    "s_add_u32 s73, s73, s69\n"
    "v_add_u32 v132, s73, v163\n"
    "v_and_b32 v159, 32, v156\n"
    "v_lshlrev_b32 v244, 3, v156\n"
    "v_xor_b32 v244, v244, v159\n"
    "v_lshl_add_u32 v244, v159, 3, v244\n"
    "s_bitcmp1_b32 s83, 0\n"
    "s_cbranch_scc1 .Lf5b\n"
    "s_lshl_b32 s60, s53, 11\n"
    "s_add_u32 s61, s60, 12288\n"
    "s_lshl_b32 s73, s58, 8\n"
    "s_add_u32 s73, s73, 122880\n"
    "v_and_b32 v243, 0xf0, v1\n"
    "v_add_u32 v243, s73, v243\n"
    "s_and_b32 s73, s73, 0x1fd00\n"
    "v_lshlrev_b32 v160, 9, v157\n"
    "v_lshl_add_u32 v160, v158, 6, v160\n"
    "v_add_u32 v160, s73, v160\n"
    "s_lshl_b32 s73, s79, 8\n"
    "s_mul_i32 s74, s59, 6144\n"
    "s_add_u32 s73, s73, s74\n"
    "v_add_u32 v163, s73, v244\n"
    "v_and_b32 v159, 32, v156\n"
    "v_lshlrev_b32 v244, 2, v156\n"
    "v_xor_b32 v244, v244, v159\n"
    "v_mad_u32_u24 v244, v159, 12, v244\n"
    "buffer_load_dwordx2 v[40:41], v242, s[28:31], s75 offen offset:3072 nt\n"
    "buffer_load_dwordx2 v[42:43], v242, s[28:31], s75 offen offset:3840 nt\n"
    "v_add_u32 v159, 0, v156\n"
    "v_mul_u32_u24 v159, 2731, v159\n"
    "v_lshrrev_b32 v159, 16, v159\n"
    "v_xor_b32 v159, v159, v156\n"
    "v_and_b32 v159, 7, v159\n"
    "v_and_b32 v88, 0x380, v1\n"
    "v_lshl_add_u32 v88, v159, 4, v88\n"
    "v_add_u32 v159, 64, v156\n"
    "v_mul_u32_u24 v159, 2731, v159\n"
    "v_lshrrev_b32 v159, 16, v159\n"
    "v_xor_b32 v159, v159, v156\n"
    "v_and_b32 v159, 7, v159\n"
    "v_and_b32 v89, 0x380, v1\n"
    "v_lshl_add_u32 v89, v159, 4, v89\n"
    "v_add_u32 v159, 128, v156\n"
    "v_mul_u32_u24 v159, 2731, v159\n"
    "v_lshrrev_b32 v159, 16, v159\n"
    "v_xor_b32 v159, v159, v156\n"
    "v_and_b32 v159, 7, v159\n"
    "v_and_b32 v90, 0x380, v1\n"
    "v_lshl_add_u32 v90, v159, 4, v90\n"
    "v_and_b32 v2, 7, v156\n"
    "v_lshrrev_b32 v144, 3, %3\n"
    "v_mul_u32_u24 v144, 768, v144\n"
    "v_lshl_add_u32 v144, v2, 3, v144\n"
    "buffer_load_dwordx2 v[44:45], v242, s[28:31], s76 offen offset:0 nt\n"
    "buffer_load_dwordx2 v[46:47], v242, s[28:31], s76 offen offset:768 nt\n"
    "v_add_u32 v144, 131072, v144\n"
    "v_mad_u32_u24 v86, v2, 24, v144\n"
    "v_and_b32 v84, 15, v156\n"
    "v_lshrrev_b32 v159, 4, v156\n"
    "v_xor_b32 v159, v159, v2\n"
    "v_mul_u32_u24 v84, 384, v84\n"
    "v_lshl_add_u32 v84, v159, 4, v84\n"
    "s_add_u32 s73, s65, 49152\n"
    "v_add_u32 v84, s73, v84\n"
    "v_xor_b32 v85, 64, v84\n"
    "v_subrev_u32 v154, 24576, v84\n"
    "v_subrev_u32 v155, 24576, v85\n"
    "s_add_u32 s67, s65, 98304\n"
    "s_add_u32 s68, s65, 24576\n"
    "s_add_u32 s69, s65, 73728\n"
    "s_mov_b32 s81, 256\n"
    "s_mul_i32 s74, s79, 24576\n"
    "s_add_u32 s70, s74, s65\n"
    "s_add_u32 s70, s70, 73728\n"
    "s_sub_u32 s64, s65, s74\n"
    "s_add_u32 s64, s64, 98304\n"
    "v_and_b32 v249, 0x310, v1\n"
    "v_and_b32 v159, 12, v156\n"
    "v_lshl_add_u32 v249, v159, 3, v249\n"
    "v_and_b32 v159, 2, v156\n"
    "buffer_load_dwordx2 v[48:49], v242, s[28:31], s76 offen offset:3072 nt\n"
    "buffer_load_dwordx2 v[50:51], v242, s[28:31], s76 offen offset:3840 nt\n"
    "v_lshl_add_u32 v249, v159, 6, v249\n"
    "s_add_u32 s73, s74, 73728\n"
    "v_add_u32 v248, s73, v249\n"
    "s_sub_u32 s73, 98304, s74\n"
    "v_add_u32 v249, s73, v249\n"
    "s_or_b32 s73, s53, s84\n"
    "s_cmp_eq_u32 s73, 0\n"
    "s_cselect_b32 s82, 0, 0x7fff0000\n"
    "v_and_b32 v252, 12, v156\n"
    "v_bfe_u32 v159, v156, 1, 1\n"
    "v_or_b32 v252, v252, v159\n"
    "v_and_b32 v159, 1, v156\n"
    "v_lshl_or_b32 v252, v159, 1, v252\n"
    "v_lshlrev_b32 v252, 6, v252\n"
    "v_lshlrev_b32 v159, 2, v156\n"
    "v_and_b32 v159, 32, v159\n"
    "v_xor_b32 v159, v159, v156\n"
    "v_and_or_b32 v252, v159, 48, v252\n"
    "v_mov_b32 v2, v252\n"
    "v_add_u32 v3, 24576, v252\n"
    "v_lshrrev_b32 v91, 3, v156\n"
    "v_lshl_add_u32 v87, v91, 6, 64\n"
    "v_add_u32 v87, 64, v87\n"
    "v_and_b32 v241, 1, v156\n"
    "v_lshlrev_b32 v241, 3, v241\n"
    "buffer_load_dwordx2 v[52:53], v242, s[28:31], s77 offen offset:0 nt\n"
    "buffer_load_dwordx2 v[54:55], v242, s[28:31], s77 offen offset:768 nt\n"
    "v_lshl_add_u32 v241, v91, 4, v241\n"
    "v_add_u32 v241, 1152, v241\n"
    "v_lshlrev_b32 v91, 24, v91\n"
    "v_lshl_add_u32 v91, v156, 3, v91\n"
    "s_lshl_b32 s73, s53, 4\n"
    "s_lshl_b32 s74, s59, 3\n"
    "s_add_u32 s73, s73, s74\n"
    "s_add_u32 s73, s73, 1152\n"
    "v_mov_b32 v240, 0x7fff0000\n"
    "v_writelane_b32 v240, s73, 0\n"
    "v_and_b32 v159, 48, v156\n"
    "s_mul_i32 s73, s79, 768\n"
    "s_sub_u32 s73, 131072, s73\n"
    "v_lshlrev_b32 v250, 1, v159\n"
    "v_add_u32 v250, s73, v250\n"
    "v_and_b32 v251, 3, v156\n"
    "v_lshl_add_u32 v251, v251, 7, v159\n"
    "s_add_u32 s73, s73, 256\n"
    "v_add_u32 v251, s73, v251\n"
    "v_and_b32 v159, 64, v1\n"
    "v_add_u32 v251, v159, v251\n"
    "v_bfe_i32 v159, v156, 2, 1\n"
    "v_and_b32 v159, 0xaaaa, v159\n"
    "v_or_b32 v253, 0x4444, v159\n"
    "s_mov_b32 s58, 0\n"
    "s_mov_b32 s59, 8\n"
    "s_waitcnt vmcnt(31)\n"
    "v_pk_min_i16 v4, v4, v10\n"
    "v_pk_min_i16 v5, v5, v11\n"
    "v_pk_min_i16 v6, v6, v12\n"
    "v_pk_min_i16 v7, v7, v13\n"
    "v_pk_min_i16 v8, v8, v14\n"
    "v_pk_min_i16 v9, v9, v15\n"
    "v_pk_min_i16 v16, v16, v18\n"
    "v_pk_min_i16 v17, v17, v19\n"
    "v_pk_min_i16 v4, v4, v7\n"
    "v_pk_min_i16 v5, v5, v8\n"
    "v_pk_min_i16 v6, v6, v9\n"
    "v_pk_min_i16 v16, v16, v17\n"
    "v_pk_min_i16 v4, v4, v5\n"
    "v_lshlrev_b32 v17, 16, v16\n"
    "v_pk_min_i16 v4, v4, v6\n"
    "v_min_i32 v16, v16, v17\n"
    "v_lshlrev_b32 v5, 16, v4\n"
    "v_mov_b32 v7, 155920\n"
    "v_min_i32 v4, v4, v5\n"
    "v_lshl_add_u32 v6, s84, 2, v7\n"
    "v_min_i32_dpp v16, v16, v16 row_shr:1\n"
    "v_min_i32_dpp v4, v4, v4 row_shr:1\n"
    "s_nop 0\n"
    "v_min_i32_dpp v16, v16, v16 row_shr:2\n"
    "v_min_i32_dpp v4, v4, v4 row_shr:2\n"
    "s_nop 0\n"
    "v_min_i32_dpp v16, v16, v16 row_shr:4\n"
    "v_min_i32_dpp v4, v4, v4 row_shr:4\n"
    "s_nop 0\n"
    "v_min_i32_dpp v16, v16, v16 row_shr:8\n"
    "v_min_i32_dpp v4, v4, v4 row_shr:8\n"
    "s_nop 0\n"
    "v_min_i32_dpp v16, v16, v16 row_bcast:15 row_mask:0xa\n"
    "v_min_i32_dpp v4, v4, v4 row_bcast:15 row_mask:0xa\n"
    "s_nop 0\n"
    "v_min_i32_dpp v16, v16, v16 row_bcast:31 row_mask:0xc\n"
    "v_min_i32_dpp v4, v4, v4 row_bcast:31 row_mask:0xc\n"
    "v_readlane_b32 s35, v16, 63\n"
    "v_readlane_b32 s73, v4, 63\n"
    "s_max_i32 s35, s35, 0\n"
    "s_and_b32 s35, s35, 0xffff0000\n"
    "v_mov_b32 v4, s73\n"
    "ds_write_b32 v6, v4\n"
    "v_mov_b32 v14, -32\n"
    "v_mov_b32 v12, -126\n"
    "v_mov_b32 v13, 0x3dd53b95\n"
    "v_mov_b32 v80, 0\n"
    "s_waitcnt vmcnt(29)\n"
    "v_lshlrev_b32 v180, 16, v164\n"
    "v_and_b32 v181, 0xffff0000, v164\n"
    "v_lshlrev_b32 v182, 16, v168\n"
    "v_and_b32 v183, 0xffff0000, v168\n"
    "v_lshlrev_b32 v184, 16, v165\n"
    "v_and_b32 v185, 0xffff0000, v165\n"
    "v_lshlrev_b32 v186, 16, v169\n"
    "v_and_b32 v187, 0xffff0000, v169\n"
    "v_lshlrev_b32 v188, 16, v166\n"
    "v_and_b32 v189, 0xffff0000, v166\n"
    "v_lshlrev_b32 v190, 16, v170\n"
    "v_and_b32 v191, 0xffff0000, v170\n"
    "v_lshlrev_b32 v192, 16, v167\n"
    "v_and_b32 v193, 0xffff0000, v167\n"
    "v_lshlrev_b32 v194, 16, v171\n"
    "v_and_b32 v195, 0xffff0000, v171\n"
    "s_waitcnt lgkmcnt(0)\n"
    "s_barrier\n"
    "ds_read_b128 v[8:11], v7 offset:0\n"
    "buffer_load_dwordx2 v[56:57], v242, s[28:31], s77 offen offset:3072 nt\n"
    "buffer_load_dwordx2 v[58:59], v242, s[28:31], s77 offen offset:3840 nt\n"
    "buffer_load_dwordx4 v[136:139], v130, s[92:95], 0 offen offset:0\n"
    "buffer_load_dwordx4 v[140:143], v130, s[96:99], 0 offen offset:0\n"
    "v_accvgpr_write_b32 a0, 0\n"
    "v_accvgpr_write_b32 a1, 0\n"
    "v_accvgpr_write_b32 a2, 0\n"
    "v_accvgpr_write_b32 a3, 0\n"
    "v_accvgpr_write_b32 a4, 0\n"
    "v_accvgpr_write_b32 a5, 0\n"
    "v_accvgpr_write_b32 a6, 0\n"
    "v_accvgpr_write_b32 a7, 0\n"
    "v_accvgpr_write_b32 a8, 0\n"
    "v_accvgpr_write_b32 a9, 0\n"
    "v_accvgpr_write_b32 a10, 0\n"
    "v_accvgpr_write_b32 a11, 0\n"
    "v_accvgpr_write_b32 a12, 0\n"
    "v_accvgpr_write_b32 a13, 0\n"
    "v_accvgpr_write_b32 a14, 0\n"
    "v_accvgpr_write_b32 a15, 0\n"
    "v_accvgpr_write_b32 a16, 0\n"
    "v_accvgpr_write_b32 a17, 0\n"
    "v_accvgpr_write_b32 a18, 0\n"
    "v_accvgpr_write_b32 a19, 0\n"
    "v_accvgpr_write_b32 a20, 0\n"
    "v_accvgpr_write_b32 a21, 0\n"
    "v_accvgpr_write_b32 a22, 0\n"
    "v_accvgpr_write_b32 a23, 0\n"
    "v_accvgpr_write_b32 a24, 0\n"
    "v_accvgpr_write_b32 a25, 0\n"
    "v_accvgpr_write_b32 a26, 0\n"
    "v_accvgpr_write_b32 a27, 0\n"
    "v_accvgpr_write_b32 a28, 0\n"
    "v_accvgpr_write_b32 a29, 0\n"
    "v_accvgpr_write_b32 a30, 0\n"
    "v_accvgpr_write_b32 a31, 0\n"
    "v_accvgpr_write_b32 a32, 0\n"
    "v_accvgpr_write_b32 a33, 0\n"
    "v_accvgpr_write_b32 a34, 0\n"
    "v_accvgpr_write_b32 a35, 0\n"
    "v_accvgpr_write_b32 a36, 0\n"
    "v_accvgpr_write_b32 a37, 0\n"
    "v_accvgpr_write_b32 a38, 0\n"
    "v_accvgpr_write_b32 a39, 0\n"
    "v_accvgpr_write_b32 a40, 0\n"
    "v_accvgpr_write_b32 a41, 0\n"
    "v_accvgpr_write_b32 a42, 0\n"
    "v_accvgpr_write_b32 a43, 0\n"
    "v_accvgpr_write_b32 a44, 0\n"
    "v_accvgpr_write_b32 a45, 0\n"
    "v_accvgpr_write_b32 a46, 0\n"
    "v_accvgpr_write_b32 a47, 0\n"
    "v_accvgpr_write_b32 a48, 0\n"
    "v_accvgpr_write_b32 a49, 0\n"
    "v_accvgpr_write_b32 a50, 0\n"
    "v_accvgpr_write_b32 a51, 0\n"
    "v_accvgpr_write_b32 a52, 0\n"
    "v_accvgpr_write_b32 a53, 0\n"
    "v_accvgpr_write_b32 a54, 0\n"
    "v_accvgpr_write_b32 a55, 0\n"
    "v_accvgpr_write_b32 a56, 0\n"
    "v_accvgpr_write_b32 a57, 0\n"
    "v_accvgpr_write_b32 a58, 0\n"
    "v_accvgpr_write_b32 a59, 0\n"
    "s_waitcnt lgkmcnt(0)\n"
    "v_min3_i32 v4, v8, v9, v10\n"
    "v_min_i32 v4, v4, v11\n"
    "v_max_i32 v4, 0, v4\n"
    "v_and_b32 v4, 0xffff0000, v4\n"
    "v_mul_f32 v4, s35, v4\n"
    "v_mul_f32 v4, 0x3ed53b95, v4\n"
    "v_lshrrev_b32 v4, 23, v4\n"
    "v_sub_u32 v4, 2, v4\n"
    "v_med3_i32 v4, v4, v14, v12\n"
    "v_ldexp_f32 v13, v13, v4\n"
    "v_readfirstlane_b32 s89, v4\n"
    "v_readfirstlane_b32 s88, v13\n"
    "s_mul_i32 s89, s89, 0xff7fff80\n"
    "s_mov_b32 s90, 149\n"
    "v_mul_f32 v180, s88, v180\n"
    "v_mul_f32 v181, s88, v181\n"
    "v_mul_f32 v184, s88, v184\n"
    "v_mul_f32 v185, s88, v185\n"
    "v_mul_f32 v188, s88, v188\n"
    "v_mul_f32 v189, s88, v189\n"
    "v_mul_f32 v192, s88, v192\n"
    "v_mul_f32 v193, s88, v193\n"
    "v_fmaak_f32 v182, v182, v180, 0x80730000\n"
    "v_fmaak_f32 v183, v183, v181, 0x80730000\n"
    "v_fmaak_f32 v186, v186, v184, 0x80730000\n"
    "v_fmaak_f32 v187, v187, v185, 0x80730000\n"
    "v_fmaak_f32 v190, v190, v188, 0x80730000\n"
    "v_fmaak_f32 v191, v191, v189, 0x80730000\n"
    "v_fmaak_f32 v194, v194, v192, 0x80730000\n"
    "v_fmaak_f32 v195, v195, v193, 0x80730000\n"
    "v_ldexp_f32 v180, v180, s90\n"
    "v_ldexp_f32 v181, v181, s90\n"
    "v_ldexp_f32 v184, v184, s90\n"
    "v_ldexp_f32 v185, v185, s90\n"
    "v_ldexp_f32 v188, v188, s90\n"
    "v_ldexp_f32 v189, v189, s90\n"
    "v_ldexp_f32 v192, v192, s90\n"
    "v_ldexp_f32 v193, v193, s90\n"
    "v_fma_f32 v180, v180, 0, v180\n"
    "v_fma_f32 v181, v181, 0, v181\n"
    "v_fma_f32 v184, v184, 0, v184\n"
    "v_fma_f32 v185, v185, 0, v185\n"
    "v_fma_f32 v188, v188, 0, v188\n"
    "v_fma_f32 v189, v189, 0, v189\n"
    "v_fma_f32 v192, v192, 0, v192\n"
    "v_fma_f32 v193, v193, 0, v193\n"
    "ds_write_b128 v160, v[180:183] offset:0\n"
    "ds_write_b128 v160, v[184:187] offset:16\n"
    "ds_write_b128 v160, v[188:191] offset:32\n"
    "ds_write_b128 v160, v[192:195] offset:48\n"
    "s_waitcnt vmcnt(17)\n"
    "s_add_u32 s54, s71, s60\n"
    "s_add_u32 s55, s71, s61\n"
    "ds_read_b128 v[92:95], v243 offset:0\n"
    "s_waitcnt lgkmcnt(0)\n"
    "v_fma_f32 v204, v204, v92, -v94\n"
    "v_fma_f32 v205, v205, v93, -v95\n"
    "v_fma_f32 v206, v206, v92, -v94\n"
    "v_fma_f32 v207, v207, v93, -v95\n"
    "v_cvt_pk_bf16_f32 v204, v204, v205\n"
    "v_cvt_pk_bf16_f32 v206, v206, v207\n"
    "buffer_store_dword v204, v244, s[16:19], s54 offen offset:0 sc1\n"
    "buffer_store_dword v206, v244, s[16:19], s54 offen offset:128 sc1\n"
    "v_fma_f32 v208, v208, v92, -v94\n"
    "v_fma_f32 v209, v209, v93, -v95\n"
    "v_fma_f32 v210, v210, v92, -v94\n"
    "v_fma_f32 v211, v211, v93, -v95\n"
    "v_cvt_pk_bf16_f32 v208, v208, v209\n"
    "v_cvt_pk_bf16_f32 v210, v210, v211\n"
    "buffer_store_dword v208, v244, s[16:19], s54 offen offset:1024 sc1\n"
    "buffer_store_dword v210, v244, s[16:19], s54 offen offset:1152 sc1\n"
    "v_fma_f32 v212, v212, v92, -v94\n"
    "v_fma_f32 v213, v213, v93, -v95\n"
    "buffer_load_dwordx2 v[60:61], v242, s[28:31], s78 offen offset:0 nt\n"
    "buffer_load_dwordx2 v[62:63], v242, s[28:31], s78 offen offset:768 nt\n"
    "buffer_load_dwordx4 v[4:7], v162, s[28:31], s57 offen offset:0\n"
    "buffer_load_dwordx4 v[8:11], v162, s[28:31], s57 offen offset:3072\n"
    "v_fma_f32 v214, v214, v92, -v94\n"
    "v_fma_f32 v215, v215, v93, -v95\n"
    "v_cvt_pk_bf16_f32 v212, v212, v213\n"
    "v_cvt_pk_bf16_f32 v214, v214, v215\n"
    "buffer_store_dword v212, v244, s[16:19], s54 offen offset:256 sc1\n"
    "buffer_store_dword v214, v244, s[16:19], s54 offen offset:384 sc1\n"
    "v_fma_f32 v216, v216, v92, -v94\n"
    "v_fma_f32 v217, v217, v93, -v95\n"
    "v_fma_f32 v218, v218, v92, -v94\n"
    "v_fma_f32 v219, v219, v93, -v95\n"
    "v_cvt_pk_bf16_f32 v216, v216, v217\n"
    "v_cvt_pk_bf16_f32 v218, v218, v219\n"
    "buffer_store_dword v216, v244, s[16:19], s54 offen offset:1280 sc1\n"
    "buffer_store_dword v218, v244, s[16:19], s54 offen offset:1408 sc1\n"
    "v_fma_f32 v220, v220, v92, -v94\n"
    "v_fma_f32 v221, v221, v93, -v95\n"
    "v_fma_f32 v222, v222, v92, -v94\n"
    "v_fma_f32 v223, v223, v93, -v95\n"
    "v_cvt_pk_bf16_f32 v220, v220, v221\n"
    "v_cvt_pk_bf16_f32 v222, v222, v223\n"
    "buffer_store_dword v220, v244, s[16:19], s55 offen offset:0 sc1\n"
    "buffer_store_dword v222, v244, s[16:19], s55 offen offset:128 sc1\n"
    "v_fma_f32 v224, v224, v92, -v94\n"
    "buffer_load_dwordx2 v[64:65], v242, s[28:31], s78 offen offset:3072 nt\n"
    "buffer_load_dwordx2 v[66:67], v242, s[28:31], s78 offen offset:3840 nt\n"
    "buffer_load_dwordx4 v[12:15], v162, s[28:31], s57 offen offset:128\n"
    "v_fma_f32 v225, v225, v93, -v95\n"
    "v_fma_f32 v226, v226, v92, -v94\n"
    "v_fma_f32 v227, v227, v93, -v95\n"
    "v_cvt_pk_bf16_f32 v224, v224, v225\n"
    "v_cvt_pk_bf16_f32 v226, v226, v227\n"
    "buffer_store_dword v224, v244, s[16:19], s55 offen offset:1024 sc1\n"
    "buffer_store_dword v226, v244, s[16:19], s55 offen offset:1152 sc1\n"
    "v_fma_f32 v228, v228, v92, -v94\n"
    "v_fma_f32 v229, v229, v93, -v95\n"
    "v_fma_f32 v230, v230, v92, -v94\n"
    "v_fma_f32 v231, v231, v93, -v95\n"
    "v_cvt_pk_bf16_f32 v228, v228, v229\n"
    "v_cvt_pk_bf16_f32 v230, v230, v231\n"
    "buffer_store_dword v228, v244, s[16:19], s55 offen offset:256 sc1\n"
    "buffer_store_dword v230, v244, s[16:19], s55 offen offset:384 sc1\n"
    "v_fma_f32 v232, v232, v92, -v94\n"
    "v_fma_f32 v233, v233, v93, -v95\n"
    "v_fma_f32 v234, v234, v92, -v94\n"
    "v_fma_f32 v235, v235, v93, -v95\n"
    "v_cvt_pk_bf16_f32 v232, v232, v233\n"
    "v_cvt_pk_bf16_f32 v234, v234, v235\n"
    "buffer_store_dword v232, v244, s[16:19], s55 offen offset:1280 sc1\n"
    "buffer_store_dword v234, v244, s[16:19], s55 offen offset:1408 sc1\n"
    "v_add_u32 v243, 512, v243\n"
    "s_add_u32 s71, s71, 98304\n"
    "s_add_u32 m0, s65, 24576\n"
    "s_add_u32 s72, s85, 0\n"
    "buffer_load_dwordx4 v88, s[20:23], s72 offen offset:0 nt lds\n"
    "buffer_load_dwordx4 v[16:19], v162, s[28:31], s57 offen offset:3200\n"
    "buffer_load_dwordx4 v[20:23], v162, s[28:31], s57 offen offset:256\n"
    "s_waitcnt vmcnt(38)\n"
    "v_add_co_u32 v238, vcc, 0x7f4a7c15, v238\n"
    "v_mov_b32 v159, 0x9e3779b9\n"
    "s_nop 0\n"
    "v_addc_co_u32 v239, vcc, v159, v239, vcc\n"
    "v_mov_b32 v246, 1\n"
    "s_waitcnt vmcnt(39)\n"
    "v_lshlrev_b32 v180, 16, v124\n"
    "v_and_b32 v181, 0xffff0000, v124\n"
    "v_lshlrev_b32 v182, 16, v125\n"
    "v_and_b32 v183, 0xffff0000, v125\n"
    "v_lshlrev_b32 v184, 16, v126\n"
    "v_and_b32 v185, 0xffff0000, v126\n"
    "v_lshlrev_b32 v186, 16, v127\n"
    "v_and_b32 v187, 0xffff0000, v127\n"
    "v_mul_f32 v180, s88, v180\n"
    "v_mul_f32 v181, s88, v181\n"
    "v_mul_f32 v182, s88, v182\n"
    "v_mul_f32 v183, s88, v183\n"
    "v_fmaak_f32 v184, v184, v180, 0x80730000\n"
    "v_fmaak_f32 v185, v185, v181, 0x80730000\n"
    "v_fmaak_f32 v186, v186, v182, 0x80730000\n"
    "v_fmaak_f32 v187, v187, v183, 0x80730000\n"
    "s_add_u32 m0, s65, 25600\n"
    "s_add_u32 s72, s85, 1024\n"
    "buffer_load_dwordx4 v89, s[20:23], s72 offen offset:0 nt lds\n"
    "buffer_load_dwordx4 v[24:27], v162, s[28:31], s57 offen offset:3328\n"
    "v_ldexp_f32 v180, v180, s90\n"
    "v_ldexp_f32 v181, v181, s90\n"
    "v_ldexp_f32 v182, v182, s90\n"
    "v_ldexp_f32 v183, v183, s90\n"
    "v_fma_f32 v180, v180, 0, v180\n"
    "v_fma_f32 v181, v181, 0, v181\n"
    "v_fma_f32 v182, v182, 0, v182\n"
    "v_fma_f32 v183, v183, 0, v183\n"
    "ds_write_b128 v131, v[180:183] offset:0\n"
    "ds_write_b128 v131, v[184:187] offset:1024\n"
    "ds_read_b128 v[180:183], v132 offset:0\n"
    "ds_read_b128 v[184:187], v132 offset:1024\n"
    "ds_read_b128 v[188:191], v132 offset:128\n"
    "ds_read_b128 v[192:195], v132 offset:1152\n"
    "ds_read_b128 v[196:199], v132 offset:256\n"
    "ds_read_b128 v[200:203], v132 offset:1280\n"
    "v_accvgpr_write_b32 a60, 0\n"
    "v_accvgpr_write_b32 a61, 0\n"
    "v_accvgpr_write_b32 a62, 0\n"
    "v_accvgpr_write_b32 a63, 0\n"
    "v_accvgpr_write_b32 a64, 0\n"
    "v_accvgpr_write_b32 a65, 0\n"
    "v_accvgpr_write_b32 a66, 0\n"
    "s_add_u32 m0, s65, 26624\n"
    "s_add_u32 s72, s85, 2048\n"
    "buffer_load_dwordx4 v90, s[20:23], s72 offen offset:0 nt lds\n"
    "buffer_load_dwordx4 v[100:103], v162, s[28:31], s66 offen offset:0\n"
    "buffer_load_dwordx4 v[104:107], v162, s[28:31], s66 offen offset:3072\n"
    "v_accvgpr_write_b32 a67, 0\n"
    "v_accvgpr_write_b32 a68, 0\n"
    "v_accvgpr_write_b32 a69, 0\n"
    "v_accvgpr_write_b32 a70, 0\n"
    "v_accvgpr_write_b32 a71, 0\n"
    "v_accvgpr_write_b32 a72, 0\n"
    "v_accvgpr_write_b32 a73, 0\n"
    "v_accvgpr_write_b32 a74, 0\n"
    "v_accvgpr_write_b32 a75, 0\n"
    "v_accvgpr_write_b32 a76, 0\n"
    "v_accvgpr_write_b32 a77, 0\n"
    "v_accvgpr_write_b32 a78, 0\n"
    "v_accvgpr_write_b32 a79, 0\n"
    "v_accvgpr_write_b32 a80, 0\n"
    "v_accvgpr_write_b32 a81, 0\n"
    "v_accvgpr_write_b32 a82, 0\n"
    "v_accvgpr_write_b32 a83, 0\n"
    "v_accvgpr_write_b32 a84, 0\n"
    "v_accvgpr_write_b32 a85, 0\n"
    "v_accvgpr_write_b32 a86, 0\n"
    "v_accvgpr_write_b32 a87, 0\n"
    "v_accvgpr_write_b32 a88, 0\n"
    "s_add_u32 m0, s65, 27648\n"
    "s_add_u32 s72, s85, 3072\n"
    "buffer_load_dwordx4 v88, s[20:23], s72 offen offset:0 nt lds\n"
    "buffer_load_dwordx4 v[108:111], v162, s[28:31], s66 offen offset:128\n"
    "v_accvgpr_write_b32 a89, 0\n"
    "v_accvgpr_write_b32 a90, 0\n"
    "v_accvgpr_write_b32 a91, 0\n"
    "v_accvgpr_write_b32 a92, 0\n"
    "v_accvgpr_write_b32 a93, 0\n"
    "v_accvgpr_write_b32 a94, 0\n"
    "v_accvgpr_write_b32 a95, 0\n"
    "v_accvgpr_write_b32 a96, 0\n"
    "v_accvgpr_write_b32 a97, 0\n"
    "v_accvgpr_write_b32 a98, 0\n"
    "v_accvgpr_write_b32 a99, 0\n"
    "v_accvgpr_write_b32 a100, 0\n"
    "v_accvgpr_write_b32 a101, 0\n"
    "v_accvgpr_write_b32 a102, 0\n"
    "v_accvgpr_write_b32 a103, 0\n"
    "v_accvgpr_write_b32 a104, 0\n"
    "v_accvgpr_write_b32 a105, 0\n"
    "v_accvgpr_write_b32 a106, 0\n"
    "v_accvgpr_write_b32 a107, 0\n"
    "v_accvgpr_write_b32 a108, 0\n"
    "v_accvgpr_write_b32 a109, 0\n"
    "v_accvgpr_write_b32 a110, 0\n"
    "v_accvgpr_write_b32 a111, 0\n"
    "s_waitcnt lgkmcnt(0)\n"
    "s_waitcnt vmcnt(10)\n"
    "ds_add_rtn_u32 v245, v247, v246\n"
    "s_lshl_b32 s73, s53, 6\n"
    "s_add_u32 s73, s73, 128\n"
    "s_waitcnt lgkmcnt(0)\n"
    "v_readfirstlane_b32 s74, v245\n"
    "s_nop 0\n"
    "s_cmp_eq_u32 s74, 3\n"
    "s_cselect_b32 s73, s73, 0x7fff0000\n"
    "buffer_store_dwordx2 v[238:239], v91, s[36:39], s73 offen offset:0 sc1\n"
    "s_waitcnt vmcnt(18)\n"
    "s_add_u32 m0, s65, 28672\n"
    "s_add_u32 s72, s85, 4096\n"
    "buffer_load_dwordx4 v89, s[20:23], s72 offen offset:0 nt lds\n"
    "buffer_load_dwordx4 v[112:115], v162, s[28:31], s66 offen offset:3200\n"
    "buffer_load_dwordx4 v[116:119], v162, s[28:31], s66 offen offset:256\n"
    "s_add_u32 s54, s71, s60\n"
    "s_add_u32 s55, s71, s61\n"
    "ds_read_b128 v[92:95], v243 offset:0\n"
    "s_waitcnt lgkmcnt(0)\n"
    "v_fma_f32 v36, v36, v92, -v94\n"
    "v_fma_f32 v37, v37, v93, -v95\n"
    "v_fma_f32 v38, v38, v92, -v94\n"
    "v_fma_f32 v39, v39, v93, -v95\n"
    "v_cvt_pk_bf16_f32 v36, v36, v37\n"
    "v_cvt_pk_bf16_f32 v38, v38, v39\n"
    "buffer_store_dword v36, v244, s[16:19], s54 offen offset:0 sc1\n"
    "buffer_store_dword v38, v244, s[16:19], s54 offen offset:128 sc1\n"
    "v_fma_f32 v40, v40, v92, -v94\n"
    "v_fma_f32 v41, v41, v93, -v95\n"
    "v_fma_f32 v42, v42, v92, -v94\n"
    "v_fma_f32 v43, v43, v93, -v95\n"
    "v_cvt_pk_bf16_f32 v40, v40, v41\n"
    "v_cvt_pk_bf16_f32 v42, v42, v43\n"
    "buffer_store_dword v40, v244, s[16:19], s54 offen offset:1024 sc1\n"
    "buffer_store_dword v42, v244, s[16:19], s54 offen offset:1152 sc1\n"
    "v_fma_f32 v44, v44, v92, -v94\n"
    "v_fma_f32 v45, v45, v93, -v95\n"
    "v_fma_f32 v46, v46, v92, -v94\n"
    "v_fma_f32 v47, v47, v93, -v95\n"
    "v_cvt_pk_bf16_f32 v44, v44, v45\n"
    "v_cvt_pk_bf16_f32 v46, v46, v47\n"
    "buffer_store_dword v44, v244, s[16:19], s54 offen offset:256 sc1\n"
    "buffer_store_dword v46, v244, s[16:19], s54 offen offset:384 sc1\n"
    "v_fma_f32 v48, v48, v92, -v94\n"
    "v_fma_f32 v49, v49, v93, -v95\n"
    "v_fma_f32 v50, v50, v92, -v94\n"
    "v_fma_f32 v51, v51, v93, -v95\n"
    "v_cvt_pk_bf16_f32 v48, v48, v49\n"
    "v_cvt_pk_bf16_f32 v50, v50, v51\n"
    "s_add_u32 m0, s65, 29696\n"
    "s_add_u32 s72, s85, 5120\n"
    "buffer_load_dwordx4 v90, s[20:23], s72 offen offset:0 nt lds\n"
    "buffer_load_dwordx4 v[120:123], v162, s[28:31], s66 offen offset:3328\n"
    "buffer_store_dword v48, v244, s[16:19], s54 offen offset:1280 sc1\n"
    "buffer_store_dword v50, v244, s[16:19], s54 offen offset:1408 sc1\n"
    "v_fma_f32 v52, v52, v92, -v94\n"
    "v_fma_f32 v53, v53, v93, -v95\n"
    "v_fma_f32 v54, v54, v92, -v94\n"
    "v_fma_f32 v55, v55, v93, -v95\n"
    "v_cvt_pk_bf16_f32 v52, v52, v53\n"
    "v_cvt_pk_bf16_f32 v54, v54, v55\n"
    "buffer_store_dword v52, v244, s[16:19], s55 offen offset:0 sc1\n"
    "buffer_store_dword v54, v244, s[16:19], s55 offen offset:128 sc1\n"
    "v_fma_f32 v56, v56, v92, -v94\n"
    "v_fma_f32 v57, v57, v93, -v95\n"
    "v_fma_f32 v58, v58, v92, -v94\n"
    "v_fma_f32 v59, v59, v93, -v95\n"
    "v_cvt_pk_bf16_f32 v56, v56, v57\n"
    "v_cvt_pk_bf16_f32 v58, v58, v59\n"
    "buffer_store_dword v56, v244, s[16:19], s55 offen offset:1024 sc1\n"
    "buffer_store_dword v58, v244, s[16:19], s55 offen offset:1152 sc1\n"
    "v_fma_f32 v60, v60, v92, -v94\n"
    "v_fma_f32 v61, v61, v93, -v95\n"
    "v_fma_f32 v62, v62, v92, -v94\n"
    "v_fma_f32 v63, v63, v93, -v95\n"
    "v_cvt_pk_bf16_f32 v60, v60, v61\n"
    "v_cvt_pk_bf16_f32 v62, v62, v63\n"
    "buffer_store_dword v60, v244, s[16:19], s55 offen offset:256 sc1\n"
    "buffer_store_dword v62, v244, s[16:19], s55 offen offset:384 sc1\n"
    "v_fma_f32 v64, v64, v92, -v94\n"
    "v_fma_f32 v65, v65, v93, -v95\n"
    "v_fma_f32 v66, v66, v92, -v94\n"
    "v_fma_f32 v67, v67, v93, -v95\n"
    "v_cvt_pk_bf16_f32 v64, v64, v65\n"
    "v_cvt_pk_bf16_f32 v66, v66, v67\n"
    "buffer_store_dword v64, v244, s[16:19], s55 offen offset:1280 sc1\n"
    "buffer_store_dword v66, v244, s[16:19], s55 offen offset:1408 sc1\n"
    "v_add_u32 v243, 512, v243\n"
    "s_add_u32 m0, s65, 73728\n"
    "s_add_u32 s72, s85, 6144\n"
    "buffer_load_dwordx4 v88, s[20:23], s72 offen offset:0 nt lds\n"
    "s_waitcnt vmcnt(56)\n"
    "v_lshlrev_b32 v204, 16, v136\n"
    "v_and_b32 v205, 0xffff0000, v136\n"
    "v_lshlrev_b32 v206, 16, v137\n"
    "v_and_b32 v207, 0xffff0000, v137\n"
    "v_lshlrev_b32 v208, 16, v138\n"
    "v_and_b32 v209, 0xffff0000, v138\n"
    "v_lshlrev_b32 v210, 16, v139\n"
    "v_and_b32 v211, 0xffff0000, v139\n"
    "v_log_f32 v68, v204\n"
    "v_log_f32 v69, v205\n"
    "v_log_f32 v70, v206\n"
    "v_log_f32 v71, v207\n"
    "v_log_f32 v72, v208\n"
    "v_log_f32 v73, v209\n"
    "v_log_f32 v74, v210\n"
    "v_log_f32 v75, v211\n"
    "v_rcp_f32 v204, v204\n"
    "v_rcp_f32 v205, v205\n"
    "v_rcp_f32 v206, v206\n"
    "s_add_u32 m0, s65, 74752\n"
    "s_add_u32 s72, s85, 7168\n"
    "buffer_load_dwordx4 v89, s[20:23], s72 offen offset:0 nt lds\n"
    "v_rcp_f32 v207, v207\n"
    "v_rcp_f32 v208, v208\n"
    "v_rcp_f32 v209, v209\n"
    "v_rcp_f32 v210, v210\n"
    "v_rcp_f32 v211, v211\n"
    "s_mov_b32 s73, 0x41a00000\n"
    "v_mov_b32 v228, 0x7fc00000\n"
    "v_cmp_le_f32 vcc, |v68|, s73\n"
    "s_nop 1\n"
    "v_cndmask_b32 v68, v228, v68, vcc\n"
    "v_cmp_le_f32 vcc, |v69|, s73\n"
    "s_nop 1\n"
    "v_cndmask_b32 v69, v228, v69, vcc\n"
    "v_cmp_le_f32 vcc, |v70|, s73\n"
    "s_nop 1\n"
    "v_cndmask_b32 v70, v228, v70, vcc\n"
    "v_cmp_le_f32 vcc, |v71|, s73\n"
    "s_nop 1\n"
    "v_cndmask_b32 v71, v228, v71, vcc\n"
    "v_cmp_le_f32 vcc, |v72|, s73\n"
    "s_nop 1\n"
    "v_cndmask_b32 v72, v228, v72, vcc\n"
    "v_cmp_le_f32 vcc, |v73|, s73\n"
    "s_nop 1\n"
    "v_cndmask_b32 v73, v228, v73, vcc\n"
    "v_cmp_le_f32 vcc, |v74|, s73\n"
    "s_nop 1\n"
    "v_cndmask_b32 v74, v228, v74, vcc\n"
    "s_add_u32 m0, s65, 75776\n"
    "s_add_u32 s72, s85, 8192\n"
    "buffer_load_dwordx4 v90, s[20:23], s72 offen offset:0 nt lds\n"
    "v_cmp_le_f32 vcc, |v75|, s73\n"
    "s_nop 1\n"
    "v_cndmask_b32 v75, v228, v75, vcc\n"
    "v_cvt_pk_bf16_f32 v220, v204, v205\n"
    "v_cvt_pk_bf16_f32 v221, v206, v207\n"
    "v_cvt_pk_bf16_f32 v222, v208, v209\n"
    "v_cvt_pk_bf16_f32 v223, v210, v211\n"
    "v_lshlrev_b32 v212, 16, v220\n"
    "v_and_b32 v213, 0xffff0000, v220\n"
    "v_lshlrev_b32 v214, 16, v221\n"
    "v_and_b32 v215, 0xffff0000, v221\n"
    "v_lshlrev_b32 v216, 16, v222\n"
    "v_and_b32 v217, 0xffff0000, v222\n"
    "v_lshlrev_b32 v218, 16, v223\n"
    "v_and_b32 v219, 0xffff0000, v223\n"
    "v_sub_f32 v204, v204, v212\n"
    "v_sub_f32 v205, v205, v213\n"
    "v_sub_f32 v206, v206, v214\n"
    "v_sub_f32 v207, v207, v215\n"
    "v_sub_f32 v208, v208, v216\n"
    "v_sub_f32 v209, v209, v217\n"
    "s_add_u32 m0, s65, 76800\n"
    "s_add_u32 s72, s85, 9216\n"
    "buffer_load_dwordx4 v88, s[20:23], s72 offen offset:0 nt lds\n"
    "v_sub_f32 v210, v210, v218\n"
    "v_sub_f32 v211, v211, v219\n"
    "v_cvt_pk_bf16_f32 v224, v204, v205\n"
    "v_cvt_pk_bf16_f32 v225, v206, v207\n"
    "v_cvt_pk_bf16_f32 v226, v208, v209\n"
    "v_cvt_pk_bf16_f32 v227, v210, v211\n"
    "v_mov_b32 v204, 0\n"
    "v_mov_b32 v205, 0\n"
    "v_mov_b32 v206, 0\n"
    "v_mov_b32 v207, 0\n"
    "v_swap_b32 v221, v222\n"
    "v_swap_b32 v225, v226\n"
    "v_swap_b32 v141, v142\n"
    "ds_write2_b64 v144, v[220:221], v[222:223] offset0:32 offset1:40\n"
    "ds_write2_b64 v144, v[224:225], v[226:227] offset0:48 offset1:56\n"
    "ds_write2_b64 v144, v[140:141], v[142:143] offset0:64 offset1:72\n"
    "ds_write2_b64 v144, v[204:205], v[206:207] offset0:80 offset1:88\n"
    "s_waitcnt vmcnt(31)\n"
    "v_fma_f32 v4, v4, v180, -v184\n"
    "v_fma_f32 v5, v5, v181, -v185\n"
    "v_fma_f32 v6, v6, v182, -v186\n"
    "s_add_u32 m0, s65, 77824\n"
    "s_add_u32 s72, s85, 10240\n"
    "buffer_load_dwordx4 v89, s[20:23], s72 offen offset:0 nt lds\n"
    "v_fma_f32 v7, v7, v183, -v187\n"
    "v_fma_f32 v8, v8, v180, -v184\n"
    "v_fma_f32 v9, v9, v181, -v185\n"
    "v_fma_f32 v10, v10, v182, -v186\n"
    "v_fma_f32 v11, v11, v183, -v187\n"
    "v_cvt_pk_bf16_f32 v4, v4, v5\n"
    "v_cvt_pk_bf16_f32 v5, v6, v7\n"
    "v_cvt_pk_bf16_f32 v6, v8, v9\n"
    "v_cvt_pk_bf16_f32 v7, v10, v11\n"
    "ds_write_b64 v163, v[4:5] offset:0\n"
    "ds_write_b64 v163, v[6:7] offset:1024\n"
    "v_fma_f32 v12, v12, v188, -v192\n"
    "v_fma_f32 v13, v13, v189, -v193\n"
    "v_fma_f32 v14, v14, v190, -v194\n"
    "v_fma_f32 v15, v15, v191, -v195\n"
    "v_fma_f32 v16, v16, v188, -v192\n"
    "v_fma_f32 v17, v17, v189, -v193\n"
    "v_fma_f32 v18, v18, v190, -v194\n"
    "v_fma_f32 v19, v19, v191, -v195\n"
    "v_cvt_pk_bf16_f32 v12, v12, v13\n"
    "s_add_u32 m0, s65, 78848\n"
    "s_add_u32 s72, s85, 11264\n"
    "buffer_load_dwordx4 v90, s[20:23], s72 offen offset:0 nt lds\n"
    "v_cvt_pk_bf16_f32 v13, v14, v15\n"
    "v_cvt_pk_bf16_f32 v14, v16, v17\n"
    "v_cvt_pk_bf16_f32 v15, v18, v19\n"
    "ds_write_b64 v163, v[12:13] offset:2048\n"
    "ds_write_b64 v163, v[14:15] offset:3072\n"
    "v_fma_f32 v20, v20, v196, -v200\n"
    "v_fma_f32 v21, v21, v197, -v201\n"
    "v_fma_f32 v22, v22, v198, -v202\n"
    "v_fma_f32 v23, v23, v199, -v203\n"
    "v_fma_f32 v24, v24, v196, -v200\n"
    "v_fma_f32 v25, v25, v197, -v201\n"
    "v_fma_f32 v26, v26, v198, -v202\n"
    "v_fma_f32 v27, v27, v199, -v203\n"
    "v_cvt_pk_bf16_f32 v20, v20, v21\n"
    "v_cvt_pk_bf16_f32 v21, v22, v23\n"
    "v_cvt_pk_bf16_f32 v22, v24, v25\n"
    "v_cvt_pk_bf16_f32 v23, v26, v27\n"
    "ds_write_b64 v163, v[20:21] offset:4096\n"
    "ds_write_b64 v163, v[22:23] offset:5120\n"
    "s_waitcnt vmcnt(16)\n"
    "v_fma_f32 v100, v100, v180, -v184\n"
    "s_add_u32 m0, s65, 49152\n"
    "s_add_u32 s72, s85, 12288\n"
    "buffer_load_dwordx4 v88, s[20:23], s72 offen offset:0 nt lds\n"
    "v_fma_f32 v101, v101, v181, -v185\n"
    "v_fma_f32 v102, v102, v182, -v186\n"
    "v_fma_f32 v103, v103, v183, -v187\n"
    "v_fma_f32 v104, v104, v180, -v184\n"
    "v_fma_f32 v105, v105, v181, -v185\n"
    "v_fma_f32 v106, v106, v182, -v186\n"
    "v_fma_f32 v107, v107, v183, -v187\n"
    "v_cvt_pk_bf16_f32 v100, v100, v101\n"
    "v_cvt_pk_bf16_f32 v101, v102, v103\n"
    "v_cvt_pk_bf16_f32 v102, v104, v105\n"
    "v_cvt_pk_bf16_f32 v103, v106, v107\n"
    "ds_write_b64 v163, v[100:101] offset:12288\n"
    "ds_write_b64 v163, v[102:103] offset:13312\n"
    "v_fma_f32 v108, v108, v188, -v192\n"
    "v_fma_f32 v109, v109, v189, -v193\n"
    "v_fma_f32 v110, v110, v190, -v194\n"
    "v_fma_f32 v111, v111, v191, -v195\n"
    "v_fma_f32 v112, v112, v188, -v192\n"
    "v_fma_f32 v113, v113, v189, -v193\n"
    "v_fma_f32 v114, v114, v190, -v194\n"
    "s_add_u32 m0, s65, 50176\n"
    "s_add_u32 s72, s85, 13312\n"
    "buffer_load_dwordx4 v89, s[20:23], s72 offen offset:0 nt lds\n"
    "v_fma_f32 v115, v115, v191, -v195\n"
    "v_cvt_pk_bf16_f32 v108, v108, v109\n"
    "v_cvt_pk_bf16_f32 v109, v110, v111\n"
    "v_cvt_pk_bf16_f32 v110, v112, v113\n"
    "v_cvt_pk_bf16_f32 v111, v114, v115\n"
    "ds_write_b64 v163, v[108:109] offset:14336\n"
    "ds_write_b64 v163, v[110:111] offset:15360\n"
    "v_fma_f32 v116, v116, v196, -v200\n"
    "v_fma_f32 v117, v117, v197, -v201\n"
    "v_fma_f32 v118, v118, v198, -v202\n"
    "v_fma_f32 v119, v119, v199, -v203\n"
    "v_fma_f32 v120, v120, v196, -v200\n"
    "v_fma_f32 v121, v121, v197, -v201\n"
    "v_fma_f32 v122, v122, v198, -v202\n"
    "v_fma_f32 v123, v123, v199, -v203\n"
    "v_cvt_pk_bf16_f32 v116, v116, v117\n"
    "v_cvt_pk_bf16_f32 v117, v118, v119\n"
    "v_cvt_pk_bf16_f32 v118, v120, v121\n"
    "v_cvt_pk_bf16_f32 v119, v122, v123\n"
    "ds_write_b64 v163, v[116:117] offset:16384\n"
    "ds_write_b64 v163, v[118:119] offset:17408\n"
    "s_waitcnt vmcnt(19)\n"
    "ds_read_b128 v[36:39], v154 offset:0\n"
    "ds_read_b128 v[40:43], v155 offset:0\n"
    "ds_read_b128 v[44:47], v154 offset:128\n"
    "ds_read_b128 v[48:51], v155 offset:128\n"
    "ds_read_b128 v[52:55], v154 offset:256\n"
    "ds_read_b128 v[56:59], v155 offset:256\n"
    "s_waitcnt lgkmcnt(0)\n"
    "s_barrier\n"
    "ds_read_b128 a[112:115], v2 offset:0\n"
    "ds_read_b128 a[116:119], v2 offset:2048\n"
    "ds_read_b128 a[120:123], v2 offset:4096\n"
    "ds_read_b128 a[124:127], v2 offset:6144\n"
    "ds_read_b128 a[128:131], v2 offset:8192\n"
    "ds_read_b128 a[132:135], v2 offset:10240\n"
    "ds_read_b128 a[136:139], v2 offset:1024\n"
    "ds_read_b128 a[140:143], v2 offset:3072\n"
    "ds_read_b128 a[144:147], v2 offset:5120\n"
    "ds_read_b128 a[148:151], v2 offset:7168\n"
    "ds_read_b128 a[152:155], v2 offset:9216\n"
    "ds_read_b128 a[156:159], v2 offset:11264\n"
    "s_add_u32 m0, s65, 51200\n"
    "s_add_u32 s72, s85, 14336\n"
    "buffer_load_dwordx4 v90, s[20:23], s72 offen offset:0 nt lds\n"
    "s_add_u32 m0, s65, 52224\n"
    "s_add_u32 s72, s85, 15360\n"
    "buffer_load_dwordx4 v88, s[20:23], s72 offen offset:0 nt lds\n"
    "v_pk_maximum3_f16 v80, v36, v37, v80\n"
    "v_pk_maximum3_f16 v80, v38, v39, v80\n"
    "v_pk_add_u16 v36, s89, v36 clamp\n"
    "v_pk_add_u16 v37, s89, v37 clamp\n"
    "v_pk_add_u16 v38, s89, v38 clamp\n"
    "v_pk_add_u16 v39, s89, v39 clamp\n"
    "v_pk_maximum3_f16 v80, v40, v41, v80\n"
    "v_pk_maximum3_f16 v80, v42, v43, v80\n"
    "v_pk_add_u16 v40, s89, v40 clamp\n"
    "v_pk_add_u16 v41, s89, v41 clamp\n"
    "v_pk_add_u16 v42, s89, v42 clamp\n"
    "v_pk_add_u16 v43, s89, v43 clamp\n"
    "v_pk_maximum3_f16 v80, v44, v45, v80\n"
    "v_pk_maximum3_f16 v80, v46, v47, v80\n"
    "v_pk_add_u16 v44, s89, v44 clamp\n"
    "v_pk_add_u16 v45, s89, v45 clamp\n"
    "v_pk_add_u16 v46, s89, v46 clamp\n"
    "v_pk_add_u16 v47, s89, v47 clamp\n"
    "v_pk_maximum3_f16 v80, v48, v49, v80\n"
    "v_pk_maximum3_f16 v80, v50, v51, v80\n"
    "v_pk_add_u16 v48, s89, v48 clamp\n"
    "v_pk_add_u16 v49, s89, v49 clamp\n"
    "v_pk_add_u16 v50, s89, v50 clamp\n"
    "v_pk_add_u16 v51, s89, v51 clamp\n"
    "v_pk_maximum3_f16 v80, v52, v53, v80\n"
    "v_pk_maximum3_f16 v80, v54, v55, v80\n"
    "v_pk_add_u16 v52, s89, v52 clamp\n"
    "v_pk_add_u16 v53, s89, v53 clamp\n"
    "v_pk_add_u16 v54, s89, v54 clamp\n"
    "v_pk_add_u16 v55, s89, v55 clamp\n"
    "v_pk_maximum3_f16 v80, v56, v57, v80\n"
    "v_pk_maximum3_f16 v80, v58, v59, v80\n"
    "v_pk_add_u16 v56, s89, v56 clamp\n"
    "v_pk_add_u16 v57, s89, v57 clamp\n"
    "v_pk_add_u16 v58, s89, v58 clamp\n"
    "v_pk_add_u16 v59, s89, v59 clamp\n"
    "v_mov_b32 v108, 0\n"
    "v_mov_b32 v109, 0\n"
    "v_mov_b32 v110, 0\n"
    "v_mov_b32 v111, 0\n"
    "v_mov_b32 v112, 0\n"
    "v_mov_b32 v113, 0\n"
    "v_mov_b32 v114, 0\n"
    "v_mov_b32 v115, 0\n"
    "v_mov_b32 v116, 0\n"
    "v_mov_b32 v117, 0\n"
    "v_mov_b32 v118, 0\n"
    "v_mov_b32 v119, 0\n"
    "v_mov_b32 v120, 0\n"
    "v_mov_b32 v121, 0\n"
    "v_mov_b32 v122, 0\n"
    "v_mov_b32 v123, 0\n"
    "s_waitcnt lgkmcnt(11)\n"
    "v_mfma_f32_16x16x32_bf16 v[4:7], a[112:115], v[36:39], 0\n"
    "s_waitcnt lgkmcnt(10)\n"
    "v_mfma_f32_16x16x32_bf16 v[4:7], a[116:119], v[40:43], v[4:7]\n"
    "s_waitcnt lgkmcnt(9)\n"
    "v_mfma_f32_16x16x32_bf16 v[4:7], a[120:123], v[44:47], v[4:7]\n"
    "s_waitcnt lgkmcnt(8)\n"
    "v_mfma_f32_16x16x32_bf16 v[4:7], a[124:127], v[48:51], v[4:7]\n"
    "s_waitcnt lgkmcnt(7)\n"
    "v_mfma_f32_16x16x32_bf16 v[4:7], a[128:131], v[52:55], v[4:7]\n"
    "s_waitcnt lgkmcnt(6)\n"
    "v_mfma_f32_16x16x32_bf16 v[4:7], a[132:135], v[56:59], v[4:7]\n"
    "s_waitcnt lgkmcnt(5)\n"
    "v_mfma_f32_16x16x32_bf16 v[20:23], a[136:139], v[36:39], 0\n"
    "v_accvgpr_write_b32 a192, v36\n"
    "v_accvgpr_write_b32 a193, v37\n"
    "v_accvgpr_write_b32 a194, v38\n"
    "v_accvgpr_write_b32 a195, v39\n"
    "s_waitcnt lgkmcnt(4)\n"
    "v_mfma_f32_16x16x32_bf16 v[20:23], a[140:143], v[40:43], v[20:23]\n"
    "v_accvgpr_write_b32 a196, v40\n"
    "v_accvgpr_write_b32 a197, v41\n"
    "v_accvgpr_write_b32 a198, v42\n"
    "v_accvgpr_write_b32 a199, v43\n"
    "s_waitcnt lgkmcnt(3)\n"
    "v_mfma_f32_16x16x32_bf16 v[20:23], a[144:147], v[44:47], v[20:23]\n"
    "v_accvgpr_write_b32 a200, v44\n"
    "v_accvgpr_write_b32 a201, v45\n"
    "v_accvgpr_write_b32 a202, v46\n"
    "v_accvgpr_write_b32 a203, v47\n"
    "s_waitcnt lgkmcnt(2)\n"
    "v_mfma_f32_16x16x32_bf16 v[20:23], a[148:151], v[48:51], v[20:23]\n"
    "v_accvgpr_write_b32 a204, v48\n"
    "v_accvgpr_write_b32 a205, v49\n"
    "v_accvgpr_write_b32 a206, v50\n"
    "v_accvgpr_write_b32 a207, v51\n"
    "s_waitcnt lgkmcnt(1)\n"
    "v_mfma_f32_16x16x32_bf16 v[20:23], a[152:155], v[52:55], v[20:23]\n"
    "v_accvgpr_write_b32 a208, v52\n"
    "v_accvgpr_write_b32 a209, v53\n"
    "v_accvgpr_write_b32 a210, v54\n"
    "v_accvgpr_write_b32 a211, v55\n"
    "s_waitcnt lgkmcnt(0)\n"
    "v_mfma_f32_16x16x32_bf16 v[20:23], a[156:159], v[56:59], v[20:23]\n"
    "v_accvgpr_write_b32 a212, v56\n"
    "v_accvgpr_write_b32 a213, v57\n"
    "v_accvgpr_write_b32 a214, v58\n"
    "v_accvgpr_write_b32 a215, v59\n"
    "s_add_u32 m0, s65, 53248\n"
    "s_add_u32 s72, s85, 16384\n"
    "buffer_load_dwordx4 v89, s[20:23], s72 offen offset:0 nt lds\n"
    "buffer_load_dwordx2 v[236:237], v87, s[36:39], 0 offen offset:0 sc1\n"
    "v_mov_b32 v77, 155904\n"
    "v_max3_f32 v76, v4, v5, v6\n"
    "v_max3_f32 v76, v76, v7, v20\n"
    "v_max3_f32 v76, v76, v21, v22\n"
    "v_max_f32 v76, v76, v23\n"
    "v_mov_b32 v78, v76\n"
    "s_nop 1\n"
    "v_permlane32_swap_b32 v76, v78\n"
    "v_max_f32 v76, v76, v78\n"
    "v_mov_b32 v78, v76\n"
    "s_nop 1\n"
    "v_permlane16_swap_b32 v76, v78\n"
    "v_max_f32 v76, v76, v78\n"
    "ds_max_f32 v77, v76\n"
    "s_add_u32 m0, s65, 54272\n"
    "s_add_u32 s72, s85, 17408\n"
    "buffer_load_dwordx4 v90, s[20:23], s72 offen offset:0 nt lds\n"
    "s_add_u32 m0, s65, 98304\n"
    "s_add_u32 s72, s85, 18432\n"
    "buffer_load_dwordx4 v88, s[20:23], s72 offen offset:0 nt lds\n"
    "s_waitcnt lgkmcnt(0)\n"
    "s_barrier\n"
    "ds_read_b32 v76, v77\n"
    "s_waitcnt vmcnt(14)\n"
    "buffer_store_dwordx2 v[238:239], v240, s[36:39], s80 offen offset:0 sc1\n"
    "s_lshl_b32 s73, s79, 8\n"
    "s_add_u32 s80, s80, s73\n"
    "s_mul_i32 s73, s79, 98304\n"
    "s_add_u32 s71, s71, s73\n"
    "s_waitcnt lgkmcnt(0)\n"
    "v_add_f32 v76, 0xc2b00000, v76\n"
    "v_sub_f32 v68, v68, v76\n"
    "v_sub_f32 v69, v69, v76\n"
    "v_sub_f32 v70, v70, v76\n"
    "v_sub_f32 v71, v71, v76\n"
    "v_sub_f32 v72, v72, v76\n"
    "v_sub_f32 v73, v73, v76\n"
    "v_sub_f32 v74, v74, v76\n"
    "v_sub_f32 v75, v75, v76\n"
    "ds_write_b128 v86, v[68:71] offset:0\n"
    "ds_write_b128 v86, v[72:75] offset:16\n"
    "s_mul_i32 s73, s79, 768\n"
    "v_add_u32 v77, s73, v250\n"
    "s_add_u32 m0, s65, 99328\n"
    "s_add_u32 s72, s85, 19456\n"
    "buffer_load_dwordx4 v89, s[20:23], s72 offen offset:0 nt lds\n"
    "s_waitcnt lgkmcnt(0)\n"
    "s_barrier\n"
    "ds_read_b128 v[100:103], v77 offset:0\n"
    "ds_read_b128 v[104:107], v77 offset:16\n"
    "s_add_u32 m0, s65, 100352\n"
    "s_add_u32 s72, s85, 20480\n"
    "buffer_load_dwordx4 v90, s[20:23], s72 offen offset:0 nt lds\n"
    "s_waitcnt lgkmcnt(1)\n"
    "v_add_f32 v4, v4, v100\n"
    "v_add_f32 v5, v5, v101\n"
    "v_add_f32 v6, v6, v102\n"
    "v_add_f32 v7, v7, v103\n"
    "s_waitcnt lgkmcnt(0)\n"
    "v_add_f32 v20, v20, v104\n"
    "v_add_f32 v21, v21, v105\n"
    "v_add_f32 v22, v22, v106\n"
    "v_add_f32 v23, v23, v107\n"
    "v_exp_f32 v4, v4\n"
    "v_exp_f32 v5, v5\n"
    "v_exp_f32 v6, v6\n"
    "v_exp_f32 v7, v7\n"
    "v_exp_f32 v20, v20\n"
    "v_exp_f32 v21, v21\n"
    "v_exp_f32 v22, v22\n"
    "v_exp_f32 v23, v23\n"
    "s_add_u32 m0, s65, 101376\n"
    "s_add_u32 s72, s85, 21504\n"
    "buffer_load_dwordx4 v88, s[20:23], s72 offen offset:0 nt lds\n"
    "s_add_u32 m0, s65, 102400\n"
    "s_add_u32 s72, s85, 22528\n"
    "buffer_load_dwordx4 v89, s[20:23], s72 offen offset:0 nt lds\n"
    "s_waitcnt vmcnt(13)\n"
    "ds_read_b128 v[56:59], v85 offset:24832\n"
    "ds_read_b128 v[52:55], v84 offset:24832\n"
    "ds_read_b128 v[48:51], v85 offset:24704\n"
    "ds_read_b128 v[44:47], v84 offset:24704\n"
    "ds_read_b128 v[40:43], v85 offset:24576\n"
    "ds_read_b128 v[36:39], v84 offset:24576\n"
    "s_waitcnt lgkmcnt(5)\n"
    "v_pk_maximum3_f16 v80, v56, v57, v80\n"
    "v_pk_maximum3_f16 v80, v58, v59, v80\n"
    "v_pk_add_u16 v56, s89, v56 clamp\n"
    "v_pk_add_u16 v57, s89, v57 clamp\n"
    "v_pk_add_u16 v58, s89, v58 clamp\n"
    "v_pk_add_u16 v59, s89, v59 clamp\n"
    "s_waitcnt lgkmcnt(4)\n"
    "v_pk_maximum3_f16 v80, v52, v53, v80\n"
    "v_pk_maximum3_f16 v80, v54, v55, v80\n"
    "v_pk_add_u16 v52, s89, v52 clamp\n"
    "v_pk_add_u16 v53, s89, v53 clamp\n"
    "v_pk_add_u16 v54, s89, v54 clamp\n"
    "v_pk_add_u16 v55, s89, v55 clamp\n"
    "v_mfma_f32_16x16x32_bf16 v[8:11], a[132:135], v[56:59], v[100:103]\n"
    "s_waitcnt lgkmcnt(3)\n"
    "v_pk_maximum3_f16 v80, v48, v49, v80\n"
    "v_pk_maximum3_f16 v80, v50, v51, v80\n"
    "v_pk_add_u16 v48, s89, v48 clamp\n"
    "v_pk_add_u16 v49, s89, v49 clamp\n"
    "v_pk_add_u16 v50, s89, v50 clamp\n"
    "v_pk_add_u16 v51, s89, v51 clamp\n"
    "v_mfma_f32_16x16x32_bf16 v[8:11], a[128:131], v[52:55], v[8:11]\n"
    "s_waitcnt lgkmcnt(2)\n"
    "v_pk_maximum3_f16 v80, v44, v45, v80\n"
    "v_pk_maximum3_f16 v80, v46, v47, v80\n"
    "v_pk_add_u16 v44, s89, v44 clamp\n"
    "v_pk_add_u16 v45, s89, v45 clamp\n"
    "v_pk_add_u16 v46, s89, v46 clamp\n"
    "v_pk_add_u16 v47, s89, v47 clamp\n"
    "v_mfma_f32_16x16x32_bf16 v[8:11], a[124:127], v[48:51], v[8:11]\n"
    "s_waitcnt lgkmcnt(1)\n"
    "v_pk_maximum3_f16 v80, v40, v41, v80\n"
    "v_pk_maximum3_f16 v80, v42, v43, v80\n"
    "v_pk_add_u16 v40, s89, v40 clamp\n"
    "v_pk_add_u16 v41, s89, v41 clamp\n"
    "v_pk_add_u16 v42, s89, v42 clamp\n"
    "v_pk_add_u16 v43, s89, v43 clamp\n"
    "v_mfma_f32_16x16x32_bf16 v[8:11], a[120:123], v[44:47], v[8:11]\n"
    "s_waitcnt lgkmcnt(0)\n"
    "v_pk_maximum3_f16 v80, v36, v37, v80\n"
    "v_pk_maximum3_f16 v80, v38, v39, v80\n"
    "v_pk_add_u16 v36, s89, v36 clamp\n"
    "v_pk_add_u16 v37, s89, v37 clamp\n"
    "v_pk_add_u16 v38, s89, v38 clamp\n"
    "v_pk_add_u16 v39, s89, v39 clamp\n"
    "v_mfma_f32_16x16x32_bf16 v[8:11], a[116:119], v[40:43], v[8:11]\n"
    "s_nop 0\n"
    "v_mfma_f32_16x16x32_bf16 v[8:11], a[112:115], v[36:39], v[8:11]\n"
    "v_mfma_f32_16x16x32_bf16 v[24:27], a[156:159], v[56:59], v[104:107]\n"
    "v_accvgpr_write_b32 a236, v56\n"
    "v_accvgpr_write_b32 a237, v57\n"
    "v_accvgpr_write_b32 a238, v58\n"
    "v_accvgpr_write_b32 a239, v59\n"
    "v_mfma_f32_16x16x32_bf16 v[24:27], a[152:155], v[52:55], v[24:27]\n"
    "v_accvgpr_write_b32 a232, v52\n"
    "v_accvgpr_write_b32 a233, v53\n"
    "v_accvgpr_write_b32 a234, v54\n"
    "v_accvgpr_write_b32 a235, v55\n"
    "v_mfma_f32_16x16x32_bf16 v[24:27], a[148:151], v[48:51], v[24:27]\n"
    "v_accvgpr_write_b32 a228, v48\n"
    "v_accvgpr_write_b32 a229, v49\n"
    "v_accvgpr_write_b32 a230, v50\n"
    "v_accvgpr_write_b32 a231, v51\n"
    "v_mfma_f32_16x16x32_bf16 v[24:27], a[144:147], v[44:47], v[24:27]\n"
    "v_accvgpr_write_b32 a224, v44\n"
    "v_accvgpr_write_b32 a225, v45\n"
    "v_accvgpr_write_b32 a226, v46\n"
    "v_accvgpr_write_b32 a227, v47\n"
    "v_mfma_f32_16x16x32_bf16 v[24:27], a[140:143], v[40:43], v[24:27]\n"
    "v_accvgpr_write_b32 a220, v40\n"
    "v_accvgpr_write_b32 a221, v41\n"
    "v_accvgpr_write_b32 a222, v42\n"
    "v_accvgpr_write_b32 a223, v43\n"
    "v_mfma_f32_16x16x32_bf16 v[24:27], a[136:139], v[36:39], v[24:27]\n"
    "v_accvgpr_write_b32 a216, v36\n"
    "v_accvgpr_write_b32 a217, v37\n"
    "v_accvgpr_write_b32 a218, v38\n"
    "v_accvgpr_write_b32 a219, v39\n"
    "s_add_u32 m0, s65, 103424\n"
    "s_add_u32 s72, s85, 23552\n"
    "buffer_load_dwordx4 v90, s[20:23], s72 offen offset:0 nt lds\n"
    "v_exp_f32 v8, v8\n"
    "v_exp_f32 v9, v9\n"
    "v_exp_f32 v10, v10\n"
    "v_exp_f32 v11, v11\n"
    "v_exp_f32 v24, v24\n"
    "v_exp_f32 v25, v25\n"
    "v_exp_f32 v26, v26\n"
    "v_exp_f32 v27, v27\n"
    "s_waitcnt vmcnt(8)\n"
    "v_cmp_eq_u64 s[54:55], v[236:237], v[238:239]\n"
    "s_nop 0\n"
    "s_cmp_eq_u64 s[54:55], -1\n"
    "s_cbranch_scc0 .LpollAK\n"
    ".LpollAKr:\n"
    "s_add_u32 m0, s65, 24576\n"
    "s_add_u32 s72, s65, 49152\n"
    "buffer_load_dwordx4 v1, s[16:19], s72 offen offset:0 sc1 lds\n"
    "v_mov_b32 v124, 0\n"
    "v_mov_b32 v125, 0\n"
    "v_mov_b32 v126, 0\n"
    "v_mov_b32 v127, 0\n"
    "v_mov_b32 v128, 0\n"
    "v_mov_b32 v129, 0\n"
    "s_add_u32 m0, s65, 25600\n"
    "s_add_u32 s72, s65, 50176\n"
    "buffer_load_dwordx4 v1, s[16:19], s72 offen offset:0 sc1 lds\n"
    "v_mov_b32 v130, 0\n"
    "v_mov_b32 v131, 0\n"
    "v_mov_b32 v132, 0\n"
    "v_mov_b32 v133, 0\n"
    "v_mov_b32 v134, 0\n"
    "v_mov_b32 v135, 0\n"
    "v_mov_b32 v136, 0\n"
    "s_add_u32 m0, s65, 26624\n"
    "s_add_u32 s72, s65, 51200\n"
    "buffer_load_dwordx4 v1, s[16:19], s72 offen offset:0 sc1 lds\n"
    "v_mov_b32 v137, 0\n"
    "v_mov_b32 v138, 0\n"
    "v_mov_b32 v139, 0\n"
    "v_mov_b32 v140, 0\n"
    "v_mov_b32 v141, 0\n"
    "v_mov_b32 v142, 0\n"
    "v_mov_b32 v143, 0\n"
    "s_add_u32 m0, s65, 27648\n"
    "s_add_u32 s72, s65, 52224\n"
    "buffer_load_dwordx4 v1, s[16:19], s72 offen offset:0 sc1 lds\n"
    "v_mov_b32 v144, 0\n"
    "v_mov_b32 v145, 0\n"
    "v_mov_b32 v146, 0\n"
    "v_mov_b32 v147, 0\n"
    "v_mov_b32 v148, 0\n"
    "v_mov_b32 v149, 0\n"
    "s_add_u32 m0, s65, 28672\n"
    "s_add_u32 s72, s65, 53248\n"
    "buffer_load_dwordx4 v1, s[16:19], s72 offen offset:0 sc1 lds\n"
    "v_mov_b32 v150, 0\n"
    "v_mov_b32 v151, 0\n"
    "v_mov_b32 v152, 0\n"
    "v_mov_b32 v153, 0\n"
    "v_mov_b32 v154, 0\n"
    "v_mov_b32 v155, 0\n"
    "v_mov_b32 v156, 0\n"
    "s_add_u32 m0, s65, 29696\n"
    "s_add_u32 s72, s65, 54272\n"
    "buffer_load_dwordx4 v1, s[16:19], s72 offen offset:0 sc1 lds\n"
    "v_mov_b32 v157, 0\n"
    "v_mov_b32 v158, 0\n"
    "v_mov_b32 v159, 0\n"
    "v_mov_b32 v160, 0\n"
    "v_mov_b32 v161, 0\n"
    "v_mov_b32 v162, 0\n"
    "v_mov_b32 v163, 0\n"
    "s_add_u32 m0, s65, 73728\n"
    "s_add_u32 s72, s65, 73728\n"
    "buffer_load_dwordx4 v1, s[16:19], s72 offen offset:0 sc1 lds\n"
    "v_mov_b32 v164, 0\n"
    "v_mov_b32 v165, 0\n"
    "v_mov_b32 v166, 0\n"
    "v_mov_b32 v167, 0\n"
    "v_mov_b32 v168, 0\n"
    "v_mov_b32 v169, 0\n"
    "s_add_u32 m0, s65, 74752\n"
    "s_add_u32 s72, s65, 74752\n"
    "buffer_load_dwordx4 v1, s[16:19], s72 offen offset:0 sc1 lds\n"
    "v_mov_b32 v170, 0\n"
    "v_mov_b32 v171, 0\n"
    "v_mov_b32 v172, 0\n"
    "v_mov_b32 v173, 0\n"
    "v_mov_b32 v174, 0\n"
    "v_mov_b32 v175, 0\n"
    "v_mov_b32 v176, 0\n"
    "s_add_u32 m0, s65, 75776\n"
    "s_add_u32 s72, s65, 75776\n"
    "buffer_load_dwordx4 v1, s[16:19], s72 offen offset:0 sc1 lds\n"
    "v_mov_b32 v177, 0\n"
    "v_mov_b32 v178, 0\n"
    "v_mov_b32 v179, 0\n"
    "v_mov_b32 v180, 0\n"
    "v_mov_b32 v181, 0\n"
    "v_mov_b32 v182, 0\n"
    "v_mov_b32 v183, 0\n"
    "s_add_u32 m0, s65, 76800\n"
    "s_add_u32 s72, s65, 76800\n"
    "buffer_load_dwordx4 v1, s[16:19], s72 offen offset:0 sc1 lds\n"
    "v_mov_b32 v184, 0\n"
    "v_mov_b32 v185, 0\n"
    "v_mov_b32 v186, 0\n"
    "v_mov_b32 v187, 0\n"
    "v_mov_b32 v188, 0\n"
    "v_mov_b32 v189, 0\n"
    "s_add_u32 m0, s65, 77824\n"
    "s_add_u32 s72, s65, 77824\n"
    "buffer_load_dwordx4 v1, s[16:19], s72 offen offset:0 sc1 lds\n"
    "v_mov_b32 v190, 0\n"
    "v_mov_b32 v191, 0\n"
    "v_mov_b32 v192, 0\n"
    "v_mov_b32 v193, 0\n"
    "v_mov_b32 v194, 0\n"
    "v_mov_b32 v195, 0\n"
    "v_mov_b32 v196, 0\n"
    "s_add_u32 m0, s65, 78848\n"
    "s_add_u32 s72, s65, 78848\n"
    "buffer_load_dwordx4 v1, s[16:19], s72 offen offset:0 sc1 lds\n"
    "v_mov_b32 v197, 0\n"
    "v_mov_b32 v198, 0\n"
    "v_mov_b32 v199, 0\n"
    "v_mov_b32 v200, 0\n"
    "v_mov_b32 v201, 0\n"
    "v_mov_b32 v202, 0\n"
    "v_mov_b32 v203, 0\n"
    "s_bitcmp1_b32 s83, 1\n"
    "s_cbranch_scc1 .Lf3b\n"
    "s_waitcnt vmcnt(19)\n"
    "ds_read_b128 v[36:39], v84 offset:0\n"
    "ds_read_b128 v[40:43], v85 offset:0\n"
    "ds_read_b128 v[44:47], v84 offset:128\n"
    "ds_read_b128 v[48:51], v85 offset:128\n"
    "ds_read_b128 v[52:55], v84 offset:256\n"
    "ds_read_b128 v[56:59], v85 offset:256\n"
    "s_waitcnt lgkmcnt(5)\n"
    "v_pk_maximum3_f16 v80, v36, v37, v80\n"
    "v_pk_maximum3_f16 v80, v38, v39, v80\n"
    "v_pk_add_u16 v36, s89, v36 clamp\n"
    "v_pk_add_u16 v37, s89, v37 clamp\n"
    "v_pk_add_u16 v38, s89, v38 clamp\n"
    "v_pk_add_u16 v39, s89, v39 clamp\n"
    "s_waitcnt lgkmcnt(4)\n"
    "v_pk_maximum3_f16 v80, v40, v41, v80\n"
    "v_pk_maximum3_f16 v80, v42, v43, v80\n"
    "v_pk_add_u16 v40, s89, v40 clamp\n"
    "v_pk_add_u16 v41, s89, v41 clamp\n"
    "v_pk_add_u16 v42, s89, v42 clamp\n"
    "v_pk_add_u16 v43, s89, v43 clamp\n"
    "v_mfma_f32_16x16x32_bf16 v[12:15], a[112:115], v[36:39], v[100:103]\n"
    "s_waitcnt lgkmcnt(3)\n"
    "v_pk_maximum3_f16 v80, v44, v45, v80\n"
    "v_pk_maximum3_f16 v80, v46, v47, v80\n"
    "v_pk_add_u16 v44, s89, v44 clamp\n"
    "v_pk_add_u16 v45, s89, v45 clamp\n"
    "v_pk_add_u16 v46, s89, v46 clamp\n"
    "v_pk_add_u16 v47, s89, v47 clamp\n"
    "v_mfma_f32_16x16x32_bf16 v[12:15], a[116:119], v[40:43], v[12:15]\n"
    "s_waitcnt lgkmcnt(2)\n"
    "v_pk_maximum3_f16 v80, v48, v49, v80\n"
    "v_pk_maximum3_f16 v80, v50, v51, v80\n"
    "v_pk_add_u16 v48, s89, v48 clamp\n"
    "v_pk_add_u16 v49, s89, v49 clamp\n"
    "v_pk_add_u16 v50, s89, v50 clamp\n"
    "v_pk_add_u16 v51, s89, v51 clamp\n"
    "v_mfma_f32_16x16x32_bf16 v[12:15], a[120:123], v[44:47], v[12:15]\n"
    "s_waitcnt lgkmcnt(1)\n"
    "v_pk_maximum3_f16 v80, v52, v53, v80\n"
    "v_pk_maximum3_f16 v80, v54, v55, v80\n"
    "v_pk_add_u16 v52, s89, v52 clamp\n"
    "v_pk_add_u16 v53, s89, v53 clamp\n"
    "v_pk_add_u16 v54, s89, v54 clamp\n"
    "v_pk_add_u16 v55, s89, v55 clamp\n"
    "v_mfma_f32_16x16x32_bf16 v[12:15], a[124:127], v[48:51], v[12:15]\n"
    "s_waitcnt lgkmcnt(0)\n"
    "v_pk_maximum3_f16 v80, v56, v57, v80\n"
    "v_pk_maximum3_f16 v80, v58, v59, v80\n"
    "v_pk_add_u16 v56, s89, v56 clamp\n"
    "v_pk_add_u16 v57, s89, v57 clamp\n"
    "v_pk_add_u16 v58, s89, v58 clamp\n"
    "v_pk_add_u16 v59, s89, v59 clamp\n"
    "v_mfma_f32_16x16x32_bf16 v[12:15], a[128:131], v[52:55], v[12:15]\n"
    "s_nop 0\n"
    "v_mfma_f32_16x16x32_bf16 v[12:15], a[132:135], v[56:59], v[12:15]\n"
    "v_mfma_f32_16x16x32_bf16 v[28:31], a[136:139], v[36:39], v[104:107]\n"
    "v_accvgpr_write_b32 a240, v36\n"
    "v_accvgpr_write_b32 a241, v37\n"
    "v_accvgpr_write_b32 a242, v38\n"
    "v_accvgpr_write_b32 a243, v39\n"
    "v_mfma_f32_16x16x32_bf16 v[28:31], a[140:143], v[40:43], v[28:31]\n"
    "v_accvgpr_write_b32 a244, v40\n"
    "v_accvgpr_write_b32 a245, v41\n"
    "v_accvgpr_write_b32 a246, v42\n"
    "v_accvgpr_write_b32 a247, v43\n"
    "v_mfma_f32_16x16x32_bf16 v[28:31], a[144:147], v[44:47], v[28:31]\n"
    "v_accvgpr_write_b32 a248, v44\n"
    "v_accvgpr_write_b32 a249, v45\n"
    "v_accvgpr_write_b32 a250, v46\n"
    "v_accvgpr_write_b32 a251, v47\n"
    "v_mfma_f32_16x16x32_bf16 v[28:31], a[148:151], v[48:51], v[28:31]\n"
    "v_accvgpr_write_b32 a252, v48\n"
    "v_accvgpr_write_b32 a253, v49\n"
    "v_accvgpr_write_b32 a254, v50\n"
    "v_accvgpr_write_b32 a255, v51\n"
    "v_mfma_f32_16x16x32_bf16 v[28:31], a[152:155], v[52:55], v[28:31]\n"
    "v_accvgpr_write_b32 a160, v52\n"
    "v_accvgpr_write_b32 a161, v53\n"
    "v_accvgpr_write_b32 a162, v54\n"
    "v_accvgpr_write_b32 a163, v55\n"
    "v_mfma_f32_16x16x32_bf16 v[28:31], a[156:159], v[56:59], v[28:31]\n"
    "v_accvgpr_write_b32 a164, v56\n"
    "v_accvgpr_write_b32 a165, v57\n"
    "v_accvgpr_write_b32 a166, v58\n"
    "v_accvgpr_write_b32 a167, v59\n"
    "v_exp_f32 v12, v12\n"
    "v_exp_f32 v13, v13\n"
    "v_exp_f32 v14, v14\n"
    "v_exp_f32 v15, v15\n"
    "v_exp_f32 v28, v28\n"
    "v_exp_f32 v29, v29\n"
    "v_exp_f32 v30, v30\n"
    "v_exp_f32 v31, v31\n"
    "s_waitcnt vmcnt(12)\n"
    "ds_read_b128 v[56:59], v85 offset:49408\n"
    "ds_read_b128 v[52:55], v84 offset:49408\n"
    "ds_read_b128 v[48:51], v85 offset:49280\n"
    "ds_read_b128 v[44:47], v84 offset:49280\n"
    "ds_read_b128 v[40:43], v85 offset:49152\n"
    "ds_read_b128 v[36:39], v84 offset:49152\n"
    "s_waitcnt lgkmcnt(5)\n"
    "v_pk_maximum3_f16 v80, v56, v57, v80\n"
    "v_pk_maximum3_f16 v80, v58, v59, v80\n"
    "v_pk_add_u16 v56, s89, v56 clamp\n"
    "v_pk_add_u16 v57, s89, v57 clamp\n"
    "v_pk_add_u16 v58, s89, v58 clamp\n"
    "v_pk_add_u16 v59, s89, v59 clamp\n"
    "s_waitcnt lgkmcnt(4)\n"
    "v_pk_maximum3_f16 v80, v52, v53, v80\n"
    "v_pk_maximum3_f16 v80, v54, v55, v80\n"
    "v_pk_add_u16 v52, s89, v52 clamp\n"
    "v_pk_add_u16 v53, s89, v53 clamp\n"
    "v_pk_add_u16 v54, s89, v54 clamp\n"
    "v_pk_add_u16 v55, s89, v55 clamp\n"
    "v_mfma_f32_16x16x32_bf16 v[16:19], a[132:135], v[56:59], v[100:103]\n"
    "s_waitcnt lgkmcnt(3)\n"
    "v_pk_maximum3_f16 v80, v48, v49, v80\n"
    "v_pk_maximum3_f16 v80, v50, v51, v80\n"
    "v_pk_add_u16 v48, s89, v48 clamp\n"
    "v_pk_add_u16 v49, s89, v49 clamp\n"
    "v_pk_add_u16 v50, s89, v50 clamp\n"
    "v_pk_add_u16 v51, s89, v51 clamp\n"
    "v_mfma_f32_16x16x32_bf16 v[16:19], a[128:131], v[52:55], v[16:19]\n"
    "s_waitcnt lgkmcnt(2)\n"
    "v_pk_maximum3_f16 v80, v44, v45, v80\n"
    "v_pk_maximum3_f16 v80, v46, v47, v80\n"
    "v_pk_add_u16 v44, s89, v44 clamp\n"
    "v_pk_add_u16 v45, s89, v45 clamp\n"
    "v_pk_add_u16 v46, s89, v46 clamp\n"
    "v_pk_add_u16 v47, s89, v47 clamp\n"
    "v_mfma_f32_16x16x32_bf16 v[16:19], a[124:127], v[48:51], v[16:19]\n"
    "s_waitcnt lgkmcnt(1)\n"
    "v_pk_maximum3_f16 v80, v40, v41, v80\n"
    "v_pk_maximum3_f16 v80, v42, v43, v80\n"
    "v_pk_add_u16 v40, s89, v40 clamp\n"
    "v_pk_add_u16 v41, s89, v41 clamp\n"
    "v_pk_add_u16 v42, s89, v42 clamp\n"
    "v_pk_add_u16 v43, s89, v43 clamp\n"
    "v_mfma_f32_16x16x32_bf16 v[16:19], a[120:123], v[44:47], v[16:19]\n"
    "s_waitcnt lgkmcnt(0)\n"
    "v_pk_maximum3_f16 v80, v36, v37, v80\n"
    "v_pk_maximum3_f16 v80, v38, v39, v80\n"
    "v_pk_add_u16 v36, s89, v36 clamp\n"
    "v_pk_add_u16 v37, s89, v37 clamp\n"
    "v_pk_add_u16 v38, s89, v38 clamp\n"
    "v_pk_add_u16 v39, s89, v39 clamp\n"
    "v_mfma_f32_16x16x32_bf16 v[16:19], a[116:119], v[40:43], v[16:19]\n"
    "s_nop 0\n"
    "v_mfma_f32_16x16x32_bf16 v[16:19], a[112:115], v[36:39], v[16:19]\n"
    "v_mfma_f32_16x16x32_bf16 v[32:35], a[156:159], v[56:59], v[104:107]\n"
    "v_accvgpr_write_b32 a188, v56\n"
    "v_accvgpr_write_b32 a189, v57\n"
    "v_accvgpr_write_b32 a190, v58\n"
    "v_accvgpr_write_b32 a191, v59\n"
    "v_mfma_f32_16x16x32_bf16 v[32:35], a[152:155], v[52:55], v[32:35]\n"
    "v_accvgpr_write_b32 a184, v52\n"
    "v_accvgpr_write_b32 a185, v53\n"
    "v_accvgpr_write_b32 a186, v54\n"
    "v_accvgpr_write_b32 a187, v55\n"
    "v_mfma_f32_16x16x32_bf16 v[32:35], a[148:151], v[48:51], v[32:35]\n"
    "v_accvgpr_write_b32 a180, v48\n"
    "v_accvgpr_write_b32 a181, v49\n"
    "v_accvgpr_write_b32 a182, v50\n"
    "v_accvgpr_write_b32 a183, v51\n"
    "v_mfma_f32_16x16x32_bf16 v[32:35], a[144:147], v[44:47], v[32:35]\n"
    "v_accvgpr_write_b32 a176, v44\n"
    "v_accvgpr_write_b32 a177, v45\n"
    "v_accvgpr_write_b32 a178, v46\n"
    "v_accvgpr_write_b32 a179, v47\n"
    "v_mfma_f32_16x16x32_bf16 v[32:35], a[140:143], v[40:43], v[32:35]\n"
    "v_accvgpr_write_b32 a172, v40\n"
    "v_accvgpr_write_b32 a173, v41\n"
    "v_accvgpr_write_b32 a174, v42\n"
    "v_accvgpr_write_b32 a175, v43\n"
    "v_mfma_f32_16x16x32_bf16 v[32:35], a[136:139], v[36:39], v[32:35]\n"
    "v_accvgpr_write_b32 a168, v36\n"
    "v_accvgpr_write_b32 a169, v37\n"
    "v_accvgpr_write_b32 a170, v38\n"
    "v_accvgpr_write_b32 a171, v39\n"
    "ds_read_b128 a[112:115], v2 offset:12288\n"
    "ds_read_b128 a[116:119], v2 offset:14336\n"
    "ds_read_b128 a[120:123], v2 offset:16384\n"
    "ds_read_b128 a[124:127], v2 offset:18432\n"
    "ds_read_b128 a[128:131], v2 offset:20480\n"
    "ds_read_b128 a[132:135], v2 offset:22528\n"
    "ds_read_b128 v[100:103], v77 offset:128\n"
    "v_pk_add_u16 v80, s89, v80\n"
    "v_and_b32 v80, 0x80008000, v80\n"
    "v_cmp_ne_u32 s[34:35], 0, v80\n"
    "v_exp_f32 v16, v16\n"
    "v_exp_f32 v17, v17\n"
    "v_exp_f32 v18, v18\n"
    "v_exp_f32 v19, v19\n"
    "v_exp_f32 v32, v32\n"
    "v_exp_f32 v33, v33\n"
    "v_exp_f32 v34, v34\n"
    "v_exp_f32 v35, v35\n"
    "s_mov_b32 s66, 0\n"
    "s_waitcnt lgkmcnt(0)\n"
    "s_waitcnt vmcnt(0)\n"
    "ds_add_u32 v247, v246\n"
    "s_nop 7\n"
    "s_branch .LPK\n"
    "s_branch .Lf3e\n"
    ".Lf3b:\n"
    "s_add_u32 s75, s71, s63\n"
    "s_add_u32 s76, s75, 6144\n"
    "s_add_u32 s77, s75, 24576\n"
    "s_add_u32 s78, s75, 30720\n"
    "buffer_load_dwordx2 v[204:205], v242, s[28:31], s75 offen offset:0 nt\n"
    "buffer_load_dwordx2 v[206:207], v242, s[28:31], s75 offen offset:768 nt\n"
    "buffer_load_dwordx2 v[208:209], v242, s[28:31], s75 offen offset:3072 nt\n"
    "buffer_load_dwordx2 v[210:211], v242, s[28:31], s75 offen offset:3840 nt\n"
    "buffer_load_dwordx2 v[212:213], v242, s[28:31], s76 offen offset:0 nt\n"
    "buffer_load_dwordx2 v[214:215], v242, s[28:31], s76 offen offset:768 nt\n"
    "buffer_load_dwordx2 v[216:217], v242, s[28:31], s76 offen offset:3072 nt\n"
    "buffer_load_dwordx2 v[218:219], v242, s[28:31], s76 offen offset:3840 nt\n"
    "buffer_load_dwordx2 v[220:221], v242, s[28:31], s77 offen offset:0 nt\n"
    "buffer_load_dwordx2 v[222:223], v242, s[28:31], s77 offen offset:768 nt\n"
    "buffer_load_dwordx2 v[224:225], v242, s[28:31], s77 offen offset:3072 nt\n"
    "buffer_load_dwordx2 v[226:227], v242, s[28:31], s77 offen offset:3840 nt\n"
    "buffer_load_dwordx2 v[228:229], v242, s[28:31], s78 offen offset:0 nt\n"
    "buffer_load_dwordx2 v[230:231], v242, s[28:31], s78 offen offset:768 nt\n"
    "buffer_load_dwordx2 v[232:233], v242, s[28:31], s78 offen offset:3072 nt\n"
    "buffer_load_dwordx2 v[234:235], v242, s[28:31], s78 offen offset:3840 nt\n"
    "s_waitcnt vmcnt(35)\n"
    "ds_read_b128 v[36:39], v84 offset:0\n"
    "ds_read_b128 v[40:43], v85 offset:0\n"
    "ds_read_b128 v[44:47], v84 offset:128\n"
    "ds_read_b128 v[48:51], v85 offset:128\n"
    "ds_read_b128 v[52:55], v84 offset:256\n"
    "ds_read_b128 v[56:59], v85 offset:256\n"
    "s_waitcnt lgkmcnt(5)\n"
    "v_pk_maximum3_f16 v80, v36, v37, v80\n"
    "v_pk_maximum3_f16 v80, v38, v39, v80\n"
    "v_pk_add_u16 v36, s89, v36 clamp\n"
    "v_pk_add_u16 v37, s89, v37 clamp\n"
    "v_pk_add_u16 v38, s89, v38 clamp\n"
    "v_pk_add_u16 v39, s89, v39 clamp\n"
    "s_waitcnt lgkmcnt(4)\n"
    "v_pk_maximum3_f16 v80, v40, v41, v80\n"
    "v_pk_maximum3_f16 v80, v42, v43, v80\n"
    "v_pk_add_u16 v40, s89, v40 clamp\n"
    "v_pk_add_u16 v41, s89, v41 clamp\n"
    "v_pk_add_u16 v42, s89, v42 clamp\n"
    "v_pk_add_u16 v43, s89, v43 clamp\n"
    "v_mfma_f32_16x16x32_bf16 v[12:15], a[112:115], v[36:39], v[100:103]\n"
    "s_waitcnt lgkmcnt(3)\n"
    "v_pk_maximum3_f16 v80, v44, v45, v80\n"
    "v_pk_maximum3_f16 v80, v46, v47, v80\n"
    "v_pk_add_u16 v44, s89, v44 clamp\n"
    "v_pk_add_u16 v45, s89, v45 clamp\n"
    "v_pk_add_u16 v46, s89, v46 clamp\n"
    "v_pk_add_u16 v47, s89, v47 clamp\n"
    "v_mfma_f32_16x16x32_bf16 v[12:15], a[116:119], v[40:43], v[12:15]\n"
    "s_waitcnt lgkmcnt(2)\n"
    "v_pk_maximum3_f16 v80, v48, v49, v80\n"
    "v_pk_maximum3_f16 v80, v50, v51, v80\n"
    "v_pk_add_u16 v48, s89, v48 clamp\n"
    "v_pk_add_u16 v49, s89, v49 clamp\n"
    "v_pk_add_u16 v50, s89, v50 clamp\n"
    "v_pk_add_u16 v51, s89, v51 clamp\n"
    "v_mfma_f32_16x16x32_bf16 v[12:15], a[120:123], v[44:47], v[12:15]\n"
    "s_waitcnt lgkmcnt(1)\n"
    "v_pk_maximum3_f16 v80, v52, v53, v80\n"
    "v_pk_maximum3_f16 v80, v54, v55, v80\n"
    "v_pk_add_u16 v52, s89, v52 clamp\n"
    "v_pk_add_u16 v53, s89, v53 clamp\n"
    "v_pk_add_u16 v54, s89, v54 clamp\n"
    "v_pk_add_u16 v55, s89, v55 clamp\n"
    "v_mfma_f32_16x16x32_bf16 v[12:15], a[124:127], v[48:51], v[12:15]\n"
    "s_waitcnt lgkmcnt(0)\n"
    "v_pk_maximum3_f16 v80, v56, v57, v80\n"
    "v_pk_maximum3_f16 v80, v58, v59, v80\n"
    "v_pk_add_u16 v56, s89, v56 clamp\n"
    "v_pk_add_u16 v57, s89, v57 clamp\n"
    "v_pk_add_u16 v58, s89, v58 clamp\n"
    "v_pk_add_u16 v59, s89, v59 clamp\n"
    "v_mfma_f32_16x16x32_bf16 v[12:15], a[128:131], v[52:55], v[12:15]\n"
    "s_nop 0\n"
    "v_mfma_f32_16x16x32_bf16 v[12:15], a[132:135], v[56:59], v[12:15]\n"
    "v_mfma_f32_16x16x32_bf16 v[28:31], a[136:139], v[36:39], v[104:107]\n"
    "v_accvgpr_write_b32 a240, v36\n"
    "v_accvgpr_write_b32 a241, v37\n"
    "v_accvgpr_write_b32 a242, v38\n"
    "v_accvgpr_write_b32 a243, v39\n"
    "v_mfma_f32_16x16x32_bf16 v[28:31], a[140:143], v[40:43], v[28:31]\n"
    "v_accvgpr_write_b32 a244, v40\n"
    "v_accvgpr_write_b32 a245, v41\n"
    "v_accvgpr_write_b32 a246, v42\n"
    "v_accvgpr_write_b32 a247, v43\n"
    "v_mfma_f32_16x16x32_bf16 v[28:31], a[144:147], v[44:47], v[28:31]\n"
    "v_accvgpr_write_b32 a248, v44\n"
    "v_accvgpr_write_b32 a249, v45\n"
    "v_accvgpr_write_b32 a250, v46\n"
    "v_accvgpr_write_b32 a251, v47\n"
    "v_mfma_f32_16x16x32_bf16 v[28:31], a[148:151], v[48:51], v[28:31]\n"
    "v_accvgpr_write_b32 a252, v48\n"
    "v_accvgpr_write_b32 a253, v49\n"
    "v_accvgpr_write_b32 a254, v50\n"
    "v_accvgpr_write_b32 a255, v51\n"
    "v_mfma_f32_16x16x32_bf16 v[28:31], a[152:155], v[52:55], v[28:31]\n"
    "v_accvgpr_write_b32 a160, v52\n"
    "v_accvgpr_write_b32 a161, v53\n"
    "v_accvgpr_write_b32 a162, v54\n"
    "v_accvgpr_write_b32 a163, v55\n"
    "v_mfma_f32_16x16x32_bf16 v[28:31], a[156:159], v[56:59], v[28:31]\n"
    "v_accvgpr_write_b32 a164, v56\n"
    "v_accvgpr_write_b32 a165, v57\n"
    "v_accvgpr_write_b32 a166, v58\n"
    "v_accvgpr_write_b32 a167, v59\n"
    "v_exp_f32 v12, v12\n"
    "v_exp_f32 v13, v13\n"
    "v_exp_f32 v14, v14\n"
    "v_exp_f32 v15, v15\n"
    "v_exp_f32 v28, v28\n"
    "v_exp_f32 v29, v29\n"
    "v_exp_f32 v30, v30\n"
    "v_exp_f32 v31, v31\n"
    "s_waitcnt vmcnt(28)\n"
    "ds_read_b128 v[56:59], v85 offset:49408\n"
    "ds_read_b128 v[52:55], v84 offset:49408\n"
    "ds_read_b128 v[48:51], v85 offset:49280\n"
    "ds_read_b128 v[44:47], v84 offset:49280\n"
    "ds_read_b128 v[40:43], v85 offset:49152\n"
    "ds_read_b128 v[36:39], v84 offset:49152\n"
    "s_waitcnt lgkmcnt(5)\n"
    "v_pk_maximum3_f16 v80, v56, v57, v80\n"
    "v_pk_maximum3_f16 v80, v58, v59, v80\n"
    "v_pk_add_u16 v56, s89, v56 clamp\n"
    "v_pk_add_u16 v57, s89, v57 clamp\n"
    "v_pk_add_u16 v58, s89, v58 clamp\n"
    "v_pk_add_u16 v59, s89, v59 clamp\n"
    "s_waitcnt lgkmcnt(4)\n"
    "v_pk_maximum3_f16 v80, v52, v53, v80\n"
    "v_pk_maximum3_f16 v80, v54, v55, v80\n"
    "v_pk_add_u16 v52, s89, v52 clamp\n"
    "v_pk_add_u16 v53, s89, v53 clamp\n"
    "v_pk_add_u16 v54, s89, v54 clamp\n"
    "v_pk_add_u16 v55, s89, v55 clamp\n"
    "v_mfma_f32_16x16x32_bf16 v[16:19], a[132:135], v[56:59], v[100:103]\n"
    "s_waitcnt lgkmcnt(3)\n"
    "v_pk_maximum3_f16 v80, v48, v49, v80\n"
    "v_pk_maximum3_f16 v80, v50, v51, v80\n"
    "v_pk_add_u16 v48, s89, v48 clamp\n"
    "v_pk_add_u16 v49, s89, v49 clamp\n"
    "v_pk_add_u16 v50, s89, v50 clamp\n"
    "v_pk_add_u16 v51, s89, v51 clamp\n"
    "v_mfma_f32_16x16x32_bf16 v[16:19], a[128:131], v[52:55], v[16:19]\n"
    "s_waitcnt lgkmcnt(2)\n"
    "v_pk_maximum3_f16 v80, v44, v45, v80\n"
    "v_pk_maximum3_f16 v80, v46, v47, v80\n"
    "v_pk_add_u16 v44, s89, v44 clamp\n"
    "v_pk_add_u16 v45, s89, v45 clamp\n"
    "v_pk_add_u16 v46, s89, v46 clamp\n"
    "v_pk_add_u16 v47, s89, v47 clamp\n"
    "v_mfma_f32_16x16x32_bf16 v[16:19], a[124:127], v[48:51], v[16:19]\n"
    "s_waitcnt lgkmcnt(1)\n"
    "v_pk_maximum3_f16 v80, v40, v41, v80\n"
    "v_pk_maximum3_f16 v80, v42, v43, v80\n"
    "v_pk_add_u16 v40, s89, v40 clamp\n"
    "v_pk_add_u16 v41, s89, v41 clamp\n"
    "v_pk_add_u16 v42, s89, v42 clamp\n"
    "v_pk_add_u16 v43, s89, v43 clamp\n"
    "v_mfma_f32_16x16x32_bf16 v[16:19], a[120:123], v[44:47], v[16:19]\n"
    "s_waitcnt lgkmcnt(0)\n"
    "v_pk_maximum3_f16 v80, v36, v37, v80\n"
    "v_pk_maximum3_f16 v80, v38, v39, v80\n"
    "v_pk_add_u16 v36, s89, v36 clamp\n"
    "v_pk_add_u16 v37, s89, v37 clamp\n"
    "v_pk_add_u16 v38, s89, v38 clamp\n"
    "v_pk_add_u16 v39, s89, v39 clamp\n"
    "v_mfma_f32_16x16x32_bf16 v[16:19], a[116:119], v[40:43], v[16:19]\n"
    "s_nop 0\n"
    "v_mfma_f32_16x16x32_bf16 v[16:19], a[112:115], v[36:39], v[16:19]\n"
    "v_mfma_f32_16x16x32_bf16 v[32:35], a[156:159], v[56:59], v[104:107]\n"
    "v_accvgpr_write_b32 a188, v56\n"
    "v_accvgpr_write_b32 a189, v57\n"
    "v_accvgpr_write_b32 a190, v58\n"
    "v_accvgpr_write_b32 a191, v59\n"
    "v_mfma_f32_16x16x32_bf16 v[32:35], a[152:155], v[52:55], v[32:35]\n"
    "v_accvgpr_write_b32 a184, v52\n"
    "v_accvgpr_write_b32 a185, v53\n"
    "v_accvgpr_write_b32 a186, v54\n"
    "v_accvgpr_write_b32 a187, v55\n"
    "v_mfma_f32_16x16x32_bf16 v[32:35], a[148:151], v[48:51], v[32:35]\n"
    "v_accvgpr_write_b32 a180, v48\n"
    "v_accvgpr_write_b32 a181, v49\n"
    "v_accvgpr_write_b32 a182, v50\n"
    "v_accvgpr_write_b32 a183, v51\n"
    "v_mfma_f32_16x16x32_bf16 v[32:35], a[144:147], v[44:47], v[32:35]\n"
    "v_accvgpr_write_b32 a176, v44\n"
    "v_accvgpr_write_b32 a177, v45\n"
    "v_accvgpr_write_b32 a178, v46\n"
    "v_accvgpr_write_b32 a179, v47\n"
    "v_mfma_f32_16x16x32_bf16 v[32:35], a[140:143], v[40:43], v[32:35]\n"
    "v_accvgpr_write_b32 a172, v40\n"
    "v_accvgpr_write_b32 a173, v41\n"
    "v_accvgpr_write_b32 a174, v42\n"
    "v_accvgpr_write_b32 a175, v43\n"
    "v_mfma_f32_16x16x32_bf16 v[32:35], a[136:139], v[36:39], v[32:35]\n"
    "v_accvgpr_write_b32 a168, v36\n"
    "v_accvgpr_write_b32 a169, v37\n"
    "v_accvgpr_write_b32 a170, v38\n"
    "v_accvgpr_write_b32 a171, v39\n"
    "ds_read_b128 a[112:115], v2 offset:12288\n"
    "ds_read_b128 a[116:119], v2 offset:14336\n"
    "ds_read_b128 a[120:123], v2 offset:16384\n"
    "ds_read_b128 a[124:127], v2 offset:18432\n"
    "ds_read_b128 a[128:131], v2 offset:20480\n"
    "ds_read_b128 a[132:135], v2 offset:22528\n"
    "ds_read_b128 v[100:103], v77 offset:128\n"
    "v_pk_add_u16 v80, s89, v80\n"
    "v_and_b32 v80, 0x80008000, v80\n"
    "v_cmp_ne_u32 s[34:35], 0, v80\n"
    "v_exp_f32 v16, v16\n"
    "v_exp_f32 v17, v17\n"
    "v_exp_f32 v18, v18\n"
    "v_exp_f32 v19, v19\n"
    "v_exp_f32 v32, v32\n"
    "v_exp_f32 v33, v33\n"
    "v_exp_f32 v34, v34\n"
    "v_exp_f32 v35, v35\n"
    "s_mov_b32 s66, 0\n"
    "s_waitcnt lgkmcnt(0)\n"
    "s_waitcnt vmcnt(16)\n"
    "ds_add_u32 v247, v246\n"
    "s_nop 7\n"
    "s_branch .LNK\n"
    ".Lf3e:\n"
    "s_nop 4\n"
    "s_branch .Lf5e\n"
    ".Lf5b:\n"
    "s_lshl_b32 s73, s79, 8\n"
    "s_mul_i32 s74, s59, 6144\n"
    "s_add_u32 s73, s73, s74\n"
    "v_add_u32 v163, s73, v244\n"
    "s_lshr_b32 s73, s53, 2\n"
    "s_mul_i32 s73, s73, 12288\n"
    "s_and_b32 s74, s53, 3\n"
    "s_lshl_b32 s74, s74, 8\n"
    "s_add_u32 s73, s73, s74\n"
    "s_cmp_eq_u32 s59, 0\n"
    "s_cselect_b32 s73, 0x7f000000, s73\n"
    "s_add_u32 s60, s73, 24576\n"
    "s_add_u32 s61, s73, 28672\n"
    "s_add_u32 s62, s73, 32768\n"
    "v_mov_b32 v244, v243\n"
    "v_mov_b32 v243, 0\n"
    "buffer_load_dwordx4 v[40:43], v242, s[40:43], s75 offen offset:768 nt\n"
    "v_add_u32 v159, 0, v156\n"
    "v_mul_u32_u24 v159, 2731, v159\n"
    "v_lshrrev_b32 v159, 16, v159\n"
    "v_xor_b32 v159, v159, v156\n"
    "v_and_b32 v159, 7, v159\n"
    "v_and_b32 v88, 0x380, v1\n"
    "v_lshl_add_u32 v88, v159, 4, v88\n"
    "v_add_u32 v159, 64, v156\n"
    "v_mul_u32_u24 v159, 2731, v159\n"
    "v_lshrrev_b32 v159, 16, v159\n"
    "v_xor_b32 v159, v159, v156\n"
    "v_and_b32 v159, 7, v159\n"
    "v_and_b32 v89, 0x380, v1\n"
    "v_lshl_add_u32 v89, v159, 4, v89\n"
    "v_add_u32 v159, 128, v156\n"
    "v_mul_u32_u24 v159, 2731, v159\n"
    "v_lshrrev_b32 v159, 16, v159\n"
    "v_xor_b32 v159, v159, v156\n"
    "v_and_b32 v159, 7, v159\n"
    "v_and_b32 v90, 0x380, v1\n"
    "v_lshl_add_u32 v90, v159, 4, v90\n"
    "v_and_b32 v2, 7, v156\n"
    "v_lshrrev_b32 v144, 3, %3\n"
    "v_mul_u32_u24 v144, 768, v144\n"
    "v_lshl_add_u32 v144, v2, 3, v144\n"
    "buffer_load_dwordx4 v[44:47], v242, s[40:43], s75 offen offset:256 nt\n"
    "v_add_u32 v144, 131072, v144\n"
    "v_mad_u32_u24 v86, v2, 24, v144\n"
    "v_and_b32 v84, 15, v156\n"
    "v_lshrrev_b32 v159, 4, v156\n"
    "v_xor_b32 v159, v159, v2\n"
    "v_mul_u32_u24 v84, 384, v84\n"
    "v_lshl_add_u32 v84, v159, 4, v84\n"
    "s_add_u32 s73, s65, 49152\n"
    "v_add_u32 v84, s73, v84\n"
    "v_xor_b32 v85, 64, v84\n"
    "v_subrev_u32 v154, 24576, v84\n"
    "v_subrev_u32 v155, 24576, v85\n"
    "s_add_u32 s67, s65, 98304\n"
    "s_add_u32 s68, s65, 24576\n"
    "s_add_u32 s69, s65, 73728\n"
    "s_mov_b32 s81, 256\n"
    "s_mul_i32 s74, s79, 24576\n"
    "s_add_u32 s70, s74, s65\n"
    "s_add_u32 s70, s70, 73728\n"
    "s_sub_u32 s64, s65, s74\n"
    "s_add_u32 s64, s64, 98304\n"
    "v_and_b32 v249, 0x310, v1\n"
    "v_and_b32 v159, 12, v156\n"
    "v_lshl_add_u32 v249, v159, 3, v249\n"
    "v_and_b32 v159, 2, v156\n"
    "buffer_load_dwordx4 v[48:51], v242, s[40:43], s75 offen offset:1024 nt\n"
    "v_lshl_add_u32 v249, v159, 6, v249\n"
    "s_add_u32 s73, s74, 73728\n"
    "v_add_u32 v248, s73, v249\n"
    "s_sub_u32 s73, 98304, s74\n"
    "v_add_u32 v249, s73, v249\n"
    "s_or_b32 s73, s53, s84\n"
    "s_cmp_eq_u32 s73, 0\n"
    "s_cselect_b32 s82, 0, 0x7fff0000\n"
    "v_and_b32 v252, 12, v156\n"
    "v_bfe_u32 v159, v156, 1, 1\n"
    "v_or_b32 v252, v252, v159\n"
    "v_and_b32 v159, 1, v156\n"
    "v_lshl_or_b32 v252, v159, 1, v252\n"
    "v_lshlrev_b32 v252, 6, v252\n"
    "v_lshlrev_b32 v159, 2, v156\n"
    "v_and_b32 v159, 32, v159\n"
    "v_xor_b32 v159, v159, v156\n"
    "v_and_or_b32 v252, v159, 48, v252\n"
    "v_mov_b32 v2, v252\n"
    "v_add_u32 v3, 24576, v252\n"
    "v_lshrrev_b32 v91, 3, v156\n"
    "v_lshl_add_u32 v87, v91, 6, 64\n"
    "v_add_u32 v87, 64, v87\n"
    "v_and_b32 v241, 1, v156\n"
    "v_lshlrev_b32 v241, 3, v241\n"
    "buffer_load_dwordx4 v[52:55], v242, s[40:43], s75 offen offset:512 nt\n"
    "v_lshl_add_u32 v241, v91, 4, v241\n"
    "v_add_u32 v241, 1152, v241\n"
    "v_lshlrev_b32 v91, 24, v91\n"
    "v_lshl_add_u32 v91, v156, 3, v91\n"
    "s_lshl_b32 s73, s53, 4\n"
    "s_lshl_b32 s74, s59, 3\n"
    "s_add_u32 s73, s73, s74\n"
    "s_add_u32 s73, s73, 1152\n"
    "v_mov_b32 v240, 0x7fff0000\n"
    "v_writelane_b32 v240, s73, 0\n"
    "v_and_b32 v159, 48, v156\n"
    "s_mul_i32 s73, s79, 768\n"
    "s_sub_u32 s73, 131072, s73\n"
    "v_lshlrev_b32 v250, 1, v159\n"
    "v_add_u32 v250, s73, v250\n"
    "v_and_b32 v251, 3, v156\n"
    "v_lshl_add_u32 v251, v251, 7, v159\n"
    "s_add_u32 s73, s73, 256\n"
    "v_add_u32 v251, s73, v251\n"
    "v_and_b32 v159, 64, v1\n"
    "v_add_u32 v251, v159, v251\n"
    "v_bfe_i32 v159, v156, 2, 1\n"
    "v_and_b32 v159, 0xaaaa, v159\n"
    "v_or_b32 v253, 0x4444, v159\n"
    "s_mov_b32 s58, 0\n"
    "s_mov_b32 s59, 8\n"
    "s_waitcnt vmcnt(14)\n"
    "v_pk_min_i16 v4, v4, v10\n"
    "v_pk_min_i16 v5, v5, v11\n"
    "v_pk_min_i16 v6, v6, v12\n"
    "v_pk_min_i16 v7, v7, v13\n"
    "v_pk_min_i16 v8, v8, v14\n"
    "v_pk_min_i16 v9, v9, v15\n"
    "v_pk_min_i16 v16, v16, v18\n"
    "v_pk_min_i16 v17, v17, v19\n"
    "v_pk_min_i16 v4, v4, v7\n"
    "v_pk_min_i16 v5, v5, v8\n"
    "v_pk_min_i16 v6, v6, v9\n"
    "v_pk_min_i16 v16, v16, v17\n"
    "v_pk_min_i16 v4, v4, v5\n"
    "v_lshlrev_b32 v17, 16, v16\n"
    "v_pk_min_i16 v4, v4, v6\n"
    "v_min_i32 v16, v16, v17\n"
    "v_lshlrev_b32 v5, 16, v4\n"
    "v_mov_b32 v7, 155920\n"
    "v_min_i32 v4, v4, v5\n"
    "v_lshl_add_u32 v6, s84, 2, v7\n"
    "v_min_i32_dpp v16, v16, v16 row_shr:1\n"
    "v_min_i32_dpp v4, v4, v4 row_shr:1\n"
    "s_nop 0\n"
    "v_min_i32_dpp v16, v16, v16 row_shr:2\n"
    "v_min_i32_dpp v4, v4, v4 row_shr:2\n"
    "s_nop 0\n"
    "v_min_i32_dpp v16, v16, v16 row_shr:4\n"
    "v_min_i32_dpp v4, v4, v4 row_shr:4\n"
    "s_nop 0\n"
    "v_min_i32_dpp v16, v16, v16 row_shr:8\n"
    "v_min_i32_dpp v4, v4, v4 row_shr:8\n"
    "s_nop 0\n"
    "v_min_i32_dpp v16, v16, v16 row_bcast:15 row_mask:0xa\n"
    "v_min_i32_dpp v4, v4, v4 row_bcast:15 row_mask:0xa\n"
    "s_nop 0\n"
    "v_min_i32_dpp v16, v16, v16 row_bcast:31 row_mask:0xc\n"
    "v_min_i32_dpp v4, v4, v4 row_bcast:31 row_mask:0xc\n"
    "v_readlane_b32 s35, v16, 63\n"
    "v_readlane_b32 s73, v4, 63\n"
    "s_max_i32 s35, s35, 0\n"
    "s_and_b32 s35, s35, 0xffff0000\n"
    "v_mov_b32 v4, s73\n"
    "ds_write_b32 v6, v4\n"
    "v_mov_b32 v14, -32\n"
    "v_mov_b32 v12, -126\n"
    "v_mov_b32 v13, 0x3dd53b95\n"
    "v_mov_b32 v80, 0\n"
    "s_waitcnt lgkmcnt(0)\n"
    "s_barrier\n"
    "ds_read_b128 v[8:11], v7 offset:0\n"
    "buffer_load_dwordx4 v[56:59], v242, s[40:43], s75 offen offset:1280 nt\n"
    "buffer_load_dwordx4 v[136:139], v130, s[92:95], 0 offen offset:0\n"
    "buffer_load_dwordx4 v[140:143], v130, s[96:99], 0 offen offset:0\n"
    "v_accvgpr_write_b32 a0, 0\n"
    "v_accvgpr_write_b32 a1, 0\n"
    "v_accvgpr_write_b32 a2, 0\n"
    "v_accvgpr_write_b32 a3, 0\n"
    "v_accvgpr_write_b32 a4, 0\n"
    "v_accvgpr_write_b32 a5, 0\n"
    "v_accvgpr_write_b32 a6, 0\n"
    "v_accvgpr_write_b32 a7, 0\n"
    "v_accvgpr_write_b32 a8, 0\n"
    "v_accvgpr_write_b32 a9, 0\n"
    "v_accvgpr_write_b32 a10, 0\n"
    "v_accvgpr_write_b32 a11, 0\n"
    "v_accvgpr_write_b32 a12, 0\n"
    "v_accvgpr_write_b32 a13, 0\n"
    "v_accvgpr_write_b32 a14, 0\n"
    "v_accvgpr_write_b32 a15, 0\n"
    "v_accvgpr_write_b32 a16, 0\n"
    "v_accvgpr_write_b32 a17, 0\n"
    "v_accvgpr_write_b32 a18, 0\n"
    "v_accvgpr_write_b32 a19, 0\n"
    "v_accvgpr_write_b32 a20, 0\n"
    "v_accvgpr_write_b32 a21, 0\n"
    "v_accvgpr_write_b32 a22, 0\n"
    "v_accvgpr_write_b32 a23, 0\n"
    "v_accvgpr_write_b32 a24, 0\n"
    "v_accvgpr_write_b32 a25, 0\n"
    "v_accvgpr_write_b32 a26, 0\n"
    "v_accvgpr_write_b32 a27, 0\n"
    "v_accvgpr_write_b32 a28, 0\n"
    "v_accvgpr_write_b32 a29, 0\n"
    "v_accvgpr_write_b32 a30, 0\n"
    "v_accvgpr_write_b32 a31, 0\n"
    "v_accvgpr_write_b32 a32, 0\n"
    "v_accvgpr_write_b32 a33, 0\n"
    "v_accvgpr_write_b32 a34, 0\n"
    "v_accvgpr_write_b32 a35, 0\n"
    "v_accvgpr_write_b32 a36, 0\n"
    "v_accvgpr_write_b32 a37, 0\n"
    "v_accvgpr_write_b32 a38, 0\n"
    "v_accvgpr_write_b32 a39, 0\n"
    "v_accvgpr_write_b32 a40, 0\n"
    "v_accvgpr_write_b32 a41, 0\n"
    "v_accvgpr_write_b32 a42, 0\n"
    "v_accvgpr_write_b32 a43, 0\n"
    "v_accvgpr_write_b32 a44, 0\n"
    "v_accvgpr_write_b32 a45, 0\n"
    "v_accvgpr_write_b32 a46, 0\n"
    "v_accvgpr_write_b32 a47, 0\n"
    "v_accvgpr_write_b32 a48, 0\n"
    "v_accvgpr_write_b32 a49, 0\n"
    "v_accvgpr_write_b32 a50, 0\n"
    "v_accvgpr_write_b32 a51, 0\n"
    "v_accvgpr_write_b32 a52, 0\n"
    "v_accvgpr_write_b32 a53, 0\n"
    "v_accvgpr_write_b32 a54, 0\n"
    "v_accvgpr_write_b32 a55, 0\n"
    "v_accvgpr_write_b32 a56, 0\n"
    "v_accvgpr_write_b32 a57, 0\n"
    "v_accvgpr_write_b32 a58, 0\n"
    "v_accvgpr_write_b32 a59, 0\n"
    "s_waitcnt lgkmcnt(0)\n"
    "v_min3_i32 v4, v8, v9, v10\n"
    "v_min_i32 v4, v4, v11\n"
    "v_max_i32 v4, 0, v4\n"
    "v_and_b32 v4, 0xffff0000, v4\n"
    "v_mul_f32 v4, s35, v4\n"
    "v_mul_f32 v4, 0x3ed53b95, v4\n"
    "v_lshrrev_b32 v4, 23, v4\n"
    "v_sub_u32 v4, 2, v4\n"
    "v_med3_i32 v4, v4, v14, v12\n"
    "v_ldexp_f32 v13, v13, v4\n"
    "v_readfirstlane_b32 s89, v4\n"
    "v_readfirstlane_b32 s88, v13\n"
    "s_mul_i32 s89, s89, 0xff7fff80\n"
    "s_mov_b32 s90, 149\n"
    "s_waitcnt vmcnt(11)\n"
    "s_add_u32 s54, s71, s60\n"
    "s_add_u32 s55, s71, s61\n"
    "s_add_u32 s56, s71, s62\n"
    "v_cvt_pk_u16_u32 v204, v204, v208\n"
    "v_cvt_pk_u16_u32 v205, v205, v209\n"
    "v_cvt_pk_u16_u32 v206, v206, v210\n"
    "v_cvt_pk_u16_u32 v207, v207, v211\n"
    "v_or3_b32 v243, v204, v205, v243\n"
    "v_or3_b32 v243, v206, v207, v243\n"
    "buffer_store_dword v204, v244, s[16:19], s54 offen offset:0 sc1\n"
    "buffer_store_dword v205, v244, s[16:19], s54 offen offset:1024 sc1\n"
    "buffer_load_dwordx4 v[4:7], v162, s[28:31], s57 offen offset:0\n"
    "buffer_load_dwordx4 v[8:11], v162, s[28:31], s57 offen offset:3072\n"
    "buffer_store_dword v206, v244, s[16:19], s54 offen offset:2048 sc1\n"
    "buffer_store_dword v207, v244, s[16:19], s54 offen offset:3072 sc1\n"
    "v_cvt_pk_u16_u32 v212, v212, v216\n"
    "v_cvt_pk_u16_u32 v213, v213, v217\n"
    "v_cvt_pk_u16_u32 v214, v214, v218\n"
    "v_cvt_pk_u16_u32 v215, v215, v219\n"
    "v_or3_b32 v243, v212, v213, v243\n"
    "v_or3_b32 v243, v214, v215, v243\n"
    "buffer_store_dword v212, v244, s[16:19], s55 offen offset:0 sc1\n"
    "buffer_store_dword v213, v244, s[16:19], s55 offen offset:1024 sc1\n"
    "buffer_store_dword v214, v244, s[16:19], s55 offen offset:2048 sc1\n"
    "buffer_load_dwordx4 v[12:15], v162, s[28:31], s57 offen offset:128\n"
    "buffer_store_dword v215, v244, s[16:19], s55 offen offset:3072 sc1\n"
    "v_cvt_pk_u16_u32 v220, v220, v224\n"
    "v_cvt_pk_u16_u32 v221, v221, v225\n"
    "v_cvt_pk_u16_u32 v222, v222, v226\n"
    "v_cvt_pk_u16_u32 v223, v223, v227\n"
    "v_or3_b32 v243, v220, v221, v243\n"
    "v_or3_b32 v243, v222, v223, v243\n"
    "buffer_store_dword v220, v244, s[16:19], s56 offen offset:0 sc1\n"
    "buffer_store_dword v221, v244, s[16:19], s56 offen offset:1024 sc1\n"
    "buffer_store_dword v222, v244, s[16:19], s56 offen offset:2048 sc1\n"
    "buffer_store_dword v223, v244, s[16:19], s56 offen offset:3072 sc1\n"
    "s_add_u32 s71, s71, 98304\n"
    "s_add_u32 m0, s65, 24576\n"
    "s_add_u32 s72, s85, 0\n"
    "buffer_load_dwordx4 v88, s[20:23], s72 offen offset:0 nt lds\n"
    "buffer_load_dwordx4 v[16:19], v162, s[28:31], s57 offen offset:3200\n"
    "buffer_load_dwordx4 v[20:23], v162, s[28:31], s57 offen offset:256\n"
    "s_waitcnt vmcnt(25)\n"
    "v_add_co_u32 v238, vcc, 0x7f4a7c15, v238\n"
    "v_mov_b32 v159, 0x9e3779b9\n"
    "s_nop 0\n"
    "v_addc_co_u32 v239, vcc, v159, v239, vcc\n"
    "v_mov_b32 v246, 1\n"
    "s_waitcnt vmcnt(26)\n"
    "v_lshlrev_b32 v180, 16, v124\n"
    "v_and_b32 v181, 0xffff0000, v124\n"
    "v_lshlrev_b32 v182, 16, v125\n"
    "v_and_b32 v183, 0xffff0000, v125\n"
    "v_lshlrev_b32 v184, 16, v126\n"
    "v_and_b32 v185, 0xffff0000, v126\n"
    "v_lshlrev_b32 v186, 16, v127\n"
    "v_and_b32 v187, 0xffff0000, v127\n"
    "v_mul_f32 v180, s88, v180\n"
    "v_mul_f32 v181, s88, v181\n"
    "v_mul_f32 v182, s88, v182\n"
    "v_mul_f32 v183, s88, v183\n"
    "v_fmaak_f32 v184, v184, v180, 0x80730000\n"
    "v_fmaak_f32 v185, v185, v181, 0x80730000\n"
    "v_fmaak_f32 v186, v186, v182, 0x80730000\n"
    "v_fmaak_f32 v187, v187, v183, 0x80730000\n"
    "s_add_u32 m0, s65, 25600\n"
    "s_add_u32 s72, s85, 1024\n"
    "buffer_load_dwordx4 v89, s[20:23], s72 offen offset:0 nt lds\n"
    "buffer_load_dwordx4 v[24:27], v162, s[28:31], s57 offen offset:3328\n"
    "v_ldexp_f32 v180, v180, s90\n"
    "v_ldexp_f32 v181, v181, s90\n"
    "v_ldexp_f32 v182, v182, s90\n"
    "v_ldexp_f32 v183, v183, s90\n"
    "v_fma_f32 v180, v180, 0, v180\n"
    "v_fma_f32 v181, v181, 0, v181\n"
    "v_fma_f32 v182, v182, 0, v182\n"
    "v_fma_f32 v183, v183, 0, v183\n"
    "ds_write_b128 v131, v[180:183] offset:0\n"
    "ds_write_b128 v131, v[184:187] offset:1024\n"
    "ds_read_b128 v[180:183], v132 offset:0\n"
    "ds_read_b128 v[184:187], v132 offset:1024\n"
    "ds_read_b128 v[188:191], v132 offset:128\n"
    "ds_read_b128 v[192:195], v132 offset:1152\n"
    "ds_read_b128 v[196:199], v132 offset:256\n"
    "ds_read_b128 v[200:203], v132 offset:1280\n"
    "v_accvgpr_write_b32 a60, 0\n"
    "v_accvgpr_write_b32 a61, 0\n"
    "v_accvgpr_write_b32 a62, 0\n"
    "v_accvgpr_write_b32 a63, 0\n"
    "v_accvgpr_write_b32 a64, 0\n"
    "v_accvgpr_write_b32 a65, 0\n"
    "v_accvgpr_write_b32 a66, 0\n"
    "s_add_u32 m0, s65, 26624\n"
    "s_add_u32 s72, s85, 2048\n"
    "buffer_load_dwordx4 v90, s[20:23], s72 offen offset:0 nt lds\n"
    "buffer_load_dwordx4 v[100:103], v162, s[28:31], s66 offen offset:0\n"
    "buffer_load_dwordx4 v[104:107], v162, s[28:31], s66 offen offset:3072\n"
    "v_accvgpr_write_b32 a67, 0\n"
    "v_accvgpr_write_b32 a68, 0\n"
    "v_accvgpr_write_b32 a69, 0\n"
    "v_accvgpr_write_b32 a70, 0\n"
    "v_accvgpr_write_b32 a71, 0\n"
    "v_accvgpr_write_b32 a72, 0\n"
    "v_accvgpr_write_b32 a73, 0\n"
    "v_accvgpr_write_b32 a74, 0\n"
    "v_accvgpr_write_b32 a75, 0\n"
    "v_accvgpr_write_b32 a76, 0\n"
    "v_accvgpr_write_b32 a77, 0\n"
    "v_accvgpr_write_b32 a78, 0\n"
    "v_accvgpr_write_b32 a79, 0\n"
    "v_accvgpr_write_b32 a80, 0\n"
    "v_accvgpr_write_b32 a81, 0\n"
    "v_accvgpr_write_b32 a82, 0\n"
    "v_accvgpr_write_b32 a83, 0\n"
    "v_accvgpr_write_b32 a84, 0\n"
    "v_accvgpr_write_b32 a85, 0\n"
    "v_accvgpr_write_b32 a86, 0\n"
    "v_accvgpr_write_b32 a87, 0\n"
    "v_accvgpr_write_b32 a88, 0\n"
    "s_add_u32 m0, s65, 27648\n"
    "s_add_u32 s72, s85, 3072\n"
    "buffer_load_dwordx4 v88, s[20:23], s72 offen offset:0 nt lds\n"
    "buffer_load_dwordx4 v[108:111], v162, s[28:31], s66 offen offset:128\n"
    "v_accvgpr_write_b32 a89, 0\n"
    "v_accvgpr_write_b32 a90, 0\n"
    "v_accvgpr_write_b32 a91, 0\n"
    "v_accvgpr_write_b32 a92, 0\n"
    "v_accvgpr_write_b32 a93, 0\n"
    "v_accvgpr_write_b32 a94, 0\n"
    "v_accvgpr_write_b32 a95, 0\n"
    "v_accvgpr_write_b32 a96, 0\n"
    "v_accvgpr_write_b32 a97, 0\n"
    "v_accvgpr_write_b32 a98, 0\n"
    "v_accvgpr_write_b32 a99, 0\n"
    "v_accvgpr_write_b32 a100, 0\n"
    "v_accvgpr_write_b32 a101, 0\n"
    "v_accvgpr_write_b32 a102, 0\n"
    "v_accvgpr_write_b32 a103, 0\n"
    "v_accvgpr_write_b32 a104, 0\n"
    "v_accvgpr_write_b32 a105, 0\n"
    "v_accvgpr_write_b32 a106, 0\n"
    "v_accvgpr_write_b32 a107, 0\n"
    "v_accvgpr_write_b32 a108, 0\n"
    "v_accvgpr_write_b32 a109, 0\n"
    "v_accvgpr_write_b32 a110, 0\n"
    "v_accvgpr_write_b32 a111, 0\n"
    "s_waitcnt lgkmcnt(0)\n"
    "s_waitcnt vmcnt(10)\n"
    "ds_add_rtn_u32 v245, v247, v246\n"
    "s_lshl_b32 s73, s53, 6\n"
    "s_add_u32 s73, s73, 128\n"
    "s_waitcnt lgkmcnt(0)\n"
    "v_readfirstlane_b32 s74, v245\n"
    "s_nop 0\n"
    "s_cmp_eq_u32 s74, 3\n"
    "s_cselect_b32 s73, s73, 0x7fff0000\n"
    "buffer_store_dwordx2 v[238:239], v91, s[36:39], s73 offen offset:0 sc1\n"
    "s_waitcnt vmcnt(28)\n"
    "s_add_u32 m0, s65, 28672\n"
    "s_add_u32 s72, s85, 4096\n"
    "buffer_load_dwordx4 v89, s[20:23], s72 offen offset:0 nt lds\n"
    "buffer_load_dwordx4 v[112:115], v162, s[28:31], s66 offen offset:3200\n"
    "buffer_load_dwordx4 v[116:119], v162, s[28:31], s66 offen offset:256\n"
    "s_add_u32 s54, s71, s60\n"
    "s_add_u32 s55, s71, s61\n"
    "s_add_u32 s56, s71, s62\n"
    "v_cvt_pk_u16_u32 v36, v36, v40\n"
    "v_cvt_pk_u16_u32 v37, v37, v41\n"
    "v_cvt_pk_u16_u32 v38, v38, v42\n"
    "v_cvt_pk_u16_u32 v39, v39, v43\n"
    "v_or3_b32 v243, v36, v37, v243\n"
    "v_or3_b32 v243, v38, v39, v243\n"
    "buffer_store_dword v36, v244, s[16:19], s54 offen offset:0 sc1\n"
    "buffer_store_dword v37, v244, s[16:19], s54 offen offset:1024 sc1\n"
    "buffer_store_dword v38, v244, s[16:19], s54 offen offset:2048 sc1\n"
    "buffer_store_dword v39, v244, s[16:19], s54 offen offset:3072 sc1\n"
    "v_cvt_pk_u16_u32 v44, v44, v48\n"
    "v_cvt_pk_u16_u32 v45, v45, v49\n"
    "v_cvt_pk_u16_u32 v46, v46, v50\n"
    "s_add_u32 m0, s65, 29696\n"
    "s_add_u32 s72, s85, 5120\n"
    "buffer_load_dwordx4 v90, s[20:23], s72 offen offset:0 nt lds\n"
    "buffer_load_dwordx4 v[120:123], v162, s[28:31], s66 offen offset:3328\n"
    "v_cvt_pk_u16_u32 v47, v47, v51\n"
    "v_or3_b32 v243, v44, v45, v243\n"
    "v_or3_b32 v243, v46, v47, v243\n"
    "buffer_store_dword v44, v244, s[16:19], s55 offen offset:0 sc1\n"
    "buffer_store_dword v45, v244, s[16:19], s55 offen offset:1024 sc1\n"
    "buffer_store_dword v46, v244, s[16:19], s55 offen offset:2048 sc1\n"
    "buffer_store_dword v47, v244, s[16:19], s55 offen offset:3072 sc1\n"
    "v_cvt_pk_u16_u32 v52, v52, v56\n"
    "v_cvt_pk_u16_u32 v53, v53, v57\n"
    "v_cvt_pk_u16_u32 v54, v54, v58\n"
    "v_cvt_pk_u16_u32 v55, v55, v59\n"
    "v_or3_b32 v243, v52, v53, v243\n"
    "v_or3_b32 v243, v54, v55, v243\n"
    "buffer_store_dword v52, v244, s[16:19], s56 offen offset:0 sc1\n"
    "buffer_store_dword v53, v244, s[16:19], s56 offen offset:1024 sc1\n"
    "buffer_store_dword v54, v244, s[16:19], s56 offen offset:2048 sc1\n"
    "buffer_store_dword v55, v244, s[16:19], s56 offen offset:3072 sc1\n"
    "s_add_u32 m0, s65, 73728\n"
    "s_add_u32 s72, s85, 6144\n"
    "buffer_load_dwordx4 v88, s[20:23], s72 offen offset:0 nt lds\n"
    "s_waitcnt vmcnt(44)\n"
    "v_lshlrev_b32 v204, 16, v136\n"
    "v_and_b32 v205, 0xffff0000, v136\n"
    "v_lshlrev_b32 v206, 16, v137\n"
    "v_and_b32 v207, 0xffff0000, v137\n"
    "v_lshlrev_b32 v208, 16, v138\n"
    "v_and_b32 v209, 0xffff0000, v138\n"
    "v_lshlrev_b32 v210, 16, v139\n"
    "v_and_b32 v211, 0xffff0000, v139\n"
    "v_log_f32 v68, v204\n"
    "v_log_f32 v69, v205\n"
    "v_log_f32 v70, v206\n"
    "v_log_f32 v71, v207\n"
    "v_log_f32 v72, v208\n"
    "v_log_f32 v73, v209\n"
    "v_log_f32 v74, v210\n"
    "v_log_f32 v75, v211\n"
    "v_rcp_f32 v204, v204\n"
    "v_rcp_f32 v205, v205\n"
    "v_rcp_f32 v206, v206\n"
    "s_add_u32 m0, s65, 74752\n"
    "s_add_u32 s72, s85, 7168\n"
    "buffer_load_dwordx4 v89, s[20:23], s72 offen offset:0 nt lds\n"
    "v_rcp_f32 v207, v207\n"
    "v_rcp_f32 v208, v208\n"
    "v_rcp_f32 v209, v209\n"
    "v_rcp_f32 v210, v210\n"
    "v_rcp_f32 v211, v211\n"
    "s_mov_b32 s73, 0x41a00000\n"
    "v_mov_b32 v228, 0x7fc00000\n"
    "v_cmp_le_f32 vcc, |v68|, s73\n"
    "s_nop 1\n"
    "v_cndmask_b32 v68, v228, v68, vcc\n"
    "v_cmp_le_f32 vcc, |v69|, s73\n"
    "s_nop 1\n"
    "v_cndmask_b32 v69, v228, v69, vcc\n"
    "v_cmp_le_f32 vcc, |v70|, s73\n"
    "s_nop 1\n"
    "v_cndmask_b32 v70, v228, v70, vcc\n"
    "v_cmp_le_f32 vcc, |v71|, s73\n"
    "s_nop 1\n"
    "v_cndmask_b32 v71, v228, v71, vcc\n"
    "v_cmp_le_f32 vcc, |v72|, s73\n"
    "s_nop 1\n"
    "v_cndmask_b32 v72, v228, v72, vcc\n"
    "v_cmp_le_f32 vcc, |v73|, s73\n"
    "s_nop 1\n"
    "v_cndmask_b32 v73, v228, v73, vcc\n"
    "v_cmp_le_f32 vcc, |v74|, s73\n"
    "s_nop 1\n"
    "v_cndmask_b32 v74, v228, v74, vcc\n"
    "s_add_u32 m0, s65, 75776\n"
    "s_add_u32 s72, s85, 8192\n"
    "buffer_load_dwordx4 v90, s[20:23], s72 offen offset:0 nt lds\n"
    "v_cmp_le_f32 vcc, |v75|, s73\n"
    "s_nop 1\n"
    "v_cndmask_b32 v75, v228, v75, vcc\n"
    "v_cvt_pk_bf16_f32 v220, v204, v205\n"
    "v_cvt_pk_bf16_f32 v221, v206, v207\n"
    "v_cvt_pk_bf16_f32 v222, v208, v209\n"
    "v_cvt_pk_bf16_f32 v223, v210, v211\n"
    "v_lshlrev_b32 v212, 16, v220\n"
    "v_and_b32 v213, 0xffff0000, v220\n"
    "v_lshlrev_b32 v214, 16, v221\n"
    "v_and_b32 v215, 0xffff0000, v221\n"
    "v_lshlrev_b32 v216, 16, v222\n"
    "v_and_b32 v217, 0xffff0000, v222\n"
    "v_lshlrev_b32 v218, 16, v223\n"
    "v_and_b32 v219, 0xffff0000, v223\n"
    "v_sub_f32 v204, v204, v212\n"
    "v_sub_f32 v205, v205, v213\n"
    "v_sub_f32 v206, v206, v214\n"
    "v_sub_f32 v207, v207, v215\n"
    "v_sub_f32 v208, v208, v216\n"
    "v_sub_f32 v209, v209, v217\n"
    "s_add_u32 m0, s65, 76800\n"
    "s_add_u32 s72, s85, 9216\n"
    "buffer_load_dwordx4 v88, s[20:23], s72 offen offset:0 nt lds\n"
    "v_sub_f32 v210, v210, v218\n"
    "v_sub_f32 v211, v211, v219\n"
    "v_cvt_pk_bf16_f32 v224, v204, v205\n"
    "v_cvt_pk_bf16_f32 v225, v206, v207\n"
    "v_cvt_pk_bf16_f32 v226, v208, v209\n"
    "v_cvt_pk_bf16_f32 v227, v210, v211\n"
    "v_mov_b32 v204, 0\n"
    "v_mov_b32 v205, 0\n"
    "v_mov_b32 v206, 0\n"
    "v_mov_b32 v207, 0\n"
    "v_swap_b32 v221, v222\n"
    "v_swap_b32 v225, v226\n"
    "v_swap_b32 v141, v142\n"
    "ds_write2_b64 v144, v[220:221], v[222:223] offset0:32 offset1:40\n"
    "ds_write2_b64 v144, v[224:225], v[226:227] offset0:48 offset1:56\n"
    "ds_write2_b64 v144, v[140:141], v[142:143] offset0:64 offset1:72\n"
    "ds_write2_b64 v144, v[204:205], v[206:207] offset0:80 offset1:88\n"
    "s_waitcnt vmcnt(27)\n"
    "v_fma_f32 v4, v4, v180, -v184\n"
    "v_fma_f32 v5, v5, v181, -v185\n"
    "v_fma_f32 v6, v6, v182, -v186\n"
    "s_add_u32 m0, s65, 77824\n"
    "s_add_u32 s72, s85, 10240\n"
    "buffer_load_dwordx4 v89, s[20:23], s72 offen offset:0 nt lds\n"
    "v_fma_f32 v7, v7, v183, -v187\n"
    "v_fma_f32 v8, v8, v180, -v184\n"
    "v_fma_f32 v9, v9, v181, -v185\n"
    "v_fma_f32 v10, v10, v182, -v186\n"
    "v_fma_f32 v11, v11, v183, -v187\n"
    "v_cvt_pk_bf16_f32 v4, v4, v5\n"
    "v_cvt_pk_bf16_f32 v5, v6, v7\n"
    "v_cvt_pk_bf16_f32 v6, v8, v9\n"
    "v_cvt_pk_bf16_f32 v7, v10, v11\n"
    "ds_write_b64 v163, v[4:5] offset:0\n"
    "ds_write_b64 v163, v[6:7] offset:1024\n"
    "v_fma_f32 v12, v12, v188, -v192\n"
    "v_fma_f32 v13, v13, v189, -v193\n"
    "v_fma_f32 v14, v14, v190, -v194\n"
    "v_fma_f32 v15, v15, v191, -v195\n"
    "v_fma_f32 v16, v16, v188, -v192\n"
    "v_fma_f32 v17, v17, v189, -v193\n"
    "v_fma_f32 v18, v18, v190, -v194\n"
    "v_fma_f32 v19, v19, v191, -v195\n"
    "v_cvt_pk_bf16_f32 v12, v12, v13\n"
    "s_add_u32 m0, s65, 78848\n"
    "s_add_u32 s72, s85, 11264\n"
    "buffer_load_dwordx4 v90, s[20:23], s72 offen offset:0 nt lds\n"
    "v_cvt_pk_bf16_f32 v13, v14, v15\n"
    "v_cvt_pk_bf16_f32 v14, v16, v17\n"
    "v_cvt_pk_bf16_f32 v15, v18, v19\n"
    "ds_write_b64 v163, v[12:13] offset:2048\n"
    "ds_write_b64 v163, v[14:15] offset:3072\n"
    "v_fma_f32 v20, v20, v196, -v200\n"
    "v_fma_f32 v21, v21, v197, -v201\n"
    "v_fma_f32 v22, v22, v198, -v202\n"
    "v_fma_f32 v23, v23, v199, -v203\n"
    "v_fma_f32 v24, v24, v196, -v200\n"
    "v_fma_f32 v25, v25, v197, -v201\n"
    "v_fma_f32 v26, v26, v198, -v202\n"
    "v_fma_f32 v27, v27, v199, -v203\n"
    "v_cvt_pk_bf16_f32 v20, v20, v21\n"
    "v_cvt_pk_bf16_f32 v21, v22, v23\n"
    "v_cvt_pk_bf16_f32 v22, v24, v25\n"
    "v_cvt_pk_bf16_f32 v23, v26, v27\n"
    "ds_write_b64 v163, v[20:21] offset:4096\n"
    "ds_write_b64 v163, v[22:23] offset:5120\n"
    "s_waitcnt vmcnt(14)\n"
    "v_fma_f32 v100, v100, v180, -v184\n"
    "s_add_u32 m0, s65, 49152\n"
    "s_add_u32 s72, s85, 12288\n"
    "buffer_load_dwordx4 v88, s[20:23], s72 offen offset:0 nt lds\n"
    "v_fma_f32 v101, v101, v181, -v185\n"
    "v_fma_f32 v102, v102, v182, -v186\n"
    "v_fma_f32 v103, v103, v183, -v187\n"
    "v_fma_f32 v104, v104, v180, -v184\n"
    "v_fma_f32 v105, v105, v181, -v185\n"
    "v_fma_f32 v106, v106, v182, -v186\n"
    "v_fma_f32 v107, v107, v183, -v187\n"
    "v_cvt_pk_bf16_f32 v100, v100, v101\n"
    "v_cvt_pk_bf16_f32 v101, v102, v103\n"
    "v_cvt_pk_bf16_f32 v102, v104, v105\n"
    "v_cvt_pk_bf16_f32 v103, v106, v107\n"
    "ds_write_b64 v163, v[100:101] offset:12288\n"
    "ds_write_b64 v163, v[102:103] offset:13312\n"
    "v_fma_f32 v108, v108, v188, -v192\n"
    "v_fma_f32 v109, v109, v189, -v193\n"
    "v_fma_f32 v110, v110, v190, -v194\n"
    "v_fma_f32 v111, v111, v191, -v195\n"
    "v_fma_f32 v112, v112, v188, -v192\n"
    "v_fma_f32 v113, v113, v189, -v193\n"
    "v_fma_f32 v114, v114, v190, -v194\n"
    "s_add_u32 m0, s65, 50176\n"
    "s_add_u32 s72, s85, 13312\n"
    "buffer_load_dwordx4 v89, s[20:23], s72 offen offset:0 nt lds\n"
    "v_fma_f32 v115, v115, v191, -v195\n"
    "v_cvt_pk_bf16_f32 v108, v108, v109\n"
    "v_cvt_pk_bf16_f32 v109, v110, v111\n"
    "v_cvt_pk_bf16_f32 v110, v112, v113\n"
    "v_cvt_pk_bf16_f32 v111, v114, v115\n"
    "ds_write_b64 v163, v[108:109] offset:14336\n"
    "ds_write_b64 v163, v[110:111] offset:15360\n"
    "v_fma_f32 v116, v116, v196, -v200\n"
    "v_fma_f32 v117, v117, v197, -v201\n"
    "v_fma_f32 v118, v118, v198, -v202\n"
    "v_fma_f32 v119, v119, v199, -v203\n"
    "v_fma_f32 v120, v120, v196, -v200\n"
    "v_fma_f32 v121, v121, v197, -v201\n"
    "v_fma_f32 v122, v122, v198, -v202\n"
    "v_fma_f32 v123, v123, v199, -v203\n"
    "v_cvt_pk_bf16_f32 v116, v116, v117\n"
    "v_cvt_pk_bf16_f32 v117, v118, v119\n"
    "v_cvt_pk_bf16_f32 v118, v120, v121\n"
    "v_cvt_pk_bf16_f32 v119, v122, v123\n"
    "ds_write_b64 v163, v[116:117] offset:16384\n"
    "ds_write_b64 v163, v[118:119] offset:17408\n"
    "s_waitcnt vmcnt(17)\n"
    "ds_read_b128 v[36:39], v154 offset:0\n"
    "ds_read_b128 v[40:43], v155 offset:0\n"
    "ds_read_b128 v[44:47], v154 offset:128\n"
    "ds_read_b128 v[48:51], v155 offset:128\n"
    "ds_read_b128 v[52:55], v154 offset:256\n"
    "ds_read_b128 v[56:59], v155 offset:256\n"
    "s_mov_b64 exec, 0\n"
    "s_bitcmp1_b32 s83, 1\n"
    "s_cbranch_scc1 .LwV\n"
    "s_cmp_eq_u32 0, 0\n"
    "s_branch .LwK\n"
    ".Lw2:\n"
    "s_bitcmp1_b32 s83, 1\n"
    "s_cbranch_scc1 .Lwp1\n"
    "s_branch .Lwp0\n"
    ".Lw3:\n"
    "s_mov_b64 exec, -1\n"
    "s_waitcnt lgkmcnt(0)\n"
    "s_barrier\n"
    "ds_read_b128 a[112:115], v2 offset:0\n"
    "ds_read_b128 a[116:119], v2 offset:2048\n"
    "ds_read_b128 a[120:123], v2 offset:4096\n"
    "ds_read_b128 a[124:127], v2 offset:6144\n"
    "ds_read_b128 a[128:131], v2 offset:8192\n"
    "ds_read_b128 a[132:135], v2 offset:10240\n"
    "ds_read_b128 a[136:139], v2 offset:1024\n"
    "ds_read_b128 a[140:143], v2 offset:3072\n"
    "ds_read_b128 a[144:147], v2 offset:5120\n"
    "ds_read_b128 a[148:151], v2 offset:7168\n"
    "ds_read_b128 a[152:155], v2 offset:9216\n"
    "ds_read_b128 a[156:159], v2 offset:11264\n"
    "s_add_u32 m0, s65, 51200\n"
    "s_add_u32 s72, s85, 14336\n"
    "buffer_load_dwordx4 v90, s[20:23], s72 offen offset:0 nt lds\n"
    "s_add_u32 m0, s65, 52224\n"
    "s_add_u32 s72, s85, 15360\n"
    "buffer_load_dwordx4 v88, s[20:23], s72 offen offset:0 nt lds\n"
    "v_pk_maximum3_f16 v80, v36, v37, v80\n"
    "v_pk_maximum3_f16 v80, v38, v39, v80\n"
    "v_pk_add_u16 v36, s89, v36 clamp\n"
    "v_pk_add_u16 v37, s89, v37 clamp\n"
    "v_pk_add_u16 v38, s89, v38 clamp\n"
    "v_pk_add_u16 v39, s89, v39 clamp\n"
    "v_pk_maximum3_f16 v80, v40, v41, v80\n"
    "v_pk_maximum3_f16 v80, v42, v43, v80\n"
    "v_pk_add_u16 v40, s89, v40 clamp\n"
    "v_pk_add_u16 v41, s89, v41 clamp\n"
    "v_pk_add_u16 v42, s89, v42 clamp\n"
    "v_pk_add_u16 v43, s89, v43 clamp\n"
    "v_pk_maximum3_f16 v80, v44, v45, v80\n"
    "v_pk_maximum3_f16 v80, v46, v47, v80\n"
    "v_pk_add_u16 v44, s89, v44 clamp\n"
    "v_pk_add_u16 v45, s89, v45 clamp\n"
    "v_pk_add_u16 v46, s89, v46 clamp\n"
    "v_pk_add_u16 v47, s89, v47 clamp\n"
    "v_pk_maximum3_f16 v80, v48, v49, v80\n"
    "v_pk_maximum3_f16 v80, v50, v51, v80\n"
    "v_pk_add_u16 v48, s89, v48 clamp\n"
    "v_pk_add_u16 v49, s89, v49 clamp\n"
    "v_pk_add_u16 v50, s89, v50 clamp\n"
    "v_pk_add_u16 v51, s89, v51 clamp\n"
    "v_pk_maximum3_f16 v80, v52, v53, v80\n"
    "v_pk_maximum3_f16 v80, v54, v55, v80\n"
    "v_pk_add_u16 v52, s89, v52 clamp\n"
    "v_pk_add_u16 v53, s89, v53 clamp\n"
    "v_pk_add_u16 v54, s89, v54 clamp\n"
    "v_pk_add_u16 v55, s89, v55 clamp\n"
    "v_pk_maximum3_f16 v80, v56, v57, v80\n"
    "v_pk_maximum3_f16 v80, v58, v59, v80\n"
    "v_pk_add_u16 v56, s89, v56 clamp\n"
    "v_pk_add_u16 v57, s89, v57 clamp\n"
    "v_pk_add_u16 v58, s89, v58 clamp\n"
    "v_pk_add_u16 v59, s89, v59 clamp\n"
    "v_mov_b32 v108, 0\n"
    "v_mov_b32 v109, 0\n"
    "v_mov_b32 v110, 0\n"
    "v_mov_b32 v111, 0\n"
    "v_mov_b32 v112, 0\n"
    "v_mov_b32 v113, 0\n"
    "v_mov_b32 v114, 0\n"
    "v_mov_b32 v115, 0\n"
    "v_mov_b32 v116, 0\n"
    "v_mov_b32 v117, 0\n"
    "v_mov_b32 v118, 0\n"
    "v_mov_b32 v119, 0\n"
    "v_mov_b32 v120, 0\n"
    "v_mov_b32 v121, 0\n"
    "v_mov_b32 v122, 0\n"
    "v_mov_b32 v123, 0\n"
    "s_waitcnt lgkmcnt(11)\n"
    "v_mfma_f32_16x16x32_bf16 v[4:7], a[112:115], v[36:39], 0\n"
    "s_waitcnt lgkmcnt(10)\n"
    "v_mfma_f32_16x16x32_bf16 v[4:7], a[116:119], v[40:43], v[4:7]\n"
    "s_waitcnt lgkmcnt(9)\n"
    "v_mfma_f32_16x16x32_bf16 v[4:7], a[120:123], v[44:47], v[4:7]\n"
    "s_waitcnt lgkmcnt(8)\n"
    "v_mfma_f32_16x16x32_bf16 v[4:7], a[124:127], v[48:51], v[4:7]\n"
    "s_waitcnt lgkmcnt(7)\n"
    "v_mfma_f32_16x16x32_bf16 v[4:7], a[128:131], v[52:55], v[4:7]\n"
    "s_waitcnt lgkmcnt(6)\n"
    "v_mfma_f32_16x16x32_bf16 v[4:7], a[132:135], v[56:59], v[4:7]\n"
    "s_waitcnt lgkmcnt(5)\n"
    "v_mfma_f32_16x16x32_bf16 v[20:23], a[136:139], v[36:39], 0\n"
    "v_accvgpr_write_b32 a192, v36\n"
    "v_accvgpr_write_b32 a193, v37\n"
    "v_accvgpr_write_b32 a194, v38\n"
    "v_accvgpr_write_b32 a195, v39\n"
    "s_waitcnt lgkmcnt(4)\n"
    "v_mfma_f32_16x16x32_bf16 v[20:23], a[140:143], v[40:43], v[20:23]\n"
    "v_accvgpr_write_b32 a196, v40\n"
    "v_accvgpr_write_b32 a197, v41\n"
    "v_accvgpr_write_b32 a198, v42\n"
    "v_accvgpr_write_b32 a199, v43\n"
    "s_waitcnt lgkmcnt(3)\n"
    "v_mfma_f32_16x16x32_bf16 v[20:23], a[144:147], v[44:47], v[20:23]\n"
    "v_accvgpr_write_b32 a200, v44\n"
    "v_accvgpr_write_b32 a201, v45\n"
    "v_accvgpr_write_b32 a202, v46\n"
    "v_accvgpr_write_b32 a203, v47\n"
    "s_waitcnt lgkmcnt(2)\n"
    "v_mfma_f32_16x16x32_bf16 v[20:23], a[148:151], v[48:51], v[20:23]\n"
    "v_accvgpr_write_b32 a204, v48\n"
    "v_accvgpr_write_b32 a205, v49\n"
    "v_accvgpr_write_b32 a206, v50\n"
    "v_accvgpr_write_b32 a207, v51\n"
    "s_waitcnt lgkmcnt(1)\n"
    "v_mfma_f32_16x16x32_bf16 v[20:23], a[152:155], v[52:55], v[20:23]\n"
    "v_accvgpr_write_b32 a208, v52\n"
    "v_accvgpr_write_b32 a209, v53\n"
    "v_accvgpr_write_b32 a210, v54\n"
    "v_accvgpr_write_b32 a211, v55\n"
    "s_waitcnt lgkmcnt(0)\n"
    "v_mfma_f32_16x16x32_bf16 v[20:23], a[156:159], v[56:59], v[20:23]\n"
    "v_accvgpr_write_b32 a212, v56\n"
    "v_accvgpr_write_b32 a213, v57\n"
    "v_accvgpr_write_b32 a214, v58\n"
    "v_accvgpr_write_b32 a215, v59\n"
    "s_add_u32 m0, s65, 53248\n"
    "s_add_u32 s72, s85, 16384\n"
    "buffer_load_dwordx4 v89, s[20:23], s72 offen offset:0 nt lds\n"
    "buffer_load_dwordx2 v[236:237], v87, s[36:39], 0 offen offset:0 sc1\n"
    "v_mov_b32 v77, 155904\n"
    "v_max3_f32 v76, v4, v5, v6\n"
    "v_max3_f32 v76, v76, v7, v20\n"
    "v_max3_f32 v76, v76, v21, v22\n"
    "v_max_f32 v76, v76, v23\n"
    "v_mov_b32 v78, v76\n"
    "s_nop 1\n"
    "v_permlane32_swap_b32 v76, v78\n"
    "v_max_f32 v76, v76, v78\n"
    "v_mov_b32 v78, v76\n"
    "s_nop 1\n"
    "v_permlane16_swap_b32 v76, v78\n"
    "v_max_f32 v76, v76, v78\n"
    "ds_max_f32 v77, v76\n"
    "s_add_u32 m0, s65, 54272\n"
    "s_add_u32 s72, s85, 17408\n"
    "buffer_load_dwordx4 v90, s[20:23], s72 offen offset:0 nt lds\n"
    "s_add_u32 m0, s65, 98304\n"
    "s_add_u32 s72, s85, 18432\n"
    "buffer_load_dwordx4 v88, s[20:23], s72 offen offset:0 nt lds\n"
    "s_waitcnt lgkmcnt(0)\n"
    "s_barrier\n"
    "ds_read_b32 v76, v77\n"
    "s_waitcnt vmcnt(14)\n"
    "buffer_store_dwordx2 v[238:239], v240, s[36:39], s80 offen offset:0 sc1\n"
    "s_lshl_b32 s73, s79, 8\n"
    "s_add_u32 s80, s80, s73\n"
    "s_mul_i32 s73, s79, 98304\n"
    "s_add_u32 s71, s71, s73\n"
    "s_waitcnt lgkmcnt(0)\n"
    "v_add_f32 v76, 0xc2b00000, v76\n"
    "v_sub_f32 v68, v68, v76\n"
    "v_sub_f32 v69, v69, v76\n"
    "v_sub_f32 v70, v70, v76\n"
    "v_sub_f32 v71, v71, v76\n"
    "v_sub_f32 v72, v72, v76\n"
    "v_sub_f32 v73, v73, v76\n"
    "v_sub_f32 v74, v74, v76\n"
    "v_sub_f32 v75, v75, v76\n"
    "ds_write_b128 v86, v[68:71] offset:0\n"
    "ds_write_b128 v86, v[72:75] offset:16\n"
    "s_mul_i32 s73, s79, 768\n"
    "v_add_u32 v77, s73, v250\n"
    "s_add_u32 m0, s65, 99328\n"
    "s_add_u32 s72, s85, 19456\n"
    "buffer_load_dwordx4 v89, s[20:23], s72 offen offset:0 nt lds\n"
    "s_waitcnt lgkmcnt(0)\n"
    "s_barrier\n"
    "ds_read_b128 v[100:103], v77 offset:0\n"
    "ds_read_b128 v[104:107], v77 offset:16\n"
    "s_add_u32 m0, s65, 100352\n"
    "s_add_u32 s72, s85, 20480\n"
    "buffer_load_dwordx4 v90, s[20:23], s72 offen offset:0 nt lds\n"
    "s_waitcnt lgkmcnt(1)\n"
    "v_add_f32 v4, v4, v100\n"
    "v_add_f32 v5, v5, v101\n"
    "v_add_f32 v6, v6, v102\n"
    "v_add_f32 v7, v7, v103\n"
    "s_waitcnt lgkmcnt(0)\n"
    "v_add_f32 v20, v20, v104\n"
    "v_add_f32 v21, v21, v105\n"
    "v_add_f32 v22, v22, v106\n"
    "v_add_f32 v23, v23, v107\n"
    "v_exp_f32 v4, v4\n"
    "v_exp_f32 v5, v5\n"
    "v_exp_f32 v6, v6\n"
    "v_exp_f32 v7, v7\n"
    "v_exp_f32 v20, v20\n"
    "v_exp_f32 v21, v21\n"
    "v_exp_f32 v22, v22\n"
    "v_exp_f32 v23, v23\n"
    "s_add_u32 m0, s65, 101376\n"
    "s_add_u32 s72, s85, 21504\n"
    "buffer_load_dwordx4 v88, s[20:23], s72 offen offset:0 nt lds\n"
    "s_add_u32 m0, s65, 102400\n"
    "s_add_u32 s72, s85, 22528\n"
    "buffer_load_dwordx4 v89, s[20:23], s72 offen offset:0 nt lds\n"
    "s_waitcnt vmcnt(13)\n"
    "ds_read_b128 v[56:59], v85 offset:24832\n"
    "ds_read_b128 v[52:55], v84 offset:24832\n"
    "ds_read_b128 v[48:51], v85 offset:24704\n"
    "ds_read_b128 v[44:47], v84 offset:24704\n"
    "ds_read_b128 v[40:43], v85 offset:24576\n"
    "ds_read_b128 v[36:39], v84 offset:24576\n"
    "s_waitcnt lgkmcnt(5)\n"
    "v_pk_maximum3_f16 v80, v56, v57, v80\n"
    "v_pk_maximum3_f16 v80, v58, v59, v80\n"
    "v_pk_add_u16 v56, s89, v56 clamp\n"
    "v_pk_add_u16 v57, s89, v57 clamp\n"
    "v_pk_add_u16 v58, s89, v58 clamp\n"
    "v_pk_add_u16 v59, s89, v59 clamp\n"
    "s_waitcnt lgkmcnt(4)\n"
    "v_pk_maximum3_f16 v80, v52, v53, v80\n"
    "v_pk_maximum3_f16 v80, v54, v55, v80\n"
    "v_pk_add_u16 v52, s89, v52 clamp\n"
    "v_pk_add_u16 v53, s89, v53 clamp\n"
    "v_pk_add_u16 v54, s89, v54 clamp\n"
    "v_pk_add_u16 v55, s89, v55 clamp\n"
    "v_mfma_f32_16x16x32_bf16 v[8:11], a[132:135], v[56:59], v[100:103]\n"
    "s_waitcnt lgkmcnt(3)\n"
    "v_pk_maximum3_f16 v80, v48, v49, v80\n"
    "v_pk_maximum3_f16 v80, v50, v51, v80\n"
    "v_pk_add_u16 v48, s89, v48 clamp\n"
    "v_pk_add_u16 v49, s89, v49 clamp\n"
    "v_pk_add_u16 v50, s89, v50 clamp\n"
    "v_pk_add_u16 v51, s89, v51 clamp\n"
    "v_mfma_f32_16x16x32_bf16 v[8:11], a[128:131], v[52:55], v[8:11]\n"
    "s_waitcnt lgkmcnt(2)\n"
    "v_pk_maximum3_f16 v80, v44, v45, v80\n"
    "v_pk_maximum3_f16 v80, v46, v47, v80\n"
    "v_pk_add_u16 v44, s89, v44 clamp\n"
    "v_pk_add_u16 v45, s89, v45 clamp\n"
    "v_pk_add_u16 v46, s89, v46 clamp\n"
    "v_pk_add_u16 v47, s89, v47 clamp\n"
    "v_mfma_f32_16x16x32_bf16 v[8:11], a[124:127], v[48:51], v[8:11]\n"
    "s_waitcnt lgkmcnt(1)\n"
    "v_pk_maximum3_f16 v80, v40, v41, v80\n"
    "v_pk_maximum3_f16 v80, v42, v43, v80\n"
    "v_pk_add_u16 v40, s89, v40 clamp\n"
    "v_pk_add_u16 v41, s89, v41 clamp\n"
    "v_pk_add_u16 v42, s89, v42 clamp\n"
    "v_pk_add_u16 v43, s89, v43 clamp\n"
    "v_mfma_f32_16x16x32_bf16 v[8:11], a[120:123], v[44:47], v[8:11]\n"
    "s_waitcnt lgkmcnt(0)\n"
    "v_pk_maximum3_f16 v80, v36, v37, v80\n"
    "v_pk_maximum3_f16 v80, v38, v39, v80\n"
    "v_pk_add_u16 v36, s89, v36 clamp\n"
    "v_pk_add_u16 v37, s89, v37 clamp\n"
    "v_pk_add_u16 v38, s89, v38 clamp\n"
    "v_pk_add_u16 v39, s89, v39 clamp\n"
    "v_mfma_f32_16x16x32_bf16 v[8:11], a[116:119], v[40:43], v[8:11]\n"
    "s_nop 0\n"
    "v_mfma_f32_16x16x32_bf16 v[8:11], a[112:115], v[36:39], v[8:11]\n"
    "v_mfma_f32_16x16x32_bf16 v[24:27], a[156:159], v[56:59], v[104:107]\n"
    "v_accvgpr_write_b32 a236, v56\n"
    "v_accvgpr_write_b32 a237, v57\n"
    "v_accvgpr_write_b32 a238, v58\n"
    "v_accvgpr_write_b32 a239, v59\n"
    "v_mfma_f32_16x16x32_bf16 v[24:27], a[152:155], v[52:55], v[24:27]\n"
    "v_accvgpr_write_b32 a232, v52\n"
    "v_accvgpr_write_b32 a233, v53\n"
    "v_accvgpr_write_b32 a234, v54\n"
    "v_accvgpr_write_b32 a235, v55\n"
    "v_mfma_f32_16x16x32_bf16 v[24:27], a[148:151], v[48:51], v[24:27]\n"
    "v_accvgpr_write_b32 a228, v48\n"
    "v_accvgpr_write_b32 a229, v49\n"
    "v_accvgpr_write_b32 a230, v50\n"
    "v_accvgpr_write_b32 a231, v51\n"
    "v_mfma_f32_16x16x32_bf16 v[24:27], a[144:147], v[44:47], v[24:27]\n"
    "v_accvgpr_write_b32 a224, v44\n"
    "v_accvgpr_write_b32 a225, v45\n"
    "v_accvgpr_write_b32 a226, v46\n"
    "v_accvgpr_write_b32 a227, v47\n"
    "v_mfma_f32_16x16x32_bf16 v[24:27], a[140:143], v[40:43], v[24:27]\n"
    "v_accvgpr_write_b32 a220, v40\n"
    "v_accvgpr_write_b32 a221, v41\n"
    "v_accvgpr_write_b32 a222, v42\n"
    "v_accvgpr_write_b32 a223, v43\n"
    "v_mfma_f32_16x16x32_bf16 v[24:27], a[136:139], v[36:39], v[24:27]\n"
    "v_accvgpr_write_b32 a216, v36\n"
    "v_accvgpr_write_b32 a217, v37\n"
    "v_accvgpr_write_b32 a218, v38\n"
    "v_accvgpr_write_b32 a219, v39\n"
    "s_add_u32 m0, s65, 103424\n"
    "s_add_u32 s72, s85, 23552\n"
    "buffer_load_dwordx4 v90, s[20:23], s72 offen offset:0 nt lds\n"
    "v_exp_f32 v8, v8\n"
    "v_exp_f32 v9, v9\n"
    "v_exp_f32 v10, v10\n"
    "v_exp_f32 v11, v11\n"
    "v_exp_f32 v24, v24\n"
    "v_exp_f32 v25, v25\n"
    "v_exp_f32 v26, v26\n"
    "v_exp_f32 v27, v27\n"
    "s_waitcnt vmcnt(8)\n"
    "v_cmp_eq_u64 s[54:55], v[236:237], v[238:239]\n"
    "s_nop 0\n"
    "s_cmp_eq_u64 s[54:55], -1\n"
    "s_cbranch_scc0 .LpollAV\n"
    ".LpollAVr:\n"
    "s_add_u32 m0, s65, 24576\n"
    "s_add_u32 s72, s65, 49152\n"
    "buffer_load_dwordx4 v1, s[16:19], s72 offen offset:0 sc1 lds\n"
    "v_mov_b32 v124, 0\n"
    "v_mov_b32 v125, 0\n"
    "v_mov_b32 v126, 0\n"
    "v_mov_b32 v127, 0\n"
    "v_mov_b32 v128, 0\n"
    "v_mov_b32 v129, 0\n"
    "s_add_u32 m0, s65, 25600\n"
    "s_add_u32 s72, s65, 50176\n"
    "buffer_load_dwordx4 v1, s[16:19], s72 offen offset:0 sc1 lds\n"
    "v_mov_b32 v130, 0\n"
    "v_mov_b32 v131, 0\n"
    "v_mov_b32 v132, 0\n"
    "v_mov_b32 v133, 0\n"
    "v_mov_b32 v134, 0\n"
    "v_mov_b32 v135, 0\n"
    "v_mov_b32 v136, 0\n"
    "s_add_u32 m0, s65, 26624\n"
    "s_add_u32 s72, s65, 51200\n"
    "buffer_load_dwordx4 v1, s[16:19], s72 offen offset:0 sc1 lds\n"
    "v_mov_b32 v137, 0\n"
    "v_mov_b32 v138, 0\n"
    "v_mov_b32 v139, 0\n"
    "v_mov_b32 v140, 0\n"
    "v_mov_b32 v141, 0\n"
    "v_mov_b32 v142, 0\n"
    "v_mov_b32 v143, 0\n"
    "s_add_u32 m0, s65, 27648\n"
    "s_add_u32 s72, s65, 52224\n"
    "buffer_load_dwordx4 v1, s[16:19], s72 offen offset:0 sc1 lds\n"
    "v_mov_b32 v144, 0\n"
    "v_mov_b32 v145, 0\n"
    "v_mov_b32 v146, 0\n"
    "v_mov_b32 v147, 0\n"
    "v_mov_b32 v148, 0\n"
    "v_mov_b32 v149, 0\n"
    "s_add_u32 m0, s65, 28672\n"
    "s_add_u32 s72, s65, 53248\n"
    "buffer_load_dwordx4 v1, s[16:19], s72 offen offset:0 sc1 lds\n"
    "v_mov_b32 v150, 0\n"
    "v_mov_b32 v151, 0\n"
    "v_mov_b32 v152, 0\n"
    "v_mov_b32 v153, 0\n"
    "v_mov_b32 v154, 0\n"
    "v_mov_b32 v155, 0\n"
    "v_mov_b32 v156, 0\n"
    "s_add_u32 m0, s65, 29696\n"
    "s_add_u32 s72, s65, 54272\n"
    "buffer_load_dwordx4 v1, s[16:19], s72 offen offset:0 sc1 lds\n"
    "v_mov_b32 v157, 0\n"
    "v_mov_b32 v158, 0\n"
    "v_mov_b32 v159, 0\n"
    "v_mov_b32 v160, 0\n"
    "v_mov_b32 v161, 0\n"
    "v_mov_b32 v162, 0\n"
    "v_mov_b32 v163, 0\n"
    "s_add_u32 m0, s65, 73728\n"
    "s_add_u32 s72, s65, 73728\n"
    "buffer_load_dwordx4 v1, s[16:19], s72 offen offset:0 sc1 lds\n"
    "v_mov_b32 v164, 0\n"
    "v_mov_b32 v165, 0\n"
    "v_mov_b32 v166, 0\n"
    "v_mov_b32 v167, 0\n"
    "v_mov_b32 v168, 0\n"
    "v_mov_b32 v169, 0\n"
    "s_add_u32 m0, s65, 74752\n"
    "s_add_u32 s72, s65, 74752\n"
    "buffer_load_dwordx4 v1, s[16:19], s72 offen offset:0 sc1 lds\n"
    "v_mov_b32 v170, 0\n"
    "v_mov_b32 v171, 0\n"
    "v_mov_b32 v172, 0\n"
    "v_mov_b32 v173, 0\n"
    "v_mov_b32 v174, 0\n"
    "v_mov_b32 v175, 0\n"
    "v_mov_b32 v176, 0\n"
    "s_add_u32 m0, s65, 75776\n"
    "s_add_u32 s72, s65, 75776\n"
    "buffer_load_dwordx4 v1, s[16:19], s72 offen offset:0 sc1 lds\n"
    "v_mov_b32 v177, 0\n"
    "v_mov_b32 v178, 0\n"
    "v_mov_b32 v179, 0\n"
    "v_mov_b32 v180, 0\n"
    "v_mov_b32 v181, 0\n"
    "v_mov_b32 v182, 0\n"
    "v_mov_b32 v183, 0\n"
    "s_add_u32 m0, s65, 76800\n"
    "s_add_u32 s72, s65, 76800\n"
    "buffer_load_dwordx4 v1, s[16:19], s72 offen offset:0 sc1 lds\n"
    "v_mov_b32 v184, 0\n"
    "v_mov_b32 v185, 0\n"
    "v_mov_b32 v186, 0\n"
    "v_mov_b32 v187, 0\n"
    "v_mov_b32 v188, 0\n"
    "v_mov_b32 v189, 0\n"
    "s_add_u32 m0, s65, 77824\n"
    "s_add_u32 s72, s65, 77824\n"
    "buffer_load_dwordx4 v1, s[16:19], s72 offen offset:0 sc1 lds\n"
    "v_mov_b32 v190, 0\n"
    "v_mov_b32 v191, 0\n"
    "v_mov_b32 v192, 0\n"
    "v_mov_b32 v193, 0\n"
    "v_mov_b32 v194, 0\n"
    "v_mov_b32 v195, 0\n"
    "v_mov_b32 v196, 0\n"
    "s_add_u32 m0, s65, 78848\n"
    "s_add_u32 s72, s65, 78848\n"
    "buffer_load_dwordx4 v1, s[16:19], s72 offen offset:0 sc1 lds\n"
    "v_mov_b32 v197, 0\n"
    "v_mov_b32 v198, 0\n"
    "v_mov_b32 v199, 0\n"
    "v_mov_b32 v200, 0\n"
    "v_mov_b32 v201, 0\n"
    "v_mov_b32 v202, 0\n"
    "v_mov_b32 v203, 0\n"
    "s_bitcmp1_b32 s83, 1\n"
    "s_cbranch_scc1 .Lf4b\n"
    "s_lshl_b32 s73, s53, 1\n"
    "s_add_u32 s73, s73, s79\n"
    "s_mul_i32 s74, s73, 6144\n"
    "s_add_u32 s74, s74, 1425408\n"
    "s_lshl_b32 s73, s73, 8\n"
    "s_add_u32 s73, s73, 1557504\n"
    "s_mov_b64 s[20:21], s[16:17]\n"
    "s_mov_b32 s44, 98304\n"
    "s_mov_b32 s22, 1572864\n"
    "s_bitcmp1_b32 s84, 0\n"
    "s_cselect_b32 s52, s71, s73\n"
    "s_cselect_b32 s71, s71, s74\n"
    "s_cselect_b32 s63, s63, 0\n"
    "s_cselect_b32 s45, s44, 12288\n"
    "s_cselect_b32 s44, s44, 24576\n"
    "s_cselect_b32 s42, 1523712, s42\n"
    "s_cselect_b32 s22, s22, s18\n"
    "s_cselect_b32 s60, s60, 24576\n"
    "s_cselect_b32 s61, s61, 28672\n"
    "s_cselect_b32 s62, s62, 32768\n"
    "s_waitcnt vmcnt(19)\n"
    "ds_read_b128 v[36:39], v84 offset:0\n"
    "ds_read_b128 v[40:43], v85 offset:0\n"
    "ds_read_b128 v[44:47], v84 offset:128\n"
    "ds_read_b128 v[48:51], v85 offset:128\n"
    "ds_read_b128 v[52:55], v84 offset:256\n"
    "ds_read_b128 v[56:59], v85 offset:256\n"
    "s_waitcnt lgkmcnt(5)\n"
    "v_pk_maximum3_f16 v80, v36, v37, v80\n"
    "v_pk_maximum3_f16 v80, v38, v39, v80\n"
    "v_pk_add_u16 v36, s89, v36 clamp\n"
    "v_pk_add_u16 v37, s89, v37 clamp\n"
    "v_pk_add_u16 v38, s89, v38 clamp\n"
    "v_pk_add_u16 v39, s89, v39 clamp\n"
    "s_waitcnt lgkmcnt(4)\n"
    "v_pk_maximum3_f16 v80, v40, v41, v80\n"
    "v_pk_maximum3_f16 v80, v42, v43, v80\n"
    "v_pk_add_u16 v40, s89, v40 clamp\n"
    "v_pk_add_u16 v41, s89, v41 clamp\n"
    "v_pk_add_u16 v42, s89, v42 clamp\n"
    "v_pk_add_u16 v43, s89, v43 clamp\n"
    "v_mfma_f32_16x16x32_bf16 v[12:15], a[112:115], v[36:39], v[100:103]\n"
    "s_waitcnt lgkmcnt(3)\n"
    "v_pk_maximum3_f16 v80, v44, v45, v80\n"
    "v_pk_maximum3_f16 v80, v46, v47, v80\n"
    "v_pk_add_u16 v44, s89, v44 clamp\n"
    "v_pk_add_u16 v45, s89, v45 clamp\n"
    "v_pk_add_u16 v46, s89, v46 clamp\n"
    "v_pk_add_u16 v47, s89, v47 clamp\n"
    "v_mfma_f32_16x16x32_bf16 v[12:15], a[116:119], v[40:43], v[12:15]\n"
    "s_waitcnt lgkmcnt(2)\n"
    "v_pk_maximum3_f16 v80, v48, v49, v80\n"
    "v_pk_maximum3_f16 v80, v50, v51, v80\n"
    "v_pk_add_u16 v48, s89, v48 clamp\n"
    "v_pk_add_u16 v49, s89, v49 clamp\n"
    "v_pk_add_u16 v50, s89, v50 clamp\n"
    "v_pk_add_u16 v51, s89, v51 clamp\n"
    "v_mfma_f32_16x16x32_bf16 v[12:15], a[120:123], v[44:47], v[12:15]\n"
    "s_waitcnt lgkmcnt(1)\n"
    "v_pk_maximum3_f16 v80, v52, v53, v80\n"
    "v_pk_maximum3_f16 v80, v54, v55, v80\n"
    "v_pk_add_u16 v52, s89, v52 clamp\n"
    "v_pk_add_u16 v53, s89, v53 clamp\n"
    "v_pk_add_u16 v54, s89, v54 clamp\n"
    "v_pk_add_u16 v55, s89, v55 clamp\n"
    "v_mfma_f32_16x16x32_bf16 v[12:15], a[124:127], v[48:51], v[12:15]\n"
    "s_waitcnt lgkmcnt(0)\n"
    "v_pk_maximum3_f16 v80, v56, v57, v80\n"
    "v_pk_maximum3_f16 v80, v58, v59, v80\n"
    "v_pk_add_u16 v56, s89, v56 clamp\n"
    "v_pk_add_u16 v57, s89, v57 clamp\n"
    "v_pk_add_u16 v58, s89, v58 clamp\n"
    "v_pk_add_u16 v59, s89, v59 clamp\n"
    "v_mfma_f32_16x16x32_bf16 v[12:15], a[128:131], v[52:55], v[12:15]\n"
    "s_nop 0\n"
    "v_mfma_f32_16x16x32_bf16 v[12:15], a[132:135], v[56:59], v[12:15]\n"
    "v_mfma_f32_16x16x32_bf16 v[28:31], a[136:139], v[36:39], v[104:107]\n"
    "v_accvgpr_write_b32 a240, v36\n"
    "v_accvgpr_write_b32 a241, v37\n"
    "v_accvgpr_write_b32 a242, v38\n"
    "v_accvgpr_write_b32 a243, v39\n"
    "v_mfma_f32_16x16x32_bf16 v[28:31], a[140:143], v[40:43], v[28:31]\n"
    "v_accvgpr_write_b32 a244, v40\n"
    "v_accvgpr_write_b32 a245, v41\n"
    "v_accvgpr_write_b32 a246, v42\n"
    "v_accvgpr_write_b32 a247, v43\n"
    "v_mfma_f32_16x16x32_bf16 v[28:31], a[144:147], v[44:47], v[28:31]\n"
    "v_accvgpr_write_b32 a248, v44\n"
    "v_accvgpr_write_b32 a249, v45\n"
    "v_accvgpr_write_b32 a250, v46\n"
    "v_accvgpr_write_b32 a251, v47\n"
    "v_mfma_f32_16x16x32_bf16 v[28:31], a[148:151], v[48:51], v[28:31]\n"
    "v_accvgpr_write_b32 a252, v48\n"
    "v_accvgpr_write_b32 a253, v49\n"
    "v_accvgpr_write_b32 a254, v50\n"
    "v_accvgpr_write_b32 a255, v51\n"
    "v_mfma_f32_16x16x32_bf16 v[28:31], a[152:155], v[52:55], v[28:31]\n"
    "v_accvgpr_write_b32 a160, v52\n"
    "v_accvgpr_write_b32 a161, v53\n"
    "v_accvgpr_write_b32 a162, v54\n"
    "v_accvgpr_write_b32 a163, v55\n"
    "v_mfma_f32_16x16x32_bf16 v[28:31], a[156:159], v[56:59], v[28:31]\n"
    "v_accvgpr_write_b32 a164, v56\n"
    "v_accvgpr_write_b32 a165, v57\n"
    "v_accvgpr_write_b32 a166, v58\n"
    "v_accvgpr_write_b32 a167, v59\n"
    "v_exp_f32 v12, v12\n"
    "v_exp_f32 v13, v13\n"
    "v_exp_f32 v14, v14\n"
    "v_exp_f32 v15, v15\n"
    "v_exp_f32 v28, v28\n"
    "v_exp_f32 v29, v29\n"
    "v_exp_f32 v30, v30\n"
    "v_exp_f32 v31, v31\n"
    "s_waitcnt vmcnt(12)\n"
    "ds_read_b128 v[56:59], v85 offset:49408\n"
    "ds_read_b128 v[52:55], v84 offset:49408\n"
    "ds_read_b128 v[48:51], v85 offset:49280\n"
    "ds_read_b128 v[44:47], v84 offset:49280\n"
    "ds_read_b128 v[40:43], v85 offset:49152\n"
    "ds_read_b128 v[36:39], v84 offset:49152\n"
    "s_waitcnt lgkmcnt(5)\n"
    "v_pk_maximum3_f16 v80, v56, v57, v80\n"
    "v_pk_maximum3_f16 v80, v58, v59, v80\n"
    "v_pk_add_u16 v56, s89, v56 clamp\n"
    "v_pk_add_u16 v57, s89, v57 clamp\n"
    "v_pk_add_u16 v58, s89, v58 clamp\n"
    "v_pk_add_u16 v59, s89, v59 clamp\n"
    "s_waitcnt lgkmcnt(4)\n"
    "v_pk_maximum3_f16 v80, v52, v53, v80\n"
    "v_pk_maximum3_f16 v80, v54, v55, v80\n"
    "v_pk_add_u16 v52, s89, v52 clamp\n"
    "v_pk_add_u16 v53, s89, v53 clamp\n"
    "v_pk_add_u16 v54, s89, v54 clamp\n"
    "v_pk_add_u16 v55, s89, v55 clamp\n"
    "v_mfma_f32_16x16x32_bf16 v[16:19], a[132:135], v[56:59], v[100:103]\n"
    "s_waitcnt lgkmcnt(3)\n"
    "v_pk_maximum3_f16 v80, v48, v49, v80\n"
    "v_pk_maximum3_f16 v80, v50, v51, v80\n"
    "v_pk_add_u16 v48, s89, v48 clamp\n"
    "v_pk_add_u16 v49, s89, v49 clamp\n"
    "v_pk_add_u16 v50, s89, v50 clamp\n"
    "v_pk_add_u16 v51, s89, v51 clamp\n"
    "v_mfma_f32_16x16x32_bf16 v[16:19], a[128:131], v[52:55], v[16:19]\n"
    "s_waitcnt lgkmcnt(2)\n"
    "v_pk_maximum3_f16 v80, v44, v45, v80\n"
    "v_pk_maximum3_f16 v80, v46, v47, v80\n"
    "v_pk_add_u16 v44, s89, v44 clamp\n"
    "v_pk_add_u16 v45, s89, v45 clamp\n"
    "v_pk_add_u16 v46, s89, v46 clamp\n"
    "v_pk_add_u16 v47, s89, v47 clamp\n"
    "v_mfma_f32_16x16x32_bf16 v[16:19], a[124:127], v[48:51], v[16:19]\n"
    "s_waitcnt lgkmcnt(1)\n"
    "v_pk_maximum3_f16 v80, v40, v41, v80\n"
    "v_pk_maximum3_f16 v80, v42, v43, v80\n"
    "v_pk_add_u16 v40, s89, v40 clamp\n"
    "v_pk_add_u16 v41, s89, v41 clamp\n"
    "v_pk_add_u16 v42, s89, v42 clamp\n"
    "v_pk_add_u16 v43, s89, v43 clamp\n"
    "v_mfma_f32_16x16x32_bf16 v[16:19], a[120:123], v[44:47], v[16:19]\n"
    "s_waitcnt lgkmcnt(0)\n"
    "v_pk_maximum3_f16 v80, v36, v37, v80\n"
    "v_pk_maximum3_f16 v80, v38, v39, v80\n"
    "v_pk_add_u16 v36, s89, v36 clamp\n"
    "v_pk_add_u16 v37, s89, v37 clamp\n"
    "v_pk_add_u16 v38, s89, v38 clamp\n"
    "v_pk_add_u16 v39, s89, v39 clamp\n"
    "v_mfma_f32_16x16x32_bf16 v[16:19], a[116:119], v[40:43], v[16:19]\n"
    "s_nop 0\n"
    "v_mfma_f32_16x16x32_bf16 v[16:19], a[112:115], v[36:39], v[16:19]\n"
    "v_mfma_f32_16x16x32_bf16 v[32:35], a[156:159], v[56:59], v[104:107]\n"
    "v_accvgpr_write_b32 a188, v56\n"
    "v_accvgpr_write_b32 a189, v57\n"
    "v_accvgpr_write_b32 a190, v58\n"
    "v_accvgpr_write_b32 a191, v59\n"
    "v_mfma_f32_16x16x32_bf16 v[32:35], a[152:155], v[52:55], v[32:35]\n"
    "v_accvgpr_write_b32 a184, v52\n"
    "v_accvgpr_write_b32 a185, v53\n"
    "v_accvgpr_write_b32 a186, v54\n"
    "v_accvgpr_write_b32 a187, v55\n"
    "v_mfma_f32_16x16x32_bf16 v[32:35], a[148:151], v[48:51], v[32:35]\n"
    "v_accvgpr_write_b32 a180, v48\n"
    "v_accvgpr_write_b32 a181, v49\n"
    "v_accvgpr_write_b32 a182, v50\n"
    "v_accvgpr_write_b32 a183, v51\n"
    "v_mfma_f32_16x16x32_bf16 v[32:35], a[144:147], v[44:47], v[32:35]\n"
    "v_accvgpr_write_b32 a176, v44\n"
    "v_accvgpr_write_b32 a177, v45\n"
    "v_accvgpr_write_b32 a178, v46\n"
    "v_accvgpr_write_b32 a179, v47\n"
    "v_mfma_f32_16x16x32_bf16 v[32:35], a[140:143], v[40:43], v[32:35]\n"
    "v_accvgpr_write_b32 a172, v40\n"
    "v_accvgpr_write_b32 a173, v41\n"
    "v_accvgpr_write_b32 a174, v42\n"
    "v_accvgpr_write_b32 a175, v43\n"
    "v_mfma_f32_16x16x32_bf16 v[32:35], a[136:139], v[36:39], v[32:35]\n"
    "v_accvgpr_write_b32 a168, v36\n"
    "v_accvgpr_write_b32 a169, v37\n"
    "v_accvgpr_write_b32 a170, v38\n"
    "v_accvgpr_write_b32 a171, v39\n"
    "ds_read_b128 a[112:115], v2 offset:12288\n"
    "ds_read_b128 a[116:119], v2 offset:14336\n"
    "ds_read_b128 a[120:123], v2 offset:16384\n"
    "ds_read_b128 a[124:127], v2 offset:18432\n"
    "ds_read_b128 a[128:131], v2 offset:20480\n"
    "ds_read_b128 a[132:135], v2 offset:22528\n"
    "ds_read_b128 v[100:103], v77 offset:128\n"
    "v_pk_add_u16 v80, s89, v80\n"
    "v_and_b32 v80, 0x80008000, v80\n"
    "v_cmp_ne_u32 s[34:35], 0, v80\n"
    "v_exp_f32 v16, v16\n"
    "v_exp_f32 v17, v17\n"
    "v_exp_f32 v18, v18\n"
    "v_exp_f32 v19, v19\n"
    "v_exp_f32 v32, v32\n"
    "v_exp_f32 v33, v33\n"
    "v_exp_f32 v34, v34\n"
    "v_exp_f32 v35, v35\n"
    "s_mov_b32 s66, 0\n"
    "s_waitcnt lgkmcnt(0)\n"
    "s_waitcnt vmcnt(0)\n"
    "ds_add_u32 v247, v246\n"
    "s_nop 7\n"
    "s_branch .LPV\n"
    "s_branch .Lf4e\n"
    ".Lf4b:\n"
    "s_add_u32 s75, s71, s63\n"
    "buffer_load_dwordx4 v[204:207], v242, s[40:43], s75 offen offset:0 nt\n"
    "buffer_load_dwordx4 v[208:211], v242, s[40:43], s75 offen offset:768 nt\n"
    "buffer_load_dwordx4 v[212:215], v242, s[40:43], s75 offen offset:256 nt\n"
    "buffer_load_dwordx4 v[216:219], v242, s[40:43], s75 offen offset:1024 nt\n"
    "buffer_load_dwordx4 v[220:223], v242, s[40:43], s75 offen offset:512 nt\n"
    "buffer_load_dwordx4 v[224:227], v242, s[40:43], s75 offen offset:1280 nt\n"
    "s_lshl_b32 s73, s53, 1\n"
    "s_add_u32 s73, s73, s79\n"
    "s_mul_i32 s74, s73, 6144\n"
    "s_add_u32 s74, s74, 1425408\n"
    "s_lshl_b32 s73, s73, 8\n"
    "s_add_u32 s73, s73, 1557504\n"
    "s_mov_b64 s[20:21], s[16:17]\n"
    "s_mov_b32 s44, 98304\n"
    "s_mov_b32 s22, 1572864\n"
    "s_bitcmp1_b32 s84, 0\n"
    "s_cselect_b32 s52, s71, s73\n"
    "s_cselect_b32 s71, s71, s74\n"
    "s_cselect_b32 s63, s63, 0\n"
    "s_cselect_b32 s45, s44, 12288\n"
    "s_cselect_b32 s44, s44, 24576\n"
    "s_cselect_b32 s42, 1523712, s42\n"
    "s_cselect_b32 s22, s22, s18\n"
    "s_cselect_b32 s60, s60, 24576\n"
    "s_cselect_b32 s61, s61, 28672\n"
    "s_cselect_b32 s62, s62, 32768\n"
    "s_waitcnt vmcnt(25)\n"
    "ds_read_b128 v[36:39], v84 offset:0\n"
    "ds_read_b128 v[40:43], v85 offset:0\n"
    "ds_read_b128 v[44:47], v84 offset:128\n"
    "ds_read_b128 v[48:51], v85 offset:128\n"
    "ds_read_b128 v[52:55], v84 offset:256\n"
    "ds_read_b128 v[56:59], v85 offset:256\n"
    "s_waitcnt lgkmcnt(5)\n"
    "v_pk_maximum3_f16 v80, v36, v37, v80\n"
    "v_pk_maximum3_f16 v80, v38, v39, v80\n"
    "v_pk_add_u16 v36, s89, v36 clamp\n"
    "v_pk_add_u16 v37, s89, v37 clamp\n"
    "v_pk_add_u16 v38, s89, v38 clamp\n"
    "v_pk_add_u16 v39, s89, v39 clamp\n"
    "s_waitcnt lgkmcnt(4)\n"
    "v_pk_maximum3_f16 v80, v40, v41, v80\n"
    "v_pk_maximum3_f16 v80, v42, v43, v80\n"
    "v_pk_add_u16 v40, s89, v40 clamp\n"
    "v_pk_add_u16 v41, s89, v41 clamp\n"
    "v_pk_add_u16 v42, s89, v42 clamp\n"
    "v_pk_add_u16 v43, s89, v43 clamp\n"
    "v_mfma_f32_16x16x32_bf16 v[12:15], a[112:115], v[36:39], v[100:103]\n"
    "s_waitcnt lgkmcnt(3)\n"
    "v_pk_maximum3_f16 v80, v44, v45, v80\n"
    "v_pk_maximum3_f16 v80, v46, v47, v80\n"
    "v_pk_add_u16 v44, s89, v44 clamp\n"
    "v_pk_add_u16 v45, s89, v45 clamp\n"
    "v_pk_add_u16 v46, s89, v46 clamp\n"
    "v_pk_add_u16 v47, s89, v47 clamp\n"
    "v_mfma_f32_16x16x32_bf16 v[12:15], a[116:119], v[40:43], v[12:15]\n"
    "s_waitcnt lgkmcnt(2)\n"
    "v_pk_maximum3_f16 v80, v48, v49, v80\n"
    "v_pk_maximum3_f16 v80, v50, v51, v80\n"
    "v_pk_add_u16 v48, s89, v48 clamp\n"
    "v_pk_add_u16 v49, s89, v49 clamp\n"
    "v_pk_add_u16 v50, s89, v50 clamp\n"
    "v_pk_add_u16 v51, s89, v51 clamp\n"
    "v_mfma_f32_16x16x32_bf16 v[12:15], a[120:123], v[44:47], v[12:15]\n"
    "s_waitcnt lgkmcnt(1)\n"
    "v_pk_maximum3_f16 v80, v52, v53, v80\n"
    "v_pk_maximum3_f16 v80, v54, v55, v80\n"
    "v_pk_add_u16 v52, s89, v52 clamp\n"
    "v_pk_add_u16 v53, s89, v53 clamp\n"
    "v_pk_add_u16 v54, s89, v54 clamp\n"
    "v_pk_add_u16 v55, s89, v55 clamp\n"
    "v_mfma_f32_16x16x32_bf16 v[12:15], a[124:127], v[48:51], v[12:15]\n"
    "s_waitcnt lgkmcnt(0)\n"
    "v_pk_maximum3_f16 v80, v56, v57, v80\n"
    "v_pk_maximum3_f16 v80, v58, v59, v80\n"
    "v_pk_add_u16 v56, s89, v56 clamp\n"
    "v_pk_add_u16 v57, s89, v57 clamp\n"
    "v_pk_add_u16 v58, s89, v58 clamp\n"
    "v_pk_add_u16 v59, s89, v59 clamp\n"
    "v_mfma_f32_16x16x32_bf16 v[12:15], a[128:131], v[52:55], v[12:15]\n"
    "s_nop 0\n"
    "v_mfma_f32_16x16x32_bf16 v[12:15], a[132:135], v[56:59], v[12:15]\n"
    "v_mfma_f32_16x16x32_bf16 v[28:31], a[136:139], v[36:39], v[104:107]\n"
    "v_accvgpr_write_b32 a240, v36\n"
    "v_accvgpr_write_b32 a241, v37\n"
    "v_accvgpr_write_b32 a242, v38\n"
    "v_accvgpr_write_b32 a243, v39\n"
    "v_mfma_f32_16x16x32_bf16 v[28:31], a[140:143], v[40:43], v[28:31]\n"
    "v_accvgpr_write_b32 a244, v40\n"
    "v_accvgpr_write_b32 a245, v41\n"
    "v_accvgpr_write_b32 a246, v42\n"
    "v_accvgpr_write_b32 a247, v43\n"
    "v_mfma_f32_16x16x32_bf16 v[28:31], a[144:147], v[44:47], v[28:31]\n"
    "v_accvgpr_write_b32 a248, v44\n"
    "v_accvgpr_write_b32 a249, v45\n"
    "v_accvgpr_write_b32 a250, v46\n"
    "v_accvgpr_write_b32 a251, v47\n"
    "v_mfma_f32_16x16x32_bf16 v[28:31], a[148:151], v[48:51], v[28:31]\n"
    "v_accvgpr_write_b32 a252, v48\n"
    "v_accvgpr_write_b32 a253, v49\n"
    "v_accvgpr_write_b32 a254, v50\n"
    "v_accvgpr_write_b32 a255, v51\n"
    "v_mfma_f32_16x16x32_bf16 v[28:31], a[152:155], v[52:55], v[28:31]\n"
    "v_accvgpr_write_b32 a160, v52\n"
    "v_accvgpr_write_b32 a161, v53\n"
    "v_accvgpr_write_b32 a162, v54\n"
    "v_accvgpr_write_b32 a163, v55\n"
    "v_mfma_f32_16x16x32_bf16 v[28:31], a[156:159], v[56:59], v[28:31]\n"
    "v_accvgpr_write_b32 a164, v56\n"
    "v_accvgpr_write_b32 a165, v57\n"
    "v_accvgpr_write_b32 a166, v58\n"
    "v_accvgpr_write_b32 a167, v59\n"
    "v_exp_f32 v12, v12\n"
    "v_exp_f32 v13, v13\n"
    "v_exp_f32 v14, v14\n"
    "v_exp_f32 v15, v15\n"
    "v_exp_f32 v28, v28\n"
    "v_exp_f32 v29, v29\n"
    "v_exp_f32 v30, v30\n"
    "v_exp_f32 v31, v31\n"
    "s_waitcnt vmcnt(18)\n"
    "ds_read_b128 v[56:59], v85 offset:49408\n"
    "ds_read_b128 v[52:55], v84 offset:49408\n"
    "ds_read_b128 v[48:51], v85 offset:49280\n"
    "ds_read_b128 v[44:47], v84 offset:49280\n"
    "ds_read_b128 v[40:43], v85 offset:49152\n"
    "ds_read_b128 v[36:39], v84 offset:49152\n"
    "s_waitcnt lgkmcnt(5)\n"
    "v_pk_maximum3_f16 v80, v56, v57, v80\n"
    "v_pk_maximum3_f16 v80, v58, v59, v80\n"
    "v_pk_add_u16 v56, s89, v56 clamp\n"
    "v_pk_add_u16 v57, s89, v57 clamp\n"
    "v_pk_add_u16 v58, s89, v58 clamp\n"
    "v_pk_add_u16 v59, s89, v59 clamp\n"
    "s_waitcnt lgkmcnt(4)\n"
    "v_pk_maximum3_f16 v80, v52, v53, v80\n"
    "v_pk_maximum3_f16 v80, v54, v55, v80\n"
    "v_pk_add_u16 v52, s89, v52 clamp\n"
    "v_pk_add_u16 v53, s89, v53 clamp\n"
    "v_pk_add_u16 v54, s89, v54 clamp\n"
    "v_pk_add_u16 v55, s89, v55 clamp\n"
    "v_mfma_f32_16x16x32_bf16 v[16:19], a[132:135], v[56:59], v[100:103]\n"
    "s_waitcnt lgkmcnt(3)\n"
    "v_pk_maximum3_f16 v80, v48, v49, v80\n"
    "v_pk_maximum3_f16 v80, v50, v51, v80\n"
    "v_pk_add_u16 v48, s89, v48 clamp\n"
    "v_pk_add_u16 v49, s89, v49 clamp\n"
    "v_pk_add_u16 v50, s89, v50 clamp\n"
    "v_pk_add_u16 v51, s89, v51 clamp\n"
    "v_mfma_f32_16x16x32_bf16 v[16:19], a[128:131], v[52:55], v[16:19]\n"
    "s_waitcnt lgkmcnt(2)\n"
    "v_pk_maximum3_f16 v80, v44, v45, v80\n"
    "v_pk_maximum3_f16 v80, v46, v47, v80\n"
    "v_pk_add_u16 v44, s89, v44 clamp\n"
    "v_pk_add_u16 v45, s89, v45 clamp\n"
    "v_pk_add_u16 v46, s89, v46 clamp\n"
    "v_pk_add_u16 v47, s89, v47 clamp\n"
    "v_mfma_f32_16x16x32_bf16 v[16:19], a[124:127], v[48:51], v[16:19]\n"
    "s_waitcnt lgkmcnt(1)\n"
    "v_pk_maximum3_f16 v80, v40, v41, v80\n"
    "v_pk_maximum3_f16 v80, v42, v43, v80\n"
    "v_pk_add_u16 v40, s89, v40 clamp\n"
    "v_pk_add_u16 v41, s89, v41 clamp\n"
    "v_pk_add_u16 v42, s89, v42 clamp\n"
    "v_pk_add_u16 v43, s89, v43 clamp\n"
    "v_mfma_f32_16x16x32_bf16 v[16:19], a[120:123], v[44:47], v[16:19]\n"
    "s_waitcnt lgkmcnt(0)\n"
    "v_pk_maximum3_f16 v80, v36, v37, v80\n"
    "v_pk_maximum3_f16 v80, v38, v39, v80\n"
    "v_pk_add_u16 v36, s89, v36 clamp\n"
    "v_pk_add_u16 v37, s89, v37 clamp\n"
    "v_pk_add_u16 v38, s89, v38 clamp\n"
    "v_pk_add_u16 v39, s89, v39 clamp\n"
    "v_mfma_f32_16x16x32_bf16 v[16:19], a[116:119], v[40:43], v[16:19]\n"
    "s_nop 0\n"
    "v_mfma_f32_16x16x32_bf16 v[16:19], a[112:115], v[36:39], v[16:19]\n"
    "v_mfma_f32_16x16x32_bf16 v[32:35], a[156:159], v[56:59], v[104:107]\n"
    "v_accvgpr_write_b32 a188, v56\n"
    "v_accvgpr_write_b32 a189, v57\n"
    "v_accvgpr_write_b32 a190, v58\n"
    "v_accvgpr_write_b32 a191, v59\n"
    "v_mfma_f32_16x16x32_bf16 v[32:35], a[152:155], v[52:55], v[32:35]\n"
    "v_accvgpr_write_b32 a184, v52\n"
    "v_accvgpr_write_b32 a185, v53\n"
    "v_accvgpr_write_b32 a186, v54\n"
    "v_accvgpr_write_b32 a187, v55\n"
    "v_mfma_f32_16x16x32_bf16 v[32:35], a[148:151], v[48:51], v[32:35]\n"
    "v_accvgpr_write_b32 a180, v48\n"
    "v_accvgpr_write_b32 a181, v49\n"
    "v_accvgpr_write_b32 a182, v50\n"
    "v_accvgpr_write_b32 a183, v51\n"
    "v_mfma_f32_16x16x32_bf16 v[32:35], a[144:147], v[44:47], v[32:35]\n"
    "v_accvgpr_write_b32 a176, v44\n"
    "v_accvgpr_write_b32 a177, v45\n"
    "v_accvgpr_write_b32 a178, v46\n"
    "v_accvgpr_write_b32 a179, v47\n"
    "v_mfma_f32_16x16x32_bf16 v[32:35], a[140:143], v[40:43], v[32:35]\n"
    "v_accvgpr_write_b32 a172, v40\n"
    "v_accvgpr_write_b32 a173, v41\n"
    "v_accvgpr_write_b32 a174, v42\n"
    "v_accvgpr_write_b32 a175, v43\n"
    "v_mfma_f32_16x16x32_bf16 v[32:35], a[136:139], v[36:39], v[32:35]\n"
    "v_accvgpr_write_b32 a168, v36\n"
    "v_accvgpr_write_b32 a169, v37\n"
    "v_accvgpr_write_b32 a170, v38\n"
    "v_accvgpr_write_b32 a171, v39\n"
    "ds_read_b128 a[112:115], v2 offset:12288\n"
    "ds_read_b128 a[116:119], v2 offset:14336\n"
    "ds_read_b128 a[120:123], v2 offset:16384\n"
    "ds_read_b128 a[124:127], v2 offset:18432\n"
    "ds_read_b128 a[128:131], v2 offset:20480\n"
    "ds_read_b128 a[132:135], v2 offset:22528\n"
    "ds_read_b128 v[100:103], v77 offset:128\n"
    "v_pk_add_u16 v80, s89, v80\n"
    "v_and_b32 v80, 0x80008000, v80\n"
    "v_cmp_ne_u32 s[34:35], 0, v80\n"
    "v_exp_f32 v16, v16\n"
    "v_exp_f32 v17, v17\n"
    "v_exp_f32 v18, v18\n"
    "v_exp_f32 v19, v19\n"
    "v_exp_f32 v32, v32\n"
    "v_exp_f32 v33, v33\n"
    "v_exp_f32 v34, v34\n"
    "v_exp_f32 v35, v35\n"
    "s_mov_b32 s66, 0\n"
    "s_waitcnt lgkmcnt(0)\n"
    "s_waitcnt vmcnt(6)\n"
    "ds_add_u32 v247, v246\n"
    "s_nop 7\n"
    "s_branch .LNV\n"
    ".Lf4e:\n"
    "s_nop 4\n"
    ".Lf5e:\n"
    "s_nop 4\n"
    ".fill ((0x20000 - (.LwKe - .LwK) - (. - _Z8attn_fwdPKtPKiS2_S0_S0_S0_S0_PcPt)) & 4095) / 4, 4, 0xBF800000\n"
    ".LwK:\n"
    "s_branch .LwKe\n"
    ".LPK:\n"
    "v_mfma_f32_16x16x32_bf16 v[36:39], a[112:115], a[192:195], v[100:103]\n"
    "s_add_u32 s71, s71, 98304\n"
    "ds_read_b128 a[136:139], v2 offset:13312\n"
    "v_cvt_pk_bf16_f32 v68, v4, v5\n"
    "v_mfma_f32_16x16x32_bf16 v[36:39], a[116:119], a[196:199], v[36:39]\n"
    "s_add_u32 s75, s71, s63\n"
    "ds_read_b128 a[140:143], v2 offset:15360\n"
    "v_cvt_pk_bf16_f32 v69, v6, v7\n"
    "v_mfma_f32_16x16x32_bf16 v[36:39], a[120:123], a[200:203], v[36:39]\n"
    "s_add_u32 s76, s75, 6144\n"
    "ds_read_b128 a[144:147], v2 offset:17408\n"
    "v_cvt_pk_bf16_f32 v76, v8, v9\n"
    "s_nop 0\n"
    "v_mfma_f32_16x16x32_bf16 v[36:39], a[124:127], a[204:207], v[36:39]\n"
    "s_add_u32 s77, s75, 24576\n"
    "ds_read_b128 a[148:151], v2 offset:19456\n"
    "v_cvt_pk_bf16_f32 v77, v10, v11\n"
    "v_mfma_f32_16x16x32_bf16 v[36:39], a[128:131], a[208:211], v[36:39]\n"
    "s_add_u32 s78, s75, 30720\n"
    "ds_read_b128 a[152:155], v2 offset:21504\n"
    "v_cvt_pk_bf16_f32 v84, v12, v13\n"
    "v_mfma_f32_16x16x32_bf16 v[36:39], a[132:135], a[212:215], v[36:39]\n"
    "ds_read_b128 a[156:159], v2 offset:23552\n"
    "v_cvt_pk_bf16_f32 v85, v14, v15\n"
    "v_mfma_f32_16x16x32_bf16 v[40:43], a[132:135], a[236:239], v[100:103]\n"
    "ds_read_b128 v[104:107], v250 offset:144\n"
    "v_cvt_pk_bf16_f32 v92, v16, v17\n"
    "v_mfma_f32_16x16x32_bf16 v[40:43], a[128:131], a[232:235], v[40:43]\n"
    "s_add_u32 s68, s68, 24576\n"
    "v_cvt_pk_bf16_f32 v93, v18, v19\n"
    "v_cvt_pk_bf16_f32 v70, v20, v21\n"
    "v_mfma_f32_16x16x32_bf16 v[40:43], a[124:127], a[228:231], v[40:43]\n"
    "s_cmp_ge_u32 s68, s69\n"
    "s_cselect_b32 s68, s65, s68\n"
    "v_cvt_pk_bf16_f32 v71, v22, v23\n"
    "v_mfma_f32_16x16x32_bf16 v[40:43], a[120:123], a[224:227], v[40:43]\n"
    "v_cvt_pk_bf16_f32 v78, v24, v25\n"
    "v_cvt_pk_bf16_f32 v79, v26, v27\n"
    "v_mfma_f32_16x16x32_bf16 v[40:43], a[116:119], a[220:223], v[40:43]\n"
    "v_cvt_pk_bf16_f32 v86, v28, v29\n"
    "v_cvt_pk_bf16_f32 v87, v30, v31\n"
    "v_mfma_f32_16x16x32_bf16 v[40:43], a[112:115], a[216:219], v[40:43]\n"
    "v_cvt_pk_bf16_f32 v94, v32, v33\n"
    "v_cvt_pk_bf16_f32 v95, v34, v35\n"
    "v_mfma_f32_16x16x32_bf16 v[44:47], a[112:115], a[240:243], v[100:103]\n"
    "v_exp_f32 v36, v36\n"
    "v_mfma_f32_16x16x32_bf16 v[44:47], a[116:119], a[244:247], v[44:47]\n"
    "v_exp_f32 v37, v37\n"
    "v_mfma_f32_16x16x32_bf16 v[44:47], a[120:123], a[248:251], v[44:47]\n"
    "v_exp_f32 v38, v38\n"
    "v_mfma_f32_16x16x32_bf16 v[44:47], a[124:127], a[252:255], v[44:47]\n"
    "v_exp_f32 v39, v39\n"
    "v_mfma_f32_16x16x32_bf16 v[44:47], a[128:131], a[160:163], v[44:47]\n"
    "v_cvt_pk_bf16_f32 v72, v36, v37\n"
    "v_cvt_pk_bf16_f32 v73, v38, v39\n"
    "v_mfma_f32_16x16x32_bf16 v[44:47], a[132:135], a[164:167], v[44:47]\n"
    "v_exp_f32 v40, v40\n"
    "v_mfma_f32_16x16x32_bf16 v[48:51], a[132:135], a[188:191], v[100:103]\n"
    "ds_read_b32 v245, v247\n"
    "v_mfma_f32_16x16x32_bf16 v[48:51], a[128:131], a[184:187], v[48:51]\n"
    "s_add_u32 m0, s68, 0\n"
    "v_exp_f32 v41, v41\n"
    "v_mfma_f32_16x16x32_bf16 v[48:51], a[124:127], a[180:183], v[48:51]\n"
    "v_exp_f32 v42, v42\n"
    "v_mfma_f32_16x16x32_bf16 v[48:51], a[120:123], a[176:179], v[48:51]\n"
    "v_exp_f32 v43, v43\n"
    "v_mfma_f32_16x16x32_bf16 v[48:51], a[116:119], a[172:175], v[48:51]\n"
    "s_waitcnt vmcnt(10)\n"
    "v_cmp_eq_u64 s[54:55], v[236:237], v[238:239]\n"
    "v_cvt_pk_bf16_f32 v80, v40, v41\n"
    "v_mfma_f32_16x16x32_bf16 v[48:51], a[112:115], a[168:171], v[48:51]\n"
    "s_waitcnt lgkmcnt(1)\n"
    "v_cvt_pk_bf16_f32 v81, v42, v43\n"
    "v_mfma_f32_16x16x32_bf16 v[52:55], a[136:139], a[192:195], v[104:107]\n"
    "s_waitcnt lgkmcnt(0)\n"
    "v_cmp_le_u32 s[56:57], s59, v245\n"
    "v_mfma_f32_16x16x32_bf16 v[52:55], a[140:143], a[196:199], v[52:55]\n"
    "v_exp_f32 v44, v44\n"
    "v_mfma_f32_16x16x32_bf16 v[52:55], a[144:147], a[200:203], v[52:55]\n"
    "s_and_b64 s[54:55], s[54:55], s[56:57]\n"
    "s_cmp_eq_u64 s[54:55], -1\n"
    "v_mfma_f32_16x16x32_bf16 v[52:55], a[148:151], a[204:207], v[52:55]\n"
    "s_cbranch_scc0 .Lpoll1\n"
    ".Lpoll1r:\n"
    "v_mfma_f32_16x16x32_bf16 v[52:55], a[152:155], a[208:211], v[52:55]\n"
    "buffer_load_dwordx4 v1, s[16:19], s67 offen offset:0 sc1 lds\n"
    "v_mfma_f32_16x16x32_bf16 v[52:55], a[156:159], a[212:215], v[52:55]\n"
    "ds_read_b128 a[112:115], v3 offset:0\n"
    "v_mfma_f32_16x16x32_bf16 v[56:59], a[156:159], a[236:239], v[104:107]\n"
    "ds_read_b128 a[116:119], v3 offset:2048\n"
    "v_mfma_f32_16x16x32_bf16 v[56:59], a[152:155], a[232:235], v[56:59]\n"
    "ds_read_b128 a[120:123], v3 offset:4096\n"
    "v_mfma_f32_16x16x32_bf16 v[56:59], a[148:151], a[228:231], v[56:59]\n"
    "buffer_load_dwordx4 v1, s[16:19], s67 offen offset:1024 sc1 lds\n"
    "v_mfma_f32_16x16x32_bf16 v[56:59], a[144:147], a[224:227], v[56:59]\n"
    "ds_read_b128 a[124:127], v3 offset:6144\n"
    "v_mfma_f32_16x16x32_bf16 v[56:59], a[140:143], a[220:223], v[56:59]\n"
    "ds_read_b128 a[128:131], v3 offset:8192\n"
    "v_mfma_f32_16x16x32_bf16 v[56:59], a[136:139], a[216:219], v[56:59]\n"
    "ds_read_b128 a[132:135], v3 offset:10240\n"
    "v_mfma_f32_16x16x32_bf16 v[60:63], a[136:139], a[240:243], v[104:107]\n"
    "buffer_load_dwordx4 v1, s[16:19], s67 offen offset:2048 sc1 lds\n"
    "v_mfma_f32_16x16x32_bf16 v[60:63], a[140:143], a[244:247], v[60:63]\n"
    "ds_read_b128 v[100:103], v250 offset:768\n"
    "v_mfma_f32_16x16x32_bf16 v[60:63], a[144:147], a[248:251], v[60:63]\n"
    "v_exp_f32 v52, v52\n"
    "v_mfma_f32_16x16x32_bf16 v[60:63], a[148:151], a[252:255], v[60:63]\n"
    "v_exp_f32 v53, v53\n"
    "v_mfma_f32_16x16x32_bf16 v[60:63], a[152:155], a[160:163], v[60:63]\n"
    "buffer_load_dwordx4 v1, s[16:19], s67 offen offset:3072 sc1 lds\n"
    "v_mfma_f32_16x16x32_bf16 v[60:63], a[156:159], a[164:167], v[60:63]\n"
    "s_add_u32 m0, s68, 4096\n"
    "v_exp_f32 v54, v54\n"
    "v_mfma_f32_16x16x32_bf16 v[64:67], a[156:159], a[188:191], v[104:107]\n"
    "s_add_u32 s83, s67, 4096\n"
    "v_exp_f32 v55, v55\n"
    "v_mfma_f32_16x16x32_bf16 v[64:67], a[152:155], a[184:187], v[64:67]\n"
    "v_cvt_pk_bf16_f32 v74, v52, v53\n"
    "v_cvt_pk_bf16_f32 v75, v54, v55\n"
    "v_mfma_f32_16x16x32_bf16 v[64:67], a[148:151], a[180:183], v[64:67]\n"
    "buffer_load_dwordx4 v1, s[16:19], s83 offen offset:0 sc1 lds\n"
    "v_mfma_f32_16x16x32_bf16 v[64:67], a[144:147], a[176:179], v[64:67]\n"
    "v_exp_f32 v56, v56\n"
    "v_mfma_f32_16x16x32_bf16 v[64:67], a[140:143], a[172:175], v[64:67]\n"
    "v_exp_f32 v57, v57\n"
    "v_mfma_f32_16x16x32_bf16 v[64:67], a[136:139], a[168:171], v[64:67]\n"
    "s_waitcnt lgkmcnt(0)\n"
    "v_exp_f32 v58, v58\n"
    "v_mfma_f32_16x16x32_bf16 v[4:7], a[112:115], a[192:195], v[100:103]\n"
    "buffer_load_dwordx4 v1, s[16:19], s83 offen offset:1024 sc1 lds\n"
    "v_mfma_f32_16x16x32_bf16 v[4:7], a[116:119], a[196:199], v[4:7]\n"
    "s_add_u32 m0, s64, 0\n"
    "ds_read_b128 a[136:139], v3 offset:1024\n"
    "v_cvt_pk_bf16_f32 v82, v56, v57\n"
    "v_mfma_f32_16x16x32_bf16 v[4:7], a[120:123], a[200:203], v[4:7]\n"
    "s_add_u32 s83, s67, 24576\n"
    "ds_read_b128 a[140:143], v3 offset:3072\n"
    "v_mfma_f32_16x16x32_bf16 v[4:7], a[124:127], a[204:207], v[4:7]\n"
    "ds_read_b128 a[144:147], v3 offset:5120\n"
    "v_mfma_f32_16x16x32_bf16 v[4:7], a[128:131], a[208:211], v[4:7]\n"
    "buffer_load_dwordx4 v1, s[16:19], s83 offen offset:0 sc1 lds\n"
    "v_mfma_f32_16x16x32_bf16 v[4:7], a[132:135], a[212:215], v[4:7]\n"
    "ds_read_b128 a[148:151], v3 offset:7168\n"
    "v_mfma_f32_16x16x32_bf16 v[8:11], a[132:135], a[236:239], v[100:103]\n"
    "ds_read_b128 a[152:155], v3 offset:9216\n"
    "v_mfma_f32_16x16x32_bf16 v[8:11], a[128:131], a[232:235], v[8:11]\n"
    "ds_read_b128 a[156:159], v3 offset:11264\n"
    "v_mfma_f32_16x16x32_bf16 v[8:11], a[124:127], a[228:231], v[8:11]\n"
    "buffer_load_dwordx4 v1, s[16:19], s83 offen offset:1024 sc1 lds\n"
    "v_mfma_f32_16x16x32_bf16 v[8:11], a[120:123], a[224:227], v[8:11]\n"
    "ds_read_b128 v[104:107], v250 offset:784\n"
    "v_mfma_f32_16x16x32_bf16 v[8:11], a[116:119], a[220:223], v[8:11]\n"
    "v_exp_f32 v59, v59\n"
    "v_mfma_f32_16x16x32_bf16 v[8:11], a[112:115], a[216:219], v[8:11]\n"
    "v_cvt_pk_bf16_f32 v83, v58, v59\n"
    "v_mfma_f32_16x16x32_bf16 v[12:15], a[112:115], a[240:243], v[100:103]\n"
    "buffer_load_dwordx4 v1, s[16:19], s83 offen offset:2048 sc1 lds\n"
    "v_mfma_f32_16x16x32_bf16 v[12:15], a[116:119], a[244:247], v[12:15]\n"
    "v_exp_f32 v45, v45\n"
    "v_mfma_f32_16x16x32_bf16 v[12:15], a[120:123], a[248:251], v[12:15]\n"
    "v_exp_f32 v46, v46\n"
    "v_mfma_f32_16x16x32_bf16 v[12:15], a[124:127], a[252:255], v[12:15]\n"
    "v_exp_f32 v47, v47\n"
    "v_mfma_f32_16x16x32_bf16 v[12:15], a[128:131], a[160:163], v[12:15]\n"
    "buffer_load_dwordx4 v1, s[16:19], s83 offen offset:3072 sc1 lds\n"
    "v_mfma_f32_16x16x32_bf16 v[12:15], a[132:135], a[164:167], v[12:15]\n"
    "s_add_u32 m0, s64, 4096\n"
    "v_exp_f32 v60, v60\n"
    "v_mfma_f32_16x16x32_bf16 v[16:19], a[132:135], a[188:191], v[100:103]\n"
    "s_add_u32 s83, s67, 28672\n"
    "v_exp_f32 v61, v61\n"
    "v_mfma_f32_16x16x32_bf16 v[16:19], a[128:131], a[184:187], v[16:19]\n"
    "v_exp_f32 v62, v62\n"
    "v_mfma_f32_16x16x32_bf16 v[16:19], a[124:127], a[180:183], v[16:19]\n"
    "buffer_load_dwordx4 v1, s[16:19], s83 offen offset:0 sc1 lds\n"
    "v_mfma_f32_16x16x32_bf16 v[16:19], a[120:123], a[176:179], v[16:19]\n"
    "v_exp_f32 v63, v63\n"
    "v_mfma_f32_16x16x32_bf16 v[16:19], a[116:119], a[172:175], v[16:19]\n"
    "v_cvt_pk_bf16_f32 v88, v44, v45\n"
    "v_cvt_pk_bf16_f32 v89, v46, v47\n"
    "v_mfma_f32_16x16x32_bf16 v[16:19], a[112:115], a[168:171], v[16:19]\n"
    "s_waitcnt lgkmcnt(0)\n"
    "v_cvt_pk_bf16_f32 v90, v60, v61\n"
    "v_cvt_pk_bf16_f32 v91, v62, v63\n"
    "v_mfma_f32_16x16x32_bf16 v[20:23], a[136:139], a[192:195], v[104:107]\n"
    "buffer_load_dwordx4 v1, s[16:19], s83 offen offset:1024 sc1 lds\n"
    "v_mfma_f32_16x16x32_bf16 v[20:23], a[140:143], a[196:199], v[20:23]\n"
    "s_add_u32 s67, s67, 49152\n"
    "ds_read_b128 a[112:115], v248 offset:0\n"
    "v_mfma_f32_16x16x32_bf16 v[20:23], a[144:147], a[200:203], v[20:23]\n"
    "s_add_u32 s59, s59, 4\n"
    "ds_read_b128 a[116:119], v248 offset:12288\n"
    "v_mfma_f32_16x16x32_bf16 v[20:23], a[148:151], a[204:207], v[20:23]\n"
    "s_waitcnt vmcnt(12)\n"
    "ds_read_b128 a[120:123], v248 offset:1024\n"
    "v_mfma_f32_16x16x32_bf16 v[20:23], a[152:155], a[208:211], v[20:23]\n"
    "buffer_store_dwordx2 v[238:239], v240, s[36:39], s80 offen offset:0 sc1\n"
    "v_mfma_f32_16x16x32_bf16 v[20:23], a[156:159], a[212:215], v[20:23]\n"
    "buffer_load_dwordx2 v[204:205], v242, s[28:31], s75 offen offset:0 nt\n"
    "v_mfma_f32_16x16x32_bf16 v[24:27], a[156:159], a[236:239], v[104:107]\n"
    "s_add_u32 s80, s80, 256\n"
    "ds_read_b128 a[124:127], v248 offset:13312\n"
    "v_mfma_f32_16x16x32_bf16 v[24:27], a[152:155], a[232:235], v[24:27]\n"
    "buffer_load_dwordx2 v[206:207], v242, s[28:31], s75 offen offset:768 nt\n"
    "v_mfma_f32_16x16x32_bf16 v[24:27], a[148:151], a[228:231], v[24:27]\n"
    "ds_read_b128 a[128:131], v248 offset:2048\n"
    "v_mfma_f32_16x16x32_bf16 v[24:27], a[144:147], a[224:227], v[24:27]\n"
    "buffer_load_dwordx2 v[208:209], v242, s[28:31], s75 offen offset:3072 nt\n"
    "v_mfma_f32_16x16x32_bf16 v[24:27], a[140:143], a[220:223], v[24:27]\n"
    "ds_read_b128 a[132:135], v248 offset:14336\n"
    "v_mfma_f32_16x16x32_bf16 v[24:27], a[136:139], a[216:219], v[24:27]\n"
    "buffer_load_dwordx2 v[210:211], v242, s[28:31], s75 offen offset:3840 nt\n"
    "v_mfma_f32_16x16x32_bf16 v[28:31], a[136:139], a[240:243], v[104:107]\n"
    "v_exp_f32 v48, v48\n"
    "v_mfma_f32_16x16x32_bf16 v[28:31], a[140:143], a[244:247], v[28:31]\n"
    "buffer_load_dwordx2 v[212:213], v242, s[28:31], s76 offen offset:0 nt\n"
    "v_mfma_f32_16x16x32_bf16 v[28:31], a[144:147], a[248:251], v[28:31]\n"
    "v_exp_f32 v49, v49\n"
    "v_mfma_f32_16x16x32_bf16 v[28:31], a[148:151], a[252:255], v[28:31]\n"
    "buffer_load_dwordx2 v[214:215], v242, s[28:31], s76 offen offset:768 nt\n"
    "v_mfma_f32_16x16x32_bf16 v[28:31], a[152:155], a[160:163], v[28:31]\n"
    "v_exp_f32 v50, v50\n"
    "v_mfma_f32_16x16x32_bf16 v[28:31], a[156:159], a[164:167], v[28:31]\n"
    "buffer_load_dwordx2 v[216:217], v242, s[28:31], s76 offen offset:3072 nt\n"
    "v_mfma_f32_16x16x32_bf16 v[32:35], a[156:159], a[188:191], v[104:107]\n"
    "v_exp_f32 v51, v51\n"
    "v_mfma_f32_16x16x32_bf16 v[32:35], a[152:155], a[184:187], v[32:35]\n"
    "buffer_load_dwordx2 v[218:219], v242, s[28:31], s76 offen offset:3840 nt\n"
    "v_mfma_f32_16x16x32_bf16 v[32:35], a[148:151], a[180:183], v[32:35]\n"
    "v_exp_f32 v64, v64\n"
    "v_mfma_f32_16x16x32_bf16 v[32:35], a[144:147], a[176:179], v[32:35]\n"
    "buffer_load_dwordx2 v[220:221], v242, s[28:31], s77 offen offset:0 nt\n"
    "v_mfma_f32_16x16x32_bf16 v[32:35], a[140:143], a[172:175], v[32:35]\n"
    "v_exp_f32 v65, v65\n"
    "v_mfma_f32_16x16x32_bf16 v[32:35], a[136:139], a[168:171], v[32:35]\n"
    "s_waitcnt lgkmcnt(0)\n"
    "v_exp_f32 v66, v66\n"
    "v_mfma_f32_16x16x32_bf16 v[124:127], a[112:115], v[68:71], v[124:127]\n"
    "buffer_load_dwordx2 v[222:223], v242, s[28:31], s77 offen offset:768 nt\n"
    "v_mfma_f32_16x16x32_bf16 v[124:127], a[116:119], v[72:75], v[124:127]\n"
    "ds_read_b128 a[136:139], v248 offset:3072\n"
    "v_cvt_pk_bf16_f32 v96, v48, v49\n"
    "v_mfma_f32_16x16x32_bf16 v[128:131], a[124:127], v[72:75], v[128:131]\n"
    "buffer_load_dwordx2 v[224:225], v242, s[28:31], s77 offen offset:3072 nt\n"
    "v_mfma_f32_16x16x32_bf16 v[128:131], a[120:123], v[68:71], v[128:131]\n"
    "ds_read_b128 a[140:143], v248 offset:15360\n"
    "v_cvt_pk_bf16_f32 v97, v50, v51\n"
    "v_mfma_f32_16x16x32_bf16 v[132:135], a[128:131], v[68:71], v[132:135]\n"
    "buffer_load_dwordx2 v[226:227], v242, s[28:31], s77 offen offset:3840 nt\n"
    "v_mfma_f32_16x16x32_bf16 v[132:135], a[132:135], v[72:75], v[132:135]\n"
    "ds_read_b128 a[144:147], v248 offset:4096\n"
    "v_cvt_pk_bf16_f32 v98, v64, v65\n"
    "v_mfma_f32_16x16x32_bf16 v[148:151], a[132:135], v[80:83], v[148:151]\n"
    "buffer_load_dwordx2 v[228:229], v242, s[28:31], s78 offen offset:0 nt\n"
    "v_mfma_f32_16x16x32_bf16 v[148:151], a[128:131], v[76:79], v[148:151]\n"
    "ds_read_b128 a[148:151], v248 offset:16384\n"
    "v_mfma_f32_16x16x32_bf16 v[144:147], a[120:123], v[76:79], v[144:147]\n"
    "buffer_load_dwordx2 v[230:231], v242, s[28:31], s78 offen offset:768 nt\n"
    "v_mfma_f32_16x16x32_bf16 v[144:147], a[124:127], v[80:83], v[144:147]\n"
    "ds_read_b128 a[152:155], v248 offset:5120\n"
    "v_mfma_f32_16x16x32_bf16 v[140:143], a[116:119], v[80:83], v[140:143]\n"
    "buffer_load_dwordx2 v[232:233], v242, s[28:31], s78 offen offset:3072 nt\n"
    "v_mfma_f32_16x16x32_bf16 v[140:143], a[112:115], v[76:79], v[140:143]\n"
    "ds_read_b128 a[156:159], v248 offset:17408\n"
    "v_mfma_f32_16x16x32_bf16 v[156:159], a[112:115], v[84:87], v[156:159]\n"
    "buffer_load_dwordx2 v[234:235], v242, s[28:31], s78 offen offset:3840 nt\n"
    "v_mfma_f32_16x16x32_bf16 v[156:159], a[116:119], v[88:91], v[156:159]\n"
    "ds_read_b128 v[104:107], v251 offset:0\n"
    "v_mfma_f32_16x16x32_bf16 v[160:163], a[124:127], v[88:91], v[160:163]\n"
    "v_exp_f32 v67, v67\n"
    "v_mfma_f32_16x16x32_bf16 v[160:163], a[120:123], v[84:87], v[160:163]\n"
    "v_cvt_pk_bf16_f32 v99, v66, v67\n"
    "v_mfma_f32_16x16x32_bf16 v[164:167], a[128:131], v[84:87], v[164:167]\n"
    "v_exp_f32 v4, v4\n"
    "v_mfma_f32_16x16x32_bf16 v[164:167], a[132:135], v[88:91], v[164:167]\n"
    "v_exp_f32 v5, v5\n"
    "v_mfma_f32_16x16x32_bf16 v[180:183], a[132:135], v[96:99], v[180:183]\n"
    "v_exp_f32 v6, v6\n"
    "v_mfma_f32_16x16x32_bf16 v[180:183], a[128:131], v[92:95], v[180:183]\n"
    "v_exp_f32 v7, v7\n"
    "v_mfma_f32_16x16x32_bf16 v[176:179], a[120:123], v[92:95], v[176:179]\n"
    "v_exp_f32 v8, v8\n"
    "v_mfma_f32_16x16x32_bf16 v[176:179], a[124:127], v[96:99], v[176:179]\n"
    "v_exp_f32 v9, v9\n"
    "v_mfma_f32_16x16x32_bf16 v[172:175], a[116:119], v[96:99], v[172:175]\n"
    "v_exp_f32 v10, v10\n"
    "v_mfma_f32_16x16x32_bf16 v[172:175], a[112:115], v[92:95], v[172:175]\n"
    "s_waitcnt lgkmcnt(0)\n"
    "v_exp_f32 v11, v11\n"
    "v_mfma_f32_16x16x32_bf16 v[136:139], a[136:139], v[68:71], v[136:139]\n"
    "ds_read_b128 a[112:115], v248 offset:6144\n"
    "v_mfma_f32_16x16x32_bf16 v[136:139], a[140:143], v[72:75], v[136:139]\n"
    "ds_read_b128 a[116:119], v248 offset:18432\n"
    "v_mfma_f32_16x16x32_bf16 v[188:191], a[148:151], v[72:75], v[188:191]\n"
    "ds_read_b128 a[120:123], v248 offset:7168\n"
    "v_mfma_f32_16x16x32_bf16 v[188:191], a[144:147], v[68:71], v[188:191]\n"
    "ds_read_b128 a[124:127], v248 offset:19456\n"
    "v_mfma_f32_16x16x32_bf16 v[192:195], a[152:155], v[68:71], v[192:195]\n"
    "ds_read_b128 a[128:131], v248 offset:8192\n"
    "v_mfma_f32_16x16x32_bf16 v[192:195], a[156:159], v[72:75], v[192:195]\n"
    "ds_read_b128 a[132:135], v248 offset:20480\n"
    "v_smfmac_f32_16x16x64_bf16 v[108:111], v[104:107], v[68:75], v253\n"
    "s_add_u32 s81, s81, 128\n"
    "s_cmp_gt_u32 s81, 4096\n"
    "s_cselect_b32 s81, 0, s81\n"
    "v_smfmac_f32_16x16x64_bf16 v[112:115], v[104:107], v[76:83], v253\n"
    "v_mfma_f32_16x16x32_bf16 a[4:7], a[156:159], v[80:83], a[4:7]\n"
    "v_exp_f32 v12, v12\n"
    "v_mfma_f32_16x16x32_bf16 a[4:7], a[152:155], v[76:79], a[4:7]\n"
    "v_exp_f32 v13, v13\n"
    "v_mfma_f32_16x16x32_bf16 a[0:3], a[144:147], v[76:79], a[0:3]\n"
    "v_exp_f32 v14, v14\n"
    "v_mfma_f32_16x16x32_bf16 a[0:3], a[148:151], v[80:83], a[0:3]\n"
    "v_exp_f32 v15, v15\n"
    "v_mfma_f32_16x16x32_bf16 v[152:155], a[140:143], v[80:83], v[152:155]\n"
    "v_exp_f32 v16, v16\n"
    "v_mfma_f32_16x16x32_bf16 v[152:155], a[136:139], v[76:79], v[152:155]\n"
    "v_mfma_f32_16x16x32_bf16 v[168:171], a[136:139], v[84:87], v[168:171]\n"
    "v_exp_f32 v17, v17\n"
    "v_mfma_f32_16x16x32_bf16 v[168:171], a[140:143], v[88:91], v[168:171]\n"
    "v_mfma_f32_16x16x32_bf16 a[16:19], a[148:151], v[88:91], a[16:19]\n"
    "v_mfma_f32_16x16x32_bf16 a[16:19], a[144:147], v[84:87], a[16:19]\n"
    "v_exp_f32 v18, v18\n"
    "v_mfma_f32_16x16x32_bf16 a[20:23], a[152:155], v[84:87], a[20:23]\n"
    "v_mfma_f32_16x16x32_bf16 a[20:23], a[156:159], v[88:91], a[20:23]\n"
    "v_smfmac_f32_16x16x64_bf16 v[116:119], v[104:107], v[84:91], v253\n"
    "v_smfmac_f32_16x16x64_bf16 v[120:123], v[104:107], v[92:99], v253\n"
    "v_mfma_f32_16x16x32_bf16 a[36:39], a[156:159], v[96:99], a[36:39]\n"
    "v_exp_f32 v19, v19\n"
    "v_mfma_f32_16x16x32_bf16 a[36:39], a[152:155], v[92:95], a[36:39]\n"
    "v_exp_f32 v20, v20\n"
    "v_mfma_f32_16x16x32_bf16 a[32:35], a[144:147], v[92:95], a[32:35]\n"
    "v_mfma_f32_16x16x32_bf16 a[32:35], a[148:151], v[96:99], a[32:35]\n"
    "v_exp_f32 v21, v21\n"
    "v_mfma_f32_16x16x32_bf16 v[184:187], a[140:143], v[96:99], v[184:187]\n"
    "v_mfma_f32_16x16x32_bf16 v[184:187], a[136:139], v[92:95], v[184:187]\n"
    "s_waitcnt lgkmcnt(0)\n"
    "v_mfma_f32_16x16x32_bf16 v[196:199], a[112:115], v[68:71], v[196:199]\n"
    "ds_read_b128 a[136:139], v248 offset:9216\n"
    "v_mfma_f32_16x16x32_bf16 v[196:199], a[116:119], v[72:75], v[196:199]\n"
    "ds_read_b128 a[140:143], v248 offset:21504\n"
    "v_mfma_f32_16x16x32_bf16 v[200:203], a[124:127], v[72:75], v[200:203]\n"
    "ds_read_b128 a[144:147], v248 offset:10240\n"
    "v_mfma_f32_16x16x32_bf16 v[200:203], a[120:123], v[68:71], v[200:203]\n"
    "ds_read_b128 a[148:151], v248 offset:22528\n"
    "v_mfma_f32_16x16x32_bf16 a[48:51], a[128:131], v[68:71], a[48:51]\n"
    "buffer_load_dwordx2 v[236:237], v241, s[36:39], s81 offen offset:0 sc1\n"
    "v_mfma_f32_16x16x32_bf16 a[48:51], a[132:135], v[72:75], a[48:51]\n"
    "ds_read_b128 a[152:155], v248 offset:11264\n"
    "v_mfma_f32_16x16x32_bf16 a[64:67], a[132:135], v[80:83], a[64:67]\n"
    "ds_read_b128 a[156:159], v248 offset:23552\n"
    "v_mfma_f32_16x16x32_bf16 a[64:67], a[128:131], v[76:79], a[64:67]\n"
    "v_exp_f32 v22, v22\n"
    "v_mfma_f32_16x16x32_bf16 a[12:15], a[120:123], v[76:79], a[12:15]\n"
    "v_exp_f32 v23, v23\n"
    "v_mfma_f32_16x16x32_bf16 a[12:15], a[124:127], v[80:83], a[12:15]\n"
    "v_exp_f32 v24, v24\n"
    "v_mfma_f32_16x16x32_bf16 a[8:11], a[116:119], v[80:83], a[8:11]\n"
    "v_exp_f32 v25, v25\n"
    "v_mfma_f32_16x16x32_bf16 a[8:11], a[112:115], v[76:79], a[8:11]\n"
    "v_exp_f32 v26, v26\n"
    "v_mfma_f32_16x16x32_bf16 a[24:27], a[112:115], v[84:87], a[24:27]\n"
    "v_mfma_f32_16x16x32_bf16 a[24:27], a[116:119], v[88:91], a[24:27]\n"
    "v_exp_f32 v27, v27\n"
    "v_mfma_f32_16x16x32_bf16 a[28:31], a[124:127], v[88:91], a[28:31]\n"
    "v_mfma_f32_16x16x32_bf16 a[28:31], a[120:123], v[84:87], a[28:31]\n"
    "v_mfma_f32_16x16x32_bf16 a[80:83], a[128:131], v[84:87], a[80:83]\n"
    "v_exp_f32 v28, v28\n"
    "v_mfma_f32_16x16x32_bf16 a[80:83], a[132:135], v[88:91], a[80:83]\n"
    "v_mfma_f32_16x16x32_bf16 a[96:99], a[132:135], v[96:99], a[96:99]\n"
    "v_mfma_f32_16x16x32_bf16 a[96:99], a[128:131], v[92:95], a[96:99]\n"
    "v_exp_f32 v29, v29\n"
    "v_mfma_f32_16x16x32_bf16 a[44:47], a[120:123], v[92:95], a[44:47]\n"
    "v_mfma_f32_16x16x32_bf16 a[44:47], a[124:127], v[96:99], a[44:47]\n"
    "v_exp_f32 v30, v30\n"
    "v_mfma_f32_16x16x32_bf16 a[40:43], a[116:119], v[96:99], a[40:43]\n"
    "v_mfma_f32_16x16x32_bf16 a[40:43], a[112:115], v[92:95], a[40:43]\n"
    "s_waitcnt lgkmcnt(0)\n"
    "v_mfma_f32_16x16x32_bf16 a[52:55], a[136:139], v[68:71], a[52:55]\n"
    "ds_read_b128 a[112:115], v3 offset:12288\n"
    "v_mfma_f32_16x16x32_bf16 a[52:55], a[140:143], v[72:75], a[52:55]\n"
    "ds_read_b128 a[116:119], v3 offset:14336\n"
    "v_mfma_f32_16x16x32_bf16 a[56:59], a[148:151], v[72:75], a[56:59]\n"
    "ds_read_b128 a[120:123], v3 offset:16384\n"
    "v_mfma_f32_16x16x32_bf16 a[56:59], a[144:147], v[68:71], a[56:59]\n"
    "ds_read_b128 a[124:127], v3 offset:18432\n"
    "v_mfma_f32_16x16x32_bf16 a[60:63], a[152:155], v[68:71], a[60:63]\n"
    "ds_read_b128 a[128:131], v3 offset:20480\n"
    "v_mfma_f32_16x16x32_bf16 a[60:63], a[156:159], v[72:75], a[60:63]\n"
    "ds_read_b128 a[132:135], v3 offset:22528\n"
    "v_mfma_f32_16x16x32_bf16 a[76:79], a[156:159], v[80:83], a[76:79]\n"
    "ds_read_b128 v[100:103], v250 offset:896\n"
    "v_mfma_f32_16x16x32_bf16 a[76:79], a[152:155], v[76:79], a[76:79]\n"
    "v_exp_f32 v31, v31\n"
    "v_mfma_f32_16x16x32_bf16 a[72:75], a[144:147], v[76:79], a[72:75]\n"
    "v_exp_f32 v32, v32\n"
    "v_mfma_f32_16x16x32_bf16 a[72:75], a[148:151], v[80:83], a[72:75]\n"
    "v_exp_f32 v33, v33\n"
    "v_mfma_f32_16x16x32_bf16 a[68:71], a[140:143], v[80:83], a[68:71]\n"
    "v_exp_f32 v34, v34\n"
    "v_mfma_f32_16x16x32_bf16 a[68:71], a[136:139], v[76:79], a[68:71]\n"
    "v_exp_f32 v35, v35\n"
    "v_mfma_f32_16x16x32_bf16 a[84:87], a[136:139], v[84:87], a[84:87]\n"
    "v_mfma_f32_16x16x32_bf16 a[84:87], a[140:143], v[88:91], a[84:87]\n"
    "v_mfma_f32_16x16x32_bf16 a[88:91], a[148:151], v[88:91], a[88:91]\n"
    "s_waitcnt vmcnt(18)\n"
    "ds_add_u32 v247, v246\n"
    "v_mfma_f32_16x16x32_bf16 a[88:91], a[144:147], v[84:87], a[88:91]\n"
    "v_mfma_f32_16x16x32_bf16 a[92:95], a[152:155], v[84:87], a[92:95]\n"
    "v_mfma_f32_16x16x32_bf16 a[92:95], a[156:159], v[88:91], a[92:95]\n"
    "v_mfma_f32_16x16x32_bf16 a[108:111], a[156:159], v[96:99], a[108:111]\n"
    "v_mfma_f32_16x16x32_bf16 a[108:111], a[152:155], v[92:95], a[108:111]\n"
    "v_mfma_f32_16x16x32_bf16 a[104:107], a[144:147], v[92:95], a[104:107]\n"
    "s_sub_u32 s74, s68, s65\n"
    "v_mfma_f32_16x16x32_bf16 a[104:107], a[148:151], v[96:99], a[104:107]\n"
    "s_add_u32 s66, s66, 1\n"
    "v_mov_b32 v2, v3\n"
    "v_add_u32_e64 v3, s74, v252\n"
    "v_mfma_f32_16x16x32_bf16 a[100:103], a[140:143], v[96:99], a[100:103]\n"
    "s_waitcnt lgkmcnt(1)\n"
    "s_cmp_eq_u32 s66, .Lend-.Lend2+(29)\n"
    "v_mfma_f32_16x16x32_bf16 a[100:103], a[136:139], v[92:95], a[100:103]\n"
    ".LwKe:\n"
    "s_cbranch_scc1 .LtailP\n"
    ".LNK:\n"
    "v_mfma_f32_16x16x32_bf16 v[36:39], a[112:115], a[192:195], v[100:103]\n"
    "ds_read_b128 a[136:139], v2 offset:13312\n"
    "v_cvt_pk_bf16_f32 v68, v4, v5\n"
    "v_mfma_f32_16x16x32_bf16 v[36:39], a[116:119], a[196:199], v[36:39]\n"
    "ds_read_b128 a[140:143], v2 offset:15360\n"
    "v_mfma_f32_16x16x32_bf16 v[36:39], a[120:123], a[200:203], v[36:39]\n"
    "ds_read_b128 a[144:147], v2 offset:17408\n"
    "v_mfma_f32_16x16x32_bf16 v[36:39], a[124:127], a[204:207], v[36:39]\n"
    "ds_read_b128 a[148:151], v2 offset:19456\n"
    "v_cvt_pk_bf16_f32 v69, v6, v7\n"
    "v_mfma_f32_16x16x32_bf16 v[36:39], a[128:131], a[208:211], v[36:39]\n"
    "ds_read_b128 a[152:155], v2 offset:21504\n"
    "v_mfma_f32_16x16x32_bf16 v[36:39], a[132:135], a[212:215], v[36:39]\n"
    "ds_read_b128 a[156:159], v2 offset:23552\n"
    "v_mfma_f32_16x16x32_bf16 v[40:43], a[132:135], a[236:239], v[100:103]\n"
    "ds_read_b128 v[104:107], v250 offset:912\n"
    "v_mfma_f32_16x16x32_bf16 v[40:43], a[128:131], a[232:235], v[40:43]\n"
    "s_add_u32 s68, s68, 24576\n"
    "v_cvt_pk_bf16_f32 v76, v8, v9\n"
    "v_mfma_f32_16x16x32_bf16 v[40:43], a[124:127], a[228:231], v[40:43]\n"
    "s_cmp_ge_u32 s68, s69\n"
    "s_cselect_b32 s68, s65, s68\n"
    "v_mfma_f32_16x16x32_bf16 v[40:43], a[120:123], a[224:227], v[40:43]\n"
    "v_exp_f32 v36, v36\n"
    "v_mfma_f32_16x16x32_bf16 v[40:43], a[116:119], a[220:223], v[40:43]\n"
    "v_exp_f32 v37, v37\n"
    "v_mfma_f32_16x16x32_bf16 v[40:43], a[112:115], a[216:219], v[40:43]\n"
    "v_cvt_pk_bf16_f32 v77, v10, v11\n"
    "v_cvt_pk_bf16_f32 v72, v36, v37\n"
    "v_mfma_f32_16x16x32_bf16 v[44:47], a[112:115], a[240:243], v[100:103]\n"
    "v_mfma_f32_16x16x32_bf16 v[44:47], a[116:119], a[244:247], v[44:47]\n"
    "v_exp_f32 v38, v38\n"
    "v_mfma_f32_16x16x32_bf16 v[44:47], a[120:123], a[248:251], v[44:47]\n"
    "v_mfma_f32_16x16x32_bf16 v[44:47], a[124:127], a[252:255], v[44:47]\n"
    "v_cvt_pk_bf16_f32 v84, v12, v13\n"
    "v_mfma_f32_16x16x32_bf16 v[44:47], a[128:131], a[160:163], v[44:47]\n"
    "v_exp_f32 v39, v39\n"
    "v_mfma_f32_16x16x32_bf16 v[44:47], a[132:135], a[164:167], v[44:47]\n"
    "v_cvt_pk_bf16_f32 v73, v38, v39\n"
    "v_mfma_f32_16x16x32_bf16 v[48:51], a[132:135], a[188:191], v[100:103]\n"
    "ds_read_b32 v245, v247\n"
    "v_cvt_pk_bf16_f32 v85, v14, v15\n"
    "v_mfma_f32_16x16x32_bf16 v[48:51], a[128:131], a[184:187], v[48:51]\n"
    "s_add_u32 m0, s68, 0\n"
    "v_exp_f32 v40, v40\n"
    "v_mfma_f32_16x16x32_bf16 v[48:51], a[124:127], a[180:183], v[48:51]\n"
    "v_mfma_f32_16x16x32_bf16 v[48:51], a[120:123], a[176:179], v[48:51]\n"
    "v_mfma_f32_16x16x32_bf16 v[48:51], a[116:119], a[172:175], v[48:51]\n"
    "s_waitcnt vmcnt(0)\n"
    "v_cmp_eq_u64 s[54:55], v[236:237], v[238:239]\n"
    "v_cvt_pk_bf16_f32 v92, v16, v17\n"
    "v_mfma_f32_16x16x32_bf16 v[48:51], a[112:115], a[168:171], v[48:51]\n"
    "s_waitcnt lgkmcnt(1)\n"
    "v_exp_f32 v41, v41\n"
    "v_mfma_f32_16x16x32_bf16 v[52:55], a[136:139], a[192:195], v[104:107]\n"
    "s_waitcnt lgkmcnt(0)\n"
    "v_cmp_le_u32 s[56:57], s59, v245\n"
    "v_cvt_pk_bf16_f32 v80, v40, v41\n"
    "v_mfma_f32_16x16x32_bf16 v[52:55], a[140:143], a[196:199], v[52:55]\n"
    "s_waitcnt vmcnt(1)\n"
    "v_exp_f32 v42, v42\n"
    "v_mfma_f32_16x16x32_bf16 v[52:55], a[144:147], a[200:203], v[52:55]\n"
    "s_and_b64 s[54:55], s[54:55], s[56:57]\n"
    "s_cmp_eq_u64 s[54:55], -1\n"
    "v_mfma_f32_16x16x32_bf16 v[52:55], a[148:151], a[204:207], v[52:55]\n"
    "s_cbranch_scc0 .Lpoll2\n"
    ".Lpoll2r:\n"
    "v_mfma_f32_16x16x32_bf16 v[52:55], a[152:155], a[208:211], v[52:55]\n"
    "buffer_load_dwordx4 v1, s[16:19], s67 offen offset:0 sc1 lds\n"
    "v_mfma_f32_16x16x32_bf16 v[52:55], a[156:159], a[212:215], v[52:55]\n"
    "s_add_u32 s76, s71, s60\n"
    "ds_read_b128 a[112:115], v3 offset:0\n"
    "v_cvt_pk_bf16_f32 v93, v18, v19\n"
    "v_mfma_f32_16x16x32_bf16 v[56:59], a[156:159], a[236:239], v[104:107]\n"
    "s_add_u32 s77, s71, s61\n"
    "ds_read_b128 a[116:119], v3 offset:2048\n"
    "v_cvt_pk_bf16_f32 v70, v20, v21\n"
    "v_mfma_f32_16x16x32_bf16 v[56:59], a[152:155], a[232:235], v[56:59]\n"
    "ds_read_b128 a[120:123], v3 offset:4096\n"
    "v_mfma_f32_16x16x32_bf16 v[56:59], a[148:151], a[228:231], v[56:59]\n"
    "buffer_load_dwordx4 v1, s[16:19], s67 offen offset:1024 sc1 lds\n"
    "v_mfma_f32_16x16x32_bf16 v[56:59], a[144:147], a[224:227], v[56:59]\n"
    "ds_read_b128 a[124:127], v3 offset:6144\n"
    "v_cvt_pk_bf16_f32 v71, v22, v23\n"
    "v_mfma_f32_16x16x32_bf16 v[56:59], a[140:143], a[220:223], v[56:59]\n"
    "ds_read_b128 a[128:131], v3 offset:8192\n"
    "v_mfma_f32_16x16x32_bf16 v[56:59], a[136:139], a[216:219], v[56:59]\n"
    "ds_read_b128 a[132:135], v3 offset:10240\n"
    "v_mfma_f32_16x16x32_bf16 v[60:63], a[136:139], a[240:243], v[104:107]\n"
    "buffer_load_dwordx4 v1, s[16:19], s67 offen offset:2048 sc1 lds\n"
    "v_mfma_f32_16x16x32_bf16 v[60:63], a[140:143], a[244:247], v[60:63]\n"
    "ds_read_b128 v[100:103], v250 offset:1536\n"
    "v_cvt_pk_bf16_f32 v78, v24, v25\n"
    "v_mfma_f32_16x16x32_bf16 v[60:63], a[144:147], a[248:251], v[60:63]\n"
    "v_exp_f32 v43, v43\n"
    "v_mfma_f32_16x16x32_bf16 v[60:63], a[148:151], a[252:255], v[60:63]\n"
    "ds_read_b128 v[36:39], v243 offset:0\n"
    "v_cvt_pk_bf16_f32 v81, v42, v43\n"
    "v_mfma_f32_16x16x32_bf16 v[60:63], a[152:155], a[160:163], v[60:63]\n"
    "buffer_load_dwordx4 v1, s[16:19], s67 offen offset:3072 sc1 lds\n"
    "v_mfma_f32_16x16x32_bf16 v[60:63], a[156:159], a[164:167], v[60:63]\n"
    "s_add_u32 m0, s68, 4096\n"
    "v_cvt_pk_bf16_f32 v79, v26, v27\n"
    "v_add_u32 v243, 512, v243\n"
    "v_mfma_f32_16x16x32_bf16 v[64:67], a[156:159], a[188:191], v[104:107]\n"
    "s_add_u32 s83, s67, 4096\n"
    "v_exp_f32 v44, v44\n"
    "v_mfma_f32_16x16x32_bf16 v[64:67], a[152:155], a[184:187], v[64:67]\n"
    "v_exp_f32 v45, v45\n"
    "v_mfma_f32_16x16x32_bf16 v[64:67], a[148:151], a[180:183], v[64:67]\n"
    "buffer_load_dwordx4 v1, s[16:19], s83 offen offset:0 sc1 lds\n"
    "v_mfma_f32_16x16x32_bf16 v[64:67], a[144:147], a[176:179], v[64:67]\n"
    "v_cvt_pk_bf16_f32 v86, v28, v29\n"
    "v_cvt_pk_bf16_f32 v88, v44, v45\n"
    "v_mfma_f32_16x16x32_bf16 v[64:67], a[140:143], a[172:175], v[64:67]\n"
    "v_exp_f32 v46, v46\n"
    "v_mfma_f32_16x16x32_bf16 v[64:67], a[136:139], a[168:171], v[64:67]\n"
    "s_waitcnt lgkmcnt(1)\n"
    "v_exp_f32 v47, v47\n"
    "v_mfma_f32_16x16x32_bf16 v[4:7], a[112:115], a[192:195], v[100:103]\n"
    "buffer_load_dwordx4 v1, s[16:19], s83 offen offset:1024 sc1 lds\n"
    "v_mfma_f32_16x16x32_bf16 v[4:7], a[116:119], a[196:199], v[4:7]\n"
    "s_add_u32 m0, s70, .Lend-.Lend2+(0)\n"
    "ds_read_b128 a[136:139], v3 offset:1024\n"
    "v_cvt_pk_bf16_f32 v87, v30, v31\n"
    "v_mfma_f32_16x16x32_bf16 v[4:7], a[120:123], a[200:203], v[4:7]\n"
    "s_add_u32 s83, s67, 24576\n"
    "ds_read_b128 a[140:143], v3 offset:3072\n"
    "v_cvt_pk_bf16_f32 v89, v46, v47\n"
    "v_mfma_f32_16x16x32_bf16 v[4:7], a[124:127], a[204:207], v[4:7]\n"
    "s_waitcnt lgkmcnt(2)\n"
    "ds_read_b128 a[144:147], v3 offset:5120\n"
    "v_fma_f32 v204, v204, v36, -v38\n"
    "v_mfma_f32_16x16x32_bf16 v[4:7], a[128:131], a[208:211], v[4:7]\n"
    "buffer_load_dwordx4 v1, s[16:19], s83 offen offset:0 sc1 lds\n"
    "v_mfma_f32_16x16x32_bf16 v[4:7], a[132:135], a[212:215], v[4:7]\n"
    "ds_read_b128 a[148:151], v3 offset:7168\n"
    "v_cvt_pk_bf16_f32 v94, v32, v33\n"
    "v_mfma_f32_16x16x32_bf16 v[8:11], a[132:135], a[236:239], v[100:103]\n"
    "ds_read_b128 a[152:155], v3 offset:9216\n"
    "v_fma_f32 v205, v205, v37, -v39\n"
    "v_mfma_f32_16x16x32_bf16 v[8:11], a[128:131], a[232:235], v[8:11]\n"
    "ds_read_b128 a[156:159], v3 offset:11264\n"
    "v_fma_f32 v206, v206, v36, -v38\n"
    "v_mfma_f32_16x16x32_bf16 v[8:11], a[124:127], a[228:231], v[8:11]\n"
    "buffer_load_dwordx4 v1, s[16:19], s83 offen offset:1024 sc1 lds\n"
    "v_mfma_f32_16x16x32_bf16 v[8:11], a[120:123], a[224:227], v[8:11]\n"
    "ds_read_b128 v[104:107], v250 offset:1552\n"
    "v_cvt_pk_bf16_f32 v95, v34, v35\n"
    "v_mfma_f32_16x16x32_bf16 v[8:11], a[116:119], a[220:223], v[8:11]\n"
    "v_exp_f32 v52, v52\n"
    "v_mfma_f32_16x16x32_bf16 v[8:11], a[112:115], a[216:219], v[8:11]\n"
    "v_exp_f32 v53, v53\n"
    "v_mfma_f32_16x16x32_bf16 v[12:15], a[112:115], a[240:243], v[100:103]\n"
    "buffer_load_dwordx4 v1, s[16:19], s83 offen offset:2048 sc1 lds\n"
    "v_mfma_f32_16x16x32_bf16 v[12:15], a[116:119], a[244:247], v[12:15]\n"
    "v_exp_f32 v54, v54\n"
    "v_mfma_f32_16x16x32_bf16 v[12:15], a[120:123], a[248:251], v[12:15]\n"
    "v_exp_f32 v55, v55\n"
    "v_mfma_f32_16x16x32_bf16 v[12:15], a[124:127], a[252:255], v[12:15]\n"
    "v_cvt_pk_bf16_f32 v74, v52, v53\n"
    "v_cvt_pk_bf16_f32 v75, v54, v55\n"
    "v_mfma_f32_16x16x32_bf16 v[12:15], a[128:131], a[160:163], v[12:15]\n"
    "buffer_load_dwordx4 v1, s[16:19], s83 offen offset:3072 sc1 lds\n"
    "v_mfma_f32_16x16x32_bf16 v[12:15], a[132:135], a[164:167], v[12:15]\n"
    "s_add_u32 m0, s70, 4096\n"
    "v_exp_f32 v56, v56\n"
    "v_mfma_f32_16x16x32_bf16 v[16:19], a[132:135], a[188:191], v[100:103]\n"
    "s_add_u32 s83, s67, 28672\n"
    "v_exp_f32 v48, v48\n"
    "v_mfma_f32_16x16x32_bf16 v[16:19], a[128:131], a[184:187], v[16:19]\n"
    "v_exp_f32 v49, v49\n"
    "v_mfma_f32_16x16x32_bf16 v[16:19], a[124:127], a[180:183], v[16:19]\n"
    "buffer_load_dwordx4 v1, s[16:19], s83 offen offset:0 sc1 lds\n"
    "v_mfma_f32_16x16x32_bf16 v[16:19], a[120:123], a[176:179], v[16:19]\n"
    "v_exp_f32 v57, v57\n"
    "v_mfma_f32_16x16x32_bf16 v[16:19], a[116:119], a[172:175], v[16:19]\n"
    "v_cvt_pk_bf16_f32 v82, v56, v57\n"
    "v_cvt_pk_bf16_f32 v96, v48, v49\n"
    "v_mfma_f32_16x16x32_bf16 v[16:19], a[112:115], a[168:171], v[16:19]\n"
    "s_waitcnt lgkmcnt(0)\n"
    "v_exp_f32 v58, v58\n"
    "v_mfma_f32_16x16x32_bf16 v[20:23], a[136:139], a[192:195], v[104:107]\n"
    "buffer_load_dwordx4 v1, s[16:19], s83 offen offset:1024 sc1 lds\n"
    "v_mfma_f32_16x16x32_bf16 v[20:23], a[140:143], a[196:199], v[20:23]\n"
    "s_add_u32 s67, s67, 49152\n"
    "ds_read_b128 a[112:115], v249 offset:0\n"
    "v_fma_f32 v207, v207, v37, -v39\n"
    "v_mfma_f32_16x16x32_bf16 v[20:23], a[144:147], a[200:203], v[20:23]\n"
    "s_add_u32 s59, s59, 4\n"
    "ds_read_b128 a[116:119], v249 offset:12288\n"
    "v_cvt_pk_bf16_f32 v204, v204, v205\n"
    "v_mfma_f32_16x16x32_bf16 v[20:23], a[148:151], a[204:207], v[20:23]\n"
    "ds_read_b128 a[120:123], v249 offset:1024\n"
    "v_cvt_pk_bf16_f32 v206, v206, v207\n"
    "v_mfma_f32_16x16x32_bf16 v[20:23], a[152:155], a[208:211], v[20:23]\n"
    "ds_read_b128 a[124:127], v249 offset:13312\n"
    "v_fma_f32 v208, v208, v36, -v38\n"
    "v_mfma_f32_16x16x32_bf16 v[20:23], a[156:159], a[212:215], v[20:23]\n"
    "ds_read_b128 a[128:131], v249 offset:2048\n"
    "v_fma_f32 v209, v209, v37, -v39\n"
    "v_mfma_f32_16x16x32_bf16 v[24:27], a[156:159], a[236:239], v[104:107]\n"
    "ds_read_b128 a[132:135], v249 offset:14336\n"
    "v_fma_f32 v210, v210, v36, -v38\n"
    "v_mfma_f32_16x16x32_bf16 v[24:27], a[152:155], a[232:235], v[24:27]\n"
    "v_exp_f32 v59, v59\n"
    "v_mfma_f32_16x16x32_bf16 v[24:27], a[148:151], a[228:231], v[24:27]\n"
    "v_cvt_pk_bf16_f32 v83, v58, v59\n"
    "v_fma_f32 v211, v211, v37, -v39\n"
    "v_mfma_f32_16x16x32_bf16 v[24:27], a[144:147], a[224:227], v[24:27]\n"
    "v_exp_f32 v60, v60\n"
    "v_mfma_f32_16x16x32_bf16 v[24:27], a[140:143], a[220:223], v[24:27]\n"
    "v_exp_f32 v61, v61\n"
    "v_mfma_f32_16x16x32_bf16 v[24:27], a[136:139], a[216:219], v[24:27]\n"
    "v_exp_f32 v62, v62\n"
    "v_mfma_f32_16x16x32_bf16 v[28:31], a[136:139], a[240:243], v[104:107]\n"
    "v_cvt_pk_bf16_f32 v90, v60, v61\n"
    "v_cvt_pk_bf16_f32 v208, v208, v209\n"
    "v_mfma_f32_16x16x32_bf16 v[28:31], a[140:143], a[244:247], v[28:31]\n"
    "v_exp_f32 v63, v63\n"
    "v_mfma_f32_16x16x32_bf16 v[28:31], a[144:147], a[248:251], v[28:31]\n"
    "v_cvt_pk_bf16_f32 v91, v62, v63\n"
    "v_cvt_pk_bf16_f32 v210, v210, v211\n"
    "v_mfma_f32_16x16x32_bf16 v[28:31], a[148:151], a[252:255], v[28:31]\n"
    "v_exp_f32 v50, v50\n"
    "v_mfma_f32_16x16x32_bf16 v[28:31], a[152:155], a[160:163], v[28:31]\n"
    "v_exp_f32 v51, v51\n"
    "v_mfma_f32_16x16x32_bf16 v[28:31], a[156:159], a[164:167], v[28:31]\n"
    "v_exp_f32 v64, v64\n"
    "v_mfma_f32_16x16x32_bf16 v[32:35], a[156:159], a[188:191], v[104:107]\n"
    "v_cvt_pk_bf16_f32 v97, v50, v51\n"
    "v_fma_f32 v212, v212, v36, -v38\n"
    "v_mfma_f32_16x16x32_bf16 v[32:35], a[152:155], a[184:187], v[32:35]\n"
    "v_exp_f32 v65, v65\n"
    "v_mfma_f32_16x16x32_bf16 v[32:35], a[148:151], a[180:183], v[32:35]\n"
    "v_cvt_pk_bf16_f32 v98, v64, v65\n"
    "v_fma_f32 v213, v213, v37, -v39\n"
    "v_mfma_f32_16x16x32_bf16 v[32:35], a[144:147], a[176:179], v[32:35]\n"
    "buffer_store_dword v204, v244, s[16:19], s76 offen offset:0 sc1\n"
    "v_mfma_f32_16x16x32_bf16 v[32:35], a[140:143], a[172:175], v[32:35]\n"
    "v_exp_f32 v66, v66\n"
    "v_mfma_f32_16x16x32_bf16 v[32:35], a[136:139], a[168:171], v[32:35]\n"
    "s_waitcnt lgkmcnt(0)\n"
    "v_fma_f32 v214, v214, v36, -v38\n"
    "v_fma_f32 v215, v215, v37, -v39\n"
    "v_mfma_f32_16x16x32_bf16 v[124:127], a[112:115], v[68:71], v[124:127]\n"
    "ds_read_b128 a[136:139], v249 offset:3072\n"
    "v_cvt_pk_bf16_f32 v212, v212, v213\n"
    "v_mfma_f32_16x16x32_bf16 v[124:127], a[116:119], v[72:75], v[124:127]\n"
    "ds_read_b128 a[140:143], v249 offset:15360\n"
    "v_cvt_pk_bf16_f32 v214, v214, v215\n"
    "v_mfma_f32_16x16x32_bf16 v[128:131], a[124:127], v[72:75], v[128:131]\n"
    "ds_read_b128 a[144:147], v249 offset:4096\n"
    "v_fma_f32 v216, v216, v36, -v38\n"
    "v_mfma_f32_16x16x32_bf16 v[128:131], a[120:123], v[68:71], v[128:131]\n"
    "ds_read_b128 a[148:151], v249 offset:16384\n"
    "v_fma_f32 v217, v217, v37, -v39\n"
    "v_mfma_f32_16x16x32_bf16 v[132:135], a[128:131], v[68:71], v[132:135]\n"
    "ds_read_b128 a[152:155], v249 offset:5120\n"
    "v_fma_f32 v218, v218, v36, -v38\n"
    "v_mfma_f32_16x16x32_bf16 v[132:135], a[132:135], v[72:75], v[132:135]\n"
    "ds_read_b128 a[156:159], v249 offset:17408\n"
    "v_fma_f32 v219, v219, v37, -v39\n"
    "v_mfma_f32_16x16x32_bf16 v[148:151], a[132:135], v[80:83], v[148:151]\n"
    "ds_read_b128 v[104:107], v251 offset:768\n"
    "v_cvt_pk_bf16_f32 v216, v216, v217\n"
    "v_mfma_f32_16x16x32_bf16 v[148:151], a[128:131], v[76:79], v[148:151]\n"
    "v_exp_f32 v67, v67\n"
    "v_mfma_f32_16x16x32_bf16 v[144:147], a[120:123], v[76:79], v[144:147]\n"
    "v_cvt_pk_bf16_f32 v99, v66, v67\n"
    "v_cvt_pk_bf16_f32 v218, v218, v219\n"
    "v_mfma_f32_16x16x32_bf16 v[144:147], a[124:127], v[80:83], v[144:147]\n"
    "buffer_store_dword v206, v244, s[16:19], s76 offen offset:128 sc1\n"
    "v_mfma_f32_16x16x32_bf16 v[140:143], a[116:119], v[80:83], v[140:143]\n"
    "v_exp_f32 v4, v4\n"
    "v_mfma_f32_16x16x32_bf16 v[140:143], a[112:115], v[76:79], v[140:143]\n"
    "buffer_store_dword v208, v244, s[16:19], s76 offen offset:1024 sc1\n"
    "v_mfma_f32_16x16x32_bf16 v[156:159], a[112:115], v[84:87], v[156:159]\n"
    "v_fma_f32 v220, v220, v36, -v38\n"
    "v_fma_f32 v221, v221, v37, -v39\n"
    "v_mfma_f32_16x16x32_bf16 v[156:159], a[116:119], v[88:91], v[156:159]\n"
    "buffer_store_dword v210, v244, s[16:19], s76 offen offset:1152 sc1\n"
    "v_mfma_f32_16x16x32_bf16 v[160:163], a[124:127], v[88:91], v[160:163]\n"
    "v_exp_f32 v5, v5\n"
    "v_mfma_f32_16x16x32_bf16 v[160:163], a[120:123], v[84:87], v[160:163]\n"
    "buffer_store_dword v212, v244, s[16:19], s76 offen offset:256 sc1\n"
    "v_mfma_f32_16x16x32_bf16 v[164:167], a[128:131], v[84:87], v[164:167]\n"
    "v_exp_f32 v6, v6\n"
    "v_mfma_f32_16x16x32_bf16 v[164:167], a[132:135], v[88:91], v[164:167]\n"
    "buffer_store_dword v214, v244, s[16:19], s76 offen offset:384 sc1\n"
    "v_mfma_f32_16x16x32_bf16 v[180:183], a[132:135], v[96:99], v[180:183]\n"
    "v_fma_f32 v222, v222, v36, -v38\n"
    "v_fma_f32 v223, v223, v37, -v39\n"
    "v_mfma_f32_16x16x32_bf16 v[180:183], a[128:131], v[92:95], v[180:183]\n"
    "v_exp_f32 v7, v7\n"
    "v_mfma_f32_16x16x32_bf16 v[176:179], a[120:123], v[92:95], v[176:179]\n"
    "v_exp_f32 v8, v8\n"
    "v_mfma_f32_16x16x32_bf16 v[176:179], a[124:127], v[96:99], v[176:179]\n"
    "v_cvt_pk_bf16_f32 v220, v220, v221\n"
    "v_cvt_pk_bf16_f32 v222, v222, v223\n"
    "v_mfma_f32_16x16x32_bf16 v[172:175], a[116:119], v[96:99], v[172:175]\n"
    "v_exp_f32 v9, v9\n"
    "v_mfma_f32_16x16x32_bf16 v[172:175], a[112:115], v[92:95], v[172:175]\n"
    "s_waitcnt lgkmcnt(0)\n"
    "v_exp_f32 v10, v10\n"
    "v_mfma_f32_16x16x32_bf16 v[136:139], a[136:139], v[68:71], v[136:139]\n"
    "ds_read_b128 a[112:115], v249 offset:6144\n"
    "v_fma_f32 v224, v224, v36, -v38\n"
    "v_mfma_f32_16x16x32_bf16 v[136:139], a[140:143], v[72:75], v[136:139]\n"
    "ds_read_b128 a[116:119], v249 offset:18432\n"
    "v_fma_f32 v225, v225, v37, -v39\n"
    "v_mfma_f32_16x16x32_bf16 v[188:191], a[148:151], v[72:75], v[188:191]\n"
    "ds_read_b128 a[120:123], v249 offset:7168\n"
    "v_fma_f32 v226, v226, v36, -v38\n"
    "v_mfma_f32_16x16x32_bf16 v[188:191], a[144:147], v[68:71], v[188:191]\n"
    "ds_read_b128 a[124:127], v249 offset:19456\n"
    "v_fma_f32 v227, v227, v37, -v39\n"
    "v_mfma_f32_16x16x32_bf16 v[192:195], a[152:155], v[68:71], v[192:195]\n"
    "ds_read_b128 a[128:131], v249 offset:8192\n"
    "v_cvt_pk_bf16_f32 v224, v224, v225\n"
    "v_mfma_f32_16x16x32_bf16 v[192:195], a[156:159], v[72:75], v[192:195]\n"
    "ds_read_b128 a[132:135], v249 offset:20480\n"
    "v_smfmac_f32_16x16x64_bf16 v[108:111], v[104:107], v[68:75], v253\n"
    "s_add_u32 s81, s81, 128\n"
    "s_cmp_gt_u32 s81, 4096\n"
    "s_cselect_b32 s81, 0, s81\n"
    "v_smfmac_f32_16x16x64_bf16 v[112:115], v[104:107], v[76:83], v253\n"
    "v_mfma_f32_16x16x32_bf16 a[4:7], a[156:159], v[80:83], a[4:7]\n"
    "v_exp_f32 v11, v11\n"
    "v_mfma_f32_16x16x32_bf16 a[4:7], a[152:155], v[76:79], a[4:7]\n"
    "v_exp_f32 v12, v12\n"
    "v_mfma_f32_16x16x32_bf16 a[0:3], a[144:147], v[76:79], a[0:3]\n"
    "v_exp_f32 v13, v13\n"
    "v_mfma_f32_16x16x32_bf16 a[0:3], a[148:151], v[80:83], a[0:3]\n"
    "v_exp_f32 v14, v14\n"
    "v_mfma_f32_16x16x32_bf16 v[152:155], a[140:143], v[80:83], v[152:155]\n"
    "v_exp_f32 v15, v15\n"
    "v_mfma_f32_16x16x32_bf16 v[152:155], a[136:139], v[76:79], v[152:155]\n"
    "v_cvt_pk_bf16_f32 v226, v226, v227\n"
    "v_fma_f32 v228, v228, v36, -v38\n"
    "v_mfma_f32_16x16x32_bf16 v[168:171], a[136:139], v[84:87], v[168:171]\n"
    "v_exp_f32 v16, v16\n"
    "v_mfma_f32_16x16x32_bf16 v[168:171], a[140:143], v[88:91], v[168:171]\n"
    "v_exp_f32 v17, v17\n"
    "v_mfma_f32_16x16x32_bf16 a[16:19], a[148:151], v[88:91], a[16:19]\n"
    "v_fma_f32 v229, v229, v37, -v39\n"
    "v_fma_f32 v230, v230, v36, -v38\n"
    "v_mfma_f32_16x16x32_bf16 a[16:19], a[144:147], v[84:87], a[16:19]\n"
    "v_exp_f32 v18, v18\n"
    "v_mfma_f32_16x16x32_bf16 a[20:23], a[152:155], v[84:87], a[20:23]\n"
    "v_fma_f32 v231, v231, v37, -v39\n"
    "v_mfma_f32_16x16x32_bf16 a[20:23], a[156:159], v[88:91], a[20:23]\n"
    "v_smfmac_f32_16x16x64_bf16 v[116:119], v[104:107], v[84:91], v253\n"
    "v_smfmac_f32_16x16x64_bf16 v[120:123], v[104:107], v[92:99], v253\n"
    "v_mfma_f32_16x16x32_bf16 a[36:39], a[156:159], v[96:99], a[36:39]\n"
    "v_exp_f32 v19, v19\n"
    "v_mfma_f32_16x16x32_bf16 a[36:39], a[152:155], v[92:95], a[36:39]\n"
    "v_exp_f32 v20, v20\n"
    "v_mfma_f32_16x16x32_bf16 a[32:35], a[144:147], v[92:95], a[32:35]\n"
    "v_cvt_pk_bf16_f32 v228, v228, v229\n"
    "v_cvt_pk_bf16_f32 v230, v230, v231\n"
    "v_mfma_f32_16x16x32_bf16 a[32:35], a[148:151], v[96:99], a[32:35]\n"
    "v_exp_f32 v21, v21\n"
    "v_mfma_f32_16x16x32_bf16 v[184:187], a[140:143], v[96:99], v[184:187]\n"
    "v_fma_f32 v232, v232, v36, -v38\n"
    "v_mfma_f32_16x16x32_bf16 v[184:187], a[136:139], v[92:95], v[184:187]\n"
    "s_waitcnt lgkmcnt(0)\n"
    "v_mfma_f32_16x16x32_bf16 v[196:199], a[112:115], v[68:71], v[196:199]\n"
    "ds_read_b128 a[136:139], v249 offset:9216\n"
    "v_fma_f32 v233, v233, v37, -v39\n"
    "v_mfma_f32_16x16x32_bf16 v[196:199], a[116:119], v[72:75], v[196:199]\n"
    "ds_read_b128 a[140:143], v249 offset:21504\n"
    "v_mfma_f32_16x16x32_bf16 v[200:203], a[124:127], v[72:75], v[200:203]\n"
    "ds_read_b128 a[144:147], v249 offset:10240\n"
    "v_mfma_f32_16x16x32_bf16 v[200:203], a[120:123], v[68:71], v[200:203]\n"
    "ds_read_b128 a[148:151], v249 offset:22528\n"
    "v_fma_f32 v234, v234, v36, -v38\n"
    "v_mfma_f32_16x16x32_bf16 a[48:51], a[128:131], v[68:71], a[48:51]\n"
    "buffer_load_dwordx2 v[236:237], v241, s[36:39], s81 offen offset:0 sc1\n"
    "v_mfma_f32_16x16x32_bf16 a[48:51], a[132:135], v[72:75], a[48:51]\n"
    "ds_read_b128 a[152:155], v249 offset:11264\n"
    "v_fma_f32 v235, v235, v37, -v39\n"
    "v_mfma_f32_16x16x32_bf16 a[64:67], a[132:135], v[80:83], a[64:67]\n"
    "ds_read_b128 a[156:159], v249 offset:23552\n"
    "v_mfma_f32_16x16x32_bf16 a[64:67], a[128:131], v[76:79], a[64:67]\n"
    "buffer_store_dword v216, v244, s[16:19], s76 offen offset:1280 sc1\n"
    "v_mfma_f32_16x16x32_bf16 a[12:15], a[120:123], v[76:79], a[12:15]\n"
    "v_exp_f32 v22, v22\n"
    "v_mfma_f32_16x16x32_bf16 a[12:15], a[124:127], v[80:83], a[12:15]\n"
    "buffer_store_dword v218, v244, s[16:19], s76 offen offset:1408 sc1\n"
    "v_mfma_f32_16x16x32_bf16 a[8:11], a[116:119], v[80:83], a[8:11]\n"
    "v_exp_f32 v23, v23\n"
    "v_mfma_f32_16x16x32_bf16 a[8:11], a[112:115], v[76:79], a[8:11]\n"
    "buffer_store_dword v220, v244, s[16:19], s77 offen offset:0 sc1\n"
    "v_mfma_f32_16x16x32_bf16 a[24:27], a[112:115], v[84:87], a[24:27]\n"
    "v_exp_f32 v24, v24\n"
    "v_mfma_f32_16x16x32_bf16 a[24:27], a[116:119], v[88:91], a[24:27]\n"
    "buffer_store_dword v222, v244, s[16:19], s77 offen offset:128 sc1\n"
    "v_mfma_f32_16x16x32_bf16 a[28:31], a[124:127], v[88:91], a[28:31]\n"
    "v_exp_f32 v25, v25\n"
    "v_mfma_f32_16x16x32_bf16 a[28:31], a[120:123], v[84:87], a[28:31]\n"
    "buffer_store_dword v224, v244, s[16:19], s77 offen offset:1024 sc1\n"
    "v_mfma_f32_16x16x32_bf16 a[80:83], a[128:131], v[84:87], a[80:83]\n"
    "v_exp_f32 v26, v26\n"
    "v_mfma_f32_16x16x32_bf16 a[80:83], a[132:135], v[88:91], a[80:83]\n"
    "buffer_store_dword v226, v244, s[16:19], s77 offen offset:1152 sc1\n"
    "v_mfma_f32_16x16x32_bf16 a[96:99], a[132:135], v[96:99], a[96:99]\n"
    "v_cvt_pk_bf16_f32 v232, v232, v233\n"
    "v_cvt_pk_bf16_f32 v234, v234, v235\n"
    "v_mfma_f32_16x16x32_bf16 a[96:99], a[128:131], v[92:95], a[96:99]\n"
    "buffer_store_dword v228, v244, s[16:19], s77 offen offset:256 sc1\n"
    "v_mfma_f32_16x16x32_bf16 a[44:47], a[120:123], v[92:95], a[44:47]\n"
    "v_exp_f32 v27, v27\n"
    "v_mfma_f32_16x16x32_bf16 a[44:47], a[124:127], v[96:99], a[44:47]\n"
    "buffer_store_dword v230, v244, s[16:19], s77 offen offset:384 sc1\n"
    "v_mfma_f32_16x16x32_bf16 a[40:43], a[116:119], v[96:99], a[40:43]\n"
    "v_exp_f32 v28, v28\n"
    "v_mfma_f32_16x16x32_bf16 a[40:43], a[112:115], v[92:95], a[40:43]\n"
    "s_waitcnt lgkmcnt(0)\n"
    "v_exp_f32 v29, v29\n"
    "v_mfma_f32_16x16x32_bf16 a[52:55], a[136:139], v[68:71], a[52:55]\n"
    "ds_read_b128 a[112:115], v3 offset:12288\n"
    "v_mfma_f32_16x16x32_bf16 a[52:55], a[140:143], v[72:75], a[52:55]\n"
    "ds_read_b128 a[116:119], v3 offset:14336\n"
    "v_mfma_f32_16x16x32_bf16 a[56:59], a[148:151], v[72:75], a[56:59]\n"
    "ds_read_b128 a[120:123], v3 offset:16384\n"
    "v_mfma_f32_16x16x32_bf16 a[56:59], a[144:147], v[68:71], a[56:59]\n"
    "ds_read_b128 a[124:127], v3 offset:18432\n"
    "v_mfma_f32_16x16x32_bf16 a[60:63], a[152:155], v[68:71], a[60:63]\n"
    "ds_read_b128 a[128:131], v3 offset:20480\n"
    "v_mfma_f32_16x16x32_bf16 a[60:63], a[156:159], v[72:75], a[60:63]\n"
    "ds_read_b128 a[132:135], v3 offset:22528\n"
    "v_mfma_f32_16x16x32_bf16 a[76:79], a[156:159], v[80:83], a[76:79]\n"
    "ds_read_b128 v[100:103], v250 offset:1664\n"
    "v_mfma_f32_16x16x32_bf16 a[76:79], a[152:155], v[76:79], a[76:79]\n"
    "buffer_store_dword v232, v244, s[16:19], s77 offen offset:1280 sc1\n"
    "v_mfma_f32_16x16x32_bf16 a[72:75], a[144:147], v[76:79], a[72:75]\n"
    "v_exp_f32 v30, v30\n"
    "v_mfma_f32_16x16x32_bf16 a[72:75], a[148:151], v[80:83], a[72:75]\n"
    "buffer_store_dword v234, v244, s[16:19], s77 offen offset:1408 sc1\n"
    "v_mfma_f32_16x16x32_bf16 a[68:71], a[140:143], v[80:83], a[68:71]\n"
    "v_exp_f32 v31, v31\n"
    "v_mfma_f32_16x16x32_bf16 a[68:71], a[136:139], v[76:79], a[68:71]\n"
    "v_exp_f32 v32, v32\n"
    "v_mfma_f32_16x16x32_bf16 a[84:87], a[136:139], v[84:87], a[84:87]\n"
    "v_exp_f32 v33, v33\n"
    "v_mfma_f32_16x16x32_bf16 a[84:87], a[140:143], v[88:91], a[84:87]\n"
    "v_exp_f32 v34, v34\n"
    "v_mfma_f32_16x16x32_bf16 a[88:91], a[148:151], v[88:91], a[88:91]\n"
    "s_waitcnt vmcnt(17)\n"
    "ds_add_u32 v247, v246\n"
    "v_mfma_f32_16x16x32_bf16 a[88:91], a[144:147], v[84:87], a[88:91]\n"
    "v_exp_f32 v35, v35\n"
    "v_mfma_f32_16x16x32_bf16 a[92:95], a[152:155], v[84:87], a[92:95]\n"
    "v_mfma_f32_16x16x32_bf16 a[92:95], a[156:159], v[88:91], a[92:95]\n"
    "v_mfma_f32_16x16x32_bf16 a[108:111], a[156:159], v[96:99], a[108:111]\n"
    "v_mfma_f32_16x16x32_bf16 a[108:111], a[152:155], v[92:95], a[108:111]\n"
    "v_mfma_f32_16x16x32_bf16 a[104:107], a[144:147], v[92:95], a[104:107]\n"
    "s_sub_u32 s74, s68, s65\n"
    "v_add_u32 v250, 1536, v250\n"
    "v_add_u32 v251, 1536, v251\n"
    "v_mfma_f32_16x16x32_bf16 a[104:107], a[148:151], v[96:99], a[104:107]\n"
    "s_add_u32 s66, s66, 1\n"
    "v_mov_b32 v2, v3\n"
    "v_add_u32 v3, s74, v252\n"
    "v_mfma_f32_16x16x32_bf16 a[100:103], a[140:143], v[96:99], a[100:103]\n"
    "s_waitcnt lgkmcnt(1)\n"
    "s_cmp_lt_u32 s66, 29\n"
    "v_mfma_f32_16x16x32_bf16 a[100:103], a[136:139], v[92:95], a[100:103]\n"
    "s_cbranch_scc1 .LPK\n"
    "s_branch .Ltail\n"
    "s_nop 0\n"
    "s_nop 0\n"
    ".LpollAK:\n"
    "s_sleep 1\n"
    "buffer_load_dwordx2 v[236:237], v87, s[36:39], 0 offen offset:0 sc1\n"
    "s_waitcnt vmcnt(0)\n"
    "v_cmp_eq_u64 s[54:55], v[236:237], v[238:239]\n"
    "s_add_u32 s58, s58, 1\n"
    "s_cmp_eq_u64 s[54:55], -1\n"
    "s_cbranch_scc1 .LpollAKr\n"
    "s_cmp_lt_u32 s58, 0x80000\n"
    "s_cbranch_scc1 .LpollAK\n"
    "s_branch .LpollAKr\n"
    "s_nop 0\n"
    "s_nop 0\n"
    ".LpollAV:\n"
    "s_sleep 1\n"
    "buffer_load_dwordx2 v[236:237], v87, s[36:39], 0 offen offset:0 sc1\n"
    "s_waitcnt vmcnt(0)\n"
    "v_cmp_eq_u64 s[54:55], v[236:237], v[238:239]\n"
    "s_add_u32 s58, s58, 1\n"
    "s_cmp_eq_u64 s[54:55], -1\n"
    "s_cbranch_scc1 .LpollAVr\n"
    "s_cmp_lt_u32 s58, 0x80000\n"
    "s_cbranch_scc1 .LpollAV\n"
    "s_branch .LpollAVr\n"
    "s_nop 0\n"
    "s_nop 0\n"
    "s_nop 0\n"
    "s_nop 0\n"
    "s_nop 0\n"
    "s_nop 0\n"
    "s_nop 0\n"
    ".Lpoll1:\n"
    "s_sleep 1\n"
    "v_cmp_eq_u64 s[54:55], v[236:237], v[238:239]\n"
    "s_nop 1\n"
    "s_cmp_eq_u64 s[54:55], -1\n"
    "s_cbranch_scc1 .Lpoll1l\n"
    "buffer_load_dwordx2 v[236:237], v241, s[36:39], s81 offen offset:0 sc1\n"
    "s_waitcnt vmcnt(0)\n"
    "v_cmp_eq_u64 s[54:55], v[236:237], v[238:239]\n"
    ".Lpoll1l:\n"
    "ds_read_b32 v245, v247\n"
    "s_waitcnt lgkmcnt(0)\n"
    "v_cmp_le_u32 s[56:57], s59, v245\n"
    "s_add_u32 s58, s58, 1\n"
    "s_nop 1\n"
    "s_and_b64 s[54:55], s[54:55], s[56:57]\n"
    "s_cmp_eq_u64 s[54:55], -1\n"
    "s_cbranch_scc1 .Lpoll1r\n"
    "s_cmp_lt_u32 s58, 0x80000\n"
    "s_cbranch_scc1 .Lpoll1\n"
    "s_branch .Lpoll1r\n"
    "s_nop 0\n"
    "s_nop 0\n"
    "s_nop 0\n"
    "s_nop 0\n"
    "s_nop 0\n"
    "s_nop 0\n"
    "s_nop 0\n"
    ".Lpoll2:\n"
    "s_sleep 1\n"
    "v_cmp_eq_u64 s[54:55], v[236:237], v[238:239]\n"
    "s_nop 1\n"
    "s_cmp_eq_u64 s[54:55], -1\n"
    "s_cbranch_scc1 .Lpoll2l\n"
    "buffer_load_dwordx2 v[236:237], v241, s[36:39], s81 offen offset:0 sc1\n"
    "s_waitcnt vmcnt(0)\n"
    "v_cmp_eq_u64 s[54:55], v[236:237], v[238:239]\n"
    ".Lpoll2l:\n"
    "ds_read_b32 v245, v247\n"
    "s_waitcnt lgkmcnt(0)\n"
    "v_cmp_le_u32 s[56:57], s59, v245\n"
    "s_add_u32 s58, s58, 1\n"
    "s_nop 1\n"
    "s_and_b64 s[54:55], s[54:55], s[56:57]\n"
    "s_cmp_eq_u64 s[54:55], -1\n"
    "s_cbranch_scc1 .Lpoll2r\n"
    "s_cmp_lt_u32 s58, 0x80000\n"
    "s_cbranch_scc1 .Lpoll2\n"
    "s_branch .Lpoll2r\n"
    "s_nop 0\n"
    "s_nop 0\n"
    "s_nop 0\n"
    "s_nop 0\n"
    "s_nop 0\n"
    "s_nop 0\n"
    "s_nop 0\n"
    ".Lpoll3:\n"
    "s_sleep 1\n"
    "v_cmp_eq_u64 s[54:55], v[236:237], v[238:239]\n"
    "s_nop 1\n"
    "s_cmp_eq_u64 s[54:55], -1\n"
    "s_cbranch_scc1 .Lpoll3l\n"
    "buffer_load_dwordx2 v[236:237], v241, s[36:39], s81 offen offset:0 sc1\n"
    "s_waitcnt vmcnt(0)\n"
    "v_cmp_eq_u64 s[54:55], v[236:237], v[238:239]\n"
    ".Lpoll3l:\n"
    "ds_read_b32 v245, v247\n"
    "s_waitcnt lgkmcnt(0)\n"
    "v_cmp_le_u32 s[56:57], s59, v245\n"
    "s_add_u32 s58, s58, 1\n"
    "s_nop 1\n"
    "s_and_b64 s[54:55], s[54:55], s[56:57]\n"
    "s_cmp_eq_u64 s[54:55], -1\n"
    "s_cbranch_scc1 .Lpoll3r\n"
    "s_cmp_lt_u32 s58, 0x80000\n"
    "s_cbranch_scc1 .Lpoll3\n"
    "s_branch .Lpoll3r\n"
    "s_nop 0\n"
    "s_nop 0\n"
    "s_nop 0\n"
    "s_nop 0\n"
    "s_nop 0\n"
    "s_nop 0\n"
    "s_nop 0\n"
    ".Lpoll4:\n"
    "s_sleep 1\n"
    "v_cmp_eq_u64 s[54:55], v[236:237], v[238:239]\n"
    "s_nop 1\n"
    "s_cmp_eq_u64 s[54:55], -1\n"
    "s_cbranch_scc1 .Lpoll4l\n"
    "buffer_load_dwordx2 v[236:237], v241, s[36:39], s81 offen offset:0 sc1\n"
    "s_waitcnt vmcnt(0)\n"
    "v_cmp_eq_u64 s[54:55], v[236:237], v[238:239]\n"
    ".Lpoll4l:\n"
    "ds_read_b32 v245, v247\n"
    "s_waitcnt lgkmcnt(0)\n"
    "v_cmp_le_u32 s[56:57], s59, v245\n"
    "s_add_u32 s58, s58, 1\n"
    "s_nop 1\n"
    "s_and_b64 s[54:55], s[54:55], s[56:57]\n"
    "s_cmp_eq_u64 s[54:55], -1\n"
    "s_cbranch_scc1 .Lpoll4r\n"
    "s_cmp_lt_u32 s58, 0x80000\n"
    "s_cbranch_scc1 .Lpoll4\n"
    "s_branch .Lpoll4r\n"
    "s_nop 0\n"
    "s_nop 0\n"
    "s_nop 0\n"
    "s_nop 0\n"
    "s_nop 0\n"
    "s_nop 0\n"
    "s_nop 0\n"
    ".Lpt0:\n"
    "s_sleep 1\n"
    "v_cmp_eq_u64 s[54:55], v[236:237], v[238:239]\n"
    "s_nop 1\n"
    "s_cmp_eq_u64 s[54:55], -1\n"
    "s_cbranch_scc1 .Lpt0l\n"
    "buffer_load_dwordx2 v[236:237], v241, s[36:39], s81 offen offset:0 sc1\n"
    "s_waitcnt vmcnt(0)\n"
    "v_cmp_eq_u64 s[54:55], v[236:237], v[238:239]\n"
    ".Lpt0l:\n"
    "ds_read_b32 v245, v247\n"
    "s_waitcnt lgkmcnt(0)\n"
    "v_cmp_le_u32 s[56:57], s59, v245\n"
    "s_add_u32 s58, s58, 1\n"
    "s_nop 1\n"
    "s_and_b64 s[54:55], s[54:55], s[56:57]\n"
    "s_cmp_eq_u64 s[54:55], -1\n"
    "s_cbranch_scc1 .Lpt0r\n"
    "s_cmp_lt_u32 s58, 0x80000\n"
    "s_cbranch_scc1 .Lpt0\n"
    "s_branch .Lpt0r\n"
    "s_nop 0\n"
    "s_nop 0\n"
    "s_nop 0\n"
    ".Lpt1:\n"
    "s_sleep 1\n"
    "ds_read_b32 v245, v247\n"
    "s_waitcnt lgkmcnt(0)\n"
    "v_cmp_le_u32 s[56:57], s59, v245\n"
    "s_add_u32 s58, s58, 1\n"
    "s_nop 1\n"
    "s_cmp_eq_u64 s[56:57], -1\n"
    "s_cbranch_scc1 .Lpt1r\n"
    "s_cmp_lt_u32 s58, 0x80000\n"
    "s_cbranch_scc1 .Lpt1\n"
    "s_branch .Lpt1r\n"
    "s_nop 0\n"
    "s_nop 0\n"
    ".Lpt4:\n"
    "s_sleep 1\n"
    "ds_read_b32 v245, v247\n"
    "s_waitcnt lgkmcnt(0)\n"
    "v_cmp_le_u32 s[56:57], s59, v245\n"
    "s_add_u32 s58, s58, 1\n"
    "s_nop 1\n"
    "s_cmp_eq_u64 s[56:57], -1\n"
    "s_cbranch_scc1 .Lpt4r\n"
    "s_cmp_lt_u32 s58, 0x80000\n"
    "s_cbranch_scc1 .Lpt4\n"
    "s_branch .Lpt4r\n"
    "s_nop 0\n"
    "s_nop 0\n"
    ".Lpt3:\n"
    "s_sleep 1\n"
    "ds_read_b32 v245, v247\n"
    "s_waitcnt lgkmcnt(0)\n"
    "v_cmp_le_u32 s[56:57], s59, v245\n"
    "s_add_u32 s58, s58, 1\n"
    "s_nop 1\n"
    "s_cmp_eq_u64 s[56:57], -1\n"
    "s_cbranch_scc1 .Lpt3r\n"
    "s_cmp_lt_u32 s58, 0x80000\n"
    "s_cbranch_scc1 .Lpt3\n"
    "s_branch .Lpt3r\n"
    ".fill ((0x20000 - (.LwVe - .LwV) - (. - _Z8attn_fwdPKtPKiS2_S0_S0_S0_S0_PcPt)) & 4095) / 4, 4, 0xBF800000\n"
    ".LwV:\n"
    "s_branch .LwVe\n"
    ".LPV:\n"
    "v_mfma_f32_16x16x32_bf16 v[36:39], a[112:115], a[192:195], v[100:103]\n"
    "s_add_u32 s71, s71, s44\n"
    "ds_read_b128 a[136:139], v2 offset:13312\n"
    "v_cvt_pk_bf16_f32 v68, v4, v5\n"
    "v_mfma_f32_16x16x32_bf16 v[36:39], a[116:119], a[196:199], v[36:39]\n"
    "s_add_u32 s75, s71, s63\n"
    "ds_read_b128 a[140:143], v2 offset:15360\n"
    "v_cvt_pk_bf16_f32 v69, v6, v7\n"
    "v_mfma_f32_16x16x32_bf16 v[36:39], a[120:123], a[200:203], v[36:39]\n"
    "ds_read_b128 a[144:147], v2 offset:17408\n"
    "v_cvt_pk_bf16_f32 v76, v8, v9\n"
    "v_mfma_f32_16x16x32_bf16 v[36:39], a[124:127], a[204:207], v[36:39]\n"
    "ds_read_b128 a[148:151], v2 offset:19456\n"
    "v_cvt_pk_bf16_f32 v77, v10, v11\n"
    "v_mfma_f32_16x16x32_bf16 v[36:39], a[128:131], a[208:211], v[36:39]\n"
    "ds_read_b128 a[152:155], v2 offset:21504\n"
    "v_cvt_pk_bf16_f32 v84, v12, v13\n"
    "v_mfma_f32_16x16x32_bf16 v[36:39], a[132:135], a[212:215], v[36:39]\n"
    "ds_read_b128 a[156:159], v2 offset:23552\n"
    "v_cvt_pk_bf16_f32 v85, v14, v15\n"
    "v_mfma_f32_16x16x32_bf16 v[40:43], a[132:135], a[236:239], v[100:103]\n"
    "ds_read_b128 v[104:107], v250 offset:144\n"
    "v_cvt_pk_bf16_f32 v92, v16, v17\n"
    "v_mfma_f32_16x16x32_bf16 v[40:43], a[128:131], a[232:235], v[40:43]\n"
    "s_add_u32 s68, s68, 24576\n"
    "v_cvt_pk_bf16_f32 v93, v18, v19\n"
    "v_cvt_pk_bf16_f32 v70, v20, v21\n"
    "v_mfma_f32_16x16x32_bf16 v[40:43], a[124:127], a[228:231], v[40:43]\n"
    "s_cmp_ge_u32 s68, s69\n"
    "s_cselect_b32 s68, s65, s68\n"
    "v_cvt_pk_bf16_f32 v71, v22, v23\n"
    "v_mfma_f32_16x16x32_bf16 v[40:43], a[120:123], a[224:227], v[40:43]\n"
    "v_cvt_pk_bf16_f32 v78, v24, v25\n"
    "v_cvt_pk_bf16_f32 v79, v26, v27\n"
    "v_mfma_f32_16x16x32_bf16 v[40:43], a[116:119], a[220:223], v[40:43]\n"
    "v_cvt_pk_bf16_f32 v86, v28, v29\n"
    "v_cvt_pk_bf16_f32 v87, v30, v31\n"
    "v_mfma_f32_16x16x32_bf16 v[40:43], a[112:115], a[216:219], v[40:43]\n"
    "v_cvt_pk_bf16_f32 v94, v32, v33\n"
    "v_cvt_pk_bf16_f32 v95, v34, v35\n"
    "v_mfma_f32_16x16x32_bf16 v[44:47], a[112:115], a[240:243], v[100:103]\n"
    "v_exp_f32 v36, v36\n"
    "v_mfma_f32_16x16x32_bf16 v[44:47], a[116:119], a[244:247], v[44:47]\n"
    "v_exp_f32 v37, v37\n"
    "v_mfma_f32_16x16x32_bf16 v[44:47], a[120:123], a[248:251], v[44:47]\n"
    "v_exp_f32 v38, v38\n"
    "v_mfma_f32_16x16x32_bf16 v[44:47], a[124:127], a[252:255], v[44:47]\n"
    "v_exp_f32 v39, v39\n"
    "v_mfma_f32_16x16x32_bf16 v[44:47], a[128:131], a[160:163], v[44:47]\n"
    "v_cvt_pk_bf16_f32 v72, v36, v37\n"
    "v_cvt_pk_bf16_f32 v73, v38, v39\n"
    "v_mfma_f32_16x16x32_bf16 v[44:47], a[132:135], a[164:167], v[44:47]\n"
    "v_exp_f32 v40, v40\n"
    "v_mfma_f32_16x16x32_bf16 v[48:51], a[132:135], a[188:191], v[100:103]\n"
    "ds_read_b32 v245, v247\n"
    "v_mfma_f32_16x16x32_bf16 v[48:51], a[128:131], a[184:187], v[48:51]\n"
    "s_add_u32 m0, s68, 0\n"
    "v_exp_f32 v41, v41\n"
    "v_mfma_f32_16x16x32_bf16 v[48:51], a[124:127], a[180:183], v[48:51]\n"
    "v_exp_f32 v42, v42\n"
    "v_mfma_f32_16x16x32_bf16 v[48:51], a[120:123], a[176:179], v[48:51]\n"
    "v_exp_f32 v43, v43\n"
    "v_mfma_f32_16x16x32_bf16 v[48:51], a[116:119], a[172:175], v[48:51]\n"
    "s_waitcnt vmcnt(13)\n"
    "v_cmp_eq_u64 s[54:55], v[236:237], v[238:239]\n"
    "v_cvt_pk_bf16_f32 v80, v40, v41\n"
    "v_mfma_f32_16x16x32_bf16 v[48:51], a[112:115], a[168:171], v[48:51]\n"
    "s_waitcnt lgkmcnt(1)\n"
    "v_cvt_pk_bf16_f32 v81, v42, v43\n"
    "v_mfma_f32_16x16x32_bf16 v[52:55], a[136:139], a[192:195], v[104:107]\n"
    "s_waitcnt lgkmcnt(0)\n"
    "v_cmp_le_u32 s[56:57], s59, v245\n"
    "v_mfma_f32_16x16x32_bf16 v[52:55], a[140:143], a[196:199], v[52:55]\n"
    "v_exp_f32 v44, v44\n"
    "v_mfma_f32_16x16x32_bf16 v[52:55], a[144:147], a[200:203], v[52:55]\n"
    "s_and_b64 s[54:55], s[54:55], s[56:57]\n"
    "s_cmp_eq_u64 s[54:55], -1\n"
    "v_mfma_f32_16x16x32_bf16 v[52:55], a[148:151], a[204:207], v[52:55]\n"
    "s_cbranch_scc0 .Lpoll3\n"
    ".Lpoll3r:\n"
    "v_mfma_f32_16x16x32_bf16 v[52:55], a[152:155], a[208:211], v[52:55]\n"
    "buffer_load_dwordx4 v1, s[16:19], s67 offen offset:0 sc1 lds\n"
    "v_mfma_f32_16x16x32_bf16 v[52:55], a[156:159], a[212:215], v[52:55]\n"
    "ds_read_b128 a[112:115], v3 offset:0\n"
    "v_mfma_f32_16x16x32_bf16 v[56:59], a[156:159], a[236:239], v[104:107]\n"
    "ds_read_b128 a[116:119], v3 offset:2048\n"
    "v_mfma_f32_16x16x32_bf16 v[56:59], a[152:155], a[232:235], v[56:59]\n"
    "ds_read_b128 a[120:123], v3 offset:4096\n"
    "v_mfma_f32_16x16x32_bf16 v[56:59], a[148:151], a[228:231], v[56:59]\n"
    "buffer_load_dwordx4 v1, s[16:19], s67 offen offset:1024 sc1 lds\n"
    "v_mfma_f32_16x16x32_bf16 v[56:59], a[144:147], a[224:227], v[56:59]\n"
    "ds_read_b128 a[124:127], v3 offset:6144\n"
    "v_mfma_f32_16x16x32_bf16 v[56:59], a[140:143], a[220:223], v[56:59]\n"
    "ds_read_b128 a[128:131], v3 offset:8192\n"
    "v_mfma_f32_16x16x32_bf16 v[56:59], a[136:139], a[216:219], v[56:59]\n"
    "ds_read_b128 a[132:135], v3 offset:10240\n"
    "v_mfma_f32_16x16x32_bf16 v[60:63], a[136:139], a[240:243], v[104:107]\n"
    "buffer_load_dwordx4 v1, s[16:19], s67 offen offset:2048 sc1 lds\n"
    "v_mfma_f32_16x16x32_bf16 v[60:63], a[140:143], a[244:247], v[60:63]\n"
    "ds_read_b128 v[100:103], v250 offset:768\n"
    "v_mfma_f32_16x16x32_bf16 v[60:63], a[144:147], a[248:251], v[60:63]\n"
    "v_exp_f32 v52, v52\n"
    "v_mfma_f32_16x16x32_bf16 v[60:63], a[148:151], a[252:255], v[60:63]\n"
    "v_exp_f32 v53, v53\n"
    "v_mfma_f32_16x16x32_bf16 v[60:63], a[152:155], a[160:163], v[60:63]\n"
    "buffer_load_dwordx4 v1, s[16:19], s67 offen offset:3072 sc1 lds\n"
    "v_mfma_f32_16x16x32_bf16 v[60:63], a[156:159], a[164:167], v[60:63]\n"
    "s_add_u32 m0, s68, 4096\n"
    "v_exp_f32 v54, v54\n"
    "v_mfma_f32_16x16x32_bf16 v[64:67], a[156:159], a[188:191], v[104:107]\n"
    "s_add_u32 s83, s67, 4096\n"
    "v_exp_f32 v55, v55\n"
    "v_mfma_f32_16x16x32_bf16 v[64:67], a[152:155], a[184:187], v[64:67]\n"
    "v_cvt_pk_bf16_f32 v74, v52, v53\n"
    "v_cvt_pk_bf16_f32 v75, v54, v55\n"
    "v_mfma_f32_16x16x32_bf16 v[64:67], a[148:151], a[180:183], v[64:67]\n"
    "buffer_load_dwordx4 v1, s[16:19], s83 offen offset:0 sc1 lds\n"
    "v_mfma_f32_16x16x32_bf16 v[64:67], a[144:147], a[176:179], v[64:67]\n"
    "v_exp_f32 v56, v56\n"
    "v_mfma_f32_16x16x32_bf16 v[64:67], a[140:143], a[172:175], v[64:67]\n"
    "v_exp_f32 v57, v57\n"
    "v_mfma_f32_16x16x32_bf16 v[64:67], a[136:139], a[168:171], v[64:67]\n"
    "s_waitcnt lgkmcnt(0)\n"
    "v_exp_f32 v58, v58\n"
    "v_mfma_f32_16x16x32_bf16 v[4:7], a[112:115], a[192:195], v[100:103]\n"
    "buffer_load_dwordx4 v1, s[16:19], s83 offen offset:1024 sc1 lds\n"
    "v_mfma_f32_16x16x32_bf16 v[4:7], a[116:119], a[196:199], v[4:7]\n"
    "s_add_u32 m0, s64, 0\n"
    "ds_read_b128 a[136:139], v3 offset:1024\n"
    "v_cvt_pk_bf16_f32 v82, v56, v57\n"
    "v_mfma_f32_16x16x32_bf16 v[4:7], a[120:123], a[200:203], v[4:7]\n"
    "s_add_u32 s83, s67, 24576\n"
    "ds_read_b128 a[140:143], v3 offset:3072\n"
    "v_mfma_f32_16x16x32_bf16 v[4:7], a[124:127], a[204:207], v[4:7]\n"
    "ds_read_b128 a[144:147], v3 offset:5120\n"
    "v_mfma_f32_16x16x32_bf16 v[4:7], a[128:131], a[208:211], v[4:7]\n"
    "buffer_load_dwordx4 v1, s[16:19], s83 offen offset:0 sc1 lds\n"
    "v_mfma_f32_16x16x32_bf16 v[4:7], a[132:135], a[212:215], v[4:7]\n"
    "ds_read_b128 a[148:151], v3 offset:7168\n"
    "v_mfma_f32_16x16x32_bf16 v[8:11], a[132:135], a[236:239], v[100:103]\n"
    "ds_read_b128 a[152:155], v3 offset:9216\n"
    "v_mfma_f32_16x16x32_bf16 v[8:11], a[128:131], a[232:235], v[8:11]\n"
    "ds_read_b128 a[156:159], v3 offset:11264\n"
    "v_mfma_f32_16x16x32_bf16 v[8:11], a[124:127], a[228:231], v[8:11]\n"
    "buffer_load_dwordx4 v1, s[16:19], s83 offen offset:1024 sc1 lds\n"
    "v_mfma_f32_16x16x32_bf16 v[8:11], a[120:123], a[224:227], v[8:11]\n"
    "ds_read_b128 v[104:107], v250 offset:784\n"
    "v_mfma_f32_16x16x32_bf16 v[8:11], a[116:119], a[220:223], v[8:11]\n"
    "v_exp_f32 v59, v59\n"
    "v_mfma_f32_16x16x32_bf16 v[8:11], a[112:115], a[216:219], v[8:11]\n"
    "v_cvt_pk_bf16_f32 v83, v58, v59\n"
    "v_mfma_f32_16x16x32_bf16 v[12:15], a[112:115], a[240:243], v[100:103]\n"
    "buffer_load_dwordx4 v1, s[16:19], s83 offen offset:2048 sc1 lds\n"
    "v_mfma_f32_16x16x32_bf16 v[12:15], a[116:119], a[244:247], v[12:15]\n"
    "v_exp_f32 v45, v45\n"
    "v_mfma_f32_16x16x32_bf16 v[12:15], a[120:123], a[248:251], v[12:15]\n"
    "v_exp_f32 v46, v46\n"
    "v_mfma_f32_16x16x32_bf16 v[12:15], a[124:127], a[252:255], v[12:15]\n"
    "v_exp_f32 v47, v47\n"
    "v_mfma_f32_16x16x32_bf16 v[12:15], a[128:131], a[160:163], v[12:15]\n"
    "buffer_load_dwordx4 v1, s[16:19], s83 offen offset:3072 sc1 lds\n"
    "v_mfma_f32_16x16x32_bf16 v[12:15], a[132:135], a[164:167], v[12:15]\n"
    "s_add_u32 m0, s64, 4096\n"
    "v_exp_f32 v60, v60\n"
    "v_mfma_f32_16x16x32_bf16 v[16:19], a[132:135], a[188:191], v[100:103]\n"
    "s_add_u32 s83, s67, 28672\n"
    "v_exp_f32 v61, v61\n"
    "v_mfma_f32_16x16x32_bf16 v[16:19], a[128:131], a[184:187], v[16:19]\n"
    "v_exp_f32 v62, v62\n"
    "v_mfma_f32_16x16x32_bf16 v[16:19], a[124:127], a[180:183], v[16:19]\n"
    "buffer_load_dwordx4 v1, s[16:19], s83 offen offset:0 sc1 lds\n"
    "v_mfma_f32_16x16x32_bf16 v[16:19], a[120:123], a[176:179], v[16:19]\n"
    "v_exp_f32 v63, v63\n"
    "v_mfma_f32_16x16x32_bf16 v[16:19], a[116:119], a[172:175], v[16:19]\n"
    "v_cvt_pk_bf16_f32 v88, v44, v45\n"
    "v_cvt_pk_bf16_f32 v89, v46, v47\n"
    "v_mfma_f32_16x16x32_bf16 v[16:19], a[112:115], a[168:171], v[16:19]\n"
    "s_waitcnt lgkmcnt(0)\n"
    "v_cvt_pk_bf16_f32 v90, v60, v61\n"
    "v_cvt_pk_bf16_f32 v91, v62, v63\n"
    "v_mfma_f32_16x16x32_bf16 v[20:23], a[136:139], a[192:195], v[104:107]\n"
    "buffer_load_dwordx4 v1, s[16:19], s83 offen offset:1024 sc1 lds\n"
    "v_mfma_f32_16x16x32_bf16 v[20:23], a[140:143], a[196:199], v[20:23]\n"
    "s_add_u32 s67, s67, 49152\n"
    "ds_read_b128 a[112:115], v248 offset:0\n"
    "v_mfma_f32_16x16x32_bf16 v[20:23], a[144:147], a[200:203], v[20:23]\n"
    "s_add_u32 s59, s59, 4\n"
    "ds_read_b128 a[116:119], v248 offset:12288\n"
    "v_mfma_f32_16x16x32_bf16 v[20:23], a[148:151], a[204:207], v[20:23]\n"
    "s_waitcnt vmcnt(12)\n"
    "ds_read_b128 a[120:123], v248 offset:1024\n"
    "v_mfma_f32_16x16x32_bf16 v[20:23], a[152:155], a[208:211], v[20:23]\n"
    "buffer_store_dwordx2 v[238:239], v240, s[36:39], s80 offen offset:0 sc1\n"
    "v_mfma_f32_16x16x32_bf16 v[20:23], a[156:159], a[212:215], v[20:23]\n"
    "buffer_load_dwordx4 v[204:207], v242, s[40:43], s75 offen offset:0 nt\n"
    "v_mfma_f32_16x16x32_bf16 v[24:27], a[156:159], a[236:239], v[104:107]\n"
    "s_add_u32 s80, s80, 256\n"
    "ds_read_b128 a[124:127], v248 offset:13312\n"
    "v_mfma_f32_16x16x32_bf16 v[24:27], a[152:155], a[232:235], v[24:27]\n"
    "buffer_load_dwordx4 v[208:211], v242, s[40:43], s75 offen offset:768 nt\n"
    "v_mfma_f32_16x16x32_bf16 v[24:27], a[148:151], a[228:231], v[24:27]\n"
    "ds_read_b128 a[128:131], v248 offset:2048\n"
    "v_mfma_f32_16x16x32_bf16 v[24:27], a[144:147], a[224:227], v[24:27]\n"
    "buffer_load_dwordx4 v[212:215], v242, s[40:43], s75 offen offset:256 nt\n"
    "v_mfma_f32_16x16x32_bf16 v[24:27], a[140:143], a[220:223], v[24:27]\n"
    "ds_read_b128 a[132:135], v248 offset:14336\n"
    "v_mfma_f32_16x16x32_bf16 v[24:27], a[136:139], a[216:219], v[24:27]\n"
    "buffer_load_dwordx4 v[216:219], v242, s[40:43], s75 offen offset:1024 nt\n"
    "v_mfma_f32_16x16x32_bf16 v[28:31], a[136:139], a[240:243], v[104:107]\n"
    "v_exp_f32 v48, v48\n"
    "v_mfma_f32_16x16x32_bf16 v[28:31], a[140:143], a[244:247], v[28:31]\n"
    "buffer_load_dwordx4 v[220:223], v242, s[40:43], s75 offen offset:512 nt\n"
    "v_mfma_f32_16x16x32_bf16 v[28:31], a[144:147], a[248:251], v[28:31]\n"
    "v_exp_f32 v49, v49\n"
    "v_mfma_f32_16x16x32_bf16 v[28:31], a[148:151], a[252:255], v[28:31]\n"
    "buffer_load_dwordx4 v[224:227], v242, s[40:43], s75 offen offset:1280 nt\n"
    "v_mfma_f32_16x16x32_bf16 v[28:31], a[152:155], a[160:163], v[28:31]\n"
    "v_exp_f32 v50, v50\n"
    "v_mfma_f32_16x16x32_bf16 v[28:31], a[156:159], a[164:167], v[28:31]\n"
    "v_exp_f32 v51, v51\n"
    "v_mfma_f32_16x16x32_bf16 v[32:35], a[156:159], a[188:191], v[104:107]\n"
    "v_exp_f32 v64, v64\n"
    "v_mfma_f32_16x16x32_bf16 v[32:35], a[152:155], a[184:187], v[32:35]\n"
    "v_exp_f32 v65, v65\n"
    "v_mfma_f32_16x16x32_bf16 v[32:35], a[148:151], a[180:183], v[32:35]\n"
    "v_exp_f32 v66, v66\n"
    "v_mfma_f32_16x16x32_bf16 v[32:35], a[144:147], a[176:179], v[32:35]\n"
    "v_exp_f32 v67, v67\n"
    "v_mfma_f32_16x16x32_bf16 v[32:35], a[140:143], a[172:175], v[32:35]\n"
    "v_cvt_pk_bf16_f32 v96, v48, v49\n"
    "v_cvt_pk_bf16_f32 v97, v50, v51\n"
    "v_mfma_f32_16x16x32_bf16 v[32:35], a[136:139], a[168:171], v[32:35]\n"
    "s_waitcnt lgkmcnt(0)\n"
    "v_cvt_pk_bf16_f32 v98, v64, v65\n"
    "v_cvt_pk_bf16_f32 v99, v66, v67\n"
    "v_mfma_f32_16x16x32_bf16 v[124:127], a[112:115], v[68:71], v[124:127]\n"
    "ds_read_b128 a[136:139], v248 offset:3072\n"
    "v_mfma_f32_16x16x32_bf16 v[124:127], a[116:119], v[72:75], v[124:127]\n"
    "ds_read_b128 a[140:143], v248 offset:15360\n"
    "v_mfma_f32_16x16x32_bf16 v[128:131], a[124:127], v[72:75], v[128:131]\n"
    "ds_read_b128 a[144:147], v248 offset:4096\n"
    "v_mfma_f32_16x16x32_bf16 v[128:131], a[120:123], v[68:71], v[128:131]\n"
    "ds_read_b128 a[148:151], v248 offset:16384\n"
    "v_mfma_f32_16x16x32_bf16 v[132:135], a[128:131], v[68:71], v[132:135]\n"
    "ds_read_b128 a[152:155], v248 offset:5120\n"
    "v_mfma_f32_16x16x32_bf16 v[132:135], a[132:135], v[72:75], v[132:135]\n"
    "ds_read_b128 a[156:159], v248 offset:17408\n"
    "v_mfma_f32_16x16x32_bf16 v[148:151], a[132:135], v[80:83], v[148:151]\n"
    "ds_read_b128 v[104:107], v251 offset:0\n"
    "v_mfma_f32_16x16x32_bf16 v[148:151], a[128:131], v[76:79], v[148:151]\n"
    "v_exp_f32 v4, v4\n"
    "v_mfma_f32_16x16x32_bf16 v[144:147], a[120:123], v[76:79], v[144:147]\n"
    "v_exp_f32 v5, v5\n"
    "v_mfma_f32_16x16x32_bf16 v[144:147], a[124:127], v[80:83], v[144:147]\n"
    "v_exp_f32 v6, v6\n"
    "v_mfma_f32_16x16x32_bf16 v[140:143], a[116:119], v[80:83], v[140:143]\n"
    "v_mfma_f32_16x16x32_bf16 v[140:143], a[112:115], v[76:79], v[140:143]\n"
    "v_mfma_f32_16x16x32_bf16 v[156:159], a[112:115], v[84:87], v[156:159]\n"
    "v_exp_f32 v7, v7\n"
    "v_mfma_f32_16x16x32_bf16 v[156:159], a[116:119], v[88:91], v[156:159]\n"
    "v_mfma_f32_16x16x32_bf16 v[160:163], a[124:127], v[88:91], v[160:163]\n"
    "v_exp_f32 v8, v8\n"
    "v_mfma_f32_16x16x32_bf16 v[160:163], a[120:123], v[84:87], v[160:163]\n"
    "v_mfma_f32_16x16x32_bf16 v[164:167], a[128:131], v[84:87], v[164:167]\n"
    "v_mfma_f32_16x16x32_bf16 v[164:167], a[132:135], v[88:91], v[164:167]\n"
    "v_exp_f32 v9, v9\n"
    "v_mfma_f32_16x16x32_bf16 v[180:183], a[132:135], v[96:99], v[180:183]\n"
    "v_mfma_f32_16x16x32_bf16 v[180:183], a[128:131], v[92:95], v[180:183]\n"
    "v_mfma_f32_16x16x32_bf16 v[176:179], a[120:123], v[92:95], v[176:179]\n"
    "v_exp_f32 v10, v10\n"
    "v_mfma_f32_16x16x32_bf16 v[176:179], a[124:127], v[96:99], v[176:179]\n"
    "v_mfma_f32_16x16x32_bf16 v[172:175], a[116:119], v[96:99], v[172:175]\n"
    "v_exp_f32 v11, v11\n"
    "v_mfma_f32_16x16x32_bf16 v[172:175], a[112:115], v[92:95], v[172:175]\n"
    "s_waitcnt lgkmcnt(0)\n"
    "v_mfma_f32_16x16x32_bf16 v[136:139], a[136:139], v[68:71], v[136:139]\n"
    "ds_read_b128 a[112:115], v248 offset:6144\n"
    "v_mfma_f32_16x16x32_bf16 v[136:139], a[140:143], v[72:75], v[136:139]\n"
    "ds_read_b128 a[116:119], v248 offset:18432\n"
    "v_mfma_f32_16x16x32_bf16 v[188:191], a[148:151], v[72:75], v[188:191]\n"
    "ds_read_b128 a[120:123], v248 offset:7168\n"
    "v_mfma_f32_16x16x32_bf16 v[188:191], a[144:147], v[68:71], v[188:191]\n"
    "ds_read_b128 a[124:127], v248 offset:19456\n"
    "v_mfma_f32_16x16x32_bf16 v[192:195], a[152:155], v[68:71], v[192:195]\n"
    "ds_read_b128 a[128:131], v248 offset:8192\n"
    "v_mfma_f32_16x16x32_bf16 v[192:195], a[156:159], v[72:75], v[192:195]\n"
    "ds_read_b128 a[132:135], v248 offset:20480\n"
    "v_smfmac_f32_16x16x64_bf16 v[108:111], v[104:107], v[68:75], v253\n"
    "s_add_u32 s81, s81, 128\n"
    "s_cmp_gt_u32 s81, 4096\n"
    "s_cselect_b32 s81, 0, s81\n"
    "v_smfmac_f32_16x16x64_bf16 v[112:115], v[104:107], v[76:83], v253\n"
    "v_mfma_f32_16x16x32_bf16 a[4:7], a[156:159], v[80:83], a[4:7]\n"
    "v_exp_f32 v12, v12\n"
    "v_mfma_f32_16x16x32_bf16 a[4:7], a[152:155], v[76:79], a[4:7]\n"
    "v_exp_f32 v13, v13\n"
    "v_mfma_f32_16x16x32_bf16 a[0:3], a[144:147], v[76:79], a[0:3]\n"
    "v_exp_f32 v14, v14\n"
    "v_mfma_f32_16x16x32_bf16 a[0:3], a[148:151], v[80:83], a[0:3]\n"
    "v_exp_f32 v15, v15\n"
    "v_mfma_f32_16x16x32_bf16 v[152:155], a[140:143], v[80:83], v[152:155]\n"
    "v_exp_f32 v16, v16\n"
    "v_mfma_f32_16x16x32_bf16 v[152:155], a[136:139], v[76:79], v[152:155]\n"
    "v_mfma_f32_16x16x32_bf16 v[168:171], a[136:139], v[84:87], v[168:171]\n"
    "v_exp_f32 v17, v17\n"
    "v_mfma_f32_16x16x32_bf16 v[168:171], a[140:143], v[88:91], v[168:171]\n"
    "v_mfma_f32_16x16x32_bf16 a[16:19], a[148:151], v[88:91], a[16:19]\n"
    "v_mfma_f32_16x16x32_bf16 a[16:19], a[144:147], v[84:87], a[16:19]\n"
    "v_exp_f32 v18, v18\n"
    "v_mfma_f32_16x16x32_bf16 a[20:23], a[152:155], v[84:87], a[20:23]\n"
    "v_mfma_f32_16x16x32_bf16 a[20:23], a[156:159], v[88:91], a[20:23]\n"
    "v_smfmac_f32_16x16x64_bf16 v[116:119], v[104:107], v[84:91], v253\n"
    "s_add_u32 s52, s52, s45\n"
    "v_smfmac_f32_16x16x64_bf16 v[120:123], v[104:107], v[92:99], v253\n"
    "v_mfma_f32_16x16x32_bf16 a[36:39], a[156:159], v[96:99], a[36:39]\n"
    "v_exp_f32 v19, v19\n"
    "v_mfma_f32_16x16x32_bf16 a[36:39], a[152:155], v[92:95], a[36:39]\n"
    "v_exp_f32 v20, v20\n"
    "v_mfma_f32_16x16x32_bf16 a[32:35], a[144:147], v[92:95], a[32:35]\n"
    "v_mfma_f32_16x16x32_bf16 a[32:35], a[148:151], v[96:99], a[32:35]\n"
    "v_exp_f32 v21, v21\n"
    "v_mfma_f32_16x16x32_bf16 v[184:187], a[140:143], v[96:99], v[184:187]\n"
    "v_mfma_f32_16x16x32_bf16 v[184:187], a[136:139], v[92:95], v[184:187]\n"
    "s_waitcnt lgkmcnt(0)\n"
    "v_mfma_f32_16x16x32_bf16 v[196:199], a[112:115], v[68:71], v[196:199]\n"
    "ds_read_b128 a[136:139], v248 offset:9216\n"
    "v_mfma_f32_16x16x32_bf16 v[196:199], a[116:119], v[72:75], v[196:199]\n"
    "ds_read_b128 a[140:143], v248 offset:21504\n"
    "v_mfma_f32_16x16x32_bf16 v[200:203], a[124:127], v[72:75], v[200:203]\n"
    "ds_read_b128 a[144:147], v248 offset:10240\n"
    "v_mfma_f32_16x16x32_bf16 v[200:203], a[120:123], v[68:71], v[200:203]\n"
    "ds_read_b128 a[148:151], v248 offset:22528\n"
    "v_mfma_f32_16x16x32_bf16 a[48:51], a[128:131], v[68:71], a[48:51]\n"
    "buffer_load_dwordx2 v[236:237], v241, s[36:39], s81 offen offset:0 sc1\n"
    "v_mfma_f32_16x16x32_bf16 a[48:51], a[132:135], v[72:75], a[48:51]\n"
    "ds_read_b128 a[152:155], v248 offset:11264\n"
    "v_mfma_f32_16x16x32_bf16 a[64:67], a[132:135], v[80:83], a[64:67]\n"
    "ds_read_b128 a[156:159], v248 offset:23552\n"
    "v_mfma_f32_16x16x32_bf16 a[64:67], a[128:131], v[76:79], a[64:67]\n"
    "v_exp_f32 v22, v22\n"
    "v_mfma_f32_16x16x32_bf16 a[12:15], a[120:123], v[76:79], a[12:15]\n"
    "v_exp_f32 v23, v23\n"
    "v_mfma_f32_16x16x32_bf16 a[12:15], a[124:127], v[80:83], a[12:15]\n"
    "v_exp_f32 v24, v24\n"
    "v_mfma_f32_16x16x32_bf16 a[8:11], a[116:119], v[80:83], a[8:11]\n"
    "v_exp_f32 v25, v25\n"
    "v_mfma_f32_16x16x32_bf16 a[8:11], a[112:115], v[76:79], a[8:11]\n"
    "v_exp_f32 v26, v26\n"
    "v_mfma_f32_16x16x32_bf16 a[24:27], a[112:115], v[84:87], a[24:27]\n"
    "v_mfma_f32_16x16x32_bf16 a[24:27], a[116:119], v[88:91], a[24:27]\n"
    "v_exp_f32 v27, v27\n"
    "v_mfma_f32_16x16x32_bf16 a[28:31], a[124:127], v[88:91], a[28:31]\n"
    "v_mfma_f32_16x16x32_bf16 a[28:31], a[120:123], v[84:87], a[28:31]\n"
    "v_mfma_f32_16x16x32_bf16 a[80:83], a[128:131], v[84:87], a[80:83]\n"
    "v_exp_f32 v28, v28\n"
    "v_mfma_f32_16x16x32_bf16 a[80:83], a[132:135], v[88:91], a[80:83]\n"
    "v_mfma_f32_16x16x32_bf16 a[96:99], a[132:135], v[96:99], a[96:99]\n"
    "v_mfma_f32_16x16x32_bf16 a[96:99], a[128:131], v[92:95], a[96:99]\n"
    "v_exp_f32 v29, v29\n"
    "v_mfma_f32_16x16x32_bf16 a[44:47], a[120:123], v[92:95], a[44:47]\n"
    "v_mfma_f32_16x16x32_bf16 a[44:47], a[124:127], v[96:99], a[44:47]\n"
    "v_exp_f32 v30, v30\n"
    "v_mfma_f32_16x16x32_bf16 a[40:43], a[116:119], v[96:99], a[40:43]\n"
    "v_mfma_f32_16x16x32_bf16 a[40:43], a[112:115], v[92:95], a[40:43]\n"
    "s_waitcnt lgkmcnt(0)\n"
    "v_mfma_f32_16x16x32_bf16 a[52:55], a[136:139], v[68:71], a[52:55]\n"
    "ds_read_b128 a[112:115], v3 offset:12288\n"
    "v_mfma_f32_16x16x32_bf16 a[52:55], a[140:143], v[72:75], a[52:55]\n"
    "ds_read_b128 a[116:119], v3 offset:14336\n"
    "v_mfma_f32_16x16x32_bf16 a[56:59], a[148:151], v[72:75], a[56:59]\n"
    "ds_read_b128 a[120:123], v3 offset:16384\n"
    "v_mfma_f32_16x16x32_bf16 a[56:59], a[144:147], v[68:71], a[56:59]\n"
    "ds_read_b128 a[124:127], v3 offset:18432\n"
    "v_mfma_f32_16x16x32_bf16 a[60:63], a[152:155], v[68:71], a[60:63]\n"
    "ds_read_b128 a[128:131], v3 offset:20480\n"
    "v_mfma_f32_16x16x32_bf16 a[60:63], a[156:159], v[72:75], a[60:63]\n"
    "ds_read_b128 a[132:135], v3 offset:22528\n"
    "v_mfma_f32_16x16x32_bf16 a[76:79], a[156:159], v[80:83], a[76:79]\n"
    "ds_read_b128 v[100:103], v250 offset:896\n"
    "v_mfma_f32_16x16x32_bf16 a[76:79], a[152:155], v[76:79], a[76:79]\n"
    "v_exp_f32 v31, v31\n"
    "v_mfma_f32_16x16x32_bf16 a[72:75], a[144:147], v[76:79], a[72:75]\n"
    "v_exp_f32 v32, v32\n"
    "v_mfma_f32_16x16x32_bf16 a[72:75], a[148:151], v[80:83], a[72:75]\n"
    "v_exp_f32 v33, v33\n"
    "v_mfma_f32_16x16x32_bf16 a[68:71], a[140:143], v[80:83], a[68:71]\n"
    "v_exp_f32 v34, v34\n"
    "v_mfma_f32_16x16x32_bf16 a[68:71], a[136:139], v[76:79], a[68:71]\n"
    "v_exp_f32_e64 v35, v35\n"
    "v_mfma_f32_16x16x32_bf16 a[84:87], a[136:139], v[84:87], a[84:87]\n"
    "v_mfma_f32_16x16x32_bf16 a[84:87], a[140:143], v[88:91], a[84:87]\n"
    "v_mfma_f32_16x16x32_bf16 a[88:91], a[148:151], v[88:91], a[88:91]\n"
    "s_waitcnt vmcnt(8)\n"
    "ds_add_u32 v247, v246\n"
    "v_mfma_f32_16x16x32_bf16 a[88:91], a[144:147], v[84:87], a[88:91]\n"
    "v_mfma_f32_16x16x32_bf16 a[92:95], a[152:155], v[84:87], a[92:95]\n"
    "v_mfma_f32_16x16x32_bf16 a[92:95], a[156:159], v[88:91], a[92:95]\n"
    "v_mfma_f32_16x16x32_bf16 a[108:111], a[156:159], v[96:99], a[108:111]\n"
    "v_mfma_f32_16x16x32_bf16 a[108:111], a[152:155], v[92:95], a[108:111]\n"
    "v_mfma_f32_16x16x32_bf16 a[104:107], a[144:147], v[92:95], a[104:107]\n"
    "s_sub_u32 s74, s68, s65\n"
    "v_mfma_f32_16x16x32_bf16 a[104:107], a[148:151], v[96:99], a[104:107]\n"
    "s_add_u32 s66, s66, .Lend-.Lend2+(1)\n"
    "v_mov_b32_e64 v2, v3\n"
    "v_add_u32_e64 v3, s74, v252\n"
    "v_mfma_f32_16x16x32_bf16 a[100:103], a[140:143], v[96:99], a[100:103]\n"
    "s_waitcnt lgkmcnt(1)\n"
    "s_cmp_eq_u32 s66, .Lend-.Lend2+(29)\n"
    "v_mfma_f32_16x16x32_bf16 a[100:103], a[136:139], v[92:95], a[100:103]\n"
    ".LwVe:\n"
    "s_cbranch_scc1 .LtailP\n"
    ".LNV:\n"
    "v_mfma_f32_16x16x32_bf16 v[36:39], a[112:115], a[192:195], v[100:103]\n"
    "ds_read_b128 a[136:139], v2 offset:13312\n"
    "v_cvt_pk_bf16_f32 v68, v4, v5\n"
    "v_mfma_f32_16x16x32_bf16 v[36:39], a[116:119], a[196:199], v[36:39]\n"
    "ds_read_b128 a[140:143], v2 offset:15360\n"
    "v_mfma_f32_16x16x32_bf16 v[36:39], a[120:123], a[200:203], v[36:39]\n"
    "ds_read_b128 a[144:147], v2 offset:17408\n"
    "v_mfma_f32_16x16x32_bf16 v[36:39], a[124:127], a[204:207], v[36:39]\n"
    "ds_read_b128 a[148:151], v2 offset:19456\n"
    "v_cvt_pk_bf16_f32 v69, v6, v7\n"
    "v_mfma_f32_16x16x32_bf16 v[36:39], a[128:131], a[208:211], v[36:39]\n"
    "ds_read_b128 a[152:155], v2 offset:21504\n"
    "v_mfma_f32_16x16x32_bf16 v[36:39], a[132:135], a[212:215], v[36:39]\n"
    "ds_read_b128 a[156:159], v2 offset:23552\n"
    "v_mfma_f32_16x16x32_bf16 v[40:43], a[132:135], a[236:239], v[100:103]\n"
    "ds_read_b128 v[104:107], v250 offset:912\n"
    "v_mfma_f32_16x16x32_bf16 v[40:43], a[128:131], a[232:235], v[40:43]\n"
    "s_add_u32 s68, s68, 24576\n"
    "v_cvt_pk_bf16_f32 v76, v8, v9\n"
    "v_mfma_f32_16x16x32_bf16 v[40:43], a[124:127], a[228:231], v[40:43]\n"
    "s_cmp_ge_u32 s68, s69\n"
    "s_cselect_b32 s68, s65, s68\n"
    "v_mfma_f32_16x16x32_bf16 v[40:43], a[120:123], a[224:227], v[40:43]\n"
    "v_exp_f32 v36, v36\n"
    "v_mfma_f32_16x16x32_bf16 v[40:43], a[116:119], a[220:223], v[40:43]\n"
    "v_exp_f32 v37, v37\n"
    "v_mfma_f32_16x16x32_bf16 v[40:43], a[112:115], a[216:219], v[40:43]\n"
    "v_cvt_pk_bf16_f32 v77, v10, v11\n"
    "v_cvt_pk_bf16_f32 v72, v36, v37\n"
    "v_mfma_f32_16x16x32_bf16 v[44:47], a[112:115], a[240:243], v[100:103]\n"
    "v_mfma_f32_16x16x32_bf16 v[44:47], a[116:119], a[244:247], v[44:47]\n"
    "v_exp_f32 v38, v38\n"
    "v_mfma_f32_16x16x32_bf16 v[44:47], a[120:123], a[248:251], v[44:47]\n"
    "v_mfma_f32_16x16x32_bf16 v[44:47], a[124:127], a[252:255], v[44:47]\n"
    "v_cvt_pk_bf16_f32 v84, v12, v13\n"
    "v_mfma_f32_16x16x32_bf16 v[44:47], a[128:131], a[160:163], v[44:47]\n"
    "v_exp_f32 v39, v39\n"
    "v_mfma_f32_16x16x32_bf16 v[44:47], a[132:135], a[164:167], v[44:47]\n"
    "v_cvt_pk_bf16_f32 v73, v38, v39\n"
    "v_mfma_f32_16x16x32_bf16 v[48:51], a[132:135], a[188:191], v[100:103]\n"
    "ds_read_b32 v245, v247\n"
    "v_cvt_pk_bf16_f32 v85, v14, v15\n"
    "v_mfma_f32_16x16x32_bf16 v[48:51], a[128:131], a[184:187], v[48:51]\n"
    "s_add_u32 m0, s68, 0\n"
    "v_exp_f32 v40, v40\n"
    "v_mfma_f32_16x16x32_bf16 v[48:51], a[124:127], a[180:183], v[48:51]\n"
    "v_mfma_f32_16x16x32_bf16 v[48:51], a[120:123], a[176:179], v[48:51]\n"
    "v_mfma_f32_16x16x32_bf16 v[48:51], a[116:119], a[172:175], v[48:51]\n"
    "s_waitcnt vmcnt(0)\n"
    "v_cmp_eq_u64 s[54:55], v[236:237], v[238:239]\n"
    "v_cvt_pk_bf16_f32 v92, v16, v17\n"
    "v_mfma_f32_16x16x32_bf16 v[48:51], a[112:115], a[168:171], v[48:51]\n"
    "s_waitcnt lgkmcnt(1)\n"
    "v_exp_f32 v41, v41\n"
    "v_mfma_f32_16x16x32_bf16 v[52:55], a[136:139], a[192:195], v[104:107]\n"
    "s_waitcnt lgkmcnt(0)\n"
    "v_cmp_le_u32 s[56:57], s59, v245\n"
    "v_cvt_pk_bf16_f32 v80, v40, v41\n"
    "v_mfma_f32_16x16x32_bf16 v[52:55], a[140:143], a[196:199], v[52:55]\n"
    "s_waitcnt vmcnt(1)\n"
    "v_exp_f32 v42, v42\n"
    "v_mfma_f32_16x16x32_bf16 v[52:55], a[144:147], a[200:203], v[52:55]\n"
    "s_and_b64 s[54:55], s[54:55], s[56:57]\n"
    "s_cmp_eq_u64 s[54:55], -1\n"
    "v_mfma_f32_16x16x32_bf16 v[52:55], a[148:151], a[204:207], v[52:55]\n"
    "s_cbranch_scc0 .Lpoll4\n"
    ".Lpoll4r:\n"
    "v_mfma_f32_16x16x32_bf16 v[52:55], a[152:155], a[208:211], v[52:55]\n"
    "buffer_load_dwordx4 v1, s[16:19], s67 offen offset:0 sc1 lds\n"
    "v_mfma_f32_16x16x32_bf16 v[52:55], a[156:159], a[212:215], v[52:55]\n"
    "s_add_u32 s76, s52, s60\n"
    "ds_read_b128 a[112:115], v3 offset:0\n"
    "v_cvt_pk_bf16_f32 v93, v18, v19\n"
    "v_mfma_f32_16x16x32_bf16 v[56:59], a[156:159], a[236:239], v[104:107]\n"
    "s_add_u32 s77, s52, s61\n"
    "ds_read_b128 a[116:119], v3 offset:2048\n"
    "v_cvt_pk_bf16_f32 v70, v20, v21\n"
    "v_mfma_f32_16x16x32_bf16 v[56:59], a[152:155], a[232:235], v[56:59]\n"
    "ds_read_b128 a[120:123], v3 offset:4096\n"
    "v_mfma_f32_16x16x32_bf16 v[56:59], a[148:151], a[228:231], v[56:59]\n"
    "buffer_load_dwordx4 v1, s[16:19], s67 offen offset:1024 sc1 lds\n"
    "v_mfma_f32_16x16x32_bf16 v[56:59], a[144:147], a[224:227], v[56:59]\n"
    "ds_read_b128 a[124:127], v3 offset:6144\n"
    "v_cvt_pk_bf16_f32 v71, v22, v23\n"
    "v_mfma_f32_16x16x32_bf16 v[56:59], a[140:143], a[220:223], v[56:59]\n"
    "s_add_u32 s78, s52, s62\n"
    "ds_read_b128 a[128:131], v3 offset:8192\n"
    "v_mfma_f32_16x16x32_bf16 v[56:59], a[136:139], a[216:219], v[56:59]\n"
    "ds_read_b128 a[132:135], v3 offset:10240\n"
    "v_mfma_f32_16x16x32_bf16 v[60:63], a[136:139], a[240:243], v[104:107]\n"
    "buffer_load_dwordx4 v1, s[16:19], s67 offen offset:2048 sc1 lds\n"
    "v_mfma_f32_16x16x32_bf16 v[60:63], a[140:143], a[244:247], v[60:63]\n"
    "ds_read_b128 v[100:103], v250 offset:1536\n"
    "v_cvt_pk_bf16_f32 v78, v24, v25\n"
    "v_mfma_f32_16x16x32_bf16 v[60:63], a[144:147], a[248:251], v[60:63]\n"
    "v_exp_f32 v43, v43\n"
    "v_mfma_f32_16x16x32_bf16 v[60:63], a[148:151], a[252:255], v[60:63]\n"
    "v_cvt_pk_bf16_f32 v81, v42, v43\n"
    "v_cvt_pk_u16_u32 v204, v204, v208\n"
    "v_mfma_f32_16x16x32_bf16 v[60:63], a[152:155], a[160:163], v[60:63]\n"
    "buffer_load_dwordx4 v1, s[16:19], s67 offen offset:3072 sc1 lds\n"
    "v_mfma_f32_16x16x32_bf16 v[60:63], a[156:159], a[164:167], v[60:63]\n"
    "s_add_u32 m0, s68, 4096\n"
    "v_cvt_pk_bf16_f32 v79, v26, v27\n"
    "v_mfma_f32_16x16x32_bf16 v[64:67], a[156:159], a[188:191], v[104:107]\n"
    "s_add_u32 s83, s67, 4096\n"
    "v_exp_f32 v44, v44\n"
    "v_mfma_f32_16x16x32_bf16 v[64:67], a[152:155], a[184:187], v[64:67]\n"
    "v_exp_f32 v45, v45\n"
    "v_mfma_f32_16x16x32_bf16 v[64:67], a[148:151], a[180:183], v[64:67]\n"
    "buffer_load_dwordx4 v1, s[16:19], s83 offen offset:0 sc1 lds\n"
    "v_mfma_f32_16x16x32_bf16 v[64:67], a[144:147], a[176:179], v[64:67]\n"
    "v_cvt_pk_bf16_f32 v86, v28, v29\n"
    "v_cvt_pk_bf16_f32 v88, v44, v45\n"
    "v_mfma_f32_16x16x32_bf16 v[64:67], a[140:143], a[172:175], v[64:67]\n"
    "v_exp_f32 v46, v46\n"
    "v_mfma_f32_16x16x32_bf16 v[64:67], a[136:139], a[168:171], v[64:67]\n"
    "s_waitcnt lgkmcnt(0)\n"
    "v_exp_f32 v47, v47\n"
    "v_mfma_f32_16x16x32_bf16 v[4:7], a[112:115], a[192:195], v[100:103]\n"
    "buffer_load_dwordx4 v1, s[16:19], s83 offen offset:1024 sc1 lds\n"
    "v_mfma_f32_16x16x32_bf16 v[4:7], a[116:119], a[196:199], v[4:7]\n"
    "s_add_u32 m0, s70, 0\n"
    "ds_read_b128 a[136:139], v3 offset:1024\n"
    "v_cvt_pk_bf16_f32 v87, v30, v31\n"
    "v_mfma_f32_16x16x32_bf16 v[4:7], a[120:123], a[200:203], v[4:7]\n"
    "s_add_u32 s83, s67, 24576\n"
    "ds_read_b128 a[140:143], v3 offset:3072\n"
    "v_cvt_pk_bf16_f32 v89, v46, v47\n"
    "v_mfma_f32_16x16x32_bf16 v[4:7], a[124:127], a[204:207], v[4:7]\n"
    "ds_read_b128 a[144:147], v3 offset:5120\n"
    "v_cvt_pk_u16_u32 v205, v205, v209\n"
    "v_mfma_f32_16x16x32_bf16 v[4:7], a[128:131], a[208:211], v[4:7]\n"
    "buffer_load_dwordx4 v1, s[16:19], s83 offen offset:0 sc1 lds\n"
    "v_mfma_f32_16x16x32_bf16 v[4:7], a[132:135], a[212:215], v[4:7]\n"
    "ds_read_b128 a[148:151], v3 offset:7168\n"
    "v_cvt_pk_bf16_f32 v94, v32, v33\n"
    "v_mfma_f32_16x16x32_bf16 v[8:11], a[132:135], a[236:239], v[100:103]\n"
    "ds_read_b128 a[152:155], v3 offset:9216\n"
    "v_cvt_pk_u16_u32 v206, v206, v210\n"
    "v_mfma_f32_16x16x32_bf16 v[8:11], a[128:131], a[232:235], v[8:11]\n"
    "ds_read_b128 a[156:159], v3 offset:11264\n"
    "v_cvt_pk_u16_u32 v207, v207, v211\n"
    "v_mfma_f32_16x16x32_bf16 v[8:11], a[124:127], a[228:231], v[8:11]\n"
    "buffer_load_dwordx4 v1, s[16:19], s83 offen offset:1024 sc1 lds\n"
    "v_mfma_f32_16x16x32_bf16 v[8:11], a[120:123], a[224:227], v[8:11]\n"
    "ds_read_b128 v[104:107], v250 offset:1552\n"
    "v_cvt_pk_bf16_f32 v95, v34, v35\n"
    "v_mfma_f32_16x16x32_bf16 v[8:11], a[116:119], a[220:223], v[8:11]\n"
    "v_exp_f32 v52, v52\n"
    "v_mfma_f32_16x16x32_bf16 v[8:11], a[112:115], a[216:219], v[8:11]\n"
    "v_exp_f32 v53, v53\n"
    "v_mfma_f32_16x16x32_bf16 v[12:15], a[112:115], a[240:243], v[100:103]\n"
    "buffer_load_dwordx4 v1, s[16:19], s83 offen offset:2048 sc1 lds\n"
    "v_mfma_f32_16x16x32_bf16 v[12:15], a[116:119], a[244:247], v[12:15]\n"
    "v_exp_f32 v54, v54\n"
    "v_mfma_f32_16x16x32_bf16 v[12:15], a[120:123], a[248:251], v[12:15]\n"
    "v_exp_f32 v55, v55\n"
    "v_mfma_f32_16x16x32_bf16 v[12:15], a[124:127], a[252:255], v[12:15]\n"
    "v_cvt_pk_bf16_f32 v74, v52, v53\n"
    "v_cvt_pk_bf16_f32 v75, v54, v55\n"
    "v_mfma_f32_16x16x32_bf16 v[12:15], a[128:131], a[160:163], v[12:15]\n"
    "buffer_load_dwordx4 v1, s[16:19], s83 offen offset:3072 sc1 lds\n"
    "v_mfma_f32_16x16x32_bf16 v[12:15], a[132:135], a[164:167], v[12:15]\n"
    "s_add_u32 m0, s70, 4096\n"
    "v_exp_f32 v56, v56\n"
    "v_mfma_f32_16x16x32_bf16 v[16:19], a[132:135], a[188:191], v[100:103]\n"
    "s_add_u32 s83, s67, 28672\n"
    "v_exp_f32 v48, v48\n"
    "v_mfma_f32_16x16x32_bf16 v[16:19], a[128:131], a[184:187], v[16:19]\n"
    "v_exp_f32 v49, v49\n"
    "v_mfma_f32_16x16x32_bf16 v[16:19], a[124:127], a[180:183], v[16:19]\n"
    "buffer_load_dwordx4 v1, s[16:19], s83 offen offset:0 sc1 lds\n"
    "v_mfma_f32_16x16x32_bf16 v[16:19], a[120:123], a[176:179], v[16:19]\n"
    "v_exp_f32 v57, v57\n"
    "v_mfma_f32_16x16x32_bf16 v[16:19], a[116:119], a[172:175], v[16:19]\n"
    "v_cvt_pk_bf16_f32 v82, v56, v57\n"
    "v_cvt_pk_bf16_f32 v96, v48, v49\n"
    "v_mfma_f32_16x16x32_bf16 v[16:19], a[112:115], a[168:171], v[16:19]\n"
    "s_waitcnt lgkmcnt(0)\n"
    "v_exp_f32 v58, v58\n"
    "v_mfma_f32_16x16x32_bf16 v[20:23], a[136:139], a[192:195], v[104:107]\n"
    "buffer_load_dwordx4 v1, s[16:19], s83 offen offset:1024 sc1 lds\n"
    "v_mfma_f32_16x16x32_bf16 v[20:23], a[140:143], a[196:199], v[20:23]\n"
    "s_add_u32 s67, s67, 49152\n"
    "ds_read_b128 a[112:115], v249 offset:0\n"
    "v_or3_b32 v243, v204, v205, v243\n"
    "v_mfma_f32_16x16x32_bf16 v[20:23], a[144:147], a[200:203], v[20:23]\n"
    "s_add_u32 s59, s59, 4\n"
    "ds_read_b128 a[116:119], v249 offset:12288\n"
    "v_or3_b32 v243, v206, v207, v243\n"
    "v_mfma_f32_16x16x32_bf16 v[20:23], a[148:151], a[204:207], v[20:23]\n"
    "ds_read_b128 a[120:123], v249 offset:1024\n"
    "v_cvt_pk_u16_u32 v212, v212, v216\n"
    "v_mfma_f32_16x16x32_bf16 v[20:23], a[152:155], a[208:211], v[20:23]\n"
    "ds_read_b128 a[124:127], v249 offset:13312\n"
    "v_cvt_pk_u16_u32 v213, v213, v217\n"
    "v_mfma_f32_16x16x32_bf16 v[20:23], a[156:159], a[212:215], v[20:23]\n"
    "ds_read_b128 a[128:131], v249 offset:2048\n"
    "v_mfma_f32_16x16x32_bf16 v[24:27], a[156:159], a[236:239], v[104:107]\n"
    "ds_read_b128 a[132:135], v249 offset:14336\n"
    "v_mfma_f32_16x16x32_bf16 v[24:27], a[152:155], a[232:235], v[24:27]\n"
    "v_exp_f32 v59, v59\n"
    "v_mfma_f32_16x16x32_bf16 v[24:27], a[148:151], a[228:231], v[24:27]\n"
    "v_cvt_pk_bf16_f32 v83, v58, v59\n"
    "v_cvt_pk_u16_u32 v214, v214, v218\n"
    "v_mfma_f32_16x16x32_bf16 v[24:27], a[144:147], a[224:227], v[24:27]\n"
    "v_exp_f32 v60, v60\n"
    "v_mfma_f32_16x16x32_bf16 v[24:27], a[140:143], a[220:223], v[24:27]\n"
    "v_exp_f32 v61, v61\n"
    "v_mfma_f32_16x16x32_bf16 v[24:27], a[136:139], a[216:219], v[24:27]\n"
    "v_exp_f32 v62, v62\n"
    "v_mfma_f32_16x16x32_bf16 v[28:31], a[136:139], a[240:243], v[104:107]\n"
    "v_cvt_pk_bf16_f32 v90, v60, v61\n"
    "v_mfma_f32_16x16x32_bf16 v[28:31], a[140:143], a[244:247], v[28:31]\n"
    "v_exp_f32 v63, v63\n"
    "v_mfma_f32_16x16x32_bf16 v[28:31], a[144:147], a[248:251], v[28:31]\n"
    "v_cvt_pk_bf16_f32 v91, v62, v63\n"
    "v_cvt_pk_u16_u32 v215, v215, v219\n"
    "v_mfma_f32_16x16x32_bf16 v[28:31], a[148:151], a[252:255], v[28:31]\n"
    "v_exp_f32 v50, v50\n"
    "v_mfma_f32_16x16x32_bf16 v[28:31], a[152:155], a[160:163], v[28:31]\n"
    "v_exp_f32 v51, v51\n"
    "v_mfma_f32_16x16x32_bf16 v[28:31], a[156:159], a[164:167], v[28:31]\n"
    "v_exp_f32 v64, v64\n"
    "v_mfma_f32_16x16x32_bf16 v[32:35], a[156:159], a[188:191], v[104:107]\n"
    "v_cvt_pk_bf16_f32 v97, v50, v51\n"
    "v_mfma_f32_16x16x32_bf16 v[32:35], a[152:155], a[184:187], v[32:35]\n"
    "v_exp_f32 v65, v65\n"
    "v_mfma_f32_16x16x32_bf16 v[32:35], a[148:151], a[180:183], v[32:35]\n"
    "v_cvt_pk_bf16_f32 v98, v64, v65\n"
    "v_or3_b32 v243, v212, v213, v243\n"
    "v_mfma_f32_16x16x32_bf16 v[32:35], a[144:147], a[176:179], v[32:35]\n"
    "v_mfma_f32_16x16x32_bf16 v[32:35], a[140:143], a[172:175], v[32:35]\n"
    "v_exp_f32 v66, v66\n"
    "v_mfma_f32_16x16x32_bf16 v[32:35], a[136:139], a[168:171], v[32:35]\n"
    "s_waitcnt lgkmcnt(0)\n"
    "v_mfma_f32_16x16x32_bf16 v[124:127], a[112:115], v[68:71], v[124:127]\n"
    "ds_read_b128 a[136:139], v249 offset:3072\n"
    "v_or3_b32 v243, v214, v215, v243\n"
    "v_mfma_f32_16x16x32_bf16 v[124:127], a[116:119], v[72:75], v[124:127]\n"
    "ds_read_b128 a[140:143], v249 offset:15360\n"
    "v_mfma_f32_16x16x32_bf16 v[128:131], a[124:127], v[72:75], v[128:131]\n"
    "ds_read_b128 a[144:147], v249 offset:4096\n"
    "v_mfma_f32_16x16x32_bf16 v[128:131], a[120:123], v[68:71], v[128:131]\n"
    "ds_read_b128 a[148:151], v249 offset:16384\n"
    "v_mfma_f32_16x16x32_bf16 v[132:135], a[128:131], v[68:71], v[132:135]\n"
    "ds_read_b128 a[152:155], v249 offset:5120\n"
    "v_mfma_f32_16x16x32_bf16 v[132:135], a[132:135], v[72:75], v[132:135]\n"
    "ds_read_b128 a[156:159], v249 offset:17408\n"
    "v_cvt_pk_u16_u32 v220, v220, v224\n"
    "v_mfma_f32_16x16x32_bf16 v[148:151], a[132:135], v[80:83], v[148:151]\n"
    "ds_read_b128 v[104:107], v251 offset:768\n"
    "v_mfma_f32_16x16x32_bf16 v[148:151], a[128:131], v[76:79], v[148:151]\n"
    "v_exp_f32 v67, v67\n"
    "v_mfma_f32_16x16x32_bf16 v[144:147], a[120:123], v[76:79], v[144:147]\n"
    "v_cvt_pk_bf16_f32 v99, v66, v67\n"
    "v_mfma_f32_16x16x32_bf16 v[144:147], a[124:127], v[80:83], v[144:147]\n"
    "v_exp_f32 v4, v4\n"
    "v_mfma_f32_16x16x32_bf16 v[140:143], a[116:119], v[80:83], v[140:143]\n"
    "v_exp_f32 v5, v5\n"
    "v_mfma_f32_16x16x32_bf16 v[140:143], a[112:115], v[76:79], v[140:143]\n"
    "v_exp_f32 v6, v6\n"
    "v_mfma_f32_16x16x32_bf16 v[156:159], a[112:115], v[84:87], v[156:159]\n"
    "v_exp_f32 v7, v7\n"
    "v_mfma_f32_16x16x32_bf16 v[156:159], a[116:119], v[88:91], v[156:159]\n"
    "v_cvt_pk_u16_u32 v221, v221, v225\n"
    "v_mfma_f32_16x16x32_bf16 v[160:163], a[124:127], v[88:91], v[160:163]\n"
    "v_exp_f32 v8, v8\n"
    "v_mfma_f32_16x16x32_bf16 v[160:163], a[120:123], v[84:87], v[160:163]\n"
    "v_cvt_pk_u16_u32 v222, v222, v226\n"
    "v_mfma_f32_16x16x32_bf16 v[164:167], a[128:131], v[84:87], v[164:167]\n"
    "v_mfma_f32_16x16x32_bf16 v[164:167], a[132:135], v[88:91], v[164:167]\n"
    "v_exp_f32 v9, v9\n"
    "v_mfma_f32_16x16x32_bf16 v[180:183], a[132:135], v[96:99], v[180:183]\n"
    "v_mfma_f32_16x16x32_bf16 v[180:183], a[128:131], v[92:95], v[180:183]\n"
    "v_mfma_f32_16x16x32_bf16 v[176:179], a[120:123], v[92:95], v[176:179]\n"
    "v_exp_f32 v10, v10\n"
    "v_mfma_f32_16x16x32_bf16 v[176:179], a[124:127], v[96:99], v[176:179]\n"
    "v_cvt_pk_u16_u32 v223, v223, v227\n"
    "v_mfma_f32_16x16x32_bf16 v[172:175], a[116:119], v[96:99], v[172:175]\n"
    "v_exp_f32 v11, v11\n"
    "v_mfma_f32_16x16x32_bf16 v[172:175], a[112:115], v[92:95], v[172:175]\n"
    "s_waitcnt lgkmcnt(0)\n"
    "v_mfma_f32_16x16x32_bf16 v[136:139], a[136:139], v[68:71], v[136:139]\n"
    "ds_read_b128 a[112:115], v249 offset:6144\n"
    "v_mfma_f32_16x16x32_bf16 v[136:139], a[140:143], v[72:75], v[136:139]\n"
    "ds_read_b128 a[116:119], v249 offset:18432\n"
    "v_mfma_f32_16x16x32_bf16 v[188:191], a[148:151], v[72:75], v[188:191]\n"
    "ds_read_b128 a[120:123], v249 offset:7168\n"
    "v_or3_b32 v243, v220, v221, v243\n"
    "v_mfma_f32_16x16x32_bf16 v[188:191], a[144:147], v[68:71], v[188:191]\n"
    "ds_read_b128 a[124:127], v249 offset:19456\n"
    "v_mfma_f32_16x16x32_bf16 v[192:195], a[152:155], v[68:71], v[192:195]\n"
    "ds_read_b128 a[128:131], v249 offset:8192\n"
    "v_mfma_f32_16x16x32_bf16 v[192:195], a[156:159], v[72:75], v[192:195]\n"
    "ds_read_b128 a[132:135], v249 offset:20480\n"
    "v_smfmac_f32_16x16x64_bf16 v[108:111], v[104:107], v[68:75], v253\n"
    "s_add_u32 s81, s81, 128\n"
    "s_cmp_gt_u32 s81, 4096\n"
    "s_cselect_b32 s81, 0, s81\n"
    "v_smfmac_f32_16x16x64_bf16 v[112:115], v[104:107], v[76:83], v253\n"
    "v_mfma_f32_16x16x32_bf16 a[4:7], a[156:159], v[80:83], a[4:7]\n"
    "v_exp_f32 v12, v12\n"
    "v_mfma_f32_16x16x32_bf16 a[4:7], a[152:155], v[76:79], a[4:7]\n"
    "v_exp_f32 v13, v13\n"
    "v_mfma_f32_16x16x32_bf16 a[0:3], a[144:147], v[76:79], a[0:3]\n"
    "v_exp_f32 v14, v14\n"
    "v_mfma_f32_16x16x32_bf16 a[0:3], a[148:151], v[80:83], a[0:3]\n"
    "v_exp_f32 v15, v15\n"
    "v_mfma_f32_16x16x32_bf16 v[152:155], a[140:143], v[80:83], v[152:155]\n"
    "v_or3_b32 v243, v222, v223, v243\n"
    "v_and_b32 v208, 0xff00ff00, v243\n"
    "v_mfma_f32_16x16x32_bf16 v[152:155], a[136:139], v[76:79], v[152:155]\n"
    "v_exp_f32 v16, v16\n"
    "v_mfma_f32_16x16x32_bf16 v[168:171], a[136:139], v[84:87], v[168:171]\n"
    "v_exp_f32 v17, v17\n"
    "v_mfma_f32_16x16x32_bf16 v[168:171], a[140:143], v[88:91], v[168:171]\n"
    "v_mfma_f32_16x16x32_bf16 a[16:19], a[148:151], v[88:91], a[16:19]\n"
    "v_mfma_f32_16x16x32_bf16 a[16:19], a[144:147], v[84:87], a[16:19]\n"
    "v_exp_f32 v18, v18\n"
    "v_mfma_f32_16x16x32_bf16 a[20:23], a[152:155], v[84:87], a[20:23]\n"
    "v_cmp_ne_u32 vcc, 0, v208\n"
    "v_mfma_f32_16x16x32_bf16 a[20:23], a[156:159], v[88:91], a[20:23]\n"
    "v_smfmac_f32_16x16x64_bf16 v[116:119], v[104:107], v[84:91], v253\n"
    "v_smfmac_f32_16x16x64_bf16 v[120:123], v[104:107], v[92:99], v253\n"
    "v_mfma_f32_16x16x32_bf16 a[36:39], a[156:159], v[96:99], a[36:39]\n"
    "v_exp_f32 v19, v19\n"
    "v_mfma_f32_16x16x32_bf16 a[36:39], a[152:155], v[92:95], a[36:39]\n"
    "v_exp_f32 v20, v20\n"
    "v_mfma_f32_16x16x32_bf16 a[32:35], a[144:147], v[92:95], a[32:35]\n"
    "v_cndmask_b32 v208, 2.0, v241, vcc\n"
    "v_mfma_f32_16x16x32_bf16 a[32:35], a[148:151], v[96:99], a[32:35]\n"
    "v_exp_f32 v21, v21\n"
    "v_mfma_f32_16x16x32_bf16 v[184:187], a[140:143], v[96:99], v[184:187]\n"
    "v_mfma_f32_16x16x32_bf16 v[184:187], a[136:139], v[92:95], v[184:187]\n"
    "s_waitcnt lgkmcnt(0)\n"
    "v_mfma_f32_16x16x32_bf16 v[196:199], a[112:115], v[68:71], v[196:199]\n"
    "ds_read_b128 a[136:139], v249 offset:9216\n"
    "v_mfma_f32_16x16x32_bf16 v[196:199], a[116:119], v[72:75], v[196:199]\n"
    "ds_read_b128 a[140:143], v249 offset:21504\n"
    "v_mfma_f32_16x16x32_bf16 v[200:203], a[124:127], v[72:75], v[200:203]\n"
    "ds_read_b128 a[144:147], v249 offset:10240\n"
    "v_mfma_f32_16x16x32_bf16 v[200:203], a[120:123], v[68:71], v[200:203]\n"
    "ds_read_b128 a[148:151], v249 offset:22528\n"
    "v_mfma_f32_16x16x32_bf16 a[48:51], a[128:131], v[68:71], a[48:51]\n"
    "buffer_load_dwordx2 v[236:237], v241, s[36:39], s81 offen offset:0 sc1\n"
    "v_mfma_f32_16x16x32_bf16 a[48:51], a[132:135], v[72:75], a[48:51]\n"
    "ds_read_b128 a[152:155], v249 offset:11264\n"
    "v_mfma_f32_16x16x32_bf16 a[64:67], a[132:135], v[80:83], a[64:67]\n"
    "ds_read_b128 a[156:159], v249 offset:23552\n"
    "v_mfma_f32_16x16x32_bf16 a[64:67], a[128:131], v[76:79], a[64:67]\n"
    "buffer_store_dword v204, v244, s[20:23], s76 offen offset:0 sc1\n"
    "v_mfma_f32_16x16x32_bf16 a[12:15], a[120:123], v[76:79], a[12:15]\n"
    "v_exp_f32 v22, v22\n"
    "v_mfma_f32_16x16x32_bf16 a[12:15], a[124:127], v[80:83], a[12:15]\n"
    "buffer_store_dword v205, v244, s[20:23], s76 offen offset:1024 sc1\n"
    "v_mfma_f32_16x16x32_bf16 a[8:11], a[116:119], v[80:83], a[8:11]\n"
    "v_exp_f32 v23, v23\n"
    "v_mfma_f32_16x16x32_bf16 a[8:11], a[112:115], v[76:79], a[8:11]\n"
    "buffer_store_dword v206, v244, s[20:23], s76 offen offset:2048 sc1\n"
    "v_mfma_f32_16x16x32_bf16 a[24:27], a[112:115], v[84:87], a[24:27]\n"
    "v_exp_f32 v24, v24\n"
    "v_mfma_f32_16x16x32_bf16 a[24:27], a[116:119], v[88:91], a[24:27]\n"
    "buffer_store_dword v207, v244, s[20:23], s76 offen offset:3072 sc1\n"
    "v_mfma_f32_16x16x32_bf16 a[28:31], a[124:127], v[88:91], a[28:31]\n"
    "v_exp_f32 v25, v25\n"
    "v_mfma_f32_16x16x32_bf16 a[28:31], a[120:123], v[84:87], a[28:31]\n"
    "buffer_store_dword v212, v244, s[20:23], s77 offen offset:0 sc1\n"
    "v_mfma_f32_16x16x32_bf16 a[80:83], a[128:131], v[84:87], a[80:83]\n"
    "v_exp_f32 v26, v26\n"
    "v_mfma_f32_16x16x32_bf16 a[80:83], a[132:135], v[88:91], a[80:83]\n"
    "buffer_store_dword v213, v244, s[20:23], s77 offen offset:1024 sc1\n"
    "v_mfma_f32_16x16x32_bf16 a[96:99], a[132:135], v[96:99], a[96:99]\n"
    "v_exp_f32 v27, v27\n"
    "v_mfma_f32_16x16x32_bf16 a[96:99], a[128:131], v[92:95], a[96:99]\n"
    "buffer_store_dword v214, v244, s[20:23], s77 offen offset:2048 sc1\n"
    "v_mfma_f32_16x16x32_bf16 a[44:47], a[120:123], v[92:95], a[44:47]\n"
    "v_exp_f32 v28, v28\n"
    "v_mfma_f32_16x16x32_bf16 a[44:47], a[124:127], v[96:99], a[44:47]\n"
    "buffer_store_dword v215, v244, s[20:23], s77 offen offset:3072 sc1\n"
    "v_mfma_f32_16x16x32_bf16 a[40:43], a[116:119], v[96:99], a[40:43]\n"
    "v_exp_f32 v29, v29\n"
    "v_mfma_f32_16x16x32_bf16 a[40:43], a[112:115], v[92:95], a[40:43]\n"
    "s_waitcnt lgkmcnt(0)\n"
    "v_exp_f32 v30, v30\n"
    "v_mfma_f32_16x16x32_bf16 a[52:55], a[136:139], v[68:71], a[52:55]\n"
    "ds_read_b128 a[112:115], v3 offset:12288\n"
    "v_mfma_f32_16x16x32_bf16 a[52:55], a[140:143], v[72:75], a[52:55]\n"
    "ds_read_b128 a[116:119], v3 offset:14336\n"
    "v_mfma_f32_16x16x32_bf16 a[56:59], a[148:151], v[72:75], a[56:59]\n"
    "ds_read_b128 a[120:123], v3 offset:16384\n"
    "v_mfma_f32_16x16x32_bf16 a[56:59], a[144:147], v[68:71], a[56:59]\n"
    "ds_read_b128 a[124:127], v3 offset:18432\n"
    "v_mfma_f32_16x16x32_bf16 a[60:63], a[152:155], v[68:71], a[60:63]\n"
    "ds_read_b128 a[128:131], v3 offset:20480\n"
    "v_mfma_f32_16x16x32_bf16 a[60:63], a[156:159], v[72:75], a[60:63]\n"
    "ds_read_b128 a[132:135], v3 offset:22528\n"
    "v_mfma_f32_16x16x32_bf16 a[76:79], a[156:159], v[80:83], a[76:79]\n"
    "ds_read_b128 v[100:103], v250 offset:1664\n"
    "v_mfma_f32_16x16x32_bf16 a[76:79], a[152:155], v[76:79], a[76:79]\n"
    "buffer_store_dword v220, v244, s[20:23], s78 offen offset:0 sc1\n"
    "v_mfma_f32_16x16x32_bf16 a[72:75], a[144:147], v[76:79], a[72:75]\n"
    "v_exp_f32 v31, v31\n"
    "v_mfma_f32_16x16x32_bf16 a[72:75], a[148:151], v[80:83], a[72:75]\n"
    "buffer_store_dword v221, v244, s[20:23], s78 offen offset:1024 sc1\n"
    "v_mfma_f32_16x16x32_bf16 a[68:71], a[140:143], v[80:83], a[68:71]\n"
    "v_exp_f32 v32, v32\n"
    "v_mfma_f32_16x16x32_bf16 a[68:71], a[136:139], v[76:79], a[68:71]\n"
    "buffer_store_dword v222, v244, s[20:23], s78 offen offset:2048 sc1\n"
    "v_mfma_f32_16x16x32_bf16 a[84:87], a[136:139], v[84:87], a[84:87]\n"
    "v_exp_f32 v33, v33\n"
    "v_mfma_f32_16x16x32_bf16 a[84:87], a[140:143], v[88:91], a[84:87]\n"
    "buffer_store_dword v223, v244, s[20:23], s78 offen offset:3072 sc1\n"
    "v_mfma_f32_16x16x32_bf16 a[88:91], a[148:151], v[88:91], a[88:91]\n"
    "s_waitcnt vmcnt(13)\n"
    "ds_add_u32 v247, v246\n"
    "v_mfma_f32_16x16x32_bf16 a[88:91], a[144:147], v[84:87], a[88:91]\n"
    "buffer_store_dwordx2 v[238:239], v208, s[36:39], 0 offen offset:0 sc1\n"
    "v_mfma_f32_16x16x32_bf16 a[92:95], a[152:155], v[84:87], a[92:95]\n"
    "v_exp_f32 v34, v34\n"
    "v_mfma_f32_16x16x32_bf16 a[92:95], a[156:159], v[88:91], a[92:95]\n"
    "v_exp_f32 v35, v35\n"
    "v_mfma_f32_16x16x32_bf16 a[108:111], a[156:159], v[96:99], a[108:111]\n"
    "v_mfma_f32_16x16x32_bf16 a[108:111], a[152:155], v[92:95], a[108:111]\n"
    "v_mfma_f32_16x16x32_bf16 a[104:107], a[144:147], v[92:95], a[104:107]\n"
    "s_sub_u32 s74, s68, s65\n"
    "v_add_u32 v250, 1536, v250\n"
    "v_add_u32 v251, 1536, v251\n"
    "v_mfma_f32_16x16x32_bf16 a[104:107], a[148:151], v[96:99], a[104:107]\n"
    "s_add_u32 s66, s66, 1\n"
    "v_mov_b32 v2, v3\n"
    "v_add_u32 v3, s74, v252\n"
    "v_mfma_f32_16x16x32_bf16 a[100:103], a[140:143], v[96:99], a[100:103]\n"
    "s_waitcnt lgkmcnt(1)\n"
    "s_cmp_lt_u32 s66, 29\n"
    "v_mfma_f32_16x16x32_bf16 a[100:103], a[136:139], v[92:95], a[100:103]\n"
    "s_cbranch_scc1 .LPV\n"
    "s_branch .Ltail\n"
    "s_nop 0\n"
    "s_nop 0\n"
    "s_nop 0\n"
    "s_nop 0\n"
    "s_nop 0\n"
    "s_nop 0\n"
    ".LtailP:\n"
    "s_cbranch_execz .Lw2\n"
    "v_swap_b32 v248, v249\n"
    "s_mov_b32 s64, s70\n"
    "v_add_u32 v250, 768, v250\n"
    "v_add_u32 v251, 768, v251\n"
    ".Ltail:\n"
    "v_mfma_f32_16x16x32_bf16 v[36:39], a[112:115], a[192:195], v[100:103]\n"
    "v_cvt_pk_bf16_f32 v93, v18, v19\n"
    "v_cvt_pk_bf16_f32 v84, v12, v13\n"
    "v_mfma_f32_16x16x32_bf16 v[36:39], a[116:119], a[196:199], v[36:39]\n"
    "ds_read_b128 a[136:139], v2 offset:13312\n"
    "ds_read_b128 a[140:143], v2 offset:15360\n"
    "v_mfma_f32_16x16x32_bf16 v[36:39], a[120:123], a[200:203], v[36:39]\n"
    "s_mul_i32 s75, s84, 24576\n"
    "v_mfma_f32_16x16x32_bf16 v[36:39], a[124:127], a[204:207], v[36:39]\n"
    "v_cvt_pk_bf16_f32 v77, v10, v11\n"
    "v_subrev_u32 v249, 24576, v249\n"
    "v_mfma_f32_16x16x32_bf16 v[36:39], a[128:131], a[208:211], v[36:39]\n"
    "ds_read_b128 a[152:155], v2 offset:21504\n"
    "v_mfma_f32_16x16x32_bf16 v[40:43], a[132:135], a[236:239], v[100:103]\n"
    "v_cvt_pk_bf16_f32 v69, v6, v7\n"
    "v_lshlrev_b32 v240, 16, v1\n"
    "v_mfma_f32_16x16x32_bf16 v[36:39], a[132:135], a[212:215], v[36:39]\n"
    "ds_read_b128 a[156:159], v2 offset:23552\n"
    "ds_read_b128 v[104:107], v250 offset:144\n"
    "v_mfma_f32_16x16x32_bf16 v[48:51], a[132:135], a[188:191], v[100:103]\n"
    "s_add_u32 s76, s75, 6144\n"
    "ds_read_b128 a[144:147], v2 offset:17408\n"
    "v_and_b32 v3, 0xf0, v1\n"
    "v_mfma_f32_16x16x32_bf16 v[48:51], a[128:131], a[184:187], v[48:51]\n"
    "ds_read_b128 a[148:151], v2 offset:19456\n"
    "s_add_u32 s77, s76, 6144\n"
    "v_mfma_f32_16x16x32_bf16 v[40:43], a[128:131], a[232:235], v[40:43]\n"
    "v_exp_f32 v37, v37\n"
    "v_mfma_f32_16x16x32_bf16 v[48:51], a[124:127], a[180:183], v[48:51]\n"
    "v_exp_f32 v38, v38\n"
    "v_mfma_f32_16x16x32_bf16 v[48:51], a[120:123], a[176:179], v[48:51]\n"
    "s_add_u32 s78, s77, 6144\n"
    "v_cvt_pk_bf16_f32 v76, v8, v9\n"
    "v_cvt_pk_bf16_f32 v68, v4, v5\n"
    "v_mfma_f32_16x16x32_bf16 v[40:43], a[124:127], a[228:231], v[40:43]\n"
    "v_cvt_pk_bf16_f32 v85, v14, v15\n"
    "v_cvt_pk_bf16_f32 v71, v22, v23\n"
    "v_mfma_f32_16x16x32_bf16 v[48:51], a[116:119], a[172:175], v[48:51]\n"
    "v_and_b32 v2, 0x70, v1\n"
    "v_cvt_pk_bf16_f32 v94, v32, v33\n"
    "v_mfma_f32_16x16x32_bf16 v[44:47], a[112:115], a[240:243], v[100:103]\n"
    "v_cvt_pk_bf16_f32 v95, v34, v35\n"
    "v_cvt_pk_bf16_f32 v86, v28, v29\n"
    "v_mfma_f32_16x16x32_bf16 v[48:51], a[112:115], a[168:171], v[48:51]\n"
    "v_exp_f32 v39, v39\n"
    "v_mfma_f32_16x16x32_bf16 v[44:47], a[116:119], a[244:247], v[44:47]\n"
    "s_waitcnt lgkmcnt(2)\n"
    "v_cvt_pk_bf16_f32 v70, v20, v21\n"
    "v_cvt_pk_bf16_f32 v92, v16, v17\n"
    "v_mfma_f32_16x16x32_bf16 v[60:63], a[136:139], a[240:243], v[104:107]\n"
    "v_exp_f32 v36, v36\n"
    "v_mfma_f32_16x16x32_bf16 v[56:59], a[156:159], a[236:239], v[104:107]\n"
    "s_add_u32 s72, s65, 122880\n"
    "ds_read_b32 v245, v247\n"
    "v_mfma_f32_16x16x32_bf16 v[40:43], a[120:123], a[224:227], v[40:43]\n"
    "v_exp_f32 v51, v51\n"
    "v_mfma_f32_16x16x32_bf16 v[60:63], a[140:143], a[244:247], v[60:63]\n"
    "v_exp_f32 v48, v48\n"
    "v_mfma_f32_16x16x32_bf16 v[44:47], a[120:123], a[248:251], v[44:47]\n"
    "v_exp_f32 v50, v50\n"
    "v_mfma_f32_16x16x32_bf16 v[40:43], a[116:119], a[220:223], v[40:43]\n"
    "s_waitcnt vmcnt(0)\n"
    "v_exp_f32 v49, v49\n"
    "v_mfma_f32_16x16x32_bf16 v[44:47], a[124:127], a[252:255], v[44:47]\n"
    "ds_read_b128 v[232:235], v251 offset:0\n"
    "ds_read_b128 v[100:103], v250 offset:768\n"
    "v_mfma_f32_16x16x32_bf16 v[52:55], a[136:139], a[192:195], v[104:107]\n"
    "v_cvt_pk_bf16_f32 v79, v26, v27\n"
    "v_lshrrev_b32 v224, 4, v1\n"
    "v_mfma_f32_16x16x32_bf16 v[64:67], a[156:159], a[188:191], v[104:107]\n"
    "v_lshl_add_u32 v3, v3, 3, s72\n"
    "v_cmp_eq_u64 s[54:55], v[236:237], v[238:239]\n"
    "v_mfma_f32_16x16x32_bf16 v[52:55], a[140:143], a[196:199], v[52:55]\n"
    "s_waitcnt lgkmcnt(2)\n"
    "v_cvt_pk_bf16_f32 v78, v24, v25\n"
    "v_cmp_le_u32 s[56:57], s59, v245\n"
    "v_mfma_f32_16x16x32_bf16 v[40:43], a[112:115], a[216:219], v[40:43]\n"
    "s_and_b64 s[54:55], s[54:55], s[56:57]\n"
    "v_and_b32 v224, 48, v224\n"
    "s_cmp_eq_u64 s[54:55], -1\n"
    "v_mfma_f32_16x16x32_bf16 v[60:63], a[144:147], a[248:251], v[60:63]\n"
    "s_cbranch_scc0 .Lpt0\n"
    ".Lpt0r:\n"
    "v_mfma_f32_16x16x32_bf16 v[44:47], a[128:131], a[160:163], v[44:47]\n"
    "s_add_u32 s81, s81, 128\n"
    "ds_read_b128 a[116:119], v252 offset:2048\n"
    "s_add_u32 m0, s65, 24576\n"
    "v_mfma_f32_16x16x32_bf16 v[52:55], a[144:147], a[200:203], v[52:55]\n"
    "s_add_u32 s83, s67, .Lend-.Lend2+(0)\n"
    "buffer_load_dwordx4 v1, s[16:19], s83 offen offset:0 sc1 lds\n"
    "v_mfma_f32_16x16x32_bf16 v[60:63], a[148:151], a[252:255], v[60:63]\n"
    "ds_read_b128 a[120:123], v252 offset:4096\n"
    "ds_read_b128 a[128:131], v252 offset:8192\n"
    "v_mfma_f32_16x16x32_bf16 v[56:59], a[152:155], a[232:235], v[56:59]\n"
    "buffer_load_dwordx4 v1, s[16:19], s83 offen offset:1024 sc1 lds\n"
    "v_mfma_f32_16x16x32_bf16 v[60:63], a[152:155], a[160:163], v[60:63]\n"
    "ds_read_b128 v[104:107], v250 offset:784\n"
    "ds_read_b128 a[112:115], v252 offset:0\n"
    "v_mfma_f32_16x16x32_bf16 v[44:47], a[132:135], a[164:167], v[44:47]\n"
    "buffer_load_dwordx4 v1, s[16:19], s83 offen offset:2048 sc1 lds\n"
    "v_mfma_f32_16x16x32_bf16 v[52:55], a[148:151], a[204:207], v[52:55]\n"
    "ds_read_b128 a[132:135], v252 offset:10240\n"
    "v_mfma_f32_16x16x32_bf16 v[52:55], a[152:155], a[208:211], v[52:55]\n"
    "buffer_load_dwordx4 v1, s[16:19], s83 offen offset:3072 sc1 lds\n"
    "v_mfma_f32_16x16x32_bf16 v[60:63], a[156:159], a[164:167], v[60:63]\n"
    "s_add_u32 m0, s65, 28672\n"
    "v_exp_f32_e64 v40, v40\n"
    "v_mfma_f32_16x16x32_bf16 v[56:59], a[148:151], a[228:231], v[56:59]\n"
    "s_add_u32 s83, s67, 4096\n"
    "buffer_load_dwordx4 v1, s[16:19], s83 offen offset:0 sc1 lds\n"
    "v_mfma_f32_16x16x32_bf16 v[56:59], a[144:147], a[224:227], v[56:59]\n"
    "ds_read_b128 a[124:127], v252 offset:6144\n"
    "v_cvt_pk_bf16_f32 v72, v36, v37\n"
    "v_mfma_f32_16x16x32_bf16 v[56:59], a[140:143], a[220:223], v[56:59]\n"
    "buffer_load_dwordx4 v1, s[16:19], s83 offen offset:1024 sc1 lds\n"
    "v_mfma_f32_16x16x32_bf16 v[52:55], a[156:159], a[212:215], v[52:55]\n"
    "v_exp_f32 v45, v45\n"
    "v_mfma_f32_16x16x32_bf16 v[56:59], a[136:139], a[216:219], v[56:59]\n"
    "v_exp_f32 v62, v62\n"
    "v_mfma_f32_16x16x32_bf16 v[64:67], a[152:155], a[184:187], v[64:67]\n"
    "s_waitcnt lgkmcnt(2)\n"
    "v_xor_b32 v224, v224, v2\n"
    "v_cvt_pk_bf16_f32 v87, v30, v31\n"
    "v_mfma_f32_16x16x32_bf16 v[12:15], a[112:115], a[240:243], v[100:103]\n"
    "s_waitcnt lgkmcnt(1)\n"
    "ds_read_b128 a[156:159], v252 offset:11264\n"
    "ds_read_b128 a[152:155], v252 offset:9216\n"
    "v_mfma_f32_16x16x32_bf16 v[16:19], a[132:135], a[188:191], v[100:103]\n"
    ".Lwp0:\n"
    "s_cbranch_execz .Lw3\n"
    "s_add_u32 m0, s65, 73728\n"
    "v_exp_f32 v56, v56\n"
    "v_mfma_f32_16x16x32_bf16 v[64:67], a[148:151], a[180:183], v[64:67]\n"
    "v_exp_f32 v58, v58\n"
    "v_mfma_f32_16x16x32_bf16 v[4:7], a[112:115], a[192:195], v[100:103]\n"
    "s_add_u32 s83, s67, 24576\n"
    "v_exp_f32 v61, v61\n"
    "v_mfma_f32_16x16x32_bf16 v[16:19], a[128:131], a[184:187], v[16:19]\n"
    "v_exp_f32 v53, v53\n"
    "v_mfma_f32_16x16x32_bf16 v[64:67], a[144:147], a[176:179], v[64:67]\n"
    "v_exp_f32 v54, v54\n"
    "v_mfma_f32_16x16x32_bf16 v[64:67], a[140:143], a[172:175], v[64:67]\n"
    "v_exp_f32 v52, v52\n"
    "v_mfma_f32_16x16x32_bf16 v[8:11], a[132:135], a[236:239], v[100:103]\n"
    "s_waitcnt lgkmcnt(2)\n"
    "v_exp_f32 v63, v63\n"
    "v_mfma_f32_16x16x32_bf16 v[16:19], a[124:127], a[180:183], v[16:19]\n"
    "v_exp_f32 v41, v41\n"
    "v_mfma_f32_16x16x32_bf16 v[64:67], a[136:139], a[168:171], v[64:67]\n"
    "v_exp_f32 v43, v43\n"
    "v_mfma_f32_16x16x32_bf16 v[8:11], a[128:131], a[232:235], v[8:11]\n"
    "ds_read_b128 a[144:147], v252 offset:5120\n"
    "ds_read_b128 a[148:151], v252 offset:7168\n"
    "v_mfma_f32_16x16x32_bf16 v[12:15], a[116:119], a[244:247], v[12:15]\n"
    "v_exp_f32 v44, v44\n"
    "v_mfma_f32_16x16x32_bf16 v[4:7], a[116:119], a[196:199], v[4:7]\n"
    "v_exp_f32 v55, v55\n"
    "v_mfma_f32_16x16x32_bf16 v[16:19], a[120:123], a[176:179], v[16:19]\n"
    "ds_read_b128 a[136:139], v252 offset:1024\n"
    "v_cvt_pk_bf16_f32 v80, v40, v41\n"
    "v_mfma_f32_16x16x32_bf16 v[12:15], a[120:123], a[248:251], v[12:15]\n"
    "buffer_load_dwordx4 v1, s[16:19], s83 offen offset:0 sc1 lds\n"
    "v_mfma_f32_16x16x32_bf16 v[4:7], a[120:123], a[200:203], v[4:7]\n"
    "v_exp_f32 v42, v42\n"
    "v_mfma_f32_16x16x32_bf16 v[16:19], a[116:119], a[172:175], v[16:19]\n"
    "v_exp_f32 v66, v66\n"
    "v_mfma_f32_16x16x32_bf16 v[4:7], a[124:127], a[204:207], v[4:7]\n"
    "v_exp_f32 v59, v59\n"
    "v_mfma_f32_16x16x32_bf16 v[4:7], a[128:131], a[208:211], v[4:7]\n"
    "buffer_load_dwordx4 v1, s[16:19], s83 offen offset:1024 sc1 lds\n"
    "v_mfma_f32_16x16x32_bf16 v[4:7], a[132:135], a[212:215], v[4:7]\n"
    "v_exp_f32 v57, v57\n"
    "v_mfma_f32_16x16x32_bf16 v[12:15], a[124:127], a[252:255], v[12:15]\n"
    "ds_read_b128 v[100:103], v250 offset:896\n"
    "ds_read_b128 a[140:143], v252 offset:3072\n"
    "v_mfma_f32_16x16x32_bf16 v[12:15], a[128:131], a[160:163], v[12:15]\n"
    "v_exp_f32 v65, v65\n"
    "v_mfma_f32_16x16x32_bf16 v[8:11], a[124:127], a[228:231], v[8:11]\n"
    "v_exp_f32 v47, v47\n"
    "v_mfma_f32_16x16x32_bf16 v[12:15], a[132:135], a[164:167], v[12:15]\n"
    "s_waitcnt lgkmcnt(6)\n"
    "buffer_load_dwordx4 v1, s[16:19], s83 offen offset:2048 sc1 lds\n"
    "v_mfma_f32_16x16x32_bf16 v[32:35], a[156:159], a[188:191], v[104:107]\n"
    "v_exp_f32 v60, v60\n"
    "v_mfma_f32_16x16x32_bf16 v[24:27], a[156:159], a[236:239], v[104:107]\n"
    "s_waitcnt lgkmcnt(2)\n"
    "v_cvt_pk_bf16_f32 v81, v42, v43\n"
    "v_mfma_f32_16x16x32_bf16 v[28:31], a[136:139], a[240:243], v[104:107]\n"
    "v_cvt_pk_bf16_f32 v96, v48, v49\n"
    "v_cvt_pk_bf16_f32 v90, v60, v61\n"
    "v_mfma_f32_16x16x32_bf16 v[8:11], a[120:123], a[224:227], v[8:11]\n"
    "v_exp_f32 v7, v7\n"
    "v_mfma_f32_16x16x32_bf16 v[24:27], a[152:155], a[232:235], v[24:27]\n"
    "v_exp_f32 v67, v67\n"
    "v_mfma_f32_16x16x32_bf16 v[8:11], a[116:119], a[220:223], v[8:11]\n"
    "ds_read_b128 a[124:127], v252 offset:18432\n"
    "v_mfma_f32_16x16x32_bf16 v[16:19], a[112:115], a[168:171], v[16:19]\n"
    "buffer_load_dwordx4 v1, s[16:19], s83 offen offset:3072 sc1 lds\n"
    "v_mfma_f32_16x16x32_bf16 v[32:35], a[152:155], a[184:187], v[32:35]\n"
    "s_waitcnt lgkmcnt(1)\n"
    "ds_read_b128 a[132:135], v252 offset:22528\n"
    "v_cvt_pk_bf16_f32 v75, v54, v55\n"
    "v_mfma_f32_16x16x32_bf16 v[24:27], a[148:151], a[228:231], v[24:27]\n"
    "s_add_u32 m0, s65, 77824\n"
    "v_cvt_pk_bf16_f32 v74, v52, v53\n"
    "v_cvt_pk_bf16_f32 v97, v50, v51\n"
    "v_mfma_f32_16x16x32_bf16 v[20:23], a[136:139], a[192:195], v[104:107]\n"
    "s_add_u32 s83, s67, 28672\n"
    "v_exp_f32 v6, v6\n"
    "v_mfma_f32_16x16x32_bf16 v[20:23], a[140:143], a[196:199], v[20:23]\n"
    "v_cvt_pk_bf16_f32 v99, v66, v67\n"
    "v_exp_f32 v14, v14\n"
    "v_mfma_f32_16x16x32_bf16 v[24:27], a[144:147], a[224:227], v[24:27]\n"
    "ds_read_b128 a[128:131], v252 offset:20480\n"
    "ds_read_b128 v[104:107], v250 offset:912\n"
    "v_mfma_f32_16x16x32_bf16 v[8:11], a[112:115], a[216:219], v[8:11]\n"
    "v_exp_f32 v46, v46\n"
    "v_mfma_f32_16x16x32_bf16 v[28:31], a[140:143], a[244:247], v[28:31]\n"
    "v_exp_f32 v13, v13\n"
    "v_mfma_f32_16x16x32_bf16 v[32:35], a[148:151], a[180:183], v[32:35]\n"
    "s_waitcnt lgkmcnt(2)\n"
    "v_exp_f32 v64, v64\n"
    "v_mfma_f32_16x16x32_bf16 v[40:43], a[132:135], a[236:239], v[100:103]\n"
    "ds_read_b128 a[120:123], v252 offset:16384\n"
    "v_cvt_pk_bf16_f32 v91, v62, v63\n"
    "v_mfma_f32_16x16x32_bf16 v[32:35], a[144:147], a[176:179], v[32:35]\n"
    "ds_read_b128 a[112:115], v252 offset:12288\n"
    "v_cvt_pk_bf16_f32 v73, v38, v39\n"
    "v_mfma_f32_16x16x32_bf16 v[28:31], a[144:147], a[248:251], v[28:31]\n"
    "v_exp_f32 v11, v11\n"
    "v_mfma_f32_16x16x32_bf16 v[32:35], a[140:143], a[172:175], v[32:35]\n"
    "v_cvt_pk_bf16_f32 v88, v44, v45\n"
    "v_cvt_pk_bf16_f32 v89, v46, v47\n"
    "v_mfma_f32_16x16x32_bf16 v[32:35], a[136:139], a[168:171], v[32:35]\n"
    "buffer_load_dwordx4 v1, s[16:19], s83 offen offset:0 sc1 lds\n"
    "v_mfma_f32_16x16x32_bf16 v[24:27], a[140:143], a[220:223], v[24:27]\n"
    "v_exp_f32 v9, v9\n"
    "v_mfma_f32_16x16x32_bf16 v[20:23], a[144:147], a[200:203], v[20:23]\n"
    "buffer_load_dwordx4 v1, s[16:19], s83 offen offset:1024 sc1 lds\n"
    "v_mfma_f32_16x16x32_bf16 v[20:23], a[148:151], a[204:207], v[20:23]\n"
    "v_exp_f32 v15, v15\n"
    "v_mfma_f32_16x16x32_bf16 v[20:23], a[152:155], a[208:211], v[20:23]\n"
    "v_exp_f32 v34, v34\n"
    "v_mfma_f32_16x16x32_bf16 v[20:23], a[156:159], a[212:215], v[20:23]\n"
    "v_exp_f32 v10, v10\n"
    "v_mfma_f32_16x16x32_bf16 v[28:31], a[148:151], a[252:255], v[28:31]\n"
    "s_add_u32 s59, s59, 4\n"
    "ds_read_b128 a[140:143], v252 offset:15360\n"
    "ds_read_b128 a[116:119], v252 offset:14336\n"
    "v_mfma_f32_16x16x32_bf16 v[28:31], a[152:155], a[160:163], v[28:31]\n"
    "s_waitcnt lgkmcnt(5)\n"
    "v_exp_f32 v4, v4\n"
    "v_mfma_f32_16x16x32_bf16 v[40:43], a[128:131], a[232:235], v[40:43]\n"
    "v_exp_f32 v12, v12\n"
    "v_mfma_f32_16x16x32_bf16 v[24:27], a[136:139], a[216:219], v[24:27]\n"
    "ds_read_b128 a[136:139], v252 offset:13312\n"
    "v_add_u32 v3, v3, v224\n"
    "v_mfma_f32_16x16x32_bf16 v[48:51], a[132:135], a[188:191], v[100:103]\n"
    "v_exp_f32 v23, v23\n"
    "v_smfmac_f32_16x16x64_bf16 v[116:119], v[232:235], v[84:91], v253\n"
    "ds_read_b128 a[152:155], v252 offset:21504\n"
    "v_mfma_f32_16x16x32_bf16 v[28:31], a[156:159], a[164:167], v[28:31]\n"
    "s_waitcnt lgkmcnt(5)\n"
    "v_exp_f32 v5, v5\n"
    "v_mfma_f32_16x16x32_bf16 v[40:43], a[124:127], a[228:231], v[40:43]\n"
    "s_mov_b32 s74, 0x47000000\n"
    "v_exp_f32 v20, v20\n"
    "v_mfma_f32_16x16x32_bf16 v[40:43], a[120:123], a[224:227], v[40:43]\n"
    "s_waitcnt lgkmcnt(4)\n"
    "ds_read_b128 a[156:159], v252 offset:23552\n"
    "v_cvt_pk_bf16_f32 v204, v4, v5\n"
    "v_mfma_f32_16x16x32_bf16 v[36:39], a[112:115], a[192:195], v[100:103]\n"
    "s_waitcnt lgkmcnt(3)\n"
    "v_exp_f32 v32, v32\n"
    "v_mfma_f32_16x16x32_bf16 v[36:39], a[116:119], a[196:199], v[36:39]\n"
    "v_cvt_pk_bf16_f32 v220, v12, v13\n"
    "v_cvt_pk_bf16_f32 v82, v56, v57\n"
    "v_mfma_f32_16x16x32_bf16 v[40:43], a[116:119], a[220:223], v[40:43]\n"
    "v_exp_f32 v16, v16\n"
    "v_mfma_f32_16x16x32_bf16 v[40:43], a[112:115], a[216:219], v[40:43]\n"
    "s_waitcnt lgkmcnt(2)\n"
    "v_exp_f32 v28, v28\n"
    "v_mfma_f32_16x16x32_bf16 v[52:55], a[136:139], a[192:195], v[104:107]\n"
    "v_exp_f32 v33, v33\n"
    "v_mfma_f32_16x16x32_bf16 v[48:51], a[128:131], a[184:187], v[48:51]\n"
    "v_exp_f32 v8, v8\n"
    "v_mfma_f32_16x16x32_bf16 v[44:47], a[112:115], a[240:243], v[100:103]\n"
    "v_exp_f32 v17, v17\n"
    "v_mfma_f32_16x16x32_bf16 v[60:63], a[136:139], a[240:243], v[104:107]\n"
    "v_cvt_pk_bf16_f32 v98, v64, v65\n"
    "v_cvt_pk_bf16_f32 v83, v58, v59\n"
    "v_mfma_f32_16x16x32_bf16 v[48:51], a[124:127], a[180:183], v[48:51]\n"
    "v_exp_f32 v35, v35\n"
    "v_mfma_f32_16x16x32_bf16 v[44:47], a[116:119], a[244:247], v[44:47]\n"
    "v_exp_f32 v41, v41\n"
    "v_mfma_f32_16x16x32_bf16 v[44:47], a[120:123], a[248:251], v[44:47]\n"
    "v_exp_f32 v29, v29\n"
    "v_mfma_f32_16x16x32_bf16 v[60:63], a[140:143], a[244:247], v[60:63]\n"
    "v_exp_f32 v31, v31\n"
    "v_mfma_f32_16x16x32_bf16 v[36:39], a[120:123], a[200:203], v[36:39]\n"
    "v_lshrrev_b32 v224, 3, v1\n"
    "v_cvt_pk_bf16_f32 v205, v6, v7\n"
    "v_mfma_f32_16x16x32_bf16 v[48:51], a[120:123], a[176:179], v[48:51]\n"
    "s_waitcnt vmcnt(6)\n"
    "ds_add_u32 v247, v246\n"
    "v_mfma_f32_16x16x32_bf16 v[48:51], a[116:119], a[172:175], v[48:51]\n"
    "s_waitcnt lgkmcnt(1)\n"
    "ds_read_b128 a[144:147], v252 offset:17408\n"
    "ds_read_b128 a[148:151], v252 offset:19456\n"
    "v_mfma_f32_16x16x32_bf16 v[56:59], a[156:159], a[236:239], v[104:107]\n"
    "v_exp_f32 v25, v25\n"
    "v_cvt_pk_bf16_f32 v228, v16, v17\n"
    "v_mfma_f32_16x16x32_bf16 v[52:55], a[140:143], a[196:199], v[52:55]\n"
    "v_exp_f32 v18, v18\n"
    "v_mfma_f32_16x16x32_bf16 v[44:47], a[124:127], a[252:255], v[44:47]\n"
    "v_exp_f32 v19, v19\n"
    "v_mfma_f32_16x16x32_bf16 v[64:67], a[156:159], a[188:191], v[104:107]\n"
    "v_exp_f32 v43, v43\n"
    "v_mfma_f32_16x16x32_bf16 v[48:51], a[112:115], a[168:171], v[48:51]\n"
    "v_exp_f32 v27, v27\n"
    "v_mfma_f32_16x16x32_bf16 v[36:39], a[124:127], a[204:207], v[36:39]\n"
    "v_exp_f32 v30, v30\n"
    "v_mfma_f32_16x16x32_bf16 v[64:67], a[152:155], a[184:187], v[64:67]\n"
    "v_exp_f32 v22, v22\n"
    "v_mfma_f32_16x16x32_bf16 v[36:39], a[128:131], a[208:211], v[36:39]\n"
    "v_exp_f32 v42, v42\n"
    "v_mfma_f32_16x16x32_bf16 v[44:47], a[128:131], a[160:163], v[44:47]\n"
    "s_waitcnt lgkmcnt(1)\n"
    "v_exp_f32 v26, v26\n"
    "v_mfma_f32_16x16x32_bf16 v[60:63], a[144:147], a[248:251], v[60:63]\n"
    "s_waitcnt lgkmcnt(0)\n"
    "v_exp_f32 v40, v40\n"
    "v_mfma_f32_16x16x32_bf16 v[64:67], a[148:151], a[180:183], v[64:67]\n"
    "ds_read_b128 v[100:103], v250 offset:1536\n"
    "ds_read_b32 v245, v247\n"
    "v_mfma_f32_16x16x32_bf16 v[56:59], a[152:155], a[232:235], v[56:59]\n"
    "v_cvt_pk_bf16_f32 v216, v40, v41\n"
    "v_cvt_pk_bf16_f32 v231, v34, v35\n"
    "v_mfma_f32_16x16x32_bf16 v[64:67], a[144:147], a[176:179], v[64:67]\n"
    "v_exp_f32 v21, v21\n"
    "v_smfmac_f32_16x16x64_bf16 v[120:123], v[232:235], v[92:99], v253\n"
    "v_cvt_pk_bf16_f32 v221, v14, v15\n"
    "v_mfma_f32_16x16x32_bf16 v[44:47], a[132:135], a[164:167], v[44:47]\n"
    "v_cvt_pk_bf16_f32 v207, v22, v23\n"
    "v_cvt_pk_bf16_f32 v229, v18, v19\n"
    "v_mfma_f32_16x16x32_bf16 v[64:67], a[140:143], a[172:175], v[64:67]\n"
    "v_cvt_pk_bf16_f32 v206, v20, v21\n"
    "v_cvt_pk_bf16_f32 v212, v8, v9\n"
    "v_mfma_f32_16x16x32_bf16 v[52:55], a[144:147], a[200:203], v[52:55]\n"
    "v_exp_f32 v50, v50\n"
    "v_mfma_f32_16x16x32_bf16 v[60:63], a[148:151], a[252:255], v[60:63]\n"
    "v_exp_f32 v24, v24\n"
    "v_mfma_f32_16x16x32_bf16 v[36:39], a[132:135], a[212:215], v[36:39]\n"
    "s_waitcnt lgkmcnt(0)\n"
    "v_cmp_le_u32 s[56:57], s59, v245\n"
    "v_cvt_pk_bf16_f32 v213, v10, v11\n"
    "v_smfmac_f32_16x16x64_bf16 v[108:111], v[232:235], v[68:75], v253\n"
    "s_cmp_eq_u64 s[56:57], -1\n"
    "v_cvt_pk_bf16_f32 v214, v24, v25\n"
    "v_smfmac_f32_16x16x64_bf16 v[112:115], v[232:235], v[76:83], v253\n"
    "s_cbranch_scc0 .Lpt1\n"
    ".Lpt1r:\n"
    "v_mfma_f32_16x16x32_bf16 v[52:55], a[148:151], a[204:207], v[52:55]\n"
    "s_add_u32 m0, s65, 49152\n"
    "ds_read_b128 a[128:131], v252 offset:32768\n"
    "ds_read_b128 a[124:127], v252 offset:30720\n"
    "v_mfma_f32_16x16x32_bf16 v[60:63], a[152:155], a[160:163], v[60:63]\n"
    "ds_read_b128 a[112:115], v252 offset:24576\n"
    "v_cvt_pk_bf16_f32 v222, v28, v29\n"
    "v_mfma_f32_16x16x32_bf16 v[60:63], a[156:159], a[164:167], v[60:63]\n"
    "s_add_u32 s83, s67, 73728\n"
    "ds_read_b128 a[132:135], v252 offset:34816\n"
    "v_cvt_pk_bf16_f32 v215, v26, v27\n"
    "v_mfma_f32_16x16x32_bf16 v[56:59], a[148:151], a[228:231], v[56:59]\n"
    "buffer_load_dwordx4 v1, s[16:19], s83 offen offset:0 sc1 lds\n"
    "v_mfma_f32_16x16x32_bf16 v[56:59], a[144:147], a[224:227], v[56:59]\n"
    "ds_read_b128 a[116:119], v252 offset:26624\n"
    "v_cvt_pk_bf16_f32 v223, v30, v31\n"
    "v_mfma_f32_16x16x32_bf16 v[56:59], a[140:143], a[220:223], v[56:59]\n"
    "v_exp_f32 v38, v38\n"
    "v_exp_f32 v47, v47\n"
    "v_mfma_f32_16x16x32_bf16 v[52:55], a[152:155], a[208:211], v[52:55]\n"
    "ds_read_b128 a[120:123], v252 offset:28672\n"
    "ds_read_b128 v[104:107], v250 offset:1552\n"
    "v_mfma_f32_16x16x32_bf16 v[64:67], a[136:139], a[168:171], v[64:67]\n"
    "buffer_load_dwordx4 v1, s[16:19], s83 offen offset:1024 sc1 lds\n"
    "v_mfma_f32_16x16x32_bf16 v[56:59], a[136:139], a[216:219], v[56:59]\n"
    "v_exp_f32 v37, v37\n"
    "v_mfma_f32_16x16x32_bf16 v[52:55], a[156:159], a[212:215], v[52:55]\n"
    "s_waitcnt lgkmcnt(4)\n"
    "buffer_load_dwordx4 v1, s[16:19], s83 offen offset:2048 sc1 lds\n"
    "v_mfma_f32_16x16x32_bf16 v[4:7], a[112:115], a[192:195], v[100:103]\n"
    "ds_read_b128 a[140:143], v252 offset:27648\n"
    "v_cvt_pk_bf16_f32 v230, v32, v33\n"
    "v_mfma_f32_16x16x32_bf16 v[20:23], a[112:115], a[240:243], v[100:103]\n"
    "s_waitcnt lgkmcnt(3)\n"
    "buffer_load_dwordx4 v1, s[16:19], s83 offen offset:3072 sc1 lds\n"
    "v_mfma_f32_16x16x32_bf16 v[20:23], a[116:119], a[244:247], v[20:23]\n"
    "s_add_u32 m0, s65, 53248\n"
    "v_exp_f32 v58, v58\n"
    "v_mfma_f32_16x16x32_bf16 v[28:31], a[132:135], a[188:191], v[100:103]\n"
    "s_add_u32 s83, s67, 77824\n"
    "v_exp_f32 v44, v44\n"
    "v_mfma_f32_16x16x32_bf16 v[28:31], a[128:131], a[184:187], v[28:31]\n"
    "ds_read_b128 a[148:151], v252 offset:31744\n"
    "ds_read_b128 a[152:155], v252 offset:33792\n"
    "v_mfma_f32_16x16x32_bf16 v[12:15], a[132:135], a[236:239], v[100:103]\n"
    "ds_read_b128 a[156:159], v252 offset:35840\n"
    "v_and_b32 v224, 0x70, v224\n"
    "v_mfma_f32_16x16x32_bf16 v[12:15], a[128:131], a[232:235], v[12:15]\n"
    "buffer_load_dwordx4 v1, s[16:19], s83 offen offset:0 sc1 lds\n"
    "v_mfma_f32_16x16x32_bf16 v[28:31], a[124:127], a[180:183], v[28:31]\n"
    "v_exp_f32 v55, v55\n"
    "v_mfma_f32_16x16x32_bf16 v[4:7], a[116:119], a[196:199], v[4:7]\n"
    "s_waitcnt lgkmcnt(5)\n"
    "buffer_load_dwordx4 v1, s[16:19], s83 offen offset:1024 sc1 lds\n"
    "v_mfma_f32_16x16x32_bf16 v[28:31], a[120:123], a[176:179], v[28:31]\n"
    "v_exp_f32 v65, v65\n"
    "v_mfma_f32_16x16x32_bf16 v[28:31], a[116:119], a[172:175], v[28:31]\n"
    "v_exp_f32 v61, v61\n"
    "v_mfma_f32_16x16x32_bf16 v[12:15], a[124:127], a[228:231], v[12:15]\n"
    "v_exp_f32 v53, v53\n"
    "v_mfma_f32_16x16x32_bf16 v[4:7], a[120:123], a[200:203], v[4:7]\n"
    "buffer_load_dwordx2 v[236:237], v241, s[36:39], 0 offen offset:0 sc1\n"
    "v_mfma_f32_16x16x32_bf16 v[20:23], a[120:123], a[248:251], v[20:23]\n"
    "s_waitcnt lgkmcnt(0)\n"
    "v_exp_f32 v67, v67\n"
    "v_mfma_f32_16x16x32_bf16 v[16:19], a[156:159], a[236:239], v[104:107]\n"
    "ds_read_b128 a[136:139], v252 offset:25600\n"
    "ds_read_b128 v[100:103], v250 offset:1664\n"
    "v_mfma_f32_16x16x32_bf16 v[4:7], a[124:127], a[204:207], v[4:7]\n"
    "v_exp_f32 v36, v36\n"
    "v_mfma_f32_16x16x32_bf16 v[28:31], a[112:115], a[168:171], v[28:31]\n"
    "s_waitcnt vmcnt(7)\n"
    "buffer_store_dwordx2 v[238:239], v240, s[36:39], s82 offen offset:0 sc1\n"
    "v_exp_f32 v46, v46\n"
    "v_mfma_f32_16x16x32_bf16 v[32:35], a[156:159], a[188:191], v[104:107]\n"
    "v_exp_f32 v60, v60\n"
    "v_mfma_f32_16x16x32_bf16 v[12:15], a[120:123], a[224:227], v[12:15]\n"
    "v_exp_f32 v64, v64\n"
    "v_mfma_f32_16x16x32_bf16 v[4:7], a[128:131], a[208:211], v[4:7]\n"
    "v_exp_f32 v66, v66\n"
    "v_mfma_f32_16x16x32_bf16 v[12:15], a[116:119], a[220:223], v[12:15]\n"
    "v_exp_f32 v49, v49\n"
    "v_mfma_f32_16x16x32_bf16 v[20:23], a[124:127], a[252:255], v[20:23]\n"
    "ds_read_b128 a[116:119], v252 offset:38912\n"
    "v_cvt_pk_bf16_f32 v235, v66, v67\n"
    "v_mfma_f32_16x16x32_bf16 v[32:35], a[152:155], a[184:187], v[32:35]\n"
    "ds_read_b128 a[120:123], v252 offset:40960\n"
    "v_cvt_pk_bf16_f32 v208, v36, v37\n"
    "v_mfma_f32_16x16x32_bf16 v[12:15], a[112:115], a[216:219], v[12:15]\n"
    "s_mov_b32 s73, 0x7f800000\n"
    "ds_read_b128 a[144:147], v252 offset:29696\n"
    "ds_read_b128 a[112:115], v252 offset:36864\n"
    "v_mfma_f32_16x16x32_bf16 v[4:7], a[132:135], a[212:215], v[4:7]\n"
    "s_waitcnt lgkmcnt(5)\n"
    "ds_add_u32 v247, v246\n"
    "v_mfma_f32_16x16x32_bf16 v[24:27], a[136:139], a[240:243], v[104:107]\n"
    "v_exp_f32 v39, v39\n"
    "v_mfma_f32_16x16x32_bf16 v[16:19], a[152:155], a[232:235], v[16:19]\n"
    "v_cvt_pk_bf16_f32 v226, v60, v61\n"
    "v_cvt_pk_bf16_f32 v234, v64, v65\n"
    "v_mfma_f32_16x16x32_bf16 v[8:11], a[136:139], a[192:195], v[104:107]\n"
    "v_exp_f32 v48, v48\n"
    "v_mfma_f32_16x16x32_bf16 v[24:27], a[140:143], a[244:247], v[24:27]\n"
    "v_exp_f32 v56, v56\n"
    "v_mfma_f32_16x16x32_bf16 v[20:23], a[128:131], a[160:163], v[20:23]\n"
    "ds_read_b128 v[240:243], v251 offset:768\n"
    "v_cvt_pk_bf16_f32 v209, v38, v39\n"
    "v_mfma_f32_16x16x32_bf16 v[16:19], a[148:151], a[228:231], v[16:19]\n"
    "v_exp_f32 v62, v62\n"
    "v_mfma_f32_16x16x32_bf16 v[20:23], a[132:135], a[164:167], v[20:23]\n"
    "v_exp_f32 v63, v63\n"
    "v_mfma_f32_16x16x32_bf16 v[8:11], a[140:143], a[196:199], v[8:11]\n"
    "s_waitcnt lgkmcnt(3)\n"
    "ds_read_b128 a[128:131], v252 offset:45056\n"
    "ds_read_b128 a[132:135], v252 offset:47104\n"
    "v_mfma_f32_16x16x32_bf16 v[24:27], a[144:147], a[248:251], v[24:27]\n"
    "v_exp_f32 v12, v12\n"
    "v_mfma_f32_16x16x32_bf16 v[8:11], a[144:147], a[200:203], v[8:11]\n"
    "v_exp_f32 v13, v13\n"
    "v_mfma_f32_16x16x32_bf16 v[24:27], a[148:151], a[252:255], v[24:27]\n"
    "v_exp_f32 v4, v4\n"
    "v_mfma_f32_16x16x32_bf16 v[24:27], a[152:155], a[160:163], v[24:27]\n"
    "v_exp_f32 v59, v59\n"
    "v_mfma_f32_16x16x32_bf16 v[16:19], a[144:147], a[224:227], v[16:19]\n"
    "s_mov_b32 s79, 133\n"
    "v_exp_f32 v5, v5\n"
    "v_mfma_f32_16x16x32_bf16 v[16:19], a[140:143], a[220:223], v[16:19]\n"
    "v_exp_f32 v54, v54\n"
    "v_mfma_f32_16x16x32_bf16 v[8:11], a[148:151], a[204:207], v[8:11]\n"
    "v_exp_f32 v45, v45\n"
    "v_mfma_f32_16x16x32_bf16 v[16:19], a[136:139], a[216:219], v[16:19]\n"
    "ds_read_b128 a[124:127], v252 offset:43008\n"
    "ds_read_b32 v245, v247\n"
    "v_mfma_f32_16x16x32_bf16 v[32:35], a[148:151], a[180:183], v[32:35]\n"
    "v_exp_f32 v6, v6\n"
    "v_mfma_f32_16x16x32_bf16 v[8:11], a[152:155], a[208:211], v[8:11]\n"
    "ds_read_b128 a[152:155], v252 offset:46080\n"
    "v_cvt_pk_bf16_f32 v217, v42, v43\n"
    "v_mfma_f32_16x16x32_bf16 v[8:11], a[156:159], a[212:215], v[8:11]\n"
    "v_exp_f32 v19, v19\n"
    "v_mfma_f32_16x16x32_bf16 v[24:27], a[156:159], a[164:167], v[24:27]\n"
    "v_exp_f32 v51, v51\n"
    "v_mfma_f32_16x16x32_bf16 v[32:35], a[144:147], a[176:179], v[32:35]\n"
    "s_waitcnt lgkmcnt(7)\n"
    "v_cvt_pk_bf16_f32 v219, v58, v59\n"
    "v_cvt_pk_bf16_f32 v233, v50, v51\n"
    "v_mfma_f32_16x16x32_bf16 v[36:39], a[112:115], a[192:195], v[100:103]\n"
    "s_waitcnt lgkmcnt(3)\n"
    "v_exp_f32 v21, v21\n"
    "v_mfma_f32_16x16x32_bf16 v[40:43], a[132:135], a[236:239], v[100:103]\n"
    "ds_read_b128 a[156:159], v252 offset:48128\n"
    "v_cvt_pk_bf16_f32 v227, v62, v63\n"
    "v_mfma_f32_16x16x32_bf16 v[36:39], a[116:119], a[196:199], v[36:39]\n"
    "s_add_u32 s59, s59, 4\n"
    "v_exp_f32 v28, v28\n"
    "v_mfma_f32_16x16x32_bf16 v[32:35], a[140:143], a[172:175], v[32:35]\n"
    "v_exp_f32 v52, v52\n"
    "v_mfma_f32_16x16x32_bf16 v[32:35], a[136:139], a[168:171], v[32:35]\n"
    "v_cvt_pk_bf16_f32 v232, v48, v49\n"
    "v_mad_u32_u24 v244, v224, 24, v2\n"
    "v_mfma_f32_16x16x32_bf16 v[48:51], a[132:135], a[188:191], v[100:103]\n"
    "ds_read_b128 a[136:139], v252 offset:37888\n"
    "ds_read_b128 v[104:107], v250 offset:1680\n"
    "v_mfma_f32_16x16x32_bf16 v[36:39], a[120:123], a[200:203], v[36:39]\n"
    "s_waitcnt lgkmcnt(5)\n"
    "v_exp_f32 v57, v57\n"
    "v_mfma_f32_16x16x32_bf16 v[36:39], a[124:127], a[204:207], v[36:39]\n"
    "v_cvt_pk_bf16_f32 v225, v46, v47\n"
    "v_cvt_pk_bf16_f32 v210, v52, v53\n"
    "v_mfma_f32_16x16x32_bf16 v[36:39], a[128:131], a[208:211], v[36:39]\n"
    "v_cvt_pk_bf16_f32 v224, v44, v45\n"
    "v_cvt_pk_bf16_f32 v211, v54, v55\n"
    "v_mfma_f32_16x16x32_bf16 v[40:43], a[128:131], a[232:235], v[40:43]\n"
    "ds_read_b128 a[144:147], v252 offset:41984\n"
    "ds_read_b128 a[140:143], v252 offset:39936\n"
    "v_mfma_f32_16x16x32_bf16 v[44:47], a[112:115], a[240:243], v[100:103]\n"
    "v_exp_f32 v20, v20\n"
    "v_mfma_f32_16x16x32_bf16 v[40:43], a[124:127], a[228:231], v[40:43]\n"
    "v_cvt_pk_bf16_f32 v12, v12, v13\n"
    "v_cvt_pk_bf16_f32 v218, v56, v57\n"
    "v_mfma_f32_16x16x32_bf16 v[36:39], a[132:135], a[212:215], v[36:39]\n"
    "v_exp_f32 v32, v32\n"
    "v_mfma_f32_16x16x32_bf16 v[40:43], a[120:123], a[224:227], v[40:43]\n"
    "v_exp_f32 v30, v30\n"
    "v_smfmac_f32_16x16x64_bf16 v[120:123], v[240:243], v[228:235], v253\n"
    "ds_read_b128 a[148:151], v252 offset:44032\n"
    "v_mfma_f32_16x16x32_bf16 v[44:47], a[116:119], a[244:247], v[44:47]\n"
    "s_waitcnt lgkmcnt(3)\n"
    "v_exp_f32 v26, v26\n"
    "v_mfma_f32_16x16x32_bf16 v[52:55], a[136:139], a[192:195], v[104:107]\n"
    "v_exp_f32 v15, v15\n"
    "v_mfma_f32_16x16x32_bf16 v[56:59], a[156:159], a[236:239], v[104:107]\n"
    "s_waitcnt lgkmcnt(1)\n"
    "v_cvt_pk_bf16_f32 v4, v4, v5\n"
    "v_cmp_le_u32 s[56:57], s59, v245\n"
    "v_smfmac_f32_16x16x64_bf16 v[108:111], v[240:243], v[204:211], v253\n"
    "s_cmp_eq_u64 s[56:57], -1\n"
    "v_lshrrev_b32 v252, 3, v1\n"
    "v_mfma_f32_16x16x32_bf16 v[52:55], a[140:143], a[196:199], v[52:55]\n"
    "v_exp_f32 v7, v7\n"
    "v_mfma_f32_16x16x32_bf16 v[60:63], a[136:139], a[240:243], v[104:107]\n"
    "v_exp_f32 v17, v17\n"
    "v_mfma_f32_16x16x32_bf16 v[60:63], a[140:143], a[244:247], v[60:63]\n"
    "v_exp_f32 v14, v14\n"
    "v_mfma_f32_16x16x32_bf16 v[56:59], a[152:155], a[232:235], v[56:59]\n"
    "v_exp_f32 v39, v39\n"
    "v_mfma_f32_16x16x32_bf16 v[64:67], a[156:159], a[188:191], v[104:107]\n"
    "v_exp_f32 v11, v11\n"
    "v_mfma_f32_16x16x32_bf16 v[60:63], a[144:147], a[248:251], v[60:63]\n"
    "v_exp_f32 v22, v22\n"
    "v_mfma_f32_16x16x32_bf16 v[52:55], a[144:147], a[200:203], v[52:55]\n"
    "s_waitcnt lgkmcnt(0)\n"
    "v_exp_f32 v27, v27\n"
    "v_mfma_f32_16x16x32_bf16 v[52:55], a[148:151], a[204:207], v[52:55]\n"
    "v_exp_f32 v8, v8\n"
    "v_mfma_f32_16x16x32_bf16 v[52:55], a[152:155], a[208:211], v[52:55]\n"
    "v_exp_f32 v9, v9\n"
    "v_mfma_f32_16x16x32_bf16 v[56:59], a[148:151], a[228:231], v[56:59]\n"
    "ds_read_b128 a[204:207], v248 offset:13312\n"
    "v_cvt_pk_bf16_f32 v5, v6, v7\n"
    "v_mfma_f32_16x16x32_bf16 v[64:67], a[152:155], a[184:187], v[64:67]\n"
    "v_exp_f32 v16, v16\n"
    "v_mfma_f32_16x16x32_bf16 v[56:59], a[144:147], a[224:227], v[56:59]\n"
    "ds_read_b128 a[208:211], v248 offset:2048\n"
    "ds_read_b128 a[196:199], v248 offset:12288\n"
    "v_mfma_f32_16x16x32_bf16 v[40:43], a[116:119], a[220:223], v[40:43]\n"
    "v_exp_f32_e64 v38, v38\n"
    "v_mfma_f32_16x16x32_bf16 v[52:55], a[156:159], a[212:215], v[52:55]\n"
    "v_exp_f32_e64 v37, v37\n"
    "v_smfmac_f32_16x16x64_bf16 v[116:119], v[240:243], v[220:227], v253\n"
    "v_cvt_pk_bf16_f32 v6, v8, v9\n"
    "v_smfmac_f32_16x16x64_bf16 v[112:115], v[240:243], v[212:219], v253\n"
    "s_cbranch_scc0 .Lpt4\n"
    ".Lpt4r:\n"
    "v_mfma_f32_16x16x32_bf16 v[44:47], a[120:123], a[248:251], v[44:47]\n"
    "v_exp_f32 v10, v10\n"
    "v_mfma_f32_16x16x32_bf16 v[40:43], a[112:115], a[216:219], v[40:43]\n"
    "v_exp_f32 v25, v25\n"
    "v_mfma_f32_16x16x32_bf16 v[48:51], a[128:131], a[184:187], v[48:51]\n"
    "v_exp_f32 v54, v54\n"
    "v_mfma_f32_16x16x32_bf16 v[56:59], a[140:143], a[220:223], v[56:59]\n"
    "s_waitcnt lgkmcnt(1)\n"
    "ds_read_b128 a[212:215], v248 offset:14336\n"
    "v_cvt_pk_bf16_f32 v20, v20, v21\n"
    "v_mfma_f32_16x16x32_bf16 v[148:151], a[208:211], v[76:79], v[148:151]\n"
    "s_waitcnt vmcnt(0)\n"
    "ds_add_u32 v247, v246\n"
    "v_mfma_f32_16x16x32_bf16 v[64:67], a[148:151], a[180:183], v[64:67]\n"
    "ds_read_b128 a[192:195], v248 offset:0\n"
    "ds_read_b128 a[200:203], v248 offset:1024\n"
    "v_mfma_f32_16x16x32_bf16 v[48:51], a[124:127], a[180:183], v[48:51]\n"
    "v_exp_f32 v52, v52\n"
    "v_mfma_f32_16x16x32_bf16 v[128:131], a[204:207], v[72:75], v[128:131]\n"
    "v_exp_f32 v41, v41\n"
    "v_mfma_f32_16x16x32_bf16 v[144:147], a[204:207], v[80:83], v[144:147]\n"
    "v_exp_f32 v36, v36\n"
    "v_mfma_f32_16x16x32_bf16 v[44:47], a[124:127], a[252:255], v[44:47]\n"
    "v_exp_f32 v24, v24\n"
    "v_mfma_f32_16x16x32_bf16 v[60:63], a[148:151], a[252:255], v[60:63]\n"
    "v_exp_f32 v23, v23\n"
    "v_mfma_f32_16x16x32_bf16 v[44:47], a[128:131], a[160:163], v[44:47]\n"
    "v_exp_f32 v55, v55\n"
    "v_mfma_f32_16x16x32_bf16 v[48:51], a[120:123], a[176:179], v[48:51]\n"
    "v_exp_f32 v29, v29\n"
    "v_mfma_f32_16x16x32_bf16 v[64:67], a[144:147], a[176:179], v[64:67]\n"
    "ds_read_b128 a[236:239], v249 offset:37888\n"
    "v_cvt_pk_bf16_f32 v9, v38, v39\n"
    "v_mfma_f32_16x16x32_bf16 v[56:59], a[136:139], a[216:219], v[56:59]\n"
    "ds_read_b128 a[220:223], v248 offset:15360\n"
    "ds_read_b128 a[244:247], v249 offset:38912\n"
    "v_mfma_f32_16x16x32_bf16 v[48:51], a[116:119], a[172:175], v[48:51]\n"
    "s_waitcnt lgkmcnt(6)\n"
    "ds_read_b128 a[232:235], v249 offset:25600\n"
    "ds_read_b128 a[240:243], v249 offset:26624\n"
    "v_mfma_f32_16x16x32_bf16 v[64:67], a[140:143], a[172:175], v[64:67]\n"
    "s_add_u32 s59, s59, 4\n"
    "v_cvt_pk_bf16_f32 v7, v10, v11\n"
    "v_cvt_pk_bf16_f32 v13, v14, v15\n"
    "v_mfma_f32_16x16x32_bf16 v[148:151], a[212:215], v[80:83], v[148:151]\n"
    "v_exp_f32 v53, v53\n"
    "v_mfma_f32_16x16x32_bf16 v[44:47], a[132:135], a[164:167], v[44:47]\n"
    "ds_read_b32 v245, v247\n"
    "v_cvt_pk_bf16_f32 v11, v54, v55\n"
    "v_mfma_f32_16x16x32_bf16 v[60:63], a[152:155], a[160:163], v[60:63]\n"
    "ds_read_b128 v[240:243], v251 offset:1536\n"
    "ds_read_b128 a[216:219], v248 offset:3072\n"
    "v_mfma_f32_16x16x32_bf16 v[64:67], a[136:139], a[168:171], v[64:67]\n"
    "s_waitcnt lgkmcnt(8)\n"
    "v_cvt_pk_bf16_f32 v8, v36, v37\n"
    "v_cvt_pk_bf16_f32 v10, v52, v53\n"
    "v_mfma_f32_16x16x32_bf16 v[128:131], a[200:203], v[68:71], v[128:131]\n"
    "v_exp_f32 v43, v43\n"
    "v_mfma_f32_16x16x32_bf16 v[60:63], a[156:159], a[164:167], v[60:63]\n"
    "v_exp_f32 v45, v45\n"
    "v_mfma_f32_16x16x32_bf16 v[140:143], a[192:195], v[76:79], v[140:143]\n"
    "ds_read_b128 a[252:255], v249 offset:39936\n"
    "ds_read_b128 a[224:227], v249 offset:24576\n"
    "v_mfma_f32_16x16x32_bf16 v[124:127], a[192:195], v[68:71], v[124:127]\n"
    "s_waitcnt lgkmcnt(9)\n"
    "v_cvt_pk_bf16_f32 v14, v16, v17\n"
    "v_cvt_pk_bf16_f32 v21, v22, v23\n"
    "v_mfma_f32_16x16x32_bf16 v[128:131], a[236:239], v[208:211], v[128:131]\n"
    "v_exp_f32 v58, v58\n"
    "v_mfma_f32_16x16x32_bf16 v[140:143], a[196:199], v[80:83], v[140:143]\n"
    "s_waitcnt lgkmcnt(4)\n"
    "ds_read_b128 a[228:231], v249 offset:36864\n"
    "v_cmp_le_u32 s[56:57], s59, v245\n"
    "v_mfma_f32_16x16x32_bf16 v[124:127], a[196:199], v[72:75], v[124:127]\n"
    "v_exp_f32 v62, v62\n"
    "v_mfma_f32_16x16x32_bf16 v[148:151], a[240:243], v[212:215], v[148:151]\n"
    "s_cmp_eq_u64 s[56:57], -1\n"
    "v_exp_f32 v40, v40\n"
    "v_mfma_f32_16x16x32_bf16 v[132:135], a[208:211], v[68:71], v[132:135]\n"
    "s_waitcnt lgkmcnt(4)\n"
    "v_exp_f32 v42, v42\n"
    "v_smfmac_f32_16x16x64_bf16 v[108:111], v[240:243], v[4:11], v253\n"
    "s_cbranch_scc0 .Lpt3\n"
    ".Lpt3r:\n"
    "v_mfma_f32_16x16x32_bf16 v[48:51], a[112:115], a[168:171], v[48:51]\n"
    "s_waitcnt lgkmcnt(1)\n"
    "ds_read_b128 a[176:179], v249 offset:2048\n"
    "ds_read_b128 a[168:171], v249 offset:1024\n"
    "v_mfma_f32_16x16x32_bf16 v[124:127], a[224:227], v[204:207], v[124:127]\n"
    "v_exp_f32 v18, v18\n"
    "v_mfma_f32_16x16x32_bf16 v[136:139], a[220:223], v[72:75], v[136:139]\n"
    "v_cvt_pk_bf16_f32 v16, v40, v41\n"
    "v_cvt_pk_bf16_f32 v15, v18, v19\n"
    "v_mfma_f32_16x16x32_bf16 v[152:155], a[220:223], v[80:83], v[152:155]\n"
    "s_waitcnt lgkmcnt(2)\n"
    "v_exp_f32 v61, v61\n"
    "v_mfma_f32_16x16x32_bf16 v[124:127], a[228:231], v[208:211], v[124:127]\n"
    "v_exp_f32 v63, v63\n"
    "v_mov_b32 v111, v110\n"
    "v_mfma_f32_16x16x32_bf16 v[152:155], a[216:219], v[76:79], v[152:155]\n"
    "v_add_f32 v108, v108, v109\n"
    "v_cvt_pk_bf16_f32 v22, v24, v25\n"
    "v_mfma_f32_16x16x32_bf16 v[132:135], a[212:215], v[72:75], v[132:135]\n"
    "ds_read_b128 a[188:191], v249 offset:15360\n"
    "ds_read_b128 a[248:251], v249 offset:27648\n"
    "v_mfma_f32_16x16x32_bf16 v[136:139], a[216:219], v[68:71], v[136:139]\n"
    "ds_read_b128 a[172:175], v249 offset:13312\n"
    "ds_read_b128 a[180:183], v249 offset:14336\n"
    "v_mfma_f32_16x16x32_bf16 v[144:147], a[200:203], v[76:79], v[144:147]\n"
    "v_exp_f32 v35, v35\n"
    "v_mfma_f32_16x16x32_bf16 v[132:135], a[240:243], v[204:207], v[132:135]\n"
    "ds_read_b128 a[160:163], v249 offset:0\n"
    "v_mov_b32 v109, v108\n"
    "v_mfma_f32_16x16x32_bf16 v[136:139], a[252:255], v[208:211], v[136:139]\n"
    "ds_read_b128 a[164:167], v249 offset:12288\n"
    "ds_read_b128 a[184:187], v249 offset:3072\n"
    "v_mfma_f32_16x16x32_bf16 v[132:135], a[244:247], v[208:211], v[132:135]\n"
    "s_waitcnt lgkmcnt(8)\n"
    "v_exp_f32 v46, v46\n"
    "v_mfma_f32_16x16x32_bf16 v[132:135], a[176:179], v[4:7], v[132:135]\n"
    "v_exp_f32 v59, v59\n"
    "v_mfma_f32_16x16x32_bf16 v[144:147], a[236:239], v[216:219], v[144:147]\n"
    "v_exp_f32 v67, v67\n"
    "v_mfma_f32_16x16x32_bf16 v[144:147], a[232:235], v[212:215], v[144:147]\n"
    "v_permlane16_swap_b32 v108, v109\n"
    "v_mfma_f32_16x16x32_bf16 v[128:131], a[232:235], v[204:207], v[128:131]\n"
    "s_waitcnt lgkmcnt(3)\n"
    "v_exp_f32 v44, v44\n"
    "v_mfma_f32_16x16x32_bf16 v[132:135], a[180:183], v[8:11], v[132:135]\n"
    "v_cvt_pk_bf16_f32 v23, v26, v27\n"
    "v_cvt_pk_bf16_f32 v27, v62, v63\n"
    "v_mfma_f32_16x16x32_bf16 v[136:139], a[248:251], v[204:207], v[136:139]\n"
    "s_waitcnt lgkmcnt(2)\n"
    "v_exp_f32 v57, v57\n"
    "v_mfma_f32_16x16x32_bf16 v[124:127], a[160:163], v[4:7], v[124:127]\n"
    "v_exp_f32 v56, v56\n"
    "v_mfma_f32_16x16x32_bf16 v[140:143], a[224:227], v[212:215], v[140:143]\n"
    "s_waitcnt lgkmcnt(1)\n"
    "v_and_b32 v252, 0x70, v252\n"
    "v_add_f32 v108, v108, v109\n"
    "v_mfma_f32_16x16x32_bf16 v[124:127], a[164:167], v[8:11], v[124:127]\n"
    "v_permlane16_swap_b32 v110, v111\n"
    "v_mfma_f32_16x16x32_bf16 v[128:131], a[172:175], v[8:11], v[128:131]\n"
    "v_add_f32 v110, v110, v111\n"
    "v_cvt_pk_bf16_f32 v18, v56, v57\n"
    "v_mfma_f32_16x16x32_bf16 v[128:131], a[168:171], v[4:7], v[128:131]\n"
    ".Lwp1:\n"
    "s_cbranch_execz .Lwp2\n"
    "v_rcp_f32 v109, v108\n"
    "v_mfma_f32_16x16x32_bf16 v[136:139], a[188:191], v[8:11], v[136:139]\n"
    "s_waitcnt lgkmcnt(0)\n"
    "v_exp_f32 v60, v60\n"
    "v_mfma_f32_16x16x32_bf16 v[136:139], a[184:187], v[4:7], v[136:139]\n"
    "v_mul_f32 v111, -v110, v109\n"
    "v_cvt_pk_bf16_f32 v19, v58, v59\n"
    "v_cvt_pk_bf16_f32 v17, v42, v43\n"
    "v_xor_b32 v252, v252, v1\n"
    "v_ldexp_f32 v109, v109, s79\n"
    "v_pk_fma_f32 v[134:135], v[134:135], v[108:109], v[110:111] op_sel:[0,1,1] op_sel_hi:[1,1,1]\n"
    "v_pk_fma_f32 v[124:125], v[124:125], v[108:109], v[110:111] op_sel:[0,1,1] op_sel_hi:[1,1,1]\n"
    "v_pk_fma_f32 v[126:127], v[126:127], v[108:109], v[110:111] op_sel:[0,1,1] op_sel_hi:[1,1,1]\n"
    "v_pk_fma_f32 v[128:129], v[128:129], v[108:109], v[110:111] op_sel:[0,1,1] op_sel_hi:[1,1,1]\n"
    "v_pk_fma_f32 v[132:133], v[132:133], v[108:109], v[110:111] op_sel:[0,1,1] op_sel_hi:[1,1,1]\n"
    "v_pk_fma_f32 v[130:131], v[130:131], v[108:109], v[110:111] op_sel:[0,1,1] op_sel_hi:[1,1,1]\n"
    "v_pk_fma_f32 v[138:139], v[138:139], v[108:109], v[110:111] op_sel:[0,1,1] op_sel_hi:[1,1,1]\n"
    "v_pk_fma_f32 v[136:137], v[136:137], v[108:109], v[110:111] op_sel:[0,1,1] op_sel_hi:[1,1,1]\n"
    "v_cvt_pk_bf16_f32 v124, v124, v128\n"
    "v_cvt_pk_bf16_f32 v128, v126, v130\n"
    "v_mfma_f32_16x16x32_bf16 v[152:155], a[252:255], v[216:219], v[152:155]\n"
    "v_cvt_pk_bf16_f32 v130, v127, v131\n"
    "v_cvt_pk_bf16_f32 v126, v125, v129\n"
    "v_cvt_pk_bf16_f32 v127, v133, v137\n"
    "v_mfma_f32_16x16x32_bf16 v[148:151], a[244:247], v[216:219], v[148:151]\n"
    "v_add_u32 v252, s72, v252\n"
    "v_cvt_pk_bf16_f32 v125, v132, v136\n"
    "v_mfma_f32_16x16x32_bf16 v[140:143], a[228:231], v[216:219], v[140:143]\n"
    "ds_write_b128 v3, v[124:127] offset:0\n"
    "v_smfmac_f32_16x16x64_bf16 v[112:115], v[240:243], v[12:19], v253\n"
    "v_cvt_pk_bf16_f32 v129, v134, v138\n"
    "v_cvt_pk_bf16_f32 v26, v60, v61\n"
    "v_mfma_f32_16x16x32_bf16 v[148:151], a[176:179], v[12:15], v[148:151]\n"
    "v_exp_f32 v51, v51\n"
    "v_mfma_f32_16x16x32_bf16 v[140:143], a[160:163], v[12:15], v[140:143]\n"
    "v_cvt_pk_bf16_f32 v131, v135, v139\n"
    "v_xor_b32 v2, 64, v3\n"
    "v_mfma_f32_16x16x32_bf16 v[156:159], a[192:195], v[84:87], v[156:159]\n"
    "ds_write_b128 v2, v[128:131] offset:0\n"
    "v_mfma_f32_16x16x32_bf16 v[156:159], a[196:199], v[88:91], v[156:159]\n"
    "v_exp_f32 v47, v47\n"
    "v_mfma_f32_16x16x32_bf16 v[152:155], a[248:251], v[212:215], v[152:155]\n"
    "v_cvt_pk_bf16_f32 v24, v44, v45\n"
    "v_cvt_pk_bf16_f32 v25, v46, v47\n"
    "v_mfma_f32_16x16x32_bf16 v[156:159], a[224:227], v[220:223], v[156:159]\n"
    "ds_read_b128 v[132:135], v252 offset:0\n"
    "v_add_f32 v112, v112, v113\n"
    "v_mfma_f32_16x16x32_bf16 v[140:143], a[164:167], v[16:19], v[140:143]\n"
    "v_exp_f32 v34, v34\n"
    "v_mfma_f32_16x16x32_bf16 v[144:147], a[172:175], v[16:19], v[144:147]\n"
    "v_mov_b32 v113, v112\n"
    "v_mov_b32 v115, v114\n"
    "v_mfma_f32_16x16x32_bf16 v[144:147], a[168:171], v[12:15], v[144:147]\n"
    "ds_read_b128 v[136:139], v252 offset:1024\n"
    "v_permlane16_swap_b32 v112, v113\n"
    "v_mfma_f32_16x16x32_bf16 v[164:167], a[208:211], v[84:87], v[164:167]\n"
    "v_permlane16_swap_b32 v114, v115\n"
    "v_mfma_f32_16x16x32_bf16 v[152:155], a[188:191], v[16:19], v[152:155]\n"
    "v_exp_f32_e64 v48, v48\n"
    "v_mfma_f32_16x16x32_bf16 v[148:151], a[180:183], v[16:19], v[148:151]\n"
    "v_add_f32 v112, v112, v113\n"
    "v_cvt_pk_bf16_f32 v28, v28, v29\n"
    "v_mfma_f32_16x16x32_bf16 v[152:155], a[184:187], v[12:15], v[152:155]\n"
    "v_rcp_f32 v113, v112\n"
    "v_smfmac_f32_16x16x64_bf16 v[116:119], v[240:243], v[20:27], v253\n"
    "s_waitcnt lgkmcnt(1)\n"
    "buffer_store_dwordx4 v[132:135], v244, s[24:27], s75 offen offset:0 sc1\n"
    "v_add_f32 v114, v114, v115\n"
    "v_mul_f32 v115, -v114, v113\n"
    "v_ldexp_f32 v113, v113, s79\n"
    "v_pk_fma_f32 v[142:143], v[142:143], v[112:113], v[114:115] op_sel:[0,1,1] op_sel_hi:[1,1,1]\n"
    "v_pk_fma_f32 v[144:145], v[144:145], v[112:113], v[114:115] op_sel:[0,1,1] op_sel_hi:[1,1,1]\n"
    "v_pk_fma_f32 v[140:141], v[140:141], v[112:113], v[114:115] op_sel:[0,1,1] op_sel_hi:[1,1,1]\n"
    "v_pk_fma_f32 v[146:147], v[146:147], v[112:113], v[114:115] op_sel:[0,1,1] op_sel_hi:[1,1,1]\n"
    "v_pk_fma_f32 v[150:151], v[150:151], v[112:113], v[114:115] op_sel:[0,1,1] op_sel_hi:[1,1,1]\n"
    "v_add_f32_e64 v116, v116, v117\n"
    "v_pk_fma_f32 v[152:153], v[152:153], v[112:113], v[114:115] op_sel:[0,1,1] op_sel_hi:[1,1,1]\n"
    "v_pk_fma_f32 v[148:149], v[148:149], v[112:113], v[114:115] op_sel:[0,1,1] op_sel_hi:[1,1,1]\n"
    "v_pk_fma_f32 v[154:155], v[154:155], v[112:113], v[114:115] op_sel:[0,1,1] op_sel_hi:[1,1,1]\n"
    "v_cvt_pk_bf16_f32 v140, v140, v144\n"
    "v_cvt_pk_bf16_f32 v144, v142, v146\n"
    "v_mfma_f32_16x16x32_bf16 v[160:163], a[204:207], v[88:91], v[160:163]\n"
    "v_cvt_pk_bf16_f32 v146, v143, v147\n"
    "v_cvt_pk_bf16_f32 v142, v141, v145\n"
    "v_mfma_f32_16x16x32_bf16 v[156:159], a[228:231], v[224:227], v[156:159]\n"
    "v_cvt_pk_bf16_f32 v141, v148, v152\n"
    "v_cvt_pk_bf16_f32 v143, v149, v153\n"
    "v_mfma_f32_16x16x32_bf16 v[156:159], a[160:163], v[20:23], v[156:159]\n"
    "s_waitcnt lgkmcnt(0)\n"
    "buffer_store_dwordx4 v[136:139], v244, s[24:27], s75 offen offset:3072 sc1\n"
    "ds_write_b128 v3, v[140:143] offset:2048\n"
    "v_mfma_f32_16x16x32_bf16 v[172:175], a[192:195], v[92:95], v[172:175]\n"
    "v_mov_b32 v119, v118\n"
    "v_cvt_pk_bf16_f32 v145, v150, v154\n"
    "v_mfma_f32_16x16x32_bf16 v[156:159], a[164:167], v[24:27], v[156:159]\n"
    "v_cvt_pk_bf16_f32 v147, v151, v155\n"
    "v_mov_b32 v117, v116\n"
    "v_mfma_f32_16x16x32_bf16 v[180:183], a[208:211], v[92:95], v[180:183]\n"
    "ds_write_b128 v2, v[144:147] offset:2048\n"
    "v_mfma_f32_16x16x32_bf16 v[160:163], a[200:203], v[84:87], v[160:163]\n"
    "v_permlane16_swap_b32 v118, v119\n"
    "v_mfma_f32_16x16x32_bf16 v[168:171], a[220:223], v[88:91], v[168:171]\n"
    "v_exp_f32 v33, v33\n"
    "v_mfma_f32_16x16x32_bf16 v[172:175], a[196:199], v[96:99], v[172:175]\n"
    "ds_read_b128 v[148:151], v252 offset:2048\n"
    "ds_read_b128 v[152:155], v252 offset:3072\n"
    "v_mfma_f32_16x16x32_bf16 v[180:183], a[212:215], v[96:99], v[180:183]\n"
    "v_exp_f32 v49, v49\n"
    "v_mfma_f32_16x16x32_bf16 v[160:163], a[236:239], v[224:227], v[160:163]\n"
    "v_permlane16_swap_b32 v116, v117\n"
    "v_mfma_f32_16x16x32_bf16 v[168:171], a[216:219], v[84:87], v[168:171]\n"
    "v_exp_f32 v31, v31\n"
    "v_mfma_f32_16x16x32_bf16 v[176:179], a[204:207], v[96:99], v[176:179]\n"
    "v_exp_f32 v66, v66\n"
    "v_mfma_f32_16x16x32_bf16 v[164:167], a[212:215], v[88:91], v[164:167]\n"
    "ds_read_b128 a[204:207], v248 offset:17408\n"
    "v_add_f32 v118, v118, v119\n"
    "v_mfma_f32_16x16x32_bf16 v[160:163], a[232:235], v[220:223], v[160:163]\n"
    "v_cvt_pk_bf16_f32 v29, v30, v31\n"
    "v_add_f32 v116, v116, v117\n"
    "v_mfma_f32_16x16x32_bf16 v[168:171], a[252:255], v[224:227], v[168:171]\n"
    "v_rcp_f32 v117, v116\n"
    "v_mfma_f32_16x16x32_bf16 v[160:163], a[172:175], v[24:27], v[160:163]\n"
    "s_waitcnt lgkmcnt(1)\n"
    "buffer_store_dwordx4 v[152:155], v244, s[24:27], s76 offen offset:3072 sc1\n"
    "v_exp_f32 v50, v50\n"
    "v_exp_f32 v64, v64\n"
    "v_mfma_f32_16x16x32_bf16 v[176:179], a[200:203], v[92:95], v[176:179]\n"
    "ds_read_b128 a[196:199], v248 offset:16384\n"
    "v_cvt_pk_bf16_f32 v31, v34, v35\n"
    "v_mfma_f32_16x16x32_bf16 v[160:163], a[168:171], v[20:23], v[160:163]\n"
    "v_mul_f32 v119, -v118, v117\n"
    "v_cvt_pk_bf16_f32 v30, v32, v33\n"
    "v_mfma_f32_16x16x32_bf16 v[164:167], a[240:243], v[220:223], v[164:167]\n"
    "buffer_store_dwordx4 v[148:151], v244, s[24:27], s76 offen offset:0 sc1\n"
    "v_cvt_pk_bf16_f32 v35, v66, v67\n"
    "v_ldexp_f32 v117, v117, s79\n"
    "v_exp_f32 v65, v65\n"
    "v_mfma_f32_16x16x32_bf16 v[184:187], a[220:223], v[96:99], v[184:187]\n"
    "v_fma_f32 v156, v156, v117, v119\n"
    "v_fma_f32 v157, v157, v117, v119\n"
    "v_mfma_f32_16x16x32_bf16 v[168:171], a[248:251], v[220:223], v[168:171]\n"
    "v_fma_f32 v158, v158, v117, v119\n"
    "v_fma_f32 v159, v159, v117, v119\n"
    "v_mfma_f32_16x16x32_bf16 v[164:167], a[244:247], v[224:227], v[164:167]\n"
    "v_fma_f32 v160, v160, v117, v119\n"
    "v_fma_f32 v161, v161, v117, v119\n"
    "v_mfma_f32_16x16x32_bf16 v[168:171], a[188:191], v[24:27], v[168:171]\n"
    "v_fma_f32 v162, v162, v117, v119\n"
    "v_fma_f32 v163, v163, v117, v119\n"
    "v_mfma_f32_16x16x32_bf16 v[172:175], a[224:227], v[228:231], v[172:175]\n"
    "ds_read_b128 a[224:227], v249 offset:28672\n"
    "v_cvt_pk_bf16_f32 v34, v64, v65\n"
    "v_mfma_f32_16x16x32_bf16 v[168:171], a[184:187], v[20:23], v[168:171]\n"
    "v_cvt_pk_bf16_f32 v156, v156, v160\n"
    "v_cvt_pk_bf16_f32 v160, v158, v162\n"
    "v_mfma_f32_16x16x32_bf16 v[164:167], a[176:179], v[20:23], v[164:167]\n"
    "v_cvt_pk_bf16_f32 v162, v159, v163\n"
    "v_cvt_pk_bf16_f32 v32, v48, v49\n"
    "v_mfma_f32_16x16x32_bf16 v[164:167], a[180:183], v[24:27], v[164:167]\n"
    "ds_read_b128 a[192:195], v248 offset:4096\n"
    "v_cvt_pk_bf16_f32 v158, v157, v161\n"
    "v_mfma_f32_16x16x32_bf16 v[180:183], a[240:243], v[228:231], v[180:183]\n"
    "v_cvt_pk_bf16_f32 v33, v50, v51\n"
    "v_fma_f32 v168, v168, v117, v119\n"
    "v_fma_f32 v169, v169, v117, v119\n"
    "v_mfma_f32_16x16x32_bf16 v[184:187], a[216:219], v[92:95], v[184:187]\n"
    "v_fma_f32 v170, v170, v117, v119\n"
    "v_fma_f32 v171, v171, v117, v119\n"
    "v_smfmac_f32_16x16x64_bf16 v[120:123], v[240:243], v[28:35], v253\n"
    "ds_read_b128 a[220:223], v248 offset:19456\n"
    "v_mfma_f32_16x16x32_bf16 v[184:187], a[252:255], v[232:235], v[184:187]\n"
    "v_fma_f32 v164, v164, v117, v119\n"
    "v_fma_f32 v165, v165, v117, v119\n"
    "v_mfma_f32_16x16x32_bf16 v[172:175], a[228:231], v[232:235], v[172:175]\n"
    "v_cvt_pk_bf16_f32 v157, v164, v168\n"
    "v_cvt_pk_bf16_f32 v159, v165, v169\n"
    "v_mfma_f32_16x16x32_bf16 v[176:179], a[236:239], v[232:235], v[176:179]\n"
    "ds_write_b128 v3, v[156:159] offset:4096\n"
    "v_mfma_f32_16x16x32_bf16 v[180:183], a[244:247], v[232:235], v[180:183]\n"
    "s_waitcnt lgkmcnt(2)\n"
    "v_fma_f32 v166, v166, v117, v119\n"
    "v_fma_f32 v167, v167, v117, v119\n"
    "v_mfma_f32_16x16x32_bf16 v[188:191], a[192:195], v[68:71], v[188:191]\n"
    "v_cvt_pk_bf16_f32 v163, v167, v171\n"
    "v_cvt_pk_bf16_f32 v161, v166, v170\n"
    "v_mfma_f32_16x16x32_bf16 v[184:187], a[248:251], v[228:231], v[184:187]\n"
    "ds_write_b128 v2, v[160:163] offset:4096\n"
    "v_mfma_f32_16x16x32_bf16 v[192:195], a[204:207], v[72:75], v[192:195]\n"
    "v_add_f32_e64 v120, v120, v121\n"
    "v_mov_b32_e64 v121, v120\n"
    "v_mfma_f32_16x16x32_bf16 v[188:191], a[196:199], v[72:75], v[188:191]\n"
    "s_nop 0\n"
    "v_permlane16_swap_b32 v120, v121\n"
    "v_mfma_f32_16x16x32_bf16 v[176:179], a[232:235], v[228:231], v[176:179]\n"
    "s_waitcnt lgkmcnt(2)\n"
    "ds_read_b128 v[164:167], v252 offset:4096\n"
    "ds_read_b128 a[228:231], v249 offset:40960\n"
    "v_mfma_f32_16x16x32_bf16 v[200:203], a[220:223], v[72:75], v[200:203]\n"
    "ds_read_b128 a[212:215], v248 offset:18432\n"
    "ds_read_b128 v[168:171], v252 offset:5120\n"
    "v_mfma_f32_16x16x32_bf16 v[180:183], a[176:179], v[28:31], v[180:183]\n"
    "ds_read_b128 a[132:135], v249 offset:18432\n"
    "v_mov_b32 v123, v122\n"
    "v_mfma_f32_16x16x32_bf16 v[176:179], a[172:175], v[32:35], v[176:179]\n"
    "ds_read_b128 a[208:211], v248 offset:6144\n"
    "ds_read_b128 a[216:219], v248 offset:7168\n"
    "v_mfma_f32_16x16x32_bf16 v[188:191], a[224:227], v[204:207], v[188:191]\n"
    "ds_read_b128 a[248:251], v249 offset:31744\n"
    "ds_read_b128 a[236:239], v249 offset:41984\n"
    "v_mfma_f32_16x16x32_bf16 v[184:187], a[188:191], v[32:35], v[184:187]\n"
    "v_permlane16_swap_b32_e64 v122, v123\n"
    "v_mfma_f32_16x16x32_bf16 v[172:175], a[160:163], v[28:31], v[172:175]\n"
    "ds_read_b128 a[140:143], v249 offset:19456\n"
    "ds_read_b128 a[200:203], v248 offset:5120\n"
    "v_mfma_f32_16x16x32_bf16 v[180:183], a[180:183], v[32:35], v[180:183]\n"
    "ds_read_b128 a[136:139], v249 offset:7168\n"
    "ds_read_b128 a[124:127], v249 offset:17408\n"
    "v_mfma_f32_16x16x32_bf16 v[184:187], a[184:187], v[28:31], v[184:187]\n"
    "s_waitcnt lgkmcnt(12)\n"
    "buffer_store_dwordx4 v[164:167], v244, s[24:27], s77 offen offset:0 sc1\n"
    "ds_read_b128 a[240:243], v249 offset:30720\n"
    "ds_read_b128 a[252:255], v249 offset:44032\n"
    "v_add_f32 v122, v122, v123\n"
    "v_add_f32 v120, v120, v121\n"
    "v_mfma_f32_16x16x32_bf16 v[176:179], a[168:171], v[28:31], v[176:179]\n"
    "ds_read_b128 a[112:115], v249 offset:4096\n"
    "ds_read_b128 a[128:131], v249 offset:6144\n"
    "v_mfma_f32_16x16x32_bf16 v[172:175], a[164:167], v[32:35], v[172:175]\n"
    "s_waitcnt lgkmcnt(11)\n"
    "v_rcp_f32 v121, v120\n"
    "v_mfma_f32_16x16x32_bf16 v[196:199], a[208:211], v[68:71], v[196:199]\n"
    "ds_read_b128 a[244:247], v249 offset:43008\n"
    "v_mul_f32 v123, -v122, v121\n"
    "v_mfma_f32_16x16x32_bf16 v[188:191], a[228:231], v[208:211], v[188:191]\n"
    "buffer_store_dwordx4 v[168:171], v244, s[24:27], s77 offen offset:3072 sc1\n"
    "ds_read_b128 a[232:235], v249 offset:29696\n"
    "ds_read_b128 a[116:119], v249 offset:16384\n"
    "ds_read_b128 a[120:123], v249 offset:5120\n"
    "v_ldexp_f32 v121, v121, s79\n"
    "v_mfma_f32_16x16x32_bf16 v[196:199], a[212:215], v[72:75], v[196:199]\n"
    "s_waitcnt lgkmcnt(10)\n"
    "v_fma_f32 v174, v174, v121, v123\n"
    "v_fma_f32 v175, v175, v121, v123\n"
    "v_mfma_f32_16x16x32_bf16 v[192:195], a[200:203], v[68:71], v[192:195]\n"
    "v_fma_f32 v176, v176, v121, v123\n"
    "v_fma_f32 v177, v177, v121, v123\n"
    "v_mfma_f32_16x16x32_bf16 v[200:203], a[216:219], v[68:71], v[200:203]\n"
    "s_waitcnt lgkmcnt(7)\n"
    "v_fma_f32 v186, v186, v121, v123\n"
    "v_fma_f32 v187, v187, v121, v123\n"
    "v_mfma_f32_16x16x32_bf16 v[196:199], a[240:243], v[204:207], v[196:199]\n"
    "s_waitcnt lgkmcnt(6)\n"
    "v_fma_f32 v182, v182, v121, v123\n"
    "v_fma_f32 v183, v183, v121, v123\n"
    "v_mfma_f32_16x16x32_bf16 v[200:203], a[252:255], v[208:211], v[200:203]\n"
    "v_fma_f32 v178, v178, v121, v123\n"
    "v_fma_f32 v179, v179, v121, v123\n"
    "v_mfma_f32_16x16x32_bf16 v[200:203], a[248:251], v[204:207], v[200:203]\n"
    "s_waitcnt lgkmcnt(3)\n"
    "v_fma_f32 v172, v172, v121, v123\n"
    "v_fma_f32 v173, v173, v121, v123\n"
    "v_mfma_f32_16x16x32_bf16 v[196:199], a[244:247], v[208:211], v[196:199]\n"
    "v_cvt_pk_bf16_f32 v172, v172, v176\n"
    "v_cvt_pk_bf16_f32 v176, v174, v178\n"
    "v_mfma_f32_16x16x32_bf16 v[192:195], a[236:239], v[208:211], v[192:195]\n"
    "s_waitcnt lgkmcnt(2)\n"
    "v_cvt_pk_bf16_f32 v174, v173, v177\n"
    "v_cvt_pk_bf16_f32 v178, v175, v179\n"
    "v_mfma_f32_16x16x32_bf16 v[192:195], a[232:235], v[204:207], v[192:195]\n"
    "v_cvt_pk_bf16_f32 v177, v182, v186\n"
    "v_cvt_pk_bf16_f32 v179, v183, v187\n"
    "v_mfma_f32_16x16x32_bf16 v[196:199], a[128:131], v[4:7], v[196:199]\n"
    "v_fma_f32 v180, v180, v121, v123\n"
    "v_fma_f32 v181, v181, v121, v123\n"
    "v_mfma_f32_16x16x32_bf16 v[188:191], a[112:115], v[4:7], v[188:191]\n"
    "s_waitcnt lgkmcnt(1)\n"
    "ds_write_b128 v2, v[176:179] offset:0\n"
    "v_mfma_f32_16x16x32_bf16 v[188:191], a[116:119], v[8:11], v[188:191]\n"
    "v_fma_f32 v184, v184, v121, v123\n"
    "v_fma_f32 v185, v185, v121, v123\n"
    "v_mfma_f32_16x16x32_bf16 v[192:195], a[124:127], v[8:11], v[192:195]\n"
    "v_cvt_pk_bf16_f32 v175, v181, v185\n"
    "v_cvt_pk_bf16_f32 v173, v180, v184\n"
    "v_mfma_f32_16x16x32_bf16 v[196:199], a[132:135], v[8:11], v[196:199]\n"
    "ds_write_b128 v3, v[172:175] offset:0\n"
    "v_mfma_f32_16x16x32_bf16 a[4:7], a[204:207], v[80:83], a[4:7]\n"
    "v_fma_f32 v188, v188, v109, v111\n"
    "v_fma_f32 v189, v189, v109, v111\n"
    "v_mfma_f32_16x16x32_bf16 a[0:3], a[192:195], v[76:79], a[0:3]\n"
    "s_waitcnt lgkmcnt(2)\n"
    "v_fma_f32 v190, v190, v109, v111\n"
    "v_fma_f32 v191, v191, v109, v111\n"
    "v_mfma_f32_16x16x32_bf16 v[192:195], a[120:123], v[4:7], v[192:195]\n"
    "ds_read_b128 v[180:183], v252 offset:0\n"
    "ds_read_b128 v[184:187], v252 offset:1024\n"
    "v_mfma_f32_16x16x32_bf16 v[200:203], a[140:143], v[8:11], v[200:203]\n"
    "v_fma_f32 v198, v198, v109, v111\n"
    "v_fma_f32 v199, v199, v109, v111\n"
    "v_mfma_f32_16x16x32_bf16 a[0:3], a[196:199], v[80:83], a[0:3]\n"
    "v_fma_f32 v196, v196, v109, v111\n"
    "v_fma_f32 v197, v197, v109, v111\n"
    "v_mfma_f32_16x16x32_bf16 a[12:15], a[220:223], v[80:83], a[12:15]\n"
    "v_fma_f32 v192, v192, v109, v111\n"
    "v_fma_f32 v193, v193, v109, v111\n"
    "v_mfma_f32_16x16x32_bf16 a[4:7], a[200:203], v[76:79], a[4:7]\n"
    "ds_read_b128 a[164:167], v249 offset:20480\n"
    "ds_read_b128 a[180:183], v249 offset:22528\n"
    "v_mfma_f32_16x16x32_bf16 v[200:203], a[136:139], v[4:7], v[200:203]\n"
    "v_fma_f32 v194, v194, v109, v111\n"
    "v_fma_f32 v195, v195, v109, v111\n"
    "v_mfma_f32_16x16x32_bf16 a[0:3], a[224:227], v[212:215], a[0:3]\n"
    "ds_read_b128 a[172:175], v249 offset:21504\n"
    "v_mfma_f32_16x16x32_bf16 a[4:7], a[236:239], v[216:219], a[4:7]\n"
    "v_cvt_pk_bf16_f32 v188, v188, v192\n"
    "v_cvt_pk_bf16_f32 v192, v190, v194\n"
    "v_mfma_f32_16x16x32_bf16 a[0:3], a[228:231], v[216:219], a[0:3]\n"
    "s_waitcnt lgkmcnt(4)\n"
    "buffer_store_dwordx4 v[180:183], v244, s[24:27], s78 offen offset:0 sc1\n"
    "v_cvt_pk_bf16_f32 v190, v189, v193\n"
    "v_cvt_pk_bf16_f32 v194, v191, v195\n"
    "v_fma_f32 v200, v200, v109, v111\n"
    "v_fma_f32 v201, v201, v109, v111\n"
    "v_mfma_f32_16x16x32_bf16 a[0:3], a[112:115], v[12:15], a[0:3]\n"
    "v_cvt_pk_bf16_f32 v189, v196, v200\n"
    "v_cvt_pk_bf16_f32 v191, v197, v201\n"
    "v_mfma_f32_16x16x32_bf16 a[0:3], a[116:119], v[16:19], a[0:3]\n"
    "ds_read_b128 a[176:179], v249 offset:10240\n"
    "v_mfma_f32_16x16x32_bf16 a[4:7], a[232:235], v[212:215], a[4:7]\n"
    "s_waitcnt lgkmcnt(4)\n"
    "buffer_store_dwordx4 v[184:187], v244, s[24:27], s78 offen offset:3072 sc1\n"
    "ds_write_b128 v3, v[188:191] offset:2048\n"
    "v_mfma_f32_16x16x32_bf16 a[8:11], a[208:211], v[76:79], a[8:11]\n"
    "v_fma_f32 v202, v202, v109, v111\n"
    "v_fma_f32 v203, v203, v109, v111\n"
    "v_mfma_f32_16x16x32_bf16 a[12:15], a[216:219], v[76:79], a[12:15]\n"
    "v_cvt_pk_bf16_f32 v195, v199, v203\n"
    "v_cvt_pk_bf16_f32 v193, v198, v202\n"
    "v_mfma_f32_16x16x32_bf16 a[12:15], a[252:255], v[216:219], a[12:15]\n"
    "ds_write_b128 v2, v[192:195] offset:2048\n"
    "v_mfma_f32_16x16x32_bf16 a[4:7], a[124:127], v[16:19], a[4:7]\n"
    "v_accvgpr_read_b32 v40, a1\n"
    "v_mfma_f32_16x16x32_bf16 a[20:23], a[204:207], v[88:91], a[20:23]\n"
    "v_accvgpr_read_b32 v56, a3\n"
    "v_mov_b32_e64 v250, 0\n"
    "v_mfma_f32_16x16x32_bf16 a[4:7], a[120:123], v[12:15], a[4:7]\n"
    "ds_read_b128 v[200:203], v252 offset:3072\n"
    "ds_read_b128 v[196:199], v252 offset:2048\n"
    "v_mfma_f32_16x16x32_bf16 a[8:11], a[212:215], v[80:83], a[8:11]\n"
    "ds_read_b128 a[188:191], v249 offset:23552\n"
    "v_mfma_f32_16x16x32_bf16 a[8:11], a[240:243], v[212:215], a[8:11]\n"
    "v_accvgpr_read_b32 v36, a0\n"
    "v_accvgpr_read_b32 v52, a2\n"
    "v_mfma_f32_16x16x32_bf16 a[8:11], a[244:247], v[216:219], a[8:11]\n"
    "v_accvgpr_read_b32 v41, a5\n"
    "v_accvgpr_read_b32 v57, a7\n"
    "v_mfma_f32_16x16x32_bf16 a[8:11], a[128:131], v[12:15], a[8:11]\n"
    "v_accvgpr_read_b32 v37, a4\n"
    "v_accvgpr_read_b32 v53, a6\n"
    "v_mfma_f32_16x16x32_bf16 a[8:11], a[132:135], v[16:19], a[8:11]\n"
    "v_fma_f32 v40, v40, v113, v115\n"
    "v_fma_f32 v41, v41, v113, v115\n"
    "v_mfma_f32_16x16x32_bf16 a[12:15], a[248:251], v[212:215], a[12:15]\n"
    "ds_read_b128 a[160:163], v249 offset:8192\n"
    "v_mfma_f32_16x16x32_bf16 a[12:15], a[140:143], v[16:19], a[12:15]\n"
    "v_fma_f32 v52, v52, v113, v115\n"
    "v_fma_f32 v53, v53, v113, v115\n"
    "v_mfma_f32_16x16x32_bf16 a[12:15], a[136:139], v[12:15], a[12:15]\n"
    "s_waitcnt lgkmcnt(3)\n"
    "buffer_store_dwordx4 v[200:203], v244, s[24:27], s75 offen offset:3200 sc1\n"
    "v_cvt_pk_bf16_f32 v52, v52, v53\n"
    "v_fma_f32 v36, v36, v113, v115\n"
    "v_fma_f32 v37, v37, v113, v115\n"
    "v_accvgpr_read_b32 v38, a8\n"
    "v_mfma_f32_16x16x32_bf16 a[16:19], a[192:195], v[84:87], a[16:19]\n"
    "ds_read_b128 a[168:171], v249 offset:9216\n"
    "v_accvgpr_read_b32 v42, a9\n"
    "v_mfma_f32_16x16x32_bf16 a[16:19], a[196:199], v[88:91], a[16:19]\n"
    "v_accvgpr_read_b32 v54, a10\n"
    "v_accvgpr_read_b32 v39, a12\n"
    "v_mfma_f32_16x16x32_bf16 a[20:23], a[200:203], v[84:87], a[20:23]\n"
    "v_accvgpr_read_b32 v55, a14\n"
    "v_accvgpr_read_b32 v58, a11\n"
    "v_mfma_f32_16x16x32_bf16 a[16:19], a[224:227], v[220:223], a[16:19]\n"
    "v_accvgpr_read_b32 v59, a15\n"
    "v_cvt_pk_bf16_f32 v36, v36, v37\n"
    "v_mfma_f32_16x16x32_bf16 a[20:23], a[236:239], v[224:227], a[20:23]\n"
    "v_fma_f32 v38, v38, v113, v115\n"
    "v_fma_f32 v39, v39, v113, v115\n"
    "v_mfma_f32_16x16x32_bf16 a[24:27], a[208:211], v[84:87], a[24:27]\n"
    "v_fma_f32 v54, v54, v113, v115\n"
    "v_fma_f32 v55, v55, v113, v115\n"
    "v_mfma_f32_16x16x32_bf16 a[20:23], a[232:235], v[220:223], a[20:23]\n"
    "v_accvgpr_read_b32 v43, a13\n"
    "v_cvt_pk_bf16_f32 v53, v54, v55\n"
    "v_mfma_f32_16x16x32_bf16 a[20:23], a[124:127], v[24:27], a[20:23]\n"
    "v_fma_f32 v42, v42, v113, v115\n"
    "v_fma_f32 v43, v43, v113, v115\n"
    "v_mfma_f32_16x16x32_bf16 a[16:19], a[228:231], v[224:227], a[16:19]\n"
    "ds_read_b128 a[184:187], v249 offset:11264\n"
    "v_cvt_pk_bf16_f32 v37, v38, v39\n"
    "v_mfma_f32_16x16x32_bf16 a[20:23], a[120:123], v[20:23], a[20:23]\n"
    "v_cvt_pk_bf16_f32 v39, v42, v43\n"
    "v_cvt_pk_bf16_f32 v38, v40, v41\n"
    "v_mfma_f32_16x16x32_bf16 a[28:31], a[220:223], v[88:91], a[28:31]\n"
    "s_waitcnt lgkmcnt(4)\n"
    "buffer_store_dwordx4 v[196:199], v244, s[24:27], s75 offen offset:128 sc1\n"
    "ds_write_b128 v3, v[36:39] offset:4096\n"
    "v_mfma_f32_16x16x32_bf16 a[16:19], a[112:115], v[20:23], a[16:19]\n"
    "v_fma_f32 v58, v58, v113, v115\n"
    "v_fma_f32 v59, v59, v113, v115\n"
    "v_mfma_f32_16x16x32_bf16 a[16:19], a[116:119], v[24:27], a[16:19]\n"
    "v_fma_f32 v56, v56, v113, v115\n"
    "v_fma_f32 v57, v57, v113, v115\n"
    "v_cvt_pk_bf16_f32 v55, v58, v59\n"
    "v_mfma_f32_16x16x32_bf16 a[28:31], a[216:219], v[84:87], a[28:31]\n"
    "v_accvgpr_read_b32 v49, a21\n"
    "v_cvt_pk_bf16_f32 v54, v56, v57\n"
    "v_mfma_f32_16x16x32_bf16 a[24:27], a[212:215], v[88:91], a[24:27]\n"
    "ds_write_b128 v2, v[52:55] offset:4096\n"
    "v_mfma_f32_16x16x32_bf16 a[32:35], a[192:195], v[92:95], a[32:35]\n"
    "v_accvgpr_read_b32 v64, a19\n"
    "v_accvgpr_read_b32 v45, a20\n"
    "v_mfma_f32_16x16x32_bf16 a[24:27], a[240:243], v[220:223], a[24:27]\n"
    "v_accvgpr_read_b32 v44, a16\n"
    "v_accvgpr_read_b32 v65, a23\n"
    "v_mfma_f32_16x16x32_bf16 a[24:27], a[244:247], v[224:227], a[24:27]\n"
    "ds_read_b128 v[56:59], v252 offset:5120\n"
    "v_accvgpr_read_b32 v48, a17\n"
    "v_mfma_f32_16x16x32_bf16 a[28:31], a[252:255], v[224:227], a[28:31]\n"
    "v_fma_f32 v64, v64, v117, v119\n"
    "v_fma_f32 v65, v65, v117, v119\n"
    "v_mfma_f32_16x16x32_bf16 a[24:27], a[128:131], v[20:23], a[24:27]\n"
    "ds_read_b128 v[40:43], v252 offset:4096\n"
    "v_accvgpr_read_b32 v60, a18\n"
    "v_mfma_f32_16x16x32_bf16 a[24:27], a[132:135], v[24:27], a[24:27]\n"
    "v_fma_f32 v44, v44, v117, v119\n"
    "v_fma_f32 v45, v45, v117, v119\n"
    "v_mfma_f32_16x16x32_bf16 a[28:31], a[248:251], v[220:223], a[28:31]\n"
    "ds_read_b128 a[192:195], v248 offset:8192\n"
    "v_accvgpr_read_b32 v61, a22\n"
    "v_mfma_f32_16x16x32_bf16 a[28:31], a[140:143], v[24:27], a[28:31]\n"
    "v_fma_f32 v48, v48, v117, v119\n"
    "v_fma_f32 v49, v49, v117, v119\n"
    "v_mfma_f32_16x16x32_bf16 a[28:31], a[136:139], v[20:23], a[28:31]\n"
    "v_fma_f32 v60, v60, v117, v119\n"
    "v_fma_f32 v61, v61, v117, v119\n"
    "v_mfma_f32_16x16x32_bf16 a[40:43], a[208:211], v[92:95], a[40:43]\n"
    "v_accvgpr_read_b32 v62, a26\n"
    "v_accvgpr_read_b32 v66, a27\n"
    "v_mfma_f32_16x16x32_bf16 a[36:39], a[204:207], v[96:99], a[36:39]\n"
    "s_waitcnt lgkmcnt(2)\n"
    "buffer_store_dwordx4 v[56:59], v244, s[24:27], s76 offen offset:3200 sc1\n"
    "v_accvgpr_read_b32 v46, a24\n"
    "v_accvgpr_read_b32 v63, a30\n"
    "v_accvgpr_read_b32 v47, a28\n"
    "v_accvgpr_read_b32 v51, a29\n"
    "v_mfma_f32_16x16x32_bf16 a[40:43], a[212:215], v[96:99], a[40:43]\n"
    "v_fma_f32 v62, v62, v117, v119\n"
    "v_fma_f32 v63, v63, v117, v119\n"
    "v_mfma_f32_16x16x32_bf16 a[44:47], a[220:223], v[96:99], a[44:47]\n"
    "v_accvgpr_read_b32 v50, a25\n"
    "v_cvt_pk_bf16_f32 v44, v44, v45\n"
    "v_cvt_pk_bf16_f32 v60, v60, v61\n"
    "v_mfma_f32_16x16x32_bf16 a[36:39], a[200:203], v[92:95], a[36:39]\n"
    "v_fma_f32 v50, v50, v117, v119\n"
    "v_fma_f32 v51, v51, v117, v119\n"
    "v_mfma_f32_16x16x32_bf16 a[44:47], a[216:219], v[92:95], a[44:47]\n"
    "v_fma_f32 v46, v46, v117, v119\n"
    "v_fma_f32 v47, v47, v117, v119\n"
    "v_mfma_f32_16x16x32_bf16 a[36:39], a[236:239], v[232:235], a[36:39]\n"
    "v_cvt_pk_bf16_f32 v61, v62, v63\n"
    "v_cvt_pk_bf16_f32 v45, v46, v47\n"
    "v_mfma_f32_16x16x32_bf16 a[40:43], a[240:243], v[228:231], a[40:43]\n"
    "v_accvgpr_read_b32 v67, a31\n"
    "v_cvt_pk_bf16_f32 v47, v50, v51\n"
    "v_mfma_f32_16x16x32_bf16 a[36:39], a[232:235], v[228:231], a[36:39]\n"
    "ds_read_b128 a[208:211], v248 offset:10240\n"
    "v_cvt_pk_bf16_f32 v46, v48, v49\n"
    "v_mfma_f32_16x16x32_bf16 a[36:39], a[124:127], v[32:35], a[36:39]\n"
    "s_waitcnt lgkmcnt(2)\n"
    "buffer_store_dwordx4 v[40:43], v244, s[24:27], s76 offen offset:128 sc1\n"
    "ds_write_b128 v3, v[44:47] offset:0\n"
    "v_mfma_f32_16x16x32_bf16 a[36:39], a[120:123], v[28:31], a[36:39]\n"
    "v_fma_f32 v66, v66, v117, v119\n"
    "v_fma_f32 v67, v67, v117, v119\n"
    "v_mfma_f32_16x16x32_bf16 a[40:43], a[244:247], v[232:235], a[40:43]\n"
    "v_cvt_pk_bf16_f32 v62, v64, v65\n"
    "v_cvt_pk_bf16_f32 v63, v66, v67\n"
    "v_mfma_f32_16x16x32_bf16 a[44:47], a[252:255], v[232:235], a[44:47]\n"
    "ds_write_b128 v2, v[60:63] offset:0\n"
    "v_mfma_f32_16x16x32_bf16 a[32:35], a[196:199], v[96:99], a[32:35]\n"
    "v_accvgpr_read_b32 v125, a36\n"
    "v_fma_f32 v250, |v110|, 4.0, v250\n"
    "v_mfma_f32_16x16x32_bf16 a[44:47], a[248:251], v[228:231], a[44:47]\n"
    "v_fma_f32 v250, v108, 4.0, v250\n"
    "v_accvgpr_read_b32 v137, a39\n"
    "v_mfma_f32_16x16x32_bf16 a[32:35], a[224:227], v[228:231], a[32:35]\n"
    "ds_read_b128 a[236:239], v249 offset:46080\n"
    "ds_read_b128 v[48:51], v252 offset:0\n"
    "ds_read_b128 a[212:215], v248 offset:22528\n"
    "v_mfma_f32_16x16x32_bf16 a[32:35], a[228:231], v[232:235], a[32:35]\n"
    "ds_read_b128 a[224:227], v249 offset:32768\n"
    "v_accvgpr_read_b32 v133, a38\n"
    "v_mfma_f32_16x16x32_bf16 a[40:43], a[128:131], v[28:31], a[40:43]\n"
    "ds_read_b128 a[204:207], v248 offset:21504\n"
    "v_mfma_f32_16x16x32_bf16 a[44:47], a[140:143], v[32:35], a[44:47]\n"
    "ds_read_b128 v[64:67], v252 offset:1024\n"
    "ds_read_b128 a[240:243], v249 offset:34816\n"
    "v_mfma_f32_16x16x32_bf16 a[32:35], a[112:115], v[28:31], a[32:35]\n"
    "ds_read_b128 a[216:219], v248 offset:11264\n"
    "ds_read_b128 a[196:199], v248 offset:20480\n"
    "v_mfma_f32_16x16x32_bf16 a[32:35], a[116:119], v[32:35], a[32:35]\n"
    "s_waitcnt lgkmcnt(12)\n"
    "ds_read_b128 a[244:247], v249 offset:47104\n"
    "ds_read_b128 a[200:203], v248 offset:9216\n"
    "v_mfma_f32_16x16x32_bf16 a[48:51], a[192:195], v[68:71], a[48:51]\n"
    "ds_read_b128 a[232:235], v249 offset:33792\n"
    "ds_read_b128 a[252:255], v249 offset:48128\n"
    "v_mfma_f32_16x16x32_bf16 a[40:43], a[132:135], v[32:35], a[40:43]\n"
    "s_nop 0\n"
    "v_accvgpr_read_b32 v124, a32\n"
    "v_mfma_f32_16x16x32_bf16 a[44:47], a[136:139], v[28:31], a[44:47]\n"
    "s_waitcnt lgkmcnt(11)\n"
    "buffer_store_dwordx4 v[48:51], v244, s[24:27], s77 offen offset:128 sc1\n"
    "v_accvgpr_read_b32 v128, a33\n"
    "v_accvgpr_read_b32 v132, a34\n"
    "v_fma_f32 v132, v132, v121, v123\n"
    "v_fma_f32 v133, v133, v121, v123\n"
    "v_accvgpr_read_b32 v134, a42\n"
    "v_mfma_f32_16x16x32_bf16 a[56:59], a[208:211], v[68:71], a[56:59]\n"
    "s_waitcnt lgkmcnt(8)\n"
    "v_accvgpr_read_b32 v129, a37\n"
    "v_accvgpr_read_b32 v131, a45\n"
    "v_mfma_f32_16x16x32_bf16 a[52:55], a[204:207], v[72:75], a[52:55]\n"
    "v_accvgpr_read_b32 v130, a41\n"
    "v_accvgpr_read_b32 v138, a43\n"
    "v_mfma_f32_16x16x32_bf16 a[56:59], a[212:215], v[72:75], a[56:59]\n"
    "s_waitcnt lgkmcnt(4)\n"
    "v_accvgpr_read_b32 v135, a46\n"
    "v_fma_f32 v124, v124, v121, v123\n"
    "v_fma_f32 v125, v125, v121, v123\n"
    "v_mfma_f32_16x16x32_bf16 a[48:51], a[196:199], v[72:75], a[48:51]\n"
    "s_waitcnt lgkmcnt(2)\n"
    "ds_read_b128 a[228:231], v249 offset:45056\n"
    "ds_read_b128 a[248:251], v249 offset:35840\n"
    "v_mfma_f32_16x16x32_bf16 a[52:55], a[200:203], v[68:71], a[52:55]\n"
    ".Lwp2:\n"
    "s_cbranch_execz .Lw3\n"
    "v_fma_f32 v130, v130, v121, v123\n"
    "v_fma_f32 v131, v131, v121, v123\n"
    "v_mfma_f32_16x16x32_bf16 a[56:59], a[240:243], v[204:207], a[56:59]\n"
    "v_accvgpr_read_b32 v126, a40\n"
    "v_fma_f32 v134, v134, v121, v123\n"
    "v_fma_f32 v135, v135, v121, v123\n"
    "v_mfma_f32_16x16x32_bf16 a[48:51], a[224:227], v[204:207], a[48:51]\n"
    "ds_read_b128 a[220:223], v248 offset:23552\n"
    "v_accvgpr_read_b32 v139, a47\n"
    "v_mfma_f32_16x16x32_bf16 a[56:59], a[244:247], v[208:211], a[56:59]\n"
    "buffer_store_dwordx4 v[64:67], v244, s[24:27], s77 offen offset:3200 sc1\n"
    "v_accvgpr_read_b32 v136, a35\n"
    "v_accvgpr_read_b32 v127, a44\n"
    "v_fma_f32 v138, v138, v121, v123\n"
    "v_fma_f32 v139, v139, v121, v123\n"
    "v_mfma_f32_16x16x32_bf16 a[56:59], a[176:179], v[4:7], a[56:59]\n"
    "v_fma_f32 v126, v126, v121, v123\n"
    "v_fma_f32 v127, v127, v121, v123\n"
    "v_mfma_f32_16x16x32_bf16 a[56:59], a[180:183], v[8:11], a[56:59]\n"
    "v_cvt_pk_bf16_f32 v124, v124, v125\n"
    "v_cvt_pk_bf16_f32 v125, v126, v127\n"
    "v_mfma_f32_16x16x32_bf16 a[52:55], a[236:239], v[208:211], a[52:55]\n"
    "s_waitcnt lgkmcnt(2)\n"
    "v_fma_f32 v128, v128, v121, v123\n"
    "v_fma_f32 v129, v129, v121, v123\n"
    "v_mfma_f32_16x16x32_bf16 a[48:51], a[228:231], v[208:211], a[48:51]\n"
    "v_cvt_pk_bf16_f32 v132, v132, v133\n"
    "v_cvt_pk_bf16_f32 v126, v128, v129\n"
    "v_mfma_f32_16x16x32_bf16 a[52:55], a[232:235], v[204:207], a[52:55]\n"
    "v_cvt_pk_bf16_f32 v133, v134, v135\n"
    "v_cvt_pk_bf16_f32 v127, v130, v131\n"
    "v_mfma_f32_16x16x32_bf16 a[52:55], a[172:175], v[8:11], a[52:55]\n"
    "s_waitcnt lgkmcnt(0)\n"
    "ds_write_b128 v3, v[124:127] offset:2048\n"
    "v_mfma_f32_16x16x32_bf16 a[60:63], a[220:223], v[72:75], a[60:63]\n"
    "v_fma_f32 v136, v136, v121, v123\n"
    "v_fma_f32 v137, v137, v121, v123\n"
    "v_mfma_f32_16x16x32_bf16 a[52:55], a[168:171], v[4:7], a[52:55]\n"
    "v_cvt_pk_bf16_f32 v135, v138, v139\n"
    "v_cvt_pk_bf16_f32 v134, v136, v137\n"
    "v_mfma_f32_16x16x32_bf16 a[60:63], a[216:219], v[68:71], a[60:63]\n"
    "ds_write_b128 v2, v[132:135] offset:2048\n"
    "v_mfma_f32_16x16x32_bf16 a[48:51], a[160:163], v[4:7], a[48:51]\n"
    "v_accvgpr_read_b32 v146, a57\n"
    "v_mfma_f32_16x16x32_bf16 a[48:51], a[164:167], v[8:11], a[48:51]\n"
    "ds_read_b128 v[128:131], v252 offset:2048\n"
    "v_mfma_f32_16x16x32_bf16 a[60:63], a[252:255], v[208:211], a[60:63]\n"
    "v_accvgpr_read_b32 v141, a52\n"
    "v_accvgpr_read_b32 v153, a55\n"
    "v_mfma_f32_16x16x32_bf16 a[64:67], a[192:195], v[76:79], a[64:67]\n"
    "v_accvgpr_read_b32 v149, a54\n"
    "v_accvgpr_read_b32 v150, a58\n"
    "v_mfma_f32_16x16x32_bf16 a[60:63], a[248:251], v[204:207], a[60:63]\n"
    "ds_read_b128 v[136:139], v252 offset:3072\n"
    "v_accvgpr_read_b32 v142, a56\n"
    "v_mfma_f32_16x16x32_bf16 a[60:63], a[188:191], v[8:11], a[60:63]\n"
    "v_accvgpr_read_b32 v145, a53\n"
    "v_accvgpr_read_b32 v148, a50\n"
    "v_mfma_f32_16x16x32_bf16 a[60:63], a[184:187], v[4:7], a[60:63]\n"
    "v_accvgpr_read_b32 v154, a59\n"
    "v_accvgpr_read_b32 v140, a48\n"
    "v_mfma_f32_16x16x32_bf16 a[64:67], a[196:199], v[80:83], a[64:67]\n"
    "v_fma_f32 v140, v140, v109, v111\n"
    "v_fma_f32 v141, v141, v109, v111\n"
    "v_mfma_f32_16x16x32_bf16 a[68:71], a[204:207], v[80:83], a[68:71]\n"
    "s_nop 1\n"
    "v_accvgpr_read_b32 v143, a60\n"
    "v_mfma_f32_16x16x32_bf16 a[68:71], a[200:203], v[76:79], a[68:71]\n"
    "s_waitcnt lgkmcnt(1)\n"
    "buffer_store_dwordx4 v[128:131], v244, s[24:27], s78 offen offset:128 sc1\n"
    "v_fma_f32 v148, v148, v109, v111\n"
    "v_fma_f32 v149, v149, v109, v111\n"
    "v_accvgpr_read_b32 v144, a49\n"
    "v_cvt_pk_bf16_f32 v148, v148, v149\n"
    "v_mfma_f32_16x16x32_bf16 a[76:79], a[220:223], v[80:83], a[76:79]\n"
    "v_cvt_pk_bf16_f32 v140, v140, v141\n"
    "v_accvgpr_read_b32 v152, a51\n"
    "v_mfma_f32_16x16x32_bf16 a[76:79], a[216:219], v[76:79], a[76:79]\n"
    "v_fma_f32 v142, v142, v109, v111\n"
    "v_fma_f32 v143, v143, v109, v111\n"
    "v_mfma_f32_16x16x32_bf16 a[64:67], a[224:227], v[212:215], a[64:67]\n"
    "v_fma_f32 v144, v144, v109, v111\n"
    "v_fma_f32 v145, v145, v109, v111\n"
    "v_accvgpr_read_b32 v151, a62\n"
    "v_mfma_f32_16x16x32_bf16 a[68:71], a[236:239], v[216:219], a[68:71]\n"
    "v_accvgpr_read_b32 v155, a63\n"
    "s_nop 0\n"
    "v_mfma_f32_16x16x32_bf16 a[68:71], a[232:235], v[212:215], a[68:71]\n"
    "s_waitcnt lgkmcnt(0)\n"
    "buffer_store_dwordx4 v[136:139], v244, s[24:27], s78 offen offset:3200 sc1\n"
    "v_fma_f32 v150, v150, v109, v111\n"
    "v_fma_f32 v151, v151, v109, v111\n"
    "v_cvt_pk_bf16_f32 v141, v142, v143\n"
    "v_accvgpr_read_b32 v147, a61\n"
    "v_mfma_f32_16x16x32_bf16 a[72:75], a[208:211], v[76:79], a[72:75]\n"
    "v_fma_f32 v146, v146, v109, v111\n"
    "v_fma_f32 v147, v147, v109, v111\n"
    "v_mfma_f32_16x16x32_bf16 a[64:67], a[228:231], v[216:219], a[64:67]\n"
    "v_mfma_f32_16x16x32_bf16 a[68:71], a[172:175], v[16:19], a[68:71]\n"
    "v_fma_f32 v154, v154, v109, v111\n"
    "v_fma_f32 v155, v155, v109, v111\n"
    "v_mfma_f32_16x16x32_bf16 a[64:67], a[160:163], v[12:15], a[64:67]\n"
    "v_cvt_pk_bf16_f32 v149, v150, v151\n"
    "v_cvt_pk_bf16_f32 v142, v144, v145\n"
    "v_cvt_pk_bf16_f32 v143, v146, v147\n"
    "v_mfma_f32_16x16x32_bf16 a[68:71], a[168:171], v[12:15], a[68:71]\n"
    "ds_write_b128 v3, v[140:143] offset:4096\n"
    "v_mfma_f32_16x16x32_bf16 a[72:75], a[212:215], v[80:83], a[72:75]\n"
    "v_fma_f32 v152, v152, v109, v111\n"
    "v_fma_f32 v153, v153, v109, v111\n"
    "v_mfma_f32_16x16x32_bf16 a[72:75], a[240:243], v[212:215], a[72:75]\n"
    "v_cvt_pk_bf16_f32 v150, v152, v153\n"
    "v_cvt_pk_bf16_f32 v151, v154, v155\n"
    "v_mfma_f32_16x16x32_bf16 a[92:95], a[220:223], v[88:91], a[92:95]\n"
    "ds_write_b128 v2, v[148:151] offset:4096\n"
    "v_mfma_f32_16x16x32_bf16 a[92:95], a[216:219], v[84:87], a[92:95]\n"
    "v_accvgpr_read_b32 v161, a69\n"
    "v_fma_f32 v250, v109, s74, v250\n"
    "v_mfma_f32_16x16x32_bf16 a[64:67], a[164:167], v[16:19], a[64:67]\n"
    "v_fma_f32 v250, |v114|, 4.0, v250\n"
    "v_mfma_f32_16x16x32_bf16 a[72:75], a[244:247], v[216:219], a[72:75]\n"
    "ds_read_b128 v[144:147], v252 offset:4096\n"
    "v_mfma_f32_16x16x32_bf16 a[72:75], a[176:179], v[12:15], a[72:75]\n"
    "ds_read_b128 v[152:155], v252 offset:5120\n"
    "v_accvgpr_read_b32 v165, a70\n"
    "v_mfma_f32_16x16x32_bf16 a[72:75], a[180:183], v[16:19], a[72:75]\n"
    "s_nop 0\n"
    "v_accvgpr_read_b32 v156, a64\n"
    "v_accvgpr_read_b32 v160, a65\n"
    "v_mfma_f32_16x16x32_bf16 a[76:79], a[252:255], v[216:219], a[76:79]\n"
    "v_fma_f32 v160, v160, v113, v115\n"
    "v_fma_f32 v161, v161, v113, v115\n"
    "v_mfma_f32_16x16x32_bf16 a[84:87], a[204:207], v[88:91], a[84:87]\n"
    "s_nop 0\n"
    "v_accvgpr_read_b32 v162, a73\n"
    "v_accvgpr_read_b32 v169, a71\n"
    "v_mfma_f32_16x16x32_bf16 a[80:83], a[192:195], v[84:87], a[80:83]\n"
    "v_fma_f32 v250, v112, 4.0, v250\n"
    "s_nop 0\n"
    "v_mfma_f32_16x16x32_bf16 a[76:79], a[248:251], v[212:215], a[76:79]\n"
    "v_accvgpr_read_b32 v166, a74\n"
    "v_accvgpr_read_b32 v157, a68\n"
    "v_mfma_f32_16x16x32_bf16 a[76:79], a[188:191], v[16:19], a[76:79]\n"
    "v_accvgpr_read_b32 v164, a66\n"
    "v_accvgpr_read_b32 v168, a67\n"
    "v_mfma_f32_16x16x32_bf16 a[76:79], a[184:187], v[12:15], a[76:79]\n"
    "s_waitcnt lgkmcnt(1)\n"
    "buffer_store_dwordx4 v[144:147], v244, s[24:27], s75 offen offset:256 sc1\n"
    "v_fma_f32 v164, v164, v113, v115\n"
    "v_fma_f32 v165, v165, v113, v115\n"
    "v_accvgpr_read_b32 v158, a72\n"
    "v_fma_f32 v156, v156, v113, v115\n"
    "v_fma_f32 v157, v157, v113, v115\n"
    "v_mfma_f32_16x16x32_bf16 a[80:83], a[196:199], v[88:91], a[80:83]\n"
    "v_accvgpr_read_b32 v167, a78\n"
    "v_cvt_pk_bf16_f32 v156, v156, v157\n"
    "v_fma_f32 v250, v113, s74, v250\n"
    "v_mfma_f32_16x16x32_bf16 a[88:91], a[208:211], v[84:87], a[88:91]\n"
    "s_nop 0\n"
    "v_accvgpr_read_b32 v159, a76\n"
    "v_fma_f32 v166, v166, v113, v115\n"
    "v_fma_f32 v167, v167, v113, v115\n"
    "v_mfma_f32_16x16x32_bf16 a[84:87], a[200:203], v[84:87], a[84:87]\n"
    "s_waitcnt lgkmcnt(0)\n"
    "buffer_store_dwordx4 v[152:155], v244, s[24:27], s75 offen offset:3328 sc1\n"
    "v_fma_f32 v158, v158, v113, v115\n"
    "v_fma_f32 v159, v159, v113, v115\n"
    "v_accvgpr_read_b32 v170, a75\n"
    "v_accvgpr_read_b32 v171, a79\n"
    "v_mfma_f32_16x16x32_bf16 a[84:87], a[236:239], v[224:227], a[84:87]\n"
    "v_cvt_pk_bf16_f32 v157, v158, v159\n"
    "v_cvt_pk_bf16_f32 v158, v160, v161\n"
    "v_mfma_f32_16x16x32_bf16 a[84:87], a[232:235], v[220:223], a[84:87]\n"
    "v_cvt_pk_bf16_f32 v164, v164, v165\n"
    "v_accvgpr_read_b32 v163, a77\n"
    "v_mfma_f32_16x16x32_bf16 a[80:83], a[224:227], v[220:223], a[80:83]\n"
    "v_fma_f32 v162, v162, v113, v115\n"
    "v_fma_f32 v163, v163, v113, v115\n"
    "v_mfma_f32_16x16x32_bf16 a[88:91], a[212:215], v[88:91], a[88:91]\n"
    "v_cvt_pk_bf16_f32 v159, v162, v163\n"
    "v_cvt_pk_bf16_f32 v165, v166, v167\n"
    "v_mfma_f32_16x16x32_bf16 a[84:87], a[172:175], v[24:27], a[84:87]\n"
    "ds_write_b128 v3, v[156:159] offset:0\n"
    "v_mfma_f32_16x16x32_bf16 a[84:87], a[168:171], v[20:23], a[84:87]\n"
    "v_fma_f32 v168, v168, v113, v115\n"
    "v_fma_f32 v169, v169, v113, v115\n"
    "v_mfma_f32_16x16x32_bf16 a[92:95], a[252:255], v[224:227], a[92:95]\n"
    "v_fma_f32 v170, v170, v113, v115\n"
    "v_fma_f32 v171, v171, v113, v115\n"
    "v_fma_f32 v250, |v118|, 4.0, v250\n"
    "v_cvt_pk_bf16_f32 v167, v170, v171\n"
    "v_cvt_pk_bf16_f32 v166, v168, v169\n"
    "v_mfma_f32_16x16x32_bf16 a[80:83], a[228:231], v[224:227], a[80:83]\n"
    "ds_write_b128 v2, v[164:167] offset:0\n"
    "v_mfma_f32_16x16x32_bf16 a[80:83], a[160:163], v[20:23], a[80:83]\n"
    "v_mfma_f32_16x16x32_bf16 a[92:95], a[248:251], v[220:223], a[92:95]\n"
    "ds_read_b128 v[160:163], v252 offset:0\n"
    "v_mfma_f32_16x16x32_bf16 a[80:83], a[164:167], v[24:27], a[80:83]\n"
    "ds_read_b128 v[168:171], v252 offset:1024\n"
    "v_accvgpr_read_b32 v173, a84\n"
    "v_mfma_f32_16x16x32_bf16 a[92:95], a[188:191], v[24:27], a[92:95]\n"
    "v_accvgpr_read_b32 v177, a85\n"
    "v_mfma_f32_16x16x32_bf16 a[88:91], a[240:243], v[220:223], a[88:91]\n"
    "v_mfma_f32_16x16x32_bf16 a[88:91], a[244:247], v[224:227], a[88:91]\n"
    "s_nop 1\n"
    "v_accvgpr_read_b32 v176, a81\n"
    "v_mfma_f32_16x16x32_bf16 a[92:95], a[184:187], v[20:23], a[92:95]\n"
    "v_accvgpr_read_b32 v172, a80\n"
    "v_accvgpr_read_b32 v184, a83\n"
    "v_mfma_f32_16x16x32_bf16 a[88:91], a[176:179], v[20:23], a[88:91]\n"
    "v_fma_f32 v172, v172, v117, v119\n"
    "v_fma_f32 v173, v173, v117, v119\n"
    "v_mfma_f32_16x16x32_bf16 a[88:91], a[180:183], v[24:27], a[88:91]\n"
    "v_fma_f32 v176, v176, v117, v119\n"
    "v_fma_f32 v177, v177, v117, v119\n"
    "v_mfma_f32_16x16x32_bf16 a[96:99], a[192:195], v[92:95], a[96:99]\n"
    "s_waitcnt lgkmcnt(1)\n"
    "buffer_store_dwordx4 v[160:163], v244, s[24:27], s76 offen offset:256 sc1\n"
    "v_accvgpr_read_b32 v175, a92\n"
    "v_accvgpr_read_b32 v180, a82\n"
    "v_accvgpr_read_b32 v181, a86\n"
    "v_cvt_pk_bf16_f32 v172, v172, v173\n"
    "v_mfma_f32_16x16x32_bf16 a[108:111], a[220:223], v[96:99], a[108:111]\n"
    "v_accvgpr_read_b32 v179, a93\n"
    "v_accvgpr_read_b32 v174, a88\n"
    "v_mfma_f32_16x16x32_bf16 a[108:111], a[216:219], v[92:95], a[108:111]\n"
    "v_fma_f32 v250, v116, 4.0, v250\n"
    "v_accvgpr_read_b32 v183, a94\n"
    "v_mfma_f32_16x16x32_bf16 a[96:99], a[196:199], v[96:99], a[96:99]\n"
    "v_accvgpr_read_b32 v187, a95\n"
    "v_accvgpr_read_b32 v178, a89\n"
    "v_mfma_f32_16x16x32_bf16 a[100:103], a[204:207], v[96:99], a[100:103]\n"
    "v_mfma_f32_16x16x32_bf16 a[104:107], a[208:211], v[92:95], a[104:107]\n"
    "s_nop 0\n"
    "v_fma_f32 v174, v174, v117, v119\n"
    "v_fma_f32 v175, v175, v117, v119\n"
    "v_mfma_f32_16x16x32_bf16 a[108:111], a[252:255], v[232:235], a[108:111]\n"
    "s_waitcnt lgkmcnt(0)\n"
    "buffer_store_dwordx4 v[168:171], v244, s[24:27], s76 offen offset:3328 sc1\n"
    "v_cvt_pk_bf16_f32 v173, v174, v175\n"
    "v_accvgpr_read_b32 v182, a90\n"
    "v_accvgpr_read_b32 v185, a87\n"
    "v_cvt_pk_bf16_f32 v174, v176, v177\n"
    "v_pk_fma_f32 v[178:179], v[178:179], v[116:117], v[118:119] op_sel:[0,1,1] op_sel_hi:[1,1,1]\n"
    "v_accvgpr_read_b32 v186, a91\n"
    "v_cvt_pk_bf16_f32 v175, v178, v179\n"
    "v_fma_f32 v186, v186, v117, v119\n"
    "v_fma_f32 v187, v187, v117, v119\n"
    "v_pk_fma_f32 v[180:181], v[180:181], v[116:117], v[118:119] op_sel:[0,1,1] op_sel_hi:[1,1,1]\n"
    "v_cmp_eq_u64 s[56:57], v[236:237], v[238:239]\n"
    "v_fma_f32 v182, v182, v117, v119\n"
    "v_fma_f32 v183, v183, v117, v119\n"
    "v_mfma_f32_16x16x32_bf16 a[108:111], a[248:251], v[228:231], a[108:111]\n"
    "ds_write_b128 v3, v[172:175] offset:2048\n"
    "v_cvt_pk_bf16_f32 v180, v180, v181\n"
    "v_mfma_f32_16x16x32_bf16 a[96:99], a[224:227], v[228:231], a[96:99]\n"
    "v_fma_f32 v184, v184, v117, v119\n"
    "v_fma_f32 v185, v185, v117, v119\n"
    "v_cvt_pk_bf16_f32 v181, v182, v183\n"
    "v_mfma_f32_16x16x32_bf16 a[104:107], a[212:215], v[96:99], a[104:107]\n"
    "v_cvt_pk_bf16_f32 v182, v184, v185\n"
    "v_cvt_pk_bf16_f32 v183, v186, v187\n"
    "v_mfma_f32_16x16x32_bf16 a[96:99], a[228:231], v[232:235], a[96:99]\n"
    "ds_write_b128 v2, v[180:183] offset:2048\n"
    "v_mfma_f32_16x16x32_bf16 a[96:99], a[160:163], v[28:31], a[96:99]\n"
    "v_mfma_f32_16x16x32_bf16 a[104:107], a[240:243], v[228:231], a[104:107]\n"
    "ds_read_b128 v[176:179], v252 offset:2048\n"
    "v_mfma_f32_16x16x32_bf16 a[104:107], a[244:247], v[232:235], a[104:107]\n"
    "v_mfma_f32_16x16x32_bf16 a[100:103], a[200:203], v[92:95], a[100:103]\n"
    "v_mfma_f32_16x16x32_bf16 a[104:107], a[176:179], v[28:31], a[104:107]\n"
    "v_mfma_f32_16x16x32_bf16 a[104:107], a[180:183], v[32:35], a[104:107]\n"
    "v_mfma_f32_16x16x32_bf16 a[108:111], a[188:191], v[32:35], a[108:111]\n"
    "s_or_b64 s[56:57], s[56:57], s[34:35]\n"
    "ds_read_b128 v[184:187], v252 offset:3072\n"
    "v_mfma_f32_16x16x32_bf16 a[100:103], a[236:239], v[232:235], a[100:103]\n"
    "v_mfma_f32_16x16x32_bf16 a[96:99], a[164:167], v[32:35], a[96:99]\n"
    "v_mfma_f32_16x16x32_bf16 a[100:103], a[232:235], v[228:231], a[100:103]\n"
    "s_waitcnt lgkmcnt(1)\n"
    "buffer_store_dwordx4 v[176:179], v244, s[24:27], s77 offen offset:256 sc1\n"
    "v_accvgpr_read_b32 v198, a106\n"
    "v_fma_f32 v250, v117, s74, v250\n"
    "v_fma_f32 v250, |v122|, 4.0, v250\n"
    "v_accvgpr_read_b32 v194, a105\n"
    "s_nop 0\n"
    "v_accvgpr_read_b32 v196, a98\n"
    "v_mfma_f32_16x16x32_bf16 a[108:111], a[184:187], v[28:31], a[108:111]\n"
    "v_accvgpr_read_b32 v202, a107\n"
    "v_mfma_f32_16x16x32_bf16 a[100:103], a[172:175], v[32:35], a[100:103]\n"
    "s_nop 0\n"
    "v_mfma_f32_16x16x32_bf16 a[100:103], a[168:171], v[28:31], a[100:103]\n"
    "s_waitcnt lgkmcnt(0)\n"
    "buffer_store_dwordx4 v[184:187], v244, s[24:27], s77 offen offset:3328 sc1\n"
    "v_accvgpr_read_b32 v190, a104\n"
    "s_nop 1\n"
    "v_accvgpr_read_b32 v191, a108\n"
    "v_accvgpr_read_b32 v199, a110\n"
    "v_fma_f32 v250, v120, 4.0, v250\n"
    "v_accvgpr_read_b32 v197, a102\n"
    "v_accvgpr_read_b32 v200, a99\n"
    "v_accvgpr_read_b32 v195, a109\n"
    "v_accvgpr_read_b32 v188, a96\n"
    "v_pk_fma_f32 v[190:191], v[190:191], v[120:121], v[122:123] op_sel:[0,1,1] op_sel_hi:[1,1,1]\n"
    "v_accvgpr_read_b32 v201, a103\n"
    "v_accvgpr_read_b32 v203, a111\n"
    "v_accvgpr_read_b32 v189, a100\n"
    "v_pk_fma_f32 v[194:195], v[194:195], v[120:121], v[122:123] op_sel:[0,1,1] op_sel_hi:[1,1,1]\n"
    "v_fma_f32 v198, v198, v121, v123\n"
    "v_fma_f32 v199, v199, v121, v123\n"
    "v_fma_f32 v188, v188, v121, v123\n"
    "v_fma_f32 v189, v189, v121, v123\n"
    "v_accvgpr_read_b32 v192, a97\n"
    "v_cvt_pk_bf16_f32 v188, v188, v189\n"
    "v_accvgpr_read_b32 v193, a101\n"
    "v_cvt_pk_bf16_f32 v189, v190, v191\n"
    "v_cvt_pk_bf16_f32 v191, v194, v195\n"
    "v_fma_f32 v192, v192, v121, v123\n"
    "v_fma_f32 v193, v193, v121, v123\n"
    "v_cvt_pk_bf16_f32 v190, v192, v193\n"
    "ds_write_b128 v3, v[188:191] offset:4096\n"
    "v_pk_fma_f32 v[196:197], v[196:197], v[120:121], v[122:123] op_sel:[0,1,1] op_sel_hi:[1,1,1]\n"
    "v_cvt_pk_bf16_f32 v196, v196, v197\n"
    "v_cvt_pk_bf16_f32 v197, v198, v199\n"
    "v_pk_fma_f32 v[200:201], v[200:201], v[120:121], v[122:123] op_sel:[0,1,1] op_sel_hi:[1,1,1]\n"
    "v_fma_f32 v250, v121, s74, v250\n"
    "v_pk_fma_f32 v[202:203], v[202:203], v[120:121], v[122:123] op_sel:[0,1,1] op_sel_hi:[1,1,1]\n"
    "v_cvt_pk_bf16_f32 v199, v202, v203\n"
    "v_cvt_pk_bf16_f32 v198, v200, v201\n"
    "ds_write_b128 v2, v[196:199] offset:4096\n"
    "v_cmp_nlt_f32 s[54:55], |v250|, s73\n"
    "ds_read_b128 v[200:203], v252 offset:5120\n"
    "ds_read_b128 v[192:195], v252 offset:4096\n"
    "s_waitcnt lgkmcnt(1)\n"
    "buffer_store_dwordx4 v[200:203], v244, s[24:27], s78 offen offset:3328 sc1\n"
    "s_or_b64 s[54:55], s[54:55], s[56:57]\n"
    "s_waitcnt lgkmcnt(0)\n"
    "buffer_store_dwordx4 v[192:195], v244, s[24:27], s78 offen offset:256 sc1\n"
    "s_or_b32 %0, s54, s55\n"
    "s_cmp_eq_u32 %0, 0\n"
    "s_cbranch_scc1 .Lfin\n"
    "s_waitcnt vmcnt(0)\n"
    ".Lfin:\n"
    ".Lend:\n"
    ".Lend2:\n"
    : "=&s"(bad), "+s"(ka) : "s"(b), "v"(tid)
    : "memory", "vcc", "s16", "s17", "s18", "s19", "s20", "s21", "s22", "s23", "s24", "s25", "s26", "s27", "s28", "s29", "s30", "s31", "s34", "s35", "s36", "s37", "s38", "s39", "s40", "s41", "s42", "s43", "s44", "s45", "s46", "s47", "s48", "s49", "s50", "s51", "s52", "s53", "s54", "s55", "s56", "s57", "s58", "s59", "s60", "s61", "s62", "s63", "s64", "s65", "s66", "s67", "s68", "s69", "s70", "s71", "s72", "s73", "s74", "s75", "s76", "s77", "s78", "s79", "s80", "s81", "s82", "s83", "s84", "s85", "s86", "s87", "s88", "s89", "s90", "s91", "s92", "s93", "s94", "s95", "s96", "s97", "s98", "s99", "v1", "v2", "v3", "v4", "v5", "v6", "v7", "v8", "v9", "v10", "v11", "v12", "v13", "v14", "v15", "v16", "v17", "v18", "v19", "v20", "v21", "v22", "v23", "v24", "v25", "v26", "v27", "v28", "v29", "v30", "v31", "v32", "v33", "v34", "v35", "v36", "v37", "v38", "v39", "v40", "v41", "v42", "v43", "v44", "v45", "v46", "v47", "v48", "v49", "v50", "v51", "v52", "v53", "v54", "v55", "v56", "v57", "v58", "v59", "v60", "v61", "v62", "v63", "v64", "v65", "v66", "v67", "v68", "v69", "v70", "v71", "v72", "v73", "v74", "v75", "v76", "v77", "v78", "v79", "v80", "v81", "v82", "v83", "v84", "v85", "v86", "v87", "v88", "v89", "v90", "v91", "v92", "v93", "v94", "v95", "v96", "v97", "v98", "v99", "v100", "v101", "v102", "v103", "v104", "v105", "v106", "v107", "v108", "v109", "v110", "v111", "v112", "v113", "v114", "v115", "v116", "v117", "v118", "v119", "v120", "v121", "v122", "v123", "v124", "v125", "v126", "v127", "v128", "v129", "v130", "v131", "v132", "v133", "v134", "v135", "v136", "v137", "v138", "v139", "v140", "v141", "v142", "v143", "v144", "v145", "v146", "v147", "v148", "v149", "v150", "v151", "v152", "v153", "v154", "v155", "v156", "v157", "v158", "v159", "v160", "v161", "v162", "v163", "v164", "v165", "v166", "v167", "v168", "v169", "v170", "v171", "v172", "v173", "v174", "v175", "v176", "v177", "v178", "v179", "v180", "v181", "v182", "v183", "v184", "v185", "v186", "v187", "v188", "v189", "v190", "v191", "v192", "v193", "v194", "v195", "v196", "v197", "v198", "v199", "v200", "v201", "v202", "v203", "v204", "v205", "v206", "v207", "v208", "v209", "v210", "v211", "v212", "v213", "v214", "v215", "v216", "v217", "v218", "v219", "v220", "v221", "v222", "v223", "v224", "v225", "v226", "v227", "v228", "v229", "v230", "v231", "v232", "v233", "v234", "v235", "v236", "v237", "v238", "v239", "v240", "v241", "v242", "v243", "v244", "v245", "v246", "v247", "v248", "v249", "v250", "v251", "v252", "v253", "v254", "v255", "a0", "a1", "a2", "a3", "a4", "a5", "a6", "a7", "a8", "a9", "a10", "a11", "a12", "a13", "a14", "a15", "a16", "a17", "a18", "a19", "a20", "a21", "a22", "a23", "a24", "a25", "a26", "a27", "a28", "a29", "a30", "a31", "a32", "a33", "a34", "a35", "a36", "a37", "a38", "a39", "a40", "a41", "a42", "a43", "a44", "a45", "a46", "a47", "a48", "a49", "a50", "a51", "a52", "a53", "a54", "a55", "a56", "a57", "a58", "a59", "a60", "a61", "a62", "a63", "a64", "a65", "a66", "a67", "a68", "a69", "a70", "a71", "a72", "a73", "a74", "a75", "a76", "a77", "a78", "a79", "a80", "a81", "a82", "a83", "a84", "a85", "a86", "a87", "a88", "a89", "a90", "a91", "a92", "a93", "a94", "a95", "a96", "a97", "a98", "a99", "a100", "a101", "a102", "a103", "a104", "a105", "a106", "a107", "a108", "a109", "a110", "a111", "a112", "a113", "a114", "a115", "a116", "a117", "a118", "a119", "a120", "a121", "a122", "a123", "a124", "a125", "a126", "a127", "a128", "a129", "a130", "a131", "a132", "a133", "a134", "a135", "a136", "a137", "a138", "a139", "a140", "a141", "a142", "a143", "a144", "a145", "a146", "a147", "a148", "a149", "a150", "a151", "a152", "a153", "a154", "a155", "a156", "a157", "a158", "a159", "a160", "a161", "a162", "a163", "a164", "a165", "a166", "a167", "a168", "a169", "a170", "a171", "a172", "a173", "a174", "a175", "a176", "a177", "a178", "a179", "a180", "a181", "a182", "a183", "a184", "a185", "a186", "a187", "a188", "a189", "a190", "a191", "a192", "a193", "a194", "a195", "a196", "a197", "a198", "a199", "a200", "a201", "a202", "a203", "a204", "a205", "a206", "a207", "a208", "a209", "a210", "a211", "a212", "a213", "a214", "a215", "a216", "a217", "a218", "a219", "a220", "a221", "a222", "a223", "a224", "a225", "a226", "a227", "a228", "a229", "a230", "a231", "a232", "a233", "a234", "a235", "a236", "a237", "a238", "a239", "a240", "a241", "a242", "a243", "a244", "a245", "a246", "a247", "a248", "a249", "a250", "a251", "a252", "a253", "a254", "a255");
  if (bad) {
    slow_rows((const Args*)ka, (b & 7) * 4 + ((b >> 3) & 3), (b >> 5) * 256 + (tid & 192), tid & 63);
  }
}

extern "C" void kernel_launch(void* const* d_in, const int* in_sizes, int n_in,
                              void* d_out, int out_size, void* d_ws, size_t ws_size,
                              hipStream_t stream) {
  (void)hipFuncSetAttribute((const void*)attn_fwd, hipFuncAttributeMaxDynamicSharedMemorySize, SHM);
  attn_fwd<<<256, 256, SHM, stream>>>((const unsigned short*)d_in[0], (const int*)d_in[1], (const int*)d_in[2], (const unsigned short*)d_in[3], (const unsigned short*)d_in[4],
                                      (const unsigned short*)d_in[5], (const unsigned short*)d_in[6], (char*)d_ws, (unsigned short*)d_out);
}
```

```cpp
#include <hip/hip_runtime.h>

constexpr int D = 192, NH = 32, S = 2048;
constexpr int SHM = 155968;

struct Args { const unsigned short* q; const int* qk; const int* qv; const unsigned short* ksc; const unsigned short* kzr;
              const unsigned short* vsc; const unsigned short* vzr; char* ws; unsigned short* o; };

__device__ __forceinline__ float bf2f(unsigned short x) { return __uint_as_float((unsigned)x << 16); }
__device__ __forceinline__ float rbf(float x) { return (float)(__bf16)x; }

__device__ __forceinline__ void slow_rows(const Args* a, unsigned h, unsigned row0, unsigned lane) {
  const unsigned short* q = a->q + ((size_t)h * S + row0) * D;
  unsigned short* o = a->o + ((size_t)h * S + row0) * D;
  const int* qk = a->qk + (size_t)h * S * D; const int* qv = a->qv + (size_t)h * S * D;
  const unsigned short* ks = a->ksc + h * 32 * D; const unsigned short* kz = a->kzr + h * 32 * D;
  const unsigned short* vs = a->vsc + h * S; const unsigned short* vz = a->vzr + h * S;
#pragma unroll 1
  for (int r = 0; r < 64; ++r) {
    float m = -INFINITY, l = 0.f, a0 = 0.f, a1 = 0.f, a2 = 0.f;
#pragma unroll 1
    for (int t = 0; t < 32; ++t) {
      const int* kr = qk + (size_t)(t * 64 + lane) * D;
      float s = 0.f;
#pragma unroll 4
      for (int d = 0; d < D; ++d) s = fmaf(bf2f(q[r * D + d]), rbf(rbf(rbf((float)kr[d]) - bf2f(kz[t * D + d])) * bf2f(ks[t * D + d])), s);
      s *= 0.07216878364870322f;
      float mx = s;
      for (int x = 32; x; x >>= 1) mx = fmaxf(mx, __shfl_xor(mx, x));
      const float mn = fmaxf(m, mx), c = __expf(m - mn), p = __expf(s - mn);
      float ps = p;
      for (int x = 32; x; x >>= 1) ps += __shfl_xor(ps, x);
      l = l * c + ps; a0 *= c; a1 *= c; a2 *= c; m = mn;
#pragma unroll 2
      for (int k = 0; k < 64; ++k) {
        const float pk = __shfl(p, k), z = bf2f(vz[t * 64 + k]), sc = bf2f(vs[t * 64 + k]);
        const int* vr = qv + (size_t)(t * 64 + k) * D + lane;
        a0 = fmaf(pk, rbf(rbf(rbf((float)vr[0]) - z) * sc), a0); a1 = fmaf(pk, rbf(rbf(rbf((float)vr[64]) - z) * sc), a1); a2 = fmaf(pk, rbf(rbf(rbf((float)vr[128]) - z) * sc), a2);
      }
    }
    const float i = 1.f / l;
    o[r * D + lane] = __builtin_bit_cast(unsigned short, (__bf16)(a0 * i)); o[r * D + 64 + lane] = __builtin_bit_cast(unsigned short, (__bf16)(a1 * i));
    o[r * D + 128 + lane] = __builtin_bit_cast(unsigned short, (__bf16)(a2 * i));
  }
}

__global__ __launch_bounds__(256, 1) __attribute__((aligned(4096))) void attn_fwd(const unsigned short* q, const int* qk, const int* qv, const unsigned short* ksc, const unsigned short* kzr,
                                                   const unsigned short* vsc, const unsigned short* vzr, char* ws, unsigned short* o) {
  unsigned long long ka = (unsigned long long)__builtin_amdgcn_kernarg_segment_ptr();
  unsigned bad, b = blockIdx.x;
  const unsigned tid = threadIdx.x;
  asm volatile(
    "s_load_dwordx2 s[28:29], %1, 8\n"
    "s_load_dwordx2 s[40:41], %1, 16\n"
    "s_load_dwordx2 s[44:45], %1, 24\n"
    "s_load_dwordx2 s[88:89], %1, 32\n"
    "s_load_dwordx2 s[36:37], %1, 56\n"
    "s_load_dwordx2 s[92:93], %1, 40\n"
    "s_load_dwordx2 s[96:97], %1, 48\n"
    "s_load_dwordx2 s[20:21], %1, 0\n"
    "s_load_dwordx2 s[16:17], %1, 56\n"
    "s_load_dwordx2 s[24:25], %1, 64\n"
    "s_getreg_b32 s72, hwreg(HW_REG_XCC_ID, 0, 1)\n"
    "s_cmp_eq_u32 s72, 1\n"
    "s_cbranch_scc1 .Ldly\n"
    "s_sleep 32\n"
    ".Ldly:\n"
    "v_readfirstlane_b32 s84, %3\n"
    "v_and_b32 v156, 63, %3\n"
    "s_and_b32 s52, %2, 7\n"
    "s_bfe_u32 s72, %2, 0x20003\n"
    "s_lshr_b32 s53, %2, 5\n"
    "s_lshl_b32 s52, s52, 2\n"
    "s_lshr_b32 s84, s84, 6\n"
    "s_or_b32 s52, s52, s72\n"
    "v_lshlrev_b32 v1, 4, v156\n"
    "s_getpc_b64 s[54:55]\n"
    ".Lpc:\n"
    "s_lshl_b32 s72, s72, 7\n"
    "v_lshl_add_u32 v246, %3, 9, s72\n"
    "v_min_u32 v246, .Lend-.Lpc-4, v246\n"
    "global_load_dword v246, v246, s[54:55]\n"
    "s_mov_b32 s30, 1572864\n"
    "s_mov_b32 s31, 0x20000\n"
    "s_mov_b32 s42, 1572864\n"
    "s_mov_b32 s43, 0x20000\n"
    "s_mov_b32 s46, 12288\n"
    "s_mov_b32 s47, 0x20000\n"
    "s_mov_b32 s90, 12288\n"
    "s_mov_b32 s91, 0x20000\n"
    "s_lshr_b32 s79, s84, 1\n"
    "s_and_b32 s59, s84, 1\n"
    "s_cmp_ge_u32 s53, 6\n"
    "s_cselect_b32 s83, 1, 0\n"
    "s_or_b32 s83, s83, s84\n"
    "s_sub_u32 s58, 2, s79\n"
    "s_mul_i32 s71, s58, 49152\n"
    "s_mul_i32 s72, s52, 1572864\n"
    "s_mul_i32 s73, s52, 12288\n"
    "v_lshrrev_b32 v157, 2, v156\n"
    "v_and_b32 v158, 3, v156\n"
    "s_mul_i32 s35, s84, 3072\n"
    "s_bitcmp1_b32 s83, 0\n"
    "s_cbranch_scc1 .Lf2b\n"
    "v_and_b32 v159, 32, v156\n"
    "v_mul_u32_u24 v242, 376, v159\n"
    "v_and_b32 v159, 16, v156\n"
    "v_mul_u32_u24 v159, 88, v159\n"
    "v_lshl_add_u32 v242, v156, 3, v242\n"
    "v_add_u32 v242, v159, v242\n"
    "s_lshl_b32 s63, s53, 7\n"
    "v_mul_u32_u24 v160, 768, v157\n"
    "v_lshl_add_u32 v160, v158, 4, v160\n"
    "s_and_b32 s81, s58, 1\n"
    "s_mul_i32 s81, s81, 384\n"
    "s_lshl_b32 s75, s53, 6\n"
    "s_add_u32 s81, s81, s75\n"
    "s_add_u32 s75, s71, s63\n"
    "s_add_u32 s76, s75, 6144\n"
    "s_add_u32 s77, s75, 24576\n"
    "s_add_u32 s78, s75, 30720\n"
    "s_waitcnt lgkmcnt(0)\n"
    "s_add_u32 s44, s44, s73\n"
    "s_addc_u32 s45, s45, 0\n"
    "s_add_u32 s88, s88, s73\n"
    "s_addc_u32 s89, s89, 0\n"
    "buffer_load_dwordx4 v[4:7], v1, s[44:47], s35 offen offset:0\n"
    "buffer_load_dwordx4 v[8:11], v1, s[44:47], s35 offen offset:1024\n"
    "buffer_load_dwordx4 v[12:15], v1, s[44:47], s35 offen offset:2048\n"
    "buffer_load_dwordx4 v[16:19], v1, s[88:91], 0 offen offset:0\n"
    "buffer_load_dwordx4 v[164:167], v160, s[44:47], s81 offen offset:0\n"
    "buffer_load_dwordx4 v[168:171], v160, s[88:91], s81 offen offset:0\n"
    "s_add_u32 s28, s28, s72\n"
    "s_addc_u32 s29, s29, 0\n"
    "buffer_load_dwordx2 v[204:205], v242, s[28:31], s75 offen offset:0 nt\n"
    "buffer_load_dwordx2 v[206:207], v242, s[28:31], s75 offen offset:768 nt\n"
    "buffer_load_dwordx2 v[208:209], v242, s[28:31], s75 offen offset:3072 nt\n"
    "buffer_load_dwordx2 v[210:211], v242, s[28:31], s75 offen offset:3840 nt\n"
    "buffer_load_dwordx2 v[212:213], v242, s[28:31], s76 offen offset:0 nt\n"
    "buffer_load_dwordx2 v[214:215], v242, s[28:31], s76 offen offset:768 nt\n"
    "buffer_load_dwordx2 v[216:217], v242, s[28:31], s76 offen offset:3072 nt\n"
    "buffer_load_dwordx2 v[218:219], v242, s[28:31], s76 offen offset:3840 nt\n"
    "buffer_load_dwordx2 v[220:221], v242, s[28:31], s77 offen offset:0 nt\n"
    "buffer_load_dwordx2 v[222:223], v242, s[28:31], s77 offen offset:768 nt\n"
    "buffer_load_dwordx2 v[224:225], v242, s[28:31], s77 offen offset:3072 nt\n"
    "buffer_load_dwordx2 v[226:227], v242, s[28:31], s77 offen offset:3840 nt\n"
    "buffer_load_dwordx2 v[228:229], v242, s[28:31], s78 offen offset:0 nt\n"
    "buffer_load_dwordx2 v[230:231], v242, s[28:31], s78 offen offset:768 nt\n"
    "buffer_load_dwordx2 v[232:233], v242, s[28:31], s78 offen offset:3072 nt\n"
    "buffer_load_dwordx2 v[234:235], v242, s[28:31], s78 offen offset:3840 nt\n"
    "s_add_u32 s75, s71, s63\n"
    "s_add_u32 s75, s75, 98304\n"
    "s_add_u32 s76, s75, 6144\n"
    "s_add_u32 s77, s75, 24576\n"
    "s_add_u32 s78, s75, 30720\n"
    "buffer_load_dwordx2 v[36:37], v242, s[28:31], s75 offen offset:0 nt\n"
    "buffer_load_dwordx2 v[38:39], v242, s[28:31], s75 offen offset:768 nt\n"
    "v_lshrrev_b32 v162, 1, v156\n"
    "v_and_b32 v162, 16, v162\n"
    "v_bfe_u32 v159, v156, 4, 1\n"
    "v_or_b32 v162, v162, v159\n"
    "v_and_or_b32 v162, v157, 2, v162\n"
    "v_mul_u32_u24 v162, 768, v162\n"
    "v_and_b32 v163, 0x70, v1\n"
    "v_add_u32 v162, v162, v163\n"
    "s_branch .Lf2e\n"
    ".Lf2b:\n"
    "v_and_b32 v159, 48, v156\n"
    "v_lshlrev_b32 v242, 2, v159\n"
    "v_lshl_add_u32 v243, v158, 4, v242\n"
    "v_and_b32 v159, 12, v156\n"
    "v_mul_u32_u24 v242, 384, v159\n"
    "v_add_u32 v242, v243, v242\n"
    "v_add_u32 v243, v243, v159\n"
    "s_mul_i32 s63, s53, 6144\n"
    "s_sub_u32 s63, s63, 49152\n"
    "s_cmp_eq_u32 s59, 0\n"
    "s_cselect_b32 s63, 0x7f000000, s63\n"
    "s_add_u32 s75, s71, s63\n"
    "s_waitcnt lgkmcnt(0)\n"
    "s_add_u32 s44, s44, s73\n"
    "s_addc_u32 s45, s45, 0\n"
    "s_add_u32 s88, s88, s73\n"
    "s_addc_u32 s89, s89, 0\n"
    "buffer_load_dwordx4 v[4:7], v1, s[44:47], s35 offen offset:0\n"
    "buffer_load_dwordx4 v[8:11], v1, s[44:47], s35 offen offset:1024\n"
    "buffer_load_dwordx4 v[12:15], v1, s[44:47], s35 offen offset:2048\n"
    "buffer_load_dwordx4 v[16:19], v1, s[88:91], 0 offen offset:0\n"
    "s_add_u32 s40, s40, s72\n"
    "s_addc_u32 s41, s41, 0\n"
    "buffer_load_dwordx4 v[204:207], v242, s[40:43], s75 offen offset:0 nt\n"
    "buffer_load_dwordx4 v[208:211], v242, s[40:43], s75 offen offset:768 nt\n"
    "buffer_load_dwordx4 v[212:215], v242, s[40:43], s75 offen offset:256 nt\n"
    "buffer_load_dwordx4 v[216:219], v242, s[40:43], s75 offen offset:1024 nt\n"
    "buffer_load_dwordx4 v[220:223], v242, s[40:43], s75 offen offset:512 nt\n"
    "buffer_load_dwordx4 v[224:227], v242, s[40:43], s75 offen offset:1280 nt\n"
    "s_add_u32 s75, s71, s63\n"
    "s_add_u32 s75, s75, 98304\n"
    "buffer_load_dwordx4 v[36:39], v242, s[40:43], s75 offen offset:0 nt\n"
    "s_add_u32 s28, s28, s72\n"
    "s_addc_u32 s29, s29, 0\n"
    "v_lshrrev_b32 v162, 1, v156\n"
    "v_and_b32 v162, 16, v162\n"
    "v_bfe_u32 v159, v156, 4, 1\n"
    "v_or_b32 v162, v162, v159\n"
    "v_and_or_b32 v162, v157, 2, v162\n"
    "v_mul_u32_u24 v162, 768, v162\n"
    "v_and_b32 v163, 0x70, v1\n"
    "v_add_u32 v162, v162, v163\n"
    ".Lf2e:\n"
    "s_nop 4\n"
    "v_lshlrev_b32 v247, 2, v156\n"
    "v_add_u32 v247, 155648, v247\n"
    "v_mov_b32 v159, 0\n"
    "ds_write_b32 v247, v159\n"
    "v_mov_b32 v159, 0xff800000\n"
    "v_mov_b32 v160, 155904\n"
    "ds_write_b32 v160, v159\n"
    "s_mov_b32 s38, 5376\n"
    "s_mov_b32 s39, 0x20000\n"
    "s_mov_b32 s94, 4096\n"
    "s_mov_b32 s95, 0x20000\n"
    "s_mov_b32 s98, 4096\n"
    "s_mov_b32 s99, 0x20000\n"
    "s_mov_b32 s22, 98304\n"
    "s_mov_b32 s23, 0x20000\n"
    "s_mov_b32 s18, 1622016\n"
    "s_mov_b32 s19, 0x20000\n"
    "s_mov_b32 s26, 98304\n"
    "s_mov_b32 s27, 0x20000\n"
    "s_lshl_b32 s80, s58, 7\n"
    "s_add_u32 s80, s80, 256\n"
    "s_lshl_b32 s74, s52, 16\n"
    "s_mul_i32 s69, s59, 384\n"
    "s_mul_i32 s57, s79, 6144\n"
    "s_add_u32 s57, s57, s69\n"
    "s_add_u32 s66, s57, 24576\n"
    "s_add_u32 s36, s36, s74\n"
    "s_addc_u32 s37, s37, 0\n"
    "s_lshr_b32 s74, s74, 4\n"
    "s_add_u32 s92, s92, s74\n"
    "s_addc_u32 s93, s93, 0\n"
    "s_add_u32 s96, s96, s74\n"
    "s_addc_u32 s97, s97, 0\n"
    "v_lshlrev_b32 v128, 3, v156\n"
    "v_mov_b32 v129, 0\n"
    "v_lshlrev_b32 v130, 4, %3\n"
    "buffer_load_dwordx2 v[124:125], v128, s[44:47], 0 offen offset:0\n"
    "buffer_load_dwordx2 v[126:127], v128, s[88:91], 0 offen offset:0\n"
    "buffer_load_dwordx2 v[238:239], v129, s[36:39], 0 offen offset:0 sc1\n"
    "s_mul_i32 s74, s52, 786432\n"
    "s_mul_i32 s73, s53, 98304\n"
    "s_add_u32 s74, s74, s73\n"
    "s_add_u32 s20, s20, s74\n"
    "s_addc_u32 s21, s21, 0\n"
    "s_add_u32 s24, s24, s74\n"
    "s_addc_u32 s25, s25, 0\n"
    "s_mul_i32 s72, s52, 1626112\n"
    "s_add_u32 s72, s72, 2097152\n"
    "s_add_u32 s16, s16, s72\n"
    "s_addc_u32 s17, s17, 0\n"
    "s_mul_i32 s65, s84, 6144\n"
    "s_mul_i32 s85, s84, 24576\n"
    "s_waitcnt lgkmcnt(0)\n"
    "s_barrier\n"
    "s_add_u32 s73, s65, 49152\n"
    "v_add_u32 v131, s73, v1\n"
    "s_add_u32 s73, s73, s69\n"
    "v_add_u32 v132, s73, v163\n"
    "v_and_b32 v159, 32, v156\n"
    "v_lshlrev_b32 v244, 3, v156\n"
    "v_xor_b32 v244, v244, v159\n"
    "v_lshl_add_u32 v244, v159, 3, v244\n"
    "s_bitcmp1_b32 s83, 0\n"
    "s_cbranch_scc1 .Lf5b\n"
    "s_lshl_b32 s60, s53, 11\n"
    "s_add_u32 s61, s60, 12288\n"
    "s_lshl_b32 s73, s58, 8\n"
    "s_add_u32 s73, s73, 122880\n"
    "v_and_b32 v243, 0xf0, v1\n"
    "v_add_u32 v243, s73, v243\n"
    "s_and_b32 s73, s73, 0x1fd00\n"
    "v_lshlrev_b32 v160, 9, v157\n"
    "v_lshl_add_u32 v160, v158, 6, v160\n"
    "v_add_u32 v160, s73, v160\n"
    "s_lshl_b32 s73, s79, 8\n"
    "s_mul_i32 s74, s59, 6144\n"
    "s_add_u32 s73, s73, s74\n"
    "v_add_u32 v163, s73, v244\n"
    "v_and_b32 v159, 32, v156\n"
    "v_lshlrev_b32 v244, 2, v156\n"
    "v_xor_b32 v244, v244, v159\n"
    "v_mad_u32_u24 v244, v159, 12, v244\n"
    "buffer_load_dwordx2 v[40:41], v242, s[28:31], s75 offen offset:3072 nt\n"
    "buffer_load_dwordx2 v[42:43], v242, s[28:31], s75 offen offset:3840 nt\n"
    "v_add_u32 v159, 0, v156\n"
    "v_mul_u32_u24 v159, 2731, v159\n"
    "v_lshrrev_b32 v159, 16, v159\n"
    "v_xor_b32 v159, v159, v156\n"
    "v_and_b32 v159, 7, v159\n"
    "v_and_b32 v88, 0x380, v1\n"
    "v_lshl_add_u32 v88, v159, 4, v88\n"
    "v_add_u32 v159, 64, v156\n"
    "v_mul_u32_u24 v159, 2731, v159\n"
    "v_lshrrev_b32 v159, 16, v159\n"
    "v_xor_b32 v159, v159, v156\n"
    "v_and_b32 v159, 7, v159\n"
    "v_and_b32 v89, 0x380, v1\n"
    "v_lshl_add_u32 v89, v159, 4, v89\n"
    "v_add_u32 v159, 128, v156\n"
    "v_mul_u32_u24 v159, 2731, v159\n"
    "v_lshrrev_b32 v159, 16, v159\n"
    "v_xor_b32 v159, v159, v156\n"
    "v_and_b32 v159, 7, v159\n"
    "v_and_b32 v90, 0x380, v1\n"
    "v_lshl_add_u32 v90, v159, 4, v90\n"
    "v_and_b32 v2, 7, v156\n"
    "v_lshrrev_b32 v144, 3, %3\n"
    "v_mul_u32_u24 v144, 768, v144\n"
    "v_lshl_add_u32 v144, v2, 3, v144\n"
    "buffer_load_dwordx2 v[44:45], v242, s[28:31], s76 offen offset:0 nt\n"
    "buffer_load_dwordx2 v[46:47], v242, s[28:31], s76 offen offset:768 nt\n"
    "v_add_u32 v144, 131072, v144\n"
    "v_mad_u32_u24 v86, v2, 24, v144\n"
    "v_and_b32 v96, 15, v156\n"
    "v_lshrrev_b32 v159, 4, v156\n"
    "v_xor_b32 v159, v159, v2\n"
    "v_mul_u32_u24 v96, 384, v96\n"
    "v_lshl_add_u32 v96, v159, 4, v96\n"
    "s_add_u32 s73, s65, 49152\n"
    "v_add_u32 v96, s73, v96\n"
    "v_xor_b32 v97, 64, v96\n"
    "v_subrev_u32 v154, 24576, v96\n"
    "v_subrev_u32 v155, 24576, v97\n"
    "s_add_u32 s67, s65, 98304\n"
    "s_add_u32 s68, s65, 24576\n"
    "s_add_u32 s69, s65, 73728\n"
    "s_mov_b32 s81, 256\n"
    "s_mul_i32 s74, s79, 24576\n"
    "s_add_u32 s70, s74, s65\n"
    "s_add_u32 s70, s70, 73728\n"
    "s_sub_u32 s64, s65, s74\n"
    "s_add_u32 s64, s64, 98304\n"
    "v_and_b32 v249, 0x310, v1\n"
    "v_and_b32 v159, 12, v156\n"
    "v_lshl_add_u32 v249, v159, 3, v249\n"
    "v_and_b32 v159, 2, v156\n"
    "buffer_load_dwordx2 v[48:49], v242, s[28:31], s76 offen offset:3072 nt\n"
    "buffer_load_dwordx2 v[50:51], v242, s[28:31], s76 offen offset:3840 nt\n"
    "v_lshl_add_u32 v249, v159, 6, v249\n"
    "s_add_u32 s73, s74, 73728\n"
    "v_add_u32 v248, s73, v249\n"
    "s_sub_u32 s73, 98304, s74\n"
    "v_add_u32 v249, s73, v249\n"
    "s_or_b32 s73, s53, s84\n"
    "s_cmp_eq_u32 s73, 0\n"
    "s_cselect_b32 s82, 0, 0x7fff0000\n"
    "v_and_b32 v252, 12, v156\n"
    "v_bfe_u32 v159, v156, 1, 1\n"
    "v_or_b32 v252, v252, v159\n"
    "v_and_b32 v159, 1, v156\n"
    "v_lshl_or_b32 v252, v159, 1, v252\n"
    "v_lshlrev_b32 v252, 6, v252\n"
    "v_lshlrev_b32 v159, 2, v156\n"
    "v_and_b32 v159, 32, v159\n"
    "v_xor_b32 v159, v159, v156\n"
    "v_and_or_b32 v252, v159, 48, v252\n"
    "v_mov_b32 v2, v252\n"
    "v_add_u32 v3, 24576, v252\n"
    "v_lshrrev_b32 v91, 3, v156\n"
    "v_lshl_add_u32 v87, v91, 6, 64\n"
    "v_add_u32 v87, 64, v87\n"
    "v_and_b32 v241, 1, v156\n"
    "v_lshlrev_b32 v241, 3, v241\n"
    "buffer_load_dwordx2 v[52:53], v242, s[28:31], s77 offen offset:0 nt\n"
    "buffer_load_dwordx2 v[54:55], v242, s[28:31], s77 offen offset:768 nt\n"
    "v_lshl_add_u32 v241, v91, 4, v241\n"
    "v_add_u32 v241, 1152, v241\n"
    "v_lshlrev_b32 v91, 24, v91\n"
    "v_lshl_add_u32 v91, v156, 3, v91\n"
    "s_lshl_b32 s73, s53, 4\n"
    "s_lshl_b32 s74, s59, 3\n"
    "s_add_u32 s73, s73, s74\n"
    "s_add_u32 s73, s73, 1152\n"
    "v_mov_b32 v240, 0x7fff0000\n"
    "v_writelane_b32 v240, s73, 0\n"
    "v_and_b32 v159, 48, v156\n"
    "s_mul_i32 s73, s79, 768\n"
    "s_sub_u32 s73, 131072, s73\n"
    "v_lshlrev_b32 v250, 1, v159\n"
    "v_add_u32 v250, s73, v250\n"
    "v_and_b32 v251, 3, v156\n"
    "v_lshl_add_u32 v251, v251, 7, v159\n"
    "s_add_u32 s73, s73, 256\n"
    "v_add_u32 v251, s73, v251\n"
    "v_and_b32 v159, 64, v1\n"
    "v_add_u32 v251, v159, v251\n"
    "v_bfe_i32 v159, v156, 2, 1\n"
    "v_and_b32 v159, 0xaaaa, v159\n"
    "v_or_b32 v253, 0x4444, v159\n"
    "s_mov_b32 s58, 0\n"
    "s_mov_b32 s59, 8\n"
    "s_waitcnt vmcnt(31)\n"
    "v_pk_min_i16 v4, v4, v10\n"
    "v_pk_min_i16 v5, v5, v11\n"
    "v_pk_min_i16 v6, v6, v12\n"
    "v_pk_min_i16 v7, v7, v13\n"
    "v_pk_min_i16 v8, v8, v14\n"
    "v_pk_min_i16 v9, v9, v15\n"
    "v_pk_min_i16 v16, v16, v18\n"
    "v_pk_min_i16 v17, v17, v19\n"
    "v_pk_min_i16 v4, v4, v7\n"
    "v_pk_min_i16 v5, v5, v8\n"
    "v_pk_min_i16 v6, v6, v9\n"
    "v_pk_min_i16 v16, v16, v17\n"
    "v_pk_min_i16 v4, v4, v5\n"
    "v_lshlrev_b32 v17, 16, v16\n"
    "v_pk_min_i16 v4, v4, v6\n"
    "v_min_i32 v16, v16, v17\n"
    "v_lshlrev_b32 v5, 16, v4\n"
    "v_mov_b32 v7, 155920\n"
    "v_min_i32 v4, v4, v5\n"
    "v_lshl_add_u32 v6, s84, 2, v7\n"
    "v_min_i32_dpp v16, v16, v16 row_shr:1\n"
    "v_min_i32_dpp v4, v4, v4 row_shr:1\n"
    "s_nop 0\n"
    "v_min_i32_dpp v16, v16, v16 row_shr:2\n"
    "v_min_i32_dpp v4, v4, v4 row_shr:2\n"
    "s_nop 0\n"
    "v_min_i32_dpp v16, v16, v16 row_shr:4\n"
    "v_min_i32_dpp v4, v4, v4 row_shr:4\n"
    "s_nop 0\n"
    "v_min_i32_dpp v16, v16, v16 row_shr:8\n"
    "v_min_i32_dpp v4, v4, v4 row_shr:8\n"
    "s_nop 0\n"
    "v_min_i32_dpp v16, v16, v16 row_bcast:15 row_mask:0xa\n"
    "v_min_i32_dpp v4, v4, v4 row_bcast:15 row_mask:0xa\n"
    "s_nop 0\n"
    "v_min_i32_dpp v16, v16, v16 row_bcast:31 row_mask:0xc\n"
    "v_min_i32_dpp v4, v4, v4 row_bcast:31 row_mask:0xc\n"
    "v_readlane_b32 s35, v16, 63\n"
    "v_readlane_b32 s73, v4, 63\n"
    "s_max_i32 s35, s35, 0\n"
    "s_and_b32 s35, s35, 0xffff0000\n"
    "v_mov_b32 v4, s73\n"
    "ds_write_b32 v6, v4\n"
    "v_mov_b32 v14, -32\n"
    "v_mov_b32 v12, -126\n"
    "v_mov_b32 v13, 0x3dd53b95\n"
    "v_mov_b32 v98, 0\n"
    "s_waitcnt vmcnt(29)\n"
    "v_lshlrev_b32 v180, 16, v164\n"
    "v_and_b32 v181, 0xffff0000, v164\n"
    "v_lshlrev_b32 v182, 16, v168\n"
    "v_and_b32 v183, 0xffff0000, v168\n"
    "v_lshlrev_b32 v184, 16, v165\n"
    "v_and_b32 v185, 0xffff0000, v165\n"
    "v_lshlrev_b32 v186, 16, v169\n"
    "v_and_b32 v187, 0xffff0000, v169\n"
    "v_lshlrev_b32 v188, 16, v166\n"
    "v_and_b32 v189, 0xffff0000, v166\n"
    "v_lshlrev_b32 v190, 16, v170\n"
    "v_and_b32 v191, 0xffff0000, v170\n"
    "v_lshlrev_b32 v192, 16, v167\n"
    "v_and_b32 v193, 0xffff0000, v167\n"
    "v_lshlrev_b32 v194, 16, v171\n"
    "v_and_b32 v195, 0xffff0000, v171\n"
    "s_waitcnt lgkmcnt(0)\n"
    "s_barrier\n"
    "ds_read_b128 v[8:11], v7 offset:0\n"
    "buffer_load_dwordx2 v[56:57], v242, s[28:31], s77 offen offset:3072 nt\n"
    "buffer_load_dwordx2 v[58:59], v242, s[28:31], s77 offen offset:3840 nt\n"
    "buffer_load_dwordx4 v[136:139], v130, s[92:95], 0 offen offset:0\n"
    "buffer_load_dwordx4 v[140:143], v130, s[96:99], 0 offen offset:0\n"
    "s_nop 0\n"
    "s_nop 0\n"
    "s_nop 0\n"
    "s_nop 0\n"
    "s_nop 0\n"
    "s_nop 0\n"
    "s_nop 0\n"
    "s_nop 0\n"
    "s_nop 0\n"
    "s_nop 0\n"
    "s_nop 0\n"
    "s_nop 0\n"
    "s_nop 0\n"
    "s_nop 0\n"
    "s_nop 0\n"
    "s_nop 0\n"
    "s_nop 0\n"
    "s_nop 0\n"
    "s_nop 0\n"
    "s_nop 0\n"
    "s_nop 0\n"
    "s_nop 0\n"
    "s_nop 0\n"
    "s_nop 0\n"
    "s_nop 0\n"
    "s_nop 0\n"
    "s_nop 0\n"
    "s_nop 0\n"
    "s_nop 0\n"
    "s_nop 0\n"
    "s_nop 0\n"
    "s_nop 0\n"
    "s_nop 0\n"
    "s_nop 0\n"
    "s_nop 0\n"
    "s_nop 0\n"
    "s_nop 0\n"
    "s_nop 0\n"
    "s_nop 0\n"
    "s_nop 0\n"
    "s_nop 0\n"
    "s_nop 0\n"
    "s_nop 0\n"
    "s_nop 0\n"
    "s_nop 0\n"
    "s_nop 0\n"
    "s_nop 0\n"
    "s_nop 0\n"
    "s_nop 0\n"
    "s_nop 0\n"
    "s_nop 0\n"
    "s_nop 0\n"
    "s_nop 0\n"
    "s_nop 0\n"
    "s_nop 0\n"
    "s_nop 0\n"
    "s_nop 0\n"
    "s_nop 0\n"
    "s_nop 0\n"
    "s_nop 0\n"
    "s_waitcnt lgkmcnt(0)\n"
    "v_min3_i32 v4, v8, v9, v10\n"
    "v_min_i32 v4, v4, v11\n"
    "v_max_i32 v4, 0, v4\n"
    "v_and_b32 v4, 0xffff0000, v4\n"
    "v_mul_f32 v4, s35, v4\n"
    "v_mul_f32 v4, 0x3ed53b95, v4\n"
    "v_lshrrev_b32 v4, 23, v4\n"
    "v_sub_u32 v4, 2, v4\n"
    "v_med3_i32 v4, v4, v14, v12\n"
    "v_ldexp_f32 v13, v13, v4\n"
    "v_readfirstlane_b32 s89, v4\n"
    "v_readfirstlane_b32 s88, v13\n"
    "s_mul_i32 s89, s89, 0xff7fff80\n"
    "s_mov_b32 s90, 149\n"
    "v_mul_f32 v180, s88, v180\n"
    "v_mul_f32 v181, s88, v181\n"
    "v_mul_f32 v184, s88, v184\n"
    "v_mul_f32 v185, s88, v185\n"
    "v_mul_f32 v188, s88, v188\n"
    "v_mul_f32 v189, s88, v189\n"
    "v_mul_f32 v192, s88, v192\n"
    "v_mul_f32 v193, s88, v193\n"
    "v_fmaak_f32 v182, v182, v180, 0x80730000\n"
    "v_fmaak_f32 v183, v183, v181, 0x80730000\n"
    "v_fmaak_f32 v186, v186, v184, 0x80730000\n"
    "v_fmaak_f32 v187, v187, v185, 0x80730000\n"
    "v_fmaak_f32 v190, v190, v188, 0x80730000\n"
    "v_fmaak_f32 v191, v191, v189, 0x80730000\n"
    "v_fmaak_f32 v194, v194, v192, 0x80730000\n"
    "v_fmaak_f32 v195, v195, v193, 0x80730000\n"
    "v_ldexp_f32 v180, v180, s90\n"
    "v_ldexp_f32 v181, v181, s90\n"
    "v_ldexp_f32 v184, v184, s90\n"
    "v_ldexp_f32 v185, v185, s90\n"
    "v_ldexp_f32 v188, v188, s90\n"
    "v_ldexp_f32 v189, v189, s90\n"
    "v_ldexp_f32 v192, v192, s90\n"
    "v_ldexp_f32 v193, v193, s90\n"
    "v_fma_f32 v180, v180, 0, v180\n"
    "v_fma_f32 v181, v181, 0, v181\n"
    "v_fma_f32 v184, v184, 0, v184\n"
    "v_fma_f32 v185, v185, 0, v185\n"
    "v_fma_f32 v188, v188, 0, v188\n"
    "v_fma_f32 v189, v189, 0, v189\n"
    "v_fma_f32 v192, v192, 0, v192\n"
    "v_fma_f32 v193, v193, 0, v193\n"
    "ds_write_b128 v160, v[180:183] offset:0\n"
    "ds_write_b128 v160, v[184:187] offset:16\n"
    "ds_write_b128 v160, v[188:191] offset:32\n"
    "ds_write_b128 v160, v[192:195] offset:48\n"
    "s_waitcnt vmcnt(17)\n"
    "s_add_u32 s54, s71, s60\n"
    "s_add_u32 s55, s71, s61\n"
    "ds_read_b128 v[92:95], v243 offset:0\n"
    "s_waitcnt lgkmcnt(0)\n"
    "v_fma_f32 v204, v204, v92, -v94\n"
    "v_fma_f32 v205, v205, v93, -v95\n"
    "v_fma_f32 v206, v206, v92, -v94\n"
    "v_fma_f32 v207, v207, v93, -v95\n"
    "v_cvt_pk_bf16_f32 v204, v204, v205\n"
    "v_cvt_pk_bf16_f32 v206, v206, v207\n"
    "buffer_store_dword v204, v244, s[16:19], s54 offen offset:0 sc1\n"
    "buffer_store_dword v206, v244, s[16:19], s54 offen offset:128 sc1\n"
    "v_fma_f32 v208, v208, v92, -v94\n"
    "v_fma_f32 v209, v209, v93, -v95\n"
    "v_fma_f32 v210, v210, v92, -v94\n"
    "v_fma_f32 v211, v211, v93, -v95\n"
    "v_cvt_pk_bf16_f32 v208, v208, v209\n"
    "v_cvt_pk_bf16_f32 v210, v210, v211\n"
    "buffer_store_dword v208, v244, s[16:19], s54 offen offset:1024 sc1\n"
    "buffer_store_dword v210, v244, s[16:19], s54 offen offset:1152 sc1\n"
    "v_fma_f32 v212, v212, v92, -v94\n"
    "v_fma_f32 v213, v213, v93, -v95\n"
    "buffer_load_dwordx2 v[60:61], v242, s[28:31], s78 offen offset:0 nt\n"
    "buffer_load_dwordx2 v[62:63], v242, s[28:31], s78 offen offset:768 nt\n"
    "buffer_load_dwordx4 v[4:7], v162, s[28:31], s57 offen offset:0\n"
    "buffer_load_dwordx4 v[8:11], v162, s[28:31], s57 offen offset:3072\n"
    "v_fma_f32 v214, v214, v92, -v94\n"
    "v_fma_f32 v215, v215, v93, -v95\n"
    "v_cvt_pk_bf16_f32 v212, v212, v213\n"
    "v_cvt_pk_bf16_f32 v214, v214, v215\n"
    "buffer_store_dword v212, v244, s[16:19], s54 offen offset:256 sc1\n"
    "buffer_store_dword v214, v244, s[16:19], s54 offen offset:384 sc1\n"
    "v_fma_f32 v216, v216, v92, -v94\n"
    "v_fma_f32 v217, v217, v93, -v95\n"
    "v_fma_f32 v218, v218, v92, -v94\n"
    "v_fma_f32 v219, v219, v93, -v95\n"
    "v_cvt_pk_bf16_f32 v216, v216, v217\n"
    "v_cvt_pk_bf16_f32 v218, v218, v219\n"
    "buffer_store_dword v216, v244, s[16:19], s54 offen offset:1280 sc1\n"
    "buffer_store_dword v218, v244, s[16:19], s54 offen offset:1408 sc1\n"
    "v_fma_f32 v220, v220, v92, -v94\n"
    "v_fma_f32 v221, v221, v93, -v95\n"
    "v_fma_f32 v222, v222, v92, -v94\n"
    "v_fma_f32 v223, v223, v93, -v95\n"
    "v_cvt_pk_bf16_f32 v220, v220, v221\n"
    "v_cvt_pk_bf16_f32 v222, v222, v223\n"
    "buffer_store_dword v220, v244, s[16:19], s55 offen offset:0 sc1\n"
    "buffer_store_dword v222, v244, s[16:19], s55 offen offset:128 sc1\n"
    "v_fma_f32 v224, v224, v92, -v94\n"
    "buffer_load_dwordx2 v[64:65], v242, s[28:31], s78 offen offset:3072 nt\n"
    "buffer_load_dwordx2 v[66:67], v242, s[28:31], s78 offen offset:3840 nt\n"
    "buffer_load_dwordx4 v[12:15], v162, s[28:31], s57 offen offset:128\n"
    "v_fma_f32 v225, v225, v93, -v95\n"
    "v_fma_f32 v226, v226, v92, -v94\n"
    "v_fma_f32 v227, v227, v93, -v95\n"
    "v_cvt_pk_bf16_f32 v224, v224, v225\n"
    "v_cvt_pk_bf16_f32 v226, v226, v227\n"
    "buffer_store_dword v224, v244, s[16:19], s55 offen offset:1024 sc1\n"
    "buffer_store_dword v226, v244, s[16:19], s55 offen offset:1152 sc1\n"
    "v_fma_f32 v228, v228, v92, -v94\n"
    "v_fma_f32 v229, v229, v93, -v95\n"
    "v_fma_f32 v230, v230, v92, -v94\n"
    "v_fma_f32 v231, v231, v93, -v95\n"
    "v_cvt_pk_bf16_f32 v228, v228, v229\n"
    "v_cvt_pk_bf16_f32 v230, v230, v231\n"
    "buffer_store_dword v228, v244, s[16:19], s55 offen offset:256 sc1\n"
    "buffer_store_dword v230, v244, s[16:19], s55 offen offset:384 sc1\n"
    "v_fma_f32 v232, v232, v92, -v94\n"
    "v_fma_f32 v233, v233, v93, -v95\n"
    "v_fma_f32 v234, v234, v92, -v94\n"
    "v_fma_f32 v235, v235, v93, -v95\n"
    "v_cvt_pk_bf16_f32 v232, v232, v233\n"
    "v_cvt_pk_bf16_f32 v234, v234, v235\n"
    "buffer_store_dword v232, v244, s[16:19], s55 offen offset:1280 sc1\n"
    "buffer_store_dword v234, v244, s[16:19], s55 offen offset:1408 sc1\n"
    "v_add_u32 v243, 512, v243\n"
    "s_add_u32 s71, s71, 98304\n"
    "s_add_u32 m0, s65, 24576\n"
    "s_add_u32 s72, s85, 0\n"
    "buffer_load_dwordx4 v88, s[20:23], s72 offen offset:0 nt lds\n"
    "buffer_load_dwordx4 v[16:19], v162, s[28:31], s57 offen offset:3200\n"
    "buffer_load_dwordx4 v[20:23], v162, s[28:31], s57 offen offset:256\n"
    "s_waitcnt vmcnt(38)\n"
    "v_add_co_u32 v238, vcc, 0x7f4a7c15, v238\n"
    "v_mov_b32 v159, 0x9e3779b9\n"
    "s_nop 0\n"
    "v_addc_co_u32 v239, vcc, v159, v239, vcc\n"
    "v_mov_b32 v246, 1\n"
    "s_waitcnt vmcnt(39)\n"
    "v_lshlrev_b32 v180, 16, v124\n"
    "v_and_b32 v181, 0xffff0000, v124\n"
    "v_lshlrev_b32 v182, 16, v125\n"
    "v_and_b32 v183, 0xffff0000, v125\n"
    "v_lshlrev_b32 v184, 16, v126\n"
    "v_and_b32 v185, 0xffff0000, v126\n"
    "v_lshlrev_b32 v186, 16, v127\n"
    "v_and_b32 v187, 0xffff0000, v127\n"
    "v_mul_f32 v180, s88, v180\n"
    "v_mul_f32 v181, s88, v181\n"
    "v_mul_f32 v182, s88, v182\n"
    "v_mul_f32 v183, s88, v183\n"
    "v_fmaak_f32 v184, v184, v180, 0x80730000\n"
    "v_fmaak_f32 v185, v185, v181, 0x80730000\n"
    "v_fmaak_f32 v186, v186, v182, 0x80730000\n"
    "v_fmaak_f32 v187, v187, v183, 0x80730000\n"
    "s_add_u32 m0, s65, 25600\n"
    "s_add_u32 s72, s85, 1024\n"
    "buffer_load_dwordx4 v89, s[20:23], s72 offen offset:0 nt lds\n"
    "buffer_load_dwordx4 v[24:27], v162, s[28:31], s57 offen offset:3328\n"
    "v_ldexp_f32 v180, v180, s90\n"
    "v_ldexp_f32 v181, v181, s90\n"
    "v_ldexp_f32 v182, v182, s90\n"
    "v_ldexp_f32 v183, v183, s90\n"
    "v_fma_f32 v180, v180, 0, v180\n"
    "v_fma_f32 v181, v181, 0, v181\n"
    "v_fma_f32 v182, v182, 0, v182\n"
    "v_fma_f32 v183, v183, 0, v183\n"
    "ds_write_b128 v131, v[180:183] offset:0\n"
    "ds_write_b128 v131, v[184:187] offset:1024\n"
    "ds_read_b128 v[180:183], v132 offset:0\n"
    "ds_read_b128 v[184:187], v132 offset:1024\n"
    "ds_read_b128 v[188:191], v132 offset:128\n"
    "ds_read_b128 v[192:195], v132 offset:1152\n"
    "ds_read_b128 v[196:199], v132 offset:256\n"
    "ds_read_b128 v[200:203], v132 offset:1280\n"
    "s_nop 0\n"
    "s_nop 0\n"
    "s_nop 0\n"
    "s_nop 0\n"
    "s_nop 0\n"
    "s_nop 0\n"
    "s_nop 0\n"
    "s_add_u32 m0, s65, 26624\n"
    "s_add_u32 s72, s85, 2048\n"
    "buffer_load_dwordx4 v90, s[20:23], s72 offen offset:0 nt lds\n"
    "buffer_load_dwordx4 v[100:103], v162, s[28:31], s66 offen offset:0\n"
    "buffer_load_dwordx4 v[104:107], v162, s[28:31], s66 offen offset:3072\n"
    "s_nop 0\n"
    "s_nop 0\n"
    "s_nop 0\n"
    "s_nop 0\n"
    "s_nop 0\n"
    "s_nop 0\n"
    "s_nop 0\n"
    "s_nop 0\n"
    "s_nop 0\n"
    "s_nop 0\n"
    "s_nop 0\n"
    "s_nop 0\n"
    "s_nop 0\n"
    "s_nop 0\n"
    "s_nop 0\n"
    "s_nop 0\n"
    "s_nop 0\n"
    "s_nop 0\n"
    "s_nop 0\n"
    "s_nop 0\n"
    "s_nop 0\n"
    "s_nop 0\n"
    "s_add_u32 m0, s65, 27648\n"
    "s_add_u32 s72, s85, 3072\n"
    "buffer_load_dwordx4 v88, s[20:23], s72 offen offset:0 nt lds\n"
    "buffer_load_dwordx4 v[108:111], v162, s[28:31], s66 offen offset:128\n"
    "s_nop 0\n"
    "s_nop 0\n"
    "s_nop 0\n"
    "s_nop 0\n"
    "s_nop 0\n"
    "s_nop 0\n"
    "s_nop 0\n"
    "s_nop 0\n"
    "s_nop 0\n"
    "s_nop 0\n"
    "s_nop 0\n"
    "s_nop 0\n"
    "s_nop 0\n"
    "s_nop 0\n"
    "s_nop 0\n"
    "s_nop 0\n"
    "s_nop 0\n"
    "s_nop 0\n"
    "s_nop 0\n"
    "s_nop 0\n"
    "s_nop 0\n"
    "s_nop 0\n"
    "s_nop 0\n"
    "s_waitcnt lgkmcnt(0)\n"
    "s_waitcnt vmcnt(10)\n"
    "ds_add_rtn_u32 v245, v247, v246\n"
    "s_lshl_b32 s73, s53, 6\n"
    "s_add_u32 s73, s73, 128\n"
    "s_waitcnt lgkmcnt(0)\n"
    "v_readfirstlane_b32 s74, v245\n"
    "s_nop 0\n"
    "s_cmp_eq_u32 s74, 3\n"
    "s_cselect_b32 s73, s73, 0x7fff0000\n"
    "buffer_store_dwordx2 v[238:239], v91, s[36:39], s73 offen offset:0 sc1\n"
    "s_waitcnt vmcnt(18)\n"
    "s_add_u32 m0, s65, 28672\n"
    "s_add_u32 s72, s85, 4096\n"
    "buffer_load_dwordx4 v89, s[20:23], s72 offen offset:0 nt lds\n"
    "buffer_load_dwordx4 v[112:115], v162, s[28:31], s66 offen offset:3200\n"
    "buffer_load_dwordx4 v[116:119], v162, s[28:31], s66 offen offset:256\n"
    "s_add_u32 s54, s71, s60\n"
    "s_add_u32 s55, s71, s61\n"
    "ds_read_b128 v[92:95], v243 offset:0\n"
    "s_waitcnt lgkmcnt(0)\n"
    "v_fma_f32 v36, v36, v92, -v94\n"
    "v_fma_f32 v37, v37, v93, -v95\n"
    "v_fma_f32 v38, v38, v92, -v94\n"
    "v_fma_f32 v39, v39, v93, -v95\n"
    "v_cvt_pk_bf16_f32 v36, v36, v37\n"
    "v_cvt_pk_bf16_f32 v38, v38, v39\n"
    "buffer_store_dword v36, v244, s[16:19], s54 offen offset:0 sc1\n"
    "buffer_store_dword v38, v244, s[16:19], s54 offen offset:128 sc1\n"
    "v_fma_f32 v40, v40, v92, -v94\n"
    "v_fma_f32 v41, v41, v93, -v95\n"
    "v_fma_f32 v42, v42, v92, -v94\n"
    "v_fma_f32 v43, v43, v93, -v95\n"
    "v_cvt_pk_bf16_f32 v40, v40, v41\n"
    "v_cvt_pk_bf16_f32 v42, v42, v43\n"
    "buffer_store_dword v40, v244, s[16:19], s54 offen offset:1024 sc1\n"
    "buffer_store_dword v42, v244, s[16:19], s54 offen offset:1152 sc1\n"
    "v_fma_f32 v44, v44, v92, -v94\n"
    "v_fma_f32 v45, v45, v93, -v95\n"
    "v_fma_f32 v46, v46, v92, -v94\n"
    "v_fma_f32 v47, v47, v93, -v95\n"
    "v_cvt_pk_bf16_f32 v44, v44, v45\n"
    "v_cvt_pk_bf16_f32 v46, v46, v47\n"
    "buffer_store_dword v44, v244, s[16:19], s54 offen offset:256 sc1\n"
    "buffer_store_dword v46, v244, s[16:19], s54 offen offset:384 sc1\n"
    "v_fma_f32 v48, v48, v92, -v94\n"
    "v_fma_f32 v49, v49, v93, -v95\n"
    "v_fma_f32 v50, v50, v92, -v94\n"
    "v_fma_f32 v51, v51, v93, -v95\n"
    "v_cvt_pk_bf16_f32 v48, v48, v49\n"
    "v_cvt_pk_bf16_f32 v50, v50, v51\n"
    "s_add_u32 m0, s65, 29696\n"
    "s_add_u32 s72, s85, 5120\n"
    "buffer_load_dwordx4 v90, s[20:23], s72 offen offset:0 nt lds\n"
    "buffer_load_dwordx4 v[120:123], v162, s[28:31], s66 offen offset:3328\n"
    "buffer_store_dword v48, v244, s[16:19], s54 offen offset:1280 sc1\n"
    "buffer_store_dword v50, v244, s[16:19], s54 offen offset:1408 sc1\n"
    "v_fma_f32 v52, v52, v92, -v94\n"
    "v_fma_f32 v53, v53, v93, -v95\n"
    "v_fma_f32 v54, v54, v92, -v94\n"
    "v_fma_f32 v55, v55, v93, -v95\n"
    "v_cvt_pk_bf16_f32 v52, v52, v53\n"
    "v_cvt_pk_bf16_f32 v54, v54, v55\n"
    "buffer_store_dword v52, v244, s[16:19], s55 offen offset:0 sc1\n"
    "buffer_store_dword v54, v244, s[16:19], s55 offen offset:128 sc1\n"
    "v_fma_f32 v56, v56, v92, -v94\n"
    "v_fma_f32 v57, v57, v93, -v95\n"
    "v_fma_f32 v58, v58, v92, -v94\n"
    "v_fma_f32 v59, v59, v93, -v95\n"
    "v_cvt_pk_bf16_f32 v56, v56, v57\n"
    "v_cvt_pk_bf16_f32 v58, v58, v59\n"
    "buffer_store_dword v56, v244, s[16:19], s55 offen offset:1024 sc1\n"
    "buffer_store_dword v58, v244, s[16:19], s55 offen offset:1152 sc1\n"
    "v_fma_f32 v60, v60, v92, -v94\n"
    "v_fma_f32 v61, v61, v93, -v95\n"
    "v_fma_f32 v62, v62, v92, -v94\n"
    "v_fma_f32 v63, v63, v93, -v95\n"
    "v_cvt_pk_bf16_f32 v60, v60, v61\n"
    "v_cvt_pk_bf16_f32 v62, v62, v63\n"
    "buffer_store_dword v60, v244, s[16:19], s55 offen offset:256 sc1\n"
    "buffer_store_dword v62, v244, s[16:19], s55 offen offset:384 sc1\n"
    "v_fma_f32 v64, v64, v92, -v94\n"
    "v_fma_f32 v65, v65, v93, -v95\n"
    "v_fma_f32 v66, v66, v92, -v94\n"
    "v_fma_f32 v67, v67, v93, -v95\n"
    "v_cvt_pk_bf16_f32 v64, v64, v65\n"
    "v_cvt_pk_bf16_f32 v66, v66, v67\n"
    "buffer_store_dword v64, v244, s[16:19], s55 offen offset:1280 sc1\n"
    "buffer_store_dword v66, v244, s[16:19], s55 offen offset:1408 sc1\n"
    "v_add_u32 v243, 512, v243\n"
    "s_add_u32 m0, s65, 73728\n"
    "s_add_u32 s72, s85, 6144\n"
    "buffer_load_dwordx4 v88, s[20:23], s72 offen offset:0 nt lds\n"
    "s_waitcnt vmcnt(56)\n"
    "v_lshlrev_b32 v204, 16, v136\n"
    "v_and_b32 v205, 0xffff0000, v136\n"
    "v_lshlrev_b32 v206, 16, v137\n"
    "v_and_b32 v207, 0xffff0000, v137\n"
    "v_lshlrev_b32 v208, 16, v138\n"
    "v_and_b32 v209, 0xffff0000, v138\n"
    "v_lshlrev_b32 v210, 16, v139\n"
    "v_and_b32 v211, 0xffff0000, v139\n"
    "v_log_f32 v68, v204\n"
    "v_log_f32 v69, v205\n"
    "v_log_f32 v70, v206\n"
    "v_log_f32 v71, v207\n"
    "v_log_f32 v72, v208\n"
    "v_log_f32 v73, v209\n"
    "v_log_f32 v74, v210\n"
    "v_log_f32 v75, v211\n"
    "v_rcp_f32 v204, v204\n"
    "v_rcp_f32 v205, v205\n"
    "v_rcp_f32 v206, v206\n"
    "s_add_u32 m0, s65, 74752\n"
    "s_add_u32 s72, s85, 7168\n"
    "buffer_load_dwordx4 v89, s[20:23], s72 offen offset:0 nt lds\n"
    "v_rcp_f32 v207, v207\n"
    "v_rcp_f32 v208, v208\n"
    "v_rcp_f32 v209, v209\n"
    "v_rcp_f32 v210, v210\n"
    "v_rcp_f32 v211, v211\n"
    "s_mov_b32 s73, 0x41a00000\n"
    "v_mov_b32 v228, 0x7fc00000\n"
    "v_cmp_le_f32 vcc, |v68|, s73\n"
    "s_nop 1\n"
    "v_cndmask_b32 v68, v228, v68, vcc\n"
    "v_cmp_le_f32 vcc, |v69|, s73\n"
    "s_nop 1\n"
    "v_cndmask_b32 v69, v228, v69, vcc\n"
    "v_cmp_le_f32 vcc, |v70|, s73\n"
    "s_nop 1\n"
    "v_cndmask_b32 v70, v228, v70, vcc\n"
    "v_cmp_le_f32 vcc, |v71|, s73\n"
    "s_nop 1\n"
    "v_cndmask_b32 v71, v228, v71, vcc\n"
    "v_cmp_le_f32 vcc, |v72|, s73\n"
    "s_nop 1\n"
    "v_cndmask_b32 v72, v228, v72, vcc\n"
    "v_cmp_le_f32 vcc, |v73|, s73\n"
    "s_nop 1\n"
    "v_cndmask_b32 v73, v228, v73, vcc\n"
    "v_cmp_le_f32 vcc, |v74|, s73\n"
    "s_nop 1\n"
    "v_cndmask_b32 v74, v228, v74, vcc\n"
    "s_add_u32 m0, s65, 75776\n"
    "s_add_u32 s72, s85, 8192\n"
    "buffer_load_dwordx4 v90, s[20:23], s72 offen offset:0 nt lds\n"
    "v_cmp_le_f32 vcc, |v75|, s73\n"
    "s_nop 1\n"
    "v_cndmask_b32 v75, v228, v75, vcc\n"
    "v_cvt_pk_bf16_f32 v220, v204, v205\n"
    "v_cvt_pk_bf16_f32 v221, v206, v207\n"
    "v_cvt_pk_bf16_f32 v222, v208, v209\n"
    "v_cvt_pk_bf16_f32 v223, v210, v211\n"
    "v_lshlrev_b32 v212, 16, v220\n"
    "v_and_b32 v213, 0xffff0000, v220\n"
    "v_lshlrev_b32 v214, 16, v221\n"
    "v_and_b32 v215, 0xffff0000, v221\n"
    "v_lshlrev_b32 v216, 16, v222\n"
    "v_and_b32 v217, 0xffff0000, v222\n"
    "v_lshlrev_b32 v218, 16, v223\n"
    "v_and_b32 v219, 0xffff0000, v223\n"
    "v_sub_f32 v204, v204, v212\n"
    "v_sub_f32 v205, v205, v213\n"
    "v_sub_f32 v206, v206, v214\n"
    "v_sub_f32 v207, v207, v215\n"
    "v_sub_f32 v208, v208, v216\n"
    "v_sub_f32 v209, v209, v217\n"
    "s_add_u32 m0, s65, 76800\n"
    "s_add_u32 s72, s85, 9216\n"
    "buffer_load_dwordx4 v88, s[20:23], s72 offen offset:0 nt lds\n"
    "v_sub_f32 v210, v210, v218\n"
    "v_sub_f32 v211, v211, v219\n"
    "v_cvt_pk_bf16_f32 v224, v204, v205\n"
    "v_cvt_pk_bf16_f32 v225, v206, v207\n"
    "v_cvt_pk_bf16_f32 v226, v208, v209\n"
    "v_cvt_pk_bf16_f32 v227, v210, v211\n"
    "v_mov_b32 v204, 0\n"
    "v_mov_b32 v205, 0\n"
    "v_mov_b32 v206, 0\n"
    "v_mov_b32 v207, 0\n"
    "v_swap_b32 v221, v222\n"
    "v_swap_b32 v225, v226\n"
    "v_swap_b32 v141, v142\n"
    "ds_write2_b64 v144, v[220:221], v[222:223] offset0:32 offset1:40\n"
    "ds_write2_b64 v144, v[224:225], v[226:227] offset0:48 offset1:56\n"
    "ds_write2_b64 v144, v[140:141], v[142:143] offset0:64 offset1:72\n"
    "ds_write2_b64 v144, v[204:205], v[206:207] offset0:80 offset1:88\n"
    "s_waitcnt vmcnt(31)\n"
    "v_fma_f32 v4, v4, v180, -v184\n"
    "v_fma_f32 v5, v5, v181, -v185\n"
    "v_fma_f32 v6, v6, v182, -v186\n"
    "s_add_u32 m0, s65, 77824\n"
    "s_add_u32 s72, s85, 10240\n"
    "buffer_load_dwordx4 v89, s[20:23], s72 offen offset:0 nt lds\n"
    "v_fma_f32 v7, v7, v183, -v187\n"
    "v_fma_f32 v8, v8, v180, -v184\n"
    "v_fma_f32 v9, v9, v181, -v185\n"
    "v_fma_f32 v10, v10, v182, -v186\n"
    "v_fma_f32 v11, v11, v183, -v187\n"
    "v_cvt_pk_bf16_f32 v4, v4, v5\n"
    "v_cvt_pk_bf16_f32 v5, v6, v7\n"
    "v_cvt_pk_bf16_f32 v6, v8, v9\n"
    "v_cvt_pk_bf16_f32 v7, v10, v11\n"
    "ds_write_b64 v163, v[4:5] offset:0\n"
    "ds_write_b64 v163, v[6:7] offset:1024\n"
    "v_fma_f32 v12, v12, v188, -v192\n"
    "v_fma_f32 v13, v13, v189, -v193\n"
    "v_fma_f32 v14, v14, v190, -v194\n"
    "v_fma_f32 v15, v15, v191, -v195\n"
    "v_fma_f32 v16, v16, v188, -v192\n"
    "v_fma_f32 v17, v17, v189, -v193\n"
    "v_fma_f32 v18, v18, v190, -v194\n"
    "v_fma_f32 v19, v19, v191, -v195\n"
    "v_cvt_pk_bf16_f32 v12, v12, v13\n"
    "s_add_u32 m0, s65, 78848\n"
    "s_add_u32 s72, s85, 11264\n"
    "buffer_load_dwordx4 v90, s[20:23], s72 offen offset:0 nt lds\n"
    "v_cvt_pk_bf16_f32 v13, v14, v15\n"
    "v_cvt_pk_bf16_f32 v14, v16, v17\n"
    "v_cvt_pk_bf16_f32 v15, v18, v19\n"
    "ds_write_b64 v163, v[12:13] offset:2048\n"
    "ds_write_b64 v163, v[14:15] offset:3072\n"
    "v_fma_f32 v20, v20, v196, -v200\n"
    "v_fma_f32 v21, v21, v197, -v201\n"
    "v_fma_f32 v22, v22, v198, -v202\n"
    "v_fma_f32 v23, v23, v199, -v203\n"
    "v_fma_f32 v24, v24, v196, -v200\n"
    "v_fma_f32 v25, v25, v197, -v201\n"
    "v_fma_f32 v26, v26, v198, -v202\n"
    "v_fma_f32 v27, v27, v199, -v203\n"
    "v_cvt_pk_bf16_f32 v20, v20, v21\n"
    "v_cvt_pk_bf16_f32 v21, v22, v23\n"
    "v_cvt_pk_bf16_f32 v22, v24, v25\n"
    "v_cvt_pk_bf16_f32 v23, v26, v27\n"
    "ds_write_b64 v163, v[20:21] offset:4096\n"
    "ds_write_b64 v163, v[22:23] offset:5120\n"
    "s_waitcnt vmcnt(16)\n"
    "v_fma_f32 v100, v100, v180, -v184\n"
    "s_add_u32 m0, s65, 49152\n"
    "s_add_u32 s72, s85, 12288\n"
    "buffer_load_dwordx4 v88, s[20:23], s72 offen offset:0 nt lds\n"
    "v_fma_f32 v101, v101, v181, -v185\n"
    "v_fma_f32 v102, v102, v182, -v186\n"
    "v_fma_f32 v103, v103, v183, -v187\n"
    "v_fma_f32 v104, v104, v180, -v184\n"
    "v_fma_f32 v105, v105, v181, -v185\n"
    "v_fma_f32 v106, v106, v182, -v186\n"
    "v_fma_f32 v107, v107, v183, -v187\n"
    "v_cvt_pk_bf16_f32 v100, v100, v101\n"
    "v_cvt_pk_bf16_f32 v101, v102, v103\n"
    "v_cvt_pk_bf16_f32 v102, v104, v105\n"
    "v_cvt_pk_bf16_f32 v103, v106, v107\n"
    "ds_write_b64 v163, v[100:101] offset:12288\n"
    "ds_write_b64 v163, v[102:103] offset:13312\n"
    "v_fma_f32 v108, v108, v188, -v192\n"
    "v_fma_f32 v109, v109, v189, -v193\n"
    "v_fma_f32 v110, v110, v190, -v194\n"
    "v_fma_f32 v111, v111, v191, -v195\n"
    "v_fma_f32 v112, v112, v188, -v192\n"
    "v_fma_f32 v113, v113, v189, -v193\n"
    "v_fma_f32 v114, v114, v190, -v194\n"
    "s_add_u32 m0, s65, 50176\n"
    "s_add_u32 s72, s85, 13312\n"
    "buffer_load_dwordx4 v89, s[20:23], s72 offen offset:0 nt lds\n"
    "v_fma_f32 v115, v115, v191, -v195\n"
    "v_cvt_pk_bf16_f32 v108, v108, v109\n"
    "v_cvt_pk_bf16_f32 v109, v110, v111\n"
    "v_cvt_pk_bf16_f32 v110, v112, v113\n"
    "v_cvt_pk_bf16_f32 v111, v114, v115\n"
    "ds_write_b64 v163, v[108:109] offset:14336\n"
    "ds_write_b64 v163, v[110:111] offset:15360\n"
    "v_fma_f32 v116, v116, v196, -v200\n"
    "v_fma_f32 v117, v117, v197, -v201\n"
    "v_fma_f32 v118, v118, v198, -v202\n"
    "v_fma_f32 v119, v119, v199, -v203\n"
    "v_fma_f32 v120, v120, v196, -v200\n"
    "v_fma_f32 v121, v121, v197, -v201\n"
    "v_fma_f32 v122, v122, v198, -v202\n"
    "v_fma_f32 v123, v123, v199, -v203\n"
    "v_cvt_pk_bf16_f32 v116, v116, v117\n"
    "v_cvt_pk_bf16_f32 v117, v118, v119\n"
    "v_cvt_pk_bf16_f32 v118, v120, v121\n"
    "v_cvt_pk_bf16_f32 v119, v122, v123\n"
    "ds_write_b64 v163, v[116:117] offset:16384\n"
    "ds_write_b64 v163, v[118:119] offset:17408\n"
    "s_waitcnt vmcnt(19)\n"
    "ds_read_b128 v[36:39], v154 offset:0\n"
    "ds_read_b128 v[40:43], v155 offset:0\n"
    "ds_read_b128 v[44:47], v154 offset:128\n"
    "ds_read_b128 v[48:51], v155 offset:128\n"
    "ds_read_b128 v[52:55], v154 offset:256\n"
    "ds_read_b128 v[56:59], v155 offset:256\n"
    "s_waitcnt lgkmcnt(0)\n"
    "s_barrier\n"
    "ds_read_b128 a[112:115], v252 offset:0\n"
    "ds_read_b128 a[116:119], v252 offset:2048\n"
    "ds_read_b128 a[120:123], v252 offset:4096\n"
    "ds_read_b128 a[124:127], v252 offset:6144\n"
    "ds_read_b128 a[128:131], v252 offset:8192\n"
    "ds_read_b128 a[132:135], v252 offset:10240\n"
    "ds_read_b128 a[136:139], v252 offset:1024\n"
    "ds_read_b128 a[140:143], v252 offset:3072\n"
    "ds_read_b128 a[144:147], v252 offset:5120\n"
    "ds_read_b128 a[148:151], v252 offset:7168\n"
    "ds_read_b128 a[152:155], v252 offset:9216\n"
    "ds_read_b128 a[156:159], v252 offset:11264\n"
    "ds_read_b128 a[0:3], v252 offset:12288\n"
    "ds_read_b128 a[4:7], v252 offset:14336\n"
    "ds_read_b128 a[8:11], v252 offset:16384\n"
    "ds_read_b128 a[12:15], v252 offset:18432\n"
    "ds_read_b128 a[16:19], v252 offset:20480\n"
    "ds_read_b128 a[20:23], v252 offset:22528\n"
    "ds_read_b128 a[24:27], v252 offset:13312\n"
    "ds_read_b128 a[28:31], v252 offset:15360\n"
    "ds_read_b128 a[32:35], v252 offset:17408\n"
    "ds_read_b128 a[36:39], v252 offset:19456\n"
    "ds_read_b128 a[40:43], v252 offset:21504\n"
    "ds_read_b128 a[44:47], v252 offset:23552\n"
    "s_add_u32 m0, s65, 51200\n"
    "s_add_u32 s72, s85, 14336\n"
    "buffer_load_dwordx4 v90, s[20:23], s72 offen offset:0 nt lds\n"
    "s_add_u32 m0, s65, 52224\n"
    "s_add_u32 s72, s85, 15360\n"
    "buffer_load_dwordx4 v88, s[20:23], s72 offen offset:0 nt lds\n"
    "v_pk_maximum3_f16 v98, v36, v37, v98\n"
    "v_pk_maximum3_f16 v98, v38, v39, v98\n"
    "v_pk_add_u16 v36, s89, v36 clamp\n"
    "v_pk_add_u16 v37, s89, v37 clamp\n"
    "v_pk_add_u16 v38, s89, v38 clamp\n"
    "v_pk_add_u16 v39, s89, v39 clamp\n"
    "v_pk_maximum3_f16 v98, v40, v41, v98\n"
    "v_pk_maximum3_f16 v98, v42, v43, v98\n"
    "v_pk_add_u16 v40, s89, v40 clamp\n"
    "v_pk_add_u16 v41, s89, v41 clamp\n"
    "v_pk_add_u16 v42, s89, v42 clamp\n"
    "v_pk_add_u16 v43, s89, v43 clamp\n"
    "v_pk_maximum3_f16 v98, v44, v45, v98\n"
    "v_pk_maximum3_f16 v98, v46, v47, v98\n"
    "v_pk_add_u16 v44, s89, v44 clamp\n"
    "v_pk_add_u16 v45, s89, v45 clamp\n"
    "v_pk_add_u16 v46, s89, v46 clamp\n"
    "v_pk_add_u16 v47, s89, v47 clamp\n"
    "v_pk_maximum3_f16 v98, v48, v49, v98\n"
    "v_pk_maximum3_f16 v98, v50, v51, v98\n"
    "v_pk_add_u16 v48, s89, v48 clamp\n"
    "v_pk_add_u16 v49, s89, v49 clamp\n"
    "v_pk_add_u16 v50, s89, v50 clamp\n"
    "v_pk_add_u16 v51, s89, v51 clamp\n"
    "v_pk_maximum3_f16 v98, v52, v53, v98\n"
    "v_pk_maximum3_f16 v98, v54, v55, v98\n"
    "v_pk_add_u16 v52, s89, v52 clamp\n"
    "v_pk_add_u16 v53, s89, v53 clamp\n"
    "v_pk_add_u16 v54, s89, v54 clamp\n"
    "v_pk_add_u16 v55, s89, v55 clamp\n"
    "v_pk_maximum3_f16 v98, v56, v57, v98\n"
    "v_pk_maximum3_f16 v98, v58, v59, v98\n"
    "v_pk_add_u16 v56, s89, v56 clamp\n"
    "v_pk_add_u16 v57, s89, v57 clamp\n"
    "v_pk_add_u16 v58, s89, v58 clamp\n"
    "v_pk_add_u16 v59, s89, v59 clamp\n"
    "v_mov_b32 v108, 0\n"
    "v_mov_b32 v109, 0\n"
    "v_mov_b32 v110, 0\n"
    "v_mov_b32 v111, 0\n"
    "v_mov_b32 v112, 0\n"
    "v_mov_b32 v113, 0\n"
    "v_mov_b32 v114, 0\n"
    "v_mov_b32 v115, 0\n"
    "v_mov_b32 v116, 0\n"
    "v_mov_b32 v117, 0\n"
    "v_mov_b32 v118, 0\n"
    "v_mov_b32 v119, 0\n"
    "v_mov_b32 v120, 0\n"
    "v_mov_b32 v121, 0\n"
    "v_mov_b32 v122, 0\n"
    "v_mov_b32 v123, 0\n"
    "s_waitcnt lgkmcnt(15)\n"
    "v_mfma_f32_16x16x32_bf16 v[4:7], a[112:115], v[36:39], 0\n"
    "s_waitcnt lgkmcnt(15)\n"
    "v_mfma_f32_16x16x32_bf16 v[4:7], a[116:119], v[40:43], v[4:7]\n"
    "s_waitcnt lgkmcnt(15)\n"
    "v_mfma_f32_16x16x32_bf16 v[4:7], a[120:123], v[44:47], v[4:7]\n"
    "s_waitcnt lgkmcnt(15)\n"
    "v_mfma_f32_16x16x32_bf16 v[4:7], a[124:127], v[48:51], v[4:7]\n"
    "s_waitcnt lgkmcnt(15)\n"
    "v_mfma_f32_16x16x32_bf16 v[4:7], a[128:131], v[52:55], v[4:7]\n"
    "s_waitcnt lgkmcnt(15)\n"
    "v_mfma_f32_16x16x32_bf16 v[4:7], a[132:135], v[56:59], v[4:7]\n"
    "s_waitcnt lgkmcnt(15)\n"
    "v_mfma_f32_16x16x32_bf16 v[20:23], a[136:139], v[36:39], 0\n"
    "v_accvgpr_write_b32 a192, v36\n"
    "v_accvgpr_write_b32 a193, v37\n"
    "v_accvgpr_write_b32 a194, v38\n"
    "v_accvgpr_write_b32 a195, v39\n"
    "s_waitcnt lgkmcnt(15)\n"
    "v_mfma_f32_16x16x32_bf16 v[20:23], a[140:143], v[40:43], v[20:23]\n"
    "v_accvgpr_write_b32 a196, v40\n"
    "v_accvgpr_write_b32 a197, v41\n"
    "v_accvgpr_write_b32 a198, v42\n"
    "v_accvgpr_write_b32 a199, v43\n"
    "s_waitcnt lgkmcnt(15)\n"
    "v_mfma_f32_16x16x32_bf16 v[20:23], a[144:147], v[44:47], v[20:23]\n"
    "v_accvgpr_write_b32 a200, v44\n"
    "v_accvgpr_write_b32 a201, v45\n"
    "v_accvgpr_write_b32 a202, v46\n"
    "v_accvgpr_write_b32 a203, v47\n"
    "s_waitcnt lgkmcnt(14)\n"
    "v_mfma_f32_16x16x32_bf16 v[20:23], a[148:151], v[48:51], v[20:23]\n"
    "v_accvgpr_write_b32 a204, v48\n"
    "v_accvgpr_write_b32 a205, v49\n"
    "v_accvgpr_write_b32 a206, v50\n"
    "v_accvgpr_write_b32 a207, v51\n"
    "s_waitcnt lgkmcnt(13)\n"
    "v_mfma_f32_16x16x32_bf16 v[20:23], a[152:155], v[52:55], v[20:23]\n"
    "v_accvgpr_write_b32 a208, v52\n"
    "v_accvgpr_write_b32 a209, v53\n"
    "v_accvgpr_write_b32 a210, v54\n"
    "v_accvgpr_write_b32 a211, v55\n"
    "s_waitcnt lgkmcnt(12)\n"
    "v_mfma_f32_16x16x32_bf16 v[20:23], a[156:159], v[56:59], v[20:23]\n"
    "v_accvgpr_write_b32 a212, v56\n"
    "v_accvgpr_write_b32 a213, v57\n"
    "v_accvgpr_write_b32 a214, v58\n"
    "v_accvgpr_write_b32 a215, v59\n"
    "s_add_u32 m0, s65, 53248\n"
    "s_add_u32 s72, s85, 16384\n"
    "buffer_load_dwordx4 v89, s[20:23], s72 offen offset:0 nt lds\n"
    "buffer_load_dwordx2 v[236:237], v87, s[36:39], 0 offen offset:0 sc1\n"
    "v_mov_b32 v77, 155904\n"
    "v_max3_f32 v76, v4, v5, v6\n"
    "v_max3_f32 v76, v76, v7, v20\n"
    "v_max3_f32 v76, v76, v21, v22\n"
    "v_max_f32 v76, v76, v23\n"
    "v_mov_b32 v78, v76\n"
    "s_nop 1\n"
    "v_permlane32_swap_b32 v76, v78\n"
    "v_max_f32 v76, v76, v78\n"
    "v_mov_b32 v78, v76\n"
    "s_nop 1\n"
    "v_permlane16_swap_b32 v76, v78\n"
    "v_max_f32 v76, v76, v78\n"
    "ds_max_f32 v77, v76\n"
    "s_add_u32 m0, s65, 54272\n"
    "s_add_u32 s72, s85, 17408\n"
    "buffer_load_dwordx4 v90, s[20:23], s72 offen offset:0 nt lds\n"
    "s_add_u32 m0, s65, 98304\n"
    "s_add_u32 s72, s85, 18432\n"
    "buffer_load_dwordx4 v88, s[20:23], s72 offen offset:0 nt lds\n"
    "s_waitcnt lgkmcnt(0)\n"
    "s_barrier\n"
    "ds_read_b32 v76, v77\n"
    "s_waitcnt vmcnt(14)\n"
    "buffer_store_dwordx2 v[238:239], v240, s[36:39], s80 offen offset:0 sc1\n"
    "s_lshl_b32 s73, s79, 8\n"
    "s_add_u32 s80, s80, s73\n"
    "s_mul_i32 s73, s79, 98304\n"
    "s_add_u32 s71, s71, s73\n"
    "s_waitcnt lgkmcnt(0)\n"
    "v_add_f32 v76, 0xc2b00000, v76\n"
    "v_sub_f32 v68, v68, v76\n"
    "v_sub_f32 v69, v69, v76\n"
    "v_sub_f32 v70, v70, v76\n"
    "v_sub_f32 v71, v71, v76\n"
    "v_sub_f32 v72, v72, v76\n"
    "v_sub_f32 v73, v73, v76\n"
    "v_sub_f32 v74, v74, v76\n"
    "v_sub_f32 v75, v75, v76\n"
    "ds_write_b128 v86, v[68:71] offset:0\n"
    "ds_write_b128 v86, v[72:75] offset:16\n"
    "s_mul_i32 s73, s79, 768\n"
    "v_add_u32 v77, s73, v250\n"
    "s_add_u32 m0, s65, 99328\n"
    "s_add_u32 s72, s85, 19456\n"
    "buffer_load_dwordx4 v89, s[20:23], s72 offen offset:0 nt lds\n"
    "s_waitcnt lgkmcnt(0)\n"
    "s_barrier\n"
    "ds_read_b128 v[16:19], v77 offset:0\n"
    "ds_read_b128 v[32:35], v77 offset:16\n"
    "ds_read_b128 v[48:51], v77 offset:128\n"
    "ds_read_b128 v[64:67], v77 offset:144\n"
    "ds_read_b128 v[100:103], v77 offset:768\n"
    "ds_read_b128 v[104:107], v77 offset:784\n"
    "s_add_u32 m0, s65, 100352\n"
    "s_add_u32 s72, s85, 20480\n"
    "buffer_load_dwordx4 v90, s[20:23], s72 offen offset:0 nt lds\n"
    "s_add_u32 m0, s65, 101376\n"
    "s_add_u32 s72, s85, 21504\n"
    "buffer_load_dwordx4 v88, s[20:23], s72 offen offset:0 nt lds\n"
    "s_add_u32 m0, s65, 102400\n"
    "s_add_u32 s72, s85, 22528\n"
    "buffer_load_dwordx4 v89, s[20:23], s72 offen offset:0 nt lds\n"
    "s_add_u32 m0, s65, 103424\n"
    "s_add_u32 s72, s85, 23552\n"
    "buffer_load_dwordx4 v90, s[20:23], s72 offen offset:0 nt lds\n"
    "s_waitcnt lgkmcnt(3)\n"
    "v_mfma_f32_16x16x32_bf16 v[36:39], a[0:3], a[192:195], v[48:51]\n"
    "s_waitcnt vmcnt(14)\n"
    "ds_read_b128 v[192:195], v97 offset:24704\n"
    "ds_read_b128 v[200:203], v97 offset:24832\n"
    "v_mfma_f32_16x16x32_bf16 v[36:39], a[4:7], a[196:199], v[36:39]\n"
    "ds_read_b128 v[196:199], v96 offset:24832\n"
    "v_add_f32 v7, v7, v19\n"
    "v_mfma_f32_16x16x32_bf16 v[36:39], a[8:11], a[200:203], v[36:39]\n"
    "ds_read_b128 v[188:191], v96 offset:24704\n"
    "v_add_f32 v6, v6, v18\n"
    "s_waitcnt lgkmcnt(6)\n"
    "v_mfma_f32_16x16x32_bf16 v[52:55], a[24:27], a[192:195], v[64:67]\n"
    "ds_read_b128 v[184:187], v97 offset:24576\n"
    "ds_read_b128 v[180:183], v96 offset:24576\n"
    "v_mfma_f32_16x16x32_bf16 v[36:39], a[12:15], a[204:207], v[36:39]\n"
    "v_exp_f32 v6, v6\n"
    "v_add_f32 v23, v23, v35\n"
    "v_add_f32 v20, v20, v32\n"
    "v_mfma_f32_16x16x32_bf16 v[52:55], a[28:31], a[196:199], v[52:55]\n"
    "v_add_f32 v4, v4, v16\n"
    "v_add_f32 v5, v5, v17\n"
    "v_mfma_f32_16x16x32_bf16 v[52:55], a[32:35], a[200:203], v[52:55]\n"
    "v_exp_f32 v4, v4\n"
    "v_mfma_f32_16x16x32_bf16 v[36:39], a[16:19], a[208:211], v[36:39]\n"
    "s_waitcnt lgkmcnt(4)\n"
    "v_add_f32 v21, v21, v33\n"
    "v_pk_maximum3_f16 v98, v200, v201, v98\n"
    "v_mfma_f32_16x16x32_bf16 v[36:39], a[20:23], a[212:215], v[36:39]\n"
    "s_waitcnt lgkmcnt(3)\n"
    "v_pk_maximum3_f16 v98, v202, v203, v98\n"
    "v_pk_add_u16 v200, s89, v200 clamp\n"
    "v_pk_maximum3_f16 v98, v196, v197, v98\n"
    "v_pk_add_u16 v203, s89, v203 clamp\n"
    "v_pk_add_u16 v201, s89, v201 clamp\n"
    "v_pk_maximum3_f16 v98, v198, v199, v98\n"
    "v_mfma_f32_16x16x32_bf16 v[52:55], a[36:39], a[204:207], v[52:55]\n"
    "v_pk_maximum3_f16 v98, v192, v193, v98\n"
    "v_pk_maximum3_f16 v98, v194, v195, v98\n"
    "v_mfma_f32_16x16x32_bf16 v[52:55], a[40:43], a[208:211], v[52:55]\n"
    "s_waitcnt lgkmcnt(1)\n"
    "v_pk_maximum3_f16 v98, v188, v189, v98\n"
    "v_pk_maximum3_f16 v98, v190, v191, v98\n"
    "v_pk_add_u16 v197, s89, v197 clamp\n"
    "v_exp_f32 v23, v23\n"
    "v_pk_add_u16 v202, s89, v202 clamp\n"
    "v_pk_maximum3_f16 v98, v184, v185, v98\n"
    "v_pk_add_u16 v194, s89, v194 clamp\n"
    "v_mfma_f32_16x16x32_bf16 v[24:27], a[156:159], v[200:203], v[32:35]\n"
    "v_pk_add_u16 v191, s89, v191 clamp\n"
    "v_accvgpr_write_b32 a237, v201\n"
    "v_mfma_f32_16x16x32_bf16 v[40:43], a[20:23], v[200:203], v[48:51]\n"
    "v_pk_add_u16 v198, s89, v198 clamp\n"
    "v_pk_add_u16 v199, s89, v199 clamp\n"
    "v_mfma_f32_16x16x32_bf16 v[52:55], a[44:47], a[212:215], v[52:55]\n"
    "v_pk_add_u16 v196, s89, v196 clamp\n"
    "v_pk_add_u16 v184, s89, v184 clamp\n"
    "v_pk_add_u16 v189, s89, v189 clamp\n"
    "v_pk_add_u16 v192, s89, v192 clamp\n"
    "v_pk_maximum3_f16 v98, v186, v187, v98\n"
    "v_pk_add_u16 v188, s89, v188 clamp\n"
    "v_pk_add_u16 v193, s89, v193 clamp\n"
    "v_mfma_f32_16x16x32_bf16 v[8:11], a[132:135], v[200:203], v[16:19]\n"
    "v_pk_add_u16 v187, s89, v187 clamp\n"
    "v_exp_f32 v21, v21\n"
    "v_mfma_f32_16x16x32_bf16 v[56:59], a[44:47], v[200:203], v[64:67]\n"
    "v_add_f32 v22, v22, v34\n"
    "v_accvgpr_write_b32 a239, v203\n"
    "v_mfma_f32_16x16x32_bf16 v[24:27], a[152:155], v[196:199], v[24:27]\n"
    "v_pk_add_u16 v186, s89, v186 clamp\n"
    "v_exp_f32 v52, v52\n"
    "v_mfma_f32_16x16x32_bf16 v[40:43], a[16:19], v[196:199], v[40:43]\n"
    "s_waitcnt lgkmcnt(0)\n"
    "v_exp_f32 v36, v36\n"
    "v_accvgpr_write_b32 a223, v187\n"
    "v_pk_add_u16 v195, s89, v195 clamp\n"
    "v_exp_f32 v7, v7\n"
    "v_accvgpr_write_b32 a227, v191\n"
    "v_pk_maximum3_f16 v98, v180, v181, v98\n"
    "v_mfma_f32_16x16x32_bf16 v[24:27], a[148:151], v[192:195], v[24:27]\n"
    "v_accvgpr_write_b32 a224, v188\n"
    "v_exp_f32 v53, v53\n"
    "v_mfma_f32_16x16x32_bf16 v[8:11], a[128:131], v[196:199], v[8:11]\n"
    "v_accvgpr_write_b32 a233, v197\n"
    "v_pk_add_u16 v185, s89, v185 clamp\n"
    "v_pk_add_u16 v190, s89, v190 clamp\n"
    "v_mfma_f32_16x16x32_bf16 v[56:59], a[40:43], v[196:199], v[56:59]\n"
    "v_pk_add_u16 v180, s89, v180 clamp\n"
    "v_pk_maximum3_f16 v98, v182, v183, v98\n"
    "v_pk_add_u16 v181, s89, v181 clamp\n"
    "v_mfma_f32_16x16x32_bf16 v[8:11], a[124:127], v[192:195], v[8:11]\n"
    "v_exp_f32 v20, v20\n"
    "v_accvgpr_write_b32 a238, v202\n"
    "v_mfma_f32_16x16x32_bf16 v[24:27], a[144:147], v[188:191], v[24:27]\n"
    "v_exp_f32 v5, v5\n"
    "v_mfma_f32_16x16x32_bf16 v[40:43], a[12:15], v[192:195], v[40:43]\n"
    "v_pk_add_u16 v182, s89, v182 clamp\n"
    "v_accvgpr_write_b32 a226, v190\n"
    "v_accvgpr_write_b32 a222, v186\n"
    "v_mfma_f32_16x16x32_bf16 v[56:59], a[36:39], v[192:195], v[56:59]\n"
    "v_cvt_pk_bf16_f32 v70, v20, v21\n"
    "v_pk_add_u16 v183, s89, v183 clamp\n"
    "v_mfma_f32_16x16x32_bf16 v[8:11], a[120:123], v[188:191], v[8:11]\n"
    "v_exp_f32 v54, v54\n"
    "v_mfma_f32_16x16x32_bf16 v[24:27], a[140:143], v[184:187], v[24:27]\n"
    "v_exp_f32 v22, v22\n"
    "v_mfma_f32_16x16x32_bf16 v[8:11], a[116:119], v[184:187], v[8:11]\n"
    "v_cvt_pk_bf16_f32 v68, v4, v5\n"
    "v_accvgpr_write_b32 a221, v185\n"
    "v_mfma_f32_16x16x32_bf16 v[40:43], a[8:11], v[188:191], v[40:43]\n"
    "v_exp_f32 v38, v38\n"
    "v_mfma_f32_16x16x32_bf16 v[24:27], a[136:139], v[180:183], v[24:27]\n"
    "v_exp_f32 v39, v39\n"
    "v_mfma_f32_16x16x32_bf16 v[56:59], a[32:35], v[188:191], v[56:59]\n"
    "v_cvt_pk_bf16_f32 v73, v38, v39\n"
    "v_cvt_pk_bf16_f32 v74, v52, v53\n"
    "v_accvgpr_write_b32 a235, v199\n"
    "v_mfma_f32_16x16x32_bf16 v[8:11], a[112:115], v[180:183], v[8:11]\n"
    "v_accvgpr_write_b32 a216, v180\n"
    "v_exp_f32 v55, v55\n"
    "v_mfma_f32_16x16x32_bf16 v[56:59], a[28:31], v[184:187], v[56:59]\n"
    "v_exp_f32 v27, v27\n"
    "v_accvgpr_write_b32 a231, v195\n"
    "v_mfma_f32_16x16x32_bf16 v[40:43], a[4:7], v[184:187], v[40:43]\n"
    "v_exp_f32 v25, v25\n"
    "v_exp_f32 v37, v37\n"
    "v_mfma_f32_16x16x32_bf16 v[40:43], a[0:3], v[180:183], v[40:43]\n"
    "v_accvgpr_write_b32 a228, v192\n"
    "v_exp_f32 v11, v11\n"
    "v_accvgpr_write_b32 a225, v189\n"
    "v_accvgpr_write_b32 a217, v181\n"
    "v_mfma_f32_16x16x32_bf16 v[56:59], a[24:27], v[180:183], v[56:59]\n"
    "v_cvt_pk_bf16_f32 v75, v54, v55\n"
    "v_cvt_pk_bf16_f32 v71, v22, v23\n"
    "v_exp_f32 v24, v24\n"
    "v_exp_f32 v8, v8\n"
    "v_exp_f32 v26, v26\n"
    "v_exp_f32 v43, v43\n"
    "v_exp_f32 v10, v10\n"
    "v_accvgpr_write_b32 a234, v198\n"
    "v_exp_f32 v58, v58\n"
    "v_cvt_pk_bf16_f32 v69, v6, v7\n"
    "v_exp_f32 v42, v42\n"
    "v_exp_f32 v41, v41\n"
    "v_accvgpr_write_b32 a219, v183\n"
    "v_accvgpr_write_b32 a220, v184\n"
    "v_accvgpr_write_b32 a230, v194\n"
    "v_cvt_pk_bf16_f32 v81, v42, v43\n"
    "v_cvt_pk_bf16_f32 v78, v24, v25\n"
    "v_cvt_pk_bf16_f32 v72, v36, v37\n"
    "v_cvt_pk_bf16_f32 v77, v10, v11\n"
    "v_exp_f32 v59, v59\n"
    "v_exp_f32 v40, v40\n"
    "v_accvgpr_write_b32 a232, v196\n"
    "v_exp_f32 v57, v57\n"
    "v_cvt_pk_bf16_f32 v80, v40, v41\n"
    "v_exp_f32 v9, v9\n"
    "v_exp_f32 v56, v56\n"
    "v_accvgpr_write_b32 a218, v182\n"
    "v_accvgpr_write_b32 a236, v200\n"
    "v_cvt_pk_bf16_f32 v76, v8, v9\n"
    "v_cvt_pk_bf16_f32 v83, v58, v59\n"
    "v_cvt_pk_bf16_f32 v82, v56, v57\n"
    "v_accvgpr_write_b32 a229, v193\n"
    "v_cvt_pk_bf16_f32 v79, v26, v27\n"
    "s_waitcnt vmcnt(8)\n"
    "v_cmp_eq_u64 s[54:55], v[236:237], v[238:239]\n"
    "s_nop 0\n"
    "s_cmp_eq_u64 s[54:55], -1\n"
    "s_cbranch_scc0 .LpollAK\n"
    ".LpollAKr:\n"
    "s_add_u32 m0, s65, 24576\n"
    "s_add_u32 s72, s65, 49152\n"
    "buffer_load_dwordx4 v1, s[16:19], s72 offen offset:0 sc1 lds\n"
    "s_add_u32 m0, s65, 25600\n"
    "s_add_u32 s72, s65, 50176\n"
    "buffer_load_dwordx4 v1, s[16:19], s72 offen offset:0 sc1 lds\n"
    "s_add_u32 m0, s65, 26624\n"
    "s_add_u32 s72, s65, 51200\n"
    "buffer_load_dwordx4 v1, s[16:19], s72 offen offset:0 sc1 lds\n"
    "s_add_u32 m0, s65, 27648\n"
    "s_add_u32 s72, s65, 52224\n"
    "buffer_load_dwordx4 v1, s[16:19], s72 offen offset:0 sc1 lds\n"
    "s_add_u32 m0, s65, 28672\n"
    "s_add_u32 s72, s65, 53248\n"
    "buffer_load_dwordx4 v1, s[16:19], s72 offen offset:0 sc1 lds\n"
    "s_add_u32 m0, s65, 29696\n"
    "s_add_u32 s72, s65, 54272\n"
    "buffer_load_dwordx4 v1, s[16:19], s72 offen offset:0 sc1 lds\n"
    "s_waitcnt vmcnt(13)\n"
    "ds_read_b128 v[156:159], v96 offset:0\n"
    "ds_read_b128 v[172:175], v96 offset:256\n"
    "ds_read_b128 v[168:171], v97 offset:128\n"
    "ds_read_b128 v[176:179], v97 offset:256\n"
    "ds_read_b128 v[160:163], v97 offset:0\n"
    "ds_read_b128 v[164:167], v96 offset:128\n"
    "s_waitcnt lgkmcnt(5)\n"
    "v_pk_maximum3_f16 v98, v156, v157, v98\n"
    "v_pk_maximum3_f16 v98, v158, v159, v98\n"
    "v_pk_add_u16 v157, s89, v157 clamp\n"
    "v_pk_add_u16 v158, s89, v158 clamp\n"
    "v_pk_add_u16 v156, s89, v156 clamp\n"
    "v_pk_add_u16 v159, s89, v159 clamp\n"
    "s_waitcnt lgkmcnt(1)\n"
    "v_pk_maximum3_f16 v98, v160, v161, v98\n"
    "s_add_u32 m0, s65, 73728\n"
    "s_add_u32 s72, s65, 73728\n"
    "buffer_load_dwordx4 v1, s[16:19], s72 offen offset:0 sc1 lds\n"
    "v_mfma_f32_16x16x32_bf16 v[28:31], a[136:139], v[156:159], v[32:35]\n"
    "s_waitcnt lgkmcnt(0)\n"
    "v_pk_maximum3_f16 v98, v162, v163, v98\n"
    "v_pk_maximum3_f16 v98, v164, v165, v98\n"
    "v_mfma_f32_16x16x32_bf16 v[12:15], a[112:115], v[156:159], v[16:19]\n"
    "v_pk_add_u16 v161, s89, v161 clamp\n"
    "v_pk_maximum3_f16 v98, v166, v167, v98\n"
    "v_pk_add_u16 v160, s89, v160 clamp\n"
    "v_pk_add_u16 v162, s89, v162 clamp\n"
    "v_mfma_f32_16x16x32_bf16 v[60:63], a[24:27], v[156:159], v[64:67]\n"
    "v_pk_maximum3_f16 v98, v168, v169, v98\n"
    "v_pk_add_u16 v163, s89, v163 clamp\n"
    "v_mfma_f32_16x16x32_bf16 v[44:47], a[0:3], v[156:159], v[48:51]\n"
    "v_pk_maximum3_f16 v98, v170, v171, v98\n"
    "v_pk_add_u16 v164, s89, v164 clamp\n"
    "v_mfma_f32_16x16x32_bf16 v[12:15], a[116:119], v[160:163], v[12:15]\n"
    "v_pk_add_u16 v165, s89, v165 clamp\n"
    "s_add_u32 m0, s65, 74752\n"
    "s_add_u32 s72, s65, 74752\n"
    "buffer_load_dwordx4 v1, s[16:19], s72 offen offset:0 sc1 lds\n"
    "v_pk_add_u16 v169, s89, v169 clamp\n"
    "v_pk_add_u16 v168, s89, v168 clamp\n"
    "v_mfma_f32_16x16x32_bf16 v[60:63], a[28:31], v[160:163], v[60:63]\n"
    "v_pk_maximum3_f16 v98, v172, v173, v98\n"
    "v_pk_add_u16 v170, s89, v170 clamp\n"
    "v_mfma_f32_16x16x32_bf16 v[28:31], a[140:143], v[160:163], v[28:31]\n"
    "v_pk_maximum3_f16 v98, v174, v175, v98\n"
    "v_pk_add_u16 v166, s89, v166 clamp\n"
    "v_pk_add_u16 v167, s89, v167 clamp\n"
    "v_mfma_f32_16x16x32_bf16 v[44:47], a[4:7], v[160:163], v[44:47]\n"
    "v_pk_maximum3_f16 v98, v176, v177, v98\n"
    "v_accvgpr_write_b32 a251, v167\n"
    "v_mfma_f32_16x16x32_bf16 v[44:47], a[8:11], v[164:167], v[44:47]\n"
    "v_pk_maximum3_f16 v98, v178, v179, v98\n"
    "v_pk_add_u16 v176, s89, v176 clamp\n"
    "v_mfma_f32_16x16x32_bf16 v[60:63], a[32:35], v[164:167], v[60:63]\n"
    "v_pk_add_u16 v171, s89, v171 clamp\n"
    "s_add_u32 m0, s65, 75776\n"
    "s_add_u32 s72, s65, 75776\n"
    "buffer_load_dwordx4 v1, s[16:19], s72 offen offset:0 sc1 lds\n"
    "v_pk_add_u16 v174, s89, v174 clamp\n"
    "v_mfma_f32_16x16x32_bf16 v[28:31], a[144:147], v[164:167], v[28:31]\n"
    "v_pk_add_u16 v173, s89, v173 clamp\n"
    "v_pk_add_u16 v172, s89, v172 clamp\n"
    "v_mfma_f32_16x16x32_bf16 v[12:15], a[120:123], v[164:167], v[12:15]\n"
    "v_accvgpr_write_b32 a254, v170\n"
    "v_pk_add_u16 v178, s89, v178 clamp\n"
    "v_mfma_f32_16x16x32_bf16 v[28:31], a[148:151], v[168:171], v[28:31]\n"
    "v_accvgpr_write_b32 a243, v159\n"
    "v_accvgpr_write_b32 a162, v174\n"
    "v_mfma_f32_16x16x32_bf16 v[12:15], a[124:127], v[168:171], v[12:15]\n"
    "v_pk_add_u16 v179, s89, v179 clamp\n"
    "v_pk_add_u16 v175, s89, v175 clamp\n"
    "v_mfma_f32_16x16x32_bf16 v[44:47], a[12:15], v[168:171], v[44:47]\n"
    "v_accvgpr_write_b32 a166, v178\n"
    "v_accvgpr_write_b32 a161, v173\n"
    "v_accvgpr_write_b32 a245, v161\n"
    "s_add_u32 m0, s65, 76800\n"
    "s_add_u32 s72, s65, 76800\n"
    "buffer_load_dwordx4 v1, s[16:19], s72 offen offset:0 sc1 lds\n"
    "v_mfma_f32_16x16x32_bf16 v[28:31], a[152:155], v[172:175], v[28:31]\n"
    "v_accvgpr_write_b32 a163, v175\n"
    "v_pk_add_u16 v177, s89, v177 clamp\n"
    "v_mfma_f32_16x16x32_bf16 v[44:47], a[16:19], v[172:175], v[44:47]\n"
    "v_accvgpr_write_b32 a160, v172\n"
    "v_accvgpr_write_b32 a252, v168\n"
    "v_mfma_f32_16x16x32_bf16 v[28:31], a[156:159], v[176:179], v[28:31]\n"
    "v_accvgpr_write_b32 a249, v165\n"
    "v_accvgpr_write_b32 a255, v171\n"
    "v_mfma_f32_16x16x32_bf16 v[12:15], a[128:131], v[172:175], v[12:15]\n"
    "v_accvgpr_write_b32 a247, v163\n"
    "v_accvgpr_write_b32 a164, v176\n"
    "v_mfma_f32_16x16x32_bf16 v[60:63], a[36:39], v[168:171], v[60:63]\n"
    "v_accvgpr_write_b32 a242, v158\n"
    "v_accvgpr_write_b32 a250, v166\n"
    "v_mfma_f32_16x16x32_bf16 v[44:47], a[20:23], v[176:179], v[44:47]\n"
    "v_accvgpr_write_b32 a241, v157\n"
    "s_add_u32 m0, s65, 77824\n"
    "s_add_u32 s72, s65, 77824\n"
    "buffer_load_dwordx4 v1, s[16:19], s72 offen offset:0 sc1 lds\n"
    "v_accvgpr_write_b32 a248, v164\n"
    "v_mfma_f32_16x16x32_bf16 v[12:15], a[132:135], v[176:179], v[12:15]\n"
    "v_accvgpr_write_b32 a246, v162\n"
    "v_exp_f32 v29, v29\n"
    "v_mfma_f32_16x16x32_bf16 v[60:63], a[40:43], v[172:175], v[60:63]\n"
    "v_exp_f32 v28, v28\n"
    "v_mfma_f32_16x16x32_bf16 v[60:63], a[44:47], v[176:179], v[60:63]\n"
    "v_exp_f32 v31, v31\n"
    "v_exp_f32 v30, v30\n"
    "v_exp_f32 v45, v45\n"
    "v_cvt_pk_bf16_f32 v86, v28, v29\n"
    "v_exp_f32 v47, v47\n"
    "v_accvgpr_write_b32 a240, v156\n"
    "v_exp_f32 v12, v12\n"
    "v_exp_f32 v14, v14\n"
    "v_accvgpr_write_b32 a167, v179\n"
    "v_exp_f32 v15, v15\n"
    "s_add_u32 m0, s65, 78848\n"
    "s_add_u32 s72, s65, 78848\n"
    "buffer_load_dwordx4 v1, s[16:19], s72 offen offset:0 sc1 lds\n"
    "v_accvgpr_write_b32 a253, v169\n"
    "v_exp_f32 v63, v63\n"
    "v_exp_f32 v13, v13\n"
    "v_exp_f32 v44, v44\n"
    "v_cvt_pk_bf16_f32 v84, v12, v13\n"
    "v_cvt_pk_bf16_f32 v87, v30, v31\n"
    "v_exp_f32 v46, v46\n"
    "v_exp_f32 v61, v61\n"
    "v_accvgpr_write_b32 a165, v177\n"
    "v_exp_f32 v60, v60\n"
    "v_exp_f32 v62, v62\n"
    "v_cvt_pk_bf16_f32 v90, v60, v61\n"
    "v_accvgpr_write_b32 a244, v160\n"
    "v_cvt_pk_bf16_f32 v85, v14, v15\n"
    "v_cvt_pk_bf16_f32 v89, v46, v47\n"
    "v_cvt_pk_bf16_f32 v88, v44, v45\n"
    "v_cvt_pk_bf16_f32 v91, v62, v63\n"
    "s_bitcmp1_b32 s83, 1\n"
    "s_cbranch_scc1 .Lf3b\n"
    "s_mov_b32 s66, 1\n"
    "s_add_u32 s68, s68, 24576\n"
    "s_add_u32 s81, s81, 128\n"
    "s_add_u32 s80, s80, 256\n"
    "s_add_u32 s71, s71, 98304\n"
    "s_add_u32 s75, s71, s63\n"
    "s_add_u32 s76, s75, 6144\n"
    "s_add_u32 s77, s75, 24576\n"
    "s_add_u32 s78, s75, 30720\n"
    "s_branch .LhKP\n"
    "s_branch .Lf3e\n"
    ".Lf3b:\n"
    "s_add_u32 s75, s71, s63\n"
    "s_add_u32 s76, s75, 6144\n"
    "s_add_u32 s77, s75, 24576\n"
    "s_add_u32 s78, s75, 30720\n"
    "buffer_load_dwordx2 v[204:205], v242, s[28:31], s75 offen offset:0 nt\n"
    "buffer_load_dwordx2 v[206:207], v242, s[28:31], s75 offen offset:768 nt\n"
    "buffer_load_dwordx2 v[208:209], v242, s[28:31], s75 offen offset:3072 nt\n"
    "buffer_load_dwordx2 v[210:211], v242, s[28:31], s75 offen offset:3840 nt\n"
    "buffer_load_dwordx2 v[212:213], v242, s[28:31], s76 offen offset:0 nt\n"
    "buffer_load_dwordx2 v[214:215], v242, s[28:31], s76 offen offset:768 nt\n"
    "buffer_load_dwordx2 v[216:217], v242, s[28:31], s76 offen offset:3072 nt\n"
    "buffer_load_dwordx2 v[218:219], v242, s[28:31], s76 offen offset:3840 nt\n"
    "buffer_load_dwordx2 v[220:221], v242, s[28:31], s77 offen offset:0 nt\n"
    "buffer_load_dwordx2 v[222:223], v242, s[28:31], s77 offen offset:768 nt\n"
    "buffer_load_dwordx2 v[224:225], v242, s[28:31], s77 offen offset:3072 nt\n"
    "buffer_load_dwordx2 v[226:227], v242, s[28:31], s77 offen offset:3840 nt\n"
    "buffer_load_dwordx2 v[228:229], v242, s[28:31], s78 offen offset:0 nt\n"
    "buffer_load_dwordx2 v[230:231], v242, s[28:31], s78 offen offset:768 nt\n"
    "buffer_load_dwordx2 v[232:233], v242, s[28:31], s78 offen offset:3072 nt\n"
    "buffer_load_dwordx2 v[234:235], v242, s[28:31], s78 offen offset:3840 nt\n"
    "s_mov_b32 s66, 1\n"
    "s_add_u32 s68, s68, 24576\n"
    "s_add_u32 s81, s81, 128\n"
    "s_branch .LhKN\n"
    ".Lf3e:\n"
    "s_nop 4\n"
    "s_branch .Lf5e\n"
    ".Lf5b:\n"
    "s_lshl_b32 s73, s79, 8\n"
    "s_mul_i32 s74, s59, 6144\n"
    "s_add_u32 s73, s73, s74\n"
    "v_add_u32 v163, s73, v244\n"
    "s_lshr_b32 s73, s53, 2\n"
    "s_mul_i32 s73, s73, 12288\n"
    "s_and_b32 s74, s53, 3\n"
    "s_lshl_b32 s74, s74, 8\n"
    "s_add_u32 s73, s73, s74\n"
    "s_cmp_eq_u32 s59, 0\n"
    "s_cselect_b32 s73, 0x7f000000, s73\n"
    "s_add_u32 s60, s73, 24576\n"
    "s_add_u32 s61, s73, 28672\n"
    "s_add_u32 s62, s73, 32768\n"
    "v_mov_b32 v244, v243\n"
    "v_mov_b32 v243, 0\n"
    "buffer_load_dwordx4 v[40:43], v242, s[40:43], s75 offen offset:768 nt\n"
    "v_add_u32 v159, 0, v156\n"
    "v_mul_u32_u24 v159, 2731, v159\n"
    "v_lshrrev_b32 v159, 16, v159\n"
    "v_xor_b32 v159, v159, v156\n"
    "v_and_b32 v159, 7, v159\n"
    "v_and_b32 v88, 0x380, v1\n"
    "v_lshl_add_u32 v88, v159, 4, v88\n"
    "v_add_u32 v159, 64, v156\n"
    "v_mul_u32_u24 v159, 2731, v159\n"
    "v_lshrrev_b32 v159, 16, v159\n"
    "v_xor_b32 v159, v159, v156\n"
    "v_and_b32 v159, 7, v159\n"
    "v_and_b32 v89, 0x380, v1\n"
    "v_lshl_add_u32 v89, v159, 4, v89\n"
    "v_add_u32 v159, 128, v156\n"
    "v_mul_u32_u24 v159, 2731, v159\n"
    "v_lshrrev_b32 v159, 16, v159\n"
    "v_xor_b32 v159, v159, v156\n"
    "v_and_b32 v159, 7, v159\n"
    "v_and_b32 v90, 0x380, v1\n"
    "v_lshl_add_u32 v90, v159, 4, v90\n"
    "v_and_b32 v2, 7, v156\n"
    "v_lshrrev_b32 v144, 3, %3\n"
    "v_mul_u32_u24 v144, 768, v144\n"
    "v_lshl_add_u32 v144, v2, 3, v144\n"
    "buffer_load_dwordx4 v[44:47], v242, s[40:43], s75 offen offset:256 nt\n"
    "v_add_u32 v144, 131072, v144\n"
    "v_mad_u32_u24 v86, v2, 24, v144\n"
    "v_and_b32 v96, 15, v156\n"
    "v_lshrrev_b32 v159, 4, v156\n"
    "v_xor_b32 v159, v159, v2\n"
    "v_mul_u32_u24 v96, 384, v96\n"
    "v_lshl_add_u32 v96, v159, 4, v96\n"
    "s_add_u32 s73, s65, 49152\n"
    "v_add_u32 v96, s73, v96\n"
    "v_xor_b32 v97, 64, v96\n"
    "v_subrev_u32 v154, 24576, v96\n"
    "v_subrev_u32 v155, 24576, v97\n"
    "s_add_u32 s67, s65, 98304\n"
    "s_add_u32 s68, s65, 24576\n"
    "s_add_u32 s69, s65, 73728\n"
    "s_mov_b32 s81, 256\n"
    "s_mul_i32 s74, s79, 24576\n"
    "s_add_u32 s70, s74, s65\n"
    "s_add_u32 s70, s70, 73728\n"
    "s_sub_u32 s64, s65, s74\n"
    "s_add_u32 s64, s64, 98304\n"
    "v_and_b32 v249, 0x310, v1\n"
    "v_and_b32 v159, 12, v156\n"
    "v_lshl_add_u32 v249, v159, 3, v249\n"
    "v_and_b32 v159, 2, v156\n"
    "buffer_load_dwordx4 v[48:51], v242, s[40:43], s75 offen offset:1024 nt\n"
    "v_lshl_add_u32 v249, v159, 6, v249\n"
    "s_add_u32 s73, s74, 73728\n"
    "v_add_u32 v248, s73, v249\n"
    "s_sub_u32 s73, 98304, s74\n"
    "v_add_u32 v249, s73, v249\n"
    "s_or_b32 s73, s53, s84\n"
    "s_cmp_eq_u32 s73, 0\n"
    "s_cselect_b32 s82, 0, 0x7fff0000\n"
    "v_and_b32 v252, 12, v156\n"
    "v_bfe_u32 v159, v156, 1, 1\n"
    "v_or_b32 v252, v252, v159\n"
    "v_and_b32 v159, 1, v156\n"
    "v_lshl_or_b32 v252, v159, 1, v252\n"
    "v_lshlrev_b32 v252, 6, v252\n"
    "v_lshlrev_b32 v159, 2, v156\n"
    "v_and_b32 v159, 32, v159\n"
    "v_xor_b32 v159, v159, v156\n"
    "v_and_or_b32 v252, v159, 48, v252\n"
    "v_mov_b32 v2, v252\n"
    "v_add_u32 v3, 24576, v252\n"
    "v_lshrrev_b32 v91, 3, v156\n"
    "v_lshl_add_u32 v87, v91, 6, 64\n"
    "v_add_u32 v87, 64, v87\n"
    "v_and_b32 v241, 1, v156\n"
    "v_lshlrev_b32 v241, 3, v241\n"
    "buffer_load_dwordx4 v[52:55], v242, s[40:43], s75 offen offset:512 nt\n"
    "v_lshl_add_u32 v241, v91, 4, v241\n"
    "v_add_u32 v241, 1152, v241\n"
    "v_lshlrev_b32 v91, 24, v91\n"
    "v_lshl_add_u32 v91, v156, 3, v91\n"
    "s_lshl_b32 s73, s53, 4\n"
    "s_lshl_b32 s74, s59, 3\n"
    "s_add_u32 s73, s73, s74\n"
    "s_add_u32 s73, s73, 1152\n"
    "v_mov_b32 v240, 0x7fff0000\n"
    "v_writelane_b32 v240, s73, 0\n"
    "v_and_b32 v159, 48, v156\n"
    "s_mul_i32 s73, s79, 768\n"
    "s_sub_u32 s73, 131072, s73\n"
    "v_lshlrev_b32 v250, 1, v159\n"
    "v_add_u32 v250, s73, v250\n"
    "v_and_b32 v251, 3, v156\n"
    "v_lshl_add_u32 v251, v251, 7, v159\n"
    "s_add_u32 s73, s73, 256\n"
    "v_add_u32 v251, s73, v251\n"
    "v_and_b32 v159, 64, v1\n"
    "v_add_u32 v251, v159, v251\n"
    "v_bfe_i32 v159, v156, 2, 1\n"
    "v_and_b32 v159, 0xaaaa, v159\n"
    "v_or_b32 v253, 0x4444, v159\n"
    "s_mov_b32 s58, 0\n"
    "s_mov_b32 s59, 8\n"
    "s_waitcnt vmcnt(14)\n"
    "v_pk_min_i16 v4, v4, v10\n"
    "v_pk_min_i16 v5, v5, v11\n"
    "v_pk_min_i16 v6, v6, v12\n"
    "v_pk_min_i16 v7, v7, v13\n"
    "v_pk_min_i16 v8, v8, v14\n"
    "v_pk_min_i16 v9, v9, v15\n"
    "v_pk_min_i16 v16, v16, v18\n"
    "v_pk_min_i16 v17, v17, v19\n"
    "v_pk_min_i16 v4, v4, v7\n"
    "v_pk_min_i16 v5, v5, v8\n"
    "v_pk_min_i16 v6, v6, v9\n"
    "v_pk_min_i16 v16, v16, v17\n"
    "v_pk_min_i16 v4, v4, v5\n"
    "v_lshlrev_b32 v17, 16, v16\n"
    "v_pk_min_i16 v4, v4, v6\n"
    "v_min_i32 v16, v16, v17\n"
    "v_lshlrev_b32 v5, 16, v4\n"
    "v_mov_b32 v7, 155920\n"
    "v_min_i32 v4, v4, v5\n"
    "v_lshl_add_u32 v6, s84, 2, v7\n"
    "v_min_i32_dpp v16, v16, v16 row_shr:1\n"
    "v_min_i32_dpp v4, v4, v4 row_shr:1\n"
    "s_nop 0\n"
    "v_min_i32_dpp v16, v16, v16 row_shr:2\n"
    "v_min_i32_dpp v4, v4, v4 row_shr:2\n"
    "s_nop 0\n"
    "v_min_i32_dpp v16, v16, v16 row_shr:4\n"
    "v_min_i32_dpp v4, v4, v4 row_shr:4\n"
    "s_nop 0\n"
    "v_min_i32_dpp v16, v16, v16 row_shr:8\n"
    "v_min_i32_dpp v4, v4, v4 row_shr:8\n"
    "s_nop 0\n"
    "v_min_i32_dpp v16, v16, v16 row_bcast:15 row_mask:0xa\n"
    "v_min_i32_dpp v4, v4, v4 row_bcast:15 row_mask:0xa\n"
    "s_nop 0\n"
    "v_min_i32_dpp v16, v16, v16 row_bcast:31 row_mask:0xc\n"
    "v_min_i32_dpp v4, v4, v4 row_bcast:31 row_mask:0xc\n"
    "v_readlane_b32 s35, v16, 63\n"
    "v_readlane_b32 s73, v4, 63\n"
    "s_max_i32 s35, s35, 0\n"
    "s_and_b32 s35, s35, 0xffff0000\n"
    "v_mov_b32 v4, s73\n"
    "ds_write_b32 v6, v4\n"
    "v_mov_b32 v14, -32\n"
    "v_mov_b32 v12, -126\n"
    "v_mov_b32 v13, 0x3dd53b95\n"
    "v_mov_b32 v98, 0\n"
    "s_waitcnt lgkmcnt(0)\n"
    "s_barrier\n"
    "ds_read_b128 v[8:11], v7 offset:0\n"
    "buffer_load_dwordx4 v[56:59], v242, s[40:43], s75 offen offset:1280 nt\n"
    "buffer_load_dwordx4 v[136:139], v130, s[92:95], 0 offen offset:0\n"
    "buffer_load_dwordx4 v[140:143], v130, s[96:99], 0 offen offset:0\n"
    "s_nop 0\n"
    "s_nop 0\n"
    "s_nop 0\n"
    "s_nop 0\n"
    "s_nop 0\n"
    "s_nop 0\n"
    "s_nop 0\n"
    "s_nop 0\n"
    "s_nop 0\n"
    "s_nop 0\n"
    "s_nop 0\n"
    "s_nop 0\n"
    "s_nop 0\n"
    "s_nop 0\n"
    "s_nop 0\n"
    "s_nop 0\n"
    "s_nop 0\n"
    "s_nop 0\n"
    "s_nop 0\n"
    "s_nop 0\n"
    "s_nop 0\n"
    "s_nop 0\n"
    "s_nop 0\n"
    "s_nop 0\n"
    "s_nop 0\n"
    "s_nop 0\n"
    "s_nop 0\n"
    "s_nop 0\n"
    "s_nop 0\n"
    "s_nop 0\n"
    "s_nop 0\n"
    "s_nop 0\n"
    "s_nop 0\n"
    "s_nop 0\n"
    "s_nop 0\n"
    "s_nop 0\n"
    "s_nop 0\n"
    "s_nop 0\n"
    "s_nop 0\n"
    "s_nop 0\n"
    "s_nop 0\n"
    "s_nop 0\n"
    "s_nop 0\n"
    "s_nop 0\n"
    "s_nop 0\n"
    "s_nop 0\n"
    "s_nop 0\n"
    "s_nop 0\n"
    "s_nop 0\n"
    "s_nop 0\n"
    "s_nop 0\n"
    "s_nop 0\n"
    "s_nop 0\n"
    "s_nop 0\n"
    "s_nop 0\n"
    "s_nop 0\n"
    "s_nop 0\n"
    "s_nop 0\n"
    "s_nop 0\n"
    "s_nop 0\n"
    "s_waitcnt lgkmcnt(0)\n"
    "v_min3_i32 v4, v8, v9, v10\n"
    "v_min_i32 v4, v4, v11\n"
    "v_max_i32 v4, 0, v4\n"
    "v_and_b32 v4, 0xffff0000, v4\n"
    "v_mul_f32 v4, s35, v4\n"
    "v_mul_f32 v4, 0x3ed53b95, v4\n"
    "v_lshrrev_b32 v4, 23, v4\n"
    "v_sub_u32 v4, 2, v4\n"
    "v_med3_i32 v4, v4, v14, v12\n"
    "v_ldexp_f32 v13, v13, v4\n"
    "v_readfirstlane_b32 s89, v4\n"
    "v_readfirstlane_b32 s88, v13\n"
    "s_mul_i32 s89, s89, 0xff7fff80\n"
    "s_mov_b32 s90, 149\n"
    "s_waitcnt vmcnt(11)\n"
    "s_add_u32 s54, s71, s60\n"
    "s_add_u32 s55, s71, s61\n"
    "s_add_u32 s56, s71, s62\n"
    "v_cvt_pk_u16_u32 v204, v204, v208\n"
    "v_cvt_pk_u16_u32 v205, v205, v209\n"
    "v_cvt_pk_u16_u32 v206, v206, v210\n"
    "v_cvt_pk_u16_u32 v207, v207, v211\n"
    "v_or3_b32 v243, v204, v205, v243\n"
    "v_or3_b32 v243, v206, v207, v243\n"
    "buffer_store_dword v204, v244, s[16:19], s54 offen offset:0 sc1\n"
    "buffer_store_dword v205, v244, s[16:19], s54 offen offset:1024 sc1\n"
    "buffer_load_dwordx4 v[4:7], v162, s[28:31], s57 offen offset:0\n"
    "buffer_load_dwordx4 v[8:11], v162, s[28:31], s57 offen offset:3072\n"
    "buffer_store_dword v206, v244, s[16:19], s54 offen offset:2048 sc1\n"
    "buffer_store_dword v207, v244, s[16:19], s54 offen offset:3072 sc1\n"
    "v_cvt_pk_u16_u32 v212, v212, v216\n"
    "v_cvt_pk_u16_u32 v213, v213, v217\n"
    "v_cvt_pk_u16_u32 v214, v214, v218\n"
    "v_cvt_pk_u16_u32 v215, v215, v219\n"
    "v_or3_b32 v243, v212, v213, v243\n"
    "v_or3_b32 v243, v214, v215, v243\n"
    "buffer_store_dword v212, v244, s[16:19], s55 offen offset:0 sc1\n"
    "buffer_store_dword v213, v244, s[16:19], s55 offen offset:1024 sc1\n"
    "buffer_store_dword v214, v244, s[16:19], s55 offen offset:2048 sc1\n"
    "buffer_load_dwordx4 v[12:15], v162, s[28:31], s57 offen offset:128\n"
    "buffer_store_dword v215, v244, s[16:19], s55 offen offset:3072 sc1\n"
    "v_cvt_pk_u16_u32 v220, v220, v224\n"
    "v_cvt_pk_u16_u32 v221, v221, v225\n"
    "v_cvt_pk_u16_u32 v222, v222, v226\n"
    "v_cvt_pk_u16_u32 v223, v223, v227\n"
    "v_or3_b32 v243, v220, v221, v243\n"
    "v_or3_b32 v243, v222, v223, v243\n"
    "buffer_store_dword v220, v244, s[16:19], s56 offen offset:0 sc1\n"
    "buffer_store_dword v221, v244, s[16:19], s56 offen offset:1024 sc1\n"
    "buffer_store_dword v222, v244, s[16:19], s56 offen offset:2048 sc1\n"
    "buffer_store_dword v223, v244, s[16:19], s56 offen offset:3072 sc1\n"
    "s_add_u32 s71, s71, 98304\n"
    "s_add_u32 m0, s65, 24576\n"
    "s_add_u32 s72, s85, 0\n"
    "buffer_load_dwordx4 v88, s[20:23], s72 offen offset:0 nt lds\n"
    "buffer_load_dwordx4 v[16:19], v162, s[28:31], s57 offen offset:3200\n"
    "buffer_load_dwordx4 v[20:23], v162, s[28:31], s57 offen offset:256\n"
    "s_waitcnt vmcnt(25)\n"
    "v_add_co_u32 v238, vcc, 0x7f4a7c15, v238\n"
    "v_mov_b32 v159, 0x9e3779b9\n"
    "s_nop 0\n"
    "v_addc_co_u32 v239, vcc, v159, v239, vcc\n"
    "v_mov_b32 v246, 1\n"
    "s_waitcnt vmcnt(26)\n"
    "v_lshlrev_b32 v180, 16, v124\n"
    "v_and_b32 v181, 0xffff0000, v124\n"
    "v_lshlrev_b32 v182, 16, v125\n"
    "v_and_b32 v183, 0xffff0000, v125\n"
    "v_lshlrev_b32 v184, 16, v126\n"
    "v_and_b32 v185, 0xffff0000, v126\n"
    "v_lshlrev_b32 v186, 16, v127\n"
    "v_and_b32 v187, 0xffff0000, v127\n"
    "v_mul_f32 v180, s88, v180\n"
    "v_mul_f32 v181, s88, v181\n"
    "v_mul_f32 v182, s88, v182\n"
    "v_mul_f32 v183, s88, v183\n"
    "v_fmaak_f32 v184, v184, v180, 0x80730000\n"
    "v_fmaak_f32 v185, v185, v181, 0x80730000\n"
    "v_fmaak_f32 v186, v186, v182, 0x80730000\n"
    "v_fmaak_f32 v187, v187, v183, 0x80730000\n"
    "s_add_u32 m0, s65, 25600\n"
    "s_add_u32 s72, s85, 1024\n"
    "buffer_load_dwordx4 v89, s[20:23], s72 offen offset:0 nt lds\n"
    "buffer_load_dwordx4 v[24:27], v162, s[28:31], s57 offen offset:3328\n"
    "v_ldexp_f32 v180, v180, s90\n"
    "v_ldexp_f32 v181, v181, s90\n"
    "v_ldexp_f32 v182, v182, s90\n"
    "v_ldexp_f32 v183, v183, s90\n"
    "v_fma_f32 v180, v180, 0, v180\n"
    "v_fma_f32 v181, v181, 0, v181\n"
    "v_fma_f32 v182, v182, 0, v182\n"
    "v_fma_f32 v183, v183, 0, v183\n"
    "ds_write_b128 v131, v[180:183] offset:0\n"
    "ds_write_b128 v131, v[184:187] offset:1024\n"
    "ds_read_b128 v[180:183], v132 offset:0\n"
    "ds_read_b128 v[184:187], v132 offset:1024\n"
    "ds_read_b128 v[188:191], v132 offset:128\n"
    "ds_read_b128 v[192:195], v132 offset:1152\n"
    "ds_read_b128 v[196:199], v132 offset:256\n"
    "ds_read_b128 v[200:203], v132 offset:1280\n"
    "s_nop 0\n"
    "s_nop 0\n"
    "s_nop 0\n"
    "s_nop 0\n"
    "s_nop 0\n"
    "s_nop 0\n"
    "s_nop 0\n"
    "s_add_u32 m0, s65, 26624\n"
    "s_add_u32 s72, s85, 2048\n"
    "buffer_load_dwordx4 v90, s[20:23], s72 offen offset:0 nt lds\n"
    "buffer_load_dwordx4 v[100:103], v162, s[28:31], s66 offen offset:0\n"
    "buffer_load_dwordx4 v[104:107], v162, s[28:31], s66 offen offset:3072\n"
    "s_nop 0\n"
    "s_nop 0\n"
    "s_nop 0\n"
    "s_nop 0\n"
    "s_nop 0\n"
    "s_nop 0\n"
    "s_nop 0\n"
    "s_nop 0\n"
    "s_nop 0\n"
    "s_nop 0\n"
    "s_nop 0\n"
    "s_nop 0\n"
    "s_nop 0\n"
    "s_nop 0\n"
    "s_nop 0\n"
    "s_nop 0\n"
    "s_nop 0\n"
    "s_nop 0\n"
    "s_nop 0\n"
    "s_nop 0\n"
    "s_nop 0\n"
    "s_nop 0\n"
    "s_add_u32 m0, s65, 27648\n"
    "s_add_u32 s72, s85, 3072\n"
    "buffer_load_dwordx4 v88, s[20:23], s72 offen offset:0 nt lds\n"
    "buffer_load_dwordx4 v[108:111], v162, s[28:31], s66 offen offset:128\n"
    "s_nop 0\n"
    "s_nop 0\n"
    "s_nop 0\n"
    "s_nop 0\n"
    "s_nop 0\n"
    "s_nop 0\n"
    "s_nop 0\n"
    "s_nop 0\n"
    "s_nop 0\n"
    "s_nop 0\n"
    "s_nop 0\n"
    "s_nop 0\n"
    "s_nop 0\n"
    "s_nop 0\n"
    "s_nop 0\n"
    "s_nop 0\n"
    "s_nop 0\n"
    "s_nop 0\n"
    "s_nop 0\n"
    "s_nop 0\n"
    "s_nop 0\n"
    "s_nop 0\n"
    "s_nop 0\n"
    "s_waitcnt lgkmcnt(0)\n"
    "s_waitcnt vmcnt(10)\n"
    "ds_add_rtn_u32 v245, v247, v246\n"
    "s_lshl_b32 s73, s53, 6\n"
    "s_add_u32 s73, s73, 128\n"
    "s_waitcnt lgkmcnt(0)\n"
    "v_readfirstlane_b32 s74, v245\n"
    "s_nop 0\n"
    "s_cmp_eq_u32 s74, 3\n"
    "s_cselect_b32 s73, s73, 0x7fff0000\n"
    "buffer_store_dwordx2 v[238:239], v91, s[36:39], s73 offen offset:0 sc1\n"
    "s_waitcnt vmcnt(28)\n"
    "s_add_u32 m0, s65, 28672\n"
    "s_add_u32 s72, s85, 4096\n"
    "buffer_load_dwordx4 v89, s[20:23], s72 offen offset:0 nt lds\n"
    "buffer_load_dwordx4 v[112:115], v162, s[28:31], s66 offen offset:3200\n"
    "buffer_load_dwordx4 v[116:119], v162, s[28:31], s66 offen offset:256\n"
    "s_add_u32 s54, s71, s60\n"
    "s_add_u32 s55, s71, s61\n"
    "s_add_u32 s56, s71, s62\n"
    "v_cvt_pk_u16_u32 v36, v36, v40\n"
    "v_cvt_pk_u16_u32 v37, v37, v41\n"
    "v_cvt_pk_u16_u32 v38, v38, v42\n"
    "v_cvt_pk_u16_u32 v39, v39, v43\n"
    "v_or3_b32 v243, v36, v37, v243\n"
    "v_or3_b32 v243, v38, v39, v243\n"
    "buffer_store_dword v36, v244, s[16:19], s54 offen offset:0 sc1\n"
    "buffer_store_dword v37, v244, s[16:19], s54 offen offset:1024 sc1\n"
    "buffer_store_dword v38, v244, s[16:19], s54 offen offset:2048 sc1\n"
    "buffer_store_dword v39, v244, s[16:19], s54 offen offset:3072 sc1\n"
    "v_cvt_pk_u16_u32 v44, v44, v48\n"
    "v_cvt_pk_u16_u32 v45, v45, v49\n"
    "v_cvt_pk_u16_u32 v46, v46, v50\n"
    "s_add_u32 m0, s65, 29696\n"
    "s_add_u32 s72, s85, 5120\n"
    "buffer_load_dwordx4 v90, s[20:23], s72 offen offset:0 nt lds\n"
    "buffer_load_dwordx4 v[120:123], v162, s[28:31], s66 offen offset:3328\n"
    "v_cvt_pk_u16_u32 v47, v47, v51\n"
    "v_or3_b32 v243, v44, v45, v243\n"
    "v_or3_b32 v243, v46, v47, v243\n"
    "buffer_store_dword v44, v244, s[16:19], s55 offen offset:0 sc1\n"
    "buffer_store_dword v45, v244, s[16:19], s55 offen offset:1024 sc1\n"
    "buffer_store_dword v46, v244, s[16:19], s55 offen offset:2048 sc1\n"
    "buffer_store_dword v47, v244, s[16:19], s55 offen offset:3072 sc1\n"
    "v_cvt_pk_u16_u32 v52, v52, v56\n"
    "v_cvt_pk_u16_u32 v53, v53, v57\n"
    "v_cvt_pk_u16_u32 v54, v54, v58\n"
    "v_cvt_pk_u16_u32 v55, v55, v59\n"
    "v_or3_b32 v243, v52, v53, v243\n"
    "v_or3_b32 v243, v54, v55, v243\n"
    "buffer_store_dword v52, v244, s[16:19], s56 offen offset:0 sc1\n"
    "buffer_store_dword v53, v244, s[16:19], s56 offen offset:1024 sc1\n"
    "buffer_store_dword v54, v244, s[16:19], s56 offen offset:2048 sc1\n"
    "buffer_store_dword v55, v244, s[16:19], s56 offen offset:3072 sc1\n"
    "s_add_u32 m0, s65, 73728\n"
    "s_add_u32 s72, s85, 6144\n"
    "buffer_load_dwordx4 v88, s[20:23], s72 offen offset:0 nt lds\n"
    "s_waitcnt vmcnt(44)\n"
    "v_lshlrev_b32 v204, 16, v136\n"
    "v_and_b32 v205, 0xffff0000, v136\n"
    "v_lshlrev_b32 v206, 16, v137\n"
    "v_and_b32 v207, 0xffff0000, v137\n"
    "v_lshlrev_b32 v208, 16, v138\n"
    "v_and_b32 v209, 0xffff0000, v138\n"
    "v_lshlrev_b32 v210, 16, v139\n"
    "v_and_b32 v211, 0xffff0000, v139\n"
    "v_log_f32 v68, v204\n"
    "v_log_f32 v69, v205\n"
    "v_log_f32 v70, v206\n"
    "v_log_f32 v71, v207\n"
    "v_log_f32 v72, v208\n"
    "v_log_f32 v73, v209\n"
    "v_log_f32 v74, v210\n"
    "v_log_f32 v75, v211\n"
    "v_rcp_f32 v204, v204\n"
    "v_rcp_f32 v205, v205\n"
    "v_rcp_f32 v206, v206\n"
    "s_add_u32 m0, s65, 74752\n"
    "s_add_u32 s72, s85, 7168\n"
    "buffer_load_dwordx4 v89, s[20:23], s72 offen offset:0 nt lds\n"
    "v_rcp_f32 v207, v207\n"
    "v_rcp_f32 v208, v208\n"
    "v_rcp_f32 v209, v209\n"
    "v_rcp_f32 v210, v210\n"
    "v_rcp_f32 v211, v211\n"
    "s_mov_b32 s73, 0x41a00000\n"
    "v_mov_b32 v228, 0x7fc00000\n"
    "v_cmp_le_f32 vcc, |v68|, s73\n"
    "s_nop 1\n"
    "v_cndmask_b32 v68, v228, v68, vcc\n"
    "v_cmp_le_f32 vcc, |v69|, s73\n"
    "s_nop 1\n"
    "v_cndmask_b32 v69, v228, v69, vcc\n"
    "v_cmp_le_f32 vcc, |v70|, s73\n"
    "s_nop 1\n"
    "v_cndmask_b32 v70, v228, v70, vcc\n"
    "v_cmp_le_f32 vcc, |v71|, s73\n"
    "s_nop 1\n"
    "v_cndmask_b32 v71, v228, v71, vcc\n"
    "v_cmp_le_f32 vcc, |v72|, s73\n"
    "s_nop 1\n"
    "v_cndmask_b32 v72, v228, v72, vcc\n"
    "v_cmp_le_f32 vcc, |v73|, s73\n"
    "s_nop 1\n"
    "v_cndmask_b32 v73, v228, v73, vcc\n"
    "v_cmp_le_f32 vcc, |v74|, s73\n"
    "s_nop 1\n"
    "v_cndmask_b32 v74, v228, v74, vcc\n"
    "s_add_u32 m0, s65, 75776\n"
    "s_add_u32 s72, s85, 8192\n"
    "buffer_load_dwordx4 v90, s[20:23], s72 offen offset:0 nt lds\n"
    "v_cmp_le_f32 vcc, |v75|, s73\n"
    "s_nop 1\n"
    "v_cndmask_b32 v75, v228, v75, vcc\n"
    "v_cvt_pk_bf16_f32 v220, v204, v205\n"
    "v_cvt_pk_bf16_f32 v221, v206, v207\n"
    "v_cvt_pk_bf16_f32 v222, v208, v209\n"
    "v_cvt_pk_bf16_f32 v223, v210, v211\n"
    "v_lshlrev_b32 v212, 16, v220\n"
    "v_and_b32 v213, 0xffff0000, v220\n"
    "v_lshlrev_b32 v214, 16, v221\n"
    "v_and_b32 v215, 0xffff0000, v221\n"
    "v_lshlrev_b32 v216, 16, v222\n"
    "v_and_b32 v217, 0xffff0000, v222\n"
    "v_lshlrev_b32 v218, 16, v223\n"
    "v_and_b32 v219, 0xffff0000, v223\n"
    "v_sub_f32 v204, v204, v212\n"
    "v_sub_f32 v205, v205, v213\n"
    "v_sub_f32 v206, v206, v214\n"
    "v_sub_f32 v207, v207, v215\n"
    "v_sub_f32 v208, v208, v216\n"
    "v_sub_f32 v209, v209, v217\n"
    "s_add_u32 m0, s65, 76800\n"
    "s_add_u32 s72, s85, 9216\n"
    "buffer_load_dwordx4 v88, s[20:23], s72 offen offset:0 nt lds\n"
    "v_sub_f32 v210, v210, v218\n"
    "v_sub_f32 v211, v211, v219\n"
    "v_cvt_pk_bf16_f32 v224, v204, v205\n"
    "v_cvt_pk_bf16_f32 v225, v206, v207\n"
    "v_cvt_pk_bf16_f32 v226, v208, v209\n"
    "v_cvt_pk_bf16_f32 v227, v210, v211\n"
    "v_mov_b32 v204, 0\n"
    "v_mov_b32 v205, 0\n"
    "v_mov_b32 v206, 0\n"
    "v_mov_b32 v207, 0\n"
    "v_swap_b32 v221, v222\n"
    "v_swap_b32 v225, v226\n"
    "v_swap_b32 v141, v142\n"
    "ds_write2_b64 v144, v[220:221], v[222:223] offset0:32 offset1:40\n"
    "ds_write2_b64 v144, v[224:225], v[226:227] offset0:48 offset1:56\n"
    "ds_write2_b64 v144, v[140:141], v[142:143] offset0:64 offset1:72\n"
    "ds_write2_b64 v144, v[204:205], v[206:207] offset0:80 offset1:88\n"
    "s_waitcnt vmcnt(27)\n"
    "v_fma_f32 v4, v4, v180, -v184\n"
    "v_fma_f32 v5, v5, v181, -v185\n"
    "v_fma_f32 v6, v6, v182, -v186\n"
    "s_add_u32 m0, s65, 77824\n"
    "s_add_u32 s72, s85, 10240\n"
    "buffer_load_dwordx4 v89, s[20:23], s72 offen offset:0 nt lds\n"
    "v_fma_f32 v7, v7, v183, -v187\n"
    "v_fma_f32 v8, v8, v180, -v184\n"
    "v_fma_f32 v9, v9, v181, -v185\n"
    "v_fma_f32 v10, v10, v182, -v186\n"
    "v_fma_f32 v11, v11, v183, -v187\n"
    "v_cvt_pk_bf16_f32 v4, v4, v5\n"
    "v_cvt_pk_bf16_f32 v5, v6, v7\n"
    "v_cvt_pk_bf16_f32 v6, v8, v9\n"
    "v_cvt_pk_bf16_f32 v7, v10, v11\n"
    "ds_write_b64 v163, v[4:5] offset:0\n"
    "ds_write_b64 v163, v[6:7] offset:1024\n"
    "v_fma_f32 v12, v12, v188, -v192\n"
    "v_fma_f32 v13, v13, v189, -v193\n"
    "v_fma_f32 v14, v14, v190, -v194\n"
    "v_fma_f32 v15, v15, v191, -v195\n"
    "v_fma_f32 v16, v16, v188, -v192\n"
    "v_fma_f32 v17, v17, v189, -v193\n"
    "v_fma_f32 v18, v18, v190, -v194\n"
    "v_fma_f32 v19, v19, v191, -v195\n"
    "v_cvt_pk_bf16_f32 v12, v12, v13\n"
    "s_add_u32 m0, s65, 78848\n"
    "s_add_u32 s72, s85, 11264\n"
    "buffer_load_dwordx4 v90, s[20:23], s72 offen offset:0 nt lds\n"
    "v_cvt_pk_bf16_f32 v13, v14, v15\n"
    "v_cvt_pk_bf16_f32 v14, v16, v17\n"
    "v_cvt_pk_bf16_f32 v15, v18, v19\n"
    "ds_write_b64 v163, v[12:13] offset:2048\n"
    "ds_write_b64 v163, v[14:15] offset:3072\n"
    "v_fma_f32 v20, v20, v196, -v200\n"
    "v_fma_f32 v21, v21, v197, -v201\n"
    "v_fma_f32 v22, v22, v198, -v202\n"
    "v_fma_f32 v23, v23, v199, -v203\n"
    "v_fma_f32 v24, v24, v196, -v200\n"
    "v_fma_f32 v25, v25, v197, -v201\n"
    "v_fma_f32 v26, v26, v198, -v202\n"
    "v_fma_f32 v27, v27, v199, -v203\n"
    "v_cvt_pk_bf16_f32 v20, v20, v21\n"
    "v_cvt_pk_bf16_f32 v21, v22, v23\n"
    "v_cvt_pk_bf16_f32 v22, v24, v25\n"
    "v_cvt_pk_bf16_f32 v23, v26, v27\n"
    "ds_write_b64 v163, v[20:21] offset:4096\n"
    "ds_write_b64 v163, v[22:23] offset:5120\n"
    "s_waitcnt vmcnt(14)\n"
    "v_fma_f32 v100, v100, v180, -v184\n"
    "s_add_u32 m0, s65, 49152\n"
    "s_add_u32 s72, s85, 12288\n"
    "buffer_load_dwordx4 v88, s[20:23], s72 offen offset:0 nt lds\n"
    "v_fma_f32 v101, v101, v181, -v185\n"
    "v_fma_f32 v102, v102, v182, -v186\n"
    "v_fma_f32 v103, v103, v183, -v187\n"
    "v_fma_f32 v104, v104, v180, -v184\n"
    "v_fma_f32 v105, v105, v181, -v185\n"
    "v_fma_f32 v106, v106, v182, -v186\n"
    "v_fma_f32 v107, v107, v183, -v187\n"
    "v_cvt_pk_bf16_f32 v100, v100, v101\n"
    "v_cvt_pk_bf16_f32 v101, v102, v103\n"
    "v_cvt_pk_bf16_f32 v102, v104, v105\n"
    "v_cvt_pk_bf16_f32 v103, v106, v107\n"
    "ds_write_b64 v163, v[100:101] offset:12288\n"
    "ds_write_b64 v163, v[102:103] offset:13312\n"
    "v_fma_f32 v108, v108, v188, -v192\n"
    "v_fma_f32 v109, v109, v189, -v193\n"
    "v_fma_f32 v110, v110, v190, -v194\n"
    "v_fma_f32 v111, v111, v191, -v195\n"
    "v_fma_f32 v112, v112, v188, -v192\n"
    "v_fma_f32 v113, v113, v189, -v193\n"
    "v_fma_f32 v114, v114, v190, -v194\n"
    "s_add_u32 m0, s65, 50176\n"
    "s_add_u32 s72, s85, 13312\n"
    "buffer_load_dwordx4 v89, s[20:23], s72 offen offset:0 nt lds\n"
    "v_fma_f32 v115, v115, v191, -v195\n"
    "v_cvt_pk_bf16_f32 v108, v108, v109\n"
    "v_cvt_pk_bf16_f32 v109, v110, v111\n"
    "v_cvt_pk_bf16_f32 v110, v112, v113\n"
    "v_cvt_pk_bf16_f32 v111, v114, v115\n"
    "ds_write_b64 v163, v[108:109] offset:14336\n"
    "ds_write_b64 v163, v[110:111] offset:15360\n"
    "v_fma_f32 v116, v116, v196, -v200\n"
    "v_fma_f32 v117, v117, v197, -v201\n"
    "v_fma_f32 v118, v118, v198, -v202\n"
    "v_fma_f32 v119, v119, v199, -v203\n"
    "v_fma_f32 v120, v120, v196, -v200\n"
    "v_fma_f32 v121, v121, v197, -v201\n"
    "v_fma_f32 v122, v122, v198, -v202\n"
    "v_fma_f32 v123, v123, v199, -v203\n"
    "v_cvt_pk_bf16_f32 v116, v116, v117\n"
    "v_cvt_pk_bf16_f32 v117, v118, v119\n"
    "v_cvt_pk_bf16_f32 v118, v120, v121\n"
    "v_cvt_pk_bf16_f32 v119, v122, v123\n"
    "ds_write_b64 v163, v[116:117] offset:16384\n"
    "ds_write_b64 v163, v[118:119] offset:17408\n"
    "s_waitcnt vmcnt(17)\n"
    "ds_read_b128 v[36:39], v154 offset:0\n"
    "ds_read_b128 v[40:43], v155 offset:0\n"
    "ds_read_b128 v[44:47], v154 offset:128\n"
    "ds_read_b128 v[48:51], v155 offset:128\n"
    "ds_read_b128 v[52:55], v154 offset:256\n"
    "ds_read_b128 v[56:59], v155 offset:256\n"
    "s_mov_b64 exec, 0\n"
    "s_bitcmp1_b32 s83, 1\n"
    "s_cbranch_scc1 .LwV\n"
    "s_cmp_eq_u32 0, 0\n"
    "s_branch .LwK\n"
    ".Lw2:\n"
    "s_bitcmp1_b32 s83, 1\n"
    "s_cbranch_scc1 .Lwp1\n"
    "s_branch .Lwp0\n"
    ".Lw3:\n"
    "s_mov_b64 exec, -1\n"
    "s_waitcnt lgkmcnt(0)\n"
    "s_barrier\n"
    "ds_read_b128 a[112:115], v252 offset:0\n"
    "ds_read_b128 a[116:119], v252 offset:2048\n"
    "ds_read_b128 a[120:123], v252 offset:4096\n"
    "ds_read_b128 a[124:127], v252 offset:6144\n"
    "ds_read_b128 a[128:131], v252 offset:8192\n"
    "ds_read_b128 a[132:135], v252 offset:10240\n"
    "ds_read_b128 a[136:139], v252 offset:1024\n"
    "ds_read_b128 a[140:143], v252 offset:3072\n"
    "ds_read_b128 a[144:147], v252 offset:5120\n"
    "ds_read_b128 a[148:151], v252 offset:7168\n"
    "ds_read_b128 a[152:155], v252 offset:9216\n"
    "ds_read_b128 a[156:159], v252 offset:11264\n"
    "ds_read_b128 a[0:3], v252 offset:12288\n"
    "ds_read_b128 a[4:7], v252 offset:14336\n"
    "ds_read_b128 a[8:11], v252 offset:16384\n"
    "ds_read_b128 a[12:15], v252 offset:18432\n"
    "ds_read_b128 a[16:19], v252 offset:20480\n"
    "ds_read_b128 a[20:23], v252 offset:22528\n"
    "ds_read_b128 a[24:27], v252 offset:13312\n"
    "ds_read_b128 a[28:31], v252 offset:15360\n"
    "ds_read_b128 a[32:35], v252 offset:17408\n"
    "ds_read_b128 a[36:39], v252 offset:19456\n"
    "ds_read_b128 a[40:43], v252 offset:21504\n"
    "ds_read_b128 a[44:47], v252 offset:23552\n"
    "s_add_u32 m0, s65, 51200\n"
    "s_add_u32 s72, s85, 14336\n"
    "buffer_load_dwordx4 v90, s[20:23], s72 offen offset:0 nt lds\n"
    "s_add_u32 m0, s65, 52224\n"
    "s_add_u32 s72, s85, 15360\n"
    "buffer_load_dwordx4 v88, s[20:23], s72 offen offset:0 nt lds\n"
    "v_pk_maximum3_f16 v98, v36, v37, v98\n"
    "v_pk_maximum3_f16 v98, v38, v39, v98\n"
    "v_pk_add_u16 v36, s89, v36 clamp\n"
    "v_pk_add_u16 v37, s89, v37 clamp\n"
    "v_pk_add_u16 v38, s89, v38 clamp\n"
    "v_pk_add_u16 v39, s89, v39 clamp\n"
    "v_pk_maximum3_f16 v98, v40, v41, v98\n"
    "v_pk_maximum3_f16 v98, v42, v43, v98\n"
    "v_pk_add_u16 v40, s89, v40 clamp\n"
    "v_pk_add_u16 v41, s89, v41 clamp\n"
    "v_pk_add_u16 v42, s89, v42 clamp\n"
    "v_pk_add_u16 v43, s89, v43 clamp\n"
    "v_pk_maximum3_f16 v98, v44, v45, v98\n"
    "v_pk_maximum3_f16 v98, v46, v47, v98\n"
    "v_pk_add_u16 v44, s89, v44 clamp\n"
    "v_pk_add_u16 v45, s89, v45 clamp\n"
    "v_pk_add_u16 v46, s89, v46 clamp\n"
    "v_pk_add_u16 v47, s89, v47 clamp\n"
    "v_pk_maximum3_f16 v98, v48, v49, v98\n"
    "v_pk_maximum3_f16 v98, v50, v51, v98\n"
    "v_pk_add_u16 v48, s89, v48 clamp\n"
    "v_pk_add_u16 v49, s89, v49 clamp\n"
    "v_pk_add_u16 v50, s89, v50 clamp\n"
    "v_pk_add_u16 v51, s89, v51 clamp\n"
    "v_pk_maximum3_f16 v98, v52, v53, v98\n"
    "v_pk_maximum3_f16 v98, v54, v55, v98\n"
    "v_pk_add_u16 v52, s89, v52 clamp\n"
    "v_pk_add_u16 v53, s89, v53 clamp\n"
    "v_pk_add_u16 v54, s89, v54 clamp\n"
    "v_pk_add_u16 v55, s89, v55 clamp\n"
    "v_pk_maximum3_f16 v98, v56, v57, v98\n"
    "v_pk_maximum3_f16 v98, v58, v59, v98\n"
    "v_pk_add_u16 v56, s89, v56 clamp\n"
    "v_pk_add_u16 v57, s89, v57 clamp\n"
    "v_pk_add_u16 v58, s89, v58 clamp\n"
    "v_pk_add_u16 v59, s89, v59 clamp\n"
    "v_mov_b32 v108, 0\n"
    "v_mov_b32 v109, 0\n"
    "v_mov_b32 v110, 0\n"
    "v_mov_b32 v111, 0\n"
    "v_mov_b32 v112, 0\n"
    "v_mov_b32 v113, 0\n"
    "v_mov_b32 v114, 0\n"
    "v_mov_b32 v115, 0\n"
    "v_mov_b32 v116, 0\n"
    "v_mov_b32 v117, 0\n"
    "v_mov_b32 v118, 0\n"
    "v_mov_b32 v119, 0\n"
    "v_mov_b32 v120, 0\n"
    "v_mov_b32 v121, 0\n"
    "v_mov_b32 v122, 0\n"
    "v_mov_b32 v123, 0\n"
    "s_waitcnt lgkmcnt(15)\n"
    "v_mfma_f32_16x16x32_bf16 v[4:7], a[112:115], v[36:39], 0\n"
    "s_waitcnt lgkmcnt(15)\n"
    "v_mfma_f32_16x16x32_bf16 v[4:7], a[116:119], v[40:43], v[4:7]\n"
    "s_waitcnt lgkmcnt(15)\n"
    "v_mfma_f32_16x16x32_bf16 v[4:7], a[120:123], v[44:47], v[4:7]\n"
    "s_waitcnt lgkmcnt(15)\n"
    "v_mfma_f32_16x16x32_bf16 v[4:7], a[124:127], v[48:51], v[4:7]\n"
    "s_waitcnt lgkmcnt(15)\n"
    "v_mfma_f32_16x16x32_bf16 v[4:7], a[128:131], v[52:55], v[4:7]\n"
    "s_waitcnt lgkmcnt(15)\n"
    "v_mfma_f32_16x16x32_bf16 v[4:7], a[132:135], v[56:59], v[4:7]\n"
    "s_waitcnt lgkmcnt(15)\n"
    "v_mfma_f32_16x16x32_bf16 v[20:23], a[136:139], v[36:39], 0\n"
    "v_accvgpr_write_b32 a192, v36\n"
    "v_accvgpr_write_b32 a193, v37\n"
    "v_accvgpr_write_b32 a194, v38\n"
    "v_accvgpr_write_b32 a195, v39\n"
    "s_waitcnt lgkmcnt(15)\n"
    "v_mfma_f32_16x16x32_bf16 v[20:23], a[140:143], v[40:43], v[20:23]\n"
    "v_accvgpr_write_b32 a196, v40\n"
    "v_accvgpr_write_b32 a197, v41\n"
    "v_accvgpr_write_b32 a198, v42\n"
    "v_accvgpr_write_b32 a199, v43\n"
    "s_waitcnt lgkmcnt(15)\n"
    "v_mfma_f32_16x16x32_bf16 v[20:23], a[144:147], v[44:47], v[20:23]\n"
    "v_accvgpr_write_b32 a200, v44\n"
    "v_accvgpr_write_b32 a201, v45\n"
    "v_accvgpr_write_b32 a202, v46\n"
    "v_accvgpr_write_b32 a203, v47\n"
    "s_waitcnt lgkmcnt(14)\n"
    "v_mfma_f32_16x16x32_bf16 v[20:23], a[148:151], v[48:51], v[20:23]\n"
    "v_accvgpr_write_b32 a204, v48\n"
    "v_accvgpr_write_b32 a205, v49\n"
    "v_accvgpr_write_b32 a206, v50\n"
    "v_accvgpr_write_b32 a207, v51\n"
    "s_waitcnt lgkmcnt(13)\n"
    "v_mfma_f32_16x16x32_bf16 v[20:23], a[152:155], v[52:55], v[20:23]\n"
    "v_accvgpr_write_b32 a208, v52\n"
    "v_accvgpr_write_b32 a209, v53\n"
    "v_accvgpr_write_b32 a210, v54\n"
    "v_accvgpr_write_b32 a211, v55\n"
    "s_waitcnt lgkmcnt(12)\n"
    "v_mfma_f32_16x16x32_bf16 v[20:23], a[156:159], v[56:59], v[20:23]\n"
    "v_accvgpr_write_b32 a212, v56\n"
    "v_accvgpr_write_b32 a213, v57\n"
    "v_accvgpr_write_b32 a214, v58\n"
    "v_accvgpr_write_b32 a215, v59\n"
    "s_add_u32 m0, s65, 53248\n"
    "s_add_u32 s72, s85, 16384\n"
    "buffer_load_dwordx4 v89, s[20:23], s72 offen offset:0 nt lds\n"
    "buffer_load_dwordx2 v[236:237], v87, s[36:39], 0 offen offset:0 sc1\n"
    "v_mov_b32 v77, 155904\n"
    "v_max3_f32 v76, v4, v5, v6\n"
    "v_max3_f32 v76, v76, v7, v20\n"
    "v_max3_f32 v76, v76, v21, v22\n"
    "v_max_f32 v76, v76, v23\n"
    "v_mov_b32 v78, v76\n"
    "s_nop 1\n"
    "v_permlane32_swap_b32 v76, v78\n"
    "v_max_f32 v76, v76, v78\n"
    "v_mov_b32 v78, v76\n"
    "s_nop 1\n"
    "v_permlane16_swap_b32 v76, v78\n"
    "v_max_f32 v76, v76, v78\n"
    "ds_max_f32 v77, v76\n"
    "s_add_u32 m0, s65, 54272\n"
    "s_add_u32 s72, s85, 17408\n"
    "buffer_load_dwordx4 v90, s[20:23], s72 offen offset:0 nt lds\n"
    "s_add_u32 m0, s65, 98304\n"
    "s_add_u32 s72, s85, 18432\n"
    "buffer_load_dwordx4 v88, s[20:23], s72 offen offset:0 nt lds\n"
    "s_waitcnt lgkmcnt(0)\n"
    "s_barrier\n"
    "ds_read_b32 v76, v77\n"
    "s_waitcnt vmcnt(14)\n"
    "buffer_store_dwordx2 v[238:239], v240, s[36:39], s80 offen offset:0 sc1\n"
    "s_lshl_b32 s73, s79, 8\n"
    "s_add_u32 s80, s80, s73\n"
    "s_mul_i32 s73, s79, 98304\n"
    "s_add_u32 s71, s71, s73\n"
    "s_waitcnt lgkmcnt(0)\n"
    "v_add_f32 v76, 0xc2b00000, v76\n"
    "v_sub_f32 v68, v68, v76\n"
    "v_sub_f32 v69, v69, v76\n"
    "v_sub_f32 v70, v70, v76\n"
    "v_sub_f32 v71, v71, v76\n"
    "v_sub_f32 v72, v72, v76\n"
    "v_sub_f32 v73, v73, v76\n"
    "v_sub_f32 v74, v74, v76\n"
    "v_sub_f32 v75, v75, v76\n"
    "ds_write_b128 v86, v[68:71] offset:0\n"
    "ds_write_b128 v86, v[72:75] offset:16\n"
    "s_mul_i32 s73, s79, 768\n"
    "v_add_u32 v77, s73, v250\n"
    "s_add_u32 m0, s65, 99328\n"
    "s_add_u32 s72, s85, 19456\n"
    "buffer_load_dwordx4 v89, s[20:23], s72 offen offset:0 nt lds\n"
    "s_waitcnt lgkmcnt(0)\n"
    "s_barrier\n"
    "ds_read_b128 v[16:19], v77 offset:0\n"
    "ds_read_b128 v[32:35], v77 offset:16\n"
    "ds_read_b128 v[48:51], v77 offset:128\n"
    "ds_read_b128 v[64:67], v77 offset:144\n"
    "ds_read_b128 v[100:103], v77 offset:768\n"
    "ds_read_b128 v[104:107], v77 offset:784\n"
    "s_add_u32 m0, s65, 100352\n"
    "s_add_u32 s72, s85, 20480\n"
    "buffer_load_dwordx4 v90, s[20:23], s72 offen offset:0 nt lds\n"
    "s_add_u32 m0, s65, 101376\n"
    "s_add_u32 s72, s85, 21504\n"
    "buffer_load_dwordx4 v88, s[20:23], s72 offen offset:0 nt lds\n"
    "s_add_u32 m0, s65, 102400\n"
    "s_add_u32 s72, s85, 22528\n"
    "buffer_load_dwordx4 v89, s[20:23], s72 offen offset:0 nt lds\n"
    "s_add_u32 m0, s65, 103424\n"
    "s_add_u32 s72, s85, 23552\n"
    "buffer_load_dwordx4 v90, s[20:23], s72 offen offset:0 nt lds\n"
    "s_waitcnt lgkmcnt(3)\n"
    "v_mfma_f32_16x16x32_bf16 v[36:39], a[0:3], a[192:195], v[48:51]\n"
    "s_waitcnt vmcnt(14)\n"
    "ds_read_b128 v[192:195], v97 offset:24704\n"
    "ds_read_b128 v[200:203], v97 offset:24832\n"
    "v_mfma_f32_16x16x32_bf16 v[36:39], a[4:7], a[196:199], v[36:39]\n"
    "ds_read_b128 v[196:199], v96 offset:24832\n"
    "v_add_f32 v7, v7, v19\n"
    "v_mfma_f32_16x16x32_bf16 v[36:39], a[8:11], a[200:203], v[36:39]\n"
    "ds_read_b128 v[188:191], v96 offset:24704\n"
    "v_add_f32 v6, v6, v18\n"
    "s_waitcnt lgkmcnt(6)\n"
    "v_mfma_f32_16x16x32_bf16 v[52:55], a[24:27], a[192:195], v[64:67]\n"
    "ds_read_b128 v[184:187], v97 offset:24576\n"
    "ds_read_b128 v[180:183], v96 offset:24576\n"
    "v_mfma_f32_16x16x32_bf16 v[36:39], a[12:15], a[204:207], v[36:39]\n"
    "v_exp_f32 v6, v6\n"
    "v_add_f32 v23, v23, v35\n"
    "v_add_f32 v20, v20, v32\n"
    "v_mfma_f32_16x16x32_bf16 v[52:55], a[28:31], a[196:199], v[52:55]\n"
    "v_add_f32 v4, v4, v16\n"
    "v_add_f32 v5, v5, v17\n"
    "v_mfma_f32_16x16x32_bf16 v[52:55], a[32:35], a[200:203], v[52:55]\n"
    "v_exp_f32 v4, v4\n"
    "v_mfma_f32_16x16x32_bf16 v[36:39], a[16:19], a[208:211], v[36:39]\n"
    "s_waitcnt lgkmcnt(4)\n"
    "v_add_f32 v21, v21, v33\n"
    "v_pk_maximum3_f16 v98, v200, v201, v98\n"
    "v_mfma_f32_16x16x32_bf16 v[36:39], a[20:23], a[212:215], v[36:39]\n"
    "s_waitcnt lgkmcnt(3)\n"
    "v_pk_maximum3_f16 v98, v202, v203, v98\n"
    "v_pk_add_u16 v200, s89, v200 clamp\n"
    "v_pk_maximum3_f16 v98, v196, v197, v98\n"
    "v_pk_add_u16 v203, s89, v203 clamp\n"
    "v_pk_add_u16 v201, s89, v201 clamp\n"
    "v_pk_maximum3_f16 v98, v198, v199, v98\n"
    "v_mfma_f32_16x16x32_bf16 v[52:55], a[36:39], a[204:207], v[52:55]\n"
    "v_pk_maximum3_f16 v98, v192, v193, v98\n"
    "v_pk_maximum3_f16 v98, v194, v195, v98\n"
    "v_mfma_f32_16x16x32_bf16 v[52:55], a[40:43], a[208:211], v[52:55]\n"
    "s_waitcnt lgkmcnt(1)\n"
    "v_pk_maximum3_f16 v98, v188, v189, v98\n"
    "v_pk_maximum3_f16 v98, v190, v191, v98\n"
    "v_pk_add_u16 v197, s89, v197 clamp\n"
    "v_exp_f32 v23, v23\n"
    "v_pk_add_u16 v202, s89, v202 clamp\n"
    "v_pk_maximum3_f16 v98, v184, v185, v98\n"
    "v_pk_add_u16 v194, s89, v194 clamp\n"
    "v_mfma_f32_16x16x32_bf16 v[24:27], a[156:159], v[200:203], v[32:35]\n"
    "v_pk_add_u16 v191, s89, v191 clamp\n"
    "v_accvgpr_write_b32 a237, v201\n"
    "v_mfma_f32_16x16x32_bf16 v[40:43], a[20:23], v[200:203], v[48:51]\n"
    "v_pk_add_u16 v198, s89, v198 clamp\n"
    "v_pk_add_u16 v199, s89, v199 clamp\n"
    "v_mfma_f32_16x16x32_bf16 v[52:55], a[44:47], a[212:215], v[52:55]\n"
    "v_pk_add_u16 v196, s89, v196 clamp\n"
    "v_pk_add_u16 v184, s89, v184 clamp\n"
    "v_pk_add_u16 v189, s89, v189 clamp\n"
    "v_pk_add_u16 v192, s89, v192 clamp\n"
    "v_pk_maximum3_f16 v98, v186, v187, v98\n"
    "v_pk_add_u16 v188, s89, v188 clamp\n"
    "v_pk_add_u16 v193, s89, v193 clamp\n"
    "v_mfma_f32_16x16x32_bf16 v[8:11], a[132:135], v[200:203], v[16:19]\n"
    "v_pk_add_u16 v187, s89, v187 clamp\n"
    "v_exp_f32 v21, v21\n"
    "v_mfma_f32_16x16x32_bf16 v[56:59], a[44:47], v[200:203], v[64:67]\n"
    "v_add_f32 v22, v22, v34\n"
    "v_accvgpr_write_b32 a239, v203\n"
    "v_mfma_f32_16x16x32_bf16 v[24:27], a[152:155], v[196:199], v[24:27]\n"
    "v_pk_add_u16 v186, s89, v186 clamp\n"
    "v_exp_f32 v52, v52\n"
    "v_mfma_f32_16x16x32_bf16 v[40:43], a[16:19], v[196:199], v[40:43]\n"
    "s_waitcnt lgkmcnt(0)\n"
    "v_exp_f32 v36, v36\n"
    "v_accvgpr_write_b32 a223, v187\n"
    "v_pk_add_u16 v195, s89, v195 clamp\n"
    "v_exp_f32 v7, v7\n"
    "v_accvgpr_write_b32 a227, v191\n"
    "v_pk_maximum3_f16 v98, v180, v181, v98\n"
    "v_mfma_f32_16x16x32_bf16 v[24:27], a[148:151], v[192:195], v[24:27]\n"
    "v_accvgpr_write_b32 a224, v188\n"
    "v_exp_f32 v53, v53\n"
    "v_mfma_f32_16x16x32_bf16 v[8:11], a[128:131], v[196:199], v[8:11]\n"
    "v_accvgpr_write_b32 a233, v197\n"
    "v_pk_add_u16 v185, s89, v185 clamp\n"
    "v_pk_add_u16 v190, s89, v190 clamp\n"
    "v_mfma_f32_16x16x32_bf16 v[56:59], a[40:43], v[196:199], v[56:59]\n"
    "v_pk_add_u16 v180, s89, v180 clamp\n"
    "v_pk_maximum3_f16 v98, v182, v183, v98\n"
    "v_pk_add_u16 v181, s89, v181 clamp\n"
    "v_mfma_f32_16x16x32_bf16 v[8:11], a[124:127], v[192:195], v[8:11]\n"
    "v_exp_f32 v20, v20\n"
    "v_accvgpr_write_b32 a238, v202\n"
    "v_mfma_f32_16x16x32_bf16 v[24:27], a[144:147], v[188:191], v[24:27]\n"
    "v_exp_f32 v5, v5\n"
    "v_mfma_f32_16x16x32_bf16 v[40:43], a[12:15], v[192:195], v[40:43]\n"
    "v_pk_add_u16 v182, s89, v182 clamp\n"
    "v_accvgpr_write_b32 a226, v190\n"
    "v_accvgpr_write_b32 a222, v186\n"
    "v_mfma_f32_16x16x32_bf16 v[56:59], a[36:39], v[192:195], v[56:59]\n"
    "v_cvt_pk_bf16_f32 v70, v20, v21\n"
    "v_pk_add_u16 v183, s89, v183 clamp\n"
    "v_mfma_f32_16x16x32_bf16 v[8:11], a[120:123], v[188:191], v[8:11]\n"
    "v_exp_f32 v54, v54\n"
    "v_mfma_f32_16x16x32_bf16 v[24:27], a[140:143], v[184:187], v[24:27]\n"
    "v_exp_f32 v22, v22\n"
    "v_mfma_f32_16x16x32_bf16 v[8:11], a[116:119], v[184:187], v[8:11]\n"
    "v_cvt_pk_bf16_f32 v68, v4, v5\n"
    "v_accvgpr_write_b32 a221, v185\n"
    "v_mfma_f32_16x16x32_bf16 v[40:43], a[8:11], v[188:191], v[40:43]\n"
    "v_exp_f32 v38, v38\n"
    "v_mfma_f32_16x16x32_bf16 v[24:27], a[136:139], v[180:183], v[24:27]\n"
    "v_exp_f32 v39, v39\n"
    "v_mfma_f32_16x16x32_bf16 v[56:59], a[32:35], v[188:191], v[56:59]\n"
    "v_cvt_pk_bf16_f32 v73, v38, v39\n"
    "v_cvt_pk_bf16_f32 v74, v52, v53\n"
    "v_accvgpr_write_b32 a235, v199\n"
    "v_mfma_f32_16x16x32_bf16 v[8:11], a[112:115], v[180:183], v[8:11]\n"
    "v_accvgpr_write_b32 a216, v180\n"
    "v_exp_f32 v55, v55\n"
    "v_mfma_f32_16x16x32_bf16 v[56:59], a[28:31], v[184:187], v[56:59]\n"
    "v_exp_f32 v27, v27\n"
    "v_accvgpr_write_b32 a231, v195\n"
    "v_mfma_f32_16x16x32_bf16 v[40:43], a[4:7], v[184:187], v[40:43]\n"
    "v_exp_f32 v25, v25\n"
    "v_exp_f32 v37, v37\n"
    "v_mfma_f32_16x16x32_bf16 v[40:43], a[0:3], v[180:183], v[40:43]\n"
    "v_accvgpr_write_b32 a228, v192\n"
    "v_exp_f32 v11, v11\n"
    "v_accvgpr_write_b32 a225, v189\n"
    "v_accvgpr_write_b32 a217, v181\n"
    "v_mfma_f32_16x16x32_bf16 v[56:59], a[24:27], v[180:183], v[56:59]\n"
    "v_cvt_pk_bf16_f32 v75, v54, v55\n"
    "v_cvt_pk_bf16_f32 v71, v22, v23\n"
    "v_exp_f32 v24, v24\n"
    "v_exp_f32 v8, v8\n"
    "v_exp_f32 v26, v26\n"
    "v_exp_f32 v43, v43\n"
    "v_exp_f32 v10, v10\n"
    "v_accvgpr_write_b32 a234, v198\n"
    "v_exp_f32 v58, v58\n"
    "v_cvt_pk_bf16_f32 v69, v6, v7\n"
    "v_exp_f32 v42, v42\n"
    "v_exp_f32 v41, v41\n"
    "v_accvgpr_write_b32 a219, v183\n"
    "v_accvgpr_write_b32 a220, v184\n"
    "v_accvgpr_write_b32 a230, v194\n"
    "v_cvt_pk_bf16_f32 v81, v42, v43\n"
    "v_cvt_pk_bf16_f32 v78, v24, v25\n"
    "v_cvt_pk_bf16_f32 v72, v36, v37\n"
    "v_cvt_pk_bf16_f32 v77, v10, v11\n"
    "v_exp_f32 v59, v59\n"
    "v_exp_f32 v40, v40\n"
    "v_accvgpr_write_b32 a232, v196\n"
    "v_exp_f32 v57, v57\n"
    "v_cvt_pk_bf16_f32 v80, v40, v41\n"
    "v_exp_f32 v9, v9\n"
    "v_exp_f32 v56, v56\n"
    "v_accvgpr_write_b32 a218, v182\n"
    "v_accvgpr_write_b32 a236, v200\n"
    "v_cvt_pk_bf16_f32 v76, v8, v9\n"
    "v_cvt_pk_bf16_f32 v83, v58, v59\n"
    "v_cvt_pk_bf16_f32 v82, v56, v57\n"
    "v_accvgpr_write_b32 a229, v193\n"
    "v_cvt_pk_bf16_f32 v79, v26, v27\n"
    "s_waitcnt vmcnt(8)\n"
    "v_cmp_eq_u64 s[54:55], v[236:237], v[238:239]\n"
    "s_nop 0\n"
    "s_cmp_eq_u64 s[54:55], -1\n"
    "s_cbranch_scc0 .LpollAV\n"
    ".LpollAVr:\n"
    "s_add_u32 m0, s65, 24576\n"
    "s_add_u32 s72, s65, 49152\n"
    "buffer_load_dwordx4 v1, s[16:19], s72 offen offset:0 sc1 lds\n"
    "s_add_u32 m0, s65, 25600\n"
    "s_add_u32 s72, s65, 50176\n"
    "buffer_load_dwordx4 v1, s[16:19], s72 offen offset:0 sc1 lds\n"
    "s_add_u32 m0, s65, 26624\n"
    "s_add_u32 s72, s65, 51200\n"
    "buffer_load_dwordx4 v1, s[16:19], s72 offen offset:0 sc1 lds\n"
    "s_add_u32 m0, s65, 27648\n"
    "s_add_u32 s72, s65, 52224\n"
    "buffer_load_dwordx4 v1, s[16:19], s72 offen offset:0 sc1 lds\n"
    "s_add_u32 m0, s65, 28672\n"
    "s_add_u32 s72, s65, 53248\n"
    "buffer_load_dwordx4 v1, s[16:19], s72 offen offset:0 sc1 lds\n"
    "s_add_u32 m0, s65, 29696\n"
    "s_add_u32 s72, s65, 54272\n"
    "buffer_load_dwordx4 v1, s[16:19], s72 offen offset:0 sc1 lds\n"
    "s_waitcnt vmcnt(13)\n"
    "ds_read_b128 v[156:159], v96 offset:0\n"
    "ds_read_b128 v[172:175], v96 offset:256\n"
    "ds_read_b128 v[168:171], v97 offset:128\n"
    "ds_read_b128 v[176:179], v97 offset:256\n"
    "ds_read_b128 v[160:163], v97 offset:0\n"
    "ds_read_b128 v[164:167], v96 offset:128\n"
    "s_waitcnt lgkmcnt(5)\n"
    "v_pk_maximum3_f16 v98, v156, v157, v98\n"
    "v_pk_maximum3_f16 v98, v158, v159, v98\n"
    "v_pk_add_u16 v157, s89, v157 clamp\n"
    "v_pk_add_u16 v158, s89, v158 clamp\n"
    "v_pk_add_u16 v156, s89, v156 clamp\n"
    "v_pk_add_u16 v159, s89, v159 clamp\n"
    "s_waitcnt lgkmcnt(1)\n"
    "v_pk_maximum3_f16 v98, v160, v161, v98\n"
    "s_add_u32 m0, s65, 73728\n"
    "s_add_u32 s72, s65, 73728\n"
    "buffer_load_dwordx4 v1, s[16:19], s72 offen offset:0 sc1 lds\n"
    "v_mfma_f32_16x16x32_bf16 v[28:31], a[136:139], v[156:159], v[32:35]\n"
    "s_waitcnt lgkmcnt(0)\n"
    "v_pk_maximum3_f16 v98, v162, v163, v98\n"
    "v_pk_maximum3_f16 v98, v164, v165, v98\n"
    "v_mfma_f32_16x16x32_bf16 v[12:15], a[112:115], v[156:159], v[16:19]\n"
    "v_pk_add_u16 v161, s89, v161 clamp\n"
    "v_pk_maximum3_f16 v98, v166, v167, v98\n"
    "v_pk_add_u16 v160, s89, v160 clamp\n"
    "v_pk_add_u16 v162, s89, v162 clamp\n"
    "v_mfma_f32_16x16x32_bf16 v[60:63], a[24:27], v[156:159], v[64:67]\n"
    "v_pk_maximum3_f16 v98, v168, v169, v98\n"
    "v_pk_add_u16 v163, s89, v163 clamp\n"
    "v_mfma_f32_16x16x32_bf16 v[44:47], a[0:3], v[156:159], v[48:51]\n"
    "v_pk_maximum3_f16 v98, v170, v171, v98\n"
    "v_pk_add_u16 v164, s89, v164 clamp\n"
    "v_mfma_f32_16x16x32_bf16 v[12:15], a[116:119], v[160:163], v[12:15]\n"
    "v_pk_add_u16 v165, s89, v165 clamp\n"
    "s_add_u32 m0, s65, 74752\n"
    "s_add_u32 s72, s65, 74752\n"
    "buffer_load_dwordx4 v1, s[16:19], s72 offen offset:0 sc1 lds\n"
    "v_pk_add_u16 v169, s89, v169 clamp\n"
    "v_pk_add_u16 v168, s89, v168 clamp\n"
    "v_mfma_f32_16x16x32_bf16 v[60:63], a[28:31], v[160:163], v[60:63]\n"
    "v_pk_maximum3_f16 v98, v172, v173, v98\n"
    "v_pk_add_u16 v170, s89, v170 clamp\n"
    "v_mfma_f32_16x16x32_bf16 v[28:31], a[140:143], v[160:163], v[28:31]\n"
    "v_pk_maximum3_f16 v98, v174, v175, v98\n"
    "v_pk_add_u16 v166, s89, v166 clamp\n"
    "v_pk_add_u16 v167, s89, v167 clamp\n"
    "v_mfma_f32_16x16x32_bf16 v[44:47], a[4:7], v[160:163], v[44:47]\n"
    "v_pk_maximum3_f16 v98, v176, v177, v98\n"
    "v_accvgpr_write_b32 a251, v167\n"
    "v_mfma_f32_16x16x32_bf16 v[44:47], a[8:11], v[164:167], v[44:47]\n"
    "v_pk_maximum3_f16 v98, v178, v179, v98\n"
    "v_pk_add_u16 v176, s89, v176 clamp\n"
    "v_mfma_f32_16x16x32_bf16 v[60:63], a[32:35], v[164:167], v[60:63]\n"
    "v_pk_add_u16 v171, s89, v171 clamp\n"
    "s_add_u32 m0, s65, 75776\n"
    "s_add_u32 s72, s65, 75776\n"
    "buffer_load_dwordx4 v1, s[16:19], s72 offen offset:0 sc1 lds\n"
    "v_pk_add_u16 v174, s89, v174 clamp\n"
    "v_mfma_f32_16x16x32_bf16 v[28:31], a[144:147], v[164:167], v[28:31]\n"
    "v_pk_add_u16 v173, s89, v173 clamp\n"
    "v_pk_add_u16 v172, s89, v172 clamp\n"
    "v_mfma_f32_16x16x32_bf16 v[12:15], a[120:123], v[164:167], v[12:15]\n"
    "v_accvgpr_write_b32 a254, v170\n"
    "v_pk_add_u16 v178, s89, v178 clamp\n"
    "v_mfma_f32_16x16x32_bf16 v[28:31], a[148:151], v[168:171], v[28:31]\n"
    "v_accvgpr_write_b32 a243, v159\n"
    "v_accvgpr_write_b32 a162, v174\n"
    "v_mfma_f32_16x16x32_bf16 v[12:15], a[124:127], v[168:171], v[12:15]\n"
    "v_pk_add_u16 v179, s89, v179 clamp\n"
    "v_pk_add_u16 v175, s89, v175 clamp\n"
    "v_mfma_f32_16x16x32_bf16 v[44:47], a[12:15], v[168:171], v[44:47]\n"
    "v_accvgpr_write_b32 a166, v178\n"
    "v_accvgpr_write_b32 a161, v173\n"
    "v_accvgpr_write_b32 a245, v161\n"
    "s_add_u32 m0, s65, 76800\n"
    "s_add_u32 s72, s65, 76800\n"
    "buffer_load_dwordx4 v1, s[16:19], s72 offen offset:0 sc1 lds\n"
    "v_mfma_f32_16x16x32_bf16 v[28:31], a[152:155], v[172:175], v[28:31]\n"
    "v_accvgpr_write_b32 a163, v175\n"
    "v_pk_add_u16 v177, s89, v177 clamp\n"
    "v_mfma_f32_16x16x32_bf16 v[44:47], a[16:19], v[172:175], v[44:47]\n"
    "v_accvgpr_write_b32 a160, v172\n"
    "v_accvgpr_write_b32 a252, v168\n"
    "v_mfma_f32_16x16x32_bf16 v[28:31], a[156:159], v[176:179], v[28:31]\n"
    "v_accvgpr_write_b32 a249, v165\n"
    "v_accvgpr_write_b32 a255, v171\n"
    "v_mfma_f32_16x16x32_bf16 v[12:15], a[128:131], v[172:175], v[12:15]\n"
    "v_accvgpr_write_b32 a247, v163\n"
    "v_accvgpr_write_b32 a164, v176\n"
    "v_mfma_f32_16x16x32_bf16 v[60:63], a[36:39], v[168:171], v[60:63]\n"
    "v_accvgpr_write_b32 a242, v158\n"
    "v_accvgpr_write_b32 a250, v166\n"
    "v_mfma_f32_16x16x32_bf16 v[44:47], a[20:23], v[176:179], v[44:47]\n"
    "v_accvgpr_write_b32 a241, v157\n"
    "s_add_u32 m0, s65, 77824\n"
    "s_add_u32 s72, s65, 77824\n"
    "buffer_load_dwordx4 v1, s[16:19], s72 offen offset:0 sc1 lds\n"
    "v_accvgpr_write_b32 a248, v164\n"
    "v_mfma_f32_16x16x32_bf16 v[12:15], a[132:135], v[176:179], v[12:15]\n"
    "v_accvgpr_write_b32 a246, v162\n"
    "v_exp_f32 v29, v29\n"
    "v_mfma_f32_16x16x32_bf16 v[60:63], a[40:43], v[172:175], v[60:63]\n"
    "v_exp_f32 v28, v28\n"
    "v_mfma_f32_16x16x32_bf16 v[60:63], a[44:47], v[176:179], v[60:63]\n"
    "v_exp_f32 v31, v31\n"
    "v_exp_f32 v30, v30\n"
    "v_exp_f32 v45, v45\n"
    "v_cvt_pk_bf16_f32 v86, v28, v29\n"
    "v_exp_f32 v47, v47\n"
    "v_accvgpr_write_b32 a240, v156\n"
    "v_exp_f32 v12, v12\n"
    "v_exp_f32 v14, v14\n"
    "v_accvgpr_write_b32 a167, v179\n"
    "v_exp_f32 v15, v15\n"
    "s_add_u32 m0, s65, 78848\n"
    "s_add_u32 s72, s65, 78848\n"
    "buffer_load_dwordx4 v1, s[16:19], s72 offen offset:0 sc1 lds\n"
    "v_accvgpr_write_b32 a253, v169\n"
    "v_exp_f32 v63, v63\n"
    "v_exp_f32 v13, v13\n"
    "v_exp_f32 v44, v44\n"
    "v_cvt_pk_bf16_f32 v84, v12, v13\n"
    "v_cvt_pk_bf16_f32 v87, v30, v31\n"
    "v_exp_f32 v46, v46\n"
    "v_exp_f32 v61, v61\n"
    "v_accvgpr_write_b32 a165, v177\n"
    "v_exp_f32 v60, v60\n"
    "v_exp_f32 v62, v62\n"
    "v_cvt_pk_bf16_f32 v90, v60, v61\n"
    "v_accvgpr_write_b32 a244, v160\n"
    "v_cvt_pk_bf16_f32 v85, v14, v15\n"
    "v_cvt_pk_bf16_f32 v89, v46, v47\n"
    "v_cvt_pk_bf16_f32 v88, v44, v45\n"
    "v_cvt_pk_bf16_f32 v91, v62, v63\n"
    "s_bitcmp1_b32 s83, 1\n"
    "s_cbranch_scc1 .Lf4b\n"
    "s_lshl_b32 s73, s53, 1\n"
    "s_add_u32 s73, s73, s79\n"
    "s_mul_i32 s74, s73, 6144\n"
    "s_add_u32 s74, s74, 1425408\n"
    "s_lshl_b32 s73, s73, 8\n"
    "s_add_u32 s73, s73, 1557504\n"
    "s_mov_b64 s[20:21], s[16:17]\n"
    "s_mov_b32 s44, 98304\n"
    "s_mov_b32 s22, 1572864\n"
    "s_bitcmp1_b32 s84, 0\n"
    "s_cselect_b32 s52, s71, s73\n"
    "s_cselect_b32 s71, s71, s74\n"
    "s_cselect_b32 s63, s63, 0\n"
    "s_cselect_b32 s45, s44, 12288\n"
    "s_cselect_b32 s44, s44, 24576\n"
    "s_cselect_b32 s42, 1523712, s42\n"
    "s_cselect_b32 s22, s22, s18\n"
    "s_cselect_b32 s60, s60, 24576\n"
    "s_cselect_b32 s61, s61, 28672\n"
    "s_cselect_b32 s62, s62, 32768\n"
    "s_mov_b32 s66, 1\n"
    "s_add_u32 s68, s68, 24576\n"
    "s_add_u32 s81, s81, 128\n"
    "s_add_u32 s80, s80, 256\n"
    "s_add_u32 s71, s71, s44\n"
    "s_add_u32 s52, s52, s45\n"
    "s_add_u32 s75, s71, s63\n"
    "s_branch .LhVP\n"
    "s_branch .Lf4e\n"
    ".Lf4b:\n"
    "s_add_u32 s75, s71, s63\n"
    "buffer_load_dwordx4 v[204:207], v242, s[40:43], s75 offen offset:0 nt\n"
    "buffer_load_dwordx4 v[208:211], v242, s[40:43], s75 offen offset:768 nt\n"
    "buffer_load_dwordx4 v[212:215], v242, s[40:43], s75 offen offset:256 nt\n"
    "buffer_load_dwordx4 v[216:219], v242, s[40:43], s75 offen offset:1024 nt\n"
    "buffer_load_dwordx4 v[220:223], v242, s[40:43], s75 offen offset:512 nt\n"
    "buffer_load_dwordx4 v[224:227], v242, s[40:43], s75 offen offset:1280 nt\n"
    "s_lshl_b32 s73, s53, 1\n"
    "s_add_u32 s73, s73, s79\n"
    "s_mul_i32 s74, s73, 6144\n"
    "s_add_u32 s74, s74, 1425408\n"
    "s_lshl_b32 s73, s73, 8\n"
    "s_add_u32 s73, s73, 1557504\n"
    "s_mov_b64 s[20:21], s[16:17]\n"
    "s_mov_b32 s44, 98304\n"
    "s_mov_b32 s22, 1572864\n"
    "s_bitcmp1_b32 s84, 0\n"
    "s_cselect_b32 s52, s71, s73\n"
    "s_cselect_b32 s71, s71, s74\n"
    "s_cselect_b32 s63, s63, 0\n"
    "s_cselect_b32 s45, s44, 12288\n"
    "s_cselect_b32 s44, s44, 24576\n"
    "s_cselect_b32 s42, 1523712, s42\n"
    "s_cselect_b32 s22, s22, s18\n"
    "s_cselect_b32 s60, s60, 24576\n"
    "s_cselect_b32 s61, s61, 28672\n"
    "s_cselect_b32 s62, s62, 32768\n"
    "s_mov_b32 s66, 1\n"
    "s_add_u32 s68, s68, 24576\n"
    "s_add_u32 s81, s81, 128\n"
    "s_branch .LhVN\n"
    ".Lf4e:\n"
    "s_nop 4\n"
    ".Lf5e:\n"
    "s_nop 4\n"
    ".fill ((0x20000 - (. - _Z8attn_fwdPKtPKiS2_S0_S0_S0_S0_PcPt)) & 4095) / 4, 4, 0xBF800000\n"
    ".LhKP:\n"
    "s_waitcnt vmcnt(12)\n"
    "ds_read_b128 v[52:55], v251 offset:0\n"
    "ds_read_b128 v[200:203], v97 offset:49408\n"
    "ds_read_b128 v[192:195], v97 offset:49280\n"
    "ds_read_b128 v[196:199], v96 offset:49408\n"
    "s_add_u32 m0, s68, 0\n"
    "ds_read_b128 v[184:187], v97 offset:49152\n"
    "ds_read_b128 v[188:191], v96 offset:49280\n"
    "ds_read_b128 v[180:183], v96 offset:49152\n"
    "s_add_u32 s83, s67, 4096\n"
    "s_waitcnt lgkmcnt(6)\n"
    "v_smfmac_f32_16x16x64_bf16 v[112:115], v[52:55], v[76:83], v253\n"
    "s_waitcnt lgkmcnt(3)\n"
    "v_pk_maximum3_f16 v98, v200, v201, v98\n"
    "v_pk_maximum3_f16 v98, v202, v203, v98\n"
    "v_pk_maximum3_f16 v98, v196, v197, v98\n"
    "v_pk_add_u16 v202, s89, v202 clamp\n"
    "v_pk_add_u16 v203, s89, v203 clamp\n"
    "v_smfmac_f32_16x16x64_bf16 v[116:119], v[52:55], v[84:91], v253\n"
    "v_pk_maximum3_f16 v98, v198, v199, v98\n"
    "v_pk_maximum3_f16 v98, v192, v193, v98\n"
    "v_pk_add_u16 v200, s89, v200 clamp\n"
    "v_pk_add_u16 v201, s89, v201 clamp\n"
    "v_pk_maximum3_f16 v98, v194, v195, v98\n"
    "v_pk_add_u16 v197, s89, v197 clamp\n"
    "v_mfma_f32_16x16x32_bf16 v[64:67], a[44:47], v[200:203], v[64:67]\n"
    "buffer_load_dwordx4 v1, s[16:19], s67 offen offset:0 sc1 lds\n"
    "s_nop 0\n"
    "v_mfma_f32_16x16x32_bf16 v[48:51], a[20:23], v[200:203], v[48:51]\n"
    "s_waitcnt lgkmcnt(1)\n"
    "v_pk_add_u16 v198, s89, v198 clamp\n"
    "v_pk_maximum3_f16 v98, v188, v189, v98\n"
    "v_pk_add_u16 v195, s89, v195 clamp\n"
    "v_pk_maximum3_f16 v98, v190, v191, v98\n"
    "v_accvgpr_write_b32 a183, v195\n"
    "v_pk_add_u16 v192, s89, v192 clamp\n"
    "v_mfma_f32_16x16x32_bf16 v[16:19], a[132:135], v[200:203], v[16:19]\n"
    "s_nop 0\n"
    "v_pk_add_u16 v196, s89, v196 clamp\n"
    "v_pk_add_u16 v199, s89, v199 clamp\n"
    "v_mfma_f32_16x16x32_bf16 v[32:35], a[156:159], v[200:203], v[32:35]\n"
    "s_waitcnt vmcnt(7)\n"
    "ds_add_u32 v247, v246\n"
    "v_pk_maximum3_f16 v98, v184, v185, v98\n"
    "v_pk_add_u16 v185, s89, v185 clamp\n"
    "v_mfma_f32_16x16x32_bf16 v[32:35], a[152:155], v[196:199], v[32:35]\n"
    "s_nop 0\n"
    "v_pk_add_u16 v191, s89, v191 clamp\n"
    "v_pk_add_u16 v188, s89, v188 clamp\n"
    "v_mfma_f32_16x16x32_bf16 v[64:67], a[40:43], v[196:199], v[64:67]\n"
    "buffer_load_dwordx4 v1, s[16:19], s67 offen offset:1024 sc1 lds\n"
    "v_pk_add_u16 v194, s89, v194 clamp\n"
    "v_mfma_f32_16x16x32_bf16 v[16:19], a[128:131], v[196:199], v[16:19]\n"
    "s_nop 0\n"
    "v_pk_add_u16 v190, s89, v190 clamp\n"
    "v_pk_maximum3_f16 v98, v186, v187, v98\n"
    "v_mfma_f32_16x16x32_bf16 v[48:51], a[16:19], v[196:199], v[48:51]\n"
    "s_nop 0\n"
    "v_pk_add_u16 v193, s89, v193 clamp\n"
    "v_pk_add_u16 v189, s89, v189 clamp\n"
    "v_accvgpr_write_b32 a188, v200\n"
    "v_mfma_f32_16x16x32_bf16 v[16:19], a[124:127], v[192:195], v[16:19]\n"
    "s_waitcnt lgkmcnt(1)\n"
    "v_pk_maximum3_f16 v98, v180, v181, v98\n"
    "v_pk_add_u16 v184, s89, v184 clamp\n"
    "v_pk_add_u16 v186, s89, v186 clamp\n"
    "v_accvgpr_write_b32 a187, v199\n"
    "v_mfma_f32_16x16x32_bf16 v[48:51], a[12:15], v[192:195], v[48:51]\n"
    "ds_read_b32 v245, v247\n"
    "v_accvgpr_write_b32 a181, v193\n"
    "v_mfma_f32_16x16x32_bf16 v[32:35], a[148:151], v[192:195], v[32:35]\n"
    "s_nop 0\n"
    "v_pk_add_u16 v187, s89, v187 clamp\n"
    "v_pk_maximum3_f16 v98, v182, v183, v98\n"
    "v_mfma_f32_16x16x32_bf16 v[32:35], a[144:147], v[188:191], v[32:35]\n"
    "s_nop 0\n"
    "v_pk_add_u16 v180, s89, v180 clamp\n"
    "v_accvgpr_write_b32 a184, v196\n"
    "v_mfma_f32_16x16x32_bf16 v[64:67], a[36:39], v[192:195], v[64:67]\n"
    "s_nop 0\n"
    "v_accvgpr_write_b32 a174, v186\n"
    "v_pk_add_u16 v182, s89, v182 clamp\n"
    "v_mfma_f32_16x16x32_bf16 v[32:35], a[140:143], v[184:187], v[32:35]\n"
    "v_pk_add_u16 v183, s89, v183 clamp\n"
    "v_accvgpr_write_b32 a176, v188\n"
    "v_mfma_f32_16x16x32_bf16 v[64:67], a[32:35], v[188:191], v[64:67]\n"
    "buffer_load_dwordx4 v1, s[16:19], s67 offen offset:2048 sc1 lds\n"
    "v_mfma_f32_16x16x32_bf16 v[48:51], a[8:11], v[188:191], v[48:51]\n"
    "v_accvgpr_write_b32 a182, v194\n"
    "v_accvgpr_write_b32 a175, v187\n"
    "v_pk_add_u16 v181, s89, v181 clamp\n"
    "v_mfma_f32_16x16x32_bf16 v[16:19], a[120:123], v[188:191], v[16:19]\n"
    "s_waitcnt lgkmcnt(0)\n"
    "v_accvgpr_write_b32 a171, v183\n"
    "v_cmp_le_u32 s[56:57], s59, v245\n"
    "v_smfmac_f32_16x16x64_bf16 v[108:111], v[52:55], v[68:75], v253\n"
    "s_cmp_eq_u64 s[56:57], -1\n"
    "s_cbranch_scc0 .LphKP1\n"
    ".LphKP1r:\n"
    "ds_read_b128 a[48:51], v252 offset:24576\n"
    "v_mfma_f32_16x16x32_bf16 v[48:51], a[4:7], v[184:187], v[48:51]\n"
    "ds_read_b128 a[52:55], v252 offset:26624\n"
    "ds_read_b128 a[92:95], v252 offset:35840\n"
    "v_mfma_f32_16x16x32_bf16 v[64:67], a[28:31], v[184:187], v[64:67]\n"
    "ds_read_b128 a[60:63], v252 offset:30720\n"
    "ds_read_b128 a[68:71], v252 offset:34816\n"
    "v_mfma_f32_16x16x32_bf16 v[64:67], a[24:27], v[180:183], v[64:67]\n"
    "buffer_load_dwordx4 v1, s[16:19], s67 offen offset:3072 sc1 lds\n"
    "ds_read_b128 a[56:59], v252 offset:28672\n"
    "v_mfma_f32_16x16x32_bf16 v[16:19], a[116:119], v[184:187], v[16:19]\n"
    "ds_read_b128 a[72:75], v252 offset:25600\n"
    "ds_read_b128 a[76:79], v252 offset:27648\n"
    "v_mfma_f32_16x16x32_bf16 v[48:51], a[0:3], v[180:183], v[48:51]\n"
    "s_add_u32 m0, s68, 4096\n"
    "ds_read_b128 a[84:87], v252 offset:31744\n"
    "v_accvgpr_write_b32 a191, v203\n"
    "v_accvgpr_write_b32 a185, v197\n"
    "v_mfma_f32_16x16x32_bf16 v[32:35], a[136:139], v[180:183], v[32:35]\n"
    "v_accvgpr_write_b32 a186, v198\n"
    "v_accvgpr_write_b32 a172, v184\n"
    "v_mfma_f32_16x16x32_bf16 v[16:19], a[112:115], v[180:183], v[16:19]\n"
    "s_waitcnt lgkmcnt(8)\n"
    "buffer_load_dwordx4 v1, s[16:19], s83 offen offset:0 sc1 lds\n"
    "v_mfma_f32_16x16x32_bf16 v[4:7], a[48:51], a[192:195], v[100:103]\n"
    "s_waitcnt lgkmcnt(7)\n"
    "ds_read_b128 a[88:91], v252 offset:33792\n"
    "v_accvgpr_write_b32 a177, v189\n"
    "v_mfma_f32_16x16x32_bf16 v[4:7], a[52:55], a[196:199], v[4:7]\n"
    "v_accvgpr_write_b32 a168, v180\n"
    "v_accvgpr_write_b32 a180, v192\n"
    "v_mfma_f32_16x16x32_bf16 v[12:15], a[48:51], a[240:243], v[100:103]\n"
    "ds_read_b128 a[80:83], v252 offset:29696\n"
    "v_accvgpr_write_b32 a169, v181\n"
    "v_mfma_f32_16x16x32_bf16 v[12:15], a[52:55], a[244:247], v[12:15]\n"
    "s_waitcnt lgkmcnt(4)\n"
    "buffer_load_dwordx4 v1, s[16:19], s83 offen offset:1024 sc1 lds\n"
    "ds_read_b128 a[64:67], v252 offset:32768\n"
    "v_mfma_f32_16x16x32_bf16 v[28:31], a[72:75], a[240:243], v[104:107]\n"
    "s_add_u32 m0, s64, 0\n"
    "v_accvgpr_write_b32 a179, v191\n"
    "v_accvgpr_write_b32 a189, v201\n"
    "v_mfma_f32_16x16x32_bf16 v[4:7], a[56:59], a[200:203], v[4:7]\n"
    "v_accvgpr_write_b32 a170, v182\n"
    "v_accvgpr_write_b32 a173, v185\n"
    "v_mfma_f32_16x16x32_bf16 v[12:15], a[56:59], a[248:251], v[12:15]\n"
    "v_accvgpr_write_b32 a190, v202\n"
    "v_accvgpr_write_b32 a178, v190\n"
    "v_mfma_f32_16x16x32_bf16 v[24:27], a[92:95], a[236:239], v[104:107]\n"
    "s_add_u32 s83, s67, 24576\n"
    "buffer_load_dwordx4 v1, s[16:19], s83 offen offset:0 sc1 lds\n"
    "v_mfma_f32_16x16x32_bf16 v[12:15], a[60:63], a[252:255], v[12:15]\n"
    "v_exp_f32 v16, v16\n"
    "v_mfma_f32_16x16x32_bf16 v[8:11], a[68:71], a[236:239], v[100:103]\n"
    "v_exp_f32 v18, v18\n"
    "v_mfma_f32_16x16x32_bf16 v[4:7], a[60:63], a[204:207], v[4:7]\n"
    "s_add_u32 s59, s59, 4\n"
    "v_exp_f32 v19, v19\n"
    "v_mfma_f32_16x16x32_bf16 v[20:23], a[72:75], a[192:195], v[104:107]\n"
    "s_waitcnt lgkmcnt(0)\n"
    "buffer_load_dwordx4 v1, s[16:19], s83 offen offset:1024 sc1 lds\n"
    "v_mfma_f32_16x16x32_bf16 v[4:7], a[64:67], a[208:211], v[4:7]\n"
    "s_waitcnt vmcnt(8)\n"
    "v_exp_f32 v17, v17\n"
    "v_mfma_f32_16x16x32_bf16 v[8:11], a[64:67], a[232:235], v[8:11]\n"
    "ds_add_u32 v247, v246\n"
    "v_mfma_f32_16x16x32_bf16 v[28:31], a[76:79], a[244:247], v[28:31]\n"
    "v_cvt_pk_bf16_f32 v93, v18, v19\n"
    "v_cvt_pk_bf16_f32 v92, v16, v17\n"
    "v_mfma_f32_16x16x32_bf16 v[4:7], a[68:71], a[212:215], v[4:7]\n"
    "buffer_load_dwordx4 v1, s[16:19], s83 offen offset:2048 sc1 lds\n"
    "v_mfma_f32_16x16x32_bf16 v[16:19], a[68:71], a[188:191], v[100:103]\n"
    "v_exp_f32 v34, v34\n"
    "v_mfma_f32_16x16x32_bf16 v[24:27], a[88:91], a[232:235], v[24:27]\n"
    "v_exp_f32 v48, v48\n"
    "v_mfma_f32_16x16x32_bf16 v[8:11], a[60:63], a[228:231], v[8:11]\n"
    "v_exp_f32 v35, v35\n"
    "v_mfma_f32_16x16x32_bf16 v[8:11], a[56:59], a[224:227], v[8:11]\n"
    "buffer_load_dwordx4 v1, s[16:19], s83 offen offset:3072 sc1 lds\n"
    "v_mfma_f32_16x16x32_bf16 v[24:27], a[84:87], a[228:231], v[24:27]\n"
    "s_add_u32 m0, s64, 4096\n"
    "v_exp_f32 v32, v32\n"
    "v_mfma_f32_16x16x32_bf16 v[16:19], a[64:67], a[184:187], v[16:19]\n"
    "ds_read_b32 v245, v247\n"
    "v_mfma_f32_16x16x32_bf16 v[16:19], a[60:63], a[180:183], v[16:19]\n"
    "v_exp_f32 v33, v33\n"
    "v_mfma_f32_16x16x32_bf16 v[8:11], a[52:55], a[220:223], v[8:11]\n"
    "s_add_u32 s83, s67, 28672\n"
    "v_cvt_pk_bf16_f32 v95, v34, v35\n"
    "v_pk_add_u16 v98, s89, v98\n"
    "v_mfma_f32_16x16x32_bf16 v[12:15], a[64:67], a[160:163], v[12:15]\n"
    "buffer_load_dwordx4 v1, s[16:19], s83 offen offset:0 sc1 lds\n"
    "v_mfma_f32_16x16x32_bf16 v[24:27], a[80:83], a[224:227], v[24:27]\n"
    "v_mfma_f32_16x16x32_bf16 v[20:23], a[76:79], a[196:199], v[20:23]\n"
    "v_cvt_pk_bf16_f32 v94, v32, v33\n"
    "v_mfma_f32_16x16x32_bf16 v[32:35], a[92:95], a[188:191], v[104:107]\n"
    "v_exp_f32_e64 v50, v50\n"
    "v_mfma_f32_16x16x32_bf16 v[32:35], a[88:91], a[184:187], v[32:35]\n"
    "buffer_load_dwordx4 v1, s[16:19], s83 offen offset:1024 sc1 lds\n"
    "v_mfma_f32_16x16x32_bf16 v[32:35], a[84:87], a[180:183], v[32:35]\n"
    "s_waitcnt lgkmcnt(0)\n"
    "v_cmp_le_u32 s[56:57], s59, v245\n"
    "v_mfma_f32_16x16x32_bf16 v[24:27], a[76:79], a[220:223], v[24:27]\n"
    "s_cmp_eq_u64 s[56:57], -1\n"
    "s_cbranch_scc0 .LphKP2\n"
    ".LphKP2r:\n"
    "v_mfma_f32_16x16x32_bf16 v[20:23], a[80:83], a[200:203], v[20:23]\n"
    "ds_read_b128 a[120:123], v248 offset:1024\n"
    "ds_read_b128 a[128:131], v248 offset:2048\n"
    "v_mfma_f32_16x16x32_bf16 v[32:35], a[80:83], a[176:179], v[32:35]\n"
    "buffer_load_dwordx2 v[204:205], v242, s[28:31], s75 offen offset:0 nt\n"
    "v_mfma_f32_16x16x32_bf16 v[16:19], a[56:59], a[176:179], v[16:19]\n"
    "ds_read_b128 a[112:115], v248 offset:0\n"
    "ds_read_b128 a[124:127], v248 offset:13312\n"
    "v_mfma_f32_16x16x32_bf16 v[16:19], a[52:55], a[172:175], v[16:19]\n"
    "buffer_load_dwordx2 v[206:207], v242, s[28:31], s75 offen offset:768 nt\n"
    "v_mfma_f32_16x16x32_bf16 v[32:35], a[76:79], a[172:175], v[32:35]\n"
    "v_exp_f32 v66, v66\n"
    "v_mfma_f32_16x16x32_bf16 v[28:31], a[80:83], a[248:251], v[28:31]\n"
    "buffer_load_dwordx2 v[208:209], v242, s[28:31], s75 offen offset:3072 nt\n"
    "v_mfma_f32_16x16x32_bf16 v[8:11], a[48:51], a[216:219], v[8:11]\n"
    "ds_read_b128 a[132:135], v248 offset:14336\n"
    "ds_read_b128 a[148:151], v248 offset:16384\n"
    "v_mfma_f32_16x16x32_bf16 v[28:31], a[84:87], a[252:255], v[28:31]\n"
    "buffer_load_dwordx2 v[210:211], v242, s[28:31], s75 offen offset:3840 nt\n"
    "v_mfma_f32_16x16x32_bf16 v[20:23], a[84:87], a[204:207], v[20:23]\n"
    "s_waitcnt lgkmcnt(5)\n"
    "ds_read_b128 a[116:119], v248 offset:12288\n"
    "v_mfma_f32_16x16x32_bf16 v[176:179], a[120:123], v[92:95], 0\n"
    "buffer_load_dwordx2 v[212:213], v242, s[28:31], s76 offen offset:0 nt\n"
    "v_mfma_f32_16x16x32_bf16 v[24:27], a[72:75], a[216:219], v[24:27]\n"
    "s_waitcnt lgkmcnt(3)\n"
    "ds_read_b128 a[152:155], v248 offset:5120\n"
    "v_mfma_f32_16x16x32_bf16 v[160:163], a[124:127], v[88:91], 0\n"
    "buffer_load_dwordx2 v[214:215], v242, s[28:31], s76 offen offset:768 nt\n"
    "v_mfma_f32_16x16x32_bf16 v[132:135], a[128:131], v[68:71], 0\n"
    "v_exp_f32 v49, v49\n"
    "v_mfma_f32_16x16x32_bf16 v[20:23], a[88:91], a[208:211], v[20:23]\n"
    "buffer_load_dwordx2 v[216:217], v242, s[28:31], s76 offen offset:3072 nt\n"
    "v_mfma_f32_16x16x32_bf16 v[160:163], a[120:123], v[84:87], v[160:163]\n"
    "ds_read_b128 a[144:147], v248 offset:4096\n"
    "v_mfma_f32_16x16x32_bf16 v[164:167], a[128:131], v[84:87], 0\n"
    "buffer_load_dwordx2 v[218:219], v242, s[28:31], s76 offen offset:3840 nt\n"
    "v_mfma_f32_16x16x32_bf16 v[28:31], a[88:91], a[160:163], v[28:31]\n"
    "s_waitcnt lgkmcnt(4)\n"
    "v_mfma_f32_16x16x32_bf16 v[164:167], a[132:135], v[88:91], v[164:167]\n"
    "s_waitcnt lgkmcnt(3)\n"
    "buffer_load_dwordx2 v[220:221], v242, s[28:31], s77 offen offset:0 nt\n"
    "v_mfma_f32_16x16x32_bf16 a[16:19], a[148:151], v[88:91], 0\n"
    "ds_read_b128 a[156:159], v248 offset:17408\n"
    "v_and_b32 v98, 0x80008000, v98\n"
    "v_mfma_f32_16x16x32_bf16 v[188:191], a[148:151], v[72:75], 0\n"
    "buffer_load_dwordx2 v[222:223], v242, s[28:31], s77 offen offset:768 nt\n"
    "v_mfma_f32_16x16x32_bf16 v[132:135], a[132:135], v[72:75], v[132:135]\n"
    "s_waitcnt lgkmcnt(3)\n"
    "v_exp_f32 v67, v67\n"
    "v_mfma_f32_16x16x32_bf16 v[140:143], a[116:119], v[80:83], 0\n"
    "s_waitcnt lgkmcnt(2)\n"
    "buffer_load_dwordx2 v[224:225], v242, s[28:31], s77 offen offset:3072 nt\n"
    "v_mfma_f32_16x16x32_bf16 v[192:195], a[152:155], v[68:71], 0\n"
    "v_exp_f32 v65, v65\n"
    "v_mfma_f32_16x16x32_bf16 v[20:23], a[92:95], a[212:215], v[20:23]\n"
    "buffer_load_dwordx2 v[226:227], v242, s[28:31], s77 offen offset:3840 nt\n"
    "v_mfma_f32_16x16x32_bf16 v[156:159], a[112:115], v[84:87], 0\n"
    "v_cmp_ne_u32 s[34:35], 0, v98\n"
    "v_cvt_pk_bf16_f32 v99, v66, v67\n"
    "v_mfma_f32_16x16x32_bf16 v[156:159], a[116:119], v[88:91], v[156:159]\n"
    "s_waitcnt lgkmcnt(1)\n"
    "buffer_load_dwordx2 v[228:229], v242, s[28:31], s78 offen offset:0 nt\n"
    "v_mfma_f32_16x16x32_bf16 v[188:191], a[144:147], v[68:71], v[188:191]\n"
    "v_exp_f32 v51, v51\n"
    "v_mfma_f32_16x16x32_bf16 v[12:15], a[68:71], a[164:167], v[12:15]\n"
    "buffer_load_dwordx2 v[230:231], v242, s[28:31], s78 offen offset:768 nt\n"
    "v_mfma_f32_16x16x32_bf16 v[128:131], a[124:127], v[72:75], 0\n"
    "ds_read_b128 a[140:143], v248 offset:15360\n"
    "v_mfma_f32_16x16x32_bf16 a[0:3], a[144:147], v[76:79], 0\n"
    "buffer_load_dwordx2 v[232:233], v242, s[28:31], s78 offen offset:3072 nt\n"
    "v_mfma_f32_16x16x32_bf16 v[124:127], a[112:115], v[68:71], 0\n"
    "ds_read_b128 a[136:139], v248 offset:3072\n"
    "v_mfma_f32_16x16x32_bf16 v[128:131], a[120:123], v[68:71], v[128:131]\n"
    "buffer_load_dwordx2 v[234:235], v242, s[28:31], s78 offen offset:3840 nt\n"
    "v_mfma_f32_16x16x32_bf16 a[32:35], a[144:147], v[92:95], 0\n"
    "v_exp_f32 v24, v24\n"
    "v_mfma_f32_16x16x32_bf16 v[16:19], a[48:51], a[168:171], v[16:19]\n"
    "v_exp_f32 v10, v10\n"
    "v_mfma_f32_16x16x32_bf16 v[144:147], a[120:123], v[76:79], 0\n"
    "v_exp_f32 v64, v64\n"
    "v_mfma_f32_16x16x32_bf16 v[148:151], a[132:135], v[80:83], 0\n"
    "v_cvt_pk_bf16_f32 v97, v50, v51\n"
    "v_cvt_pk_bf16_f32 v98, v64, v65\n"
    "v_mfma_f32_16x16x32_bf16 v[124:127], a[116:119], v[72:75], v[124:127]\n"
    "v_exp_f32 v22, v22\n"
    "v_mfma_f32_16x16x32_bf16 v[28:31], a[92:95], a[164:167], v[28:31]\n"
    "s_waitcnt lgkmcnt(2)\n"
    "v_exp_f32 v6, v6\n"
    "v_mfma_f32_16x16x32_bf16 v[192:195], a[156:159], v[72:75], v[192:195]\n"
    "v_mfma_f32_16x16x32_bf16 v[148:151], a[128:131], v[76:79], v[148:151]\n"
    "s_waitcnt lgkmcnt(1)\n"
    "v_cvt_pk_bf16_f32 v96, v48, v49\n"
    "v_mfma_f32_16x16x32_bf16 v[152:155], a[140:143], v[80:83], 0\n"
    "v_exp_f32 v16, v16\n"
    "v_mfma_f32_16x16x32_bf16 v[32:35], a[72:75], a[168:171], v[32:35]\n"
    "v_exp_f32 v15, v15\n"
    "v_mfma_f32_16x16x32_bf16 v[172:175], a[116:119], v[96:99], 0\n"
    "v_exp_f32 v14, v14\n"
    "v_mfma_f32_16x16x32_bf16 v[184:187], a[140:143], v[96:99], 0\n"
    "s_add_u32 s59, s59, 4\n"
    "v_exp_f32 v31, v31\n"
    "v_mfma_f32_16x16x32_bf16 v[180:183], a[132:135], v[96:99], 0\n"
    "v_mfma_f32_16x16x32_bf16 v[144:147], a[124:127], v[80:83], v[144:147]\n"
    "v_mfma_f32_16x16x32_bf16 v[180:183], a[128:131], v[92:95], v[180:183]\n"
    "ds_read_b128 a[132:135], v248 offset:20480\n"
    "v_mfma_f32_16x16x32_bf16 a[16:19], a[144:147], v[84:87], a[16:19]\n"
    "s_waitcnt lgkmcnt(1)\n"
    "v_exp_f32 v13, v13\n"
    "v_mfma_f32_16x16x32_bf16 v[168:171], a[136:139], v[84:87], 0\n"
    "ds_read_b128 v[100:103], v250 offset:896\n"
    "ds_read_b128 a[116:119], v248 offset:18432\n"
    "v_mfma_f32_16x16x32_bf16 a[0:3], a[148:151], v[80:83], a[0:3]\n"
    "v_exp_f32 v21, v21\n"
    "v_mfma_f32_16x16x32_bf16 a[4:7], a[156:159], v[80:83], 0\n"
    "v_exp_f32 v29, v29\n"
    "v_mfma_f32_16x16x32_bf16 a[20:23], a[152:155], v[84:87], 0\n"
    "ds_read_b128 a[144:147], v248 offset:10240\n"
    "ds_read_b128 a[128:131], v248 offset:8192\n"
    "v_mfma_f32_16x16x32_bf16 v[140:143], a[112:115], v[76:79], v[140:143]\n"
    "v_exp_f32 v5, v5\n"
    "v_mfma_f32_16x16x32_bf16 a[32:35], a[148:151], v[96:99], a[32:35]\n"
    "v_exp_f32 v7, v7\n"
    "v_smfmac_f32_16x16x64_bf16 v[120:123], v[52:55], v[92:99], v253\n"
    "ds_read_b128 a[120:123], v248 offset:7168\n"
    "v_mfma_f32_16x16x32_bf16 v[184:187], a[136:139], v[92:95], v[184:187]\n"
    "v_mfma_f32_16x16x32_bf16 v[176:179], a[124:127], v[96:99], v[176:179]\n"
    "v_exp_f32 v20, v20\n"
    "v_mfma_f32_16x16x32_bf16 v[172:175], a[112:115], v[92:95], v[172:175]\n"
    "s_waitcnt lgkmcnt(5)\n"
    "ds_read_b128 a[148:151], v248 offset:22528\n"
    "ds_read_b128 a[124:127], v248 offset:19456\n"
    "v_mfma_f32_16x16x32_bf16 a[64:67], a[132:135], v[80:83], 0\n"
    "v_exp_f32 v12, v12\n"
    "v_mfma_f32_16x16x32_bf16 v[152:155], a[136:139], v[76:79], v[152:155]\n"
    "v_mfma_f32_16x16x32_bf16 a[96:99], a[132:135], v[96:99], 0\n"
    "v_exp_f32 v28, v28\n"
    "v_mfma_f32_16x16x32_bf16 v[136:139], a[136:139], v[68:71], 0\n"
    "s_waitcnt lgkmcnt(4)\n"
    "v_mfma_f32_16x16x32_bf16 a[104:107], a[144:147], v[92:95], 0\n"
    "v_exp_f32 v17, v17\n"
    "v_mfma_f32_16x16x32_bf16 a[4:7], a[152:155], v[76:79], a[4:7]\n"
    "v_mfma_f32_16x16x32_bf16 a[72:75], a[144:147], v[76:79], 0\n"
    "v_exp_f32 v34, v34\n"
    "v_mfma_f32_16x16x32_bf16 a[36:39], a[156:159], v[96:99], 0\n"
    "v_exp_f32 v11, v11\n"
    "v_mfma_f32_16x16x32_bf16 v[136:139], a[140:143], v[72:75], v[136:139]\n"
    "buffer_load_dwordx2 v[236:237], v241, s[36:39], s81 offen offset:0 sc1\n"
    "v_mfma_f32_16x16x32_bf16 a[36:39], a[152:155], v[92:95], a[36:39]\n"
    "s_waitcnt lgkmcnt(3)\n"
    "ds_read_b128 a[112:115], v248 offset:6144\n"
    "ds_read_b128 a[136:139], v248 offset:9216\n"
    "v_mfma_f32_16x16x32_bf16 a[48:51], a[128:131], v[68:71], 0\n"
    "v_mfma_f32_16x16x32_bf16 a[20:23], a[156:159], v[88:91], a[20:23]\n"
    "v_exp_f32 v9, v9\n"
    "v_mfma_f32_16x16x32_bf16 a[48:51], a[132:135], v[72:75], a[48:51]\n"
    "s_waitcnt lgkmcnt(4)\n"
    "v_exp_f32 v32, v32\n"
    "v_mfma_f32_16x16x32_bf16 a[44:47], a[120:123], v[92:95], 0\n"
    "s_waitcnt lgkmcnt(2)\n"
    "v_exp_f32 v30, v30\n"
    "v_mfma_f32_16x16x32_bf16 a[28:31], a[124:127], v[88:91], 0\n"
    "v_exp_f32 v26, v26\n"
    "v_mfma_f32_16x16x32_bf16 a[44:47], a[124:127], v[96:99], a[44:47]\n"
    "v_exp_f32 v35, v35\n"
    "v_mfma_f32_16x16x32_bf16 v[200:203], a[124:127], v[72:75], 0\n"
    "v_exp_f32 v8, v8\n"
    "v_mfma_f32_16x16x32_bf16 a[40:43], a[116:119], v[96:99], 0\n"
    "v_mfma_f32_16x16x32_bf16 a[12:15], a[120:123], v[76:79], 0\n"
    "v_mfma_f32_16x16x32_bf16 a[80:83], a[128:131], v[84:87], 0\n"
    "v_mfma_f32_16x16x32_bf16 a[12:15], a[124:127], v[80:83], a[12:15]\n"
    "s_waitcnt lgkmcnt(1)\n"
    "v_mfma_f32_16x16x32_bf16 a[24:27], a[112:115], v[84:87], 0\n"
    "ds_read_b128 a[152:155], v248 offset:11264\n"
    "v_mfma_f32_16x16x32_bf16 v[168:171], a[140:143], v[88:91], v[168:171]\n"
    "s_waitcnt lgkmcnt(1)\n"
    "ds_read_b128 a[140:143], v248 offset:21504\n"
    "v_mfma_f32_16x16x32_bf16 a[84:87], a[136:139], v[84:87], 0\n"
    "ds_read_b128 a[156:159], v248 offset:23552\n"
    "v_mfma_f32_16x16x32_bf16 a[96:99], a[128:131], v[92:95], a[96:99]\n"
    "v_mfma_f32_16x16x32_bf16 v[196:199], a[112:115], v[68:71], 0\n"
    "v_mfma_f32_16x16x32_bf16 a[64:67], a[128:131], v[76:79], a[64:67]\n"
    "v_mfma_f32_16x16x32_bf16 a[104:107], a[148:151], v[96:99], a[104:107]\n"
    "v_mfma_f32_16x16x32_bf16 a[88:91], a[148:151], v[88:91], 0\n"
    "v_mfma_f32_16x16x32_bf16 a[28:31], a[120:123], v[84:87], a[28:31]\n"
    "ds_read_b128 a[124:127], v3 offset:18432\n"
    "v_mfma_f32_16x16x32_bf16 a[52:55], a[136:139], v[68:71], 0\n"
    "v_mfma_f32_16x16x32_bf16 a[72:75], a[148:151], v[80:83], a[72:75]\n"
    "v_mfma_f32_16x16x32_bf16 a[8:11], a[116:119], v[80:83], 0\n"
    "s_waitcnt lgkmcnt(3)\n"
    "v_exp_f32 v27, v27\n"
    "v_mfma_f32_16x16x32_bf16 a[92:95], a[152:155], v[84:87], 0\n"
    "v_mfma_f32_16x16x32_bf16 a[40:43], a[112:115], v[92:95], a[40:43]\n"
    "s_waitcnt lgkmcnt(2)\n"
    "v_mfma_f32_16x16x32_bf16 a[100:103], a[140:143], v[96:99], 0\n"
    "ds_read_b128 a[128:131], v3 offset:20480\n"
    "v_mfma_f32_16x16x32_bf16 a[80:83], a[132:135], v[88:91], a[80:83]\n"
    "v_exp_f32 v25, v25\n"
    "v_mfma_f32_16x16x32_bf16 a[60:63], a[152:155], v[68:71], 0\n"
    "s_waitcnt lgkmcnt(2)\n"
    "v_mfma_f32_16x16x32_bf16 a[108:111], a[156:159], v[96:99], 0\n"
    "v_exp_f32 v23, v23\n"
    "v_mfma_f32_16x16x32_bf16 a[108:111], a[152:155], v[92:95], a[108:111]\n"
    "v_mov_b32 v2, v3\n"
    "v_mfma_f32_16x16x32_bf16 a[8:11], a[112:115], v[76:79], a[8:11]\n"
    "v_mfma_f32_16x16x32_bf16 v[200:203], a[120:123], v[68:71], v[200:203]\n"
    "v_exp_f32 v33, v33\n"
    "v_mfma_f32_16x16x32_bf16 v[196:199], a[116:119], v[72:75], v[196:199]\n"
    "s_waitcnt vmcnt(17)\n"
    "s_add_u32 s67, s67, 49152\n"
    "v_mfma_f32_16x16x32_bf16 a[52:55], a[140:143], v[72:75], a[52:55]\n"
    "ds_read_b128 a[112:115], v3 offset:12288\n"
    "ds_read_b128 a[132:135], v3 offset:22528\n"
    "v_mfma_f32_16x16x32_bf16 a[56:59], a[148:151], v[72:75], 0\n"
    "ds_add_u32 v247, v246\n"
    "v_mfma_f32_16x16x32_bf16 a[60:63], a[156:159], v[72:75], a[60:63]\n"
    "v_exp_f32 v18, v18\n"
    "v_mfma_f32_16x16x32_bf16 a[24:27], a[116:119], v[88:91], a[24:27]\n"
    "ds_read_b128 a[116:119], v3 offset:14336\n"
    "ds_read_b128 a[120:123], v3 offset:16384\n"
    "v_mfma_f32_16x16x32_bf16 a[88:91], a[144:147], v[84:87], a[88:91]\n"
    "v_mfma_f32_16x16x32_bf16 a[68:71], a[140:143], v[80:83], 0\n"
    "v_add_u32 v3, 49152, v252\n"
    "v_mfma_f32_16x16x32_bf16 a[76:79], a[156:159], v[80:83], 0\n"
    "v_mfma_f32_16x16x32_bf16 a[76:79], a[152:155], v[76:79], a[76:79]\n"
    "v_exp_f32 v19, v19\n"
    "v_mfma_f32_16x16x32_bf16 a[84:87], a[140:143], v[88:91], a[84:87]\n"
    "v_exp_f32 v4, v4\n"
    "v_mfma_f32_16x16x32_bf16 a[56:59], a[144:147], v[68:71], a[56:59]\n"
    "v_mfma_f32_16x16x32_bf16 a[68:71], a[136:139], v[76:79], a[68:71]\n"
    "v_mfma_f32_16x16x32_bf16 a[92:95], a[156:159], v[88:91], a[92:95]\n"
    "s_waitcnt lgkmcnt(0)\n"
    "v_mfma_f32_16x16x32_bf16 a[100:103], a[136:139], v[92:95], a[100:103]\n"
    "s_branch .LNK\n"
    ".fill ((0x20000 - (. - _Z8attn_fwdPKtPKiS2_S0_S0_S0_S0_PcPt)) & 4095) / 4, 4, 0xBF800000\n"
    ".LhKN:\n"
    "s_waitcnt vmcnt(28)\n"
    "ds_read_b128 v[52:55], v251 offset:768\n"
    "ds_read_b128 v[196:199], v96 offset:49408\n"
    "s_add_u32 m0, s68, 0\n"
    "ds_read_b128 v[200:203], v97 offset:49408\n"
    "ds_read_b128 v[36:39], v243 offset:0\n"
    "ds_read_b128 v[188:191], v96 offset:49280\n"
    "ds_read_b128 v[180:183], v96 offset:49152\n"
    "ds_read_b128 v[184:187], v97 offset:49152\n"
    "ds_read_b128 v[192:195], v97 offset:49280\n"
    "s_waitcnt lgkmcnt(7)\n"
    "v_smfmac_f32_16x16x64_bf16 v[112:115], v[52:55], v[76:83], v253\n"
    "s_waitcnt lgkmcnt(5)\n"
    "v_pk_maximum3_f16 v98, v200, v201, v98\n"
    "v_pk_maximum3_f16 v98, v202, v203, v98\n"
    "v_pk_maximum3_f16 v98, v196, v197, v98\n"
    "v_pk_add_u16 v201, s89, v201 clamp\n"
    "v_pk_add_u16 v202, s89, v202 clamp\n"
    "v_pk_add_u16 v200, s89, v200 clamp\n"
    "v_pk_add_u16 v203, s89, v203 clamp\n"
    "v_pk_maximum3_f16 v98, v198, v199, v98\n"
    "v_pk_add_u16 v198, s89, v198 clamp\n"
    "v_mfma_f32_16x16x32_bf16 v[48:51], a[20:23], v[200:203], v[48:51]\n"
    "s_waitcnt lgkmcnt(0)\n"
    "v_pk_maximum3_f16 v98, v192, v193, v98\n"
    "v_pk_add_u16 v193, s89, v193 clamp\n"
    "v_pk_add_u16 v196, s89, v196 clamp\n"
    "v_mfma_f32_16x16x32_bf16 v[64:67], a[44:47], v[200:203], v[64:67]\n"
    "s_nop 0\n"
    "v_pk_add_u16 v197, s89, v197 clamp\n"
    "v_pk_maximum3_f16 v98, v194, v195, v98\n"
    "v_mfma_f32_16x16x32_bf16 v[32:35], a[156:159], v[200:203], v[32:35]\n"
    "s_add_u32 s83, s67, 4096\n"
    "v_pk_add_u16 v194, s89, v194 clamp\n"
    "v_pk_add_u16 v199, s89, v199 clamp\n"
    "v_pk_add_u16 v195, s89, v195 clamp\n"
    "v_mfma_f32_16x16x32_bf16 v[16:19], a[132:135], v[200:203], v[16:19]\n"
    "v_pk_maximum3_f16 v98, v188, v189, v98\n"
    "v_pk_maximum3_f16 v98, v190, v191, v98\n"
    "v_mfma_f32_16x16x32_bf16 v[48:51], a[16:19], v[196:199], v[48:51]\n"
    "s_waitcnt vmcnt(22)\n"
    "ds_add_u32 v247, v246\n"
    "buffer_load_dwordx4 v1, s[16:19], s67 offen offset:0 sc1 lds\n"
    "v_smfmac_f32_16x16x64_bf16 v[116:119], v[52:55], v[84:91], v253\n"
    "v_pk_maximum3_f16 v98, v184, v185, v98\n"
    "v_pk_add_u16 v191, s89, v191 clamp\n"
    "v_mfma_f32_16x16x32_bf16 v[64:67], a[40:43], v[196:199], v[64:67]\n"
    "s_nop 0\n"
    "v_pk_maximum3_f16 v98, v186, v187, v98\n"
    "v_pk_add_u16 v186, s89, v186 clamp\n"
    "v_mfma_f32_16x16x32_bf16 v[16:19], a[128:131], v[196:199], v[16:19]\n"
    "v_pk_add_u16 v190, s89, v190 clamp\n"
    "v_accvgpr_write_b32 a190, v202\n"
    "v_pk_add_u16 v189, s89, v189 clamp\n"
    "v_pk_add_u16 v192, s89, v192 clamp\n"
    "v_mfma_f32_16x16x32_bf16 v[32:35], a[152:155], v[196:199], v[32:35]\n"
    "v_accvgpr_write_b32 a174, v186\n"
    "v_pk_add_u16 v188, s89, v188 clamp\n"
    "v_pk_add_u16 v185, s89, v185 clamp\n"
    "v_mfma_f32_16x16x32_bf16 v[48:51], a[12:15], v[192:195], v[48:51]\n"
    "v_accvgpr_write_b32 a179, v191\n"
    "v_accvgpr_write_b32 a184, v196\n"
    "v_mfma_f32_16x16x32_bf16 v[64:67], a[36:39], v[192:195], v[64:67]\n"
    "ds_read_b32 v245, v247\n"
    "buffer_load_dwordx4 v1, s[16:19], s67 offen offset:1024 sc1 lds\n"
    "v_mfma_f32_16x16x32_bf16 v[16:19], a[124:127], v[192:195], v[16:19]\n"
    "v_pk_maximum3_f16 v98, v180, v181, v98\n"
    "v_pk_add_u16 v181, s89, v181 clamp\n"
    "v_mfma_f32_16x16x32_bf16 v[48:51], a[8:11], v[188:191], v[48:51]\n"
    "v_pk_add_u16 v184, s89, v184 clamp\n"
    "v_pk_maximum3_f16 v98, v182, v183, v98\n"
    "v_mfma_f32_16x16x32_bf16 v[64:67], a[32:35], v[188:191], v[64:67]\n"
    "v_pk_add_u16 v187, s89, v187 clamp\n"
    "v_pk_add_u16 v182, s89, v182 clamp\n"
    "v_mfma_f32_16x16x32_bf16 v[32:35], a[148:151], v[192:195], v[32:35]\n"
    "buffer_load_dwordx4 v1, s[16:19], s67 offen offset:2048 sc1 lds\n"
    "s_nop 0\n"
    "v_mfma_f32_16x16x32_bf16 v[16:19], a[120:123], v[188:191], v[16:19]\n"
    "s_nop 0\n"
    "v_pk_add_u16 v180, s89, v180 clamp\n"
    "v_accvgpr_write_b32 a173, v185\n"
    "v_mfma_f32_16x16x32_bf16 v[32:35], a[144:147], v[188:191], v[32:35]\n"
    "v_pk_add_u16 v183, s89, v183 clamp\n"
    "v_accvgpr_write_b32 a191, v203\n"
    "v_mfma_f32_16x16x32_bf16 v[32:35], a[140:143], v[184:187], v[32:35]\n"
    "s_waitcnt lgkmcnt(0)\n"
    "v_accvgpr_write_b32 a169, v181\n"
    "v_cmp_le_u32 s[56:57], s59, v245\n"
    "v_smfmac_f32_16x16x64_bf16 v[108:111], v[52:55], v[68:75], v253\n"
    "s_cmp_eq_u64 s[56:57], -1\n"
    "s_cbranch_scc0 .LphKN1\n"
    ".LphKN1r:\n"
    "ds_read_b128 a[48:51], v252 offset:24576\n"
    "v_mfma_f32_16x16x32_bf16 v[48:51], a[4:7], v[184:187], v[48:51]\n"
    "ds_read_b128 a[72:75], v252 offset:25600\n"
    "ds_read_b128 a[88:91], v252 offset:33792\n"
    "v_mfma_f32_16x16x32_bf16 v[16:19], a[116:119], v[184:187], v[16:19]\n"
    "ds_read_b128 a[52:55], v252 offset:26624\n"
    "ds_read_b128 a[76:79], v252 offset:27648\n"
    "v_mfma_f32_16x16x32_bf16 v[16:19], a[112:115], v[180:183], v[16:19]\n"
    "ds_read_b128 a[56:59], v252 offset:28672\n"
    "ds_read_b128 a[64:67], v252 offset:32768\n"
    "v_accvgpr_write_b32 a185, v197\n"
    "v_mfma_f32_16x16x32_bf16 v[48:51], a[0:3], v[180:183], v[48:51]\n"
    "buffer_load_dwordx4 v1, s[16:19], s67 offen offset:3072 sc1 lds\n"
    "v_mfma_f32_16x16x32_bf16 v[64:67], a[28:31], v[184:187], v[64:67]\n"
    "ds_read_b128 a[92:95], v252 offset:35840\n"
    "ds_read_b128 a[60:63], v252 offset:30720\n"
    "v_mfma_f32_16x16x32_bf16 v[32:35], a[136:139], v[180:183], v[32:35]\n"
    "ds_read_b128 a[68:71], v252 offset:34816\n"
    "v_accvgpr_write_b32 a183, v195\n"
    "v_mfma_f32_16x16x32_bf16 v[64:67], a[24:27], v[180:183], v[64:67]\n"
    "s_add_u32 m0, s68, 4096\n"
    "v_accvgpr_write_b32 a171, v183\n"
    "s_waitcnt lgkmcnt(9)\n"
    "v_mfma_f32_16x16x32_bf16 v[4:7], a[48:51], a[192:195], v[100:103]\n"
    "s_waitcnt lgkmcnt(8)\n"
    "buffer_load_dwordx4 v1, s[16:19], s83 offen offset:0 sc1 lds\n"
    "v_mfma_f32_16x16x32_bf16 v[20:23], a[72:75], a[192:195], v[104:107]\n"
    "s_add_u32 s59, s59, 4\n"
    "v_accvgpr_write_b32 a187, v199\n"
    "v_accvgpr_write_b32 a177, v189\n"
    "v_mfma_f32_16x16x32_bf16 v[12:15], a[48:51], a[240:243], v[100:103]\n"
    "s_waitcnt lgkmcnt(5)\n"
    "v_accvgpr_write_b32 a189, v201\n"
    "v_accvgpr_write_b32 a172, v184\n"
    "v_mfma_f32_16x16x32_bf16 v[20:23], a[76:79], a[196:199], v[20:23]\n"
    "v_accvgpr_write_b32 a170, v182\n"
    "v_accvgpr_write_b32 a182, v194\n"
    "v_mfma_f32_16x16x32_bf16 v[4:7], a[52:55], a[196:199], v[4:7]\n"
    "v_accvgpr_write_b32 a180, v192\n"
    "v_accvgpr_write_b32 a188, v200\n"
    "v_mfma_f32_16x16x32_bf16 v[28:31], a[72:75], a[240:243], v[104:107]\n"
    "buffer_load_dwordx4 v1, s[16:19], s83 offen offset:1024 sc1 lds\n"
    "v_mfma_f32_16x16x32_bf16 v[12:15], a[52:55], a[244:247], v[12:15]\n"
    "s_waitcnt lgkmcnt(0)\n"
    "v_exp_f32 v35, v35\n"
    "v_mfma_f32_16x16x32_bf16 v[8:11], a[68:71], a[236:239], v[100:103]\n"
    "s_add_u32 m0, s70, 0\n"
    "v_accvgpr_write_b32 a181, v193\n"
    "v_accvgpr_write_b32 a168, v180\n"
    "v_mfma_f32_16x16x32_bf16 v[24:27], a[92:95], a[236:239], v[104:107]\n"
    "ds_read_b128 a[84:87], v252 offset:31744\n"
    "ds_read_b128 a[80:83], v252 offset:29696\n"
    "v_mfma_f32_16x16x32_bf16 v[12:15], a[56:59], a[248:251], v[12:15]\n"
    "s_add_u32 s83, s67, 24576\n"
    "v_accvgpr_write_b32 a186, v198\n"
    "v_accvgpr_write_b32 a176, v188\n"
    "v_mfma_f32_16x16x32_bf16 v[8:11], a[64:67], a[232:235], v[8:11]\n"
    "v_exp_f32 v17, v17\n"
    "v_mfma_f32_16x16x32_bf16 v[8:11], a[60:63], a[228:231], v[8:11]\n"
    "v_accvgpr_write_b32 a175, v187\n"
    "v_accvgpr_write_b32 a178, v190\n"
    "v_mfma_f32_16x16x32_bf16 v[4:7], a[56:59], a[200:203], v[4:7]\n"
    "buffer_load_dwordx4 v1, s[16:19], s83 offen offset:0 sc1 lds\n"
    "v_mfma_f32_16x16x32_bf16 v[4:7], a[60:63], a[204:207], v[4:7]\n"
    "s_waitcnt vmcnt(23)\n"
    "ds_add_u32 v247, v246\n"
    "v_mfma_f32_16x16x32_bf16 v[12:15], a[60:63], a[252:255], v[12:15]\n"
    "v_exp_f32 v16, v16\n"
    "v_mfma_f32_16x16x32_bf16 v[24:27], a[88:91], a[232:235], v[24:27]\n"
    "v_exp_f32 v33, v33\n"
    "v_mfma_f32_16x16x32_bf16 v[4:7], a[64:67], a[208:211], v[4:7]\n"
    "buffer_load_dwordx4 v1, s[16:19], s83 offen offset:1024 sc1 lds\n"
    "v_mfma_f32_16x16x32_bf16 v[28:31], a[76:79], a[244:247], v[28:31]\n"
    "s_waitcnt lgkmcnt(1)\n"
    "v_exp_f32 v18, v18\n"
    "v_mfma_f32_16x16x32_bf16 v[20:23], a[80:83], a[200:203], v[20:23]\n"
    "v_exp_f32 v34, v34\n"
    "v_mfma_f32_16x16x32_bf16 v[24:27], a[84:87], a[228:231], v[24:27]\n"
    "v_exp_f32 v32, v32\n"
    "v_mfma_f32_16x16x32_bf16 v[20:23], a[84:87], a[204:207], v[20:23]\n"
    "buffer_load_dwordx4 v1, s[16:19], s83 offen offset:2048 sc1 lds\n"
    "v_mfma_f32_16x16x32_bf16 v[20:23], a[88:91], a[208:211], v[20:23]\n"
    "ds_read_b32 v245, v247\n"
    "v_mfma_f32_16x16x32_bf16 v[28:31], a[80:83], a[248:251], v[28:31]\n"
    "v_exp_f32_e64 v48, v48\n"
    "v_mfma_f32_16x16x32_bf16 v[28:31], a[84:87], a[252:255], v[28:31]\n"
    "v_exp_f32 v49, v49\n"
    "v_mfma_f32_16x16x32_bf16 v[12:15], a[64:67], a[160:163], v[12:15]\n"
    "buffer_load_dwordx4 v1, s[16:19], s83 offen offset:3072 sc1 lds\n"
    "v_mfma_f32_16x16x32_bf16 v[8:11], a[56:59], a[224:227], v[8:11]\n"
    "s_add_u32 m0, s70, 4096\n"
    "v_exp_f32 v19, v19\n"
    "v_mfma_f32_16x16x32_bf16 v[8:11], a[52:55], a[220:223], v[8:11]\n"
    "s_add_u32 s83, s67, 28672\n"
    "v_cvt_pk_bf16_f32 v92, v16, v17\n"
    "v_cvt_pk_bf16_f32 v95, v34, v35\n"
    "v_mfma_f32_16x16x32_bf16 v[8:11], a[48:51], a[216:219], v[8:11]\n"
    "v_exp_f32 v66, v66\n"
    "v_mfma_f32_16x16x32_bf16 v[24:27], a[80:83], a[224:227], v[24:27]\n"
    "buffer_load_dwordx4 v1, s[16:19], s83 offen offset:0 sc1 lds\n"
    "v_mfma_f32_16x16x32_bf16 v[28:31], a[88:91], a[160:163], v[28:31]\n"
    "s_waitcnt lgkmcnt(0)\n"
    "v_cvt_pk_bf16_f32 v94, v32, v33\n"
    "v_cmp_le_u32 s[56:57], s59, v245\n"
    "v_mfma_f32_16x16x32_bf16 v[4:7], a[68:71], a[212:215], v[4:7]\n"
    "s_cmp_eq_u64 s[56:57], -1\n"
    "s_cbranch_scc0 .LphKN2\n"
    ".LphKN2r:\n"
    "v_mfma_f32_16x16x32_bf16 v[20:23], a[92:95], a[212:215], v[20:23]\n"
    "ds_read_b128 a[132:135], v249 offset:14336\n"
    "ds_read_b128 a[148:151], v249 offset:16384\n"
    "v_mfma_f32_16x16x32_bf16 v[32:35], a[92:95], a[188:191], v[104:107]\n"
    "buffer_load_dwordx4 v1, s[16:19], s83 offen offset:1024 sc1 lds\n"
    "v_mfma_f32_16x16x32_bf16 v[24:27], a[76:79], a[220:223], v[24:27]\n"
    "ds_read_b128 a[120:123], v249 offset:1024\n"
    "v_mfma_f32_16x16x32_bf16 v[32:35], a[88:91], a[184:187], v[32:35]\n"
    "ds_read_b128 a[116:119], v249 offset:12288\n"
    "v_mfma_f32_16x16x32_bf16 v[28:31], a[92:95], a[164:167], v[28:31]\n"
    "ds_read_b128 a[140:143], v249 offset:15360\n"
    "v_cvt_pk_bf16_f32 v93, v18, v19\n"
    "v_mfma_f32_16x16x32_bf16 v[32:35], a[84:87], a[180:183], v[32:35]\n"
    "ds_read_b128 a[124:127], v249 offset:13312\n"
    "ds_read_b128 a[128:131], v249 offset:2048\n"
    "v_mfma_f32_16x16x32_bf16 v[16:19], a[68:71], a[188:191], v[100:103]\n"
    "v_mfma_f32_16x16x32_bf16 v[32:35], a[80:83], a[176:179], v[32:35]\n"
    "v_cvt_pk_bf16_f32 v96, v48, v49\n"
    "v_mfma_f32_16x16x32_bf16 v[12:15], a[68:71], a[164:167], v[12:15]\n"
    "s_waitcnt lgkmcnt(6)\n"
    "ds_read_b128 a[144:147], v249 offset:4096\n"
    "ds_read_b128 a[112:115], v249 offset:0\n"
    "v_mfma_f32_16x16x32_bf16 v[148:151], a[132:135], v[80:83], 0\n"
    "v_mfma_f32_16x16x32_bf16 v[32:35], a[76:79], a[172:175], v[32:35]\n"
    "ds_read_b128 a[152:155], v249 offset:5120\n"
    "v_mfma_f32_16x16x32_bf16 v[16:19], a[64:67], a[184:187], v[16:19]\n"
    "v_exp_f32 v51, v51\n"
    "v_mfma_f32_16x16x32_bf16 v[16:19], a[60:63], a[180:183], v[16:19]\n"
    "s_waitcnt lgkmcnt(6)\n"
    "v_exp_f32 v65, v65\n"
    "v_mfma_f32_16x16x32_bf16 v[140:143], a[116:119], v[80:83], 0\n"
    "s_waitcnt vmcnt(12)\n"
    "v_exp_f32 v50, v50\n"
    "v_mfma_f32_16x16x32_bf16 v[16:19], a[56:59], a[176:179], v[16:19]\n"
    "s_waitcnt lgkmcnt(4)\n"
    "v_pk_add_u16 v98, s89, v98\n"
    "v_mfma_f32_16x16x32_bf16 v[160:163], a[124:127], v[88:91], 0\n"
    "v_fma_f32 v223, v223, v37, -v39\n"
    "v_and_b32 v98, 0x80008000, v98\n"
    "v_mfma_f32_16x16x32_bf16 v[176:179], a[120:123], v[92:95], 0\n"
    "s_waitcnt lgkmcnt(1)\n"
    "v_exp_f32 v6, v6\n"
    "v_mfma_f32_16x16x32_bf16 v[124:127], a[112:115], v[68:71], 0\n"
    "v_fma_f32 v209, v209, v37, -v39\n"
    "s_add_u32 s59, s59, 4\n"
    "v_mfma_f32_16x16x32_bf16 v[16:19], a[52:55], a[172:175], v[16:19]\n"
    "ds_read_b128 v[100:103], v250 offset:1664\n"
    "v_fma_f32 v226, v226, v36, -v38\n"
    "v_mfma_f32_16x16x32_bf16 v[24:27], a[72:75], a[216:219], v[24:27]\n"
    "s_waitcnt lgkmcnt(1)\n"
    "v_fma_f32 v211, v211, v37, -v39\n"
    "v_fma_f32 v206, v206, v36, -v38\n"
    "v_mfma_f32_16x16x32_bf16 a[20:23], a[152:155], v[84:87], 0\n"
    "v_exp_f32 v5, v5\n"
    "v_mfma_f32_16x16x32_bf16 a[16:19], a[148:151], v[88:91], 0\n"
    "v_fma_f32 v215, v215, v37, -v39\n"
    "v_fma_f32 v227, v227, v37, -v39\n"
    "v_mfma_f32_16x16x32_bf16 v[132:135], a[128:131], v[68:71], 0\n"
    "ds_read_b128 a[136:139], v249 offset:3072\n"
    "v_fma_f32 v232, v232, v36, -v38\n"
    "v_mfma_f32_16x16x32_bf16 v[152:155], a[140:143], v[80:83], 0\n"
    "v_fma_f32 v204, v204, v36, -v38\n"
    "v_mfma_f32_16x16x32_bf16 v[16:19], a[48:51], a[168:171], v[16:19]\n"
    "v_fma_f32 v207, v207, v37, -v39\n"
    "v_mfma_f32_16x16x32_bf16 v[124:127], a[116:119], v[72:75], v[124:127]\n"
    "v_fma_f32 v218, v218, v36, -v38\n"
    "v_fma_f32 v214, v214, v36, -v38\n"
    "v_mfma_f32_16x16x32_bf16 v[156:159], a[112:115], v[84:87], 0\n"
    "v_exp_f32 v67, v67\n"
    "v_mfma_f32_16x16x32_bf16 v[188:191], a[148:151], v[72:75], 0\n"
    "v_fma_f32 v216, v216, v36, -v38\n"
    "v_fma_f32 v205, v205, v37, -v39\n"
    "v_mfma_f32_16x16x32_bf16 v[144:147], a[120:123], v[76:79], 0\n"
    "v_exp_f32 v64, v64\n"
    "v_mfma_f32_16x16x32_bf16 v[160:163], a[120:123], v[84:87], v[160:163]\n"
    "v_fma_f32 v217, v217, v37, -v39\n"
    "v_mfma_f32_16x16x32_bf16 a[0:3], a[144:147], v[76:79], 0\n"
    "v_fma_f32 v212, v212, v36, -v38\n"
    "v_cmp_ne_u32 s[34:35], 0, v98\n"
    "v_mfma_f32_16x16x32_bf16 v[192:195], a[152:155], v[68:71], 0\n"
    "s_waitcnt lgkmcnt(0)\n"
    "v_exp_f32 v26, v26\n"
    "v_mfma_f32_16x16x32_bf16 v[152:155], a[136:139], v[76:79], v[152:155]\n"
    "s_add_u32 s76, s71, s60\n"
    "v_exp_f32 v22, v22\n"
    "v_mfma_f32_16x16x32_bf16 v[164:167], a[128:131], v[84:87], 0\n"
    "ds_read_b128 a[156:159], v249 offset:17408\n"
    "v_mfma_f32_16x16x32_bf16 v[140:143], a[112:115], v[76:79], v[140:143]\n"
    "v_fma_f32 v210, v210, v36, -v38\n"
    "v_mfma_f32_16x16x32_bf16 v[164:167], a[132:135], v[88:91], v[164:167]\n"
    "v_fma_f32 v222, v222, v36, -v38\n"
    "v_fma_f32 v225, v225, v37, -v39\n"
    "v_mfma_f32_16x16x32_bf16 v[128:131], a[124:127], v[72:75], 0\n"
    "v_cvt_pk_bf16_f32 v98, v64, v65\n"
    "v_cvt_pk_bf16_f32 v206, v206, v207\n"
    "v_mfma_f32_16x16x32_bf16 a[32:35], a[144:147], v[92:95], 0\n"
    "v_fma_f32 v228, v228, v36, -v38\n"
    "v_mfma_f32_16x16x32_bf16 v[168:171], a[136:139], v[84:87], 0\n"
    "v_cvt_pk_bf16_f32 v99, v66, v67\n"
    "v_mov_b32 v2, v3\n"
    "v_mfma_f32_16x16x32_bf16 v[188:191], a[144:147], v[68:71], v[188:191]\n"
    "v_fma_f32 v221, v221, v37, -v39\n"
    "v_cvt_pk_bf16_f32 v97, v50, v51\n"
    "v_mfma_f32_16x16x32_bf16 a[16:19], a[144:147], v[84:87], a[16:19]\n"
    "v_fma_f32 v208, v208, v36, -v38\n"
    "v_cvt_pk_bf16_f32 v204, v204, v205\n"
    "v_mfma_f32_16x16x32_bf16 v[128:131], a[120:123], v[68:71], v[128:131]\n"
    "ds_read_b128 a[144:147], v249 offset:10240\n"
    "v_fma_f32 v235, v235, v37, -v39\n"
    "v_mfma_f32_16x16x32_bf16 v[172:175], a[116:119], v[96:99], 0\n"
    "s_add_u32 s77, s71, s61\n"
    "v_fma_f32 v213, v213, v37, -v39\n"
    "v_add_u32 v251, 1536, v251\n"
    "v_mfma_f32_16x16x32_bf16 a[32:35], a[148:151], v[96:99], a[32:35]\n"
    "v_exp_f32 v23, v23\n"
    "v_mfma_f32_16x16x32_bf16 v[136:139], a[136:139], v[68:71], 0\n"
    "v_cvt_pk_bf16_f32 v210, v210, v211\n"
    "v_cvt_pk_bf16_f32 v226, v226, v227\n"
    "v_mfma_f32_16x16x32_bf16 v[148:151], a[128:131], v[76:79], v[148:151]\n"
    "v_exp_f32 v19, v19\n"
    "v_mfma_f32_16x16x32_bf16 v[132:135], a[132:135], v[72:75], v[132:135]\n"
    "s_waitcnt lgkmcnt(1)\n"
    "v_fma_f32 v229, v229, v37, -v39\n"
    "v_mfma_f32_16x16x32_bf16 v[192:195], a[156:159], v[72:75], v[192:195]\n"
    "v_mfma_f32_16x16x32_bf16 v[168:171], a[140:143], v[88:91], v[168:171]\n"
    "v_exp_f32 v9, v9\n"
    "v_mfma_f32_16x16x32_bf16 v[156:159], a[116:119], v[88:91], v[156:159]\n"
    "buffer_store_dword v204, v244, s[16:19], s76 offen offset:0 sc1\n"
    "v_exp_f32 v21, v21\n"
    "v_mfma_f32_16x16x32_bf16 v[172:175], a[112:115], v[92:95], v[172:175]\n"
    "v_cvt_pk_bf16_f32 v208, v208, v209\n"
    "v_mfma_f32_16x16x32_bf16 a[0:3], a[148:151], v[80:83], a[0:3]\n"
    "ds_read_b128 a[116:119], v249 offset:18432\n"
    "ds_read_b128 a[112:115], v249 offset:6144\n"
    "v_mfma_f32_16x16x32_bf16 v[32:35], a[72:75], a[168:171], v[32:35]\n"
    "buffer_store_dword v206, v244, s[16:19], s76 offen offset:128 sc1\n"
    "v_fma_f32 v233, v233, v37, -v39\n"
    "v_cvt_pk_bf16_f32 v214, v214, v215\n"
    "v_mfma_f32_16x16x32_bf16 v[184:187], a[140:143], v[96:99], 0\n"
    "v_cvt_pk_bf16_f32 v216, v216, v217\n"
    "v_fma_f32 v230, v230, v36, -v38\n"
    "v_mfma_f32_16x16x32_bf16 v[180:183], a[132:135], v[96:99], 0\n"
    "buffer_store_dword v208, v244, s[16:19], s76 offen offset:1024 sc1\n"
    "v_cvt_pk_bf16_f32 v212, v212, v213\n"
    "v_mfma_f32_16x16x32_bf16 v[176:179], a[124:127], v[96:99], v[176:179]\n"
    "v_exp_f32 v28, v28\n"
    "v_mfma_f32_16x16x32_bf16 a[36:39], a[156:159], v[96:99], 0\n"
    "buffer_store_dword v210, v244, s[16:19], s76 offen offset:1152 sc1\n"
    "v_cvt_pk_bf16_f32 v222, v222, v223\n"
    "v_fma_f32 v220, v220, v36, -v38\n"
    "v_mfma_f32_16x16x32_bf16 a[4:7], a[156:159], v[80:83], 0\n"
    "v_exp_f32 v8, v8\n"
    "v_mfma_f32_16x16x32_bf16 v[180:183], a[128:131], v[92:95], v[180:183]\n"
    "buffer_store_dword v212, v244, s[16:19], s76 offen offset:256 sc1\n"
    "v_fma_f32 v219, v219, v37, -v39\n"
    "v_cvt_pk_bf16_f32 v220, v220, v221\n"
    "v_mfma_f32_16x16x32_bf16 v[136:139], a[140:143], v[72:75], v[136:139]\n"
    "v_mfma_f32_16x16x32_bf16 a[20:23], a[156:159], v[88:91], a[20:23]\n"
    "v_exp_f32 v34, v34\n"
    "v_mfma_f32_16x16x32_bf16 v[184:187], a[136:139], v[92:95], v[184:187]\n"
    "buffer_store_dword v214, v244, s[16:19], s76 offen offset:384 sc1\n"
    "ds_read_b128 a[132:135], v249 offset:20480\n"
    "ds_read_b128 a[156:159], v249 offset:23552\n"
    "v_mfma_f32_16x16x32_bf16 v[144:147], a[124:127], v[80:83], v[144:147]\n"
    "s_waitcnt lgkmcnt(2)\n"
    "ds_read_b128 a[124:127], v249 offset:19456\n"
    "ds_read_b128 a[148:151], v249 offset:22528\n"
    "v_mfma_f32_16x16x32_bf16 a[24:27], a[112:115], v[84:87], 0\n"
    "ds_read_b128 a[128:131], v249 offset:8192\n"
    "v_mfma_f32_16x16x32_bf16 a[104:107], a[144:147], v[92:95], 0\n"
    "v_exp_f32 v30, v30\n"
    "v_smfmac_f32_16x16x64_bf16 v[120:123], v[52:55], v[92:99], v253\n"
    "v_add_u32 v250, 1536, v250\n"
    "v_mfma_f32_16x16x32_bf16 a[24:27], a[116:119], v[88:91], a[24:27]\n"
    "v_exp_f32 v29, v29\n"
    "v_mfma_f32_16x16x32_bf16 v[196:199], a[112:115], v[68:71], 0\n"
    "ds_read_b128 a[120:123], v249 offset:7168\n"
    "v_mfma_f32_16x16x32_bf16 a[72:75], a[144:147], v[76:79], 0\n"
    "v_mfma_f32_16x16x32_bf16 v[196:199], a[116:119], v[72:75], v[196:199]\n"
    "v_mfma_f32_16x16x32_bf16 a[8:11], a[116:119], v[80:83], 0\n"
    "v_exp_f32 v7, v7\n"
    "v_mfma_f32_16x16x32_bf16 a[4:7], a[152:155], v[76:79], a[4:7]\n"
    "s_waitcnt lgkmcnt(1)\n"
    "buffer_load_dwordx2 v[236:237], v241, s[36:39], s81 offen offset:0 sc1\n"
    "v_mfma_f32_16x16x32_bf16 a[80:83], a[128:131], v[84:87], 0\n"
    "v_exp_f32 v17, v17\n"
    "v_mfma_f32_16x16x32_bf16 a[8:11], a[112:115], v[76:79], a[8:11]\n"
    "s_add_u32 s67, s67, 49152\n"
    "buffer_store_dword v216, v244, s[16:19], s76 offen offset:1280 sc1\n"
    "ds_read_b128 a[140:143], v249 offset:21504\n"
    "v_mfma_f32_16x16x32_bf16 a[28:31], a[124:127], v[88:91], 0\n"
    "v_exp_f32 v24, v24\n"
    "v_mfma_f32_16x16x32_bf16 a[72:75], a[148:151], v[80:83], a[72:75]\n"
    "v_exp_f32 v14, v14\n"
    "v_mfma_f32_16x16x32_bf16 v[200:203], a[124:127], v[72:75], 0\n"
    "v_mfma_f32_16x16x32_bf16 a[36:39], a[152:155], v[92:95], a[36:39]\n"
    "v_cvt_pk_bf16_f32 v218, v218, v219\n"
    "v_fma_f32 v234, v234, v36, -v38\n"
    "v_mfma_f32_16x16x32_bf16 a[56:59], a[148:151], v[72:75], 0\n"
    "v_exp_f32 v13, v13\n"
    "v_mfma_f32_16x16x32_bf16 a[108:111], a[156:159], v[96:99], 0\n"
    "v_exp_f32 v11, v11\n"
    "v_mfma_f32_16x16x32_bf16 a[48:51], a[128:131], v[68:71], 0\n"
    "v_exp_f32 v33, v33\n"
    "v_mfma_f32_16x16x32_bf16 a[48:51], a[132:135], v[72:75], a[48:51]\n"
    "v_exp_f32 v10, v10\n"
    "v_mfma_f32_16x16x32_bf16 a[80:83], a[132:135], v[88:91], a[80:83]\n"
    "v_fma_f32 v231, v231, v37, -v39\n"
    "v_fma_f32 v224, v224, v36, -v38\n"
    "v_mfma_f32_16x16x32_bf16 a[40:43], a[116:119], v[96:99], 0\n"
    "s_waitcnt lgkmcnt(0)\n"
    "v_cvt_pk_bf16_f32 v234, v234, v235\n"
    "v_mfma_f32_16x16x32_bf16 a[100:103], a[140:143], v[96:99], 0\n"
    "buffer_store_dword v218, v244, s[16:19], s76 offen offset:1408 sc1\n"
    "v_exp_f32 v18, v18\n"
    "v_mfma_f32_16x16x32_bf16 a[104:107], a[148:151], v[96:99], a[104:107]\n"
    "v_exp_f32 v20, v20\n"
    "v_mfma_f32_16x16x32_bf16 a[56:59], a[144:147], v[68:71], a[56:59]\n"
    "ds_read_b128 a[136:139], v249 offset:9216\n"
    "v_mfma_f32_16x16x32_bf16 a[12:15], a[120:123], v[76:79], 0\n"
    "buffer_store_dword v220, v244, s[16:19], s77 offen offset:0 sc1\n"
    "ds_read_b128 a[152:155], v249 offset:11264\n"
    "v_cvt_pk_bf16_f32 v230, v230, v231\n"
    "v_mfma_f32_16x16x32_bf16 a[64:67], a[132:135], v[80:83], 0\n"
    "v_exp_f32 v27, v27\n"
    "v_mfma_f32_16x16x32_bf16 v[200:203], a[120:123], v[68:71], v[200:203]\n"
    "v_exp_f32 v4, v4\n"
    "v_mfma_f32_16x16x32_bf16 a[64:67], a[128:131], v[76:79], a[64:67]\n"
    "v_cvt_pk_bf16_f32 v224, v224, v225\n"
    "v_cvt_pk_bf16_f32 v228, v228, v229\n"
    "v_mfma_f32_16x16x32_bf16 a[44:47], a[120:123], v[92:95], 0\n"
    "v_exp_f32 v16, v16\n"
    "v_mfma_f32_16x16x32_bf16 a[88:91], a[148:151], v[88:91], 0\n"
    "buffer_store_dword v222, v244, s[16:19], s77 offen offset:128 sc1\n"
    "v_mfma_f32_16x16x32_bf16 a[28:31], a[120:123], v[84:87], a[28:31]\n"
    "v_exp_f32 v31, v31\n"
    "v_mfma_f32_16x16x32_bf16 a[96:99], a[132:135], v[96:99], 0\n"
    "s_waitcnt lgkmcnt(1)\n"
    "ds_read_b128 a[120:123], v3 offset:16384\n"
    "v_mfma_f32_16x16x32_bf16 a[100:103], a[136:139], v[92:95], a[100:103]\n"
    "v_exp_f32 v12, v12\n"
    "v_mfma_f32_16x16x32_bf16 a[52:55], a[136:139], v[68:71], 0\n"
    "v_exp_f32 v25, v25\n"
    "v_mfma_f32_16x16x32_bf16 a[44:47], a[124:127], v[96:99], a[44:47]\n"
    "v_mfma_f32_16x16x32_bf16 a[40:43], a[112:115], v[92:95], a[40:43]\n"
    "s_waitcnt lgkmcnt(1)\n"
    "v_exp_f32 v15, v15\n"
    "v_mfma_f32_16x16x32_bf16 a[92:95], a[152:155], v[84:87], 0\n"
    "v_add_u32 v243, 512, v243\n"
    "v_mfma_f32_16x16x32_bf16 a[12:15], a[124:127], v[80:83], a[12:15]\n"
    "buffer_store_dword v224, v244, s[16:19], s77 offen offset:1024 sc1\n"
    "ds_read_b128 a[132:135], v3 offset:22528\n"
    "ds_read_b128 a[116:119], v3 offset:14336\n"
    "v_mfma_f32_16x16x32_bf16 a[96:99], a[128:131], v[92:95], a[96:99]\n"
    "v_exp_f32 v35, v35\n"
    "v_mfma_f32_16x16x32_bf16 a[60:63], a[152:155], v[68:71], 0\n"
    "ds_read_b128 a[112:115], v3 offset:12288\n"
    "v_cvt_pk_bf16_f32 v232, v232, v233\n"
    "v_mfma_f32_16x16x32_bf16 a[68:71], a[140:143], v[80:83], 0\n"
    "s_waitcnt vmcnt(12)\n"
    "buffer_store_dword v226, v244, s[16:19], s77 offen offset:1152 sc1\n"
    "ds_add_u32 v247, v246\n"
    "v_mfma_f32_16x16x32_bf16 a[84:87], a[136:139], v[84:87], 0\n"
    "ds_read_b128 a[124:127], v3 offset:18432\n"
    "v_mfma_f32_16x16x32_bf16 a[88:91], a[144:147], v[84:87], a[88:91]\n"
    "ds_read_b128 a[128:131], v3 offset:20480\n"
    "v_mfma_f32_16x16x32_bf16 a[84:87], a[140:143], v[88:91], a[84:87]\n"
    "buffer_store_dword v228, v244, s[16:19], s77 offen offset:256 sc1\n"
    "v_mfma_f32_16x16x32_bf16 a[76:79], a[156:159], v[80:83], 0\n"
    "v_mfma_f32_16x16x32_bf16 a[108:111], a[152:155], v[92:95], a[108:111]\n"
    "buffer_store_dword v230, v244, s[16:19], s77 offen offset:384 sc1\n"
    "v_exp_f32 v32, v32\n"
    "v_mfma_f32_16x16x32_bf16 a[92:95], a[156:159], v[88:91], a[92:95]\n"
    "v_mfma_f32_16x16x32_bf16 a[60:63], a[156:159], v[72:75], a[60:63]\n"
    "buffer_store_dword v232, v244, s[16:19], s77 offen offset:1280 sc1\n"
    "v_mfma_f32_16x16x32_bf16 a[68:71], a[136:139], v[76:79], a[68:71]\n"
    "v_add_u32 v3, 49152, v252\n"
    "v_mfma_f32_16x16x32_bf16 a[76:79], a[152:155], v[76:79], a[76:79]\n"
    "s_waitcnt lgkmcnt(0)\n"
    "buffer_store_dword v234, v244, s[16:19], s77 offen offset:1408 sc1\n"
    "v_mfma_f32_16x16x32_bf16 a[52:55], a[140:143], v[72:75], a[52:55]\n"
    "s_branch .LPK\n"
    ".fill ((0x20000 - (. - _Z8attn_fwdPKtPKiS2_S0_S0_S0_S0_PcPt)) & 4095) / 4, 4, 0xBF800000\n"
    ".LhVP:\n"
    "s_waitcnt vmcnt(12)\n"
    "ds_read_b128 v[200:203], v97 offset:49408\n"
    "ds_read_b128 v[196:199], v96 offset:49408\n"
    "ds_read_b128 v[188:191], v96 offset:49280\n"
    "ds_read_b128 v[192:195], v97 offset:49280\n"
    "ds_read_b128 v[184:187], v97 offset:49152\n"
    "s_add_u32 m0, s68, 0\n"
    "ds_read_b128 v[52:55], v251 offset:0\n"
    "ds_read_b128 v[180:183], v96 offset:49152\n"
    "s_waitcnt lgkmcnt(6)\n"
    "v_pk_maximum3_f16 v98, v200, v201, v98\n"
    "v_pk_add_u16 v200, s89, v200 clamp\n"
    "v_pk_maximum3_f16 v98, v202, v203, v98\n"
    "v_pk_add_u16 v201, s89, v201 clamp\n"
    "v_pk_add_u16 v202, s89, v202 clamp\n"
    "v_pk_add_u16 v203, s89, v203 clamp\n"
    "s_waitcnt lgkmcnt(5)\n"
    "v_pk_maximum3_f16 v98, v196, v197, v98\n"
    "v_pk_maximum3_f16 v98, v198, v199, v98\n"
    "v_mfma_f32_16x16x32_bf16 v[48:51], a[20:23], v[200:203], v[48:51]\n"
    "s_waitcnt lgkmcnt(3)\n"
    "v_pk_maximum3_f16 v98, v192, v193, v98\n"
    "v_pk_maximum3_f16 v98, v194, v195, v98\n"
    "v_mfma_f32_16x16x32_bf16 v[32:35], a[156:159], v[200:203], v[32:35]\n"
    "s_nop 0\n"
    "v_pk_add_u16 v199, s89, v199 clamp\n"
    "v_pk_add_u16 v196, s89, v196 clamp\n"
    "v_mfma_f32_16x16x32_bf16 v[16:19], a[132:135], v[200:203], v[16:19]\n"
    "s_add_u32 s83, s67, 4096\n"
    "v_pk_add_u16 v195, s89, v195 clamp\n"
    "v_pk_maximum3_f16 v98, v188, v189, v98\n"
    "v_mfma_f32_16x16x32_bf16 v[64:67], a[44:47], v[200:203], v[64:67]\n"
    "buffer_load_dwordx4 v1, s[16:19], s67 offen offset:0 sc1 lds\n"
    "v_pk_add_u16 v197, s89, v197 clamp\n"
    "v_pk_add_u16 v198, s89, v198 clamp\n"
    "v_pk_maximum3_f16 v98, v190, v191, v98\n"
    "v_pk_add_u16 v194, s89, v194 clamp\n"
    "v_pk_add_u16 v193, s89, v193 clamp\n"
    "v_mfma_f32_16x16x32_bf16 v[32:35], a[152:155], v[196:199], v[32:35]\n"
    "s_waitcnt vmcnt(7)\n"
    "ds_add_u32 v247, v246\n"
    "v_mfma_f32_16x16x32_bf16 v[48:51], a[16:19], v[196:199], v[48:51]\n"
    "s_nop 0\n"
    "v_pk_add_u16 v188, s89, v188 clamp\n"
    "v_pk_add_u16 v192, s89, v192 clamp\n"
    "v_mfma_f32_16x16x32_bf16 v[16:19], a[128:131], v[196:199], v[16:19]\n"
    "buffer_load_dwordx4 v1, s[16:19], s67 offen offset:1024 sc1 lds\n"
    "v_mfma_f32_16x16x32_bf16 v[64:67], a[40:43], v[196:199], v[64:67]\n"
    "s_waitcnt lgkmcnt(3)\n"
    "v_pk_maximum3_f16 v98, v184, v185, v98\n"
    "v_pk_maximum3_f16 v98, v186, v187, v98\n"
    "v_mfma_f32_16x16x32_bf16 v[48:51], a[12:15], v[192:195], v[48:51]\n"
    "s_waitcnt lgkmcnt(1)\n"
    "v_pk_maximum3_f16 v98, v180, v181, v98\n"
    "v_accvgpr_write_b32 a185, v197\n"
    "v_mfma_f32_16x16x32_bf16 v[64:67], a[36:39], v[192:195], v[64:67]\n"
    "v_accvgpr_write_b32 a180, v192\n"
    "v_pk_add_u16 v190, s89, v190 clamp\n"
    "v_smfmac_f32_16x16x64_bf16 v[112:115], v[52:55], v[76:83], v253\n"
    "buffer_load_dwordx4 v1, s[16:19], s67 offen offset:2048 sc1 lds\n"
    "v_pk_maximum3_f16 v98, v182, v183, v98\n"
    "v_mfma_f32_16x16x32_bf16 v[16:19], a[124:127], v[192:195], v[16:19]\n"
    "ds_read_b32 v245, v247\n"
    "v_pk_add_u16 v191, s89, v191 clamp\n"
    "v_pk_add_u16 v189, s89, v189 clamp\n"
    "v_pk_add_u16 v184, s89, v184 clamp\n"
    "v_mfma_f32_16x16x32_bf16 v[32:35], a[148:151], v[192:195], v[32:35]\n"
    "v_pk_add_u16 v186, s89, v186 clamp\n"
    "v_pk_add_u16 v187, s89, v187 clamp\n"
    "v_mfma_f32_16x16x32_bf16 v[48:51], a[8:11], v[188:191], v[48:51]\n"
    "v_accvgpr_write_b32 a181, v193\n"
    "v_accvgpr_write_b32 a191, v203\n"
    "v_mfma_f32_16x16x32_bf16 v[16:19], a[120:123], v[188:191], v[16:19]\n"
    "v_pk_add_u16 v182, s89, v182 clamp\n"
    "v_pk_add_u16 v180, s89, v180 clamp\n"
    "v_accvgpr_write_b32 a174, v186\n"
    "v_mfma_f32_16x16x32_bf16 v[32:35], a[144:147], v[188:191], v[32:35]\n"
    "v_pk_add_u16 v185, s89, v185 clamp\n"
    "v_pk_add_u16 v183, s89, v183 clamp\n"
    "v_pk_add_u16 v181, s89, v181 clamp\n"
    "v_accvgpr_write_b32 a186, v198\n"
    "v_mfma_f32_16x16x32_bf16 v[32:35], a[140:143], v[184:187], v[32:35]\n"
    "v_accvgpr_write_b32 a184, v196\n"
    "v_accvgpr_write_b32 a177, v189\n"
    "v_mfma_f32_16x16x32_bf16 v[64:67], a[32:35], v[188:191], v[64:67]\n"
    "s_waitcnt lgkmcnt(0)\n"
    "v_accvgpr_write_b32 a189, v201\n"
    "v_cmp_le_u32 s[56:57], s59, v245\n"
    "v_mfma_f32_16x16x32_bf16 v[32:35], a[136:139], v[180:183], v[32:35]\n"
    "s_cmp_eq_u64 s[56:57], -1\n"
    "s_cbranch_scc0 .LphVP1\n"
    ".LphVP1r:\n"
    "ds_read_b128 a[48:51], v252 offset:24576\n"
    "v_mfma_f32_16x16x32_bf16 v[48:51], a[4:7], v[184:187], v[48:51]\n"
    "ds_read_b128 a[56:59], v252 offset:28672\n"
    "ds_read_b128 a[52:55], v252 offset:26624\n"
    "v_mfma_f32_16x16x32_bf16 v[16:19], a[116:119], v[184:187], v[16:19]\n"
    "buffer_load_dwordx4 v1, s[16:19], s67 offen offset:3072 sc1 lds\n"
    "v_mfma_f32_16x16x32_bf16 v[64:67], a[28:31], v[184:187], v[64:67]\n"
    "ds_read_b128 a[72:75], v252 offset:25600\n"
    "ds_read_b128 a[68:71], v252 offset:34816\n"
    "ds_read_b128 a[80:83], v252 offset:29696\n"
    "v_mfma_f32_16x16x32_bf16 v[64:67], a[24:27], v[180:183], v[64:67]\n"
    "ds_read_b128 a[76:79], v252 offset:27648\n"
    "ds_read_b128 a[92:95], v252 offset:35840\n"
    "v_mfma_f32_16x16x32_bf16 v[16:19], a[112:115], v[180:183], v[16:19]\n"
    "v_accvgpr_write_b32 a178, v190\n"
    "v_accvgpr_write_b32 a170, v182\n"
    "v_mfma_f32_16x16x32_bf16 v[48:51], a[0:3], v[180:183], v[48:51]\n"
    "v_accvgpr_write_b32 a182, v194\n"
    "v_accvgpr_write_b32 a187, v199\n"
    "v_smfmac_f32_16x16x64_bf16 v[108:111], v[52:55], v[68:75], v253\n"
    "s_add_u32 m0, s68, 4096\n"
    "ds_read_b128 a[64:67], v252 offset:32768\n"
    "s_waitcnt lgkmcnt(8)\n"
    "v_mfma_f32_16x16x32_bf16 v[4:7], a[48:51], a[192:195], v[100:103]\n"
    "buffer_load_dwordx4 v1, s[16:19], s83 offen offset:0 sc1 lds\n"
    "v_mfma_f32_16x16x32_bf16 v[12:15], a[48:51], a[240:243], v[100:103]\n"
    "s_waitcnt lgkmcnt(6)\n"
    "v_exp_f32 v18, v18\n"
    "v_mfma_f32_16x16x32_bf16 v[4:7], a[52:55], a[196:199], v[4:7]\n"
    "s_add_u32 s59, s59, 4\n"
    "ds_read_b128 a[60:63], v252 offset:30720\n"
    "v_accvgpr_write_b32 a173, v185\n"
    "v_mfma_f32_16x16x32_bf16 v[12:15], a[52:55], a[244:247], v[12:15]\n"
    "s_waitcnt lgkmcnt(6)\n"
    "v_exp_f32 v19, v19\n"
    "v_mfma_f32_16x16x32_bf16 v[28:31], a[72:75], a[240:243], v[104:107]\n"
    "s_waitcnt lgkmcnt(5)\n"
    "buffer_load_dwordx4 v1, s[16:19], s83 offen offset:1024 sc1 lds\n"
    "v_mfma_f32_16x16x32_bf16 v[8:11], a[68:71], a[236:239], v[100:103]\n"
    "ds_read_b128 a[84:87], v252 offset:31744\n"
    "v_accvgpr_write_b32 a168, v180\n"
    "v_mfma_f32_16x16x32_bf16 v[4:7], a[56:59], a[200:203], v[4:7]\n"
    "s_waitcnt lgkmcnt(3)\n"
    "v_accvgpr_write_b32 a179, v191\n"
    "v_accvgpr_write_b32 a188, v200\n"
    "v_mfma_f32_16x16x32_bf16 v[28:31], a[76:79], a[244:247], v[28:31]\n"
    "s_add_u32 m0, s64, 0\n"
    "v_accvgpr_write_b32 a183, v195\n"
    "v_accvgpr_write_b32 a190, v202\n"
    "v_mfma_f32_16x16x32_bf16 v[20:23], a[72:75], a[192:195], v[104:107]\n"
    "s_add_u32 s83, s67, 24576\n"
    "ds_read_b128 a[88:91], v252 offset:33792\n"
    "v_accvgpr_write_b32 a171, v183\n"
    "v_mfma_f32_16x16x32_bf16 v[24:27], a[92:95], a[236:239], v[104:107]\n"
    "v_accvgpr_write_b32 a169, v181\n"
    "v_accvgpr_write_b32 a176, v188\n"
    "v_mfma_f32_16x16x32_bf16 v[20:23], a[76:79], a[196:199], v[20:23]\n"
    "s_waitcnt lgkmcnt(2)\n"
    "v_accvgpr_write_b32 a175, v187\n"
    "v_accvgpr_write_b32 a172, v184\n"
    "v_mfma_f32_16x16x32_bf16 v[4:7], a[60:63], a[204:207], v[4:7]\n"
    "buffer_load_dwordx4 v1, s[16:19], s83 offen offset:0 sc1 lds\n"
    "v_mfma_f32_16x16x32_bf16 v[8:11], a[64:67], a[232:235], v[8:11]\n"
    "v_exp_f32 v50, v50\n"
    "v_mfma_f32_16x16x32_bf16 v[20:23], a[80:83], a[200:203], v[20:23]\n"
    "v_exp_f32 v16, v16\n"
    "v_mfma_f32_16x16x32_bf16 v[8:11], a[60:63], a[228:231], v[8:11]\n"
    "s_waitcnt vmcnt(7)\n"
    "ds_add_u32 v247, v246\n"
    "v_mfma_f32_16x16x32_bf16 v[12:15], a[56:59], a[248:251], v[12:15]\n"
    "buffer_load_dwordx4 v1, s[16:19], s83 offen offset:1024 sc1 lds\n"
    "v_mfma_f32_16x16x32_bf16 v[8:11], a[56:59], a[224:227], v[8:11]\n"
    "v_exp_f32 v17, v17\n"
    "v_mfma_f32_16x16x32_bf16 v[12:15], a[60:63], a[252:255], v[12:15]\n"
    "s_waitcnt lgkmcnt(2)\n"
    "v_exp_f32 v65, v65\n"
    "v_mfma_f32_16x16x32_bf16 v[20:23], a[84:87], a[204:207], v[20:23]\n"
    "v_cvt_pk_bf16_f32 v92, v16, v17\n"
    "v_cvt_pk_bf16_f32 v93, v18, v19\n"
    "v_mfma_f32_16x16x32_bf16 v[16:19], a[68:71], a[188:191], v[100:103]\n"
    "buffer_load_dwordx4 v1, s[16:19], s83 offen offset:2048 sc1 lds\n"
    "v_mfma_f32_16x16x32_bf16 v[28:31], a[80:83], a[248:251], v[28:31]\n"
    "v_exp_f32 v32, v32\n"
    "v_mfma_f32_16x16x32_bf16 v[28:31], a[84:87], a[252:255], v[28:31]\n"
    "v_exp_f32 v33, v33\n"
    "v_mfma_f32_16x16x32_bf16 v[12:15], a[64:67], a[160:163], v[12:15]\n"
    "ds_read_b32 v245, v247\n"
    "v_mfma_f32_16x16x32_bf16 v[16:19], a[64:67], a[184:187], v[16:19]\n"
    "s_waitcnt lgkmcnt(2)\n"
    "buffer_load_dwordx4 v1, s[16:19], s83 offen offset:3072 sc1 lds\n"
    "v_mfma_f32_16x16x32_bf16 v[20:23], a[88:91], a[208:211], v[20:23]\n"
    "v_exp_f32 v51, v51\n"
    "v_mfma_f32_16x16x32_bf16 v[16:19], a[60:63], a[180:183], v[16:19]\n"
    "v_exp_f32 v34, v34\n"
    "v_mfma_f32_16x16x32_bf16 v[16:19], a[56:59], a[176:179], v[16:19]\n"
    "s_add_u32 m0, s64, 4096\n"
    "v_pk_add_u16 v98, s89, v98\n"
    "s_add_u32 s83, s67, 28672\n"
    "v_mfma_f32_16x16x32_bf16 v[16:19], a[52:55], a[172:175], v[16:19]\n"
    "buffer_load_dwordx4 v1, s[16:19], s83 offen offset:0 sc1 lds\n"
    "v_mfma_f32_16x16x32_bf16 v[12:15], a[68:71], a[164:167], v[12:15]\n"
    "v_mfma_f32_16x16x32_bf16 v[28:31], a[88:91], a[160:163], v[28:31]\n"
    "v_exp_f32_e64 v35, v35\n"
    "v_mfma_f32_16x16x32_bf16 v[4:7], a[64:67], a[208:211], v[4:7]\n"
    "s_waitcnt lgkmcnt(0)\n"
    "v_cvt_pk_bf16_f32 v94, v32, v33\n"
    "v_cmp_le_u32 s[56:57], s59, v245\n"
    "v_mfma_f32_16x16x32_bf16 v[28:31], a[92:95], a[164:167], v[28:31]\n"
    "buffer_load_dwordx4 v1, s[16:19], s83 offen offset:1024 sc1 lds\n"
    "v_mfma_f32_16x16x32_bf16 v[16:19], a[48:51], a[168:171], v[16:19]\n"
    "s_cmp_eq_u64 s[56:57], -1\n"
    "s_cbranch_scc0 .LphVP2\n"
    ".LphVP2r:\n"
    "v_mfma_f32_16x16x32_bf16 v[24:27], a[88:91], a[232:235], v[24:27]\n"
    "ds_read_b128 a[112:115], v248 offset:0\n"
    "v_and_b32 v98, 0x80008000, v98\n"
    "v_mfma_f32_16x16x32_bf16 v[8:11], a[52:55], a[220:223], v[8:11]\n"
    "ds_read_b128 a[116:119], v248 offset:12288\n"
    "v_cvt_pk_bf16_f32 v95, v34, v35\n"
    "v_mfma_f32_16x16x32_bf16 v[32:35], a[92:95], a[188:191], v[104:107]\n"
    "ds_read_b128 a[132:135], v248 offset:14336\n"
    "ds_read_b128 a[152:155], v248 offset:5120\n"
    "v_mfma_f32_16x16x32_bf16 v[8:11], a[48:51], a[216:219], v[8:11]\n"
    "ds_read_b128 a[120:123], v248 offset:1024\n"
    "v_cmp_ne_u32 s[34:35], 0, v98\n"
    "v_mfma_f32_16x16x32_bf16 v[32:35], a[88:91], a[184:187], v[32:35]\n"
    "ds_read_b128 a[156:159], v248 offset:17408\n"
    "ds_read_b128 a[128:131], v248 offset:2048\n"
    "v_mfma_f32_16x16x32_bf16 v[4:7], a[68:71], a[212:215], v[4:7]\n"
    "v_exp_f32 v14, v14\n"
    "v_mfma_f32_16x16x32_bf16 v[24:27], a[84:87], a[228:231], v[24:27]\n"
    "ds_read_b128 a[124:127], v248 offset:13312\n"
    "v_mfma_f32_16x16x32_bf16 v[32:35], a[84:87], a[180:183], v[32:35]\n"
    "buffer_load_dwordx4 v[204:207], v242, s[40:43], s75 offen offset:0 nt\n"
    "v_mfma_f32_16x16x32_bf16 v[24:27], a[80:83], a[224:227], v[24:27]\n"
    "v_mfma_f32_16x16x32_bf16 v[20:23], a[92:95], a[212:215], v[20:23]\n"
    "s_waitcnt lgkmcnt(7)\n"
    "ds_read_b128 a[148:151], v248 offset:16384\n"
    "v_mfma_f32_16x16x32_bf16 v[124:127], a[112:115], v[68:71], 0\n"
    "s_waitcnt lgkmcnt(7)\n"
    "buffer_load_dwordx4 v[208:211], v242, s[40:43], s75 offen offset:768 nt\n"
    "v_mfma_f32_16x16x32_bf16 v[124:127], a[116:119], v[72:75], v[124:127]\n"
    "s_waitcnt lgkmcnt(3)\n"
    "v_mfma_f32_16x16x32_bf16 a[4:7], a[156:159], v[80:83], 0\n"
    "v_exp_f32 v49, v49\n"
    "v_mfma_f32_16x16x32_bf16 a[20:23], a[152:155], v[84:87], 0\n"
    "buffer_load_dwordx4 v[212:215], v242, s[40:43], s75 offen offset:256 nt\n"
    "v_mfma_f32_16x16x32_bf16 v[176:179], a[120:123], v[92:95], 0\n"
    "s_waitcnt lgkmcnt(1)\n"
    "v_exp_f32 v48, v48\n"
    "v_mfma_f32_16x16x32_bf16 v[128:131], a[124:127], v[72:75], 0\n"
    "v_exp_f32 v64, v64\n"
    "v_mfma_f32_16x16x32_bf16 a[20:23], a[156:159], v[88:91], a[20:23]\n"
    "ds_read_b128 a[136:139], v248 offset:3072\n"
    "ds_read_b128 a[144:147], v248 offset:4096\n"
    "v_mfma_f32_16x16x32_bf16 v[32:35], a[80:83], a[176:179], v[32:35]\n"
    "buffer_load_dwordx4 v[216:219], v242, s[40:43], s75 offen offset:1024 nt\n"
    "v_mfma_f32_16x16x32_bf16 v[160:163], a[124:127], v[88:91], 0\n"
    "v_mfma_f32_16x16x32_bf16 v[164:167], a[128:131], v[84:87], 0\n"
    "v_mfma_f32_16x16x32_bf16 v[132:135], a[128:131], v[68:71], 0\n"
    "v_exp_f32 v66, v66\n"
    "v_mfma_f32_16x16x32_bf16 v[128:131], a[120:123], v[68:71], v[128:131]\n"
    "v_cvt_pk_bf16_f32 v96, v48, v49\n"
    "v_mfma_f32_16x16x32_bf16 v[140:143], a[116:119], v[80:83], 0\n"
    "s_waitcnt lgkmcnt(2)\n"
    "buffer_load_dwordx4 v[220:223], v242, s[40:43], s75 offen offset:512 nt\n"
    "v_mfma_f32_16x16x32_bf16 v[188:191], a[148:151], v[72:75], 0\n"
    "v_mov_b32 v2, v3\n"
    "v_cvt_pk_bf16_f32 v97, v50, v51\n"
    "v_mfma_f32_16x16x32_bf16 v[144:147], a[120:123], v[76:79], 0\n"
    "v_exp_f32 v67, v67\n"
    "v_mfma_f32_16x16x32_bf16 v[148:151], a[132:135], v[80:83], 0\n"
    "v_mfma_f32_16x16x32_bf16 v[144:147], a[124:127], v[80:83], v[144:147]\n"
    "buffer_load_dwordx4 v[224:227], v242, s[40:43], s75 offen offset:1280 nt\n"
    "v_mfma_f32_16x16x32_bf16 v[132:135], a[132:135], v[72:75], v[132:135]\n"
    "s_waitcnt lgkmcnt(1)\n"
    "ds_read_b128 a[140:143], v248 offset:15360\n"
    "v_mfma_f32_16x16x32_bf16 v[136:139], a[136:139], v[68:71], 0\n"
    "v_exp_f32 v12, v12\n"
    "v_mfma_f32_16x16x32_bf16 v[160:163], a[120:123], v[84:87], v[160:163]\n"
    "v_cvt_pk_bf16_f32 v98, v64, v65\n"
    "v_mfma_f32_16x16x32_bf16 v[156:159], a[112:115], v[84:87], 0\n"
    "v_mfma_f32_16x16x32_bf16 v[24:27], a[76:79], a[220:223], v[24:27]\n"
    "v_mfma_f32_16x16x32_bf16 a[16:19], a[148:151], v[88:91], 0\n"
    "v_cvt_pk_bf16_f32 v99, v66, v67\n"
    "v_mfma_f32_16x16x32_bf16 v[156:159], a[116:119], v[88:91], v[156:159]\n"
    "s_waitcnt lgkmcnt(1)\n"
    "v_exp_f32 v8, v8\n"
    "v_mfma_f32_16x16x32_bf16 a[0:3], a[144:147], v[76:79], 0\n"
    "v_mfma_f32_16x16x32_bf16 v[148:151], a[128:131], v[76:79], v[148:151]\n"
    "v_mfma_f32_16x16x32_bf16 v[172:175], a[116:119], v[96:99], 0\n"
    "v_exp_f32 v18, v18\n"
    "v_mfma_f32_16x16x32_bf16 v[164:167], a[132:135], v[88:91], v[164:167]\n"
    "v_exp_f32 v28, v28\n"
    "v_mfma_f32_16x16x32_bf16 v[180:183], a[132:135], v[96:99], 0\n"
    "ds_read_b128 a[120:123], v248 offset:7168\n"
    "s_add_u32 s59, s59, 4\n"
    "v_mfma_f32_16x16x32_bf16 v[188:191], a[144:147], v[68:71], v[188:191]\n"
    "v_mfma_f32_16x16x32_bf16 a[32:35], a[144:147], v[92:95], 0\n"
    "s_waitcnt lgkmcnt(1)\n"
    "ds_read_b128 a[132:135], v248 offset:20480\n"
    "v_mfma_f32_16x16x32_bf16 v[152:155], a[140:143], v[80:83], 0\n"
    "ds_read_b128 a[116:119], v248 offset:18432\n"
    "v_mfma_f32_16x16x32_bf16 v[176:179], a[124:127], v[96:99], v[176:179]\n"
    "v_exp_f32 v19, v19\n"
    "v_mfma_f32_16x16x32_bf16 v[172:175], a[112:115], v[92:95], v[172:175]\n"
    "v_exp_f32 v6, v6\n"
    "v_mfma_f32_16x16x32_bf16 v[192:195], a[152:155], v[68:71], 0\n"
    "v_exp_f32 v7, v7\n"
    "v_mfma_f32_16x16x32_bf16 v[140:143], a[112:115], v[76:79], v[140:143]\n"
    "v_exp_f32 v11, v11\n"
    "v_mfma_f32_16x16x32_bf16 a[0:3], a[148:151], v[80:83], a[0:3]\n"
    "v_mfma_f32_16x16x32_bf16 v[24:27], a[72:75], a[216:219], v[24:27]\n"
    "v_exp_f32 v4, v4\n"
    "v_mfma_f32_16x16x32_bf16 v[152:155], a[136:139], v[76:79], v[152:155]\n"
    "v_exp_f32 v31, v31\n"
    "v_mfma_f32_16x16x32_bf16 a[32:35], a[148:151], v[96:99], a[32:35]\n"
    "s_waitcnt lgkmcnt(2)\n"
    "v_mfma_f32_16x16x32_bf16 a[44:47], a[120:123], v[92:95], 0\n"
    "v_exp_f32 v10, v10\n"
    "v_mfma_f32_16x16x32_bf16 a[16:19], a[144:147], v[84:87], a[16:19]\n"
    "v_mfma_f32_16x16x32_bf16 a[36:39], a[156:159], v[96:99], 0\n"
    "ds_read_b128 a[112:115], v248 offset:6144\n"
    "ds_read_b128 a[124:127], v248 offset:19456\n"
    "v_mfma_f32_16x16x32_bf16 v[192:195], a[156:159], v[72:75], v[192:195]\n"
    "v_exp_f32 v5, v5\n"
    "v_mfma_f32_16x16x32_bf16 a[4:7], a[152:155], v[76:79], a[4:7]\n"
    "v_exp_f32 v15, v15\n"
    "v_mfma_f32_16x16x32_bf16 a[36:39], a[152:155], v[92:95], a[36:39]\n"
    "v_mfma_f32_16x16x32_bf16 v[184:187], a[140:143], v[96:99], 0\n"
    "s_waitcnt lgkmcnt(2)\n"
    "v_mfma_f32_16x16x32_bf16 a[8:11], a[116:119], v[80:83], 0\n"
    "ds_read_b128 a[148:151], v248 offset:22528\n"
    "ds_read_b128 a[156:159], v248 offset:23552\n"
    "v_mfma_f32_16x16x32_bf16 a[40:43], a[116:119], v[96:99], 0\n"
    "v_smfmac_f32_16x16x64_bf16 v[116:119], v[52:55], v[84:91], v253\n"
    "s_waitcnt lgkmcnt(3)\n"
    "v_mfma_f32_16x16x32_bf16 a[8:11], a[112:115], v[76:79], a[8:11]\n"
    "v_exp_f32 v9, v9\n"
    "v_mfma_f32_16x16x32_bf16 a[64:67], a[132:135], v[80:83], 0\n"
    "ds_read_b128 a[152:155], v248 offset:11264\n"
    "v_mfma_f32_16x16x32_bf16 v[32:35], a[76:79], a[172:175], v[32:35]\n"
    "v_exp_f32 v30, v30\n"
    "v_mfma_f32_16x16x32_bf16 a[12:15], a[120:123], v[76:79], 0\n"
    "v_mfma_f32_16x16x32_bf16 v[196:199], a[112:115], v[68:71], 0\n"
    "v_exp_f32 v17, v17\n"
    "v_mfma_f32_16x16x32_bf16 v[180:183], a[128:131], v[92:95], v[180:183]\n"
    "s_waitcnt lgkmcnt(3)\n"
    "v_exp_f32 v20, v20\n"
    "v_mfma_f32_16x16x32_bf16 a[44:47], a[124:127], v[96:99], a[44:47]\n"
    "v_mfma_f32_16x16x32_bf16 v[32:35], a[72:75], a[168:171], v[32:35]\n"
    "v_exp_f32 v22, v22\n"
    "v_mfma_f32_16x16x32_bf16 v[168:171], a[136:139], v[84:87], 0\n"
    "ds_read_b128 a[128:131], v248 offset:8192\n"
    "v_mfma_f32_16x16x32_bf16 v[136:139], a[140:143], v[72:75], v[136:139]\n"
    "s_waitcnt lgkmcnt(3)\n"
    "v_mfma_f32_16x16x32_bf16 a[88:91], a[148:151], v[88:91], 0\n"
    "s_add_u32 s67, s67, 49152\n"
    "buffer_load_dwordx2 v[236:237], v241, s[36:39], s81 offen offset:0 sc1\n"
    "v_mfma_f32_16x16x32_bf16 a[28:31], a[124:127], v[88:91], 0\n"
    "v_mfma_f32_16x16x32_bf16 v[168:171], a[140:143], v[88:91], v[168:171]\n"
    "v_exp_f32 v29, v29\n"
    "v_mfma_f32_16x16x32_bf16 v[184:187], a[136:139], v[92:95], v[184:187]\n"
    "v_mfma_f32_16x16x32_bf16 a[40:43], a[112:115], v[92:95], a[40:43]\n"
    "v_exp_f32 v16, v16\n"
    "v_mfma_f32_16x16x32_bf16 a[96:99], a[132:135], v[96:99], 0\n"
    "s_waitcnt lgkmcnt(1)\n"
    "v_exp_f32 v26, v26\n"
    "v_mfma_f32_16x16x32_bf16 a[60:63], a[152:155], v[68:71], 0\n"
    "v_exp_f32 v23, v23\n"
    "v_mfma_f32_16x16x32_bf16 a[108:111], a[156:159], v[96:99], 0\n"
    "s_waitcnt lgkmcnt(0)\n"
    "v_mfma_f32_16x16x32_bf16 a[48:51], a[128:131], v[68:71], 0\n"
    "v_mfma_f32_16x16x32_bf16 a[28:31], a[120:123], v[84:87], a[28:31]\n"
    "v_mfma_f32_16x16x32_bf16 a[96:99], a[128:131], v[92:95], a[96:99]\n"
    "ds_read_b128 a[140:143], v248 offset:21504\n"
    "v_mfma_f32_16x16x32_bf16 v[196:199], a[116:119], v[72:75], v[196:199]\n"
    "ds_read_b128 a[144:147], v248 offset:10240\n"
    "v_mfma_f32_16x16x32_bf16 a[64:67], a[128:131], v[76:79], a[64:67]\n"
    "v_exp_f32 v21, v21\n"
    "v_mfma_f32_16x16x32_bf16 a[48:51], a[132:135], v[72:75], a[48:51]\n"
    "v_mfma_f32_16x16x32_bf16 v[200:203], a[124:127], v[72:75], 0\n"
    "ds_read_b128 a[136:139], v248 offset:9216\n"
    "v_mfma_f32_16x16x32_bf16 a[80:83], a[128:131], v[84:87], 0\n"
    "ds_read_b128 v[100:103], v250 offset:896\n"
    "v_mfma_f32_16x16x32_bf16 a[76:79], a[156:159], v[80:83], 0\n"
    "v_exp_f32 v13, v13\n"
    "v_mfma_f32_16x16x32_bf16 a[60:63], a[156:159], v[72:75], a[60:63]\n"
    "v_exp_f32 v24, v24\n"
    "v_mfma_f32_16x16x32_bf16 a[24:27], a[112:115], v[84:87], 0\n"
    "s_waitcnt lgkmcnt(3)\n"
    "v_exp_f32 v25, v25\n"
    "v_mfma_f32_16x16x32_bf16 a[68:71], a[140:143], v[80:83], 0\n"
    "v_exp_f32 v27, v27\n"
    "v_mfma_f32_16x16x32_bf16 a[12:15], a[124:127], v[80:83], a[12:15]\n"
    "s_waitcnt lgkmcnt(2)\n"
    "v_exp_f32 v32, v32\n"
    "v_mfma_f32_16x16x32_bf16 a[88:91], a[144:147], v[84:87], a[88:91]\n"
    "v_exp_f32 v33, v33\n"
    "v_mfma_f32_16x16x32_bf16 a[108:111], a[152:155], v[92:95], a[108:111]\n"
    "v_exp_f32 v34, v34\n"
    "v_mfma_f32_16x16x32_bf16 a[72:75], a[144:147], v[76:79], 0\n"
    "ds_read_b128 a[124:127], v3 offset:18432\n"
    "v_mfma_f32_16x16x32_bf16 a[24:27], a[116:119], v[88:91], a[24:27]\n"
    "ds_read_b128 a[112:115], v3 offset:12288\n"
    "ds_read_b128 a[116:119], v3 offset:14336\n"
    "v_mfma_f32_16x16x32_bf16 a[92:95], a[152:155], v[84:87], 0\n"
    "v_mfma_f32_16x16x32_bf16 a[80:83], a[132:135], v[88:91], a[80:83]\n"
    "ds_read_b128 a[128:131], v3 offset:20480\n"
    "v_mfma_f32_16x16x32_bf16 a[56:59], a[148:151], v[72:75], 0\n"
    "s_waitcnt lgkmcnt(5)\n"
    "v_mfma_f32_16x16x32_bf16 a[68:71], a[136:139], v[76:79], a[68:71]\n"
    "v_smfmac_f32_16x16x64_bf16 v[120:123], v[52:55], v[92:99], v253\n"
    "ds_read_b128 a[132:135], v3 offset:22528\n"
    "v_mfma_f32_16x16x32_bf16 v[200:203], a[120:123], v[68:71], v[200:203]\n"
    "v_mfma_f32_16x16x32_bf16 a[92:95], a[156:159], v[88:91], a[92:95]\n"
    "s_waitcnt vmcnt(7)\n"
    "ds_read_b128 a[120:123], v3 offset:16384\n"
    "v_mfma_f32_16x16x32_bf16 a[100:103], a[140:143], v[96:99], 0\n"
    "ds_add_u32 v247, v246\n"
    "v_mfma_f32_16x16x32_bf16 a[104:107], a[144:147], v[92:95], 0\n"
    "v_exp_f32 v35, v35\n"
    "v_mfma_f32_16x16x32_bf16 a[84:87], a[136:139], v[84:87], 0\n"
    "v_mfma_f32_16x16x32_bf16 a[84:87], a[140:143], v[88:91], a[84:87]\n"
    "v_mfma_f32_16x16x32_bf16 a[72:75], a[148:151], v[80:83], a[72:75]\n"
    "v_mfma_f32_16x16x32_bf16 a[100:103], a[136:139], v[92:95], a[100:103]\n"
    "v_mfma_f32_16x16x32_bf16 a[104:107], a[148:151], v[96:99], a[104:107]\n"
    "v_mfma_f32_16x16x32_bf16 a[76:79], a[152:155], v[76:79], a[76:79]\n"
    "v_mfma_f32_16x16x32_bf16 a[56:59], a[144:147], v[68:71], a[56:59]\n"
    "s_waitcnt lgkmcnt(1)\n"
    "v_add_u32 v3, 49152, v252\n"
    "v_mfma_f32_16x16x32_bf16 a[52:55], a[136:139], v[68:71], 0\n"
    "v_mfma_f32_16x16x32_bf16 a[52:55], a[140:143], v[72:75], a[52:55]\n"
    "s_branch .LNV\n"
    ".fill ((0x20000 - (. - _Z8attn_fwdPKtPKiS2_S0_S0_S0_S0_PcPt)) & 4095) / 4, 4, 0xBF800000\n"
    ".LhVN:\n"
    "s_waitcnt vmcnt(18)\n"
    "ds_read_b128 v[52:55], v251 offset:768\n"
    "ds_read_b128 v[200:203], v97 offset:49408\n"
    "s_add_u32 m0, s68, 0\n"
    "ds_read_b128 v[188:191], v96 offset:49280\n"
    "ds_read_b128 v[180:183], v96 offset:49152\n"
    "ds_read_b128 v[192:195], v97 offset:49280\n"
    "ds_read_b128 v[184:187], v97 offset:49152\n"
    "ds_read_b128 v[196:199], v96 offset:49408\n"
    "s_waitcnt lgkmcnt(6)\n"
    "v_smfmac_f32_16x16x64_bf16 v[112:115], v[52:55], v[76:83], v253\n"
    "s_add_u32 s83, s67, 4096\n"
    "s_waitcnt lgkmcnt(5)\n"
    "v_pk_maximum3_f16 v98, v200, v201, v98\n"
    "v_smfmac_f32_16x16x64_bf16 v[116:119], v[52:55], v[84:91], v253\n"
    "s_waitcnt lgkmcnt(0)\n"
    "s_nop 0\n"
    "v_pk_maximum3_f16 v98, v202, v203, v98\n"
    "v_pk_maximum3_f16 v98, v196, v197, v98\n"
    "v_pk_maximum3_f16 v98, v198, v199, v98\n"
    "v_pk_maximum3_f16 v98, v192, v193, v98\n"
    "v_pk_maximum3_f16 v98, v194, v195, v98\n"
    "v_pk_add_u16 v202, s89, v202 clamp\n"
    "v_pk_add_u16 v201, s89, v201 clamp\n"
    "v_pk_add_u16 v197, s89, v197 clamp\n"
    "v_pk_add_u16 v196, s89, v196 clamp\n"
    "v_pk_add_u16 v194, s89, v194 clamp\n"
    "v_pk_maximum3_f16 v98, v188, v189, v98\n"
    "v_pk_add_u16 v200, s89, v200 clamp\n"
    "v_pk_add_u16 v203, s89, v203 clamp\n"
    "v_pk_maximum3_f16 v98, v190, v191, v98\n"
    "v_pk_add_u16 v189, s89, v189 clamp\n"
    "v_pk_add_u16 v199, s89, v199 clamp\n"
    "v_pk_add_u16 v198, s89, v198 clamp\n"
    "v_pk_add_u16 v192, s89, v192 clamp\n"
    "v_mfma_f32_16x16x32_bf16 v[16:19], a[132:135], v[200:203], v[16:19]\n"
    "v_pk_add_u16 v193, s89, v193 clamp\n"
    "v_pk_add_u16 v188, s89, v188 clamp\n"
    "v_pk_add_u16 v195, s89, v195 clamp\n"
    "v_mfma_f32_16x16x32_bf16 v[64:67], a[44:47], v[200:203], v[64:67]\n"
    "s_waitcnt vmcnt(12)\n"
    "ds_add_u32 v247, v246\n"
    "v_mfma_f32_16x16x32_bf16 v[48:51], a[20:23], v[200:203], v[48:51]\n"
    "buffer_load_dwordx4 v1, s[16:19], s67 offen offset:0 sc1 lds\n"
    "v_mfma_f32_16x16x32_bf16 v[64:67], a[40:43], v[196:199], v[64:67]\n"
    "v_accvgpr_write_b32 a188, v200\n"
    "v_accvgpr_write_b32 a183, v195\n"
    "v_mfma_f32_16x16x32_bf16 v[16:19], a[128:131], v[196:199], v[16:19]\n"
    "v_pk_maximum3_f16 v98, v184, v185, v98\n"
    "v_pk_add_u16 v184, s89, v184 clamp\n"
    "v_mfma_f32_16x16x32_bf16 v[32:35], a[156:159], v[200:203], v[32:35]\n"
    "s_nop 0\n"
    "v_pk_add_u16 v190, s89, v190 clamp\n"
    "v_pk_maximum3_f16 v98, v186, v187, v98\n"
    "v_mfma_f32_16x16x32_bf16 v[64:67], a[36:39], v[192:195], v[64:67]\n"
    "v_pk_maximum3_f16 v98, v180, v181, v98\n"
    "v_pk_add_u16 v191, s89, v191 clamp\n"
    "v_mfma_f32_16x16x32_bf16 v[16:19], a[124:127], v[192:195], v[16:19]\n"
    "buffer_load_dwordx4 v1, s[16:19], s67 offen offset:1024 sc1 lds\n"
    "v_pk_maximum3_f16 v98, v182, v183, v98\n"
    "v_pk_add_u16 v185, s89, v185 clamp\n"
    "v_mfma_f32_16x16x32_bf16 v[48:51], a[16:19], v[196:199], v[48:51]\n"
    "ds_read_b32 v245, v247\n"
    "v_pk_add_u16 v180, s89, v180 clamp\n"
    "v_pk_add_u16 v183, s89, v183 clamp\n"
    "v_mfma_f32_16x16x32_bf16 v[32:35], a[152:155], v[196:199], v[32:35]\n"
    "v_accvgpr_write_b32 a171, v183\n"
    "v_pk_add_u16 v186, s89, v186 clamp\n"
    "v_mfma_f32_16x16x32_bf16 v[32:35], a[148:151], v[192:195], v[32:35]\n"
    "v_pk_add_u16 v187, s89, v187 clamp\n"
    "v_accvgpr_write_b32 a168, v180\n"
    "v_mfma_f32_16x16x32_bf16 v[32:35], a[144:147], v[188:191], v[32:35]\n"
    "buffer_load_dwordx4 v1, s[16:19], s67 offen offset:2048 sc1 lds\n"
    "s_nop 0\n"
    "v_mfma_f32_16x16x32_bf16 v[48:51], a[12:15], v[192:195], v[48:51]\n"
    "s_add_u32 s78, s52, s62\n"
    "v_accvgpr_write_b32 a172, v184\n"
    "v_pk_add_u16 v181, s89, v181 clamp\n"
    "v_mfma_f32_16x16x32_bf16 v[48:51], a[8:11], v[188:191], v[48:51]\n"
    "v_pk_add_u16 v182, s89, v182 clamp\n"
    "v_accvgpr_write_b32 a177, v189\n"
    "v_mfma_f32_16x16x32_bf16 v[16:19], a[120:123], v[188:191], v[16:19]\n"
    "v_accvgpr_write_b32 a185, v197\n"
    "v_accvgpr_write_b32 a189, v201\n"
    "v_mfma_f32_16x16x32_bf16 v[16:19], a[116:119], v[184:187], v[16:19]\n"
    "s_waitcnt lgkmcnt(0)\n"
    "v_accvgpr_write_b32 a186, v198\n"
    "v_cmp_le_u32 s[56:57], s59, v245\n"
    "v_smfmac_f32_16x16x64_bf16 v[108:111], v[52:55], v[68:75], v253\n"
    "s_cmp_eq_u64 s[56:57], -1\n"
    "s_cbranch_scc0 .LphVN1\n"
    ".LphVN1r:\n"
    "v_mfma_f32_16x16x32_bf16 v[32:35], a[140:143], v[184:187], v[32:35]\n"
    "ds_read_b128 a[72:75], v252 offset:25600\n"
    "ds_read_b128 a[92:95], v252 offset:35840\n"
    "v_mfma_f32_16x16x32_bf16 v[64:67], a[32:35], v[188:191], v[64:67]\n"
    "ds_read_b128 a[48:51], v252 offset:24576\n"
    "v_accvgpr_write_b32 a182, v194\n"
    "v_mfma_f32_16x16x32_bf16 v[48:51], a[4:7], v[184:187], v[48:51]\n"
    "v_accvgpr_write_b32 a191, v203\n"
    "v_accvgpr_write_b32 a187, v199\n"
    "v_mfma_f32_16x16x32_bf16 v[32:35], a[136:139], v[180:183], v[32:35]\n"
    "ds_read_b128 a[52:55], v252 offset:26624\n"
    "v_accvgpr_write_b32 a176, v188\n"
    "v_mfma_f32_16x16x32_bf16 v[48:51], a[0:3], v[180:183], v[48:51]\n"
    "ds_read_b128 a[56:59], v252 offset:28672\n"
    "ds_read_b128 a[76:79], v252 offset:27648\n"
    "v_mfma_f32_16x16x32_bf16 v[64:67], a[28:31], v[184:187], v[64:67]\n"
    "buffer_load_dwordx4 v1, s[16:19], s67 offen offset:3072 sc1 lds\n"
    "v_mfma_f32_16x16x32_bf16 v[64:67], a[24:27], v[180:183], v[64:67]\n"
    "ds_read_b128 a[68:71], v252 offset:34816\n"
    "ds_read_b128 a[60:63], v252 offset:30720\n"
    "v_mfma_f32_16x16x32_bf16 v[16:19], a[112:115], v[180:183], v[16:19]\n"
    "ds_read_b128 a[88:91], v252 offset:33792\n"
    "s_waitcnt lgkmcnt(8)\n"
    "v_mfma_f32_16x16x32_bf16 v[28:31], a[72:75], a[240:243], v[104:107]\n"
    "ds_read_b128 a[64:67], v252 offset:32768\n"
    "ds_read_b128 a[80:83], v252 offset:29696\n"
    "v_mfma_f32_16x16x32_bf16 v[20:23], a[72:75], a[192:195], v[104:107]\n"
    "s_waitcnt lgkmcnt(8)\n"
    "ds_read_b128 a[84:87], v252 offset:31744\n"
    "v_pk_add_u16 v98, s89, v98\n"
    "v_mfma_f32_16x16x32_bf16 v[12:15], a[48:51], a[240:243], v[100:103]\n"
    "v_accvgpr_write_b32 a180, v192\n"
    "v_accvgpr_write_b32 a170, v182\n"
    "v_mfma_f32_16x16x32_bf16 v[4:7], a[48:51], a[192:195], v[100:103]\n"
    "s_add_u32 m0, s68, 4096\n"
    "v_accvgpr_write_b32 a174, v186\n"
    "s_waitcnt lgkmcnt(8)\n"
    "v_mfma_f32_16x16x32_bf16 v[4:7], a[52:55], a[196:199], v[4:7]\n"
    "v_exp_f32 v65, v65\n"
    "v_mfma_f32_16x16x32_bf16 v[12:15], a[52:55], a[244:247], v[12:15]\n"
    "s_waitcnt lgkmcnt(7)\n"
    "buffer_load_dwordx4 v1, s[16:19], s83 offen offset:0 sc1 lds\n"
    "v_mfma_f32_16x16x32_bf16 v[4:7], a[56:59], a[200:203], v[4:7]\n"
    "s_waitcnt lgkmcnt(6)\n"
    "v_accvgpr_write_b32 a179, v191\n"
    "v_accvgpr_write_b32 a169, v181\n"
    "v_mfma_f32_16x16x32_bf16 v[20:23], a[76:79], a[196:199], v[20:23]\n"
    "v_exp_f32 v16, v16\n"
    "v_mfma_f32_16x16x32_bf16 v[28:31], a[76:79], a[244:247], v[28:31]\n"
    "s_waitcnt lgkmcnt(4)\n"
    "v_accvgpr_write_b32 a175, v187\n"
    "v_accvgpr_write_b32 a190, v202\n"
    "v_mfma_f32_16x16x32_bf16 v[4:7], a[60:63], a[204:207], v[4:7]\n"
    "s_waitcnt lgkmcnt(1)\n"
    "buffer_load_dwordx4 v1, s[16:19], s83 offen offset:1024 sc1 lds\n"
    "v_mfma_f32_16x16x32_bf16 v[4:7], a[64:67], a[208:211], v[4:7]\n"
    "s_add_u32 m0, s70, 0\n"
    "v_accvgpr_write_b32 a181, v193\n"
    "v_accvgpr_write_b32 a173, v185\n"
    "v_mfma_f32_16x16x32_bf16 v[20:23], a[80:83], a[200:203], v[20:23]\n"
    "s_add_u32 s59, s59, 4\n"
    "v_accvgpr_write_b32 a184, v196\n"
    "v_accvgpr_write_b32 a178, v190\n"
    "v_mfma_f32_16x16x32_bf16 v[8:11], a[68:71], a[236:239], v[100:103]\n"
    "s_add_u32 s83, s67, 24576\n"
    "v_mfma_f32_16x16x32_bf16 v[28:31], a[80:83], a[248:251], v[28:31]\n"
    "s_waitcnt lgkmcnt(0)\n"
    "buffer_load_dwordx4 v1, s[16:19], s83 offen offset:0 sc1 lds\n"
    "v_mfma_f32_16x16x32_bf16 v[28:31], a[84:87], a[252:255], v[28:31]\n"
    "v_exp_f32 v19, v19\n"
    "v_mfma_f32_16x16x32_bf16 v[12:15], a[56:59], a[248:251], v[12:15]\n"
    "s_waitcnt vmcnt(13)\n"
    "ds_add_u32 v247, v246\n"
    "v_mfma_f32_16x16x32_bf16 v[8:11], a[64:67], a[232:235], v[8:11]\n"
    "v_exp_f32 v18, v18\n"
    "v_mfma_f32_16x16x32_bf16 v[12:15], a[60:63], a[252:255], v[12:15]\n"
    "buffer_load_dwordx4 v1, s[16:19], s83 offen offset:1024 sc1 lds\n"
    "v_mfma_f32_16x16x32_bf16 v[20:23], a[84:87], a[204:207], v[20:23]\n"
    "v_exp_f32 v66, v66\n"
    "v_mfma_f32_16x16x32_bf16 v[24:27], a[92:95], a[236:239], v[104:107]\n"
    "v_cvt_pk_bf16_f32 v93, v18, v19\n"
    "v_mfma_f32_16x16x32_bf16 v[24:27], a[88:91], a[232:235], v[24:27]\n"
    "v_exp_f32_e64 v17, v17\n"
    "v_mfma_f32_16x16x32_bf16 v[24:27], a[84:87], a[228:231], v[24:27]\n"
    "buffer_load_dwordx4 v1, s[16:19], s83 offen offset:2048 sc1 lds\n"
    "v_mfma_f32_16x16x32_bf16 v[24:27], a[80:83], a[224:227], v[24:27]\n"
    "v_exp_f32_e64 v35, v35\n"
    "v_mfma_f32_16x16x32_bf16 v[20:23], a[88:91], a[208:211], v[20:23]\n"
    "ds_read_b32 v245, v247\n"
    "v_cvt_pk_bf16_f32 v92, v16, v17\n"
    "v_mfma_f32_16x16x32_bf16 v[16:19], a[68:71], a[188:191], v[100:103]\n"
    "v_exp_f32_e64 v32, v32\n"
    "v_mfma_f32_16x16x32_bf16 v[16:19], a[64:67], a[184:187], v[16:19]\n"
    "buffer_load_dwordx4 v1, s[16:19], s83 offen offset:3072 sc1 lds\n"
    "v_mfma_f32_16x16x32_bf16 v[4:7], a[68:71], a[212:215], v[4:7]\n"
    "v_exp_f32 v33, v33\n"
    "v_mfma_f32_16x16x32_bf16 v[8:11], a[60:63], a[228:231], v[8:11]\n"
    "v_exp_f32_e64 v34, v34\n"
    "v_mfma_f32_16x16x32_bf16 v[16:19], a[60:63], a[180:183], v[16:19]\n"
    "v_cvt_pk_bf16_f32 v95, v34, v35\n"
    "s_add_u32 m0, s70, 4096\n"
    "v_mfma_f32_16x16x32_bf16 v[8:11], a[56:59], a[224:227], v[8:11]\n"
    "s_add_u32 s83, s67, 28672\n"
    "buffer_load_dwordx4 v1, s[16:19], s83 offen offset:0 sc1 lds\n"
    "v_mfma_f32_16x16x32_bf16 v[16:19], a[56:59], a[176:179], v[16:19]\n"
    "v_cvt_pk_bf16_f32 v94, v32, v33\n"
    "v_and_b32 v98, 0x80008000, v98\n"
    "v_mfma_f32_16x16x32_bf16 v[8:11], a[52:55], a[220:223], v[8:11]\n"
    "s_waitcnt lgkmcnt(0)\n"
    "v_cmp_le_u32 s[56:57], s59, v245\n"
    "s_add_u32 s67, s67, 49152\n"
    "v_mfma_f32_16x16x32_bf16 v[20:23], a[92:95], a[212:215], v[20:23]\n"
    "v_cmp_ne_u32 s[34:35], 0, v98\n"
    "v_mfma_f32_16x16x32_bf16 v[16:19], a[52:55], a[172:175], v[16:19]\n"
    "buffer_load_dwordx4 v1, s[16:19], s83 offen offset:1024 sc1 lds\n"
    "v_mfma_f32_16x16x32_bf16 v[32:35], a[92:95], a[188:191], v[104:107]\n"
    "s_cmp_eq_u64 s[56:57], -1\n"
    "s_cbranch_scc0 .LphVN2\n"
    ".LphVN2r:\n"
    "v_mfma_f32_16x16x32_bf16 v[28:31], a[88:91], a[160:163], v[28:31]\n"
    "ds_read_b128 a[112:115], v249 offset:0\n"
    "ds_read_b128 a[136:139], v249 offset:3072\n"
    "v_mfma_f32_16x16x32_bf16 v[32:35], a[88:91], a[184:187], v[32:35]\n"
    "ds_read_b128 a[152:155], v249 offset:5120\n"
    "ds_read_b128 a[128:131], v249 offset:2048\n"
    "v_mfma_f32_16x16x32_bf16 v[8:11], a[48:51], a[216:219], v[8:11]\n"
    "ds_read_b128 a[132:135], v249 offset:14336\n"
    "v_mfma_f32_16x16x32_bf16 v[12:15], a[64:67], a[160:163], v[12:15]\n"
    "s_add_u32 s59, s59, 4\n"
    "ds_read_b128 a[116:119], v249 offset:12288\n"
    "v_mfma_f32_16x16x32_bf16 v[12:15], a[68:71], a[164:167], v[12:15]\n"
    "ds_read_b128 a[124:127], v249 offset:13312\n"
    "v_mfma_f32_16x16x32_bf16 v[28:31], a[92:95], a[164:167], v[28:31]\n"
    "ds_read_b128 a[140:143], v249 offset:15360\n"
    "ds_read_b128 a[144:147], v249 offset:4096\n"
    "v_mfma_f32_16x16x32_bf16 v[16:19], a[48:51], a[168:171], v[16:19]\n"
    "s_add_u32 s77, s52, s61\n"
    "v_exp_f32 v49, v49\n"
    "v_mfma_f32_16x16x32_bf16 v[32:35], a[84:87], a[180:183], v[32:35]\n"
    "ds_read_b128 a[120:123], v249 offset:1024\n"
    "s_waitcnt vmcnt(12)\n"
    "v_mfma_f32_16x16x32_bf16 v[24:27], a[76:79], a[220:223], v[24:27]\n"
    "s_waitcnt lgkmcnt(9)\n"
    "v_exp_f32 v67, v67\n"
    "v_mfma_f32_16x16x32_bf16 v[124:127], a[112:115], v[68:71], 0\n"
    "s_waitcnt lgkmcnt(8)\n"
    "v_exp_f32 v7, v7\n"
    "v_mfma_f32_16x16x32_bf16 v[136:139], a[136:139], v[68:71], 0\n"
    "s_waitcnt lgkmcnt(6)\n"
    "ds_read_b128 a[156:159], v249 offset:17408\n"
    "v_mfma_f32_16x16x32_bf16 v[164:167], a[128:131], v[84:87], 0\n"
    "s_waitcnt lgkmcnt(5)\n"
    "v_exp_f32 v64, v64\n"
    "v_mfma_f32_16x16x32_bf16 v[140:143], a[116:119], v[80:83], 0\n"
    "v_exp_f32 v51, v51\n"
    "v_mfma_f32_16x16x32_bf16 a[20:23], a[152:155], v[84:87], 0\n"
    "s_waitcnt lgkmcnt(4)\n"
    "v_cvt_pk_u16_u32 v207, v207, v211\n"
    "v_mfma_f32_16x16x32_bf16 v[160:163], a[124:127], v[88:91], 0\n"
    "v_mfma_f32_16x16x32_bf16 v[140:143], a[112:115], v[76:79], v[140:143]\n"
    "v_exp_f32 v48, v48\n"
    "v_mfma_f32_16x16x32_bf16 v[128:131], a[124:127], v[72:75], 0\n"
    "s_waitcnt lgkmcnt(2)\n"
    "v_exp_f32 v20, v20\n"
    "v_mfma_f32_16x16x32_bf16 a[0:3], a[144:147], v[76:79], 0\n"
    "s_waitcnt lgkmcnt(1)\n"
    "v_exp_f32 v50, v50\n"
    "v_mfma_f32_16x16x32_bf16 v[144:147], a[120:123], v[76:79], 0\n"
    "v_cvt_pk_u16_u32 v215, v215, v219\n"
    "v_mfma_f32_16x16x32_bf16 v[132:135], a[128:131], v[68:71], 0\n"
    "v_mfma_f32_16x16x32_bf16 v[32:35], a[80:83], a[176:179], v[32:35]\n"
    "ds_read_b128 a[148:151], v249 offset:16384\n"
    "v_mfma_f32_16x16x32_bf16 v[148:151], a[132:135], v[80:83], 0\n"
    "v_exp_f32 v5, v5\n"
    "v_mfma_f32_16x16x32_bf16 v[152:155], a[140:143], v[80:83], 0\n"
    "s_waitcnt lgkmcnt(1)\n"
    "v_exp_f32 v13, v13\n"
    "v_mfma_f32_16x16x32_bf16 a[4:7], a[156:159], v[80:83], 0\n"
    "v_mfma_f32_16x16x32_bf16 v[32:35], a[76:79], a[172:175], v[32:35]\n"
    "v_mfma_f32_16x16x32_bf16 v[148:151], a[128:131], v[76:79], v[148:151]\n"
    "v_cvt_pk_bf16_f32 v96, v48, v49\n"
    "v_mfma_f32_16x16x32_bf16 v[144:147], a[124:127], v[80:83], v[144:147]\n"
    "v_mfma_f32_16x16x32_bf16 v[176:179], a[120:123], v[92:95], 0\n"
    "v_exp_f32 v6, v6\n"
    "v_mfma_f32_16x16x32_bf16 v[160:163], a[120:123], v[84:87], v[160:163]\n"
    "v_cvt_pk_u16_u32 v220, v220, v224\n"
    "s_add_u32 s76, s52, s60\n"
    "v_mfma_f32_16x16x32_bf16 v[192:195], a[152:155], v[68:71], 0\n"
    "v_cvt_pk_bf16_f32 v99, v66, v67\n"
    "v_cvt_pk_u16_u32 v205, v205, v209\n"
    "v_mfma_f32_16x16x32_bf16 v[136:139], a[140:143], v[72:75], v[136:139]\n"
    "v_cvt_pk_u16_u32 v221, v221, v225\n"
    "v_cvt_pk_u16_u32 v214, v214, v218\n"
    "v_mfma_f32_16x16x32_bf16 a[20:23], a[156:159], v[88:91], a[20:23]\n"
    "v_exp_f32 v22, v22\n"
    "v_mfma_f32_16x16x32_bf16 v[32:35], a[72:75], a[168:171], v[32:35]\n"
    "v_exp_f32 v21, v21\n"
    "v_mfma_f32_16x16x32_bf16 v[156:159], a[112:115], v[84:87], 0\n"
    "v_mfma_f32_16x16x32_bf16 v[168:171], a[136:139], v[84:87], 0\n"
    "v_cvt_pk_bf16_f32 v97, v50, v51\n"
    "v_cvt_pk_u16_u32 v204, v204, v208\n"
    "v_mfma_f32_16x16x32_bf16 v[192:195], a[156:159], v[72:75], v[192:195]\n"
    "v_or3_b32 v243, v204, v205, v243\n"
    "v_cvt_pk_bf16_f32 v98, v64, v65\n"
    "v_mfma_f32_16x16x32_bf16 v[128:131], a[120:123], v[68:71], v[128:131]\n"
    "s_nop 0\n"
    "v_mfma_f32_16x16x32_bf16 a[36:39], a[156:159], v[96:99], 0\n"
    "ds_read_b128 a[120:123], v249 offset:7168\n"
    "v_cvt_pk_u16_u32 v206, v206, v210\n"
    "v_mfma_f32_16x16x32_bf16 v[132:135], a[132:135], v[72:75], v[132:135]\n"
    "v_exp_f32 v17, v17\n"
    "v_mfma_f32_16x16x32_bf16 v[156:159], a[116:119], v[88:91], v[156:159]\n"
    "v_cvt_pk_u16_u32 v212, v212, v216\n"
    "v_mfma_f32_16x16x32_bf16 v[172:175], a[116:119], v[96:99], 0\n"
    "s_waitcnt lgkmcnt(1)\n"
    "v_mfma_f32_16x16x32_bf16 a[16:19], a[148:151], v[88:91], 0\n"
    "v_mfma_f32_16x16x32_bf16 v[176:179], a[124:127], v[96:99], v[176:179]\n"
    "v_mfma_f32_16x16x32_bf16 v[184:187], a[140:143], v[96:99], 0\n"
    "v_mfma_f32_16x16x32_bf16 a[0:3], a[148:151], v[80:83], a[0:3]\n"
    "ds_read_b128 a[124:127], v249 offset:19456\n"
    "v_mfma_f32_16x16x32_bf16 v[180:183], a[132:135], v[96:99], 0\n"
    "v_exp_f32 v34, v34\n"
    "v_mfma_f32_16x16x32_bf16 v[188:191], a[148:151], v[72:75], 0\n"
    "v_or3_b32 v243, v206, v207, v243\n"
    "v_mfma_f32_16x16x32_bf16 a[16:19], a[144:147], v[84:87], a[16:19]\n"
    "v_exp_f32 v33, v33\n"
    "v_mfma_f32_16x16x32_bf16 v[164:167], a[132:135], v[88:91], v[164:167]\n"
    "v_cvt_pk_u16_u32 v213, v213, v217\n"
    "v_mfma_f32_16x16x32_bf16 v[124:127], a[116:119], v[72:75], v[124:127]\n"
    "v_exp_f32 v28, v28\n"
    "v_mfma_f32_16x16x32_bf16 a[4:7], a[152:155], v[76:79], a[4:7]\n"
    "v_exp_f32 v11, v11\n"
    "v_mfma_f32_16x16x32_bf16 v[184:187], a[136:139], v[92:95], v[184:187]\n"
    "v_exp_f32 v23, v23\n"
    "v_mfma_f32_16x16x32_bf16 v[188:191], a[144:147], v[68:71], v[188:191]\n"
    "s_waitcnt lgkmcnt(1)\n"
    "ds_read_b128 a[132:135], v249 offset:20480\n"
    "v_mfma_f32_16x16x32_bf16 a[12:15], a[120:123], v[76:79], 0\n"
    "ds_read_b128 a[156:159], v249 offset:23552\n"
    "v_mfma_f32_16x16x32_bf16 v[24:27], a[72:75], a[216:219], v[24:27]\n"
    "v_exp_f32 v12, v12\n"
    "v_mfma_f32_16x16x32_bf16 v[152:155], a[136:139], v[76:79], v[152:155]\n"
    "ds_read_b128 a[116:119], v249 offset:18432\n"
    "v_mfma_f32_16x16x32_bf16 a[44:47], a[120:123], v[92:95], 0\n"
    "s_waitcnt lgkmcnt(3)\n"
    "v_exp_f32 v10, v10\n"
    "v_mfma_f32_16x16x32_bf16 a[28:31], a[124:127], v[88:91], 0\n"
    "v_mfma_f32_16x16x32_bf16 a[36:39], a[152:155], v[92:95], a[36:39]\n"
    "v_exp_f32 v26, v26\n"
    "v_mfma_f32_16x16x32_bf16 v[180:183], a[128:131], v[92:95], v[180:183]\n"
    "v_exp_f32 v4, v4\n"
    "v_mfma_f32_16x16x32_bf16 v[172:175], a[112:115], v[92:95], v[172:175]\n"
    "ds_read_b128 a[136:139], v249 offset:9216\n"
    "ds_read_b128 a[128:131], v249 offset:8192\n"
    "v_mfma_f32_16x16x32_bf16 a[32:35], a[144:147], v[92:95], 0\n"
    "v_exp_f32 v30, v30\n"
    "v_mfma_f32_16x16x32_bf16 a[44:47], a[124:127], v[96:99], a[44:47]\n"
    "s_waitcnt lgkmcnt(4)\n"
    "v_exp_f32 v35, v35\n"
    "v_mfma_f32_16x16x32_bf16 a[64:67], a[132:135], v[80:83], 0\n"
    "buffer_load_dwordx2 v[236:237], v241, s[36:39], s81 offen offset:0 sc1\n"
    "v_mfma_f32_16x16x32_bf16 a[28:31], a[120:123], v[84:87], a[28:31]\n"
    "ds_read_b128 a[112:115], v249 offset:6144\n"
    "v_or3_b32 v243, v212, v213, v243\n"
    "v_mfma_f32_16x16x32_bf16 v[200:203], a[124:127], v[72:75], 0\n"
    "buffer_store_dword v204, v244, s[20:23], s76 offen offset:0 sc1\n"
    "v_exp_f32 v14, v14\n"
    "v_mfma_f32_16x16x32_bf16 v[200:203], a[120:123], v[68:71], v[200:203]\n"
    "v_mfma_f32_16x16x32_bf16 a[32:35], a[148:151], v[96:99], a[32:35]\n"
    "ds_read_b128 a[144:147], v249 offset:10240\n"
    "ds_read_b128 a[152:155], v249 offset:11264\n"
    "v_mfma_f32_16x16x32_bf16 v[168:171], a[140:143], v[88:91], v[168:171]\n"
    "s_waitcnt lgkmcnt(3)\n"
    "v_exp_f32 v16, v16\n"
    "v_mfma_f32_16x16x32_bf16 a[64:67], a[128:131], v[76:79], a[64:67]\n"
    "v_cvt_pk_u16_u32 v223, v223, v227\n"
    "v_mfma_f32_16x16x32_bf16 a[8:11], a[116:119], v[80:83], 0\n"
    "buffer_store_dword v205, v244, s[20:23], s76 offen offset:1024 sc1\n"
    "v_exp_f32 v31, v31\n"
    "v_mfma_f32_16x16x32_bf16 a[48:51], a[128:131], v[68:71], 0\n"
    "s_waitcnt lgkmcnt(2)\n"
    "v_mfma_f32_16x16x32_bf16 v[196:199], a[112:115], v[68:71], 0\n"
    "v_mfma_f32_16x16x32_bf16 a[40:43], a[116:119], v[96:99], 0\n"
    "ds_read_b128 a[140:143], v249 offset:21504\n"
    "v_mfma_f32_16x16x32_bf16 a[24:27], a[112:115], v[84:87], 0\n"
    "ds_read_b128 v[100:103], v250 offset:1664\n"
    "v_cvt_pk_u16_u32 v222, v222, v226\n"
    "v_mfma_f32_16x16x32_bf16 a[80:83], a[128:131], v[84:87], 0\n"
    "v_mfma_f32_16x16x32_bf16 a[8:11], a[112:115], v[76:79], a[8:11]\n"
    "buffer_store_dword v206, v244, s[20:23], s76 offen offset:2048 sc1\n"
    "v_mfma_f32_16x16x32_bf16 a[40:43], a[112:115], v[92:95], a[40:43]\n"
    "ds_read_b128 a[148:151], v249 offset:22528\n"
    "v_mfma_f32_16x16x32_bf16 a[12:15], a[124:127], v[80:83], a[12:15]\n"
    "v_mov_b32 v2, v3\n"
    "v_mfma_f32_16x16x32_bf16 a[96:99], a[132:135], v[96:99], 0\n"
    "s_waitcnt lgkmcnt(3)\n"
    "v_mfma_f32_16x16x32_bf16 a[60:63], a[152:155], v[68:71], 0\n"
    "v_mfma_f32_16x16x32_bf16 a[96:99], a[128:131], v[92:95], a[96:99]\n"
    "buffer_store_dword v207, v244, s[20:23], s76 offen offset:3072 sc1\n"
    "ds_read_b128 a[128:131], v3 offset:20480\n"
    "ds_read_b128 a[112:115], v3 offset:12288\n"
    "v_mfma_f32_16x16x32_bf16 a[80:83], a[132:135], v[88:91], a[80:83]\n"
    "ds_read_b128 a[120:123], v3 offset:16384\n"
    "v_or3_b32 v243, v214, v215, v243\n"
    "v_mfma_f32_16x16x32_bf16 a[48:51], a[132:135], v[72:75], a[48:51]\n"
    "ds_read_b128 a[124:127], v3 offset:18432\n"
    "v_mfma_f32_16x16x32_bf16 v[196:199], a[116:119], v[72:75], v[196:199]\n"
    "buffer_store_dword v212, v244, s[20:23], s77 offen offset:0 sc1\n"
    "v_mfma_f32_16x16x32_bf16 a[108:111], a[156:159], v[96:99], 0\n"
    "v_mfma_f32_16x16x32_bf16 a[92:95], a[152:155], v[84:87], 0\n"
    "v_exp_f32 v19, v19\n"
    "v_mfma_f32_16x16x32_bf16 a[52:55], a[136:139], v[68:71], 0\n"
    "buffer_store_dword v213, v244, s[20:23], s77 offen offset:1024 sc1\n"
    "v_or3_b32 v243, v220, v221, v243\n"
    "v_or3_b32 v243, v222, v223, v243\n"
    "v_mfma_f32_16x16x32_bf16 a[84:87], a[136:139], v[84:87], 0\n"
    "v_exp_f32 v32, v32\n"
    "v_mfma_f32_16x16x32_bf16 a[92:95], a[156:159], v[88:91], a[92:95]\n"
    "s_waitcnt lgkmcnt(4)\n"
    "buffer_store_dword v214, v244, s[20:23], s77 offen offset:2048 sc1\n"
    "v_and_b32 v208, 0xff00ff00, v243\n"
    "v_mfma_f32_16x16x32_bf16 a[88:91], a[148:151], v[88:91], 0\n"
    "v_exp_f32 v9, v9\n"
    "v_mfma_f32_16x16x32_bf16 a[24:27], a[116:119], v[88:91], a[24:27]\n"
    "buffer_store_dword v215, v244, s[20:23], s77 offen offset:3072 sc1\n"
    "v_mfma_f32_16x16x32_bf16 a[76:79], a[156:159], v[80:83], 0\n"
    "v_exp_f32 v15, v15\n"
    "v_mfma_f32_16x16x32_bf16 a[100:103], a[140:143], v[96:99], 0\n"
    "v_mfma_f32_16x16x32_bf16 a[52:55], a[140:143], v[72:75], a[52:55]\n"
    "ds_read_b128 a[132:135], v3 offset:22528\n"
    "v_mfma_f32_16x16x32_bf16 a[108:111], a[152:155], v[92:95], a[108:111]\n"
    "buffer_store_dword v220, v244, s[20:23], s78 offen offset:0 sc1\n"
    "v_exp_f32 v18, v18\n"
    "v_mfma_f32_16x16x32_bf16 a[60:63], a[156:159], v[72:75], a[60:63]\n"
    "v_exp_f32 v8, v8\n"
    "v_mfma_f32_16x16x32_bf16 a[72:75], a[144:147], v[76:79], 0\n"
    "v_exp_f32 v25, v25\n"
    "v_mfma_f32_16x16x32_bf16 a[104:107], a[144:147], v[92:95], 0\n"
    "ds_read_b128 a[116:119], v3 offset:14336\n"
    "v_add_u32 v251, 1536, v251\n"
    "v_mfma_f32_16x16x32_bf16 a[84:87], a[140:143], v[88:91], a[84:87]\n"
    "s_waitcnt vmcnt(10)\n"
    "buffer_store_dword v221, v244, s[20:23], s78 offen offset:1024 sc1\n"
    "ds_add_u32 v247, v246\n"
    "v_mfma_f32_16x16x32_bf16 a[104:107], a[148:151], v[96:99], a[104:107]\n"
    "v_exp_f32 v29, v29\n"
    "v_mfma_f32_16x16x32_bf16 a[68:71], a[140:143], v[80:83], 0\n"
    "v_add_u32 v3, 49152, v252\n"
    "v_smfmac_f32_16x16x64_bf16 v[120:123], v[52:55], v[92:99], v253\n"
    "v_mfma_f32_16x16x32_bf16 a[72:75], a[148:151], v[80:83], a[72:75]\n"
    "buffer_store_dword v222, v244, s[20:23], s78 offen offset:2048 sc1\n"
    "v_add_u32 v250, 1536, v250\n"
    "v_cmp_ne_u32 vcc, 0, v208\n"
    "v_mfma_f32_16x16x32_bf16 a[56:59], a[148:151], v[72:75], 0\n"
    "v_mfma_f32_16x16x32_bf16 a[56:59], a[144:147], v[68:71], a[56:59]\n"
    "v_exp_f32 v27, v27\n"
    "v_mfma_f32_16x16x32_bf16 a[100:103], a[136:139], v[92:95], a[100:103]\n"
    "buffer_store_dword v223, v244, s[20:23], s78 offen offset:3072 sc1\n"
    "v_cndmask_b32 v208, 2.0, v241, vcc\n"
    "v_mfma_f32_16x16x32_bf16 a[68:71], a[136:139], v[76:79], a[68:71]\n"
    "v_exp_f32 v24, v24\n"
    "v_mfma_f32_16x16x32_bf16 a[88:91], a[144:147], v[84:87], a[88:91]\n"
    "s_waitcnt lgkmcnt(1)\n"
    "buffer_store_dwordx2 v[238:239], v208, s[36:39], 0 offen offset:0 sc1\n"
    "v_mfma_f32_16x16x32_bf16 a[76:79], a[152:155], v[76:79], a[76:79]\n"
    "s_branch .LPV\n"
    ".fill ((0x20000 - (.LwKe - .LwK) - (. - _Z8attn_fwdPKtPKiS2_S0_S0_S0_S0_PcPt)) & 4095) / 4, 4, 0xBF800000\n"
    ".LwK:\n"
    "s_branch .LwKe\n"
    ".LPK:\n"
    "v_mfma_f32_16x16x32_bf16 v[36:39], a[112:115], a[192:195], v[100:103]\n"
    "s_add_u32 s71, s71, 98304\n"
    "ds_read_b128 a[136:139], v2 offset:13312\n"
    "v_cvt_pk_bf16_f32 v68, v4, v5\n"
    "v_mfma_f32_16x16x32_bf16 v[36:39], a[116:119], a[196:199], v[36:39]\n"
    "s_add_u32 s75, s71, s63\n"
    "ds_read_b128 a[140:143], v2 offset:15360\n"
    "v_cvt_pk_bf16_f32 v69, v6, v7\n"
    "v_mfma_f32_16x16x32_bf16 v[36:39], a[120:123], a[200:203], v[36:39]\n"
    "s_add_u32 s76, s75, 6144\n"
    "ds_read_b128 a[144:147], v2 offset:17408\n"
    "v_cvt_pk_bf16_f32 v76, v8, v9\n"
    "s_nop 0\n"
    "v_mfma_f32_16x16x32_bf16 v[36:39], a[124:127], a[204:207], v[36:39]\n"
    "s_add_u32 s77, s75, 24576\n"
    "ds_read_b128 a[148:151], v2 offset:19456\n"
    "v_cvt_pk_bf16_f32 v77, v10, v11\n"
    "v_mfma_f32_16x16x32_bf16 v[36:39], a[128:131], a[208:211], v[36:39]\n"
    "s_add_u32 s78, s75, 30720\n"
    "ds_read_b128 a[152:155], v2 offset:21504\n"
    "v_cvt_pk_bf16_f32 v84, v12, v13\n"
    "v_mfma_f32_16x16x32_bf16 v[36:39], a[132:135], a[212:215], v[36:39]\n"
    "ds_read_b128 a[156:159], v2 offset:23552\n"
    "v_cvt_pk_bf16_f32 v85, v14, v15\n"
    "v_mfma_f32_16x16x32_bf16 v[40:43], a[132:135], a[236:239], v[100:103]\n"
    "ds_read_b128 v[104:107], v250 offset:144\n"
    "v_cvt_pk_bf16_f32 v92, v16, v17\n"
    "v_mfma_f32_16x16x32_bf16 v[40:43], a[128:131], a[232:235], v[40:43]\n"
    "s_add_u32 s68, s68, 24576\n"
    "v_cvt_pk_bf16_f32 v93, v18, v19\n"
    "v_cvt_pk_bf16_f32 v70, v20, v21\n"
    "v_mfma_f32_16x16x32_bf16 v[40:43], a[124:127], a[228:231], v[40:43]\n"
    "s_cmp_ge_u32 s68, s69\n"
    "s_cselect_b32 s68, s65, s68\n"
    "v_cvt_pk_bf16_f32 v71, v22, v23\n"
    "v_mfma_f32_16x16x32_bf16 v[40:43], a[120:123], a[224:227], v[40:43]\n"
    "v_cvt_pk_bf16_f32 v78, v24, v25\n"
    "v_cvt_pk_bf16_f32 v79, v26, v27\n"
    "v_mfma_f32_16x16x32_bf16 v[40:43], a[116:119], a[220:223], v[40:43]\n"
    "v_cvt_pk_bf16_f32 v86, v28, v29\n"
    "v_cvt_pk_bf16_f32 v87, v30, v31\n"
    "v_mfma_f32_16x16x32_bf16 v[40:43], a[112:115], a[216:219], v[40:43]\n"
    "v_cvt_pk_bf16_f32 v94, v32, v33\n"
    "v_cvt_pk_bf16_f32 v95, v34, v35\n"
    "v_mfma_f32_16x16x32_bf16 v[44:47], a[112:115], a[240:243], v[100:103]\n"
    "v_exp_f32 v36, v36\n"
    "v_mfma_f32_16x16x32_bf16 v[44:47], a[116:119], a[244:247], v[44:47]\n"
    "v_exp_f32 v37, v37\n"
    "v_mfma_f32_16x16x32_bf16 v[44:47], a[120:123], a[248:251], v[44:47]\n"
    "v_exp_f32 v38, v38\n"
    "v_mfma_f32_16x16x32_bf16 v[44:47], a[124:127], a[252:255], v[44:47]\n"
    "v_exp_f32 v39, v39\n"
    "v_mfma_f32_16x16x32_bf16 v[44:47], a[128:131], a[160:163], v[44:47]\n"
    "v_cvt_pk_bf16_f32 v72, v36, v37\n"
    "v_cvt_pk_bf16_f32 v73, v38, v39\n"
    "v_mfma_f32_16x16x32_bf16 v[44:47], a[132:135], a[164:167], v[44:47]\n"
    "v_exp_f32 v40, v40\n"
    "v_mfma_f32_16x16x32_bf16 v[48:51], a[132:135], a[188:191], v[100:103]\n"
    "ds_read_b32 v245, v247\n"
    "v_mfma_f32_16x16x32_bf16 v[48:51], a[128:131], a[184:187], v[48:51]\n"
    "s_add_u32 m0, s68, 0\n"
    "v_exp_f32 v41, v41\n"
    "v_mfma_f32_16x16x32_bf16 v[48:51], a[124:127], a[180:183], v[48:51]\n"
    "v_exp_f32 v42, v42\n"
    "v_mfma_f32_16x16x32_bf16 v[48:51], a[120:123], a[176:179], v[48:51]\n"
    "v_exp_f32 v43, v43\n"
    "v_mfma_f32_16x16x32_bf16 v[48:51], a[116:119], a[172:175], v[48:51]\n"
    "s_waitcnt vmcnt(10)\n"
    "v_cmp_eq_u64 s[54:55], v[236:237], v[238:239]\n"
    "v_cvt_pk_bf16_f32 v80, v40, v41\n"
    "v_mfma_f32_16x16x32_bf16 v[48:51], a[112:115], a[168:171], v[48:51]\n"
    "s_waitcnt lgkmcnt(1)\n"
    "v_cvt_pk_bf16_f32 v81, v42, v43\n"
    "v_mfma_f32_16x16x32_bf16 v[52:55], a[136:139], a[192:195], v[104:107]\n"
    "s_waitcnt lgkmcnt(0)\n"
    "v_cmp_le_u32 s[56:57], s59, v245\n"
    "v_mfma_f32_16x16x32_bf16 v[52:55], a[140:143], a[196:199], v[52:55]\n"
    "v_exp_f32 v44, v44\n"
    "v_mfma_f32_16x16x32_bf16 v[52:55], a[144:147], a[200:203], v[52:55]\n"
    "s_and_b64 s[54:55], s[54:55], s[56:57]\n"
    "s_cmp_eq_u64 s[54:55], -1\n"
    "v_mfma_f32_16x16x32_bf16 v[52:55], a[148:151], a[204:207], v[52:55]\n"
    "s_cbranch_scc0 .Lpoll1\n"
    ".Lpoll1r:\n"
    "v_mfma_f32_16x16x32_bf16 v[52:55], a[152:155], a[208:211], v[52:55]\n"
    "buffer_load_dwordx4 v1, s[16:19], s67 offen offset:0 sc1 lds\n"
    "v_mfma_f32_16x16x32_bf16 v[52:55], a[156:159], a[212:215], v[52:55]\n"
    "ds_read_b128 a[112:115], v3 offset:0\n"
    "v_mfma_f32_16x16x32_bf16 v[56:59], a[156:159], a[236:239], v[104:107]\n"
    "ds_read_b128 a[116:119], v3 offset:2048\n"
    "v_mfma_f32_16x16x32_bf16 v[56:59], a[152:155], a[232:235], v[56:59]\n"
    "ds_read_b128 a[120:123], v3 offset:4096\n"
    "v_mfma_f32_16x16x32_bf16 v[56:59], a[148:151], a[228:231], v[56:59]\n"
    "buffer_load_dwordx4 v1, s[16:19], s67 offen offset:1024 sc1 lds\n"
    "v_mfma_f32_16x16x32_bf16 v[56:59], a[144:147], a[224:227], v[56:59]\n"
    "ds_read_b128 a[124:127], v3 offset:6144\n"
    "v_mfma_f32_16x16x32_bf16 v[56:59], a[140:143], a[220:223], v[56:59]\n"
    "ds_read_b128 a[128:131], v3 offset:8192\n"
    "v_mfma_f32_16x16x32_bf16 v[56:59], a[136:139], a[216:219], v[56:59]\n"
    "ds_read_b128 a[132:135], v3 offset:10240\n"
    "v_mfma_f32_16x16x32_bf16 v[60:63], a[136:139], a[240:243], v[104:107]\n"
    "buffer_load_dwordx4 v1, s[16:19], s67 offen offset:2048 sc1 lds\n"
    "v_mfma_f32_16x16x32_bf16 v[60:63], a[140:143], a[244:247], v[60:63]\n"
    "ds_read_b128 v[100:103], v250 offset:768\n"
    "v_mfma_f32_16x16x32_bf16 v[60:63], a[144:147], a[248:251], v[60:63]\n"
    "v_exp_f32 v52, v52\n"
    "v_mfma_f32_16x16x32_bf16 v[60:63], a[148:151], a[252:255], v[60:63]\n"
    "v_exp_f32 v53, v53\n"
    "v_mfma_f32_16x16x32_bf16 v[60:63], a[152:155], a[160:163], v[60:63]\n"
    "buffer_load_dwordx4 v1, s[16:19], s67 offen offset:3072 sc1 lds\n"
    "v_mfma_f32_16x16x32_bf16 v[60:63], a[156:159], a[164:167], v[60:63]\n"
    "s_add_u32 m0, s68, 4096\n"
    "v_exp_f32 v54, v54\n"
    "v_mfma_f32_16x16x32_bf16 v[64:67], a[156:159], a[188:191], v[104:107]\n"
    "s_add_u32 s83, s67, 4096\n"
    "v_exp_f32 v55, v55\n"
    "v_mfma_f32_16x16x32_bf16 v[64:67], a[152:155], a[184:187], v[64:67]\n"
    "v_cvt_pk_bf16_f32 v74, v52, v53\n"
    "v_cvt_pk_bf16_f32 v75, v54, v55\n"
    "v_mfma_f32_16x16x32_bf16 v[64:67], a[148:151], a[180:183], v[64:67]\n"
    "buffer_load_dwordx4 v1, s[16:19], s83 offen offset:0 sc1 lds\n"
    "v_mfma_f32_16x16x32_bf16 v[64:67], a[144:147], a[176:179], v[64:67]\n"
    "v_exp_f32 v56, v56\n"
    "v_mfma_f32_16x16x32_bf16 v[64:67], a[140:143], a[172:175], v[64:67]\n"
    "v_exp_f32 v57, v57\n"
    "v_mfma_f32_16x16x32_bf16 v[64:67], a[136:139], a[168:171], v[64:67]\n"
    "s_waitcnt lgkmcnt(0)\n"
    "v_exp_f32 v58, v58\n"
    "v_mfma_f32_16x16x32_bf16 v[4:7], a[112:115], a[192:195], v[100:103]\n"
    "buffer_load_dwordx4 v1, s[16:19], s83 offen offset:1024 sc1 lds\n"
    "v_mfma_f32_16x16x32_bf16 v[4:7], a[116:119], a[196:199], v[4:7]\n"
    "s_add_u32 m0, s64, 0\n"
    "ds_read_b128 a[136:139], v3 offset:1024\n"
    "v_cvt_pk_bf16_f32 v82, v56, v57\n"
    "v_mfma_f32_16x16x32_bf16 v[4:7], a[120:123], a[200:203], v[4:7]\n"
    "s_add_u32 s83, s67, 24576\n"
    "ds_read_b128 a[140:143], v3 offset:3072\n"
    "v_mfma_f32_16x16x32_bf16 v[4:7], a[124:127], a[204:207], v[4:7]\n"
    "ds_read_b128 a[144:147], v3 offset:5120\n"
    "v_mfma_f32_16x16x32_bf16 v[4:7], a[128:131], a[208:211], v[4:7]\n"
    "buffer_load_dwordx4 v1, s[16:19], s83 offen offset:0 sc1 lds\n"
    "v_mfma_f32_16x16x32_bf16 v[4:7], a[132:135], a[212:215], v[4:7]\n"
    "ds_read_b128 a[148:151], v3 offset:7168\n"
    "v_mfma_f32_16x16x32_bf16 v[8:11], a[132:135], a[236:239], v[100:103]\n"
    "ds_read_b128 a[152:155], v3 offset:9216\n"
    "v_mfma_f32_16x16x32_bf16 v[8:11], a[128:131], a[232:235], v[8:11]\n"
    "ds_read_b128 a[156:159], v3 offset:11264\n"
    "v_mfma_f32_16x16x32_bf16 v[8:11], a[124:127], a[228:231], v[8:11]\n"
    "buffer_load_dwordx4 v1, s[16:19], s83 offen offset:1024 sc1 lds\n"
    "v_mfma_f32_16x16x32_bf16 v[8:11], a[120:123], a[224:227], v[8:11]\n"
    "ds_read_b128 v[104:107], v250 offset:784\n"
    "v_mfma_f32_16x16x32_bf16 v[8:11], a[116:119], a[220:223], v[8:11]\n"
    "v_exp_f32 v59, v59\n"
    "v_mfma_f32_16x16x32_bf16 v[8:11], a[112:115], a[216:219], v[8:11]\n"
    "v_cvt_pk_bf16_f32 v83, v58, v59\n"
    "v_mfma_f32_16x16x32_bf16 v[12:15], a[112:115], a[240:243], v[100:103]\n"
    "buffer_load_dwordx4 v1, s[16:19], s83 offen offset:2048 sc1 lds\n"
    "v_mfma_f32_16x16x32_bf16 v[12:15], a[116:119], a[244:247], v[12:15]\n"
    "v_exp_f32 v45, v45\n"
    "v_mfma_f32_16x16x32_bf16 v[12:15], a[120:123], a[248:251], v[12:15]\n"
    "v_exp_f32 v46, v46\n"
    "v_mfma_f32_16x16x32_bf16 v[12:15], a[124:127], a[252:255], v[12:15]\n"
    "v_exp_f32 v47, v47\n"
    "v_mfma_f32_16x16x32_bf16 v[12:15], a[128:131], a[160:163], v[12:15]\n"
    "buffer_load_dwordx4 v1, s[16:19], s83 offen offset:3072 sc1 lds\n"
    "v_mfma_f32_16x16x32_bf16 v[12:15], a[132:135], a[164:167], v[12:15]\n"
    "s_add_u32 m0, s64, 4096\n"
    "v_exp_f32 v60, v60\n"
    "v_mfma_f32_16x16x32_bf16 v[16:19], a[132:135], a[188:191], v[100:103]\n"
    "s_add_u32 s83, s67, 28672\n"
    "v_exp_f32 v61, v61\n"
    "v_mfma_f32_16x16x32_bf16 v[16:19], a[128:131], a[184:187], v[16:19]\n"
    "v_exp_f32 v62, v62\n"
    "v_mfma_f32_16x16x32_bf16 v[16:19], a[124:127], a[180:183], v[16:19]\n"
    "buffer_load_dwordx4 v1, s[16:19], s83 offen offset:0 sc1 lds\n"
    "v_mfma_f32_16x16x32_bf16 v[16:19], a[120:123], a[176:179], v[16:19]\n"
    "v_exp_f32 v63, v63\n"
    "v_mfma_f32_16x16x32_bf16 v[16:19], a[116:119], a[172:175], v[16:19]\n"
    "v_cvt_pk_bf16_f32 v88, v44, v45\n"
    "v_cvt_pk_bf16_f32 v89, v46, v47\n"
    "v_mfma_f32_16x16x32_bf16 v[16:19], a[112:115], a[168:171], v[16:19]\n"
    "s_waitcnt lgkmcnt(0)\n"
    "v_cvt_pk_bf16_f32 v90, v60, v61\n"
    "v_cvt_pk_bf16_f32 v91, v62, v63\n"
    "v_mfma_f32_16x16x32_bf16 v[20:23], a[136:139], a[192:195], v[104:107]\n"
    "buffer_load_dwordx4 v1, s[16:19], s83 offen offset:1024 sc1 lds\n"
    "v_mfma_f32_16x16x32_bf16 v[20:23], a[140:143], a[196:199], v[20:23]\n"
    "s_add_u32 s67, s67, 49152\n"
    "ds_read_b128 a[112:115], v248 offset:0\n"
    "v_mfma_f32_16x16x32_bf16 v[20:23], a[144:147], a[200:203], v[20:23]\n"
    "s_add_u32 s59, s59, 4\n"
    "ds_read_b128 a[116:119], v248 offset:12288\n"
    "v_mfma_f32_16x16x32_bf16 v[20:23], a[148:151], a[204:207], v[20:23]\n"
    "s_waitcnt vmcnt(12)\n"
    "ds_read_b128 a[120:123], v248 offset:1024\n"
    "v_mfma_f32_16x16x32_bf16 v[20:23], a[152:155], a[208:211], v[20:23]\n"
    "buffer_store_dwordx2 v[238:239], v240, s[36:39], s80 offen offset:0 sc1\n"
    "v_mfma_f32_16x16x32_bf16 v[20:23], a[156:159], a[212:215], v[20:23]\n"
    "buffer_load_dwordx2 v[204:205], v242, s[28:31], s75 offen offset:0 nt\n"
    "v_mfma_f32_16x16x32_bf16 v[24:27], a[156:159], a[236:239], v[104:107]\n"
    "s_add_u32 s80, s80, 256\n"
    "ds_read_b128 a[124:127], v248 offset:13312\n"
    "v_mfma_f32_16x16x32_bf16 v[24:27], a[152:155], a[232:235], v[24:27]\n"
    "buffer_load_dwordx2 v[206:207], v242, s[28:31], s75 offen offset:768 nt\n"
    "v_mfma_f32_16x16x32_bf16 v[24:27], a[148:151], a[228:231], v[24:27]\n"
    "ds_read_b128 a[128:131], v248 offset:2048\n"
    "v_mfma_f32_16x16x32_bf16 v[24:27], a[144:147], a[224:227], v[24:27]\n"
    "buffer_load_dwordx2 v[208:209], v242, s[28:31], s75 offen offset:3072 nt\n"
    "v_mfma_f32_16x16x32_bf16 v[24:27], a[140:143], a[220:223], v[24:27]\n"
    "ds_read_b128 a[132:135], v248 offset:14336\n"
    "v_mfma_f32_16x16x32_bf16 v[24:27], a[136:139], a[216:219], v[24:27]\n"
    "buffer_load_dwordx2 v[210:211], v242, s[28:31], s75 offen offset:3840 nt\n"
    "v_mfma_f32_16x16x32_bf16 v[28:31], a[136:139], a[240:243], v[104:107]\n"
    "v_exp_f32 v48, v48\n"
    "v_mfma_f32_16x16x32_bf16 v[28:31], a[140:143], a[244:247], v[28:31]\n"
    "buffer_load_dwordx2 v[212:213], v242, s[28:31], s76 offen offset:0 nt\n"
    "v_mfma_f32_16x16x32_bf16 v[28:31], a[144:147], a[248:251], v[28:31]\n"
    "v_exp_f32 v49, v49\n"
    "v_mfma_f32_16x16x32_bf16 v[28:31], a[148:151], a[252:255], v[28:31]\n"
    "buffer_load_dwordx2 v[214:215], v242, s[28:31], s76 offen offset:768 nt\n"
    "v_mfma_f32_16x16x32_bf16 v[28:31], a[152:155], a[160:163], v[28:31]\n"
    "v_exp_f32 v50, v50\n"
    "v_mfma_f32_16x16x32_bf16 v[28:31], a[156:159], a[164:167], v[28:31]\n"
    "buffer_load_dwordx2 v[216:217], v242, s[28:31], s76 offen offset:3072 nt\n"
    "v_mfma_f32_16x16x32_bf16 v[32:35], a[156:159], a[188:191], v[104:107]\n"
    "v_exp_f32 v51, v51\n"
    "v_mfma_f32_16x16x32_bf16 v[32:35], a[152:155], a[184:187], v[32:35]\n"
    "buffer_load_dwordx2 v[218:219], v242, s[28:31], s76 offen offset:3840 nt\n"
    "v_mfma_f32_16x16x32_bf16 v[32:35], a[148:151], a[180:183], v[32:35]\n"
    "v_exp_f32 v64, v64\n"
    "v_mfma_f32_16x16x32_bf16 v[32:35], a[144:147], a[176:179], v[32:35]\n"
    "buffer_load_dwordx2 v[220:221], v242, s[28:31], s77 offen offset:0 nt\n"
    "v_mfma_f32_16x16x32_bf16 v[32:35], a[140:143], a[172:175], v[32:35]\n"
    "v_exp_f32 v65, v65\n"
    "v_mfma_f32_16x16x32_bf16 v[32:35], a[136:139], a[168:171], v[32:35]\n"
    "s_waitcnt lgkmcnt(0)\n"
    "v_exp_f32 v66, v66\n"
    "v_mfma_f32_16x16x32_bf16 v[124:127], a[112:115], v[68:71], v[124:127]\n"
    "buffer_load_dwordx2 v[222:223], v242, s[28:31], s77 offen offset:768 nt\n"
    "v_mfma_f32_16x16x32_bf16 v[124:127], a[116:119], v[72:75], v[124:127]\n"
    "ds_read_b128 a[136:139], v248 offset:3072\n"
    "v_cvt_pk_bf16_f32 v96, v48, v49\n"
    "v_mfma_f32_16x16x32_bf16 v[128:131], a[124:127], v[72:75], v[128:131]\n"
    "buffer_load_dwordx2 v[224:225], v242, s[28:31], s77 offen offset:3072 nt\n"
    "v_mfma_f32_16x16x32_bf16 v[128:131], a[120:123], v[68:71], v[128:131]\n"
    "ds_read_b128 a[140:143], v248 offset:15360\n"
    "v_cvt_pk_bf16_f32 v97, v50, v51\n"
    "v_mfma_f32_16x16x32_bf16 v[132:135], a[128:131], v[68:71], v[132:135]\n"
    "buffer_load_dwordx2 v[226:227], v242, s[28:31], s77 offen offset:3840 nt\n"
    "v_mfma_f32_16x16x32_bf16 v[132:135], a[132:135], v[72:75], v[132:135]\n"
    "ds_read_b128 a[144:147], v248 offset:4096\n"
    "v_cvt_pk_bf16_f32 v98, v64, v65\n"
    "v_mfma_f32_16x16x32_bf16 v[148:151], a[132:135], v[80:83], v[148:151]\n"
    "buffer_load_dwordx2 v[228:229], v242, s[28:31], s78 offen offset:0 nt\n"
    "v_mfma_f32_16x16x32_bf16 v[148:151], a[128:131], v[76:79], v[148:151]\n"
    "ds_read_b128 a[148:151], v248 offset:16384\n"
    "v_mfma_f32_16x16x32_bf16 v[144:147], a[120:123], v[76:79], v[144:147]\n"
    "buffer_load_dwordx2 v[230:231], v242, s[28:31], s78 offen offset:768 nt\n"
    "v_mfma_f32_16x16x32_bf16 v[144:147], a[124:127], v[80:83], v[144:147]\n"
    "ds_read_b128 a[152:155], v248 offset:5120\n"
    "v_mfma_f32_16x16x32_bf16 v[140:143], a[116:119], v[80:83], v[140:143]\n"
    "buffer_load_dwordx2 v[232:233], v242, s[28:31], s78 offen offset:3072 nt\n"
    "v_mfma_f32_16x16x32_bf16 v[140:143], a[112:115], v[76:79], v[140:143]\n"
    "ds_read_b128 a[156:159], v248 offset:17408\n"
    "v_mfma_f32_16x16x32_bf16 v[156:159], a[112:115], v[84:87], v[156:159]\n"
    "buffer_load_dwordx2 v[234:235], v242, s[28:31], s78 offen offset:3840 nt\n"
    "v_mfma_f32_16x16x32_bf16 v[156:159], a[116:119], v[88:91], v[156:159]\n"
    "ds_read_b128 v[104:107], v251 offset:0\n"
    "v_mfma_f32_16x16x32_bf16 v[160:163], a[124:127], v[88:91], v[160:163]\n"
    "v_exp_f32 v67, v67\n"
    "v_mfma_f32_16x16x32_bf16 v[160:163], a[120:123], v[84:87], v[160:163]\n"
    "v_cvt_pk_bf16_f32 v99, v66, v67\n"
    "v_mfma_f32_16x16x32_bf16 v[164:167], a[128:131], v[84:87], v[164:167]\n"
    "v_exp_f32 v4, v4\n"
    "v_mfma_f32_16x16x32_bf16 v[164:167], a[132:135], v[88:91], v[164:167]\n"
    "v_exp_f32 v5, v5\n"
    "v_mfma_f32_16x16x32_bf16 v[180:183], a[132:135], v[96:99], v[180:183]\n"
    "v_exp_f32 v6, v6\n"
    "v_mfma_f32_16x16x32_bf16 v[180:183], a[128:131], v[92:95], v[180:183]\n"
    "v_exp_f32 v7, v7\n"
    "v_mfma_f32_16x16x32_bf16 v[176:179], a[120:123], v[92:95], v[176:179]\n"
    "v_exp_f32 v8, v8\n"
    "v_mfma_f32_16x16x32_bf16 v[176:179], a[124:127], v[96:99], v[176:179]\n"
    "v_exp_f32 v9, v9\n"
    "v_mfma_f32_16x16x32_bf16 v[172:175], a[116:119], v[96:99], v[172:175]\n"
    "v_exp_f32 v10, v10\n"
    "v_mfma_f32_16x16x32_bf16 v[172:175], a[112:115], v[92:95], v[172:175]\n"
    "s_waitcnt lgkmcnt(0)\n"
    "v_exp_f32 v11, v11\n"
    "v_mfma_f32_16x16x32_bf16 v[136:139], a[136:139], v[68:71], v[136:139]\n"
    "ds_read_b128 a[112:115], v248 offset:6144\n"
    "v_mfma_f32_16x16x32_bf16 v[136:139], a[140:143], v[72:75], v[136:139]\n"
    "ds_read_b128 a[116:119], v248 offset:18432\n"
    "v_mfma_f32_16x16x32_bf16 v[188:191], a[148:151], v[72:75], v[188:191]\n"
    "ds_read_b128 a[120:123], v248 offset:7168\n"
    "v_mfma_f32_16x16x32_bf16 v[188:191], a[144:147], v[68:71], v[188:191]\n"
    "ds_read_b128 a[124:127], v248 offset:19456\n"
    "v_mfma_f32_16x16x32_bf16 v[192:195], a[152:155], v[68:71], v[192:195]\n"
    "ds_read_b128 a[128:131], v248 offset:8192\n"
    "v_mfma_f32_16x16x32_bf16 v[192:195], a[156:159], v[72:75], v[192:195]\n"
    "ds_read_b128 a[132:135], v248 offset:20480\n"
    "v_smfmac_f32_16x16x64_bf16 v[108:111], v[104:107], v[68:75], v253\n"
    "s_add_u32 s81, s81, 128\n"
    "s_cmp_gt_u32 s81, 4096\n"
    "s_cselect_b32 s81, 0, s81\n"
    "v_smfmac_f32_16x16x64_bf16 v[112:115], v[104:107], v[76:83], v253\n"
    "v_mfma_f32_16x16x32_bf16 a[4:7], a[156:159], v[80:83], a[4:7]\n"
    "v_exp_f32 v12, v12\n"
    "v_mfma_f32_16x16x32_bf16 a[4:7], a[152:155], v[76:79], a[4:7]\n"
    "v_exp_f32 v13, v13\n"
    "v_mfma_f32_16x16x32_bf16 a[0:3], a[144:147], v[76:79], a[0:3]\n"
    "v_exp_f32 v14, v14\n"
    "v_mfma_f32_16x16x32_bf16 a[0:3], a[148:151], v[80:83], a[0:3]\n"
    "v_exp_f32 v15, v15\n"
    "v_mfma_f32_16x16x32_bf16 v[152:155], a[140:143], v[80:83], v[152:155]\n"
    "v_exp_f32 v16, v16\n"
    "v_mfma_f32_16x16x32_bf16 v[152:155], a[136:139], v[76:79], v[152:155]\n"
    "v_mfma_f32_16x16x32_bf16 v[168:171], a[136:139], v[84:87], v[168:171]\n"
    "v_exp_f32 v17, v17\n"
    "v_mfma_f32_16x16x32_bf16 v[168:171], a[140:143], v[88:91], v[168:171]\n"
    "v_mfma_f32_16x16x32_bf16 a[16:19], a[148:151], v[88:91], a[16:19]\n"
    "v_mfma_f32_16x16x32_bf16 a[16:19], a[144:147], v[84:87], a[16:19]\n"
    "v_exp_f32 v18, v18\n"
    "v_mfma_f32_16x16x32_bf16 a[20:23], a[152:155], v[84:87], a[20:23]\n"
    "v_mfma_f32_16x16x32_bf16 a[20:23], a[156:159], v[88:91], a[20:23]\n"
    "v_smfmac_f32_16x16x64_bf16 v[116:119], v[104:107], v[84:91], v253\n"
    "v_smfmac_f32_16x16x64_bf16 v[120:123], v[104:107], v[92:99], v253\n"
    "v_mfma_f32_16x16x32_bf16 a[36:39], a[156:159], v[96:99], a[36:39]\n"
    "v_exp_f32 v19, v19\n"
    "v_mfma_f32_16x16x32_bf16 a[36:39], a[152:155], v[92:95], a[36:39]\n"
    "v_exp_f32 v20, v20\n"
    "v_mfma_f32_16x16x32_bf16 a[32:35], a[144:147], v[92:95], a[32:35]\n"
    "v_mfma_f32_16x16x32_bf16 a[32:35], a[148:151], v[96:99], a[32:35]\n"
    "v_exp_f32 v21, v21\n"
    "v_mfma_f32_16x16x32_bf16 v[184:187], a[140:143], v[96:99], v[184:187]\n"
    "v_mfma_f32_16x16x32_bf16 v[184:187], a[136:139], v[92:95], v[184:187]\n"
    "s_waitcnt lgkmcnt(0)\n"
    "v_mfma_f32_16x16x32_bf16 v[196:199], a[112:115], v[68:71], v[196:199]\n"
    "ds_read_b128 a[136:139], v248 offset:9216\n"
    "v_mfma_f32_16x16x32_bf16 v[196:199], a[116:119], v[72:75], v[196:199]\n"
    "ds_read_b128 a[140:143], v248 offset:21504\n"
    "v_mfma_f32_16x16x32_bf16 v[200:203], a[124:127], v[72:75], v[200:203]\n"
    "ds_read_b128 a[144:147], v248 offset:10240\n"
    "v_mfma_f32_16x16x32_bf16 v[200:203], a[120:123], v[68:71], v[200:203]\n"
    "ds_read_b128 a[148:151], v248 offset:22528\n"
    "v_mfma_f32_16x16x32_bf16 a[48:51], a[128:131], v[68:71], a[48:51]\n"
    "buffer_load_dwordx2 v[236:237], v241, s[36:39], s81 offen offset:0 sc1\n"
    "v_mfma_f32_16x16x32_bf16 a[48:51], a[132:135], v[72:75], a[48:51]\n"
    "ds_read_b128 a[152:155], v248 offset:11264\n"
    "v_mfma_f32_16x16x32_bf16 a[64:67], a[132:135], v[80:83], a[64:67]\n"
    "ds_read_b128 a[156:159], v248 offset:23552\n"
    "v_mfma_f32_16x16x32_bf16 a[64:67], a[128:131], v[76:79], a[64:67]\n"
    "v_exp_f32 v22, v22\n"
    "v_mfma_f32_16x16x32_bf16 a[12:15], a[120:123], v[76:79], a[12:15]\n"
    "v_exp_f32 v23, v23\n"
    "v_mfma_f32_16x16x32_bf16 a[12:15], a[124:127], v[80:83], a[12:15]\n"
    "v_exp_f32 v24, v24\n"
    "v_mfma_f32_16x16x32_bf16 a[8:11], a[116:119], v[80:83], a[8:11]\n"
    "v_exp_f32 v25, v25\n"
    "v_mfma_f32_16x16x32_bf16 a[8:11], a[112:115], v[76:79], a[8:11]\n"
    "v_exp_f32 v26, v26\n"
    "v_mfma_f32_16x16x32_bf16 a[24:27], a[112:115], v[84:87], a[24:27]\n"
    "v_mfma_f32_16x16x32_bf16 a[24:27], a[116:119], v[88:91], a[24:27]\n"
    "v_exp_f32 v27, v27\n"
    "v_mfma_f32_16x16x32_bf16 a[28:31], a[124:127], v[88:91], a[28:31]\n"
    "v_mfma_f32_16x16x32_bf16 a[28:31], a[120:123], v[84:87], a[28:31]\n"
    "v_mfma_f32_16x16x32_bf16 a[80:83], a[128:131], v[84:87], a[80:83]\n"
    "v_exp_f32 v28, v28\n"
    "v_mfma_f32_16x16x32_bf16 a[80:83], a[132:135], v[88:91], a[80:83]\n"
    "v_mfma_f32_16x16x32_bf16 a[96:99], a[132:135], v[96:99], a[96:99]\n"
    "v_mfma_f32_16x16x32_bf16 a[96:99], a[128:131], v[92:95], a[96:99]\n"
    "v_exp_f32 v29, v29\n"
    "v_mfma_f32_16x16x32_bf16 a[44:47], a[120:123], v[92:95], a[44:47]\n"
    "v_mfma_f32_16x16x32_bf16 a[44:47], a[124:127], v[96:99], a[44:47]\n"
    "v_exp_f32 v30, v30\n"
    "v_mfma_f32_16x16x32_bf16 a[40:43], a[116:119], v[96:99], a[40:43]\n"
    "v_mfma_f32_16x16x32_bf16 a[40:43], a[112:115], v[92:95], a[40:43]\n"
    "s_waitcnt lgkmcnt(0)\n"
    "v_mfma_f32_16x16x32_bf16 a[52:55], a[136:139], v[68:71], a[52:55]\n"
    "ds_read_b128 a[112:115], v3 offset:12288\n"
    "v_mfma_f32_16x16x32_bf16 a[52:55], a[140:143], v[72:75], a[52:55]\n"
    "ds_read_b128 a[116:119], v3 offset:14336\n"
    "v_mfma_f32_16x16x32_bf16 a[56:59], a[148:151], v[72:75], a[56:59]\n"
    "ds_read_b128 a[120:123], v3 offset:16384\n"
    "v_mfma_f32_16x16x32_bf16 a[56:59], a[144:147], v[68:71], a[56:59]\n"
    "ds_read_b128 a[124:127], v3 offset:18432\n"
    "v_mfma_f32_16x16x32_bf16 a[60:63], a[152:155], v[68:71], a[60:63]\n"
    "ds_read_b128 a[128:131], v3 offset:20480\n"
    "v_mfma_f32_16x16x32_bf16 a[60:63], a[156:159], v[72:75], a[60:63]\n"
    "ds_read_b128 a[132:135], v3 offset:22528\n"
    "v_mfma_f32_16x16x32_bf16 a[76:79], a[156:159], v[80:83], a[76:79]\n"
    "ds_read_b128 v[100:103], v250 offset:896\n"
    "v_mfma_f32_16x16x32_bf16 a[76:79], a[152:155], v[76:79], a[76:79]\n"
    "v_exp_f32 v31, v31\n"
    "v_mfma_f32_16x16x32_bf16 a[72:75], a[144:147], v[76:79], a[72:75]\n"
    "v_exp_f32 v32, v32\n"
    "v_mfma_f32_16x16x32_bf16 a[72:75], a[148:151], v[80:83], a[72:75]\n"
    "v_exp_f32 v33, v33\n"
    "v_mfma_f32_16x16x32_bf16 a[68:71], a[140:143], v[80:83], a[68:71]\n"
    "v_exp_f32 v34, v34\n"
    "v_mfma_f32_16x16x32_bf16 a[68:71], a[136:139], v[76:79], a[68:71]\n"
    "v_exp_f32 v35, v35\n"
    "v_mfma_f32_16x16x32_bf16 a[84:87], a[136:139], v[84:87], a[84:87]\n"
    "v_mfma_f32_16x16x32_bf16 a[84:87], a[140:143], v[88:91], a[84:87]\n"
    "v_mfma_f32_16x16x32_bf16 a[88:91], a[148:151], v[88:91], a[88:91]\n"
    "s_waitcnt vmcnt(18)\n"
    "ds_add_u32 v247, v246\n"
    "v_mfma_f32_16x16x32_bf16 a[88:91], a[144:147], v[84:87], a[88:91]\n"
    "v_mfma_f32_16x16x32_bf16 a[92:95], a[152:155], v[84:87], a[92:95]\n"
    "v_mfma_f32_16x16x32_bf16 a[92:95], a[156:159], v[88:91], a[92:95]\n"
    "v_mfma_f32_16x16x32_bf16 a[108:111], a[156:159], v[96:99], a[108:111]\n"
    "v_mfma_f32_16x16x32_bf16 a[108:111], a[152:155], v[92:95], a[108:111]\n"
    "v_mfma_f32_16x16x32_bf16 a[104:107], a[144:147], v[92:95], a[104:107]\n"
    "s_sub_u32 s74, s68, s65\n"
    "v_mfma_f32_16x16x32_bf16 a[104:107], a[148:151], v[96:99], a[104:107]\n"
    "s_add_u32 s66, s66, 1\n"
    "v_mov_b32 v2, v3\n"
    "v_add_u32_e64 v3, s74, v252\n"
    "v_mfma_f32_16x16x32_bf16 a[100:103], a[140:143], v[96:99], a[100:103]\n"
    "s_waitcnt lgkmcnt(1)\n"
    "s_cmp_eq_u32 s66, .Lend-.Lend2+(29)\n"
    "v_mfma_f32_16x16x32_bf16 a[100:103], a[136:139], v[92:95], a[100:103]\n"
    ".LwKe:\n"
    "s_cbranch_scc1 .LtailP\n"
    ".LNK:\n"
    "v_mfma_f32_16x16x32_bf16 v[36:39], a[112:115], a[192:195], v[100:103]\n"
    "ds_read_b128 a[136:139], v2 offset:13312\n"
    "v_cvt_pk_bf16_f32 v68, v4, v5\n"
    "v_mfma_f32_16x16x32_bf16 v[36:39], a[116:119], a[196:199], v[36:39]\n"
    "ds_read_b128 a[140:143], v2 offset:15360\n"
    "v_mfma_f32_16x16x32_bf16 v[36:39], a[120:123], a[200:203], v[36:39]\n"
    "ds_read_b128 a[144:147], v2 offset:17408\n"
    "v_mfma_f32_16x16x32_bf16 v[36:39], a[124:127], a[204:207], v[36:39]\n"
    "ds_read_b128 a[148:151], v2 offset:19456\n"
    "v_cvt_pk_bf16_f32 v69, v6, v7\n"
    "v_mfma_f32_16x16x32_bf16 v[36:39], a[128:131], a[208:211], v[36:39]\n"
    "ds_read_b128 a[152:155], v2 offset:21504\n"
    "v_mfma_f32_16x16x32_bf16 v[36:39], a[132:135], a[212:215], v[36:39]\n"
    "ds_read_b128 a[156:159], v2 offset:23552\n"
    "v_mfma_f32_16x16x32_bf16 v[40:43], a[132:135], a[236:239], v[100:103]\n"
    "ds_read_b128 v[104:107], v250 offset:912\n"
    "v_mfma_f32_16x16x32_bf16 v[40:43], a[128:131], a[232:235], v[40:43]\n"
    "s_add_u32 s68, s68, 24576\n"
    "v_cvt_pk_bf16_f32 v76, v8, v9\n"
    "v_mfma_f32_16x16x32_bf16 v[40:43], a[124:127], a[228:231], v[40:43]\n"
    "s_cmp_ge_u32 s68, s69\n"
    "s_cselect_b32 s68, s65, s68\n"
    "v_mfma_f32_16x16x32_bf16 v[40:43], a[120:123], a[224:227], v[40:43]\n"
    "v_exp_f32 v36, v36\n"
    "v_mfma_f32_16x16x32_bf16 v[40:43], a[116:119], a[220:223], v[40:43]\n"
    "v_exp_f32 v37, v37\n"
    "v_mfma_f32_16x16x32_bf16 v[40:43], a[112:115], a[216:219], v[40:43]\n"
    "v_cvt_pk_bf16_f32 v77, v10, v11\n"
    "v_cvt_pk_bf16_f32 v72, v36, v37\n"
    "v_mfma_f32_16x16x32_bf16 v[44:47], a[112:115], a[240:243], v[100:103]\n"
    "v_mfma_f32_16x16x32_bf16 v[44:47], a[116:119], a[244:247], v[44:47]\n"
    "v_exp_f32 v38, v38\n"
    "v_mfma_f32_16x16x32_bf16 v[44:47], a[120:123], a[248:251], v[44:47]\n"
    "v_mfma_f32_16x16x32_bf16 v[44:47], a[124:127], a[252:255], v[44:47]\n"
    "v_cvt_pk_bf16_f32 v84, v12, v13\n"
    "v_mfma_f32_16x16x32_bf16 v[44:47], a[128:131], a[160:163], v[44:47]\n"
    "v_exp_f32 v39, v39\n"
    "v_mfma_f32_16x16x32_bf16 v[44:47], a[132:135], a[164:167], v[44:47]\n"
    "v_cvt_pk_bf16_f32 v73, v38, v39\n"
    "v_mfma_f32_16x16x32_bf16 v[48:51], a[132:135], a[188:191], v[100:103]\n"
    "ds_read_b32 v245, v247\n"
    "v_cvt_pk_bf16_f32 v85, v14, v15\n"
    "v_mfma_f32_16x16x32_bf16 v[48:51], a[128:131], a[184:187], v[48:51]\n"
    "s_add_u32 m0, s68, 0\n"
    "v_exp_f32 v40, v40\n"
    "v_mfma_f32_16x16x32_bf16 v[48:51], a[124:127], a[180:183], v[48:51]\n"
    "v_mfma_f32_16x16x32_bf16 v[48:51], a[120:123], a[176:179], v[48:51]\n"
    "v_mfma_f32_16x16x32_bf16 v[48:51], a[116:119], a[172:175], v[48:51]\n"
    "s_waitcnt vmcnt(0)\n"
    "v_cmp_eq_u64 s[54:55], v[236:237], v[238:239]\n"
    "v_cvt_pk_bf16_f32 v92, v16, v17\n"
    "v_mfma_f32_16x16x32_bf16 v[48:51], a[112:115], a[168:171], v[48:51]\n"
    "s_waitcnt lgkmcnt(1)\n"
    "v_exp_f32 v41, v41\n"
    "v_mfma_f32_16x16x32_bf16 v[52:55], a[136:139], a[192:195], v[104:107]\n"
    "s_waitcnt lgkmcnt(0)\n"
    "v_cmp_le_u32 s[56:57], s59, v245\n"
    "v_cvt_pk_bf16_f32 v80, v40, v41\n"
    "v_mfma_f32_16x16x32_bf16 v[52:55], a[140:143], a[196:199], v[52:55]\n"
    "s_waitcnt vmcnt(1)\n"
    "v_exp_f32 v42, v42\n"
    "v_mfma_f32_16x16x32_bf16 v[52:55], a[144:147], a[200:203], v[52:55]\n"
    "s_and_b64 s[54:55], s[54:55], s[56:57]\n"
    "s_cmp_eq_u64 s[54:55], -1\n"
    "v_mfma_f32_16x16x32_bf16 v[52:55], a[148:151], a[204:207], v[52:55]\n"
    "s_cbranch_scc0 .Lpoll2\n"
    ".Lpoll2r:\n"
    "v_mfma_f32_16x16x32_bf16 v[52:55], a[152:155], a[208:211], v[52:55]\n"
    "buffer_load_dwordx4 v1, s[16:19], s67 offen offset:0 sc1 lds\n"
    "v_mfma_f32_16x16x32_bf16 v[52:55], a[156:159], a[212:215], v[52:55]\n"
    "s_add_u32 s76, s71, s60\n"
    "ds_read_b128 a[112:115], v3 offset:0\n"
    "v_cvt_pk_bf16_f32 v93, v18, v19\n"
    "v_mfma_f32_16x16x32_bf16 v[56:59], a[156:159], a[236:239], v[104:107]\n"
    "s_add_u32 s77, s71, s61\n"
    "ds_read_b128 a[116:119], v3 offset:2048\n"
    "v_cvt_pk_bf16_f32 v70, v20, v21\n"
    "v_mfma_f32_16x16x32_bf16 v[56:59], a[152:155], a[232:235], v[56:59]\n"
    "ds_read_b128 a[120:123], v3 offset:4096\n"
    "v_mfma_f32_16x16x32_bf16 v[56:59], a[148:151], a[228:231], v[56:59]\n"
    "buffer_load_dwordx4 v1, s[16:19], s67 offen offset:1024 sc1 lds\n"
    "v_mfma_f32_16x16x32_bf16 v[56:59], a[144:147], a[224:227], v[56:59]\n"
    "ds_read_b128 a[124:127], v3 offset:6144\n"
    "v_cvt_pk_bf16_f32 v71, v22, v23\n"
    "v_mfma_f32_16x16x32_bf16 v[56:59], a[140:143], a[220:223], v[56:59]\n"
    "ds_read_b128 a[128:131], v3 offset:8192\n"
    "v_mfma_f32_16x16x32_bf16 v[56:59], a[136:139], a[216:219], v[56:59]\n"
    "ds_read_b128 a[132:135], v3 offset:10240\n"
    "v_mfma_f32_16x16x32_bf16 v[60:63], a[136:139], a[240:243], v[104:107]\n"
    "buffer_load_dwordx4 v1, s[16:19], s67 offen offset:2048 sc1 lds\n"
    "v_mfma_f32_16x16x32_bf16 v[60:63], a[140:143], a[244:247], v[60:63]\n"
    "ds_read_b128 v[100:103], v250 offset:1536\n"
    "v_cvt_pk_bf16_f32 v78, v24, v25\n"
    "v_mfma_f32_16x16x32_bf16 v[60:63], a[144:147], a[248:251], v[60:63]\n"
    "v_exp_f32 v43, v43\n"
    "v_mfma_f32_16x16x32_bf16 v[60:63], a[148:151], a[252:255], v[60:63]\n"
    "ds_read_b128 v[36:39], v243 offset:0\n"
    "v_cvt_pk_bf16_f32 v81, v42, v43\n"
    "v_mfma_f32_16x16x32_bf16 v[60:63], a[152:155], a[160:163], v[60:63]\n"
    "buffer_load_dwordx4 v1, s[16:19], s67 offen offset:3072 sc1 lds\n"
    "v_mfma_f32_16x16x32_bf16 v[60:63], a[156:159], a[164:167], v[60:63]\n"
    "s_add_u32 m0, s68, 4096\n"
    "v_cvt_pk_bf16_f32 v79, v26, v27\n"
    "v_add_u32 v243, 512, v243\n"
    "v_mfma_f32_16x16x32_bf16 v[64:67], a[156:159], a[188:191], v[104:107]\n"
    "s_add_u32 s83, s67, 4096\n"
    "v_exp_f32 v44, v44\n"
    "v_mfma_f32_16x16x32_bf16 v[64:67], a[152:155], a[184:187], v[64:67]\n"
    "v_exp_f32 v45, v45\n"
    "v_mfma_f32_16x16x32_bf16 v[64:67], a[148:151], a[180:183], v[64:67]\n"
    "buffer_load_dwordx4 v1, s[16:19], s83 offen offset:0 sc1 lds\n"
    "v_mfma_f32_16x16x32_bf16 v[64:67], a[144:147], a[176:179], v[64:67]\n"
    "v_cvt_pk_bf16_f32 v86, v28, v29\n"
    "v_cvt_pk_bf16_f32 v88, v44, v45\n"
    "v_mfma_f32_16x16x32_bf16 v[64:67], a[140:143], a[172:175], v[64:67]\n"
    "v_exp_f32 v46, v46\n"
    "v_mfma_f32_16x16x32_bf16 v[64:67], a[136:139], a[168:171], v[64:67]\n"
    "s_waitcnt lgkmcnt(1)\n"
    "v_exp_f32 v47, v47\n"
    "v_mfma_f32_16x16x32_bf16 v[4:7], a[112:115], a[192:195], v[100:103]\n"
    "buffer_load_dwordx4 v1, s[16:19], s83 offen offset:1024 sc1 lds\n"
    "v_mfma_f32_16x16x32_bf16 v[4:7], a[116:119], a[196:199], v[4:7]\n"
    "s_add_u32 m0, s70, .Lend-.Lend2+(0)\n"
    "ds_read_b128 a[136:139], v3 offset:1024\n"
    "v_cvt_pk_bf16_f32 v87, v30, v31\n"
    "v_mfma_f32_16x16x32_bf16 v[4:7], a[120:123], a[200:203], v[4:7]\n"
    "s_add_u32 s83, s67, 24576\n"
    "ds_read_b128 a[140:143], v3 offset:3072\n"
    "v_cvt_pk_bf16_f32 v89, v46, v47\n"
    "v_mfma_f32_16x16x32_bf16 v[4:7], a[124:127], a[204:207], v[4:7]\n"
    "s_waitcnt lgkmcnt(2)\n"
    "ds_read_b128 a[144:147], v3 offset:5120\n"
    "v_fma_f32 v204, v204, v36, -v38\n"
    "v_mfma_f32_16x16x32_bf16 v[4:7], a[128:131], a[208:211], v[4:7]\n"
    "buffer_load_dwordx4 v1, s[16:19], s83 offen offset:0 sc1 lds\n"
    "v_mfma_f32_16x16x32_bf16 v[4:7], a[132:135], a[212:215], v[4:7]\n"
    "ds_read_b128 a[148:151], v3 offset:7168\n"
    "v_cvt_pk_bf16_f32 v94, v32, v33\n"
    "v_mfma_f32_16x16x32_bf16 v[8:11], a[132:135], a[236:239], v[100:103]\n"
    "ds_read_b128 a[152:155], v3 offset:9216\n"
    "v_fma_f32 v205, v205, v37, -v39\n"
    "v_mfma_f32_16x16x32_bf16 v[8:11], a[128:131], a[232:235], v[8:11]\n"
    "ds_read_b128 a[156:159], v3 offset:11264\n"
    "v_fma_f32 v206, v206, v36, -v38\n"
    "v_mfma_f32_16x16x32_bf16 v[8:11], a[124:127], a[228:231], v[8:11]\n"
    "buffer_load_dwordx4 v1, s[16:19], s83 offen offset:1024 sc1 lds\n"
    "v_mfma_f32_16x16x32_bf16 v[8:11], a[120:123], a[224:227], v[8:11]\n"
    "ds_read_b128 v[104:107], v250 offset:1552\n"
    "v_cvt_pk_bf16_f32 v95, v34, v35\n"
    "v_mfma_f32_16x16x32_bf16 v[8:11], a[116:119], a[220:223], v[8:11]\n"
    "v_exp_f32 v52, v52\n"
    "v_mfma_f32_16x16x32_bf16 v[8:11], a[112:115], a[216:219], v[8:11]\n"
    "v_exp_f32 v53, v53\n"
    "v_mfma_f32_16x16x32_bf16 v[12:15], a[112:115], a[240:243], v[100:103]\n"
    "buffer_load_dwordx4 v1, s[16:19], s83 offen offset:2048 sc1 lds\n"
    "v_mfma_f32_16x16x32_bf16 v[12:15], a[116:119], a[244:247], v[12:15]\n"
    "v_exp_f32 v54, v54\n"
    "v_mfma_f32_16x16x32_bf16 v[12:15], a[120:123], a[248:251], v[12:15]\n"
    "v_exp_f32 v55, v55\n"
    "v_mfma_f32_16x16x32_bf16 v[12:15], a[124:127], a[252:255], v[12:15]\n"
    "v_cvt_pk_bf16_f32 v74, v52, v53\n"
    "v_cvt_pk_bf16_f32 v75, v54, v55\n"
    "v_mfma_f32_16x16x32_bf16 v[12:15], a[128:131], a[160:163], v[12:15]\n"
    "buffer_load_dwordx4 v1, s[16:19], s83 offen offset:3072 sc1 lds\n"
    "v_mfma_f32_16x16x32_bf16 v[12:15], a[132:135], a[164:167], v[12:15]\n"
    "s_add_u32 m0, s70, 4096\n"
    "v_exp_f32 v56, v56\n"
    "v_mfma_f32_16x16x32_bf16 v[16:19], a[132:135], a[188:191], v[100:103]\n"
    "s_add_u32 s83, s67, 28672\n"
    "v_exp_f32 v48, v48\n"
    "v_mfma_f32_16x16x32_bf16 v[16:19], a[128:131], a[184:187], v[16:19]\n"
    "v_exp_f32 v49, v49\n"
    "v_mfma_f32_16x16x32_bf16 v[16:19], a[124:127], a[180:183], v[16:19]\n"
    "buffer_load_dwordx4 v1, s[16:19], s83 offen offset:0 sc1 lds\n"
    "v_mfma_f32_16x16x32_bf16 v[16:19], a[120:123], a[176:179], v[16:19]\n"
    "v_exp_f32 v57, v57\n"
    "v_mfma_f32_16x16x32_bf16 v[16:19], a[116:119], a[172:175], v[16:19]\n"
    "v_cvt_pk_bf16_f32 v82, v56, v57\n"
    "v_cvt_pk_bf16_f32 v96, v48, v49\n"
    "v_mfma_f32_16x16x32_bf16 v[16:19], a[112:115], a[168:171], v[16:19]\n"
    "s_waitcnt lgkmcnt(0)\n"
    "v_exp_f32 v58, v58\n"
    "v_mfma_f32_16x16x32_bf16 v[20:23], a[136:139], a[192:195], v[104:107]\n"
    "buffer_load_dwordx4 v1, s[16:19], s83 offen offset:1024 sc1 lds\n"
    "v_mfma_f32_16x16x32_bf16 v[20:23], a[140:143], a[196:199], v[20:23]\n"
    "s_add_u32 s67, s67, 49152\n"
    "ds_read_b128 a[112:115], v249 offset:0\n"
    "v_fma_f32 v207, v207, v37, -v39\n"
    "v_mfma_f32_16x16x32_bf16 v[20:23], a[144:147], a[200:203], v[20:23]\n"
    "s_add_u32 s59, s59, 4\n"
    "ds_read_b128 a[116:119], v249 offset:12288\n"
    "v_cvt_pk_bf16_f32 v204, v204, v205\n"
    "v_mfma_f32_16x16x32_bf16 v[20:23], a[148:151], a[204:207], v[20:23]\n"
    "ds_read_b128 a[120:123], v249 offset:1024\n"
    "v_cvt_pk_bf16_f32 v206, v206, v207\n"
    "v_mfma_f32_16x16x32_bf16 v[20:23], a[152:155], a[208:211], v[20:23]\n"
    "ds_read_b128 a[124:127], v249 offset:13312\n"
    "v_fma_f32 v208, v208, v36, -v38\n"
    "v_mfma_f32_16x16x32_bf16 v[20:23], a[156:159], a[212:215], v[20:23]\n"
    "ds_read_b128 a[128:131], v249 offset:2048\n"
    "v_fma_f32 v209, v209, v37, -v39\n"
    "v_mfma_f32_16x16x32_bf16 v[24:27], a[156:159], a[236:239], v[104:107]\n"
    "ds_read_b128 a[132:135], v249 offset:14336\n"
    "v_fma_f32 v210, v210, v36, -v38\n"
    "v_mfma_f32_16x16x32_bf16 v[24:27], a[152:155], a[232:235], v[24:27]\n"
    "v_exp_f32 v59, v59\n"
    "v_mfma_f32_16x16x32_bf16 v[24:27], a[148:151], a[228:231], v[24:27]\n"
    "v_cvt_pk_bf16_f32 v83, v58, v59\n"
    "v_fma_f32 v211, v211, v37, -v39\n"
    "v_mfma_f32_16x16x32_bf16 v[24:27], a[144:147], a[224:227], v[24:27]\n"
    "v_exp_f32 v60, v60\n"
    "v_mfma_f32_16x16x32_bf16 v[24:27], a[140:143], a[220:223], v[24:27]\n"
    "v_exp_f32 v61, v61\n"
    "v_mfma_f32_16x16x32_bf16 v[24:27], a[136:139], a[216:219], v[24:27]\n"
    "v_exp_f32 v62, v62\n"
    "v_mfma_f32_16x16x32_bf16 v[28:31], a[136:139], a[240:243], v[104:107]\n"
    "v_cvt_pk_bf16_f32 v90, v60, v61\n"
    "v_cvt_pk_bf16_f32 v208, v208, v209\n"
    "v_mfma_f32_16x16x32_bf16 v[28:31], a[140:143], a[244:247], v[28:31]\n"
    "v_exp_f32 v63, v63\n"
    "v_mfma_f32_16x16x32_bf16 v[28:31], a[144:147], a[248:251], v[28:31]\n"
    "v_cvt_pk_bf16_f32 v91, v62, v63\n"
    "v_cvt_pk_bf16_f32 v210, v210, v211\n"
    "v_mfma_f32_16x16x32_bf16 v[28:31], a[148:151], a[252:255], v[28:31]\n"
    "v_exp_f32 v50, v50\n"
    "v_mfma_f32_16x16x32_bf16 v[28:31], a[152:155], a[160:163], v[28:31]\n"
    "v_exp_f32 v51, v51\n"
    "v_mfma_f32_16x16x32_bf16 v[28:31], a[156:159], a[164:167], v[28:31]\n"
    "v_exp_f32 v64, v64\n"
    "v_mfma_f32_16x16x32_bf16 v[32:35], a[156:159], a[188:191], v[104:107]\n"
    "v_cvt_pk_bf16_f32 v97, v50, v51\n"
    "v_fma_f32 v212, v212, v36, -v38\n"
    "v_mfma_f32_16x16x32_bf16 v[32:35], a[152:155], a[184:187], v[32:35]\n"
    "v_exp_f32 v65, v65\n"
    "v_mfma_f32_16x16x32_bf16 v[32:35], a[148:151], a[180:183], v[32:35]\n"
    "v_cvt_pk_bf16_f32 v98, v64, v65\n"
    "v_fma_f32 v213, v213, v37, -v39\n"
    "v_mfma_f32_16x16x32_bf16 v[32:35], a[144:147], a[176:179], v[32:35]\n"
    "buffer_store_dword v204, v244, s[16:19], s76 offen offset:0 sc1\n"
    "v_mfma_f32_16x16x32_bf16 v[32:35], a[140:143], a[172:175], v[32:35]\n"
    "v_exp_f32 v66, v66\n"
    "v_mfma_f32_16x16x32_bf16 v[32:35], a[136:139], a[168:171], v[32:35]\n"
    "s_waitcnt lgkmcnt(0)\n"
    "v_fma_f32 v214, v214, v36, -v38\n"
    "v_fma_f32 v215, v215, v37, -v39\n"
    "v_mfma_f32_16x16x32_bf16 v[124:127], a[112:115], v[68:71], v[124:127]\n"
    "ds_read_b128 a[136:139], v249 offset:3072\n"
    "v_cvt_pk_bf16_f32 v212, v212, v213\n"
    "v_mfma_f32_16x16x32_bf16 v[124:127], a[116:119], v[72:75], v[124:127]\n"
    "ds_read_b128 a[140:143], v249 offset:15360\n"
    "v_cvt_pk_bf16_f32 v214, v214, v215\n"
    "v_mfma_f32_16x16x32_bf16 v[128:131], a[124:127], v[72:75], v[128:131]\n"
    "ds_read_b128 a[144:147], v249 offset:4096\n"
    "v_fma_f32 v216, v216, v36, -v38\n"
    "v_mfma_f32_16x16x32_bf16 v[128:131], a[120:123], v[68:71], v[128:131]\n"
    "ds_read_b128 a[148:151], v249 offset:16384\n"
    "v_fma_f32 v217, v217, v37, -v39\n"
    "v_mfma_f32_16x16x32_bf16 v[132:135], a[128:131], v[68:71], v[132:135]\n"
    "ds_read_b128 a[152:155], v249 offset:5120\n"
    "v_fma_f32 v218, v218, v36, -v38\n"
    "v_mfma_f32_16x16x32_bf16 v[132:135], a[132:135], v[72:75], v[132:135]\n"
    "ds_read_b128 a[156:159], v249 offset:17408\n"
    "v_fma_f32 v219, v219, v37, -v39\n"
    "v_mfma_f32_16x16x32_bf16 v[148:151], a[132:135], v[80:83], v[148:151]\n"
    "ds_read_b128 v[104:107], v251 offset:768\n"
    "v_cvt_pk_bf16_f32 v216, v216, v217\n"
    "v_mfma_f32_16x16x32_bf16 v[148:151], a[128:131], v[76:79], v[148:151]\n"
    "v_exp_f32 v67, v67\n"
    "v_mfma_f32_16x16x32_bf16 v[144:147], a[120:123], v[76:79], v[144:147]\n"
    "v_cvt_pk_bf16_f32 v99, v66, v67\n"
    "v_cvt_pk_bf16_f32 v218, v218, v219\n"
    "v_mfma_f32_16x16x32_bf16 v[144:147], a[124:127], v[80:83], v[144:147]\n"
    "buffer_store_dword v206, v244, s[16:19], s76 offen offset:128 sc1\n"
    "v_mfma_f32_16x16x32_bf16 v[140:143], a[116:119], v[80:83], v[140:143]\n"
    "v_exp_f32 v4, v4\n"
    "v_mfma_f32_16x16x32_bf16 v[140:143], a[112:115], v[76:79], v[140:143]\n"
    "buffer_store_dword v208, v244, s[16:19], s76 offen offset:1024 sc1\n"
    "v_mfma_f32_16x16x32_bf16 v[156:159], a[112:115], v[84:87], v[156:159]\n"
    "v_fma_f32 v220, v220, v36, -v38\n"
    "v_fma_f32 v221, v221, v37, -v39\n"
    "v_mfma_f32_16x16x32_bf16 v[156:159], a[116:119], v[88:91], v[156:159]\n"
    "buffer_store_dword v210, v244, s[16:19], s76 offen offset:1152 sc1\n"
    "v_mfma_f32_16x16x32_bf16 v[160:163], a[124:127], v[88:91], v[160:163]\n"
    "v_exp_f32 v5, v5\n"
    "v_mfma_f32_16x16x32_bf16 v[160:163], a[120:123], v[84:87], v[160:163]\n"
    "buffer_store_dword v212, v244, s[16:19], s76 offen offset:256 sc1\n"
    "v_mfma_f32_16x16x32_bf16 v[164:167], a[128:131], v[84:87], v[164:167]\n"
    "v_exp_f32 v6, v6\n"
    "v_mfma_f32_16x16x32_bf16 v[164:167], a[132:135], v[88:91], v[164:167]\n"
    "buffer_store_dword v214, v244, s[16:19], s76 offen offset:384 sc1\n"
    "v_mfma_f32_16x16x32_bf16 v[180:183], a[132:135], v[96:99], v[180:183]\n"
    "v_fma_f32 v222, v222, v36, -v38\n"
    "v_fma_f32 v223, v223, v37, -v39\n"
    "v_mfma_f32_16x16x32_bf16 v[180:183], a[128:131], v[92:95], v[180:183]\n"
    "v_exp_f32 v7, v7\n"
    "v_mfma_f32_16x16x32_bf16 v[176:179], a[120:123], v[92:95], v[176:179]\n"
    "v_exp_f32 v8, v8\n"
    "v_mfma_f32_16x16x32_bf16 v[176:179], a[124:127], v[96:99], v[176:179]\n"
    "v_cvt_pk_bf16_f32 v220, v220, v221\n"
    "v_cvt_pk_bf16_f32 v222, v222, v223\n"
    "v_mfma_f32_16x16x32_bf16 v[172:175], a[116:119], v[96:99], v[172:175]\n"
    "v_exp_f32 v9, v9\n"
    "v_mfma_f32_16x16x32_bf16 v[172:175], a[112:115], v[92:95], v[172:175]\n"
    "s_waitcnt lgkmcnt(0)\n"
    "v_exp_f32 v10, v10\n"
    "v_mfma_f32_16x16x32_bf16 v[136:139], a[136:139], v[68:71], v[136:139]\n"
    "ds_read_b128 a[112:115], v249 offset:6144\n"
    "v_fma_f32 v224, v224, v36, -v38\n"
    "v_mfma_f32_16x16x32_bf16 v[136:139], a[140:143], v[72:75], v[136:139]\n"
    "ds_read_b128 a[116:119], v249 offset:18432\n"
    "v_fma_f32 v225, v225, v37, -v39\n"
    "v_mfma_f32_16x16x32_bf16 v[188:191], a[148:151], v[72:75], v[188:191]\n"
    "ds_read_b128 a[120:123], v249 offset:7168\n"
    "v_fma_f32 v226, v226, v36, -v38\n"
    "v_mfma_f32_16x16x32_bf16 v[188:191], a[144:147], v[68:71], v[188:191]\n"
    "ds_read_b128 a[124:127], v249 offset:19456\n"
    "v_fma_f32 v227, v227, v37, -v39\n"
    "v_mfma_f32_16x16x32_bf16 v[192:195], a[152:155], v[68:71], v[192:195]\n"
    "ds_read_b128 a[128:131], v249 offset:8192\n"
    "v_cvt_pk_bf16_f32 v224, v224, v225\n"
    "v_mfma_f32_16x16x32_bf16 v[192:195], a[156:159], v[72:75], v[192:195]\n"
    "ds_read_b128 a[132:135], v249 offset:20480\n"
    "v_smfmac_f32_16x16x64_bf16 v[108:111], v[104:107], v[68:75], v253\n"
    "s_add_u32 s81, s81, 128\n"
    "s_cmp_gt_u32 s81, 4096\n"
    "s_cselect_b32 s81, 0, s81\n"
    "v_smfmac_f32_16x16x64_bf16 v[112:115], v[104:107], v[76:83], v253\n"
    "v_mfma_f32_16x16x32_bf16 a[4:7], a[156:159], v[80:83], a[4:7]\n"
    "v_exp_f32 v11, v11\n"
    "v_mfma_f32_16x16x32_bf16 a[4:7], a[152:155], v[76:79], a[4:7]\n"
    "v_exp_f32 v12, v12\n"
    "v_mfma_f32_16x16x32_bf16 a[0:3], a[144:147], v[76:79], a[0:3]\n"
    "v_exp_f32 v13, v13\n"
    "v_mfma_f32_16x16x32_bf16 a[0:3], a[148:151], v[80:83], a[0:3]\n"
    "v_exp_f32 v14, v14\n"
    "v_mfma_f32_16x16x32_bf16 v[152:155], a[140:143], v[80:83], v[152:155]\n"
    "v_exp_f32 v15, v15\n"
    "v_mfma_f32_16x16x32_bf16 v[152:155], a[136:139], v[76:79], v[152:155]\n"
    "v_cvt_pk_bf16_f32 v226, v226, v227\n"
    "v_fma_f32 v228, v228, v36, -v38\n"
    "v_mfma_f32_16x16x32_bf16 v[168:171], a[136:139], v[84:87], v[168:171]\n"
    "v_exp_f32 v16, v16\n"
    "v_mfma_f32_16x16x32_bf16 v[168:171], a[140:143], v[88:91], v[168:171]\n"
    "v_exp_f32 v17, v17\n"
    "v_mfma_f32_16x16x32_bf16 a[16:19], a[148:151], v[88:91], a[16:19]\n"
    "v_fma_f32 v229, v229, v37, -v39\n"
    "v_fma_f32 v230, v230, v36, -v38\n"
    "v_mfma_f32_16x16x32_bf16 a[16:19], a[144:147], v[84:87], a[16:19]\n"
    "v_exp_f32 v18, v18\n"
    "v_mfma_f32_16x16x32_bf16 a[20:23], a[152:155], v[84:87], a[20:23]\n"
    "v_fma_f32 v231, v231, v37, -v39\n"
    "v_mfma_f32_16x16x32_bf16 a[20:23], a[156:159], v[88:91], a[20:23]\n"
    "v_smfmac_f32_16x16x64_bf16 v[116:119], v[104:107], v[84:91], v253\n"
    "v_smfmac_f32_16x16x64_bf16 v[120:123], v[104:107], v[92:99], v253\n"
    "v_mfma_f32_16x16x32_bf16 a[36:39], a[156:159], v[96:99], a[36:39]\n"
    "v_exp_f32 v19, v19\n"
    "v_mfma_f32_16x16x32_bf16 a[36:39], a[152:155], v[92:95], a[36:39]\n"
    "v_exp_f32 v20, v20\n"
    "v_mfma_f32_16x16x32_bf16 a[32:35], a[144:147], v[92:95], a[32:35]\n"
    "v_cvt_pk_bf16_f32 v228, v228, v229\n"
    "v_cvt_pk_bf16_f32 v230, v230, v231\n"
    "v_mfma_f32_16x16x32_bf16 a[32:35], a[148:151], v[96:99], a[32:35]\n"
    "v_exp_f32 v21, v21\n"
    "v_mfma_f32_16x16x32_bf16 v[184:187], a[140:143], v[96:99], v[184:187]\n"
    "v_fma_f32 v232, v232, v36, -v38\n"
    "v_mfma_f32_16x16x32_bf16 v[184:187], a[136:139], v[92:95], v[184:187]\n"
    "s_waitcnt lgkmcnt(0)\n"
    "v_mfma_f32_16x16x32_bf16 v[196:199], a[112:115], v[68:71], v[196:199]\n"
    "ds_read_b128 a[136:139], v249 offset:9216\n"
    "v_fma_f32 v233, v233, v37, -v39\n"
    "v_mfma_f32_16x16x32_bf16 v[196:199], a[116:119], v[72:75], v[196:199]\n"
    "ds_read_b128 a[140:143], v249 offset:21504\n"
    "v_mfma_f32_16x16x32_bf16 v[200:203], a[124:127], v[72:75], v[200:203]\n"
    "ds_read_b128 a[144:147], v249 offset:10240\n"
    "v_mfma_f32_16x16x32_bf16 v[200:203], a[120:123], v[68:71], v[200:203]\n"
    "ds_read_b128 a[148:151], v249 offset:22528\n"
    "v_fma_f32 v234, v234, v36, -v38\n"
    "v_mfma_f32_16x16x32_bf16 a[48:51], a[128:131], v[68:71], a[48:51]\n"
    "buffer_load_dwordx2 v[236:237], v241, s[36:39], s81 offen offset:0 sc1\n"
    "v_mfma_f32_16x16x32_bf16 a[48:51], a[132:135], v[72:75], a[48:51]\n"
    "ds_read_b128 a[152:155], v249 offset:11264\n"
    "v_fma_f32 v235, v235, v37, -v39\n"
    "v_mfma_f32_16x16x32_bf16 a[64:67], a[132:135], v[80:83], a[64:67]\n"
    "ds_read_b128 a[156:159], v249 offset:23552\n"
    "v_mfma_f32_16x16x32_bf16 a[64:67], a[128:131], v[76:79], a[64:67]\n"
    "buffer_store_dword v216, v244, s[16:19], s76 offen offset:1280 sc1\n"
    "v_mfma_f32_16x16x32_bf16 a[12:15], a[120:123], v[76:79], a[12:15]\n"
    "v_exp_f32 v22, v22\n"
    "v_mfma_f32_16x16x32_bf16 a[12:15], a[124:127], v[80:83], a[12:15]\n"
    "buffer_store_dword v218, v244, s[16:19], s76 offen offset:1408 sc1\n"
    "v_mfma_f32_16x16x32_bf16 a[8:11], a[116:119], v[80:83], a[8:11]\n"
    "v_exp_f32 v23, v23\n"
    "v_mfma_f32_16x16x32_bf16 a[8:11], a[112:115], v[76:79], a[8:11]\n"
    "buffer_store_dword v220, v244, s[16:19], s77 offen offset:0 sc1\n"
    "v_mfma_f32_16x16x32_bf16 a[24:27], a[112:115], v[84:87], a[24:27]\n"
    "v_exp_f32 v24, v24\n"
    "v_mfma_f32_16x16x32_bf16 a[24:27], a[116:119], v[88:91], a[24:27]\n"
    "buffer_store_dword v222, v244, s[16:19], s77 offen offset:128 sc1\n"
    "v_mfma_f32_16x16x32_bf16 a[28:31], a[124:127], v[88:91], a[28:31]\n"
    "v_exp_f32 v25, v25\n"
    "v_mfma_f32_16x16x32_bf16 a[28:31], a[120:123], v[84:87], a[28:31]\n"
    "buffer_store_dword v224, v244, s[16:19], s77 offen offset:1024 sc1\n"
    "v_mfma_f32_16x16x32_bf16 a[80:83], a[128:131], v[84:87], a[80:83]\n"
    "v_exp_f32 v26, v26\n"
    "v_mfma_f32_16x16x32_bf16 a[80:83], a[132:135], v[88:91], a[80:83]\n"
    "buffer_store_dword v226, v244, s[16:19], s77 offen offset:1152 sc1\n"
    "v_mfma_f32_16x16x32_bf16 a[96:99], a[132:135], v[96:99], a[96:99]\n"
    "v_cvt_pk_bf16_f32 v232, v232, v233\n"
    "v_cvt_pk_bf16_f32 v234, v234, v235\n"
    "v_mfma_f32_16x16x32_bf16 a[96:99], a[128:131], v[92:95], a[96:99]\n"
    "buffer_store_dword v228, v244, s[16:19], s77 offen offset:256 sc1\n"
    "v_mfma_f32_16x16x32_bf16 a[44:47], a[120:123], v[92:95], a[44:47]\n"
    "v_exp_f32 v27, v27\n"
    "v_mfma_f32_16x16x32_bf16 a[44:47], a[124:127], v[96:99], a[44:47]\n"
    "buffer_store_dword v230, v244, s[16:19], s77 offen offset:384 sc1\n"
    "v_mfma_f32_16x16x32_bf16 a[40:43], a[116:119], v[96:99], a[40:43]\n"
    "v_exp_f32 v28, v28\n"
    "v_mfma_f32_16x16x32_bf16 a[40:43], a[112:115], v[92:95], a[40:43]\n"
    "s_waitcnt lgkmcnt(0)\n"
    "v_exp_f32 v29, v29\n"
    "v_mfma_f32_16x16x32_bf16 a[52:55], a[136:139], v[68:71], a[52:55]\n"
    "ds_read_b128 a[112:115], v3 offset:12288\n"
    "v_mfma_f32_16x16x32_bf16 a[52:55], a[140:143], v[72:75], a[52:55]\n"
    "ds_read_b128 a[116:119], v3 offset:14336\n"
    "v_mfma_f32_16x16x32_bf16 a[56:59], a[148:151], v[72:75], a[56:59]\n"
    "ds_read_b128 a[120:123], v3 offset:16384\n"
    "v_mfma_f32_16x16x32_bf16 a[56:59], a[144:147], v[68:71], a[56:59]\n"
    "ds_read_b128 a[124:127], v3 offset:18432\n"
    "v_mfma_f32_16x16x32_bf16 a[60:63], a[152:155], v[68:71], a[60:63]\n"
    "ds_read_b128 a[128:131], v3 offset:20480\n"
    "v_mfma_f32_16x16x32_bf16 a[60:63], a[156:159], v[72:75], a[60:63]\n"
    "ds_read_b128 a[132:135], v3 offset:22528\n"
    "v_mfma_f32_16x16x32_bf16 a[76:79], a[156:159], v[80:83], a[76:79]\n"
    "ds_read_b128 v[100:103], v250 offset:1664\n"
    "v_mfma_f32_16x16x32_bf16 a[76:79], a[152:155], v[76:79], a[76:79]\n"
    "buffer_store_dword v232, v244, s[16:19], s77 offen offset:1280 sc1\n"
    "v_mfma_f32_16x16x32_bf16 a[72:75], a[144:147], v[76:79], a[72:75]\n"
    "v_exp_f32 v30, v30\n"
    "v_mfma_f32_16x16x32_bf16 a[72:75], a[148:151], v[80:83], a[72:75]\n"
    "buffer_store_dword v234, v244, s[16:19], s77 offen offset:1408 sc1\n"
    "v_mfma_f32_16x16x32_bf16 a[68:71], a[140:143], v[80:83], a[68:71]\n"
    "v_exp_f32 v31, v31\n"
    "v_mfma_f32_16x16x32_bf16 a[68:71], a[136:139], v[76:79], a[68:71]\n"
    "v_exp_f32 v32, v32\n"
    "v_mfma_f32_16x16x32_bf16 a[84:87], a[136:139], v[84:87], a[84:87]\n"
    "v_exp_f32 v33, v33\n"
    "v_mfma_f32_16x16x32_bf16 a[84:87], a[140:143], v[88:91], a[84:87]\n"
    "v_exp_f32 v34, v34\n"
    "v_mfma_f32_16x16x32_bf16 a[88:91], a[148:151], v[88:91], a[88:91]\n"
    "s_waitcnt vmcnt(17)\n"
    "ds_add_u32 v247, v246\n"
    "v_mfma_f32_16x16x32_bf16 a[88:91], a[144:147], v[84:87], a[88:91]\n"
    "v_exp_f32 v35, v35\n"
    "v_mfma_f32_16x16x32_bf16 a[92:95], a[152:155], v[84:87], a[92:95]\n"
    "v_mfma_f32_16x16x32_bf16 a[92:95], a[156:159], v[88:91], a[92:95]\n"
    "v_mfma_f32_16x16x32_bf16 a[108:111], a[156:159], v[96:99], a[108:111]\n"
    "v_mfma_f32_16x16x32_bf16 a[108:111], a[152:155], v[92:95], a[108:111]\n"
    "v_mfma_f32_16x16x32_bf16 a[104:107], a[144:147], v[92:95], a[104:107]\n"
    "s_sub_u32 s74, s68, s65\n"
    "v_add_u32 v250, 1536, v250\n"
    "v_add_u32 v251, 1536, v251\n"
    "v_mfma_f32_16x16x32_bf16 a[104:107], a[148:151], v[96:99], a[104:107]\n"
    "s_add_u32 s66, s66, 1\n"
    "v_mov_b32 v2, v3\n"
    "v_add_u32 v3, s74, v252\n"
    "v_mfma_f32_16x16x32_bf16 a[100:103], a[140:143], v[96:99], a[100:103]\n"
    "s_waitcnt lgkmcnt(1)\n"
    "s_cmp_lt_u32 s66, 29\n"
    "v_mfma_f32_16x16x32_bf16 a[100:103], a[136:139], v[92:95], a[100:103]\n"
    "s_cbranch_scc1 .LPK\n"
    "s_branch .Ltail\n"
    "s_nop 0\n"
    ".LpollAK:\n"
    "s_sleep 1\n"
    "buffer_load_dwordx2 v[236:237], v87, s[36:39], 0 offen offset:0 sc1\n"
    "s_waitcnt vmcnt(0)\n"
    "v_cmp_eq_u64 s[54:55], v[236:237], v[238:239]\n"
    "s_add_u32 s58, s58, 1\n"
    "s_cmp_eq_u64 s[54:55], -1\n"
    "s_cbranch_scc1 .LpollAKr\n"
    "s_cmp_lt_u32 s58, 0x80000\n"
    "s_cbranch_scc1 .LpollAK\n"
    "s_branch .LpollAKr\n"
    "s_nop 0\n"
    "s_nop 0\n"
    "s_nop 0\n"
    ".LphKP1:\n"
    "s_sleep 1\n"
    "ds_read_b32 v245, v247\n"
    "s_waitcnt lgkmcnt(0)\n"
    "v_cmp_le_u32 s[56:57], s59, v245\n"
    "s_add_u32 s58, s58, 1\n"
    "s_nop 1\n"
    "s_cmp_eq_u64 s[56:57], -1\n"
    "s_cbranch_scc1 .LphKP1r\n"
    "s_cmp_lt_u32 s58, 0x80000\n"
    "s_cbranch_scc1 .LphKP1\n"
    "s_branch .LphKP1r\n"
    "s_nop 0\n"
    "s_nop 0\n"
    "s_nop 0\n"
    ".LphKP2:\n"
    "s_sleep 1\n"
    "ds_read_b32 v245, v247\n"
    "s_waitcnt lgkmcnt(0)\n"
    "v_cmp_le_u32 s[56:57], s59, v245\n"
    "s_add_u32 s58, s58, 1\n"
    "s_nop 1\n"
    "s_cmp_eq_u64 s[56:57], -1\n"
    "s_cbranch_scc1 .LphKP2r\n"
    "s_cmp_lt_u32 s58, 0x80000\n"
    "s_cbranch_scc1 .LphKP2\n"
    "s_branch .LphKP2r\n"
    "s_nop 0\n"
    ".LphKN1:\n"
    "s_sleep 1\n"
    "ds_read_b32 v245, v247\n"
    "s_waitcnt lgkmcnt(0)\n"
    "v_cmp_le_u32 s[56:57], s59, v245\n"
    "s_add_u32 s58, s58, 1\n"
    "s_nop 1\n"
    "s_cmp_eq_u64 s[56:57], -1\n"
    "s_cbranch_scc1 .LphKN1r\n"
    "s_cmp_lt_u32 s58, 0x80000\n"
    "s_cbranch_scc1 .LphKN1\n"
    "s_branch .LphKN1r\n"
    "s_nop 0\n"
    "s_nop 0\n"
    "s_nop 0\n"
    ".LphKN2:\n"
    "s_sleep 1\n"
    "ds_read_b32 v245, v247\n"
    "s_waitcnt lgkmcnt(0)\n"
    "v_cmp_le_u32 s[56:57], s59, v245\n"
    "s_add_u32 s58, s58, 1\n"
    "s_nop 1\n"
    "s_cmp_eq_u64 s[56:57], -1\n"
    "s_cbranch_scc1 .LphKN2r\n"
    "s_cmp_lt_u32 s58, 0x80000\n"
    "s_cbranch_scc1 .LphKN2\n"
    "s_branch .LphKN2r\n"
    "s_nop 0\n"
    ".LpollAV:\n"
    "s_sleep 1\n"
    "buffer_load_dwordx2 v[236:237], v87, s[36:39], 0 offen offset:0 sc1\n"
    "s_waitcnt vmcnt(0)\n"
    "v_cmp_eq_u64 s[54:55], v[236:237], v[238:239]\n"
    "s_add_u32 s58, s58, 1\n"
    "s_cmp_eq_u64 s[54:55], -1\n"
    "s_cbranch_scc1 .LpollAVr\n"
    "s_cmp_lt_u32 s58, 0x80000\n"
    "s_cbranch_scc1 .LpollAV\n"
    "s_branch .LpollAVr\n"
    "s_nop 0\n"
    "s_nop 0\n"
    "s_nop 0\n"
    ".LphVP1:\n"
    "s_sleep 1\n"
    "ds_read_b32 v245, v247\n"
    "s_waitcnt lgkmcnt(0)\n"
    "v_cmp_le_u32 s[56:57], s59, v245\n"
    "s_add_u32 s58, s58, 1\n"
    "s_nop 1\n"
    "s_cmp_eq_u64 s[56:57], -1\n"
    "s_cbranch_scc1 .LphVP1r\n"
    "s_cmp_lt_u32 s58, 0x80000\n"
    "s_cbranch_scc1 .LphVP1\n"
    "s_branch .LphVP1r\n"
    "s_nop 0\n"
    "s_nop 0\n"
    "s_nop 0\n"
    ".LphVP2:\n"
    "s_sleep 1\n"
    "ds_read_b32 v245, v247\n"
    "s_waitcnt lgkmcnt(0)\n"
    "v_cmp_le_u32 s[56:57], s59, v245\n"
    "s_add_u32 s58, s58, 1\n"
    "s_nop 1\n"
    "s_cmp_eq_u64 s[56:57], -1\n"
    "s_cbranch_scc1 .LphVP2r\n"
    "s_cmp_lt_u32 s58, 0x80000\n"
    "s_cbranch_scc1 .LphVP2\n"
    "s_branch .LphVP2r\n"
    "s_nop 0\n"
    ".LphVN1:\n"
    "s_sleep 1\n"
    "ds_read_b32 v245, v247\n"
    "s_waitcnt lgkmcnt(0)\n"
    "v_cmp_le_u32 s[56:57], s59, v245\n"
    "s_add_u32 s58, s58, 1\n"
    "s_nop 1\n"
    "s_cmp_eq_u64 s[56:57], -1\n"
    "s_cbranch_scc1 .LphVN1r\n"
    "s_cmp_lt_u32 s58, 0x80000\n"
    "s_cbranch_scc1 .LphVN1\n"
    "s_branch .LphVN1r\n"
    "s_nop 0\n"
    "s_nop 0\n"
    ".LphVN2:\n"
    "s_sleep 1\n"
    "ds_read_b32 v245, v247\n"
    "s_waitcnt lgkmcnt(0)\n"
    "v_cmp_le_u32 s[56:57], s59, v245\n"
    "s_add_u32 s58, s58, 1\n"
    "s_nop 1\n"
    "s_cmp_eq_u64 s[56:57], -1\n"
    "s_cbranch_scc1 .LphVN2r\n"
    "s_cmp_lt_u32 s58, 0x80000\n"
    "s_cbranch_scc1 .LphVN2\n"
    "s_branch .LphVN2r\n"
    "s_nop 0\n"
    "s_nop 0\n"
    "s_nop 0\n"
    "s_nop 0\n"
    "s_nop 0\n"
    "s_nop 0\n"
    ".Lpoll1:\n"
    "s_sleep 1\n"
    "v_cmp_eq_u64 s[54:55], v[236:237], v[238:239]\n"
    "s_nop 1\n"
    "s_cmp_eq_u64 s[54:55], -1\n"
    "s_cbranch_scc1 .Lpoll1l\n"
    "buffer_load_dwordx2 v[236:237], v241, s[36:39], s81 offen offset:0 sc1\n"
    "s_waitcnt vmcnt(0)\n"
    "v_cmp_eq_u64 s[54:55], v[236:237], v[238:239]\n"
    ".Lpoll1l:\n"
    "ds_read_b32 v245, v247\n"
    "s_waitcnt lgkmcnt(0)\n"
    "v_cmp_le_u32 s[56:57], s59, v245\n"
    "s_add_u32 s58, s58, 1\n"
    "s_nop 1\n"
    "s_and_b64 s[54:55], s[54:55], s[56:57]\n"
    "s_cmp_eq_u64 s[54:55], -1\n"
    "s_cbranch_scc1 .Lpoll1r\n"
    "s_cmp_lt_u32 s58, 0x80000\n"
    "s_cbranch_scc1 .Lpoll1\n"
    "s_branch .Lpoll1r\n"
    "s_nop 0\n"
    "s_nop 0\n"
    "s_nop 0\n"
    "s_nop 0\n"
    "s_nop 0\n"
    "s_nop 0\n"
    "s_nop 0\n"
    ".Lpoll2:\n"
    "s_sleep 1\n"
    "v_cmp_eq_u64 s[54:55], v[236:237], v[238:239]\n"
    "s_nop 1\n"
    "s_cmp_eq_u64 s[54:55], -1\n"
    "s_cbranch_scc1 .Lpoll2l\n"
    "buffer_load_dwordx2 v[236:237], v241, s[36:39], s81 offen offset:0 sc1\n"
    "s_waitcnt vmcnt(0)\n"
    "v_cmp_eq_u64 s[54:55], v[236:237], v[238:239]\n"
    ".Lpoll2l:\n"
    "ds_read_b32 v245, v247\n"
    "s_waitcnt lgkmcnt(0)\n"
    "v_cmp_le_u32 s[56:57], s59, v245\n"
    "s_add_u32 s58, s58, 1\n"
    "s_nop 1\n"
    "s_and_b64 s[54:55], s[54:55], s[56:57]\n"
    "s_cmp_eq_u64 s[54:55], -1\n"
    "s_cbranch_scc1 .Lpoll2r\n"
    "s_cmp_lt_u32 s58, 0x80000\n"
    "s_cbranch_scc1 .Lpoll2\n"
    "s_branch .Lpoll2r\n"
    "s_nop 0\n"
    "s_nop 0\n"
    "s_nop 0\n"
    "s_nop 0\n"
    "s_nop 0\n"
    "s_nop 0\n"
    "s_nop 0\n"
    ".Lpoll3:\n"
    "s_sleep 1\n"
    "v_cmp_eq_u64 s[54:55], v[236:237], v[238:239]\n"
    "s_nop 1\n"
    "s_cmp_eq_u64 s[54:55], -1\n"
    "s_cbranch_scc1 .Lpoll3l\n"
    "buffer_load_dwordx2 v[236:237], v241, s[36:39], s81 offen offset:0 sc1\n"
    "s_waitcnt vmcnt(0)\n"
    "v_cmp_eq_u64 s[54:55], v[236:237], v[238:239]\n"
    ".Lpoll3l:\n"
    "ds_read_b32 v245, v247\n"
    "s_waitcnt lgkmcnt(0)\n"
    "v_cmp_le_u32 s[56:57], s59, v245\n"
    "s_add_u32 s58, s58, 1\n"
    "s_nop 1\n"
    "s_and_b64 s[54:55], s[54:55], s[56:57]\n"
    "s_cmp_eq_u64 s[54:55], -1\n"
    "s_cbranch_scc1 .Lpoll3r\n"
    "s_cmp_lt_u32 s58, 0x80000\n"
    "s_cbranch_scc1 .Lpoll3\n"
    "s_branch .Lpoll3r\n"
    "s_nop 0\n"
    "s_nop 0\n"
    "s_nop 0\n"
    "s_nop 0\n"
    "s_nop 0\n"
    "s_nop 0\n"
    "s_nop 0\n"
    ".Lpoll4:\n"
    "s_sleep 1\n"
    "v_cmp_eq_u64 s[54:55], v[236:237], v[238:239]\n"
    "s_nop 1\n"
    "s_cmp_eq_u64 s[54:55], -1\n"
    "s_cbranch_scc1 .Lpoll4l\n"
    "buffer_load_dwordx2 v[236:237], v241, s[36:39], s81 offen offset:0 sc1\n"
    "s_waitcnt vmcnt(0)\n"
    "v_cmp_eq_u64 s[54:55], v[236:237], v[238:239]\n"
    ".Lpoll4l:\n"
    "ds_read_b32 v245, v247\n"
    "s_waitcnt lgkmcnt(0)\n"
    "v_cmp_le_u32 s[56:57], s59, v245\n"
    "s_add_u32 s58, s58, 1\n"
    "s_nop 1\n"
    "s_and_b64 s[54:55], s[54:55], s[56:57]\n"
    "s_cmp_eq_u64 s[54:55], -1\n"
    "s_cbranch_scc1 .Lpoll4r\n"
    "s_cmp_lt_u32 s58, 0x80000\n"
    "s_cbranch_scc1 .Lpoll4\n"
    "s_branch .Lpoll4r\n"
    "s_nop 0\n"
    "s_nop 0\n"
    "s_nop 0\n"
    "s_nop 0\n"
    "s_nop 0\n"
    "s_nop 0\n"
    "s_nop 0\n"
    ".Lpt0:\n"
    "s_sleep 1\n"
    "v_cmp_eq_u64 s[54:55], v[236:237], v[238:239]\n"
    "s_nop 1\n"
    "s_cmp_eq_u64 s[54:55], -1\n"
    "s_cbranch_scc1 .Lpt0l\n"
    "buffer_load_dwordx2 v[236:237], v241, s[36:39], s81 offen offset:0 sc1\n"
    "s_waitcnt vmcnt(0)\n"
    "v_cmp_eq_u64 s[54:55], v[236:237], v[238:239]\n"
    ".Lpt0l:\n"
    "ds_read_b32 v245, v247\n"
    "s_waitcnt lgkmcnt(0)\n"
    "v_cmp_le_u32 s[56:57], s59, v245\n"
    "s_add_u32 s58, s58, 1\n"
    "s_nop 1\n"
    "s_and_b64 s[54:55], s[54:55], s[56:57]\n"
    "s_cmp_eq_u64 s[54:55], -1\n"
    "s_cbranch_scc1 .Lpt0r\n"
    "s_cmp_lt_u32 s58, 0x80000\n"
    "s_cbranch_scc1 .Lpt0\n"
    "s_branch .Lpt0r\n"
    "s_nop 0\n"
    "s_nop 0\n"
    "s_nop 0\n"
    ".Lpt1:\n"
    "s_sleep 1\n"
    "ds_read_b32 v245, v247\n"
    "s_waitcnt lgkmcnt(0)\n"
    "v_cmp_le_u32 s[56:57], s59, v245\n"
    "s_add_u32 s58, s58, 1\n"
    "s_nop 1\n"
    "s_cmp_eq_u64 s[56:57], -1\n"
    "s_cbranch_scc1 .Lpt1r\n"
    "s_cmp_lt_u32 s58, 0x80000\n"
    "s_cbranch_scc1 .Lpt1\n"
    "s_branch .Lpt1r\n"
    "s_nop 0\n"
    "s_nop 0\n"
    "s_nop 0\n"
    ".Lpt4:\n"
    "s_sleep 1\n"
    "ds_read_b32 v245, v247\n"
    "s_waitcnt lgkmcnt(0)\n"
    "v_cmp_le_u32 s[56:57], s59, v245\n"
    "s_add_u32 s58, s58, 1\n"
    "s_nop 1\n"
    "s_cmp_eq_u64 s[56:57], -1\n"
    "s_cbranch_scc1 .Lpt4r\n"
    "s_cmp_lt_u32 s58, 0x80000\n"
    "s_cbranch_scc1 .Lpt4\n"
    "s_branch .Lpt4r\n"
    "s_nop 0\n"
    ".Lpt3:\n"
    "s_sleep 1\n"
    "ds_read_b32 v245, v247\n"
    "s_waitcnt lgkmcnt(0)\n"
    "v_cmp_le_u32 s[56:57], s59, v245\n"
    "s_add_u32 s58, s58, 1\n"
    "s_nop 1\n"
    "s_cmp_eq_u64 s[56:57], -1\n"
    "s_cbranch_scc1 .Lpt3r\n"
    "s_cmp_lt_u32 s58, 0x80000\n"
    "s_cbranch_scc1 .Lpt3\n"
    "s_branch .Lpt3r\n"
    ".fill ((0x20000 - (.LwVe - .LwV) - (. - _Z8attn_fwdPKtPKiS2_S0_S0_S0_S0_PcPt)) & 4095) / 4, 4, 0xBF800000\n"
    ".LwV:\n"
    "s_branch .LwVe\n"
    ".LPV:\n"
    "v_mfma_f32_16x16x32_bf16 v[36:39], a[112:115], a[192:195], v[100:103]\n"
    "s_add_u32 s71, s71, s44\n"
    "ds_read_b128 a[136:139], v2 offset:13312\n"
    "v_cvt_pk_bf16_f32 v68, v4, v5\n"
    "v_mfma_f32_16x16x32_bf16 v[36:39], a[116:119], a[196:199], v[36:39]\n"
    "s_add_u32 s75, s71, s63\n"
    "ds_read_b128 a[140:143], v2 offset:15360\n"
    "v_cvt_pk_bf16_f32 v69, v6, v7\n"
    "v_mfma_f32_16x16x32_bf16 v[36:39], a[120:123], a[200:203], v[36:39]\n"
    "ds_read_b128 a[144:147], v2 offset:17408\n"
    "v_cvt_pk_bf16_f32 v76, v8, v9\n"
    "v_mfma_f32_16x16x32_bf16 v[36:39], a[124:127], a[204:207], v[36:39]\n"
    "ds_read_b128 a[148:151], v2 offset:19456\n"
    "v_cvt_pk_bf16_f32 v77, v10, v11\n"
    "v_mfma_f32_16x16x32_bf16 v[36:39], a[128:131], a[208:211], v[36:39]\n"
    "ds_read_b128 a[152:155], v2 offset:21504\n"
    "v_cvt_pk_bf16_f32 v84, v12, v13\n"
    "v_mfma_f32_16x16x32_bf16 v[36:39], a[132:135], a[212:215], v[36:39]\n"
    "ds_read_b128 a[156:159], v2 offset:23552\n"
    "v_cvt_pk_bf16_f32 v85, v14, v15\n"
    "v_mfma_f32_16x16x32_bf16 v[40:43], a[132:135], a[236:239], v[100:103]\n"
    "ds_read_b128 v[104:107], v250 offset:144\n"
    "v_cvt_pk_bf16_f32 v92, v16, v17\n"
    "v_mfma_f32_16x16x32_bf16 v[40:43], a[128:131], a[232:235], v[40:43]\n"
    "s_add_u32 s68, s68, 24576\n"
    "v_cvt_pk_bf16_f32 v93, v18, v19\n"
    "v_cvt_pk_bf16_f32 v70, v20, v21\n"
    "v_mfma_f32_16x16x32_bf16 v[40:43], a[124:127], a[228:231], v[40:43]\n"
    "s_cmp_ge_u32 s68, s69\n"
    "s_cselect_b32 s68, s65, s68\n"
    "v_cvt_pk_bf16_f32 v71, v22, v23\n"
    "v_mfma_f32_16x16x32_bf16 v[40:43], a[120:123], a[224:227], v[40:43]\n"
    "v_cvt_pk_bf16_f32 v78, v24, v25\n"
    "v_cvt_pk_bf16_f32 v79, v26, v27\n"
    "v_mfma_f32_16x16x32_bf16 v[40:43], a[116:119], a[220:223], v[40:43]\n"
    "v_cvt_pk_bf16_f32 v86, v28, v29\n"
    "v_cvt_pk_bf16_f32 v87, v30, v31\n"
    "v_mfma_f32_16x16x32_bf16 v[40:43], a[112:115], a[216:219], v[40:43]\n"
    "v_cvt_pk_bf16_f32 v94, v32, v33\n"
    "v_cvt_pk_bf16_f32 v95, v34, v35\n"
    "v_mfma_f32_16x16x32_bf16 v[44:47], a[112:115], a[240:243], v[100:103]\n"
    "v_exp_f32 v36, v36\n"
    "v_mfma_f32_16x16x32_bf16 v[44:47], a[116:119], a[244:247], v[44:47]\n"
    "v_exp_f32 v37, v37\n"
    "v_mfma_f32_16x16x32_bf16 v[44:47], a[120:123], a[248:251], v[44:47]\n"
    "v_exp_f32 v38, v38\n"
    "v_mfma_f32_16x16x32_bf16 v[44:47], a[124:127], a[252:255], v[44:47]\n"
    "v_exp_f32 v39, v39\n"
    "v_mfma_f32_16x16x32_bf16 v[44:47], a[128:131], a[160:163], v[44:47]\n"
    "v_cvt_pk_bf16_f32 v72, v36, v37\n"
    "v_cvt_pk_bf16_f32 v73, v38, v39\n"
    "v_mfma_f32_16x16x32_bf16 v[44:47], a[132:135], a[164:167], v[44:47]\n"
    "v_exp_f32 v40, v40\n"
    "v_mfma_f32_16x16x32_bf16 v[48:51], a[132:135], a[188:191], v[100:103]\n"
    "ds_read_b32 v245, v247\n"
    "v_mfma_f32_16x16x32_bf16 v[48:51], a[128:131], a[184:187], v[48:51]\n"
    "s_add_u32 m0, s68, 0\n"
    "v_exp_f32 v41, v41\n"
    "v_mfma_f32_16x16x32_bf16 v[48:51], a[124:127], a[180:183], v[48:51]\n"
    "v_exp_f32 v42, v42\n"
    "v_mfma_f32_16x16x32_bf16 v[48:51], a[120:123], a[176:179], v[48:51]\n"
    "v_exp_f32 v43, v43\n"
    "v_mfma_f32_16x16x32_bf16 v[48:51], a[116:119], a[172:175], v[48:51]\n"
    "s_waitcnt vmcnt(13)\n"
    "v_cmp_eq_u64 s[54:55], v[236:237], v[238:239]\n"
    "v_cvt_pk_bf16_f32 v80, v40, v41\n"
    "v_mfma_f32_16x16x32_bf16 v[48:51], a[112:115], a[168:171], v[48:51]\n"
    "s_waitcnt lgkmcnt(1)\n"
    "v_cvt_pk_bf16_f32 v81, v42, v43\n"
    "v_mfma_f32_16x16x32_bf16 v[52:55], a[136:139], a[192:195], v[104:107]\n"
    "s_waitcnt lgkmcnt(0)\n"
    "v_cmp_le_u32 s[56:57], s59, v245\n"
    "v_mfma_f32_16x16x32_bf16 v[52:55], a[140:143], a[196:199], v[52:55]\n"
    "v_exp_f32 v44, v44\n"
    "v_mfma_f32_16x16x32_bf16 v[52:55], a[144:147], a[200:203], v[52:55]\n"
    "s_and_b64 s[54:55], s[54:55], s[56:57]\n"
    "s_cmp_eq_u64 s[54:55], -1\n"
    "v_mfma_f32_16x16x32_bf16 v[52:55], a[148:151], a[204:207], v[52:55]\n"
    "s_cbranch_scc0 .Lpoll3\n"
    ".Lpoll3r:\n"
    "v_mfma_f32_16x16x32_bf16 v[52:55], a[152:155], a[208:211], v[52:55]\n"
    "buffer_load_dwordx4 v1, s[16:19], s67 offen offset:0 sc1 lds\n"
    "v_mfma_f32_16x16x32_bf16 v[52:55], a[156:159], a[212:215], v[52:55]\n"
    "ds_read_b128 a[112:115], v3 offset:0\n"
    "v_mfma_f32_16x16x32_bf16 v[56:59], a[156:159], a[236:239], v[104:107]\n"
    "ds_read_b128 a[116:119], v3 offset:2048\n"
    "v_mfma_f32_16x16x32_bf16 v[56:59], a[152:155], a[232:235], v[56:59]\n"
    "ds_read_b128 a[120:123], v3 offset:4096\n"
    "v_mfma_f32_16x16x32_bf16 v[56:59], a[148:151], a[228:231], v[56:59]\n"
    "buffer_load_dwordx4 v1, s[16:19], s67 offen offset:1024 sc1 lds\n"
    "v_mfma_f32_16x16x32_bf16 v[56:59], a[144:147], a[224:227], v[56:59]\n"
    "ds_read_b128 a[124:127], v3 offset:6144\n"
    "v_mfma_f32_16x16x32_bf16 v[56:59], a[140:143], a[220:223], v[56:59]\n"
    "ds_read_b128 a[128:131], v3 offset:8192\n"
    "v_mfma_f32_16x16x32_bf16 v[56:59], a[136:139], a[216:219], v[56:59]\n"
    "ds_read_b128 a[132:135], v3 offset:10240\n"
    "v_mfma_f32_16x16x32_bf16 v[60:63], a[136:139], a[240:243], v[104:107]\n"
    "buffer_load_dwordx4 v1, s[16:19], s67 offen offset:2048 sc1 lds\n"
    "v_mfma_f32_16x16x32_bf16 v[60:63], a[140:143], a[244:247], v[60:63]\n"
    "ds_read_b128 v[100:103], v250 offset:768\n"
    "v_mfma_f32_16x16x32_bf16 v[60:63], a[144:147], a[248:251], v[60:63]\n"
    "v_exp_f32 v52, v52\n"
    "v_mfma_f32_16x16x32_bf16 v[60:63], a[148:151], a[252:255], v[60:63]\n"
    "v_exp_f32 v53, v53\n"
    "v_mfma_f32_16x16x32_bf16 v[60:63], a[152:155], a[160:163], v[60:63]\n"
    "buffer_load_dwordx4 v1, s[16:19], s67 offen offset:3072 sc1 lds\n"
    "v_mfma_f32_16x16x32_bf16 v[60:63], a[156:159], a[164:167], v[60:63]\n"
    "s_add_u32 m0, s68, 4096\n"
    "v_exp_f32 v54, v54\n"
    "v_mfma_f32_16x16x32_bf16 v[64:67], a[156:159], a[188:191], v[104:107]\n"
    "s_add_u32 s83, s67, 4096\n"
    "v_exp_f32 v55, v55\n"
    "v_mfma_f32_16x16x32_bf16 v[64:67], a[152:155], a[184:187], v[64:67]\n"
    "v_cvt_pk_bf16_f32 v74, v52, v53\n"
    "v_cvt_pk_bf16_f32 v75, v54, v55\n"
    "v_mfma_f32_16x16x32_bf16 v[64:67], a[148:151], a[180:183], v[64:67]\n"
    "buffer_load_dwordx4 v1, s[16:19], s83 offen offset:0 sc1 lds\n"
    "v_mfma_f32_16x16x32_bf16 v[64:67], a[144:147], a[176:179], v[64:67]\n"
    "v_exp_f32 v56, v56\n"
    "v_mfma_f32_16x16x32_bf16 v[64:67], a[140:143], a[172:175], v[64:67]\n"
    "v_exp_f32 v57, v57\n"
    "v_mfma_f32_16x16x32_bf16 v[64:67], a[136:139], a[168:171], v[64:67]\n"
    "s_waitcnt lgkmcnt(0)\n"
    "v_exp_f32 v58, v58\n"
    "v_mfma_f32_16x16x32_bf16 v[4:7], a[112:115], a[192:195], v[100:103]\n"
    "buffer_load_dwordx4 v1, s[16:19], s83 offen offset:1024 sc1 lds\n"
    "v_mfma_f32_16x16x32_bf16 v[4:7], a[116:119], a[196:199], v[4:7]\n"
    "s_add_u32 m0, s64, 0\n"
    "ds_read_b128 a[136:139], v3 offset:1024\n"
    "v_cvt_pk_bf16_f32 v82, v56, v57\n"
    "v_mfma_f32_16x16x32_bf16 v[4:7], a[120:123], a[200:203], v[4:7]\n"
    "s_add_u32 s83, s67, 24576\n"
    "ds_read_b128 a[140:143], v3 offset:3072\n"
    "v_mfma_f32_16x16x32_bf16 v[4:7], a[124:127], a[204:207], v[4:7]\n"
    "ds_read_b128 a[144:147], v3 offset:5120\n"
    "v_mfma_f32_16x16x32_bf16 v[4:7], a[128:131], a[208:211], v[4:7]\n"
    "buffer_load_dwordx4 v1, s[16:19], s83 offen offset:0 sc1 lds\n"
    "v_mfma_f32_16x16x32_bf16 v[4:7], a[132:135], a[212:215], v[4:7]\n"
    "ds_read_b128 a[148:151], v3 offset:7168\n"
    "v_mfma_f32_16x16x32_bf16 v[8:11], a[132:135], a[236:239], v[100:103]\n"
    "ds_read_b128 a[152:155], v3 offset:9216\n"
    "v_mfma_f32_16x16x32_bf16 v[8:11], a[128:131], a[232:235], v[8:11]\n"
    "ds_read_b128 a[156:159], v3 offset:11264\n"
    "v_mfma_f32_16x16x32_bf16 v[8:11], a[124:127], a[228:231], v[8:11]\n"
    "buffer_load_dwordx4 v1, s[16:19], s83 offen offset:1024 sc1 lds\n"
    "v_mfma_f32_16x16x32_bf16 v[8:11], a[120:123], a[224:227], v[8:11]\n"
    "ds_read_b128 v[104:107], v250 offset:784\n"
    "v_mfma_f32_16x16x32_bf16 v[8:11], a[116:119], a[220:223], v[8:11]\n"
    "v_exp_f32 v59, v59\n"
    "v_mfma_f32_16x16x32_bf16 v[8:11], a[112:115], a[216:219], v[8:11]\n"
    "v_cvt_pk_bf16_f32 v83, v58, v59\n"
    "v_mfma_f32_16x16x32_bf16 v[12:15], a[112:115], a[240:243], v[100:103]\n"
    "buffer_load_dwordx4 v1, s[16:19], s83 offen offset:2048 sc1 lds\n"
    "v_mfma_f32_16x16x32_bf16 v[12:15], a[116:119], a[244:247], v[12:15]\n"
    "v_exp_f32 v45, v45\n"
    "v_mfma_f32_16x16x32_bf16 v[12:15], a[120:123], a[248:251], v[12:15]\n"
    "v_exp_f32 v46, v46\n"
    "v_mfma_f32_16x16x32_bf16 v[12:15], a[124:127], a[252:255], v[12:15]\n"
    "v_exp_f32 v47, v47\n"
    "v_mfma_f32_16x16x32_bf16 v[12:15], a[128:131], a[160:163], v[12:15]\n"
    "buffer_load_dwordx4 v1, s[16:19], s83 offen offset:3072 sc1 lds\n"
    "v_mfma_f32_16x16x32_bf16 v[12:15], a[132:135], a[164:167], v[12:15]\n"
    "s_add_u32 m0, s64, 4096\n"
    "v_exp_f32 v60, v60\n"
    "v_mfma_f32_16x16x32_bf16 v[16:19], a[132:135], a[188:191], v[100:103]\n"
    "s_add_u32 s83, s67, 28672\n"
    "v_exp_f32 v61, v61\n"
    "v_mfma_f32_16x16x32_bf16 v[16:19], a[128:131], a[184:187], v[16:19]\n"
    "v_exp_f32 v62, v62\n"
    "v_mfma_f32_16x16x32_bf16 v[16:19], a[124:127], a[180:183], v[16:19]\n"
    "buffer_load_dwordx4 v1, s[16:19], s83 offen offset:0 sc1 lds\n"
    "v_mfma_f32_16x16x32_bf16 v[16:19], a[120:123], a[176:179], v[16:19]\n"
    "v_exp_f32 v63, v63\n"
    "v_mfma_f32_16x16x32_bf16 v[16:19], a[116:119], a[172:175], v[16:19]\n"
    "v_cvt_pk_bf16_f32 v88, v44, v45\n"
    "v_cvt_pk_bf16_f32 v89, v46, v47\n"
    "v_mfma_f32_16x16x32_bf16 v[16:19], a[112:115], a[168:171], v[16:19]\n"
    "s_waitcnt lgkmcnt(0)\n"
    "v_cvt_pk_bf16_f32 v90, v60, v61\n"
    "v_cvt_pk_bf16_f32 v91, v62, v63\n"
    "v_mfma_f32_16x16x32_bf16 v[20:23], a[136:139], a[192:195], v[104:107]\n"
    "buffer_load_dwordx4 v1, s[16:19], s83 offen offset:1024 sc1 lds\n"
    "v_mfma_f32_16x16x32_bf16 v[20:23], a[140:143], a[196:199], v[20:23]\n"
    "s_add_u32 s67, s67, 49152\n"
    "ds_read_b128 a[112:115], v248 offset:0\n"
    "v_mfma_f32_16x16x32_bf16 v[20:23], a[144:147], a[200:203], v[20:23]\n"
    "s_add_u32 s59, s59, 4\n"
    "ds_read_b128 a[116:119], v248 offset:12288\n"
    "v_mfma_f32_16x16x32_bf16 v[20:23], a[148:151], a[204:207], v[20:23]\n"
    "s_waitcnt vmcnt(12)\n"
    "ds_read_b128 a[120:123], v248 offset:1024\n"
    "v_mfma_f32_16x16x32_bf16 v[20:23], a[152:155], a[208:211], v[20:23]\n"
    "buffer_store_dwordx2 v[238:239], v240, s[36:39], s80 offen offset:0 sc1\n"
    "v_mfma_f32_16x16x32_bf16 v[20:23], a[156:159], a[212:215], v[20:23]\n"
    "buffer_load_dwordx4 v[204:207], v242, s[40:43], s75 offen offset:0 nt\n"
    "v_mfma_f32_16x16x32_bf16 v[24:27], a[156:159], a[236:239], v[104:107]\n"
    "s_add_u32 s80, s80, 256\n"
    "ds_read_b128 a[124:127], v248 offset:13312\n"
    "v_mfma_f32_16x16x32_bf16 v[24:27], a[152:155], a[232:235], v[24:27]\n"
    "buffer_load_dwordx4 v[208:211], v242, s[40:43], s75 offen offset:768 nt\n"
    "v_mfma_f32_16x16x32_bf16 v[24:27], a[148:151], a[228:231], v[24:27]\n"
    "ds_read_b128 a[128:131], v248 offset:2048\n"
    "v_mfma_f32_16x16x32_bf16 v[24:27], a[144:147], a[224:227], v[24:27]\n"
    "buffer_load_dwordx4 v[212:215], v242, s[40:43], s75 offen offset:256 nt\n"
    "v_mfma_f32_16x16x32_bf16 v[24:27], a[140:143], a[220:223], v[24:27]\n"
    "ds_read_b128 a[132:135], v248 offset:14336\n"
    "v_mfma_f32_16x16x32_bf16 v[24:27], a[136:139], a[216:219], v[24:27]\n"
    "buffer_load_dwordx4 v[216:219], v242, s[40:43], s75 offen offset:1024 nt\n"
    "v_mfma_f32_16x16x32_bf16 v[28:31], a[136:139], a[240:243], v[104:107]\n"
    "v_exp_f32 v48, v48\n"
    "v_mfma_f32_16x16x32_bf16 v[28:31], a[140:143], a[244:247], v[28:31]\n"
    "buffer_load_dwordx4 v[220:223], v242, s[40:43], s75 offen offset:512 nt\n"
    "v_mfma_f32_16x16x32_bf16 v[28:31], a[144:147], a[248:251], v[28:31]\n"
    "v_exp_f32 v49, v49\n"
    "v_mfma_f32_16x16x32_bf16 v[28:31], a[148:151], a[252:255], v[28:31]\n"
    "buffer_load_dwordx4 v[224:227], v242, s[40:43], s75 offen offset:1280 nt\n"
    "v_mfma_f32_16x16x32_bf16 v[28:31], a[152:155], a[160:163], v[28:31]\n"
    "v_exp_f32 v50, v50\n"
    "v_mfma_f32_16x16x32_bf16 v[28:31], a[156:159], a[164:167], v[28:31]\n"
    "v_exp_f32 v51, v51\n"
    "v_mfma_f32_16x16x32_bf16 v[32:35], a[156:159], a[188:191], v[104:107]\n"
    "v_exp_f32 v64, v64\n"
    "v_mfma_f32_16x16x32_bf16 v[32:35], a[152:155], a[184:187], v[32:35]\n"
    "v_exp_f32 v65, v65\n"
    "v_mfma_f32_16x16x32_bf16 v[32:35], a[148:151], a[180:183], v[32:35]\n"
    "v_exp_f32 v66, v66\n"
    "v_mfma_f32_16x16x32_bf16 v[32:35], a[144:147], a[176:179], v[32:35]\n"
    "v_exp_f32 v67, v67\n"
    "v_mfma_f32_16x16x32_bf16 v[32:35], a[140:143], a[172:175], v[32:35]\n"
    "v_cvt_pk_bf16_f32 v96, v48, v49\n"
    "v_cvt_pk_bf16_f32 v97, v50, v51\n"
    "v_mfma_f32_16x16x32_bf16 v[32:35], a[136:139], a[168:171], v[32:35]\n"
    "s_waitcnt lgkmcnt(0)\n"
    "v_cvt_pk_bf16_f32 v98, v64, v65\n"
    "v_cvt_pk_bf16_f32 v99, v66, v67\n"
    "v_mfma_f32_16x16x32_bf16 v[124:127], a[112:115], v[68:71], v[124:127]\n"
    "ds_read_b128 a[136:139], v248 offset:3072\n"
    "v_mfma_f32_16x16x32_bf16 v[124:127], a[116:119], v[72:75], v[124:127]\n"
    "ds_read_b128 a[140:143], v248 offset:15360\n"
    "v_mfma_f32_16x16x32_bf16 v[128:131], a[124:127], v[72:75], v[128:131]\n"
    "ds_read_b128 a[144:147], v248 offset:4096\n"
    "v_mfma_f32_16x16x32_bf16 v[128:131], a[120:123], v[68:71], v[128:131]\n"
    "ds_read_b128 a[148:151], v248 offset:16384\n"
    "v_mfma_f32_16x16x32_bf16 v[132:135], a[128:131], v[68:71], v[132:135]\n"
    "ds_read_b128 a[152:155], v248 offset:5120\n"
    "v_mfma_f32_16x16x32_bf16 v[132:135], a[132:135], v[72:75], v[132:135]\n"
    "ds_read_b128 a[156:159], v248 offset:17408\n"
    "v_mfma_f32_16x16x32_bf16 v[148:151], a[132:135], v[80:83], v[148:151]\n"
    "ds_read_b128 v[104:107], v251 offset:0\n"
    "v_mfma_f32_16x16x32_bf16 v[148:151], a[128:131], v[76:79], v[148:151]\n"
    "v_exp_f32 v4, v4\n"
    "v_mfma_f32_16x16x32_bf16 v[144:147], a[120:123], v[76:79], v[144:147]\n"
    "v_exp_f32 v5, v5\n"
    "v_mfma_f32_16x16x32_bf16 v[144:147], a[124:127], v[80:83], v[144:147]\n"
    "v_exp_f32 v6, v6\n"
    "v_mfma_f32_16x16x32_bf16 v[140:143], a[116:119], v[80:83], v[140:143]\n"
    "v_mfma_f32_16x16x32_bf16 v[140:143], a[112:115], v[76:79], v[140:143]\n"
    "v_mfma_f32_16x16x32_bf16 v[156:159], a[112:115], v[84:87], v[156:159]\n"
    "v_exp_f32 v7, v7\n"
    "v_mfma_f32_16x16x32_bf16 v[156:159], a[116:119], v[88:91], v[156:159]\n"
    "v_mfma_f32_16x16x32_bf16 v[160:163], a[124:127], v[88:91], v[160:163]\n"
    "v_exp_f32 v8, v8\n"
    "v_mfma_f32_16x16x32_bf16 v[160:163], a[120:123], v[84:87], v[160:163]\n"
    "v_mfma_f32_16x16x32_bf16 v[164:167], a[128:131], v[84:87], v[164:167]\n"
    "v_mfma_f32_16x16x32_bf16 v[164:167], a[132:135], v[88:91], v[164:167]\n"
    "v_exp_f32 v9, v9\n"
    "v_mfma_f32_16x16x32_bf16 v[180:183], a[132:135], v[96:99], v[180:183]\n"
    "v_mfma_f32_16x16x32_bf16 v[180:183], a[128:131], v[92:95], v[180:183]\n"
    "v_mfma_f32_16x16x32_bf16 v[176:179], a[120:123], v[92:95], v[176:179]\n"
    "v_exp_f32 v10, v10\n"
    "v_mfma_f32_16x16x32_bf16 v[176:179], a[124:127], v[96:99], v[176:179]\n"
    "v_mfma_f32_16x16x32_bf16 v[172:175], a[116:119], v[96:99], v[172:175]\n"
    "v_exp_f32 v11, v11\n"
    "v_mfma_f32_16x16x32_bf16 v[172:175], a[112:115], v[92:95], v[172:175]\n"
    "s_waitcnt lgkmcnt(0)\n"
    "v_mfma_f32_16x16x32_bf16 v[136:139], a[136:139], v[68:71], v[136:139]\n"
    "ds_read_b128 a[112:115], v248 offset:6144\n"
    "v_mfma_f32_16x16x32_bf16 v[136:139], a[140:143], v[72:75], v[136:139]\n"
    "ds_read_b128 a[116:119], v248 offset:18432\n"
    "v_mfma_f32_16x16x32_bf16 v[188:191], a[148:151], v[72:75], v[188:191]\n"
    "ds_read_b128 a[120:123], v248 offset:7168\n"
    "v_mfma_f32_16x16x32_bf16 v[188:191], a[144:147], v[68:71], v[188:191]\n"
    "ds_read_b128 a[124:127], v248 offset:19456\n"
    "v_mfma_f32_16x16x32_bf16 v[192:195], a[152:155], v[68:71], v[192:195]\n"
    "ds_read_b128 a[128:131], v248 offset:8192\n"
    "v_mfma_f32_16x16x32_bf16 v[192:195], a[156:159], v[72:75], v[192:195]\n"
    "ds_read_b128 a[132:135], v248 offset:20480\n"
    "v_smfmac_f32_16x16x64_bf16 v[108:111], v[104:107], v[68:75], v253\n"
    "s_add_u32 s81, s81, 128\n"
    "s_cmp_gt_u32 s81, 4096\n"
    "s_cselect_b32 s81, 0, s81\n"
    "v_smfmac_f32_16x16x64_bf16 v[112:115], v[104:107], v[76:83], v253\n"
    "v_mfma_f32_16x16x32_bf16 a[4:7], a[156:159], v[80:83], a[4:7]\n"
    "v_exp_f32 v12, v12\n"
    "v_mfma_f32_16x16x32_bf16 a[4:7], a[152:155], v[76:79], a[4:7]\n"
    "v_exp_f32 v13, v13\n"
    "v_mfma_f32_16x16x32_bf16 a[0:3], a[144:147], v[76:79], a[0:3]\n"
    "v_exp_f32 v14, v14\n"
    "v_mfma_f32_16x16x32_bf16 a[0:3], a[148:151], v[80:83], a[0:3]\n"
    "v_exp_f32 v15, v15\n"
    "v_mfma_f32_16x16x32_bf16 v[152:155], a[140:143], v[80:83], v[152:155]\n"
    "v_exp_f32 v16, v16\n"
    "v_mfma_f32_16x16x32_bf16 v[152:155], a[136:139], v[76:79], v[152:155]\n"
    "v_mfma_f32_16x16x32_bf16 v[168:171], a[136:139], v[84:87], v[168:171]\n"
    "v_exp_f32 v17, v17\n"
    "v_mfma_f32_16x16x32_bf16 v[168:171], a[140:143], v[88:91], v[168:171]\n"
    "v_mfma_f32_16x16x32_bf16 a[16:19], a[148:151], v[88:91], a[16:19]\n"
    "v_mfma_f32_16x16x32_bf16 a[16:19], a[144:147], v[84:87], a[16:19]\n"
    "v_exp_f32 v18, v18\n"
    "v_mfma_f32_16x16x32_bf16 a[20:23], a[152:155], v[84:87], a[20:23]\n"
    "v_mfma_f32_16x16x32_bf16 a[20:23], a[156:159], v[88:91], a[20:23]\n"
    "v_smfmac_f32_16x16x64_bf16 v[116:119], v[104:107], v[84:91], v253\n"
    "s_add_u32 s52, s52, s45\n"
    "v_smfmac_f32_16x16x64_bf16 v[120:123], v[104:107], v[92:99], v253\n"
    "v_mfma_f32_16x16x32_bf16 a[36:39], a[156:159], v[96:99], a[36:39]\n"
    "v_exp_f32 v19, v19\n"
    "v_mfma_f32_16x16x32_bf16 a[36:39], a[152:155], v[92:95], a[36:39]\n"
    "v_exp_f32 v20, v20\n"
    "v_mfma_f32_16x16x32_bf16 a[32:35], a[144:147], v[92:95], a[32:35]\n"
    "v_mfma_f32_16x16x32_bf16 a[32:35], a[148:151], v[96:99], a[32:35]\n"
    "v_exp_f32 v21, v21\n"
    "v_mfma_f32_16x16x32_bf16 v[184:187], a[140:143], v[96:99], v[184:187]\n"
    "v_mfma_f32_16x16x32_bf16 v[184:187], a[136:139], v[92:95], v[184:187]\n"
    "s_waitcnt lgkmcnt(0)\n"
    "v_mfma_f32_16x16x32_bf16 v[196:199], a[112:115], v[68:71], v[196:199]\n"
    "ds_read_b128 a[136:139], v248 offset:9216\n"
    "v_mfma_f32_16x16x32_bf16 v[196:199], a[116:119], v[72:75], v[196:199]\n"
    "ds_read_b128 a[140:143], v248 offset:21504\n"
    "v_mfma_f32_16x16x32_bf16 v[200:203], a[124:127], v[72:75], v[200:203]\n"
    "ds_read_b128 a[144:147], v248 offset:10240\n"
    "v_mfma_f32_16x16x32_bf16 v[200:203], a[120:123], v[68:71], v[200:203]\n"
    "ds_read_b128 a[148:151], v248 offset:22528\n"
    "v_mfma_f32_16x16x32_bf16 a[48:51], a[128:131], v[68:71], a[48:51]\n"
    "buffer_load_dwordx2 v[236:237], v241, s[36:39], s81 offen offset:0 sc1\n"
    "v_mfma_f32_16x16x32_bf16 a[48:51], a[132:135], v[72:75], a[48:51]\n"
    "ds_read_b128 a[152:155], v248 offset:11264\n"
    "v_mfma_f32_16x16x32_bf16 a[64:67], a[132:135], v[80:83], a[64:67]\n"
    "ds_read_b128 a[156:159], v248 offset:23552\n"
    "v_mfma_f32_16x16x32_bf16 a[64:67], a[128:131], v[76:79], a[64:67]\n"
    "v_exp_f32 v22, v22\n"
    "v_mfma_f32_16x16x32_bf16 a[12:15], a[120:123], v[76:79], a[12:15]\n"
    "v_exp_f32 v23, v23\n"
    "v_mfma_f32_16x16x32_bf16 a[12:15], a[124:127], v[80:83], a[12:15]\n"
    "v_exp_f32 v24, v24\n"
    "v_mfma_f32_16x16x32_bf16 a[8:11], a[116:119], v[80:83], a[8:11]\n"
    "v_exp_f32 v25, v25\n"
    "v_mfma_f32_16x16x32_bf16 a[8:11], a[112:115], v[76:79], a[8:11]\n"
    "v_exp_f32 v26, v26\n"
    "v_mfma_f32_16x16x32_bf16 a[24:27], a[112:115], v[84:87], a[24:27]\n"
    "v_mfma_f32_16x16x32_bf16 a[24:27], a[116:119], v[88:91], a[24:27]\n"
    "v_exp_f32 v27, v27\n"
    "v_mfma_f32_16x16x32_bf16 a[28:31], a[124:127], v[88:91], a[28:31]\n"
    "v_mfma_f32_16x16x32_bf16 a[28:31], a[120:123], v[84:87], a[28:31]\n"
    "v_mfma_f32_16x16x32_bf16 a[80:83], a[128:131], v[84:87], a[80:83]\n"
    "v_exp_f32 v28, v28\n"
    "v_mfma_f32_16x16x32_bf16 a[80:83], a[132:135], v[88:91], a[80:83]\n"
    "v_mfma_f32_16x16x32_bf16 a[96:99], a[132:135], v[96:99], a[96:99]\n"
    "v_mfma_f32_16x16x32_bf16 a[96:99], a[128:131], v[92:95], a[96:99]\n"
    "v_exp_f32 v29, v29\n"
    "v_mfma_f32_16x16x32_bf16 a[44:47], a[120:123], v[92:95], a[44:47]\n"
    "v_mfma_f32_16x16x32_bf16 a[44:47], a[124:127], v[96:99], a[44:47]\n"
    "v_exp_f32 v30, v30\n"
    "v_mfma_f32_16x16x32_bf16 a[40:43], a[116:119], v[96:99], a[40:43]\n"
    "v_mfma_f32_16x16x32_bf16 a[40:43], a[112:115], v[92:95], a[40:43]\n"
    "s_waitcnt lgkmcnt(0)\n"
    "v_mfma_f32_16x16x32_bf16 a[52:55], a[136:139], v[68:71], a[52:55]\n"
    "ds_read_b128 a[112:115], v3 offset:12288\n"
    "v_mfma_f32_16x16x32_bf16 a[52:55], a[140:143], v[72:75], a[52:55]\n"
    "ds_read_b128 a[116:119], v3 offset:14336\n"
    "v_mfma_f32_16x16x32_bf16 a[56:59], a[148:151], v[72:75], a[56:59]\n"
    "ds_read_b128 a[120:123], v3 offset:16384\n"
    "v_mfma_f32_16x16x32_bf16 a[56:59], a[144:147], v[68:71], a[56:59]\n"
    "ds_read_b128 a[124:127], v3 offset:18432\n"
    "v_mfma_f32_16x16x32_bf16 a[60:63], a[152:155], v[68:71], a[60:63]\n"
    "ds_read_b128 a[128:131], v3 offset:20480\n"
    "v_mfma_f32_16x16x32_bf16 a[60:63], a[156:159], v[72:75], a[60:63]\n"
    "ds_read_b128 a[132:135], v3 offset:22528\n"
    "v_mfma_f32_16x16x32_bf16 a[76:79], a[156:159], v[80:83], a[76:79]\n"
    "ds_read_b128 v[100:103], v250 offset:896\n"
    "v_mfma_f32_16x16x32_bf16 a[76:79], a[152:155], v[76:79], a[76:79]\n"
    "v_exp_f32 v31, v31\n"
    "v_mfma_f32_16x16x32_bf16 a[72:75], a[144:147], v[76:79], a[72:75]\n"
    "v_exp_f32 v32, v32\n"
    "v_mfma_f32_16x16x32_bf16 a[72:75], a[148:151], v[80:83], a[72:75]\n"
    "v_exp_f32 v33, v33\n"
    "v_mfma_f32_16x16x32_bf16 a[68:71], a[140:143], v[80:83], a[68:71]\n"
    "v_exp_f32 v34, v34\n"
    "v_mfma_f32_16x16x32_bf16 a[68:71], a[136:139], v[76:79], a[68:71]\n"
    "v_exp_f32_e64 v35, v35\n"
    "v_mfma_f32_16x16x32_bf16 a[84:87], a[136:139], v[84:87], a[84:87]\n"
    "v_mfma_f32_16x16x32_bf16 a[84:87], a[140:143], v[88:91], a[84:87]\n"
    "v_mfma_f32_16x16x32_bf16 a[88:91], a[148:151], v[88:91], a[88:91]\n"
    "s_waitcnt vmcnt(8)\n"
    "ds_add_u32 v247, v246\n"
    "v_mfma_f32_16x16x32_bf16 a[88:91], a[144:147], v[84:87], a[88:91]\n"
    "v_mfma_f32_16x16x32_bf16 a[92:95], a[152:155], v[84:87], a[92:95]\n"
    "v_mfma_f32_16x16x32_bf16 a[92:95], a[156:159], v[88:91], a[92:95]\n"
    "v_mfma_f32_16x16x32_bf16 a[108:111], a[156:159], v[96:99], a[108:111]\n"
    "v_mfma_f32_16x16x32_bf16 a[108:111], a[152:155], v[92:95], a[108:111]\n"
    "v_mfma_f32_16x16x32_bf16 a[104:107], a[144:147], v[92:95], a[104:107]\n"
    "s_sub_u32 s74, s68, s65\n"
    "v_mfma_f32_16x16x32_bf16 a[104:107], a[148:151], v[96:99], a[104:107]\n"
    "s_add_u32 s66, s66, .Lend-.Lend2+(1)\n"
    "v_mov_b32_e64 v2, v3\n"
    "v_add_u32_e64 v3, s74, v252\n"
    "v_mfma_f32_16x16x32_bf16 a[100:103], a[140:143], v[96:99], a[100:103]\n"
    "s_waitcnt lgkmcnt(1)\n"
    "s_cmp_eq_u32 s66, .Lend-.Lend2+(29)\n"
    "v_mfma_f32_16x16x32_bf16 a[100:103], a[136:139], v[92:95], a[100:103]\n"
    ".LwVe:\n"
    "s_cbranch_scc1 .LtailP\n"
    ".LNV:\n"
    "v_mfma_f32_16x16x32_bf16 v[36:39], a[112:115], a[192:195], v[100:103]\n"
    "ds_read_b128 a[136:139], v2 offset:13312\n"
    "v_cvt_pk_bf16_f32 v68, v4, v5\n"
    "v_mfma_f32_16x16x32_bf16 v[36:39], a[116:119], a[196:199], v[36:39]\n"
    "ds_read_b128 a[140:143], v2 offset:15360\n"
    "v_mfma_f32_16x16x32_bf16 v[36:39], a[120:123], a[200:203], v[36:39]\n"
    "ds_read_b128 a[144:147], v2 offset:17408\n"
    "v_mfma_f32_16x16x32_bf16 v[36:39], a[124:127], a[204:207], v[36:39]\n"
    "ds_read_b128 a[148:151], v2 offset:19456\n"
    "v_cvt_pk_bf16_f32 v69, v6, v7\n"
    "v_mfma_f32_16x16x32_bf16 v[36:39], a[128:131], a[208:211], v[36:39]\n"
    "ds_read_b128 a[152:155], v2 offset:21504\n"
    "v_mfma_f32_16x16x32_bf16 v[36:39], a[132:135], a[212:215], v[36:39]\n"
    "ds_read_b128 a[156:159], v2 offset:23552\n"
    "v_mfma_f32_16x16x32_bf16 v[40:43], a[132:135], a[236:239], v[100:103]\n"
    "ds_read_b128 v[104:107], v250 offset:912\n"
    "v_mfma_f32_16x16x32_bf16 v[40:43], a[128:131], a[232:235], v[40:43]\n"
    "s_add_u32 s68, s68, 24576\n"
    "v_cvt_pk_bf16_f32 v76, v8, v9\n"
    "v_mfma_f32_16x16x32_bf16 v[40:43], a[124:127], a[228:231], v[40:43]\n"
    "s_cmp_ge_u32 s68, s69\n"
    "s_cselect_b32 s68, s65, s68\n"
    "v_mfma_f32_16x16x32_bf16 v[40:43], a[120:123], a[224:227], v[40:43]\n"
    "v_exp_f32 v36, v36\n"
    "v_mfma_f32_16x16x32_bf16 v[40:43], a[116:119], a[220:223], v[40:43]\n"
    "v_exp_f32 v37, v37\n"
    "v_mfma_f32_16x16x32_bf16 v[40:43], a[112:115], a[216:219], v[40:43]\n"
    "v_cvt_pk_bf16_f32 v77, v10, v11\n"
    "v_cvt_pk_bf16_f32 v72, v36, v37\n"
    "v_mfma_f32_16x16x32_bf16 v[44:47], a[112:115], a[240:243], v[100:103]\n"
    "v_mfma_f32_16x16x32_bf16 v[44:47], a[116:119], a[244:247], v[44:47]\n"
    "v_exp_f32 v38, v38\n"
    "v_mfma_f32_16x16x32_bf16 v[44:47], a[120:123], a[248:251], v[44:47]\n"
    "v_mfma_f32_16x16x32_bf16 v[44:47], a[124:127], a[252:255], v[44:47]\n"
    "v_cvt_pk_bf16_f32 v84, v12, v13\n"
    "v_mfma_f32_16x16x32_bf16 v[44:47], a[128:131], a[160:163], v[44:47]\n"
    "v_exp_f32 v39, v39\n"
    "v_mfma_f32_16x16x32_bf16 v[44:47], a[132:135], a[164:167], v[44:47]\n"
    "v_cvt_pk_bf16_f32 v73, v38, v39\n"
    "v_mfma_f32_16x16x32_bf16 v[48:51], a[132:135], a[188:191], v[100:103]\n"
    "ds_read_b32 v245, v247\n"
    "v_cvt_pk_bf16_f32 v85, v14, v15\n"
    "v_mfma_f32_16x16x32_bf16 v[48:51], a[128:131], a[184:187], v[48:51]\n"
    "s_add_u32 m0, s68, 0\n"
    "v_exp_f32 v40, v40\n"
    "v_mfma_f32_16x16x32_bf16 v[48:51], a[124:127], a[180:183], v[48:51]\n"
    "v_mfma_f32_16x16x32_bf16 v[48:51], a[120:123], a[176:179], v[48:51]\n"
    "v_mfma_f32_16x16x32_bf16 v[48:51], a[116:119], a[172:175], v[48:51]\n"
    "s_waitcnt vmcnt(0)\n"
    "v_cmp_eq_u64 s[54:55], v[236:237], v[238:239]\n"
    "v_cvt_pk_bf16_f32 v92, v16, v17\n"
    "v_mfma_f32_16x16x32_bf16 v[48:51], a[112:115], a[168:171], v[48:51]\n"
    "s_waitcnt lgkmcnt(1)\n"
    "v_exp_f32 v41, v41\n"
    "v_mfma_f32_16x16x32_bf16 v[52:55], a[136:139], a[192:195], v[104:107]\n"
    "s_waitcnt lgkmcnt(0)\n"
    "v_cmp_le_u32 s[56:57], s59, v245\n"
    "v_cvt_pk_bf16_f32 v80, v40, v41\n"
    "v_mfma_f32_16x16x32_bf16 v[52:55], a[140:143], a[196:199], v[52:55]\n"
    "s_waitcnt vmcnt(1)\n"
    "v_exp_f32 v42, v42\n"
    "v_mfma_f32_16x16x32_bf16 v[52:55], a[144:147], a[200:203], v[52:55]\n"
    "s_and_b64 s[54:55], s[54:55], s[56:57]\n"
    "s_cmp_eq_u64 s[54:55], -1\n"
    "v_mfma_f32_16x16x32_bf16 v[52:55], a[148:151], a[204:207], v[52:55]\n"
    "s_cbranch_scc0 .Lpoll4\n"
    ".Lpoll4r:\n"
    "v_mfma_f32_16x16x32_bf16 v[52:55], a[152:155], a[208:211], v[52:55]\n"
    "buffer_load_dwordx4 v1, s[16:19], s67 offen offset:0 sc1 lds\n"
    "v_mfma_f32_16x16x32_bf16 v[52:55], a[156:159], a[212:215], v[52:55]\n"
    "s_add_u32 s76, s52, s60\n"
    "ds_read_b128 a[112:115], v3 offset:0\n"
    "v_cvt_pk_bf16_f32 v93, v18, v19\n"
    "v_mfma_f32_16x16x32_bf16 v[56:59], a[156:159], a[236:239], v[104:107]\n"
    "s_add_u32 s77, s52, s61\n"
    "ds_read_b128 a[116:119], v3 offset:2048\n"
    "v_cvt_pk_bf16_f32 v70, v20, v21\n"
    "v_mfma_f32_16x16x32_bf16 v[56:59], a[152:155], a[232:235], v[56:59]\n"
    "ds_read_b128 a[120:123], v3 offset:4096\n"
    "v_mfma_f32_16x16x32_bf16 v[56:59], a[148:151], a[228:231], v[56:59]\n"
    "buffer_load_dwordx4 v1, s[16:19], s67 offen offset:1024 sc1 lds\n"
    "v_mfma_f32_16x16x32_bf16 v[56:59], a[144:147], a[224:227], v[56:59]\n"
    "ds_read_b128 a[124:127], v3 offset:6144\n"
    "v_cvt_pk_bf16_f32 v71, v22, v23\n"
    "v_mfma_f32_16x16x32_bf16 v[56:59], a[140:143], a[220:223], v[56:59]\n"
    "s_add_u32 s78, s52, s62\n"
    "ds_read_b128 a[128:131], v3 offset:8192\n"
    "v_mfma_f32_16x16x32_bf16 v[56:59], a[136:139], a[216:219], v[56:59]\n"
    "ds_read_b128 a[132:135], v3 offset:10240\n"
    "v_mfma_f32_16x16x32_bf16 v[60:63], a[136:139], a[240:243], v[104:107]\n"
    "buffer_load_dwordx4 v1, s[16:19], s67 offen offset:2048 sc1 lds\n"
    "v_mfma_f32_16x16x32_bf16 v[60:63], a[140:143], a[244:247], v[60:63]\n"
    "ds_read_b128 v[100:103], v250 offset:1536\n"
    "v_cvt_pk_bf16_f32 v78, v24, v25\n"
    "v_mfma_f32_16x16x32_bf16 v[60:63], a[144:147], a[248:251], v[60:63]\n"
    "v_exp_f32 v43, v43\n"
    "v_mfma_f32_16x16x32_bf16 v[60:63], a[148:151], a[252:255], v[60:63]\n"
    "v_cvt_pk_bf16_f32 v81, v42, v43\n"
    "v_cvt_pk_u16_u32 v204, v204, v208\n"
    "v_mfma_f32_16x16x32_bf16 v[60:63], a[152:155], a[160:163], v[60:63]\n"
    "buffer_load_dwordx4 v1, s[16:19], s67 offen offset:3072 sc1 lds\n"
    "v_mfma_f32_16x16x32_bf16 v[60:63], a[156:159], a[164:167], v[60:63]\n"
    "s_add_u32 m0, s68, 4096\n"
    "v_cvt_pk_bf16_f32 v79, v26, v27\n"
    "v_mfma_f32_16x16x32_bf16 v[64:67], a[156:159], a[188:191], v[104:107]\n"
    "s_add_u32 s83, s67, 4096\n"
    "v_exp_f32 v44, v44\n"
    "v_mfma_f32_16x16x32_bf16 v[64:67], a[152:155], a[184:187], v[64:67]\n"
    "v_exp_f32 v45, v45\n"
    "v_mfma_f32_16x16x32_bf16 v[64:67], a[148:151], a[180:183], v[64:67]\n"
    "buffer_load_dwordx4 v1, s[16:19], s83 offen offset:0 sc1 lds\n"
    "v_mfma_f32_16x16x32_bf16 v[64:67], a[144:147], a[176:179], v[64:67]\n"
    "v_cvt_pk_bf16_f32 v86, v28, v29\n"
    "v_cvt_pk_bf16_f32 v88, v44, v45\n"
    "v_mfma_f32_16x16x32_bf16 v[64:67], a[140:143], a[172:175], v[64:67]\n"
    "v_exp_f32 v46, v46\n"
    "v_mfma_f32_16x16x32_bf16 v[64:67], a[136:139], a[168:171], v[64:67]\n"
    "s_waitcnt lgkmcnt(0)\n"
    "v_exp_f32 v47, v47\n"
    "v_mfma_f32_16x16x32_bf16 v[4:7], a[112:115], a[192:195], v[100:103]\n"
    "buffer_load_dwordx4 v1, s[16:19], s83 offen offset:1024 sc1 lds\n"
    "v_mfma_f32_16x16x32_bf16 v[4:7], a[116:119], a[196:199], v[4:7]\n"
    "s_add_u32 m0, s70, 0\n"
    "ds_read_b128 a[136:139], v3 offset:1024\n"
    "v_cvt_pk_bf16_f32 v87, v30, v31\n"
    "v_mfma_f32_16x16x32_bf16 v[4:7], a[120:123], a[200:203], v[4:7]\n"
    "s_add_u32 s83, s67, 24576\n"
    "ds_read_b128 a[140:143], v3 offset:3072\n"
    "v_cvt_pk_bf16_f32 v89, v46, v47\n"
    "v_mfma_f32_16x16x32_bf16 v[4:7], a[124:127], a[204:207], v[4:7]\n"
    "ds_read_b128 a[144:147], v3 offset:5120\n"
    "v_cvt_pk_u16_u32 v205, v205, v209\n"
    "v_mfma_f32_16x16x32_bf16 v[4:7], a[128:131], a[208:211], v[4:7]\n"
    "buffer_load_dwordx4 v1, s[16:19], s83 offen offset:0 sc1 lds\n"
    "v_mfma_f32_16x16x32_bf16 v[4:7], a[132:135], a[212:215], v[4:7]\n"
    "ds_read_b128 a[148:151], v3 offset:7168\n"
    "v_cvt_pk_bf16_f32 v94, v32, v33\n"
    "v_mfma_f32_16x16x32_bf16 v[8:11], a[132:135], a[236:239], v[100:103]\n"
    "ds_read_b128 a[152:155], v3 offset:9216\n"
    "v_cvt_pk_u16_u32 v206, v206, v210\n"
    "v_mfma_f32_16x16x32_bf16 v[8:11], a[128:131], a[232:235], v[8:11]\n"
    "ds_read_b128 a[156:159], v3 offset:11264\n"
    "v_cvt_pk_u16_u32 v207, v207, v211\n"
    "v_mfma_f32_16x16x32_bf16 v[8:11], a[124:127], a[228:231], v[8:11]\n"
    "buffer_load_dwordx4 v1, s[16:19], s83 offen offset:1024 sc1 lds\n"
    "v_mfma_f32_16x16x32_bf16 v[8:11], a[120:123], a[224:227], v[8:11]\n"
    "ds_read_b128 v[104:107], v250 offset:1552\n"
    "v_cvt_pk_bf16_f32 v95, v34, v35\n"
    "v_mfma_f32_16x16x32_bf16 v[8:11], a[116:119], a[220:223], v[8:11]\n"
    "v_exp_f32 v52, v52\n"
    "v_mfma_f32_16x16x32_bf16 v[8:11], a[112:115], a[216:219], v[8:11]\n"
    "v_exp_f32 v53, v53\n"
    "v_mfma_f32_16x16x32_bf16 v[12:15], a[112:115], a[240:243], v[100:103]\n"
    "buffer_load_dwordx4 v1, s[16:19], s83 offen offset:2048 sc1 lds\n"
    "v_mfma_f32_16x16x32_bf16 v[12:15], a[116:119], a[244:247], v[12:15]\n"
    "v_exp_f32 v54, v54\n"
    "v_mfma_f32_16x16x32_bf16 v[12:15], a[120:123], a[248:251], v[12:15]\n"
    "v_exp_f32 v55, v55\n"
    "v_mfma_f32_16x16x32_bf16 v[12:15], a[124:127], a[252:255], v[12:15]\n"
    "v_cvt_pk_bf16_f32 v74, v52, v53\n"
    "v_cvt_pk_bf16_f32 v75, v54, v55\n"
    "v_mfma_f32_16x16x32_bf16 v[12:15], a[128:131], a[160:163], v[12:15]\n"
    "buffer_load_dwordx4 v1, s[16:19], s83 offen offset:3072 sc1 lds\n"
    "v_mfma_f32_16x16x32_bf16 v[12:15], a[132:135], a[164:167], v[12:15]\n"
    "s_add_u32 m0, s70, 4096\n"
    "v_exp_f32 v56, v56\n"
    "v_mfma_f32_16x16x32_bf16 v[16:19], a[132:135], a[188:191], v[100:103]\n"
    "s_add_u32 s83, s67, 28672\n"
    "v_exp_f32 v48, v48\n"
    "v_mfma_f32_16x16x32_bf16 v[16:19], a[128:131], a[184:187], v[16:19]\n"
    "v_exp_f32 v49, v49\n"
    "v_mfma_f32_16x16x32_bf16 v[16:19], a[124:127], a[180:183], v[16:19]\n"
    "buffer_load_dwordx4 v1, s[16:19], s83 offen offset:0 sc1 lds\n"
    "v_mfma_f32_16x16x32_bf16 v[16:19], a[120:123], a[176:179], v[16:19]\n"
    "v_exp_f32 v57, v57\n"
    "v_mfma_f32_16x16x32_bf16 v[16:19], a[116:119], a[172:175], v[16:19]\n"
    "v_cvt_pk_bf16_f32 v82, v56, v57\n"
    "v_cvt_pk_bf16_f32 v96, v48, v49\n"
    "v_mfma_f32_16x16x32_bf16 v[16:19], a[112:115], a[168:171], v[16:19]\n"
    "s_waitcnt lgkmcnt(0)\n"
    "v_exp_f32 v58, v58\n"
    "v_mfma_f32_16x16x32_bf16 v[20:23], a[136:139], a[192:195], v[104:107]\n"
    "buffer_load_dwordx4 v1, s[16:19], s83 offen offset:1024 sc1 lds\n"
    "v_mfma_f32_16x16x32_bf16 v[20:23], a[140:143], a[196:199], v[20:23]\n"
    "s_add_u32 s67, s67, 49152\n"
    "ds_read_b128 a[112:115], v249 offset:0\n"
    "v_or3_b32 v243, v204, v205, v243\n"
    "v_mfma_f32_16x16x32_bf16 v[20:23], a[144:147], a[200:203], v[20:23]\n"
    "s_add_u32 s59, s59, 4\n"
    "ds_read_b128 a[116:119], v249 offset:12288\n"
    "v_or3_b32 v243, v206, v207, v243\n"
    "v_mfma_f32_16x16x32_bf16 v[20:23], a[148:151], a[204:207], v[20:23]\n"
    "ds_read_b128 a[120:123], v249 offset:1024\n"
    "v_cvt_pk_u16_u32 v212, v212, v216\n"
    "v_mfma_f32_16x16x32_bf16 v[20:23], a[152:155], a[208:211], v[20:23]\n"
    "ds_read_b128 a[124:127], v249 offset:13312\n"
    "v_cvt_pk_u16_u32 v213, v213, v217\n"
    "v_mfma_f32_16x16x32_bf16 v[20:23], a[156:159], a[212:215], v[20:23]\n"
    "ds_read_b128 a[128:131], v249 offset:2048\n"
    "v_mfma_f32_16x16x32_bf16 v[24:27], a[156:159], a[236:239], v[104:107]\n"
    "ds_read_b128 a[132:135], v249 offset:14336\n"
    "v_mfma_f32_16x16x32_bf16 v[24:27], a[152:155], a[232:235], v[24:27]\n"
    "v_exp_f32 v59, v59\n"
    "v_mfma_f32_16x16x32_bf16 v[24:27], a[148:151], a[228:231], v[24:27]\n"
    "v_cvt_pk_bf16_f32 v83, v58, v59\n"
    "v_cvt_pk_u16_u32 v214, v214, v218\n"
    "v_mfma_f32_16x16x32_bf16 v[24:27], a[144:147], a[224:227], v[24:27]\n"
    "v_exp_f32 v60, v60\n"
    "v_mfma_f32_16x16x32_bf16 v[24:27], a[140:143], a[220:223], v[24:27]\n"
    "v_exp_f32 v61, v61\n"
    "v_mfma_f32_16x16x32_bf16 v[24:27], a[136:139], a[216:219], v[24:27]\n"
    "v_exp_f32 v62, v62\n"
    "v_mfma_f32_16x16x32_bf16 v[28:31], a[136:139], a[240:243], v[104:107]\n"
    "v_cvt_pk_bf16_f32 v90, v60, v61\n"
    "v_mfma_f32_16x16x32_bf16 v[28:31], a[140:143], a[244:247], v[28:31]\n"
    "v_exp_f32 v63, v63\n"
    "v_mfma_f32_16x16x32_bf16 v[28:31], a[144:147], a[248:251], v[28:31]\n"
    "v_cvt_pk_bf16_f32 v91, v62, v63\n"
    "v_cvt_pk_u16_u32 v215, v215, v219\n"
    "v_mfma_f32_16x16x32_bf16 v[28:31], a[148:151], a[252:255], v[28:31]\n"
    "v_exp_f32 v50, v50\n"
    "v_mfma_f32_16x16x32_bf16 v[28:31], a[152:155], a[160:163], v[28:31]\n"
    "v_exp_f32 v51, v51\n"
    "v_mfma_f32_16x16x32_bf16 v[28:31], a[156:159], a[164:167], v[28:31]\n"
    "v_exp_f32 v64, v64\n"
    "v_mfma_f32_16x16x32_bf16 v[32:35], a[156:159], a[188:191], v[104:107]\n"
    "v_cvt_pk_bf16_f32 v97, v50, v51\n"
    "v_mfma_f32_16x16x32_bf16 v[32:35], a[152:155], a[184:187], v[32:35]\n"
    "v_exp_f32 v65, v65\n"
    "v_mfma_f32_16x16x32_bf16 v[32:35], a[148:151], a[180:183], v[32:35]\n"
    "v_cvt_pk_bf16_f32 v98, v64, v65\n"
    "v_or3_b32 v243, v212, v213, v243\n"
    "v_mfma_f32_16x16x32_bf16 v[32:35], a[144:147], a[176:179], v[32:35]\n"
    "v_mfma_f32_16x16x32_bf16 v[32:35], a[140:143], a[172:175], v[32:35]\n"
    "v_exp_f32 v66, v66\n"
    "v_mfma_f32_16x16x32_bf16 v[32:35], a[136:139], a[168:171], v[32:35]\n"
    "s_waitcnt lgkmcnt(0)\n"
    "v_mfma_f32_16x16x32_bf16 v[124:127], a[112:115], v[68:71], v[124:127]\n"
    "ds_read_b128 a[136:139], v249 offset:3072\n"
    "v_or3_b32 v243, v214, v215, v243\n"
    "v_mfma_f32_16x16x32_bf16 v[124:127], a[116:119], v[72:75], v[124:127]\n"
    "ds_read_b128 a[140:143], v249 offset:15360\n"
    "v_mfma_f32_16x16x32_bf16 v[128:131], a[124:127], v[72:75], v[128:131]\n"
    "ds_read_b128 a[144:147], v249 offset:4096\n"
    "v_mfma_f32_16x16x32_bf16 v[128:131], a[120:123], v[68:71], v[128:131]\n"
    "ds_read_b128 a[148:151], v249 offset:16384\n"
    "v_mfma_f32_16x16x32_bf16 v[132:135], a[128:131], v[68:71], v[132:135]\n"
    "ds_read_b128 a[152:155], v249 offset:5120\n"
    "v_mfma_f32_16x16x32_bf16 v[132:135], a[132:135], v[72:75], v[132:135]\n"
    "ds_read_b128 a[156:159], v249 offset:17408\n"
    "v_cvt_pk_u16_u32 v220, v220, v224\n"
    "v_mfma_f32_16x16x32_bf16 v[148:151], a[132:135], v[80:83], v[148:151]\n"
    "ds_read_b128 v[104:107], v251 offset:768\n"
    "v_mfma_f32_16x16x32_bf16 v[148:151], a[128:131], v[76:79], v[148:151]\n"
    "v_exp_f32 v67, v67\n"
    "v_mfma_f32_16x16x32_bf16 v[144:147], a[120:123], v[76:79], v[144:147]\n"
    "v_cvt_pk_bf16_f32 v99, v66, v67\n"
    "v_mfma_f32_16x16x32_bf16 v[144:147], a[124:127], v[80:83], v[144:147]\n"
    "v_exp_f32 v4, v4\n"
    "v_mfma_f32_16x16x32_bf16 v[140:143], a[116:119], v[80:83], v[140:143]\n"
    "v_exp_f32 v5, v5\n"
    "v_mfma_f32_16x16x32_bf16 v[140:143], a[112:115], v[76:79], v[140:143]\n"
    "v_exp_f32 v6, v6\n"
    "v_mfma_f32_16x16x32_bf16 v[156:159], a[112:115], v[84:87], v[156:159]\n"
    "v_exp_f32 v7, v7\n"
    "v_mfma_f32_16x16x32_bf16 v[156:159], a[116:119], v[88:91], v[156:159]\n"
    "v_cvt_pk_u16_u32 v221, v221, v225\n"
    "v_mfma_f32_16x16x32_bf16 v[160:163], a[124:127], v[88:91], v[160:163]\n"
    "v_exp_f32 v8, v8\n"
    "v_mfma_f32_16x16x32_bf16 v[160:163], a[120:123], v[84:87], v[160:163]\n"
    "v_cvt_pk_u16_u32 v222, v222, v226\n"
    "v_mfma_f32_16x16x32_bf16 v[164:167], a[128:131], v[84:87], v[164:167]\n"
    "v_mfma_f32_16x16x32_bf16 v[164:167], a[132:135], v[88:91], v[164:167]\n"
    "v_exp_f32 v9, v9\n"
    "v_mfma_f32_16x16x32_bf16 v[180:183], a[132:135], v[96:99], v[180:183]\n"
    "v_mfma_f32_16x16x32_bf16 v[180:183], a[128:131], v[92:95], v[180:183]\n"
    "v_mfma_f32_16x16x32_bf16 v[176:179], a[120:123], v[92:95], v[176:179]\n"
    "v_exp_f32 v10, v10\n"
    "v_mfma_f32_16x16x32_bf16 v[176:179], a[124:127], v[96:99], v[176:179]\n"
    "v_cvt_pk_u16_u32 v223, v223, v227\n"
    "v_mfma_f32_16x16x32_bf16 v[172:175], a[116:119], v[96:99], v[172:175]\n"
    "v_exp_f32 v11, v11\n"
    "v_mfma_f32_16x16x32_bf16 v[172:175], a[112:115], v[92:95], v[172:175]\n"
    "s_waitcnt lgkmcnt(0)\n"
    "v_mfma_f32_16x16x32_bf16 v[136:139], a[136:139], v[68:71], v[136:139]\n"
    "ds_read_b128 a[112:115], v249 offset:6144\n"
    "v_mfma_f32_16x16x32_bf16 v[136:139], a[140:143], v[72:75], v[136:139]\n"
    "ds_read_b128 a[116:119], v249 offset:18432\n"
    "v_mfma_f32_16x16x32_bf16 v[188:191], a[148:151], v[72:75], v[188:191]\n"
    "ds_read_b128 a[120:123], v249 offset:7168\n"
    "v_or3_b32 v243, v220, v221, v243\n"
    "v_mfma_f32_16x16x32_bf16 v[188:191], a[144:147], v[68:71], v[188:191]\n"
    "ds_read_b128 a[124:127], v249 offset:19456\n"
    "v_mfma_f32_16x16x32_bf16 v[192:195], a[152:155], v[68:71], v[192:195]\n"
    "ds_read_b128 a[128:131], v249 offset:8192\n"
    "v_mfma_f32_16x16x32_bf16 v[192:195], a[156:159], v[72:75], v[192:195]\n"
    "ds_read_b128 a[132:135], v249 offset:20480\n"
    "v_smfmac_f32_16x16x64_bf16 v[108:111], v[104:107], v[68:75], v253\n"
    "s_add_u32 s81, s81, 128\n"
    "s_cmp_gt_u32 s81, 4096\n"
    "s_cselect_b32 s81, 0, s81\n"
    "v_smfmac_f32_16x16x64_bf16 v[112:115], v[104:107], v[76:83], v253\n"
    "v_mfma_f32_16x16x32_bf16 a[4:7], a[156:159], v[80:83], a[4:7]\n"
    "v_exp_f32 v12, v12\n"
    "v_mfma_f32_16x16x32_bf16 a[4:7], a[152:155], v[76:79], a[4:7]\n"
    "v_exp_f32 v13, v13\n"
    "v_mfma_f32_16x16x32_bf16 a[0:3], a[144:147], v[76:79], a[0:3]\n"
    "v_exp_f32 v14, v14\n"
    "v_mfma_f32_16x16x32_bf16 a[0:3], a[148:151], v[80:83], a[0:3]\n"
    "v_exp_f32 v15, v15\n"
    "v_mfma_f32_16x16x32_bf16 v[152:155], a[140:143], v[80:83], v[152:155]\n"
    "v_or3_b32 v243, v222, v223, v243\n"
    "v_and_b32 v208, 0xff00ff00, v243\n"
    "v_mfma_f32_16x16x32_bf16 v[152:155], a[136:139], v[76:79], v[152:155]\n"
    "v_exp_f32 v16, v16\n"
    "v_mfma_f32_16x16x32_bf16 v[168:171], a[136:139], v[84:87], v[168:171]\n"
    "v_exp_f32 v17, v17\n"
    "v_mfma_f32_16x16x32_bf16 v[168:171], a[140:143], v[88:91], v[168:171]\n"
    "v_mfma_f32_16x16x32_bf16 a[16:19], a[148:151], v[88:91], a[16:19]\n"
    "v_mfma_f32_16x16x32_bf16 a[16:19], a[144:147], v[84:87], a[16:19]\n"
    "v_exp_f32 v18, v18\n"
    "v_mfma_f32_16x16x32_bf16 a[20:23], a[152:155], v[84:87], a[20:23]\n"
    "v_cmp_ne_u32 vcc, 0, v208\n"
    "v_mfma_f32_16x16x32_bf16 a[20:23], a[156:159], v[88:91], a[20:23]\n"
    "v_smfmac_f32_16x16x64_bf16 v[116:119], v[104:107], v[84:91], v253\n"
    "v_smfmac_f32_16x16x64_bf16 v[120:123], v[104:107], v[92:99], v253\n"
    "v_mfma_f32_16x16x32_bf16 a[36:39], a[156:159], v[96:99], a[36:39]\n"
    "v_exp_f32 v19, v19\n"
    "v_mfma_f32_16x16x32_bf16 a[36:39], a[152:155], v[92:95], a[36:39]\n"
    "v_exp_f32 v20, v20\n"
    "v_mfma_f32_16x16x32_bf16 a[32:35], a[144:147], v[92:95], a[32:35]\n"
    "v_cndmask_b32 v208, 2.0, v241, vcc\n"
    "v_mfma_f32_16x16x32_bf16 a[32:35], a[148:151], v[96:99], a[32:35]\n"
    "v_exp_f32 v21, v21\n"
    "v_mfma_f32_16x16x32_bf16 v[184:187], a[140:143], v[96:99], v[184:187]\n"
    "v_mfma_f32_16x16x32_bf16 v[184:187], a[136:139], v[92:95], v[184:187]\n"
    "s_waitcnt lgkmcnt(0)\n"
    "v_mfma_f32_16x16x32_bf16 v[196:199], a[112:115], v[68:71], v[196:199]\n"
    "ds_read_b128 a[136:139], v249 offset:9216\n"
    "v_mfma_f32_16x16x32_bf16 v[196:199], a[116:119], v[72:75], v[196:199]\n"
    "ds_read_b128 a[140:143], v249 offset:21504\n"
    "v_mfma_f32_16x16x32_bf16 v[200:203], a[124:127], v[72:75], v[200:203]\n"
    "ds_read_b128 a[144:147], v249 offset:10240\n"
    "v_mfma_f32_16x16x32_bf16 v[200:203], a[120:123], v[68:71], v[200:203]\n"
    "ds_read_b128 a[148:151], v249 offset:22528\n"
    "v_mfma_f32_16x16x32_bf16 a[48:51], a[128:131], v[68:71], a[48:51]\n"
    "buffer_load_dwordx2 v[236:237], v241, s[36:39], s81 offen offset:0 sc1\n"
    "v_mfma_f32_16x16x32_bf16 a[48:51], a[132:135], v[72:75], a[48:51]\n"
    "ds_read_b128 a[152:155], v249 offset:11264\n"
    "v_mfma_f32_16x16x32_bf16 a[64:67], a[132:135], v[80:83], a[64:67]\n"
    "ds_read_b128 a[156:159], v249 offset:23552\n"
    "v_mfma_f32_16x16x32_bf16 a[64:67], a[128:131], v[76:79], a[64:67]\n"
    "buffer_store_dword v204, v244, s[20:23], s76 offen offset:0 sc1\n"
    "v_mfma_f32_16x16x32_bf16 a[12:15], a[120:123], v[76:79], a[12:15]\n"
    "v_exp_f32 v22, v22\n"
    "v_mfma_f32_16x16x32_bf16 a[12:15], a[124:127], v[80:83], a[12:15]\n"
    "buffer_store_dword v205, v244, s[20:23], s76 offen offset:1024 sc1\n"
    "v_mfma_f32_16x16x32_bf16 a[8:11], a[116:119], v[80:83], a[8:11]\n"
    "v_exp_f32 v23, v23\n"
    "v_mfma_f32_16x16x32_bf16 a[8:11], a[112:115], v[76:79], a[8:11]\n"
    "buffer_store_dword v206, v244, s[20:23], s76 offen offset:2048 sc1\n"
    "v_mfma_f32_16x16x32_bf16 a[24:27], a[112:115], v[84:87], a[24:27]\n"
    "v_exp_f32 v24, v24\n"
    "v_mfma_f32_16x16x32_bf16 a[24:27], a[116:119], v[88:91], a[24:27]\n"
    "buffer_store_dword v207, v244, s[20:23], s76 offen offset:3072 sc1\n"
    "v_mfma_f32_16x16x32_bf16 a[28:31], a[124:127], v[88:91], a[28:31]\n"
    "v_exp_f32 v25, v25\n"
    "v_mfma_f32_16x16x32_bf16 a[28:31], a[120:123], v[84:87], a[28:31]\n"
    "buffer_store_dword v212, v244, s[20:23], s77 offen offset:0 sc1\n"
    "v_mfma_f32_16x16x32_bf16 a[80:83], a[128:131], v[84:87], a[80:83]\n"
    "v_exp_f32 v26, v26\n"
    "v_mfma_f32_16x16x32_bf16 a[80:83], a[132:135], v[88:91], a[80:83]\n"
    "buffer_store_dword v213, v244, s[20:23], s77 offen offset:1024 sc1\n"
    "v_mfma_f32_16x16x32_bf16 a[96:99], a[132:135], v[96:99], a[96:99]\n"
    "v_exp_f32 v27, v27\n"
    "v_mfma_f32_16x16x32_bf16 a[96:99], a[128:131], v[92:95], a[96:99]\n"
    "buffer_store_dword v214, v244, s[20:23], s77 offen offset:2048 sc1\n"
    "v_mfma_f32_16x16x32_bf16 a[44:47], a[120:123], v[92:95], a[44:47]\n"
    "v_exp_f32 v28, v28\n"
    "v_mfma_f32_16x16x32_bf16 a[44:47], a[124:127], v[96:99], a[44:47]\n"
    "buffer_store_dword v215, v244, s[20:23], s77 offen offset:3072 sc1\n"
    "v_mfma_f32_16x16x32_bf16 a[40:43], a[116:119], v[96:99], a[40:43]\n"
    "v_exp_f32 v29, v29\n"
    "v_mfma_f32_16x16x32_bf16 a[40:43], a[112:115], v[92:95], a[40:43]\n"
    "s_waitcnt lgkmcnt(0)\n"
    "v_exp_f32 v30, v30\n"
    "v_mfma_f32_16x16x32_bf16 a[52:55], a[136:139], v[68:71], a[52:55]\n"
    "ds_read_b128 a[112:115], v3 offset:12288\n"
    "v_mfma_f32_16x16x32_bf16 a[52:55], a[140:143], v[72:75], a[52:55]\n"
    "ds_read_b128 a[116:119], v3 offset:14336\n"
    "v_mfma_f32_16x16x32_bf16 a[56:59], a[148:151], v[72:75], a[56:59]\n"
    "ds_read_b128 a[120:123], v3 offset:16384\n"
    "v_mfma_f32_16x16x32_bf16 a[56:59], a[144:147], v[68:71], a[56:59]\n"
    "ds_read_b128 a[124:127], v3 offset:18432\n"
    "v_mfma_f32_16x16x32_bf16 a[60:63], a[152:155], v[68:71], a[60:63]\n"
    "ds_read_b128 a[128:131], v3 offset:20480\n"
    "v_mfma_f32_16x16x32_bf16 a[60:63], a[156:159], v[72:75], a[60:63]\n"
    "ds_read_b128 a[132:135], v3 offset:22528\n"
    "v_mfma_f32_16x16x32_bf16 a[76:79], a[156:159], v[80:83], a[76:79]\n"
    "ds_read_b128 v[100:103], v250 offset:1664\n"
    "v_mfma_f32_16x16x32_bf16 a[76:79], a[152:155], v[76:79], a[76:79]\n"
    "buffer_store_dword v220, v244, s[20:23], s78 offen offset:0 sc1\n"
    "v_mfma_f32_16x16x32_bf16 a[72:75], a[144:147], v[76:79], a[72:75]\n"
    "v_exp_f32 v31, v31\n"
    "v_mfma_f32_16x16x32_bf16 a[72:75], a[148:151], v[80:83], a[72:75]\n"
    "buffer_store_dword v221, v244, s[20:23], s78 offen offset:1024 sc1\n"
    "v_mfma_f32_16x16x32_bf16 a[68:71], a[140:143], v[80:83], a[68:71]\n"
    "v_exp_f32 v32, v32\n"
    "v_mfma_f32_16x16x32_bf16 a[68:71], a[136:139], v[76:79], a[68:71]\n"
    "buffer_store_dword v222, v244, s[20:23], s78 offen offset:2048 sc1\n"
    "v_mfma_f32_16x16x32_bf16 a[84:87], a[136:139], v[84:87], a[84:87]\n"
    "v_exp_f32 v33, v33\n"
    "v_mfma_f32_16x16x32_bf16 a[84:87], a[140:143], v[88:91], a[84:87]\n"
    "buffer_store_dword v223, v244, s[20:23], s78 offen offset:3072 sc1\n"
    "v_mfma_f32_16x16x32_bf16 a[88:91], a[148:151], v[88:91], a[88:91]\n"
    "s_waitcnt vmcnt(13)\n"
    "ds_add_u32 v247, v246\n"
    "v_mfma_f32_16x16x32_bf16 a[88:91], a[144:147], v[84:87], a[88:91]\n"
    "buffer_store_dwordx2 v[238:239], v208, s[36:39], 0 offen offset:0 sc1\n"
    "v_mfma_f32_16x16x32_bf16 a[92:95], a[152:155], v[84:87], a[92:95]\n"
    "v_exp_f32 v34, v34\n"
    "v_mfma_f32_16x16x32_bf16 a[92:95], a[156:159], v[88:91], a[92:95]\n"
    "v_exp_f32 v35, v35\n"
    "v_mfma_f32_16x16x32_bf16 a[108:111], a[156:159], v[96:99], a[108:111]\n"
    "v_mfma_f32_16x16x32_bf16 a[108:111], a[152:155], v[92:95], a[108:111]\n"
    "v_mfma_f32_16x16x32_bf16 a[104:107], a[144:147], v[92:95], a[104:107]\n"
    "s_sub_u32 s74, s68, s65\n"
    "v_add_u32 v250, 1536, v250\n"
    "v_add_u32 v251, 1536, v251\n"
    "v_mfma_f32_16x16x32_bf16 a[104:107], a[148:151], v[96:99], a[104:107]\n"
    "s_add_u32 s66, s66, 1\n"
    "v_mov_b32 v2, v3\n"
    "v_add_u32 v3, s74, v252\n"
    "v_mfma_f32_16x16x32_bf16 a[100:103], a[140:143], v[96:99], a[100:103]\n"
    "s_waitcnt lgkmcnt(1)\n"
    "s_cmp_lt_u32 s66, 29\n"
    "v_mfma_f32_16x16x32_bf16 a[100:103], a[136:139], v[92:95], a[100:103]\n"
    "s_cbranch_scc1 .LPV\n"
    "s_branch .Ltail\n"
    "s_nop 0\n"
    "s_nop 0\n"
    "s_nop 0\n"
    "s_nop 0\n"
    "s_nop 0\n"
    "s_nop 0\n"
    ".LtailP:\n"
    "s_cbranch_execz .Lw2\n"
    "v_swap_b32 v248, v249\n"
    "s_mov_b32 s64, s70\n"
    "v_add_u32 v250, 768, v250\n"
    "v_add_u32 v251, 768, v251\n"
    ".Ltail:\n"
    "v_mfma_f32_16x16x32_bf16 v[36:39], a[112:115], a[192:195], v[100:103]\n"
    "v_cvt_pk_bf16_f32 v93, v18, v19\n"
    "v_cvt_pk_bf16_f32 v84, v12, v13\n"
    "v_mfma_f32_16x16x32_bf16 v[36:39], a[116:119], a[196:199], v[36:39]\n"
    "ds_read_b128 a[136:139], v2 offset:13312\n"
    "ds_read_b128 a[140:143], v2 offset:15360\n"
    "v_mfma_f32_16x16x32_bf16 v[36:39], a[120:123], a[200:203], v[36:39]\n"
    "s_mul_i32 s75, s84, 24576\n"
    "v_mfma_f32_16x16x32_bf16 v[36:39], a[124:127], a[204:207], v[36:39]\n"
    "v_cvt_pk_bf16_f32 v77, v10, v11\n"
    "v_subrev_u32 v249, 24576, v249\n"
    "v_mfma_f32_16x16x32_bf16 v[36:39], a[128:131], a[208:211], v[36:39]\n"
    "ds_read_b128 a[152:155], v2 offset:21504\n"
    "v_mfma_f32_16x16x32_bf16 v[40:43], a[132:135], a[236:239], v[100:103]\n"
    "v_cvt_pk_bf16_f32 v69, v6, v7\n"
    "v_lshlrev_b32 v240, 16, v1\n"
    "v_mfma_f32_16x16x32_bf16 v[36:39], a[132:135], a[212:215], v[36:39]\n"
    "ds_read_b128 a[156:159], v2 offset:23552\n"
    "ds_read_b128 v[104:107], v250 offset:144\n"
    "v_mfma_f32_16x16x32_bf16 v[48:51], a[132:135], a[188:191], v[100:103]\n"
    "s_add_u32 s76, s75, 6144\n"
    "ds_read_b128 a[144:147], v2 offset:17408\n"
    "v_and_b32 v3, 0xf0, v1\n"
    "v_mfma_f32_16x16x32_bf16 v[48:51], a[128:131], a[184:187], v[48:51]\n"
    "ds_read_b128 a[148:151], v2 offset:19456\n"
    "s_add_u32 s77, s76, 6144\n"
    "v_mfma_f32_16x16x32_bf16 v[40:43], a[128:131], a[232:235], v[40:43]\n"
    "v_exp_f32 v37, v37\n"
    "v_mfma_f32_16x16x32_bf16 v[48:51], a[124:127], a[180:183], v[48:51]\n"
    "v_exp_f32 v38, v38\n"
    "v_mfma_f32_16x16x32_bf16 v[48:51], a[120:123], a[176:179], v[48:51]\n"
    "s_add_u32 s78, s77, 6144\n"
    "v_cvt_pk_bf16_f32 v76, v8, v9\n"
    "v_cvt_pk_bf16_f32 v68, v4, v5\n"
    "v_mfma_f32_16x16x32_bf16 v[40:43], a[124:127], a[228:231], v[40:43]\n"
    "v_cvt_pk_bf16_f32 v85, v14, v15\n"
    "v_cvt_pk_bf16_f32 v71, v22, v23\n"
    "v_mfma_f32_16x16x32_bf16 v[48:51], a[116:119], a[172:175], v[48:51]\n"
    "v_and_b32 v2, 0x70, v1\n"
    "v_cvt_pk_bf16_f32 v94, v32, v33\n"
    "v_mfma_f32_16x16x32_bf16 v[44:47], a[112:115], a[240:243], v[100:103]\n"
    "v_cvt_pk_bf16_f32 v95, v34, v35\n"
    "v_cvt_pk_bf16_f32 v86, v28, v29\n"
    "v_mfma_f32_16x16x32_bf16 v[48:51], a[112:115], a[168:171], v[48:51]\n"
    "v_exp_f32 v39, v39\n"
    "v_mfma_f32_16x16x32_bf16 v[44:47], a[116:119], a[244:247], v[44:47]\n"
    "s_waitcnt lgkmcnt(2)\n"
    "v_cvt_pk_bf16_f32 v70, v20, v21\n"
    "v_cvt_pk_bf16_f32 v92, v16, v17\n"
    "v_mfma_f32_16x16x32_bf16 v[60:63], a[136:139], a[240:243], v[104:107]\n"
    "v_exp_f32 v36, v36\n"
    "v_mfma_f32_16x16x32_bf16 v[56:59], a[156:159], a[236:239], v[104:107]\n"
    "s_add_u32 s72, s65, 122880\n"
    "ds_read_b32 v245, v247\n"
    "v_mfma_f32_16x16x32_bf16 v[40:43], a[120:123], a[224:227], v[40:43]\n"
    "v_exp_f32 v51, v51\n"
    "v_mfma_f32_16x16x32_bf16 v[60:63], a[140:143], a[244:247], v[60:63]\n"
    "v_exp_f32 v48, v48\n"
    "v_mfma_f32_16x16x32_bf16 v[44:47], a[120:123], a[248:251], v[44:47]\n"
    "v_exp_f32 v50, v50\n"
    "v_mfma_f32_16x16x32_bf16 v[40:43], a[116:119], a[220:223], v[40:43]\n"
    "s_waitcnt vmcnt(0)\n"
    "v_exp_f32 v49, v49\n"
    "v_mfma_f32_16x16x32_bf16 v[44:47], a[124:127], a[252:255], v[44:47]\n"
    "ds_read_b128 v[232:235], v251 offset:0\n"
    "ds_read_b128 v[100:103], v250 offset:768\n"
    "v_mfma_f32_16x16x32_bf16 v[52:55], a[136:139], a[192:195], v[104:107]\n"
    "v_cvt_pk_bf16_f32 v79, v26, v27\n"
    "v_lshrrev_b32 v224, 4, v1\n"
    "v_mfma_f32_16x16x32_bf16 v[64:67], a[156:159], a[188:191], v[104:107]\n"
    "v_lshl_add_u32 v3, v3, 3, s72\n"
    "v_cmp_eq_u64 s[54:55], v[236:237], v[238:239]\n"
    "v_mfma_f32_16x16x32_bf16 v[52:55], a[140:143], a[196:199], v[52:55]\n"
    "s_waitcnt lgkmcnt(2)\n"
    "v_cvt_pk_bf16_f32 v78, v24, v25\n"
    "v_cmp_le_u32 s[56:57], s59, v245\n"
    "v_mfma_f32_16x16x32_bf16 v[40:43], a[112:115], a[216:219], v[40:43]\n"
    "s_and_b64 s[54:55], s[54:55], s[56:57]\n"
    "v_and_b32 v224, 48, v224\n"
    "s_cmp_eq_u64 s[54:55], -1\n"
    "v_mfma_f32_16x16x32_bf16 v[60:63], a[144:147], a[248:251], v[60:63]\n"
    "s_cbranch_scc0 .Lpt0\n"
    ".Lpt0r:\n"
    "v_mfma_f32_16x16x32_bf16 v[44:47], a[128:131], a[160:163], v[44:47]\n"
    "s_add_u32 s81, s81, 128\n"
    "ds_read_b128 a[116:119], v252 offset:2048\n"
    "s_add_u32 m0, s65, 24576\n"
    "v_mfma_f32_16x16x32_bf16 v[52:55], a[144:147], a[200:203], v[52:55]\n"
    "s_add_u32 s83, s67, .Lend-.Lend2+(0)\n"
    "buffer_load_dwordx4 v1, s[16:19], s83 offen offset:0 sc1 lds\n"
    "v_mfma_f32_16x16x32_bf16 v[60:63], a[148:151], a[252:255], v[60:63]\n"
    "ds_read_b128 a[120:123], v252 offset:4096\n"
    "ds_read_b128 a[128:131], v252 offset:8192\n"
    "v_mfma_f32_16x16x32_bf16 v[56:59], a[152:155], a[232:235], v[56:59]\n"
    "buffer_load_dwordx4 v1, s[16:19], s83 offen offset:1024 sc1 lds\n"
    "v_mfma_f32_16x16x32_bf16 v[60:63], a[152:155], a[160:163], v[60:63]\n"
    "ds_read_b128 v[104:107], v250 offset:784\n"
    "ds_read_b128 a[112:115], v252 offset:0\n"
    "v_mfma_f32_16x16x32_bf16 v[44:47], a[132:135], a[164:167], v[44:47]\n"
    "buffer_load_dwordx4 v1, s[16:19], s83 offen offset:2048 sc1 lds\n"
    "v_mfma_f32_16x16x32_bf16 v[52:55], a[148:151], a[204:207], v[52:55]\n"
    "ds_read_b128 a[132:135], v252 offset:10240\n"
    "v_mfma_f32_16x16x32_bf16 v[52:55], a[152:155], a[208:211], v[52:55]\n"
    "buffer_load_dwordx4 v1, s[16:19], s83 offen offset:3072 sc1 lds\n"
    "v_mfma_f32_16x16x32_bf16 v[60:63], a[156:159], a[164:167], v[60:63]\n"
    "s_add_u32 m0, s65, 28672\n"
    "v_exp_f32_e64 v40, v40\n"
    "v_mfma_f32_16x16x32_bf16 v[56:59], a[148:151], a[228:231], v[56:59]\n"
    "s_add_u32 s83, s67, 4096\n"
    "buffer_load_dwordx4 v1, s[16:19], s83 offen offset:0 sc1 lds\n"
    "v_mfma_f32_16x16x32_bf16 v[56:59], a[144:147], a[224:227], v[56:59]\n"
    "ds_read_b128 a[124:127], v252 offset:6144\n"
    "v_cvt_pk_bf16_f32 v72, v36, v37\n"
    "v_mfma_f32_16x16x32_bf16 v[56:59], a[140:143], a[220:223], v[56:59]\n"
    "buffer_load_dwordx4 v1, s[16:19], s83 offen offset:1024 sc1 lds\n"
    "v_mfma_f32_16x16x32_bf16 v[52:55], a[156:159], a[212:215], v[52:55]\n"
    "v_exp_f32 v45, v45\n"
    "v_mfma_f32_16x16x32_bf16 v[56:59], a[136:139], a[216:219], v[56:59]\n"
    "v_exp_f32 v62, v62\n"
    "v_mfma_f32_16x16x32_bf16 v[64:67], a[152:155], a[184:187], v[64:67]\n"
    "s_waitcnt lgkmcnt(2)\n"
    "v_xor_b32 v224, v224, v2\n"
    "v_cvt_pk_bf16_f32 v87, v30, v31\n"
    "v_mfma_f32_16x16x32_bf16 v[12:15], a[112:115], a[240:243], v[100:103]\n"
    "s_waitcnt lgkmcnt(1)\n"
    "ds_read_b128 a[156:159], v252 offset:11264\n"
    "ds_read_b128 a[152:155], v252 offset:9216\n"
    "v_mfma_f32_16x16x32_bf16 v[16:19], a[132:135], a[188:191], v[100:103]\n"
    ".Lwp0:\n"
    "s_cbranch_execz .Lw3\n"
    "s_add_u32 m0, s65, 73728\n"
    "v_exp_f32 v56, v56\n"
    "v_mfma_f32_16x16x32_bf16 v[64:67], a[148:151], a[180:183], v[64:67]\n"
    "v_exp_f32 v58, v58\n"
    "v_mfma_f32_16x16x32_bf16 v[4:7], a[112:115], a[192:195], v[100:103]\n"
    "s_add_u32 s83, s67, 24576\n"
    "v_exp_f32 v61, v61\n"
    "v_mfma_f32_16x16x32_bf16 v[16:19], a[128:131], a[184:187], v[16:19]\n"
    "v_exp_f32 v53, v53\n"
    "v_mfma_f32_16x16x32_bf16 v[64:67], a[144:147], a[176:179], v[64:67]\n"
    "v_exp_f32 v54, v54\n"
    "v_mfma_f32_16x16x32_bf16 v[64:67], a[140:143], a[172:175], v[64:67]\n"
    "v_exp_f32 v52, v52\n"
    "v_mfma_f32_16x16x32_bf16 v[8:11], a[132:135], a[236:239], v[100:103]\n"
    "s_waitcnt lgkmcnt(2)\n"
    "v_exp_f32 v63, v63\n"
    "v_mfma_f32_16x16x32_bf16 v[16:19], a[124:127], a[180:183], v[16:19]\n"
    "v_exp_f32 v41, v41\n"
    "v_mfma_f32_16x16x32_bf16 v[64:67], a[136:139], a[168:171], v[64:67]\n"
    "v_exp_f32 v43, v43\n"
    "v_mfma_f32_16x16x32_bf16 v[8:11], a[128:131], a[232:235], v[8:11]\n"
    "ds_read_b128 a[144:147], v252 offset:5120\n"
    "ds_read_b128 a[148:151], v252 offset:7168\n"
    "v_mfma_f32_16x16x32_bf16 v[12:15], a[116:119], a[244:247], v[12:15]\n"
    "v_exp_f32 v44, v44\n"
    "v_mfma_f32_16x16x32_bf16 v[4:7], a[116:119], a[196:199], v[4:7]\n"
    "v_exp_f32 v55, v55\n"
    "v_mfma_f32_16x16x32_bf16 v[16:19], a[120:123], a[176:179], v[16:19]\n"
    "ds_read_b128 a[136:139], v252 offset:1024\n"
    "v_cvt_pk_bf16_f32 v80, v40, v41\n"
    "v_mfma_f32_16x16x32_bf16 v[12:15], a[120:123], a[248:251], v[12:15]\n"
    "buffer_load_dwordx4 v1, s[16:19], s83 offen offset:0 sc1 lds\n"
    "v_mfma_f32_16x16x32_bf16 v[4:7], a[120:123], a[200:203], v[4:7]\n"
    "v_exp_f32 v42, v42\n"
    "v_mfma_f32_16x16x32_bf16 v[16:19], a[116:119], a[172:175], v[16:19]\n"
    "v_exp_f32 v66, v66\n"
    "v_mfma_f32_16x16x32_bf16 v[4:7], a[124:127], a[204:207], v[4:7]\n"
    "v_exp_f32 v59, v59\n"
    "v_mfma_f32_16x16x32_bf16 v[4:7], a[128:131], a[208:211], v[4:7]\n"
    "buffer_load_dwordx4 v1, s[16:19], s83 offen offset:1024 sc1 lds\n"
    "v_mfma_f32_16x16x32_bf16 v[4:7], a[132:135], a[212:215], v[4:7]\n"
    "v_exp_f32 v57, v57\n"
    "v_mfma_f32_16x16x32_bf16 v[12:15], a[124:127], a[252:255], v[12:15]\n"
    "ds_read_b128 v[100:103], v250 offset:896\n"
    "ds_read_b128 a[140:143], v252 offset:3072\n"
    "v_mfma_f32_16x16x32_bf16 v[12:15], a[128:131], a[160:163], v[12:15]\n"
    "v_exp_f32 v65, v65\n"
    "v_mfma_f32_16x16x32_bf16 v[8:11], a[124:127], a[228:231], v[8:11]\n"
    "v_exp_f32 v47, v47\n"
    "v_mfma_f32_16x16x32_bf16 v[12:15], a[132:135], a[164:167], v[12:15]\n"
    "s_waitcnt lgkmcnt(6)\n"
    "buffer_load_dwordx4 v1, s[16:19], s83 offen offset:2048 sc1 lds\n"
    "v_mfma_f32_16x16x32_bf16 v[32:35], a[156:159], a[188:191], v[104:107]\n"
    "v_exp_f32 v60, v60\n"
    "v_mfma_f32_16x16x32_bf16 v[24:27], a[156:159], a[236:239], v[104:107]\n"
    "s_waitcnt lgkmcnt(2)\n"
    "v_cvt_pk_bf16_f32 v81, v42, v43\n"
    "v_mfma_f32_16x16x32_bf16 v[28:31], a[136:139], a[240:243], v[104:107]\n"
    "v_cvt_pk_bf16_f32 v96, v48, v49\n"
    "v_cvt_pk_bf16_f32 v90, v60, v61\n"
    "v_mfma_f32_16x16x32_bf16 v[8:11], a[120:123], a[224:227], v[8:11]\n"
    "v_exp_f32 v7, v7\n"
    "v_mfma_f32_16x16x32_bf16 v[24:27], a[152:155], a[232:235], v[24:27]\n"
    "v_exp_f32 v67, v67\n"
    "v_mfma_f32_16x16x32_bf16 v[8:11], a[116:119], a[220:223], v[8:11]\n"
    "ds_read_b128 a[124:127], v252 offset:18432\n"
    "v_mfma_f32_16x16x32_bf16 v[16:19], a[112:115], a[168:171], v[16:19]\n"
    "buffer_load_dwordx4 v1, s[16:19], s83 offen offset:3072 sc1 lds\n"
    "v_mfma_f32_16x16x32_bf16 v[32:35], a[152:155], a[184:187], v[32:35]\n"
    "s_waitcnt lgkmcnt(1)\n"
    "ds_read_b128 a[132:135], v252 offset:22528\n"
    "v_cvt_pk_bf16_f32 v75, v54, v55\n"
    "v_mfma_f32_16x16x32_bf16 v[24:27], a[148:151], a[228:231], v[24:27]\n"
    "s_add_u32 m0, s65, 77824\n"
    "v_cvt_pk_bf16_f32 v74, v52, v53\n"
    "v_cvt_pk_bf16_f32 v97, v50, v51\n"
    "v_mfma_f32_16x16x32_bf16 v[20:23], a[136:139], a[192:195], v[104:107]\n"
    "s_add_u32 s83, s67, 28672\n"
    "v_exp_f32 v6, v6\n"
    "v_mfma_f32_16x16x32_bf16 v[20:23], a[140:143], a[196:199], v[20:23]\n"
    "v_cvt_pk_bf16_f32 v99, v66, v67\n"
    "v_exp_f32 v14, v14\n"
    "v_mfma_f32_16x16x32_bf16 v[24:27], a[144:147], a[224:227], v[24:27]\n"
    "ds_read_b128 a[128:131], v252 offset:20480\n"
    "ds_read_b128 v[104:107], v250 offset:912\n"
    "v_mfma_f32_16x16x32_bf16 v[8:11], a[112:115], a[216:219], v[8:11]\n"
    "v_exp_f32 v46, v46\n"
    "v_mfma_f32_16x16x32_bf16 v[28:31], a[140:143], a[244:247], v[28:31]\n"
    "v_exp_f32 v13, v13\n"
    "v_mfma_f32_16x16x32_bf16 v[32:35], a[148:151], a[180:183], v[32:35]\n"
    "s_waitcnt lgkmcnt(2)\n"
    "v_exp_f32 v64, v64\n"
    "v_mfma_f32_16x16x32_bf16 v[40:43], a[132:135], a[236:239], v[100:103]\n"
    "ds_read_b128 a[120:123], v252 offset:16384\n"
    "v_cvt_pk_bf16_f32 v91, v62, v63\n"
    "v_mfma_f32_16x16x32_bf16 v[32:35], a[144:147], a[176:179], v[32:35]\n"
    "ds_read_b128 a[112:115], v252 offset:12288\n"
    "v_cvt_pk_bf16_f32 v73, v38, v39\n"
    "v_mfma_f32_16x16x32_bf16 v[28:31], a[144:147], a[248:251], v[28:31]\n"
    "v_exp_f32 v11, v11\n"
    "v_mfma_f32_16x16x32_bf16 v[32:35], a[140:143], a[172:175], v[32:35]\n"
    "v_cvt_pk_bf16_f32 v88, v44, v45\n"
    "v_cvt_pk_bf16_f32 v89, v46, v47\n"
    "v_mfma_f32_16x16x32_bf16 v[32:35], a[136:139], a[168:171], v[32:35]\n"
    "buffer_load_dwordx4 v1, s[16:19], s83 offen offset:0 sc1 lds\n"
    "v_mfma_f32_16x16x32_bf16 v[24:27], a[140:143], a[220:223], v[24:27]\n"
    "v_exp_f32 v9, v9\n"
    "v_mfma_f32_16x16x32_bf16 v[20:23], a[144:147], a[200:203], v[20:23]\n"
    "buffer_load_dwordx4 v1, s[16:19], s83 offen offset:1024 sc1 lds\n"
    "v_mfma_f32_16x16x32_bf16 v[20:23], a[148:151], a[204:207], v[20:23]\n"
    "v_exp_f32 v15, v15\n"
    "v_mfma_f32_16x16x32_bf16 v[20:23], a[152:155], a[208:211], v[20:23]\n"
    "v_exp_f32 v34, v34\n"
    "v_mfma_f32_16x16x32_bf16 v[20:23], a[156:159], a[212:215], v[20:23]\n"
    "v_exp_f32 v10, v10\n"
    "v_mfma_f32_16x16x32_bf16 v[28:31], a[148:151], a[252:255], v[28:31]\n"
    "s_add_u32 s59, s59, 4\n"
    "ds_read_b128 a[140:143], v252 offset:15360\n"
    "ds_read_b128 a[116:119], v252 offset:14336\n"
    "v_mfma_f32_16x16x32_bf16 v[28:31], a[152:155], a[160:163], v[28:31]\n"
    "s_waitcnt lgkmcnt(5)\n"
    "v_exp_f32 v4, v4\n"
    "v_mfma_f32_16x16x32_bf16 v[40:43], a[128:131], a[232:235], v[40:43]\n"
    "v_exp_f32 v12, v12\n"
    "v_mfma_f32_16x16x32_bf16 v[24:27], a[136:139], a[216:219], v[24:27]\n"
    "ds_read_b128 a[136:139], v252 offset:13312\n"
    "v_add_u32 v3, v3, v224\n"
    "v_mfma_f32_16x16x32_bf16 v[48:51], a[132:135], a[188:191], v[100:103]\n"
    "v_exp_f32 v23, v23\n"
    "v_smfmac_f32_16x16x64_bf16 v[116:119], v[232:235], v[84:91], v253\n"
    "ds_read_b128 a[152:155], v252 offset:21504\n"
    "v_mfma_f32_16x16x32_bf16 v[28:31], a[156:159], a[164:167], v[28:31]\n"
    "s_waitcnt lgkmcnt(5)\n"
    "v_exp_f32 v5, v5\n"
    "v_mfma_f32_16x16x32_bf16 v[40:43], a[124:127], a[228:231], v[40:43]\n"
    "s_mov_b32 s74, 0x47000000\n"
    "v_exp_f32 v20, v20\n"
    "v_mfma_f32_16x16x32_bf16 v[40:43], a[120:123], a[224:227], v[40:43]\n"
    "s_waitcnt lgkmcnt(4)\n"
    "ds_read_b128 a[156:159], v252 offset:23552\n"
    "v_cvt_pk_bf16_f32 v204, v4, v5\n"
    "v_mfma_f32_16x16x32_bf16 v[36:39], a[112:115], a[192:195], v[100:103]\n"
    "s_waitcnt lgkmcnt(3)\n"
    "v_exp_f32 v32, v32\n"
    "v_mfma_f32_16x16x32_bf16 v[36:39], a[116:119], a[196:199], v[36:39]\n"
    "v_cvt_pk_bf16_f32 v220, v12, v13\n"
    "v_cvt_pk_bf16_f32 v82, v56, v57\n"
    "v_mfma_f32_16x16x32_bf16 v[40:43], a[116:119], a[220:223], v[40:43]\n"
    "v_exp_f32 v16, v16\n"
    "v_mfma_f32_16x16x32_bf16 v[40:43], a[112:115], a[216:219], v[40:43]\n"
    "s_waitcnt lgkmcnt(2)\n"
    "v_exp_f32 v28, v28\n"
    "v_mfma_f32_16x16x32_bf16 v[52:55], a[136:139], a[192:195], v[104:107]\n"
    "v_exp_f32 v33, v33\n"
    "v_mfma_f32_16x16x32_bf16 v[48:51], a[128:131], a[184:187], v[48:51]\n"
    "v_exp_f32 v8, v8\n"
    "v_mfma_f32_16x16x32_bf16 v[44:47], a[112:115], a[240:243], v[100:103]\n"
    "v_exp_f32 v17, v17\n"
    "v_mfma_f32_16x16x32_bf16 v[60:63], a[136:139], a[240:243], v[104:107]\n"
    "v_cvt_pk_bf16_f32 v98, v64, v65\n"
    "v_cvt_pk_bf16_f32 v83, v58, v59\n"
    "v_mfma_f32_16x16x32_bf16 v[48:51], a[124:127], a[180:183], v[48:51]\n"
    "v_exp_f32 v35, v35\n"
    "v_mfma_f32_16x16x32_bf16 v[44:47], a[116:119], a[244:247], v[44:47]\n"
    "v_exp_f32 v41, v41\n"
    "v_mfma_f32_16x16x32_bf16 v[44:47], a[120:123], a[248:251], v[44:47]\n"
    "v_exp_f32 v29, v29\n"
    "v_mfma_f32_16x16x32_bf16 v[60:63], a[140:143], a[244:247], v[60:63]\n"
    "v_exp_f32 v31, v31\n"
    "v_mfma_f32_16x16x32_bf16 v[36:39], a[120:123], a[200:203], v[36:39]\n"
    "v_lshrrev_b32 v224, 3, v1\n"
    "v_cvt_pk_bf16_f32 v205, v6, v7\n"
    "v_mfma_f32_16x16x32_bf16 v[48:51], a[120:123], a[176:179], v[48:51]\n"
    "s_waitcnt vmcnt(6)\n"
    "ds_add_u32 v247, v246\n"
    "v_mfma_f32_16x16x32_bf16 v[48:51], a[116:119], a[172:175], v[48:51]\n"
    "s_waitcnt lgkmcnt(1)\n"
    "ds_read_b128 a[144:147], v252 offset:17408\n"
    "ds_read_b128 a[148:151], v252 offset:19456\n"
    "v_mfma_f32_16x16x32_bf16 v[56:59], a[156:159], a[236:239], v[104:107]\n"
    "v_exp_f32 v25, v25\n"
    "v_cvt_pk_bf16_f32 v228, v16, v17\n"
    "v_mfma_f32_16x16x32_bf16 v[52:55], a[140:143], a[196:199], v[52:55]\n"
    "v_exp_f32 v18, v18\n"
    "v_mfma_f32_16x16x32_bf16 v[44:47], a[124:127], a[252:255], v[44:47]\n"
    "v_exp_f32 v19, v19\n"
    "v_mfma_f32_16x16x32_bf16 v[64:67], a[156:159], a[188:191], v[104:107]\n"
    "v_exp_f32 v43, v43\n"
    "v_mfma_f32_16x16x32_bf16 v[48:51], a[112:115], a[168:171], v[48:51]\n"
    "v_exp_f32 v27, v27\n"
    "v_mfma_f32_16x16x32_bf16 v[36:39], a[124:127], a[204:207], v[36:39]\n"
    "v_exp_f32 v30, v30\n"
    "v_mfma_f32_16x16x32_bf16 v[64:67], a[152:155], a[184:187], v[64:67]\n"
    "v_exp_f32 v22, v22\n"
    "v_mfma_f32_16x16x32_bf16 v[36:39], a[128:131], a[208:211], v[36:39]\n"
    "v_exp_f32 v42, v42\n"
    "v_mfma_f32_16x16x32_bf16 v[44:47], a[128:131], a[160:163], v[44:47]\n"
    "s_waitcnt lgkmcnt(1)\n"
    "v_exp_f32 v26, v26\n"
    "v_mfma_f32_16x16x32_bf16 v[60:63], a[144:147], a[248:251], v[60:63]\n"
    "s_waitcnt lgkmcnt(0)\n"
    "v_exp_f32 v40, v40\n"
    "v_mfma_f32_16x16x32_bf16 v[64:67], a[148:151], a[180:183], v[64:67]\n"
    "ds_read_b128 v[100:103], v250 offset:1536\n"
    "ds_read_b32 v245, v247\n"
    "v_mfma_f32_16x16x32_bf16 v[56:59], a[152:155], a[232:235], v[56:59]\n"
    "v_cvt_pk_bf16_f32 v216, v40, v41\n"
    "v_cvt_pk_bf16_f32 v231, v34, v35\n"
    "v_mfma_f32_16x16x32_bf16 v[64:67], a[144:147], a[176:179], v[64:67]\n"
    "v_exp_f32 v21, v21\n"
    "v_smfmac_f32_16x16x64_bf16 v[120:123], v[232:235], v[92:99], v253\n"
    "v_cvt_pk_bf16_f32 v221, v14, v15\n"
    "v_mfma_f32_16x16x32_bf16 v[44:47], a[132:135], a[164:167], v[44:47]\n"
    "v_cvt_pk_bf16_f32 v207, v22, v23\n"
    "v_cvt_pk_bf16_f32 v229, v18, v19\n"
    "v_mfma_f32_16x16x32_bf16 v[64:67], a[140:143], a[172:175], v[64:67]\n"
    "v_cvt_pk_bf16_f32 v206, v20, v21\n"
    "v_cvt_pk_bf16_f32 v212, v8, v9\n"
    "v_mfma_f32_16x16x32_bf16 v[52:55], a[144:147], a[200:203], v[52:55]\n"
    "v_exp_f32 v50, v50\n"
    "v_mfma_f32_16x16x32_bf16 v[60:63], a[148:151], a[252:255], v[60:63]\n"
    "v_exp_f32 v24, v24\n"
    "v_mfma_f32_16x16x32_bf16 v[36:39], a[132:135], a[212:215], v[36:39]\n"
    "s_waitcnt lgkmcnt(0)\n"
    "v_cmp_le_u32 s[56:57], s59, v245\n"
    "v_cvt_pk_bf16_f32 v213, v10, v11\n"
    "v_smfmac_f32_16x16x64_bf16 v[108:111], v[232:235], v[68:75], v253\n"
    "s_cmp_eq_u64 s[56:57], -1\n"
    "v_cvt_pk_bf16_f32 v214, v24, v25\n"
    "v_smfmac_f32_16x16x64_bf16 v[112:115], v[232:235], v[76:83], v253\n"
    "s_cbranch_scc0 .Lpt1\n"
    ".Lpt1r:\n"
    "v_mfma_f32_16x16x32_bf16 v[52:55], a[148:151], a[204:207], v[52:55]\n"
    "s_add_u32 m0, s65, 49152\n"
    "ds_read_b128 a[128:131], v252 offset:32768\n"
    "ds_read_b128 a[124:127], v252 offset:30720\n"
    "v_mfma_f32_16x16x32_bf16 v[60:63], a[152:155], a[160:163], v[60:63]\n"
    "ds_read_b128 a[112:115], v252 offset:24576\n"
    "v_cvt_pk_bf16_f32 v222, v28, v29\n"
    "v_mfma_f32_16x16x32_bf16 v[60:63], a[156:159], a[164:167], v[60:63]\n"
    "s_add_u32 s83, s67, 73728\n"
    "ds_read_b128 a[132:135], v252 offset:34816\n"
    "v_cvt_pk_bf16_f32 v215, v26, v27\n"
    "v_mfma_f32_16x16x32_bf16 v[56:59], a[148:151], a[228:231], v[56:59]\n"
    "buffer_load_dwordx4 v1, s[16:19], s83 offen offset:0 sc1 lds\n"
    "v_mfma_f32_16x16x32_bf16 v[56:59], a[144:147], a[224:227], v[56:59]\n"
    "ds_read_b128 a[116:119], v252 offset:26624\n"
    "v_cvt_pk_bf16_f32 v223, v30, v31\n"
    "v_mfma_f32_16x16x32_bf16 v[56:59], a[140:143], a[220:223], v[56:59]\n"
    "v_exp_f32 v38, v38\n"
    "v_exp_f32 v47, v47\n"
    "v_mfma_f32_16x16x32_bf16 v[52:55], a[152:155], a[208:211], v[52:55]\n"
    "ds_read_b128 a[120:123], v252 offset:28672\n"
    "ds_read_b128 v[104:107], v250 offset:1552\n"
    "v_mfma_f32_16x16x32_bf16 v[64:67], a[136:139], a[168:171], v[64:67]\n"
    "buffer_load_dwordx4 v1, s[16:19], s83 offen offset:1024 sc1 lds\n"
    "v_mfma_f32_16x16x32_bf16 v[56:59], a[136:139], a[216:219], v[56:59]\n"
    "v_exp_f32 v37, v37\n"
    "v_mfma_f32_16x16x32_bf16 v[52:55], a[156:159], a[212:215], v[52:55]\n"
    "s_waitcnt lgkmcnt(4)\n"
    "buffer_load_dwordx4 v1, s[16:19], s83 offen offset:2048 sc1 lds\n"
    "v_mfma_f32_16x16x32_bf16 v[4:7], a[112:115], a[192:195], v[100:103]\n"
    "ds_read_b128 a[140:143], v252 offset:27648\n"
    "v_cvt_pk_bf16_f32 v230, v32, v33\n"
    "v_mfma_f32_16x16x32_bf16 v[20:23], a[112:115], a[240:243], v[100:103]\n"
    "s_waitcnt lgkmcnt(3)\n"
    "buffer_load_dwordx4 v1, s[16:19], s83 offen offset:3072 sc1 lds\n"
    "v_mfma_f32_16x16x32_bf16 v[20:23], a[116:119], a[244:247], v[20:23]\n"
    "s_add_u32 m0, s65, 53248\n"
    "v_exp_f32 v58, v58\n"
    "v_mfma_f32_16x16x32_bf16 v[28:31], a[132:135], a[188:191], v[100:103]\n"
    "s_add_u32 s83, s67, 77824\n"
    "v_exp_f32 v44, v44\n"
    "v_mfma_f32_16x16x32_bf16 v[28:31], a[128:131], a[184:187], v[28:31]\n"
    "ds_read_b128 a[148:151], v252 offset:31744\n"
    "ds_read_b128 a[152:155], v252 offset:33792\n"
    "v_mfma_f32_16x16x32_bf16 v[12:15], a[132:135], a[236:239], v[100:103]\n"
    "ds_read_b128 a[156:159], v252 offset:35840\n"
    "v_and_b32 v224, 0x70, v224\n"
    "v_mfma_f32_16x16x32_bf16 v[12:15], a[128:131], a[232:235], v[12:15]\n"
    "buffer_load_dwordx4 v1, s[16:19], s83 offen offset:0 sc1 lds\n"
    "v_mfma_f32_16x16x32_bf16 v[28:31], a[124:127], a[180:183], v[28:31]\n"
    "v_exp_f32 v55, v55\n"
    "v_mfma_f32_16x16x32_bf16 v[4:7], a[116:119], a[196:199], v[4:7]\n"
    "s_waitcnt lgkmcnt(5)\n"
    "buffer_load_dwordx4 v1, s[16:19], s83 offen offset:1024 sc1 lds\n"
    "v_mfma_f32_16x16x32_bf16 v[28:31], a[120:123], a[176:179], v[28:31]\n"
    "v_exp_f32 v65, v65\n"
    "v_mfma_f32_16x16x32_bf16 v[28:31], a[116:119], a[172:175], v[28:31]\n"
    "v_exp_f32 v61, v61\n"
    "v_mfma_f32_16x16x32_bf16 v[12:15], a[124:127], a[228:231], v[12:15]\n"
    "v_exp_f32 v53, v53\n"
    "v_mfma_f32_16x16x32_bf16 v[4:7], a[120:123], a[200:203], v[4:7]\n"
    "buffer_load_dwordx2 v[236:237], v241, s[36:39], 0 offen offset:0 sc1\n"
    "v_mfma_f32_16x16x32_bf16 v[20:23], a[120:123], a[248:251], v[20:23]\n"
    "s_waitcnt lgkmcnt(0)\n"
    "v_exp_f32 v67, v67\n"
    "v_mfma_f32_16x16x32_bf16 v[16:19], a[156:159], a[236:239], v[104:107]\n"
    "ds_read_b128 a[136:139], v252 offset:25600\n"
    "ds_read_b128 v[100:103], v250 offset:1664\n"
    "v_mfma_f32_16x16x32_bf16 v[4:7], a[124:127], a[204:207], v[4:7]\n"
    "v_exp_f32 v36, v36\n"
    "v_mfma_f32_16x16x32_bf16 v[28:31], a[112:115], a[168:171], v[28:31]\n"
    "s_waitcnt vmcnt(7)\n"
    "buffer_store_dwordx2 v[238:239], v240, s[36:39], s82 offen offset:0 sc1\n"
    "v_exp_f32 v46, v46\n"
    "v_mfma_f32_16x16x32_bf16 v[32:35], a[156:159], a[188:191], v[104:107]\n"
    "v_exp_f32 v60, v60\n"
    "v_mfma_f32_16x16x32_bf16 v[12:15], a[120:123], a[224:227], v[12:15]\n"
    "v_exp_f32 v64, v64\n"
    "v_mfma_f32_16x16x32_bf16 v[4:7], a[128:131], a[208:211], v[4:7]\n"
    "v_exp_f32 v66, v66\n"
    "v_mfma_f32_16x16x32_bf16 v[12:15], a[116:119], a[220:223], v[12:15]\n"
    "v_exp_f32 v49, v49\n"
    "v_mfma_f32_16x16x32_bf16 v[20:23], a[124:127], a[252:255], v[20:23]\n"
    "ds_read_b128 a[116:119], v252 offset:38912\n"
    "v_cvt_pk_bf16_f32 v235, v66, v67\n"
    "v_mfma_f32_16x16x32_bf16 v[32:35], a[152:155], a[184:187], v[32:35]\n"
    "ds_read_b128 a[120:123], v252 offset:40960\n"
    "v_cvt_pk_bf16_f32 v208, v36, v37\n"
    "v_mfma_f32_16x16x32_bf16 v[12:15], a[112:115], a[216:219], v[12:15]\n"
    "s_mov_b32 s73, 0x7f800000\n"
    "ds_read_b128 a[144:147], v252 offset:29696\n"
    "ds_read_b128 a[112:115], v252 offset:36864\n"
    "v_mfma_f32_16x16x32_bf16 v[4:7], a[132:135], a[212:215], v[4:7]\n"
    "s_waitcnt lgkmcnt(5)\n"
    "ds_add_u32 v247, v246\n"
    "v_mfma_f32_16x16x32_bf16 v[24:27], a[136:139], a[240:243], v[104:107]\n"
    "v_exp_f32 v39, v39\n"
    "v_mfma_f32_16x16x32_bf16 v[16:19], a[152:155], a[232:235], v[16:19]\n"
    "v_cvt_pk_bf16_f32 v226, v60, v61\n"
    "v_cvt_pk_bf16_f32 v234, v64, v65\n"
    "v_mfma_f32_16x16x32_bf16 v[8:11], a[136:139], a[192:195], v[104:107]\n"
    "v_exp_f32 v48, v48\n"
    "v_mfma_f32_16x16x32_bf16 v[24:27], a[140:143], a[244:247], v[24:27]\n"
    "v_exp_f32 v56, v56\n"
    "v_mfma_f32_16x16x32_bf16 v[20:23], a[128:131], a[160:163], v[20:23]\n"
    "ds_read_b128 v[240:243], v251 offset:768\n"
    "v_cvt_pk_bf16_f32 v209, v38, v39\n"
    "v_mfma_f32_16x16x32_bf16 v[16:19], a[148:151], a[228:231], v[16:19]\n"
    "v_exp_f32 v62, v62\n"
    "v_mfma_f32_16x16x32_bf16 v[20:23], a[132:135], a[164:167], v[20:23]\n"
    "v_exp_f32 v63, v63\n"
    "v_mfma_f32_16x16x32_bf16 v[8:11], a[140:143], a[196:199], v[8:11]\n"
    "s_waitcnt lgkmcnt(3)\n"
    "ds_read_b128 a[128:131], v252 offset:45056\n"
    "ds_read_b128 a[132:135], v252 offset:47104\n"
    "v_mfma_f32_16x16x32_bf16 v[24:27], a[144:147], a[248:251], v[24:27]\n"
    "v_exp_f32 v12, v12\n"
    "v_mfma_f32_16x16x32_bf16 v[8:11], a[144:147], a[200:203], v[8:11]\n"
    "v_exp_f32 v13, v13\n"
    "v_mfma_f32_16x16x32_bf16 v[24:27], a[148:151], a[252:255], v[24:27]\n"
    "v_exp_f32 v4, v4\n"
    "v_mfma_f32_16x16x32_bf16 v[24:27], a[152:155], a[160:163], v[24:27]\n"
    "v_exp_f32 v59, v59\n"
    "v_mfma_f32_16x16x32_bf16 v[16:19], a[144:147], a[224:227], v[16:19]\n"
    "s_mov_b32 s79, 133\n"
    "v_exp_f32 v5, v5\n"
    "v_mfma_f32_16x16x32_bf16 v[16:19], a[140:143], a[220:223], v[16:19]\n"
    "v_exp_f32 v54, v54\n"
    "v_mfma_f32_16x16x32_bf16 v[8:11], a[148:151], a[204:207], v[8:11]\n"
    "v_exp_f32 v45, v45\n"
    "v_mfma_f32_16x16x32_bf16 v[16:19], a[136:139], a[216:219], v[16:19]\n"
    "ds_read_b128 a[124:127], v252 offset:43008\n"
    "ds_read_b32 v245, v247\n"
    "v_mfma_f32_16x16x32_bf16 v[32:35], a[148:151], a[180:183], v[32:35]\n"
    "v_exp_f32 v6, v6\n"
    "v_mfma_f32_16x16x32_bf16 v[8:11], a[152:155], a[208:211], v[8:11]\n"
    "ds_read_b128 a[152:155], v252 offset:46080\n"
    "v_cvt_pk_bf16_f32 v217, v42, v43\n"
    "v_mfma_f32_16x16x32_bf16 v[8:11], a[156:159], a[212:215], v[8:11]\n"
    "v_exp_f32 v19, v19\n"
    "v_mfma_f32_16x16x32_bf16 v[24:27], a[156:159], a[164:167], v[24:27]\n"
    "v_exp_f32 v51, v51\n"
    "v_mfma_f32_16x16x32_bf16 v[32:35], a[144:147], a[176:179], v[32:35]\n"
    "s_waitcnt lgkmcnt(7)\n"
    "v_cvt_pk_bf16_f32 v219, v58, v59\n"
    "v_cvt_pk_bf16_f32 v233, v50, v51\n"
    "v_mfma_f32_16x16x32_bf16 v[36:39], a[112:115], a[192:195], v[100:103]\n"
    "s_waitcnt lgkmcnt(3)\n"
    "v_exp_f32 v21, v21\n"
    "v_mfma_f32_16x16x32_bf16 v[40:43], a[132:135], a[236:239], v[100:103]\n"
    "ds_read_b128 a[156:159], v252 offset:48128\n"
    "v_cvt_pk_bf16_f32 v227, v62, v63\n"
    "v_mfma_f32_16x16x32_bf16 v[36:39], a[116:119], a[196:199], v[36:39]\n"
    "s_add_u32 s59, s59, 4\n"
    "v_exp_f32 v28, v28\n"
    "v_mfma_f32_16x16x32_bf16 v[32:35], a[140:143], a[172:175], v[32:35]\n"
    "v_exp_f32 v52, v52\n"
    "v_mfma_f32_16x16x32_bf16 v[32:35], a[136:139], a[168:171], v[32:35]\n"
    "v_cvt_pk_bf16_f32 v232, v48, v49\n"
    "v_mad_u32_u24 v244, v224, 24, v2\n"
    "v_mfma_f32_16x16x32_bf16 v[48:51], a[132:135], a[188:191], v[100:103]\n"
    "ds_read_b128 a[136:139], v252 offset:37888\n"
    "ds_read_b128 v[104:107], v250 offset:1680\n"
    "v_mfma_f32_16x16x32_bf16 v[36:39], a[120:123], a[200:203], v[36:39]\n"
    "s_waitcnt lgkmcnt(5)\n"
    "v_exp_f32 v57, v57\n"
    "v_mfma_f32_16x16x32_bf16 v[36:39], a[124:127], a[204:207], v[36:39]\n"
    "v_cvt_pk_bf16_f32 v225, v46, v47\n"
    "v_cvt_pk_bf16_f32 v210, v52, v53\n"
    "v_mfma_f32_16x16x32_bf16 v[36:39], a[128:131], a[208:211], v[36:39]\n"
    "v_cvt_pk_bf16_f32 v224, v44, v45\n"
    "v_cvt_pk_bf16_f32 v211, v54, v55\n"
    "v_mfma_f32_16x16x32_bf16 v[40:43], a[128:131], a[232:235], v[40:43]\n"
    "ds_read_b128 a[144:147], v252 offset:41984\n"
    "ds_read_b128 a[140:143], v252 offset:39936\n"
    "v_mfma_f32_16x16x32_bf16 v[44:47], a[112:115], a[240:243], v[100:103]\n"
    "v_exp_f32 v20, v20\n"
    "v_mfma_f32_16x16x32_bf16 v[40:43], a[124:127], a[228:231], v[40:43]\n"
    "v_cvt_pk_bf16_f32 v12, v12, v13\n"
    "v_cvt_pk_bf16_f32 v218, v56, v57\n"
    "v_mfma_f32_16x16x32_bf16 v[36:39], a[132:135], a[212:215], v[36:39]\n"
    "v_exp_f32 v32, v32\n"
    "v_mfma_f32_16x16x32_bf16 v[40:43], a[120:123], a[224:227], v[40:43]\n"
    "v_exp_f32 v30, v30\n"
    "v_smfmac_f32_16x16x64_bf16 v[120:123], v[240:243], v[228:235], v253\n"
    "ds_read_b128 a[148:151], v252 offset:44032\n"
    "v_mfma_f32_16x16x32_bf16 v[44:47], a[116:119], a[244:247], v[44:47]\n"
    "s_waitcnt lgkmcnt(3)\n"
    "v_exp_f32 v26, v26\n"
    "v_mfma_f32_16x16x32_bf16 v[52:55], a[136:139], a[192:195], v[104:107]\n"
    "v_exp_f32 v15, v15\n"
    "v_mfma_f32_16x16x32_bf16 v[56:59], a[156:159], a[236:239], v[104:107]\n"
    "s_waitcnt lgkmcnt(1)\n"
    "v_cvt_pk_bf16_f32 v4, v4, v5\n"
    "v_cmp_le_u32 s[56:57], s59, v245\n"
    "v_smfmac_f32_16x16x64_bf16 v[108:111], v[240:243], v[204:211], v253\n"
    "s_cmp_eq_u64 s[56:57], -1\n"
    "v_lshrrev_b32 v252, 3, v1\n"
    "v_mfma_f32_16x16x32_bf16 v[52:55], a[140:143], a[196:199], v[52:55]\n"
    "v_exp_f32 v7, v7\n"
    "v_mfma_f32_16x16x32_bf16 v[60:63], a[136:139], a[240:243], v[104:107]\n"
    "v_exp_f32 v17, v17\n"
    "v_mfma_f32_16x16x32_bf16 v[60:63], a[140:143], a[244:247], v[60:63]\n"
    "v_exp_f32 v14, v14\n"
    "v_mfma_f32_16x16x32_bf16 v[56:59], a[152:155], a[232:235], v[56:59]\n"
    "v_exp_f32 v39, v39\n"
    "v_mfma_f32_16x16x32_bf16 v[64:67], a[156:159], a[188:191], v[104:107]\n"
    "v_exp_f32 v11, v11\n"
    "v_mfma_f32_16x16x32_bf16 v[60:63], a[144:147], a[248:251], v[60:63]\n"
    "v_exp_f32 v22, v22\n"
    "v_mfma_f32_16x16x32_bf16 v[52:55], a[144:147], a[200:203], v[52:55]\n"
    "s_waitcnt lgkmcnt(0)\n"
    "v_exp_f32 v27, v27\n"
    "v_mfma_f32_16x16x32_bf16 v[52:55], a[148:151], a[204:207], v[52:55]\n"
    "v_exp_f32 v8, v8\n"
    "v_mfma_f32_16x16x32_bf16 v[52:55], a[152:155], a[208:211], v[52:55]\n"
    "v_exp_f32 v9, v9\n"
    "v_mfma_f32_16x16x32_bf16 v[56:59], a[148:151], a[228:231], v[56:59]\n"
    "ds_read_b128 a[204:207], v248 offset:13312\n"
    "v_cvt_pk_bf16_f32 v5, v6, v7\n"
    "v_mfma_f32_16x16x32_bf16 v[64:67], a[152:155], a[184:187], v[64:67]\n"
    "v_exp_f32 v16, v16\n"
    "v_mfma_f32_16x16x32_bf16 v[56:59], a[144:147], a[224:227], v[56:59]\n"
    "ds_read_b128 a[208:211], v248 offset:2048\n"
    "ds_read_b128 a[196:199], v248 offset:12288\n"
    "v_mfma_f32_16x16x32_bf16 v[40:43], a[116:119], a[220:223], v[40:43]\n"
    "v_exp_f32_e64 v38, v38\n"
    "v_mfma_f32_16x16x32_bf16 v[52:55], a[156:159], a[212:215], v[52:55]\n"
    "v_exp_f32_e64 v37, v37\n"
    "v_smfmac_f32_16x16x64_bf16 v[116:119], v[240:243], v[220:227], v253\n"
    "v_cvt_pk_bf16_f32 v6, v8, v9\n"
    "v_smfmac_f32_16x16x64_bf16 v[112:115], v[240:243], v[212:219], v253\n"
    "s_cbranch_scc0 .Lpt4\n"
    ".Lpt4r:\n"
    "v_mfma_f32_16x16x32_bf16 v[44:47], a[120:123], a[248:251], v[44:47]\n"
    "v_exp_f32 v10, v10\n"
    "v_mfma_f32_16x16x32_bf16 v[40:43], a[112:115], a[216:219], v[40:43]\n"
    "v_exp_f32 v25, v25\n"
    "v_mfma_f32_16x16x32_bf16 v[48:51], a[128:131], a[184:187], v[48:51]\n"
    "v_exp_f32 v54, v54\n"
    "v_mfma_f32_16x16x32_bf16 v[56:59], a[140:143], a[220:223], v[56:59]\n"
    "s_waitcnt lgkmcnt(1)\n"
    "ds_read_b128 a[212:215], v248 offset:14336\n"
    "v_cvt_pk_bf16_f32 v20, v20, v21\n"
    "v_mfma_f32_16x16x32_bf16 v[148:151], a[208:211], v[76:79], v[148:151]\n"
    "s_waitcnt vmcnt(0)\n"
    "ds_add_u32 v247, v246\n"
    "v_mfma_f32_16x16x32_bf16 v[64:67], a[148:151], a[180:183], v[64:67]\n"
    "ds_read_b128 a[192:195], v248 offset:0\n"
    "ds_read_b128 a[200:203], v248 offset:1024\n"
    "v_mfma_f32_16x16x32_bf16 v[48:51], a[124:127], a[180:183], v[48:51]\n"
    "v_exp_f32 v52, v52\n"
    "v_mfma_f32_16x16x32_bf16 v[128:131], a[204:207], v[72:75], v[128:131]\n"
    "v_exp_f32 v41, v41\n"
    "v_mfma_f32_16x16x32_bf16 v[144:147], a[204:207], v[80:83], v[144:147]\n"
    "v_exp_f32 v36, v36\n"
    "v_mfma_f32_16x16x32_bf16 v[44:47], a[124:127], a[252:255], v[44:47]\n"
    "v_exp_f32 v24, v24\n"
    "v_mfma_f32_16x16x32_bf16 v[60:63], a[148:151], a[252:255], v[60:63]\n"
    "v_exp_f32 v23, v23\n"
    "v_mfma_f32_16x16x32_bf16 v[44:47], a[128:131], a[160:163], v[44:47]\n"
    "v_exp_f32 v55, v55\n"
    "v_mfma_f32_16x16x32_bf16 v[48:51], a[120:123], a[176:179], v[48:51]\n"
    "v_exp_f32 v29, v29\n"
    "v_mfma_f32_16x16x32_bf16 v[64:67], a[144:147], a[176:179], v[64:67]\n"
    "ds_read_b128 a[236:239], v249 offset:37888\n"
    "v_cvt_pk_bf16_f32 v9, v38, v39\n"
    "v_mfma_f32_16x16x32_bf16 v[56:59], a[136:139], a[216:219], v[56:59]\n"
    "ds_read_b128 a[220:223], v248 offset:15360\n"
    "ds_read_b128 a[244:247], v249 offset:38912\n"
    "v_mfma_f32_16x16x32_bf16 v[48:51], a[116:119], a[172:175], v[48:51]\n"
    "s_waitcnt lgkmcnt(6)\n"
    "ds_read_b128 a[232:235], v249 offset:25600\n"
    "ds_read_b128 a[240:243], v249 offset:26624\n"
    "v_mfma_f32_16x16x32_bf16 v[64:67], a[140:143], a[172:175], v[64:67]\n"
    "s_add_u32 s59, s59, 4\n"
    "v_cvt_pk_bf16_f32 v7, v10, v11\n"
    "v_cvt_pk_bf16_f32 v13, v14, v15\n"
    "v_mfma_f32_16x16x32_bf16 v[148:151], a[212:215], v[80:83], v[148:151]\n"
    "v_exp_f32 v53, v53\n"
    "v_mfma_f32_16x16x32_bf16 v[44:47], a[132:135], a[164:167], v[44:47]\n"
    "ds_read_b32 v245, v247\n"
    "v_cvt_pk_bf16_f32 v11, v54, v55\n"
    "v_mfma_f32_16x16x32_bf16 v[60:63], a[152:155], a[160:163], v[60:63]\n"
    "ds_read_b128 v[240:243], v251 offset:1536\n"
    "ds_read_b128 a[216:219], v248 offset:3072\n"
    "v_mfma_f32_16x16x32_bf16 v[64:67], a[136:139], a[168:171], v[64:67]\n"
    "s_waitcnt lgkmcnt(8)\n"
    "v_cvt_pk_bf16_f32 v8, v36, v37\n"
    "v_cvt_pk_bf16_f32 v10, v52, v53\n"
    "v_mfma_f32_16x16x32_bf16 v[128:131], a[200:203], v[68:71], v[128:131]\n"
    "v_exp_f32 v43, v43\n"
    "v_mfma_f32_16x16x32_bf16 v[60:63], a[156:159], a[164:167], v[60:63]\n"
    "v_exp_f32 v45, v45\n"
    "v_mfma_f32_16x16x32_bf16 v[140:143], a[192:195], v[76:79], v[140:143]\n"
    "ds_read_b128 a[252:255], v249 offset:39936\n"
    "ds_read_b128 a[224:227], v249 offset:24576\n"
    "v_mfma_f32_16x16x32_bf16 v[124:127], a[192:195], v[68:71], v[124:127]\n"
    "s_waitcnt lgkmcnt(9)\n"
    "v_cvt_pk_bf16_f32 v14, v16, v17\n"
    "v_cvt_pk_bf16_f32 v21, v22, v23\n"
    "v_mfma_f32_16x16x32_bf16 v[128:131], a[236:239], v[208:211], v[128:131]\n"
    "v_exp_f32 v58, v58\n"
    "v_mfma_f32_16x16x32_bf16 v[140:143], a[196:199], v[80:83], v[140:143]\n"
    "s_waitcnt lgkmcnt(4)\n"
    "ds_read_b128 a[228:231], v249 offset:36864\n"
    "v_cmp_le_u32 s[56:57], s59, v245\n"
    "v_mfma_f32_16x16x32_bf16 v[124:127], a[196:199], v[72:75], v[124:127]\n"
    "v_exp_f32 v62, v62\n"
    "v_mfma_f32_16x16x32_bf16 v[148:151], a[240:243], v[212:215], v[148:151]\n"
    "s_cmp_eq_u64 s[56:57], -1\n"
    "v_exp_f32 v40, v40\n"
    "v_mfma_f32_16x16x32_bf16 v[132:135], a[208:211], v[68:71], v[132:135]\n"
    "s_waitcnt lgkmcnt(4)\n"
    "v_exp_f32 v42, v42\n"
    "v_smfmac_f32_16x16x64_bf16 v[108:111], v[240:243], v[4:11], v253\n"
    "s_cbranch_scc0 .Lpt3\n"
    ".Lpt3r:\n"
    "v_mfma_f32_16x16x32_bf16 v[48:51], a[112:115], a[168:171], v[48:51]\n"
    "s_waitcnt lgkmcnt(1)\n"
    "ds_read_b128 a[176:179], v249 offset:2048\n"
    "ds_read_b128 a[168:171], v249 offset:1024\n"
    "v_mfma_f32_16x16x32_bf16 v[124:127], a[224:227], v[204:207], v[124:127]\n"
    "v_exp_f32 v18, v18\n"
    "v_mfma_f32_16x16x32_bf16 v[136:139], a[220:223], v[72:75], v[136:139]\n"
    "v_cvt_pk_bf16_f32 v16, v40, v41\n"
    "v_cvt_pk_bf16_f32 v15, v18, v19\n"
    "v_mfma_f32_16x16x32_bf16 v[152:155], a[220:223], v[80:83], v[152:155]\n"
    "s_waitcnt lgkmcnt(2)\n"
    "v_exp_f32 v61, v61\n"
    "v_mfma_f32_16x16x32_bf16 v[124:127], a[228:231], v[208:211], v[124:127]\n"
    "v_exp_f32 v63, v63\n"
    "v_mov_b32 v111, v110\n"
    "v_mfma_f32_16x16x32_bf16 v[152:155], a[216:219], v[76:79], v[152:155]\n"
    "v_add_f32 v108, v108, v109\n"
    "v_cvt_pk_bf16_f32 v22, v24, v25\n"
    "v_mfma_f32_16x16x32_bf16 v[132:135], a[212:215], v[72:75], v[132:135]\n"
    "ds_read_b128 a[188:191], v249 offset:15360\n"
    "ds_read_b128 a[248:251], v249 offset:27648\n"
    "v_mfma_f32_16x16x32_bf16 v[136:139], a[216:219], v[68:71], v[136:139]\n"
    "ds_read_b128 a[172:175], v249 offset:13312\n"
    "ds_read_b128 a[180:183], v249 offset:14336\n"
    "v_mfma_f32_16x16x32_bf16 v[144:147], a[200:203], v[76:79], v[144:147]\n"
    "v_exp_f32 v35, v35\n"
    "v_mfma_f32_16x16x32_bf16 v[132:135], a[240:243], v[204:207], v[132:135]\n"
    "ds_read_b128 a[160:163], v249 offset:0\n"
    "v_mov_b32 v109, v108\n"
    "v_mfma_f32_16x16x32_bf16 v[136:139], a[252:255], v[208:211], v[136:139]\n"
    "ds_read_b128 a[164:167], v249 offset:12288\n"
    "ds_read_b128 a[184:187], v249 offset:3072\n"
    "v_mfma_f32_16x16x32_bf16 v[132:135], a[244:247], v[208:211], v[132:135]\n"
    "s_waitcnt lgkmcnt(8)\n"
    "v_exp_f32 v46, v46\n"
    "v_mfma_f32_16x16x32_bf16 v[132:135], a[176:179], v[4:7], v[132:135]\n"
    "v_exp_f32 v59, v59\n"
    "v_mfma_f32_16x16x32_bf16 v[144:147], a[236:239], v[216:219], v[144:147]\n"
    "v_exp_f32 v67, v67\n"
    "v_mfma_f32_16x16x32_bf16 v[144:147], a[232:235], v[212:215], v[144:147]\n"
    "v_permlane16_swap_b32 v108, v109\n"
    "v_mfma_f32_16x16x32_bf16 v[128:131], a[232:235], v[204:207], v[128:131]\n"
    "s_waitcnt lgkmcnt(3)\n"
    "v_exp_f32 v44, v44\n"
    "v_mfma_f32_16x16x32_bf16 v[132:135], a[180:183], v[8:11], v[132:135]\n"
    "v_cvt_pk_bf16_f32 v23, v26, v27\n"
    "v_cvt_pk_bf16_f32 v27, v62, v63\n"
    "v_mfma_f32_16x16x32_bf16 v[136:139], a[248:251], v[204:207], v[136:139]\n"
    "s_waitcnt lgkmcnt(2)\n"
    "v_exp_f32 v57, v57\n"
    "v_mfma_f32_16x16x32_bf16 v[124:127], a[160:163], v[4:7], v[124:127]\n"
    "v_exp_f32 v56, v56\n"
    "v_mfma_f32_16x16x32_bf16 v[140:143], a[224:227], v[212:215], v[140:143]\n"
    "s_waitcnt lgkmcnt(1)\n"
    "v_and_b32 v252, 0x70, v252\n"
    "v_add_f32 v108, v108, v109\n"
    "v_mfma_f32_16x16x32_bf16 v[124:127], a[164:167], v[8:11], v[124:127]\n"
    "v_permlane16_swap_b32 v110, v111\n"
    "v_mfma_f32_16x16x32_bf16 v[128:131], a[172:175], v[8:11], v[128:131]\n"
    "v_add_f32 v110, v110, v111\n"
    "v_cvt_pk_bf16_f32 v18, v56, v57\n"
    "v_mfma_f32_16x16x32_bf16 v[128:131], a[168:171], v[4:7], v[128:131]\n"
    ".Lwp1:\n"
    "s_cbranch_execz .Lwp2\n"
    "v_rcp_f32 v109, v108\n"
    "v_mfma_f32_16x16x32_bf16 v[136:139], a[188:191], v[8:11], v[136:139]\n"
    "s_waitcnt lgkmcnt(0)\n"
    "v_exp_f32 v60, v60\n"
    "v_mfma_f32_16x16x32_bf16 v[136:139], a[184:187], v[4:7], v[136:139]\n"
    "v_mul_f32 v111, -v110, v109\n"
    "v_cvt_pk_bf16_f32 v19, v58, v59\n"
    "v_cvt_pk_bf16_f32 v17, v42, v43\n"
    "v_xor_b32 v252, v252, v1\n"
    "v_ldexp_f32 v109, v109, s79\n"
    "v_pk_fma_f32 v[134:135], v[134:135], v[108:109], v[110:111] op_sel:[0,1,1] op_sel_hi:[1,1,1]\n"
    "v_pk_fma_f32 v[124:125], v[124:125], v[108:109], v[110:111] op_sel:[0,1,1] op_sel_hi:[1,1,1]\n"
    "v_pk_fma_f32 v[126:127], v[126:127], v[108:109], v[110:111] op_sel:[0,1,1] op_sel_hi:[1,1,1]\n"
    "v_pk_fma_f32 v[128:129], v[128:129], v[108:109], v[110:111] op_sel:[0,1,1] op_sel_hi:[1,1,1]\n"
    "v_pk_fma_f32 v[132:133], v[132:133], v[108:109], v[110:111] op_sel:[0,1,1] op_sel_hi:[1,1,1]\n"
    "v_pk_fma_f32 v[130:131], v[130:131], v[108:109], v[110:111] op_sel:[0,1,1] op_sel_hi:[1,1,1]\n"
    "v_pk_fma_f32 v[138:139], v[138:139], v[108:109], v[110:111] op_sel:[0,1,1] op_sel_hi:[1,1,1]\n"
    "v_pk_fma_f32 v[136:137], v[136:137], v[108:109], v[110:111] op_sel:[0,1,1] op_sel_hi:[1,1,1]\n"
    "v_cvt_pk_bf16_f32 v124, v124, v128\n"
    "v_cvt_pk_bf16_f32 v128, v126, v130\n"
    "v_mfma_f32_16x16x32_bf16 v[152:155], a[252:255], v[216:219], v[152:155]\n"
    "v_cvt_pk_bf16_f32 v130, v127, v131\n"
    "v_cvt_pk_bf16_f32 v126, v125, v129\n"
    "v_cvt_pk_bf16_f32 v127, v133, v137\n"
    "v_mfma_f32_16x16x32_bf16 v[148:151], a[244:247], v[216:219], v[148:151]\n"
    "v_add_u32 v252, s72, v252\n"
    "v_cvt_pk_bf16_f32 v125, v132, v136\n"
    "v_mfma_f32_16x16x32_bf16 v[140:143], a[228:231], v[216:219], v[140:143]\n"
    "ds_write_b128 v3, v[124:127] offset:0\n"
    "v_smfmac_f32_16x16x64_bf16 v[112:115], v[240:243], v[12:19], v253\n"
    "v_cvt_pk_bf16_f32 v129, v134, v138\n"
    "v_cvt_pk_bf16_f32 v26, v60, v61\n"
    "v_mfma_f32_16x16x32_bf16 v[148:151], a[176:179], v[12:15], v[148:151]\n"
    "v_exp_f32 v51, v51\n"
    "v_mfma_f32_16x16x32_bf16 v[140:143], a[160:163], v[12:15], v[140:143]\n"
    "v_cvt_pk_bf16_f32 v131, v135, v139\n"
    "v_xor_b32 v2, 64, v3\n"
    "v_mfma_f32_16x16x32_bf16 v[156:159], a[192:195], v[84:87], v[156:159]\n"
    "ds_write_b128 v2, v[128:131] offset:0\n"
    "v_mfma_f32_16x16x32_bf16 v[156:159], a[196:199], v[88:91], v[156:159]\n"
    "v_exp_f32 v47, v47\n"
    "v_mfma_f32_16x16x32_bf16 v[152:155], a[248:251], v[212:215], v[152:155]\n"
    "v_cvt_pk_bf16_f32 v24, v44, v45\n"
    "v_cvt_pk_bf16_f32 v25, v46, v47\n"
    "v_mfma_f32_16x16x32_bf16 v[156:159], a[224:227], v[220:223], v[156:159]\n"
    "ds_read_b128 v[132:135], v252 offset:0\n"
    "v_add_f32 v112, v112, v113\n"
    "v_mfma_f32_16x16x32_bf16 v[140:143], a[164:167], v[16:19], v[140:143]\n"
    "v_exp_f32 v34, v34\n"
    "v_mfma_f32_16x16x32_bf16 v[144:147], a[172:175], v[16:19], v[144:147]\n"
    "v_mov_b32 v113, v112\n"
    "v_mov_b32 v115, v114\n"
    "v_mfma_f32_16x16x32_bf16 v[144:147], a[168:171], v[12:15], v[144:147]\n"
    "ds_read_b128 v[136:139], v252 offset:1024\n"
    "v_permlane16_swap_b32 v112, v113\n"
    "v_mfma_f32_16x16x32_bf16 v[164:167], a[208:211], v[84:87], v[164:167]\n"
    "v_permlane16_swap_b32 v114, v115\n"
    "v_mfma_f32_16x16x32_bf16 v[152:155], a[188:191], v[16:19], v[152:155]\n"
    "v_exp_f32_e64 v48, v48\n"
    "v_mfma_f32_16x16x32_bf16 v[148:151], a[180:183], v[16:19], v[148:151]\n"
    "v_add_f32 v112, v112, v113\n"
    "v_cvt_pk_bf16_f32 v28, v28, v29\n"
    "v_mfma_f32_16x16x32_bf16 v[152:155], a[184:187], v[12:15], v[152:155]\n"
    "v_rcp_f32 v113, v112\n"
    "v_smfmac_f32_16x16x64_bf16 v[116:119], v[240:243], v[20:27], v253\n"
    "s_waitcnt lgkmcnt(1)\n"
    "buffer_store_dwordx4 v[132:135], v244, s[24:27], s75 offen offset:0 sc1\n"
    "v_add_f32 v114, v114, v115\n"
    "v_mul_f32 v115, -v114, v113\n"
    "v_ldexp_f32 v113, v113, s79\n"
    "v_pk_fma_f32 v[142:143], v[142:143], v[112:113], v[114:115] op_sel:[0,1,1] op_sel_hi:[1,1,1]\n"
    "v_pk_fma_f32 v[144:145], v[144:145], v[112:113], v[114:115] op_sel:[0,1,1] op_sel_hi:[1,1,1]\n"
    "v_pk_fma_f32 v[140:141], v[140:141], v[112:113], v[114:115] op_sel:[0,1,1] op_sel_hi:[1,1,1]\n"
    "v_pk_fma_f32 v[146:147], v[146:147], v[112:113], v[114:115] op_sel:[0,1,1] op_sel_hi:[1,1,1]\n"
    "v_pk_fma_f32 v[150:151], v[150:151], v[112:113], v[114:115] op_sel:[0,1,1] op_sel_hi:[1,1,1]\n"
    "v_add_f32_e64 v116, v116, v117\n"
    "v_pk_fma_f32 v[152:153], v[152:153], v[112:113], v[114:115] op_sel:[0,1,1] op_sel_hi:[1,1,1]\n"
    "v_pk_fma_f32 v[148:149], v[148:149], v[112:113], v[114:115] op_sel:[0,1,1] op_sel_hi:[1,1,1]\n"
    "v_pk_fma_f32 v[154:155], v[154:155], v[112:113], v[114:115] op_sel:[0,1,1] op_sel_hi:[1,1,1]\n"
    "v_cvt_pk_bf16_f32 v140, v140, v144\n"
    "v_cvt_pk_bf16_f32 v144, v142, v146\n"
    "v_mfma_f32_16x16x32_bf16 v[160:163], a[204:207], v[88:91], v[160:163]\n"
    "v_cvt_pk_bf16_f32 v146, v143, v147\n"
    "v_cvt_pk_bf16_f32 v142, v141, v145\n"
    "v_mfma_f32_16x16x32_bf16 v[156:159], a[228:231], v[224:227], v[156:159]\n"
    "v_cvt_pk_bf16_f32 v141, v148, v152\n"
    "v_cvt_pk_bf16_f32 v143, v149, v153\n"
    "v_mfma_f32_16x16x32_bf16 v[156:159], a[160:163], v[20:23], v[156:159]\n"
    "s_waitcnt lgkmcnt(0)\n"
    "buffer_store_dwordx4 v[136:139], v244, s[24:27], s75 offen offset:3072 sc1\n"
    "ds_write_b128 v3, v[140:143] offset:2048\n"
    "v_mfma_f32_16x16x32_bf16 v[172:175], a[192:195], v[92:95], v[172:175]\n"
    "v_mov_b32 v119, v118\n"
    "v_cvt_pk_bf16_f32 v145, v150, v154\n"
    "v_mfma_f32_16x16x32_bf16 v[156:159], a[164:167], v[24:27], v[156:159]\n"
    "v_cvt_pk_bf16_f32 v147, v151, v155\n"
    "v_mov_b32 v117, v116\n"
    "v_mfma_f32_16x16x32_bf16 v[180:183], a[208:211], v[92:95], v[180:183]\n"
    "ds_write_b128 v2, v[144:147] offset:2048\n"
    "v_mfma_f32_16x16x32_bf16 v[160:163], a[200:203], v[84:87], v[160:163]\n"
    "v_permlane16_swap_b32 v118, v119\n"
    "v_mfma_f32_16x16x32_bf16 v[168:171], a[220:223], v[88:91], v[168:171]\n"
    "v_exp_f32 v33, v33\n"
    "v_mfma_f32_16x16x32_bf16 v[172:175], a[196:199], v[96:99], v[172:175]\n"
    "ds_read_b128 v[148:151], v252 offset:2048\n"
    "ds_read_b128 v[152:155], v252 offset:3072\n"
    "v_mfma_f32_16x16x32_bf16 v[180:183], a[212:215], v[96:99], v[180:183]\n"
    "v_exp_f32 v49, v49\n"
    "v_mfma_f32_16x16x32_bf16 v[160:163], a[236:239], v[224:227], v[160:163]\n"
    "v_permlane16_swap_b32 v116, v117\n"
    "v_mfma_f32_16x16x32_bf16 v[168:171], a[216:219], v[84:87], v[168:171]\n"
    "v_exp_f32 v31, v31\n"
    "v_mfma_f32_16x16x32_bf16 v[176:179], a[204:207], v[96:99], v[176:179]\n"
    "v_exp_f32 v66, v66\n"
    "v_mfma_f32_16x16x32_bf16 v[164:167], a[212:215], v[88:91], v[164:167]\n"
    "ds_read_b128 a[204:207], v248 offset:17408\n"
    "v_add_f32 v118, v118, v119\n"
    "v_mfma_f32_16x16x32_bf16 v[160:163], a[232:235], v[220:223], v[160:163]\n"
    "v_cvt_pk_bf16_f32 v29, v30, v31\n"
    "v_add_f32 v116, v116, v117\n"
    "v_mfma_f32_16x16x32_bf16 v[168:171], a[252:255], v[224:227], v[168:171]\n"
    "v_rcp_f32 v117, v116\n"
    "v_mfma_f32_16x16x32_bf16 v[160:163], a[172:175], v[24:27], v[160:163]\n"
    "s_waitcnt lgkmcnt(1)\n"
    "buffer_store_dwordx4 v[152:155], v244, s[24:27], s76 offen offset:3072 sc1\n"
    "v_exp_f32 v50, v50\n"
    "v_exp_f32 v64, v64\n"
    "v_mfma_f32_16x16x32_bf16 v[176:179], a[200:203], v[92:95], v[176:179]\n"
    "ds_read_b128 a[196:199], v248 offset:16384\n"
    "v_cvt_pk_bf16_f32 v31, v34, v35\n"
    "v_mfma_f32_16x16x32_bf16 v[160:163], a[168:171], v[20:23], v[160:163]\n"
    "v_mul_f32 v119, -v118, v117\n"
    "v_cvt_pk_bf16_f32 v30, v32, v33\n"
    "v_mfma_f32_16x16x32_bf16 v[164:167], a[240:243], v[220:223], v[164:167]\n"
    "buffer_store_dwordx4 v[148:151], v244, s[24:27], s76 offen offset:0 sc1\n"
    "v_cvt_pk_bf16_f32 v35, v66, v67\n"
    "v_ldexp_f32 v117, v117, s79\n"
    "v_exp_f32 v65, v65\n"
    "v_mfma_f32_16x16x32_bf16 v[184:187], a[220:223], v[96:99], v[184:187]\n"
    "v_fma_f32 v156, v156, v117, v119\n"
    "v_fma_f32 v157, v157, v117, v119\n"
    "v_mfma_f32_16x16x32_bf16 v[168:171], a[248:251], v[220:223], v[168:171]\n"
    "v_fma_f32 v158, v158, v117, v119\n"
    "v_fma_f32 v159, v159, v117, v119\n"
    "v_mfma_f32_16x16x32_bf16 v[164:167], a[244:247], v[224:227], v[164:167]\n"
    "v_fma_f32 v160, v160, v117, v119\n"
    "v_fma_f32 v161, v161, v117, v119\n"
    "v_mfma_f32_16x16x32_bf16 v[168:171], a[188:191], v[24:27], v[168:171]\n"
    "v_fma_f32 v162, v162, v117, v119\n"
    "v_fma_f32 v163, v163, v117, v119\n"
    "v_mfma_f32_16x16x32_bf16 v[172:175], a[224:227], v[228:231], v[172:175]\n"
    "ds_read_b128 a[224:227], v249 offset:28672\n"
    "v_cvt_pk_bf16_f32 v34, v64, v65\n"
    "v_mfma_f32_16x16x32_bf16 v[168:171], a[184:187], v[20:23], v[168:171]\n"
    "v_cvt_pk_bf16_f32 v156, v156, v160\n"
    "v_cvt_pk_bf16_f32 v160, v158, v162\n"
    "v_mfma_f32_16x16x32_bf16 v[164:167], a[176:179], v[20:23], v[164:167]\n"
    "v_cvt_pk_bf16_f32 v162, v159, v163\n"
    "v_cvt_pk_bf16_f32 v32, v48, v49\n"
    "v_mfma_f32_16x16x32_bf16 v[164:167], a[180:183], v[24:27], v[164:167]\n"
    "ds_read_b128 a[192:195], v248 offset:4096\n"
    "v_cvt_pk_bf16_f32 v158, v157, v161\n"
    "v_mfma_f32_16x16x32_bf16 v[180:183], a[240:243], v[228:231], v[180:183]\n"
    "v_cvt_pk_bf16_f32 v33, v50, v51\n"
    "v_fma_f32 v168, v168, v117, v119\n"
    "v_fma_f32 v169, v169, v117, v119\n"
    "v_mfma_f32_16x16x32_bf16 v[184:187], a[216:219], v[92:95], v[184:187]\n"
    "v_fma_f32 v170, v170, v117, v119\n"
    "v_fma_f32 v171, v171, v117, v119\n"
    "v_smfmac_f32_16x16x64_bf16 v[120:123], v[240:243], v[28:35], v253\n"
    "ds_read_b128 a[220:223], v248 offset:19456\n"
    "v_mfma_f32_16x16x32_bf16 v[184:187], a[252:255], v[232:235], v[184:187]\n"
    "v_fma_f32 v164, v164, v117, v119\n"
    "v_fma_f32 v165, v165, v117, v119\n"
    "v_mfma_f32_16x16x32_bf16 v[172:175], a[228:231], v[232:235], v[172:175]\n"
    "v_cvt_pk_bf16_f32 v157, v164, v168\n"
    "v_cvt_pk_bf16_f32 v159, v165, v169\n"
    "v_mfma_f32_16x16x32_bf16 v[176:179], a[236:239], v[232:235], v[176:179]\n"
    "ds_write_b128 v3, v[156:159] offset:4096\n"
    "v_mfma_f32_16x16x32_bf16 v[180:183], a[244:247], v[232:235], v[180:183]\n"
    "s_waitcnt lgkmcnt(2)\n"
    "v_fma_f32 v166, v166, v117, v119\n"
    "v_fma_f32 v167, v167, v117, v119\n"
    "v_mfma_f32_16x16x32_bf16 v[188:191], a[192:195], v[68:71], v[188:191]\n"
    "v_cvt_pk_bf16_f32 v163, v167, v171\n"
    "v_cvt_pk_bf16_f32 v161, v166, v170\n"
    "v_mfma_f32_16x16x32_bf16 v[184:187], a[248:251], v[228:231], v[184:187]\n"
    "ds_write_b128 v2, v[160:163] offset:4096\n"
    "v_mfma_f32_16x16x32_bf16 v[192:195], a[204:207], v[72:75], v[192:195]\n"
    "v_add_f32_e64 v120, v120, v121\n"
    "v_mov_b32_e64 v121, v120\n"
    "v_mfma_f32_16x16x32_bf16 v[188:191], a[196:199], v[72:75], v[188:191]\n"
    "s_nop 0\n"
    "v_permlane16_swap_b32 v120, v121\n"
    "v_mfma_f32_16x16x32_bf16 v[176:179], a[232:235], v[228:231], v[176:179]\n"
    "s_waitcnt lgkmcnt(2)\n"
    "ds_read_b128 v[164:167], v252 offset:4096\n"
    "ds_read_b128 a[228:231], v249 offset:40960\n"
    "v_mfma_f32_16x16x32_bf16 v[200:203], a[220:223], v[72:75], v[200:203]\n"
    "ds_read_b128 a[212:215], v248 offset:18432\n"
    "ds_read_b128 v[168:171], v252 offset:5120\n"
    "v_mfma_f32_16x16x32_bf16 v[180:183], a[176:179], v[28:31], v[180:183]\n"
    "ds_read_b128 a[132:135], v249 offset:18432\n"
    "v_mov_b32 v123, v122\n"
    "v_mfma_f32_16x16x32_bf16 v[176:179], a[172:175], v[32:35], v[176:179]\n"
    "ds_read_b128 a[208:211], v248 offset:6144\n"
    "ds_read_b128 a[216:219], v248 offset:7168\n"
    "v_mfma_f32_16x16x32_bf16 v[188:191], a[224:227], v[204:207], v[188:191]\n"
    "ds_read_b128 a[248:251], v249 offset:31744\n"
    "ds_read_b128 a[236:239], v249 offset:41984\n"
    "v_mfma_f32_16x16x32_bf16 v[184:187], a[188:191], v[32:35], v[184:187]\n"
    "v_permlane16_swap_b32_e64 v122, v123\n"
    "v_mfma_f32_16x16x32_bf16 v[172:175], a[160:163], v[28:31], v[172:175]\n"
    "ds_read_b128 a[140:143], v249 offset:19456\n"
    "ds_read_b128 a[200:203], v248 offset:5120\n"
    "v_mfma_f32_16x16x32_bf16 v[180:183], a[180:183], v[32:35], v[180:183]\n"
    "ds_read_b128 a[136:139], v249 offset:7168\n"
    "ds_read_b128 a[124:127], v249 offset:17408\n"
    "v_mfma_f32_16x16x32_bf16 v[184:187], a[184:187], v[28:31], v[184:187]\n"
    "s_waitcnt lgkmcnt(12)\n"
    "buffer_store_dwordx4 v[164:167], v244, s[24:27], s77 offen offset:0 sc1\n"
    "ds_read_b128 a[240:243], v249 offset:30720\n"
    "ds_read_b128 a[252:255], v249 offset:44032\n"
    "v_add_f32 v122, v122, v123\n"
    "v_add_f32 v120, v120, v121\n"
    "v_mfma_f32_16x16x32_bf16 v[176:179], a[168:171], v[28:31], v[176:179]\n"
    "ds_read_b128 a[112:115], v249 offset:4096\n"
    "ds_read_b128 a[128:131], v249 offset:6144\n"
    "v_mfma_f32_16x16x32_bf16 v[172:175], a[164:167], v[32:35], v[172:175]\n"
    "s_waitcnt lgkmcnt(11)\n"
    "v_rcp_f32 v121, v120\n"
    "v_mfma_f32_16x16x32_bf16 v[196:199], a[208:211], v[68:71], v[196:199]\n"
    "ds_read_b128 a[244:247], v249 offset:43008\n"
    "v_mul_f32 v123, -v122, v121\n"
    "v_mfma_f32_16x16x32_bf16 v[188:191], a[228:231], v[208:211], v[188:191]\n"
    "buffer_store_dwordx4 v[168:171], v244, s[24:27], s77 offen offset:3072 sc1\n"
    "ds_read_b128 a[232:235], v249 offset:29696\n"
    "ds_read_b128 a[116:119], v249 offset:16384\n"
    "ds_read_b128 a[120:123], v249 offset:5120\n"
    "v_ldexp_f32 v121, v121, s79\n"
    "v_mfma_f32_16x16x32_bf16 v[196:199], a[212:215], v[72:75], v[196:199]\n"
    "s_waitcnt lgkmcnt(10)\n"
    "v_fma_f32 v174, v174, v121, v123\n"
    "v_fma_f32 v175, v175, v121, v123\n"
    "v_mfma_f32_16x16x32_bf16 v[192:195], a[200:203], v[68:71], v[192:195]\n"
    "v_fma_f32 v176, v176, v121, v123\n"
    "v_fma_f32 v177, v177, v121, v123\n"
    "v_mfma_f32_16x16x32_bf16 v[200:203], a[216:219], v[68:71], v[200:203]\n"
    "s_waitcnt lgkmcnt(7)\n"
    "v_fma_f32 v186, v186, v121, v123\n"
    "v_fma_f32 v187, v187, v121, v123\n"
    "v_mfma_f32_16x16x32_bf16 v[196:199], a[240:243], v[204:207], v[196:199]\n"
    "s_waitcnt lgkmcnt(6)\n"
    "v_fma_f32 v182, v182, v121, v123\n"
    "v_fma_f32 v183, v183, v121, v123\n"
    "v_mfma_f32_16x16x32_bf16 v[200:203], a[252:255], v[208:211], v[200:203]\n"
    "v_fma_f32 v178, v178, v121, v123\n"
    "v_fma_f32 v179, v179, v121, v123\n"
    "v_mfma_f32_16x16x32_bf16 v[200:203], a[248:251], v[204:207], v[200:203]\n"
    "s_waitcnt lgkmcnt(3)\n"
    "v_fma_f32 v172, v172, v121, v123\n"
    "v_fma_f32 v173, v173, v121, v123\n"
    "v_mfma_f32_16x16x32_bf16 v[196:199], a[244:247], v[208:211], v[196:199]\n"
    "v_cvt_pk_bf16_f32 v172, v172, v176\n"
    "v_cvt_pk_bf16_f32 v176, v174, v178\n"
    "v_mfma_f32_16x16x32_bf16 v[192:195], a[236:239], v[208:211], v[192:195]\n"
    "s_waitcnt lgkmcnt(2)\n"
    "v_cvt_pk_bf16_f32 v174, v173, v177\n"
    "v_cvt_pk_bf16_f32 v178, v175, v179\n"
    "v_mfma_f32_16x16x32_bf16 v[192:195], a[232:235], v[204:207], v[192:195]\n"
    "v_cvt_pk_bf16_f32 v177, v182, v186\n"
    "v_cvt_pk_bf16_f32 v179, v183, v187\n"
    "v_mfma_f32_16x16x32_bf16 v[196:199], a[128:131], v[4:7], v[196:199]\n"
    "v_fma_f32 v180, v180, v121, v123\n"
    "v_fma_f32 v181, v181, v121, v123\n"
    "v_mfma_f32_16x16x32_bf16 v[188:191], a[112:115], v[4:7], v[188:191]\n"
    "s_waitcnt lgkmcnt(1)\n"
    "ds_write_b128 v2, v[176:179] offset:0\n"
    "v_mfma_f32_16x16x32_bf16 v[188:191], a[116:119], v[8:11], v[188:191]\n"
    "v_fma_f32 v184, v184, v121, v123\n"
    "v_fma_f32 v185, v185, v121, v123\n"
    "v_mfma_f32_16x16x32_bf16 v[192:195], a[124:127], v[8:11], v[192:195]\n"
    "v_cvt_pk_bf16_f32 v175, v181, v185\n"
    "v_cvt_pk_bf16_f32 v173, v180, v184\n"
    "v_mfma_f32_16x16x32_bf16 v[196:199], a[132:135], v[8:11], v[196:199]\n"
    "ds_write_b128 v3, v[172:175] offset:0\n"
    "v_mfma_f32_16x16x32_bf16 a[4:7], a[204:207], v[80:83], a[4:7]\n"
    "v_fma_f32 v188, v188, v109, v111\n"
    "v_fma_f32 v189, v189, v109, v111\n"
    "v_mfma_f32_16x16x32_bf16 a[0:3], a[192:195], v[76:79], a[0:3]\n"
    "s_waitcnt lgkmcnt(2)\n"
    "v_fma_f32 v190, v190, v109, v111\n"
    "v_fma_f32 v191, v191, v109, v111\n"
    "v_mfma_f32_16x16x32_bf16 v[192:195], a[120:123], v[4:7], v[192:195]\n"
    "ds_read_b128 v[180:183], v252 offset:0\n"
    "ds_read_b128 v[184:187], v252 offset:1024\n"
    "v_mfma_f32_16x16x32_bf16 v[200:203], a[140:143], v[8:11], v[200:203]\n"
    "v_fma_f32 v198, v198, v109, v111\n"
    "v_fma_f32 v199, v199, v109, v111\n"
    "v_mfma_f32_16x16x32_bf16 a[0:3], a[196:199], v[80:83], a[0:3]\n"
    "v_fma_f32 v196, v196, v109, v111\n"
    "v_fma_f32 v197, v197, v109, v111\n"
    "v_mfma_f32_16x16x32_bf16 a[12:15], a[220:223], v[80:83], a[12:15]\n"
    "v_fma_f32 v192, v192, v109, v111\n"
    "v_fma_f32 v193, v193, v109, v111\n"
    "v_mfma_f32_16x16x32_bf16 a[4:7], a[200:203], v[76:79], a[4:7]\n"
    "ds_read_b128 a[164:167], v249 offset:20480\n"
    "ds_read_b128 a[180:183], v249 offset:22528\n"
    "v_mfma_f32_16x16x32_bf16 v[200:203], a[136:139], v[4:7], v[200:203]\n"
    "v_fma_f32 v194, v194, v109, v111\n"
    "v_fma_f32 v195, v195, v109, v111\n"
    "v_mfma_f32_16x16x32_bf16 a[0:3], a[224:227], v[212:215], a[0:3]\n"
    "ds_read_b128 a[172:175], v249 offset:21504\n"
    "v_mfma_f32_16x16x32_bf16 a[4:7], a[236:239], v[216:219], a[4:7]\n"
    "v_cvt_pk_bf16_f32 v188, v188, v192\n"
    "v_cvt_pk_bf16_f32 v192, v190, v194\n"
    "v_mfma_f32_16x16x32_bf16 a[0:3], a[228:231], v[216:219], a[0:3]\n"
    "s_waitcnt lgkmcnt(4)\n"
    "buffer_store_dwordx4 v[180:183], v244, s[24:27], s78 offen offset:0 sc1\n"
    "v_cvt_pk_bf16_f32 v190, v189, v193\n"
    "v_cvt_pk_bf16_f32 v194, v191, v195\n"
    "v_fma_f32 v200, v200, v109, v111\n"
    "v_fma_f32 v201, v201, v109, v111\n"
    "v_mfma_f32_16x16x32_bf16 a[0:3], a[112:115], v[12:15], a[0:3]\n"
    "v_cvt_pk_bf16_f32 v189, v196, v200\n"
    "v_cvt_pk_bf16_f32 v191, v197, v201\n"
    "v_mfma_f32_16x16x32_bf16 a[0:3], a[116:119], v[16:19], a[0:3]\n"
    "ds_read_b128 a[176:179], v249 offset:10240\n"
    "v_mfma_f32_16x16x32_bf16 a[4:7], a[232:235], v[212:215], a[4:7]\n"
    "s_waitcnt lgkmcnt(4)\n"
    "buffer_store_dwordx4 v[184:187], v244, s[24:27], s78 offen offset:3072 sc1\n"
    "ds_write_b128 v3, v[188:191] offset:2048\n"
    "v_mfma_f32_16x16x32_bf16 a[8:11], a[208:211], v[76:79], a[8:11]\n"
    "v_fma_f32 v202, v202, v109, v111\n"
    "v_fma_f32 v203, v203, v109, v111\n"
    "v_mfma_f32_16x16x32_bf16 a[12:15], a[216:219], v[76:79], a[12:15]\n"
    "v_cvt_pk_bf16_f32 v195, v199, v203\n"
    "v_cvt_pk_bf16_f32 v193, v198, v202\n"
    "v_mfma_f32_16x16x32_bf16 a[12:15], a[252:255], v[216:219], a[12:15]\n"
    "ds_write_b128 v2, v[192:195] offset:2048\n"
    "v_mfma_f32_16x16x32_bf16 a[4:7], a[124:127], v[16:19], a[4:7]\n"
    "v_accvgpr_read_b32 v40, a1\n"
    "v_mfma_f32_16x16x32_bf16 a[20:23], a[204:207], v[88:91], a[20:23]\n"
    "v_accvgpr_read_b32 v56, a3\n"
    "v_mov_b32_e64 v250, 0\n"
    "v_mfma_f32_16x16x32_bf16 a[4:7], a[120:123], v[12:15], a[4:7]\n"
    "ds_read_b128 v[200:203], v252 offset:3072\n"
    "ds_read_b128 v[196:199], v252 offset:2048\n"
    "v_mfma_f32_16x16x32_bf16 a[8:11], a[212:215], v[80:83], a[8:11]\n"
    "ds_read_b128 a[188:191], v249 offset:23552\n"
    "v_mfma_f32_16x16x32_bf16 a[8:11], a[240:243], v[212:215], a[8:11]\n"
    "v_accvgpr_read_b32 v36, a0\n"
    "v_accvgpr_read_b32 v52, a2\n"
    "v_mfma_f32_16x16x32_bf16 a[8:11], a[244:247], v[216:219], a[8:11]\n"
    "v_accvgpr_read_b32 v41, a5\n"
    "v_accvgpr_read_b32 v57, a7\n"
    "v_mfma_f32_16x16x32_bf16 a[8:11], a[128:131], v[12:15], a[8:11]\n"
    "v_accvgpr_read_b32 v37, a4\n"
    "v_accvgpr_read_b32 v53, a6\n"
    "v_mfma_f32_16x16x32_bf16 a[8:11], a[132:135], v[16:19], a[8:11]\n"
    "v_fma_f32 v40, v40, v113, v115\n"
    "v_fma_f32 v41, v41, v113, v115\n"
    "v_mfma_f32_16x16x32_bf16 a[12:15], a[248:251], v[212:215], a[12:15]\n"
    "ds_read_b128 a[160:163], v249 offset:8192\n"
    "v_mfma_f32_16x16x32_bf16 a[12:15], a[140:143], v[16:19], a[12:15]\n"
    "v_fma_f32 v52, v52, v113, v115\n"
    "v_fma_f32 v53, v53, v113, v115\n"
    "v_mfma_f32_16x16x32_bf16 a[12:15], a[136:139], v[12:15], a[12:15]\n"
    "s_waitcnt lgkmcnt(3)\n"
    "buffer_store_dwordx4 v[200:203], v244, s[24:27], s75 offen offset:3200 sc1\n"
    "v_cvt_pk_bf16_f32 v52, v52, v53\n"
    "v_fma_f32 v36, v36, v113, v115\n"
    "v_fma_f32 v37, v37, v113, v115\n"
    "v_accvgpr_read_b32 v38, a8\n"
    "v_mfma_f32_16x16x32_bf16 a[16:19], a[192:195], v[84:87], a[16:19]\n"
    "ds_read_b128 a[168:171], v249 offset:9216\n"
    "v_accvgpr_read_b32 v42, a9\n"
    "v_mfma_f32_16x16x32_bf16 a[16:19], a[196:199], v[88:91], a[16:19]\n"
    "v_accvgpr_read_b32 v54, a10\n"
    "v_accvgpr_read_b32 v39, a12\n"
    "v_mfma_f32_16x16x32_bf16 a[20:23], a[200:203], v[84:87], a[20:23]\n"
    "v_accvgpr_read_b32 v55, a14\n"
    "v_accvgpr_read_b32 v58, a11\n"
    "v_mfma_f32_16x16x32_bf16 a[16:19], a[224:227], v[220:223], a[16:19]\n"
    "v_accvgpr_read_b32 v59, a15\n"
    "v_cvt_pk_bf16_f32 v36, v36, v37\n"
    "v_mfma_f32_16x16x32_bf16 a[20:23], a[236:239], v[224:227], a[20:23]\n"
    "v_fma_f32 v38, v38, v113, v115\n"
    "v_fma_f32 v39, v39, v113, v115\n"
    "v_mfma_f32_16x16x32_bf16 a[24:27], a[208:211], v[84:87], a[24:27]\n"
    "v_fma_f32 v54, v54, v113, v115\n"
    "v_fma_f32 v55, v55, v113, v115\n"
    "v_mfma_f32_16x16x32_bf16 a[20:23], a[232:235], v[220:223], a[20:23]\n"
    "v_accvgpr_read_b32 v43, a13\n"
    "v_cvt_pk_bf16_f32 v53, v54, v55\n"
    "v_mfma_f32_16x16x32_bf16 a[20:23], a[124:127], v[24:27], a[20:23]\n"
    "v_fma_f32 v42, v42, v113, v115\n"
    "v_fma_f32 v43, v43, v113, v115\n"
    "v_mfma_f32_16x16x32_bf16 a[16:19], a[228:231], v[224:227], a[16:19]\n"
    "ds_read_b128 a[184:187], v249 offset:11264\n"
    "v_cvt_pk_bf16_f32 v37, v38, v39\n"
    "v_mfma_f32_16x16x32_bf16 a[20:23], a[120:123], v[20:23], a[20:23]\n"
    "v_cvt_pk_bf16_f32 v39, v42, v43\n"
    "v_cvt_pk_bf16_f32 v38, v40, v41\n"
    "v_mfma_f32_16x16x32_bf16 a[28:31], a[220:223], v[88:91], a[28:31]\n"
    "s_waitcnt lgkmcnt(4)\n"
    "buffer_store_dwordx4 v[196:199], v244, s[24:27], s75 offen offset:128 sc1\n"
    "ds_write_b128 v3, v[36:39] offset:4096\n"
    "v_mfma_f32_16x16x32_bf16 a[16:19], a[112:115], v[20:23], a[16:19]\n"
    "v_fma_f32 v58, v58, v113, v115\n"
    "v_fma_f32 v59, v59, v113, v115\n"
    "v_mfma_f32_16x16x32_bf16 a[16:19], a[116:119], v[24:27], a[16:19]\n"
    "v_fma_f32 v56, v56, v113, v115\n"
    "v_fma_f32 v57, v57, v113, v115\n"
    "v_cvt_pk_bf16_f32 v55, v58, v59\n"
    "v_mfma_f32_16x16x32_bf16 a[28:31], a[216:219], v[84:87], a[28:31]\n"
    "v_accvgpr_read_b32 v49, a21\n"
    "v_cvt_pk_bf16_f32 v54, v56, v57\n"
    "v_mfma_f32_16x16x32_bf16 a[24:27], a[212:215], v[88:91], a[24:27]\n"
    "ds_write_b128 v2, v[52:55] offset:4096\n"
    "v_mfma_f32_16x16x32_bf16 a[32:35], a[192:195], v[92:95], a[32:35]\n"
    "v_accvgpr_read_b32 v64, a19\n"
    "v_accvgpr_read_b32 v45, a20\n"
    "v_mfma_f32_16x16x32_bf16 a[24:27], a[240:243], v[220:223], a[24:27]\n"
    "v_accvgpr_read_b32 v44, a16\n"
    "v_accvgpr_read_b32 v65, a23\n"
    "v_mfma_f32_16x16x32_bf16 a[24:27], a[244:247], v[224:227], a[24:27]\n"
    "ds_read_b128 v[56:59], v252 offset:5120\n"
    "v_accvgpr_read_b32 v48, a17\n"
    "v_mfma_f32_16x16x32_bf16 a[28:31], a[252:255], v[224:227], a[28:31]\n"
    "v_fma_f32 v64, v64, v117, v119\n"
    "v_fma_f32 v65, v65, v117, v119\n"
    "v_mfma_f32_16x16x32_bf16 a[24:27], a[128:131], v[20:23], a[24:27]\n"
    "ds_read_b128 v[40:43], v252 offset:4096\n"
    "v_accvgpr_read_b32 v60, a18\n"
    "v_mfma_f32_16x16x32_bf16 a[24:27], a[132:135], v[24:27], a[24:27]\n"
    "v_fma_f32 v44, v44, v117, v119\n"
    "v_fma_f32 v45, v45, v117, v119\n"
    "v_mfma_f32_16x16x32_bf16 a[28:31], a[248:251], v[220:223], a[28:31]\n"
    "ds_read_b128 a[192:195], v248 offset:8192\n"
    "v_accvgpr_read_b32 v61, a22\n"
    "v_mfma_f32_16x16x32_bf16 a[28:31], a[140:143], v[24:27], a[28:31]\n"
    "v_fma_f32 v48, v48, v117, v119\n"
    "v_fma_f32 v49, v49, v117, v119\n"
    "v_mfma_f32_16x16x32_bf16 a[28:31], a[136:139], v[20:23], a[28:31]\n"
    "v_fma_f32 v60, v60, v117, v119\n"
    "v_fma_f32 v61, v61, v117, v119\n"
    "v_mfma_f32_16x16x32_bf16 a[40:43], a[208:211], v[92:95], a[40:43]\n"
    "v_accvgpr_read_b32 v62, a26\n"
    "v_accvgpr_read_b32 v66, a27\n"
    "v_mfma_f32_16x16x32_bf16 a[36:39], a[204:207], v[96:99], a[36:39]\n"
    "s_waitcnt lgkmcnt(2)\n"
    "buffer_store_dwordx4 v[56:59], v244, s[24:27], s76 offen offset:3200 sc1\n"
    "v_accvgpr_read_b32 v46, a24\n"
    "v_accvgpr_read_b32 v63, a30\n"
    "v_accvgpr_read_b32 v47, a28\n"
    "v_accvgpr_read_b32 v51, a29\n"
    "v_mfma_f32_16x16x32_bf16 a[40:43], a[212:215], v[96:99], a[40:43]\n"
    "v_fma_f32 v62, v62, v117, v119\n"
    "v_fma_f32 v63, v63, v117, v119\n"
    "v_mfma_f32_16x16x32_bf16 a[44:47], a[220:223], v[96:99], a[44:47]\n"
    "v_accvgpr_read_b32 v50, a25\n"
    "v_cvt_pk_bf16_f32 v44, v44, v45\n"
    "v_cvt_pk_bf16_f32 v60, v60, v61\n"
    "v_mfma_f32_16x16x32_bf16 a[36:39], a[200:203], v[92:95], a[36:39]\n"
    "v_fma_f32 v50, v50, v117, v119\n"
    "v_fma_f32 v51, v51, v117, v119\n"
    "v_mfma_f32_16x16x32_bf16 a[44:47], a[216:219], v[92:95], a[44:47]\n"
    "v_fma_f32 v46, v46, v117, v119\n"
    "v_fma_f32 v47, v47, v117, v119\n"
    "v_mfma_f32_16x16x32_bf16 a[36:39], a[236:239], v[232:235], a[36:39]\n"
    "v_cvt_pk_bf16_f32 v61, v62, v63\n"
    "v_cvt_pk_bf16_f32 v45, v46, v47\n"
    "v_mfma_f32_16x16x32_bf16 a[40:43], a[240:243], v[228:231], a[40:43]\n"
    "v_accvgpr_read_b32 v67, a31\n"
    "v_cvt_pk_bf16_f32 v47, v50, v51\n"
    "v_mfma_f32_16x16x32_bf16 a[36:39], a[232:235], v[228:231], a[36:39]\n"
    "ds_read_b128 a[208:211], v248 offset:10240\n"
    "v_cvt_pk_bf16_f32 v46, v48, v49\n"
    "v_mfma_f32_16x16x32_bf16 a[36:39], a[124:127], v[32:35], a[36:39]\n"
    "s_waitcnt lgkmcnt(2)\n"
    "buffer_store_dwordx4 v[40:43], v244, s[24:27], s76 offen offset:128 sc1\n"
    "ds_write_b128 v3, v[44:47] offset:0\n"
    "v_mfma_f32_16x16x32_bf16 a[36:39], a[120:123], v[28:31], a[36:39]\n"
    "v_fma_f32 v66, v66, v117, v119\n"
    "v_fma_f32 v67, v67, v117, v119\n"
    "v_mfma_f32_16x16x32_bf16 a[40:43], a[244:247], v[232:235], a[40:43]\n"
    "v_cvt_pk_bf16_f32 v62, v64, v65\n"
    "v_cvt_pk_bf16_f32 v63, v66, v67\n"
    "v_mfma_f32_16x16x32_bf16 a[44:47], a[252:255], v[232:235], a[44:47]\n"
    "ds_write_b128 v2, v[60:63] offset:0\n"
    "v_mfma_f32_16x16x32_bf16 a[32:35], a[196:199], v[96:99], a[32:35]\n"
    "v_accvgpr_read_b32 v125, a36\n"
    "v_fma_f32 v250, |v110|, 4.0, v250\n"
    "v_mfma_f32_16x16x32_bf16 a[44:47], a[248:251], v[228:231], a[44:47]\n"
    "v_fma_f32 v250, v108, 4.0, v250\n"
    "v_accvgpr_read_b32 v137, a39\n"
    "v_mfma_f32_16x16x32_bf16 a[32:35], a[224:227], v[228:231], a[32:35]\n"
    "ds_read_b128 a[236:239], v249 offset:46080\n"
    "ds_read_b128 v[48:51], v252 offset:0\n"
    "ds_read_b128 a[212:215], v248 offset:22528\n"
    "v_mfma_f32_16x16x32_bf16 a[32:35], a[228:231], v[232:235], a[32:35]\n"
    "ds_read_b128 a[224:227], v249 offset:32768\n"
    "v_accvgpr_read_b32 v133, a38\n"
    "v_mfma_f32_16x16x32_bf16 a[40:43], a[128:131], v[28:31], a[40:43]\n"
    "ds_read_b128 a[204:207], v248 offset:21504\n"
    "v_mfma_f32_16x16x32_bf16 a[44:47], a[140:143], v[32:35], a[44:47]\n"
    "ds_read_b128 v[64:67], v252 offset:1024\n"
    "ds_read_b128 a[240:243], v249 offset:34816\n"
    "v_mfma_f32_16x16x32_bf16 a[32:35], a[112:115], v[28:31], a[32:35]\n"
    "ds_read_b128 a[216:219], v248 offset:11264\n"
    "ds_read_b128 a[196:199], v248 offset:20480\n"
    "v_mfma_f32_16x16x32_bf16 a[32:35], a[116:119], v[32:35], a[32:35]\n"
    "s_waitcnt lgkmcnt(12)\n"
    "ds_read_b128 a[244:247], v249 offset:47104\n"
    "ds_read_b128 a[200:203], v248 offset:9216\n"
    "v_mfma_f32_16x16x32_bf16 a[48:51], a[192:195], v[68:71], a[48:51]\n"
    "ds_read_b128 a[232:235], v249 offset:33792\n"
    "ds_read_b128 a[252:255], v249 offset:48128\n"
    "v_mfma_f32_16x16x32_bf16 a[40:43], a[132:135], v[32:35], a[40:43]\n"
    "s_nop 0\n"
    "v_accvgpr_read_b32 v124, a32\n"
    "v_mfma_f32_16x16x32_bf16 a[44:47], a[136:139], v[28:31], a[44:47]\n"
    "s_waitcnt lgkmcnt(11)\n"
    "buffer_store_dwordx4 v[48:51], v244, s[24:27], s77 offen offset:128 sc1\n"
    "v_accvgpr_read_b32 v128, a33\n"
    "v_accvgpr_read_b32 v132, a34\n"
    "v_fma_f32 v132, v132, v121, v123\n"
    "v_fma_f32 v133, v133, v121, v123\n"
    "v_accvgpr_read_b32 v134, a42\n"
    "v_mfma_f32_16x16x32_bf16 a[56:59], a[208:211], v[68:71], a[56:59]\n"
    "s_waitcnt lgkmcnt(8)\n"
    "v_accvgpr_read_b32 v129, a37\n"
    "v_accvgpr_read_b32 v131, a45\n"
    "v_mfma_f32_16x16x32_bf16 a[52:55], a[204:207], v[72:75], a[52:55]\n"
    "v_accvgpr_read_b32 v130, a41\n"
    "v_accvgpr_read_b32 v138, a43\n"
    "v_mfma_f32_16x16x32_bf16 a[56:59], a[212:215], v[72:75], a[56:59]\n"
    "s_waitcnt lgkmcnt(4)\n"
    "v_accvgpr_read_b32 v135, a46\n"
    "v_fma_f32 v124, v124, v121, v123\n"
    "v_fma_f32 v125, v125, v121, v123\n"
    "v_mfma_f32_16x16x32_bf16 a[48:51], a[196:199], v[72:75], a[48:51]\n"
    "s_waitcnt lgkmcnt(2)\n"
    "ds_read_b128 a[228:231], v249 offset:45056\n"
    "ds_read_b128 a[248:251], v249 offset:35840\n"
    "v_mfma_f32_16x16x32_bf16 a[52:55], a[200:203], v[68:71], a[52:55]\n"
    ".Lwp2:\n"
    "s_cbranch_execz .Lw3\n"
    "v_fma_f32 v130, v130, v121, v123\n"
    "v_fma_f32 v131, v131, v121, v123\n"
    "v_mfma_f32_16x16x32_bf16 a[56:59], a[240:243], v[204:207], a[56:59]\n"
    "v_accvgpr_read_b32 v126, a40\n"
    "v_fma_f32 v134, v134, v121, v123\n"
    "v_fma_f32 v135, v135, v121, v123\n"
    "v_mfma_f32_16x16x32_bf16 a[48:51], a[224:227], v[204:207], a[48:51]\n"
    "ds_read_b128 a[220:223], v248 offset:23552\n"
    "v_accvgpr_read_b32 v139, a47\n"
    "v_mfma_f32_16x16x32_bf16 a[56:59], a[244:247], v[208:211], a[56:59]\n"
    "buffer_store_dwordx4 v[64:67], v244, s[24:27], s77 offen offset:3200 sc1\n"
    "v_accvgpr_read_b32 v136, a35\n"
    "v_accvgpr_read_b32 v127, a44\n"
    "v_fma_f32 v138, v138, v121, v123\n"
    "v_fma_f32 v139, v139, v121, v123\n"
    "v_mfma_f32_16x16x32_bf16 a[56:59], a[176:179], v[4:7], a[56:59]\n"
    "v_fma_f32 v126, v126, v121, v123\n"
    "v_fma_f32 v127, v127, v121, v123\n"
    "v_mfma_f32_16x16x32_bf16 a[56:59], a[180:183], v[8:11], a[56:59]\n"
    "v_cvt_pk_bf16_f32 v124, v124, v125\n"
    "v_cvt_pk_bf16_f32 v125, v126, v127\n"
    "v_mfma_f32_16x16x32_bf16 a[52:55], a[236:239], v[208:211], a[52:55]\n"
    "s_waitcnt lgkmcnt(2)\n"
    "v_fma_f32 v128, v128, v121, v123\n"
    "v_fma_f32 v129, v129, v121, v123\n"
    "v_mfma_f32_16x16x32_bf16 a[48:51], a[228:231], v[208:211], a[48:51]\n"
    "v_cvt_pk_bf16_f32 v132, v132, v133\n"
    "v_cvt_pk_bf16_f32 v126, v128, v129\n"
    "v_mfma_f32_16x16x32_bf16 a[52:55], a[232:235], v[204:207], a[52:55]\n"
    "v_cvt_pk_bf16_f32 v133, v134, v135\n"
    "v_cvt_pk_bf16_f32 v127, v130, v131\n"
    "v_mfma_f32_16x16x32_bf16 a[52:55], a[172:175], v[8:11], a[52:55]\n"
    "s_waitcnt lgkmcnt(0)\n"
    "ds_write_b128 v3, v[124:127] offset:2048\n"
    "v_mfma_f32_16x16x32_bf16 a[60:63], a[220:223], v[72:75], a[60:63]\n"
    "v_fma_f32 v136, v136, v121, v123\n"
    "v_fma_f32 v137, v137, v121, v123\n"
    "v_mfma_f32_16x16x32_bf16 a[52:55], a[168:171], v[4:7], a[52:55]\n"
    "v_cvt_pk_bf16_f32 v135, v138, v139\n"
    "v_cvt_pk_bf16_f32 v134, v136, v137\n"
    "v_mfma_f32_16x16x32_bf16 a[60:63], a[216:219], v[68:71], a[60:63]\n"
    "ds_write_b128 v2, v[132:135] offset:2048\n"
    "v_mfma_f32_16x16x32_bf16 a[48:51], a[160:163], v[4:7], a[48:51]\n"
    "v_accvgpr_read_b32 v146, a57\n"
    "v_mfma_f32_16x16x32_bf16 a[48:51], a[164:167], v[8:11], a[48:51]\n"
    "ds_read_b128 v[128:131], v252 offset:2048\n"
    "v_mfma_f32_16x16x32_bf16 a[60:63], a[252:255], v[208:211], a[60:63]\n"
    "v_accvgpr_read_b32 v141, a52\n"
    "v_accvgpr_read_b32 v153, a55\n"
    "v_mfma_f32_16x16x32_bf16 a[64:67], a[192:195], v[76:79], a[64:67]\n"
    "v_accvgpr_read_b32 v149, a54\n"
    "v_accvgpr_read_b32 v150, a58\n"
    "v_mfma_f32_16x16x32_bf16 a[60:63], a[248:251], v[204:207], a[60:63]\n"
    "ds_read_b128 v[136:139], v252 offset:3072\n"
    "v_accvgpr_read_b32 v142, a56\n"
    "v_mfma_f32_16x16x32_bf16 a[60:63], a[188:191], v[8:11], a[60:63]\n"
    "v_accvgpr_read_b32 v145, a53\n"
    "v_accvgpr_read_b32 v148, a50\n"
    "v_mfma_f32_16x16x32_bf16 a[60:63], a[184:187], v[4:7], a[60:63]\n"
    "v_accvgpr_read_b32 v154, a59\n"
    "v_accvgpr_read_b32 v140, a48\n"
    "v_mfma_f32_16x16x32_bf16 a[64:67], a[196:199], v[80:83], a[64:67]\n"
    "v_fma_f32 v140, v140, v109, v111\n"
    "v_fma_f32 v141, v141, v109, v111\n"
    "v_mfma_f32_16x16x32_bf16 a[68:71], a[204:207], v[80:83], a[68:71]\n"
    "s_nop 1\n"
    "v_accvgpr_read_b32 v143, a60\n"
    "v_mfma_f32_16x16x32_bf16 a[68:71], a[200:203], v[76:79], a[68:71]\n"
    "s_waitcnt lgkmcnt(1)\n"
    "buffer_store_dwordx4 v[128:131], v244, s[24:27], s78 offen offset:128 sc1\n"
    "v_fma_f32 v148, v148, v109, v111\n"
    "v_fma_f32 v149, v149, v109, v111\n"
    "v_accvgpr_read_b32 v144, a49\n"
    "v_cvt_pk_bf16_f32 v148, v148, v149\n"
    "v_mfma_f32_16x16x32_bf16 a[76:79], a[220:223], v[80:83], a[76:79]\n"
    "v_cvt_pk_bf16_f32 v140, v140, v141\n"
    "v_accvgpr_read_b32 v152, a51\n"
    "v_mfma_f32_16x16x32_bf16 a[76:79], a[216:219], v[76:79], a[76:79]\n"
    "v_fma_f32 v142, v142, v109, v111\n"
    "v_fma_f32 v143, v143, v109, v111\n"
    "v_mfma_f32_16x16x32_bf16 a[64:67], a[224:227], v[212:215], a[64:67]\n"
    "v_fma_f32 v144, v144, v109, v111\n"
    "v_fma_f32 v145, v145, v109, v111\n"
    "v_accvgpr_read_b32 v151, a62\n"
    "v_mfma_f32_16x16x32_bf16 a[68:71], a[236:239], v[216:219], a[68:71]\n"
    "v_accvgpr_read_b32 v155, a63\n"
    "s_nop 0\n"
    "v_mfma_f32_16x16x32_bf16 a[68:71], a[232:235], v[212:215], a[68:71]\n"
    "s_waitcnt lgkmcnt(0)\n"
    "buffer_store_dwordx4 v[136:139], v244, s[24:27], s78 offen offset:3200 sc1\n"
    "v_fma_f32 v150, v150, v109, v111\n"
    "v_fma_f32 v151, v151, v109, v111\n"
    "v_cvt_pk_bf16_f32 v141, v142, v143\n"
    "v_accvgpr_read_b32 v147, a61\n"
    "v_mfma_f32_16x16x32_bf16 a[72:75], a[208:211], v[76:79], a[72:75]\n"
    "v_fma_f32 v146, v146, v109, v111\n"
    "v_fma_f32 v147, v147, v109, v111\n"
    "v_mfma_f32_16x16x32_bf16 a[64:67], a[228:231], v[216:219], a[64:67]\n"
    "v_mfma_f32_16x16x32_bf16 a[68:71], a[172:175], v[16:19], a[68:71]\n"
    "v_fma_f32 v154, v154, v109, v111\n"
    "v_fma_f32 v155, v155, v109, v111\n"
    "v_mfma_f32_16x16x32_bf16 a[64:67], a[160:163], v[12:15], a[64:67]\n"
    "v_cvt_pk_bf16_f32 v149, v150, v151\n"
    "v_cvt_pk_bf16_f32 v142, v144, v145\n"
    "v_cvt_pk_bf16_f32 v143, v146, v147\n"
    "v_mfma_f32_16x16x32_bf16 a[68:71], a[168:171], v[12:15], a[68:71]\n"
    "ds_write_b128 v3, v[140:143] offset:4096\n"
    "v_mfma_f32_16x16x32_bf16 a[72:75], a[212:215], v[80:83], a[72:75]\n"
    "v_fma_f32 v152, v152, v109, v111\n"
    "v_fma_f32 v153, v153, v109, v111\n"
    "v_mfma_f32_16x16x32_bf16 a[72:75], a[240:243], v[212:215], a[72:75]\n"
    "v_cvt_pk_bf16_f32 v150, v152, v153\n"
    "v_cvt_pk_bf16_f32 v151, v154, v155\n"
    "v_mfma_f32_16x16x32_bf16 a[92:95], a[220:223], v[88:91], a[92:95]\n"
    "ds_write_b128 v2, v[148:151] offset:4096\n"
    "v_mfma_f32_16x16x32_bf16 a[92:95], a[216:219], v[84:87], a[92:95]\n"
    "v_accvgpr_read_b32 v161, a69\n"
    "v_fma_f32 v250, v109, s74, v250\n"
    "v_mfma_f32_16x16x32_bf16 a[64:67], a[164:167], v[16:19], a[64:67]\n"
    "v_fma_f32 v250, |v114|, 4.0, v250\n"
    "v_mfma_f32_16x16x32_bf16 a[72:75], a[244:247], v[216:219], a[72:75]\n"
    "ds_read_b128 v[144:147], v252 offset:4096\n"
    "v_mfma_f32_16x16x32_bf16 a[72:75], a[176:179], v[12:15], a[72:75]\n"
    "ds_read_b128 v[152:155], v252 offset:5120\n"
    "v_accvgpr_read_b32 v165, a70\n"
    "v_mfma_f32_16x16x32_bf16 a[72:75], a[180:183], v[16:19], a[72:75]\n"
    "s_nop 0\n"
    "v_accvgpr_read_b32 v156, a64\n"
    "v_accvgpr_read_b32 v160, a65\n"
    "v_mfma_f32_16x16x32_bf16 a[76:79], a[252:255], v[216:219], a[76:79]\n"
    "v_fma_f32 v160, v160, v113, v115\n"
    "v_fma_f32 v161, v161, v113, v115\n"
    "v_mfma_f32_16x16x32_bf16 a[84:87], a[204:207], v[88:91], a[84:87]\n"
    "s_nop 0\n"
    "v_accvgpr_read_b32 v162, a73\n"
    "v_accvgpr_read_b32 v169, a71\n"
    "v_mfma_f32_16x16x32_bf16 a[80:83], a[192:195], v[84:87], a[80:83]\n"
    "v_fma_f32 v250, v112, 4.0, v250\n"
    "s_nop 0\n"
    "v_mfma_f32_16x16x32_bf16 a[76:79], a[248:251], v[212:215], a[76:79]\n"
    "v_accvgpr_read_b32 v166, a74\n"
    "v_accvgpr_read_b32 v157, a68\n"
    "v_mfma_f32_16x16x32_bf16 a[76:79], a[188:191], v[16:19], a[76:79]\n"
    "v_accvgpr_read_b32 v164, a66\n"
    "v_accvgpr_read_b32 v168, a67\n"
    "v_mfma_f32_16x16x32_bf16 a[76:79], a[184:187], v[12:15], a[76:79]\n"
    "s_waitcnt lgkmcnt(1)\n"
    "buffer_store_dwordx4 v[144:147], v244, s[24:27], s75 offen offset:256 sc1\n"
    "v_fma_f32 v164, v164, v113, v115\n"
    "v_fma_f32 v165, v165, v113, v115\n"
    "v_accvgpr_read_b32 v158, a72\n"
    "v_fma_f32 v156, v156, v113, v115\n"
    "v_fma_f32 v157, v157, v113, v115\n"
    "v_mfma_f32_16x16x32_bf16 a[80:83], a[196:199], v[88:91], a[80:83]\n"
    "v_accvgpr_read_b32 v167, a78\n"
    "v_cvt_pk_bf16_f32 v156, v156, v157\n"
    "v_fma_f32 v250, v113, s74, v250\n"
    "v_mfma_f32_16x16x32_bf16 a[88:91], a[208:211], v[84:87], a[88:91]\n"
    "s_nop 0\n"
    "v_accvgpr_read_b32 v159, a76\n"
    "v_fma_f32 v166, v166, v113, v115\n"
    "v_fma_f32 v167, v167, v113, v115\n"
    "v_mfma_f32_16x16x32_bf16 a[84:87], a[200:203], v[84:87], a[84:87]\n"
    "s_waitcnt lgkmcnt(0)\n"
    "buffer_store_dwordx4 v[152:155], v244, s[24:27], s75 offen offset:3328 sc1\n"
    "v_fma_f32 v158, v158, v113, v115\n"
    "v_fma_f32 v159, v159, v113, v115\n"
    "v_accvgpr_read_b32 v170, a75\n"
    "v_accvgpr_read_b32 v171, a79\n"
    "v_mfma_f32_16x16x32_bf16 a[84:87], a[236:239], v[224:227], a[84:87]\n"
    "v_cvt_pk_bf16_f32 v157, v158, v159\n"
    "v_cvt_pk_bf16_f32 v158, v160, v161\n"
    "v_mfma_f32_16x16x32_bf16 a[84:87], a[232:235], v[220:223], a[84:87]\n"
    "v_cvt_pk_bf16_f32 v164, v164, v165\n"
    "v_accvgpr_read_b32 v163, a77\n"
    "v_mfma_f32_16x16x32_bf16 a[80:83], a[224:227], v[220:223], a[80:83]\n"
    "v_fma_f32 v162, v162, v113, v115\n"
    "v_fma_f32 v163, v163, v113, v115\n"
    "v_mfma_f32_16x16x32_bf16 a[88:91], a[212:215], v[88:91], a[88:91]\n"
    "v_cvt_pk_bf16_f32 v159, v162, v163\n"
    "v_cvt_pk_bf16_f32 v165, v166, v167\n"
    "v_mfma_f32_16x16x32_bf16 a[84:87], a[172:175], v[24:27], a[84:87]\n"
    "ds_write_b128 v3, v[156:159] offset:0\n"
    "v_mfma_f32_16x16x32_bf16 a[84:87], a[168:171], v[20:23], a[84:87]\n"
    "v_fma_f32 v168, v168, v113, v115\n"
    "v_fma_f32 v169, v169, v113, v115\n"
    "v_mfma_f32_16x16x32_bf16 a[92:95], a[252:255], v[224:227], a[92:95]\n"
    "v_fma_f32 v170, v170, v113, v115\n"
    "v_fma_f32 v171, v171, v113, v115\n"
    "v_fma_f32 v250, |v118|, 4.0, v250\n"
    "v_cvt_pk_bf16_f32 v167, v170, v171\n"
    "v_cvt_pk_bf16_f32 v166, v168, v169\n"
    "v_mfma_f32_16x16x32_bf16 a[80:83], a[228:231], v[224:227], a[80:83]\n"
    "ds_write_b128 v2, v[164:167] offset:0\n"
    "v_mfma_f32_16x16x32_bf16 a[80:83], a[160:163], v[20:23], a[80:83]\n"
    "v_mfma_f32_16x16x32_bf16 a[92:95], a[248:251], v[220:223], a[92:95]\n"
    "ds_read_b128 v[160:163], v252 offset:0\n"
    "v_mfma_f32_16x16x32_bf16 a[80:83], a[164:167], v[24:27], a[80:83]\n"
    "ds_read_b128 v[168:171], v252 offset:1024\n"
    "v_accvgpr_read_b32 v173, a84\n"
    "v_mfma_f32_16x16x32_bf16 a[92:95], a[188:191], v[24:27], a[92:95]\n"
    "v_accvgpr_read_b32 v177, a85\n"
    "v_mfma_f32_16x16x32_bf16 a[88:91], a[240:243], v[220:223], a[88:91]\n"
    "v_mfma_f32_16x16x32_bf16 a[88:91], a[244:247], v[224:227], a[88:91]\n"
    "s_nop 1\n"
    "v_accvgpr_read_b32 v176, a81\n"
    "v_mfma_f32_16x16x32_bf16 a[92:95], a[184:187], v[20:23], a[92:95]\n"
    "v_accvgpr_read_b32 v172, a80\n"
    "v_accvgpr_read_b32 v184, a83\n"
    "v_mfma_f32_16x16x32_bf16 a[88:91], a[176:179], v[20:23], a[88:91]\n"
    "v_fma_f32 v172, v172, v117, v119\n"
    "v_fma_f32 v173, v173, v117, v119\n"
    "v_mfma_f32_16x16x32_bf16 a[88:91], a[180:183], v[24:27], a[88:91]\n"
    "v_fma_f32 v176, v176, v117, v119\n"
    "v_fma_f32 v177, v177, v117, v119\n"
    "v_mfma_f32_16x16x32_bf16 a[96:99], a[192:195], v[92:95], a[96:99]\n"
    "s_waitcnt lgkmcnt(1)\n"
    "buffer_store_dwordx4 v[160:163], v244, s[24:27], s76 offen offset:256 sc1\n"
    "v_accvgpr_read_b32 v175, a92\n"
    "v_accvgpr_read_b32 v180, a82\n"
    "v_accvgpr_read_b32 v181, a86\n"
    "v_cvt_pk_bf16_f32 v172, v172, v173\n"
    "v_mfma_f32_16x16x32_bf16 a[108:111], a[220:223], v[96:99], a[108:111]\n"
    "v_accvgpr_read_b32 v179, a93\n"
    "v_accvgpr_read_b32 v174, a88\n"
    "v_mfma_f32_16x16x32_bf16 a[108:111], a[216:219], v[92:95], a[108:111]\n"
    "v_fma_f32 v250, v116, 4.0, v250\n"
    "v_accvgpr_read_b32 v183, a94\n"
    "v_mfma_f32_16x16x32_bf16 a[96:99], a[196:199], v[96:99], a[96:99]\n"
    "v_accvgpr_read_b32 v187, a95\n"
    "v_accvgpr_read_b32 v178, a89\n"
    "v_mfma_f32_16x16x32_bf16 a[100:103], a[204:207], v[96:99], a[100:103]\n"
    "v_mfma_f32_16x16x32_bf16 a[104:107], a[208:211], v[92:95], a[104:107]\n"
    "s_nop 0\n"
    "v_fma_f32 v174, v174, v117, v119\n"
    "v_fma_f32 v175, v175, v117, v119\n"
    "v_mfma_f32_16x16x32_bf16 a[108:111], a[252:255], v[232:235], a[108:111]\n"
    "s_waitcnt lgkmcnt(0)\n"
    "buffer_store_dwordx4 v[168:171], v244, s[24:27], s76 offen offset:3328 sc1\n"
    "v_cvt_pk_bf16_f32 v173, v174, v175\n"
    "v_accvgpr_read_b32 v182, a90\n"
    "v_accvgpr_read_b32 v185, a87\n"
    "v_cvt_pk_bf16_f32 v174, v176, v177\n"
    "v_pk_fma_f32 v[178:179], v[178:179], v[116:117], v[118:119] op_sel:[0,1,1] op_sel_hi:[1,1,1]\n"
    "v_accvgpr_read_b32 v186, a91\n"
    "v_cvt_pk_bf16_f32 v175, v178, v179\n"
    "v_fma_f32 v186, v186, v117, v119\n"
    "v_fma_f32 v187, v187, v117, v119\n"
    "v_pk_fma_f32 v[180:181], v[180:181], v[116:117], v[118:119] op_sel:[0,1,1] op_sel_hi:[1,1,1]\n"
    "v_cmp_eq_u64 s[56:57], v[236:237], v[238:239]\n"
    "v_fma_f32 v182, v182, v117, v119\n"
    "v_fma_f32 v183, v183, v117, v119\n"
    "v_mfma_f32_16x16x32_bf16 a[108:111], a[248:251], v[228:231], a[108:111]\n"
    "ds_write_b128 v3, v[172:175] offset:2048\n"
    "v_cvt_pk_bf16_f32 v180, v180, v181\n"
    "v_mfma_f32_16x16x32_bf16 a[96:99], a[224:227], v[228:231], a[96:99]\n"
    "v_fma_f32 v184, v184, v117, v119\n"
    "v_fma_f32 v185, v185, v117, v119\n"
    "v_cvt_pk_bf16_f32 v181, v182, v183\n"
    "v_mfma_f32_16x16x32_bf16 a[104:107], a[212:215], v[96:99], a[104:107]\n"
    "v_cvt_pk_bf16_f32 v182, v184, v185\n"
    "v_cvt_pk_bf16_f32 v183, v186, v187\n"
    "v_mfma_f32_16x16x32_bf16 a[96:99], a[228:231], v[232:235], a[96:99]\n"
    "ds_write_b128 v2, v[180:183] offset:2048\n"
    "v_mfma_f32_16x16x32_bf16 a[96:99], a[160:163], v[28:31], a[96:99]\n"
    "v_mfma_f32_16x16x32_bf16 a[104:107], a[240:243], v[228:231], a[104:107]\n"
    "ds_read_b128 v[176:179], v252 offset:2048\n"
    "v_mfma_f32_16x16x32_bf16 a[104:107], a[244:247], v[232:235], a[104:107]\n"
    "v_mfma_f32_16x16x32_bf16 a[100:103], a[200:203], v[92:95], a[100:103]\n"
    "v_mfma_f32_16x16x32_bf16 a[104:107], a[176:179], v[28:31], a[104:107]\n"
    "v_mfma_f32_16x16x32_bf16 a[104:107], a[180:183], v[32:35], a[104:107]\n"
    "v_mfma_f32_16x16x32_bf16 a[108:111], a[188:191], v[32:35], a[108:111]\n"
    "s_or_b64 s[56:57], s[56:57], s[34:35]\n"
    "ds_read_b128 v[184:187], v252 offset:3072\n"
    "v_mfma_f32_16x16x32_bf16 a[100:103], a[236:239], v[232:235], a[100:103]\n"
    "v_mfma_f32_16x16x32_bf16 a[96:99], a[164:167], v[32:35], a[96:99]\n"
    "v_mfma_f32_16x16x32_bf16 a[100:103], a[232:235], v[228:231], a[100:103]\n"
    "s_waitcnt lgkmcnt(1)\n"
    "buffer_store_dwordx4 v[176:179], v244, s[24:27], s77 offen offset:256 sc1\n"
    "v_accvgpr_read_b32 v198, a106\n"
    "v_fma_f32 v250, v117, s74, v250\n"
    "v_fma_f32 v250, |v122|, 4.0, v250\n"
    "v_accvgpr_read_b32 v194, a105\n"
    "s_nop 0\n"
    "v_accvgpr_read_b32 v196, a98\n"
    "v_mfma_f32_16x16x32_bf16 a[108:111], a[184:187], v[28:31], a[108:111]\n"
    "v_accvgpr_read_b32 v202, a107\n"
    "v_mfma_f32_16x16x32_bf16 a[100:103], a[172:175], v[32:35], a[100:103]\n"
    "s_nop 0\n"
    "v_mfma_f32_16x16x32_bf16 a[100:103], a[168:171], v[28:31], a[100:103]\n"
    "s_waitcnt lgkmcnt(0)\n"
    "buffer_store_dwordx4 v[184:187], v244, s[24:27], s77 offen offset:3328 sc1\n"
    "v_accvgpr_read_b32 v190, a104\n"
    "s_nop 1\n"
    "v_accvgpr_read_b32 v191, a108\n"
    "v_accvgpr_read_b32 v199, a110\n"
    "v_fma_f32 v250, v120, 4.0, v250\n"
    "v_accvgpr_read_b32 v197, a102\n"
    "v_accvgpr_read_b32 v200, a99\n"
    "v_accvgpr_read_b32 v195, a109\n"
    "v_accvgpr_read_b32 v188, a96\n"
    "v_pk_fma_f32 v[190:191], v[190:191], v[120:121], v[122:123] op_sel:[0,1,1] op_sel_hi:[1,1,1]\n"
    "v_accvgpr_read_b32 v201, a103\n"
    "v_accvgpr_read_b32 v203, a111\n"
    "v_accvgpr_read_b32 v189, a100\n"
    "v_pk_fma_f32 v[194:195], v[194:195], v[120:121], v[122:123] op_sel:[0,1,1] op_sel_hi:[1,1,1]\n"
    "v_fma_f32 v198, v198, v121, v123\n"
    "v_fma_f32 v199, v199, v121, v123\n"
    "v_fma_f32 v188, v188, v121, v123\n"
    "v_fma_f32 v189, v189, v121, v123\n"
    "v_accvgpr_read_b32 v192, a97\n"
    "v_cvt_pk_bf16_f32 v188, v188, v189\n"
    "v_accvgpr_read_b32 v193, a101\n"
    "v_cvt_pk_bf16_f32 v189, v190, v191\n"
    "v_cvt_pk_bf16_f32 v191, v194, v195\n"
    "v_fma_f32 v192, v192, v121, v123\n"
    "v_fma_f32 v193, v193, v121, v123\n"
    "v_cvt_pk_bf16_f32 v190, v192, v193\n"
    "ds_write_b128 v3, v[188:191] offset:4096\n"
    "v_pk_fma_f32 v[196:197], v[196:197], v[120:121], v[122:123] op_sel:[0,1,1] op_sel_hi:[1,1,1]\n"
    "v_cvt_pk_bf16_f32 v196, v196, v197\n"
    "v_cvt_pk_bf16_f32 v197, v198, v199\n"
    "v_pk_fma_f32 v[200:201], v[200:201], v[120:121], v[122:123] op_sel:[0,1,1] op_sel_hi:[1,1,1]\n"
    "v_fma_f32 v250, v121, s74, v250\n"
    "v_pk_fma_f32 v[202:203], v[202:203], v[120:121], v[122:123] op_sel:[0,1,1] op_sel_hi:[1,1,1]\n"
    "v_cvt_pk_bf16_f32 v199, v202, v203\n"
    "v_cvt_pk_bf16_f32 v198, v200, v201\n"
    "ds_write_b128 v2, v[196:199] offset:4096\n"
    "v_cmp_nlt_f32 s[54:55], |v250|, s73\n"
    "ds_read_b128 v[200:203], v252 offset:5120\n"
    "ds_read_b128 v[192:195], v252 offset:4096\n"
    "s_waitcnt lgkmcnt(1)\n"
    "buffer_store_dwordx4 v[200:203], v244, s[24:27], s78 offen offset:3328 sc1\n"
    "s_or_b64 s[54:55], s[54:55], s[56:57]\n"
    "s_waitcnt lgkmcnt(0)\n"
    "buffer_store_dwordx4 v[192:195], v244, s[24:27], s78 offen offset:256 sc1\n"
    "s_or_b32 %0, s54, s55\n"
    "s_cmp_eq_u32 %0, 0\n"
    "s_cbranch_scc1 .Lfin\n"
    "s_waitcnt vmcnt(0)\n"
    ".Lfin:\n"
    ".Lend:\n"
    ".Lend2:\n"
    : "=&s"(bad), "+s"(ka) : "s"(b), "v"(tid)
    : "memory", "vcc", "s16", "s17", "s18", "s19", "s20", "s21", "s22", "s23", "s24", "s25", "s26", "s27", "s28", "s29", "s30", "s31", "s34", "s35", "s36", "s37", "s38", "s39", "s40", "s41", "s42", "s43", "s44", "s45", "s46", "s47", "s48", "s49", "s50", "s51", "s52", "s53", "s54", "s55", "s56", "s57", "s58", "s59", "s60", "s61", "s62", "s63", "s64", "s65", "s66", "s67", "s68", "s69", "s70", "s71", "s72", "s73", "s74", "s75", "s76", "s77", "s78", "s79", "s80", "s81", "s82", "s83", "s84", "s85", "s86", "s87", "s88", "s89", "s90", "s91", "s92", "s93", "s94", "s95", "s96", "s97", "s98", "s99", "v1", "v2", "v3", "v4", "v5", "v6", "v7", "v8", "v9", "v10", "v11", "v12", "v13", "v14", "v15", "v16", "v17", "v18", "v19", "v20", "v21", "v22", "v23", "v24", "v25", "v26", "v27", "v28", "v29", "v30", "v31", "v32", "v33", "v34", "v35", "v36", "v37", "v38", "v39", "v40", "v41", "v42", "v43", "v44", "v45", "v46", "v47", "v48", "v49", "v50", "v51", "v52", "v53", "v54", "v55", "v56", "v57", "v58", "v59", "v60", "v61", "v62", "v63", "v64", "v65", "v66", "v67", "v68", "v69", "v70", "v71", "v72", "v73", "v74", "v75", "v76", "v77", "v78", "v79", "v80", "v81", "v82", "v83", "v84", "v85", "v86", "v87", "v88", "v89", "v90", "v91", "v92", "v93", "v94", "v95", "v96", "v97", "v98", "v99", "v100", "v101", "v102", "v103", "v104", "v105", "v106", "v107", "v108", "v109", "v110", "v111", "v112", "v113", "v114", "v115", "v116", "v117", "v118", "v119", "v120", "v121", "v122", "v123", "v124", "v125", "v126", "v127", "v128", "v129", "v130", "v131", "v132", "v133", "v134", "v135", "v136", "v137", "v138", "v139", "v140", "v141", "v142", "v143", "v144", "v145", "v146", "v147", "v148", "v149", "v150", "v151", "v152", "v153", "v154", "v155", "v156", "v157", "v158", "v159", "v160", "v161", "v162", "v163", "v164", "v165", "v166", "v167", "v168", "v169", "v170", "v171", "v172", "v173", "v174", "v175", "v176", "v177", "v178", "v179", "v180", "v181", "v182", "v183", "v184", "v185", "v186", "v187", "v188", "v189", "v190", "v191", "v192", "v193", "v194", "v195", "v196", "v197", "v198", "v199", "v200", "v201", "v202", "v203", "v204", "v205", "v206", "v207", "v208", "v209", "v210", "v211", "v212", "v213", "v214", "v215", "v216", "v217", "v218", "v219", "v220", "v221", "v222", "v223", "v224", "v225", "v226", "v227", "v228", "v229", "v230", "v231", "v232", "v233", "v234", "v235", "v236", "v237", "v238", "v239", "v240", "v241", "v242", "v243", "v244", "v245", "v246", "v247", "v248", "v249", "v250", "v251", "v252", "v253", "v254", "v255", "a0", "a1", "a2", "a3", "a4", "a5", "a6", "a7", "a8", "a9", "a10", "a11", "a12", "a13", "a14", "a15", "a16", "a17", "a18", "a19", "a20", "a21", "a22", "a23", "a24", "a25", "a26", "a27", "a28", "a29", "a30", "a31", "a32", "a33", "a34", "a35", "a36", "a37", "a38", "a39", "a40", "a41", "a42", "a43", "a44", "a45", "a46", "a47", "a48", "a49", "a50", "a51", "a52", "a53", "a54", "a55", "a56", "a57", "a58", "a59", "a60", "a61", "a62", "a63", "a64", "a65", "a66", "a67", "a68", "a69", "a70", "a71", "a72", "a73", "a74", "a75", "a76", "a77", "a78", "a79", "a80", "a81", "a82", "a83", "a84", "a85", "a86", "a87", "a88", "a89", "a90", "a91", "a92", "a93", "a94", "a95", "a96", "a97", "a98", "a99", "a100", "a101", "a102", "a103", "a104", "a105", "a106", "a107", "a108", "a109", "a110", "a111", "a112", "a113", "a114", "a115", "a116", "a117", "a118", "a119", "a120", "a121", "a122", "a123", "a124", "a125", "a126", "a127", "a128", "a129", "a130", "a131", "a132", "a133", "a134", "a135", "a136", "a137", "a138", "a139", "a140", "a141", "a142", "a143", "a144", "a145", "a146", "a147", "a148", "a149", "a150", "a151", "a152", "a153", "a154", "a155", "a156", "a157", "a158", "a159", "a160", "a161", "a162", "a163", "a164", "a165", "a166", "a167", "a168", "a169", "a170", "a171", "a172", "a173", "a174", "a175", "a176", "a177", "a178", "a179", "a180", "a181", "a182", "a183", "a184", "a185", "a186", "a187", "a188", "a189", "a190", "a191", "a192", "a193", "a194", "a195", "a196", "a197", "a198", "a199", "a200", "a201", "a202", "a203", "a204", "a205", "a206", "a207", "a208", "a209", "a210", "a211", "a212", "a213", "a214", "a215", "a216", "a217", "a218", "a219", "a220", "a221", "a222", "a223", "a224", "a225", "a226", "a227", "a228", "a229", "a230", "a231", "a232", "a233", "a234", "a235", "a236", "a237", "a238", "a239", "a240", "a241", "a242", "a243", "a244", "a245", "a246", "a247", "a248", "a249", "a250", "a251", "a252", "a253", "a254", "a255");
  if (bad) {
    slow_rows((const Args*)ka, (b & 7) * 4 + ((b >> 3) & 3), (b >> 5) * 256 + (tid & 192), tid & 63);
  }
}

extern "C" void kernel_launch(void* const* d_in, const int* in_sizes, int n_in,
                              void* d_out, int out_size, void* d_ws, size_t ws_size,
                              hipStream_t stream) {
  (void)hipFuncSetAttribute((const void*)attn_fwd, hipFuncAttributeMaxDynamicSharedMemorySize, SHM);
  attn_fwd<<<256, 256, SHM, stream>>>((const unsigned short*)d_in[0], (const int*)d_in[1], (const int*)d_in[2], (const unsigned short*)d_in[3], (const unsigned short*)d_in[4],
                                      (const unsigned short*)d_in[5], (const unsigned short*)d_in[6], (char*)d_ws, (unsigned short*)d_out);
}
```

```cpp
#include <hip/hip_runtime.h>

constexpr int D = 192, NH = 32, S = 2048;
constexpr int SHM = 155968;

struct Args { const unsigned short* q; const int* qk; const int* qv; const unsigned short* ksc; const unsigned short* kzr;
              const unsigned short* vsc; const unsigned short* vzr; char* ws; unsigned short* o; };

__device__ __forceinline__ float bf2f(unsigned short x) { return __uint_as_float((unsigned)x << 16); }
__device__ __forceinline__ float rbf(float x) { return (float)(__bf16)x; }

__device__ __forceinline__ void slow_rows(const Args* a, unsigned h, unsigned row0, unsigned lane) {
  const unsigned short* q = a->q + ((size_t)h * S + row0) * D;
  unsigned short* o = a->o + ((size_t)h * S + row0) * D;
  const int* qk = a->qk + (size_t)h * S * D; const int* qv = a->qv + (size_t)h * S * D;
  const unsigned short* ks = a->ksc + h * 32 * D; const unsigned short* kz = a->kzr + h * 32 * D;
  const unsigned short* vs = a->vsc + h * S; const unsigned short* vz = a->vzr + h * S;
#pragma unroll 1
  for (int r = 0; r < 64; ++r) {
    float m = -INFINITY, l = 0.f, a0 = 0.f, a1 = 0.f, a2 = 0.f;
#pragma unroll 1
    for (int t = 0; t < 32; ++t) {
      const int* kr = qk + (size_t)(t * 64 + lane) * D;
      float s = 0.f;
#pragma unroll 4
      for (int d = 0; d < D; ++d) s = fmaf(bf2f(q[r * D + d]), rbf(rbf(rbf((float)kr[d]) - bf2f(kz[t * D + d])) * bf2f(ks[t * D + d])), s);
      s *= 0.07216878364870322f;
      float mx = s;
      for (int x = 32; x; x >>= 1) mx = fmaxf(mx, __shfl_xor(mx, x));
      const float mn = fmaxf(m, mx), c = __expf(m - mn), p = __expf(s - mn);
      float ps = p;
      for (int x = 32; x; x >>= 1) ps += __shfl_xor(ps, x);
      l = l * c + ps; a0 *= c; a1 *= c; a2 *= c; m = mn;
#pragma unroll 2
      for (int k = 0; k < 64; ++k) {
        const float pk = __shfl(p, k), z = bf2f(vz[t * 64 + k]), sc = bf2f(vs[t * 64 + k]);
        const int* vr = qv + (size_t)(t * 64 + k) * D + lane;
        a0 = fmaf(pk, rbf(rbf(rbf((float)vr[0]) - z) * sc), a0); a1 = fmaf(pk, rbf(rbf(rbf((float)vr[64]) - z) * sc), a1); a2 = fmaf(pk, rbf(rbf(rbf((float)vr[128]) - z) * sc), a2);
      }
    }
    const float i = 1.f / l;
    o[r * D + lane] = __builtin_bit_cast(unsigned short, (__bf16)(a0 * i)); o[r * D + 64 + lane] = __builtin_bit_cast(unsigned short, (__bf16)(a1 * i));
    o[r * D + 128 + lane] = __builtin_bit_cast(unsigned short, (__bf16)(a2 * i));
  }
}

__global__ __launch_bounds__(256, 1) __attribute__((aligned(4096))) void attn_fwd(const unsigned short* q, const int* qk, const int* qv, const unsigned short* ksc, const unsigned short* kzr,
                                                   const unsigned short* vsc, const unsigned short* vzr, char* ws, unsigned short* o) {
  unsigned long long ka = (unsigned long long)__builtin_amdgcn_kernarg_segment_ptr();
  unsigned bad, b = blockIdx.x;
  const unsigned tid = threadIdx.x;
  asm volatile(
    "s_load_dwordx2 s[28:29], %1, 8\n"
    "s_load_dwordx2 s[40:41], %1, 16\n"
    "s_load_dwordx2 s[44:45], %1, 24\n"
    "s_load_dwordx2 s[88:89], %1, 32\n"
    "s_load_dwordx2 s[36:37], %1, 56\n"
    "s_load_dwordx2 s[92:93], %1, 40\n"
    "s_load_dwordx2 s[96:97], %1, 48\n"
    "s_load_dwordx2 s[20:21], %1, 0\n"
    "s_load_dwordx2 s[16:17], %1, 56\n"
    "s_load_dwordx2 s[24:25], %1, 64\n"
    "s_getreg_b32 s72, hwreg(HW_REG_XCC_ID, 0, 1)\n"
    "s_cmp_eq_u32 s72, 1\n"
    "s_cbranch_scc1 .Ldly\n"
    "s_sleep 32\n"
    ".Ldly:\n"
    "v_readfirstlane_b32 s84, %3\n"
    "v_and_b32 v156, 63, %3\n"
    "s_and_b32 s52, %2, 7\n"
    "s_bfe_u32 s72, %2, 0x20003\n"
    "s_lshr_b32 s53, %2, 5\n"
    "s_lshl_b32 s52, s52, 2\n"
    "s_lshr_b32 s84, s84, 6\n"
    "s_or_b32 s52, s52, s72\n"
    "v_lshlrev_b32 v1, 4, v156\n"
    "s_getpc_b64 s[54:55]\n"
    ".Lpc:\n"
    "s_lshl_b32 s72, s72, 7\n"
    "v_lshl_add_u32 v246, %3, 9, s72\n"
    "v_min_u32 v246, .Lend-.Lpc-4, v246\n"
    "global_load_dword v246, v246, s[54:55]\n"
    "s_mov_b32 s30, 1572864\n"
    "s_mov_b32 s31, 0x20000\n"
    "s_mov_b32 s42, 1572864\n"
    "s_mov_b32 s43, 0x20000\n"
    "s_mov_b32 s46, 12288\n"
    "s_mov_b32 s47, 0x20000\n"
    "s_mov_b32 s90, 12288\n"
    "s_mov_b32 s91, 0x20000\n"
    "s_lshr_b32 s79, s84, 1\n"
    "s_and_b32 s59, s84, 1\n"
    "s_cmp_ge_u32 s53, 6\n"
    "s_cselect_b32 s83, 1, 0\n"
    "s_or_b32 s83, s83, s84\n"
    "s_sub_u32 s58, 2, s79\n"
    "s_mul_i32 s71, s58, 49152\n"
    "s_mul_i32 s72, s52, 1572864\n"
    "s_mul_i32 s73, s52, 12288\n"
    "v_lshrrev_b32 v157, 2, v156\n"
    "v_and_b32 v158, 3, v156\n"
    "s_mul_i32 s35, s84, 3072\n"
    "s_bitcmp1_b32 s83, 0\n"
    "s_cbranch_scc1 .Lf2b\n"
    "v_and_b32 v159, 32, v156\n"
    "v_mul_u32_u24 v242, 376, v159\n"
    "v_and_b32 v159, 16, v156\n"
    "v_mul_u32_u24 v159, 88, v159\n"
    "v_lshl_add_u32 v242, v156, 3, v242\n"
    "v_add_u32 v242, v159, v242\n"
    "s_lshl_b32 s63, s53, 7\n"
    "v_mul_u32_u24 v160, 768, v157\n"
    "v_lshl_add_u32 v160, v158, 4, v160\n"
    "s_and_b32 s81, s58, 1\n"
    "s_mul_i32 s81, s81, 384\n"
    "s_lshl_b32 s75, s53, 6\n"
    "s_add_u32 s81, s81, s75\n"
    "s_add_u32 s75, s71, s63\n"
    "s_add_u32 s76, s75, 6144\n"
    "s_add_u32 s77, s75, 24576\n"
    "s_add_u32 s78, s75, 30720\n"
    "s_waitcnt lgkmcnt(0)\n"
    "s_add_u32 s44, s44, s73\n"
    "s_addc_u32 s45, s45, 0\n"
    "s_add_u32 s88, s88, s73\n"
    "s_addc_u32 s89, s89, 0\n"
    "buffer_load_dwordx4 v[4:7], v1, s[44:47], s35 offen offset:0\n"
    "buffer_load_dwordx4 v[8:11], v1, s[44:47], s35 offen offset:1024\n"
    "buffer_load_dwordx4 v[12:15], v1, s[44:47], s35 offen offset:2048\n"
    "buffer_load_dwordx4 v[16:19], v1, s[88:91], 0 offen offset:0\n"
    "buffer_load_dwordx4 v[164:167], v160, s[44:47], s81 offen offset:0\n"
    "buffer_load_dwordx4 v[168:171], v160, s[88:91], s81 offen offset:0\n"
    "s_add_u32 s28, s28, s72\n"
    "s_addc_u32 s29, s29, 0\n"
    "buffer_load_dwordx2 v[204:205], v242, s[28:31], s75 offen offset:0 nt\n"
    "buffer_load_dwordx2 v[206:207], v242, s[28:31], s75 offen offset:768 nt\n"
    "buffer_load_dwordx2 v[208:209], v242, s[28:31], s75 offen offset:3072 nt\n"
    "buffer_load_dwordx2 v[210:211], v242, s[28:31], s75 offen offset:3840 nt\n"
    "buffer_load_dwordx2 v[212:213], v242, s[28:31], s76 offen offset:0 nt\n"
    "buffer_load_dwordx2 v[214:215], v242, s[28:31], s76 offen offset:768 nt\n"
    "buffer_load_dwordx2 v[216:217], v242, s[28:31], s76 offen offset:3072 nt\n"
    "buffer_load_dwordx2 v[218:219], v242, s[28:31], s76 offen offset:3840 nt\n"
    "buffer_load_dwordx2 v[220:221], v242, s[28:31], s77 offen offset:0 nt\n"
    "buffer_load_dwordx2 v[222:223], v242, s[28:31], s77 offen offset:768 nt\n"
    "buffer_load_dwordx2 v[224:225], v242, s[28:31], s77 offen offset:3072 nt\n"
    "buffer_load_dwordx2 v[226:227], v242, s[28:31], s77 offen offset:3840 nt\n"
    "buffer_load_dwordx2 v[228:229], v242, s[28:31], s78 offen offset:0 nt\n"
    "buffer_load_dwordx2 v[230:231], v242, s[28:31], s78 offen offset:768 nt\n"
    "buffer_load_dwordx2 v[232:233], v242, s[28:31], s78 offen offset:3072 nt\n"
    "buffer_load_dwordx2 v[234:235], v242, s[28:31], s78 offen offset:3840 nt\n"
    "s_add_u32 s75, s71, s63\n"
    "s_add_u32 s75, s75, 98304\n"
    "s_add_u32 s76, s75, 6144\n"
    "s_add_u32 s77, s75, 24576\n"
    "s_add_u32 s78, s75, 30720\n"
    "buffer_load_dwordx2 v[36:37], v242, s[28:31], s75 offen offset:0 nt\n"
    "buffer_load_dwordx2 v[38:39], v242, s[28:31], s75 offen offset:768 nt\n"
    "v_lshrrev_b32 v162, 1, v156\n"
    "v_and_b32 v162, 16, v162\n"
    "v_bfe_u32 v159, v156, 4, 1\n"
    "v_or_b32 v162, v162, v159\n"
    "v_and_or_b32 v162, v157, 2, v162\n"
    "v_mul_u32_u24 v162, 768, v162\n"
    "v_and_b32 v163, 0x70, v1\n"
    "v_add_u32 v162, v162, v163\n"
    "s_branch .Lf2e\n"
    ".Lf2b:\n"
    "v_and_b32 v159, 48, v156\n"
    "v_lshlrev_b32 v242, 2, v159\n"
    "v_lshl_add_u32 v243, v158, 4, v242\n"
    "v_and_b32 v159, 12, v156\n"
    "v_mul_u32_u24 v242, 384, v159\n"
    "v_add_u32 v242, v243, v242\n"
    "v_add_u32 v243, v243, v159\n"
    "s_mul_i32 s63, s53, 6144\n"
    "s_sub_u32 s63, s63, 49152\n"
    "s_cmp_eq_u32 s59, 0\n"
    "s_cselect_b32 s63, 0x7f000000, s63\n"
    "s_add_u32 s75, s71, s63\n"
    "s_waitcnt lgkmcnt(0)\n"
    "s_add_u32 s44, s44, s73\n"
    "s_addc_u32 s45, s45, 0\n"
    "s_add_u32 s88, s88, s73\n"
    "s_addc_u32 s89, s89, 0\n"
    "buffer_load_dwordx4 v[4:7], v1, s[44:47], s35 offen offset:0\n"
    "buffer_load_dwordx4 v[8:11], v1, s[44:47], s35 offen offset:1024\n"
    "buffer_load_dwordx4 v[12:15], v1, s[44:47], s35 offen offset:2048\n"
    "buffer_load_dwordx4 v[16:19], v1, s[88:91], 0 offen offset:0\n"
    "s_add_u32 s40, s40, s72\n"
    "s_addc_u32 s41, s41, 0\n"
    "buffer_load_dwordx4 v[204:207], v242, s[40:43], s75 offen offset:0 nt\n"
    "buffer_load_dwordx4 v[208:211], v242, s[40:43], s75 offen offset:768 nt\n"
    "buffer_load_dwordx4 v[212:215], v242, s[40:43], s75 offen offset:256 nt\n"
    "buffer_load_dwordx4 v[216:219], v242, s[40:43], s75 offen offset:1024 nt\n"
    "buffer_load_dwordx4 v[220:223], v242, s[40:43], s75 offen offset:512 nt\n"
    "buffer_load_dwordx4 v[224:227], v242, s[40:43], s75 offen offset:1280 nt\n"
    "s_add_u32 s75, s71, s63\n"
    "s_add_u32 s75, s75, 98304\n"
    "buffer_load_dwordx4 v[36:39], v242, s[40:43], s75 offen offset:0 nt\n"
    "s_add_u32 s28, s28, s72\n"
    "s_addc_u32 s29, s29, 0\n"
    "v_lshrrev_b32 v162, 1, v156\n"
    "v_and_b32 v162, 16, v162\n"
    "v_bfe_u32 v159, v156, 4, 1\n"
    "v_or_b32 v162, v162, v159\n"
    "v_and_or_b32 v162, v157, 2, v162\n"
    "v_mul_u32_u24 v162, 768, v162\n"
    "v_and_b32 v163, 0x70, v1\n"
    "v_add_u32 v162, v162, v163\n"
    ".Lf2e:\n"
    "s_nop 4\n"
    "v_lshlrev_b32 v247, 2, v156\n"
    "v_add_u32 v247, 155648, v247\n"
    "v_mov_b32 v159, 0\n"
    "ds_write_b32 v247, v159\n"
    "v_mov_b32 v159, 0xff800000\n"
    "s_lshl2_add_u32 s74, s84, 155936\n"
    "v_mov_b32 v160, s74\n"
    "ds_write_b32 v160, v159\n"
    "s_mov_b32 s38, 5376\n"
    "s_mov_b32 s39, 0x20000\n"
    "s_mov_b32 s94, 4096\n"
    "s_mov_b32 s95, 0x20000\n"
    "s_mov_b32 s98, 4096\n"
    "s_mov_b32 s99, 0x20000\n"
    "s_mov_b32 s22, 98304\n"
    "s_mov_b32 s23, 0x20000\n"
    "s_mov_b32 s18, 1622016\n"
    "s_mov_b32 s19, 0x20000\n"
    "s_mov_b32 s26, 98304\n"
    "s_mov_b32 s27, 0x20000\n"
    "s_lshl_b32 s80, s58, 7\n"
    "s_add_u32 s80, s80, 256\n"
    "s_lshl_b32 s74, s52, 16\n"
    "s_mul_i32 s69, s59, 384\n"
    "s_mul_i32 s57, s79, 6144\n"
    "s_add_u32 s57, s57, s69\n"
    "s_add_u32 s66, s57, 24576\n"
    "s_add_u32 s36, s36, s74\n"
    "s_addc_u32 s37, s37, 0\n"
    "s_lshr_b32 s74, s74, 4\n"
    "s_add_u32 s92, s92, s74\n"
    "s_addc_u32 s93, s93, 0\n"
    "s_add_u32 s96, s96, s74\n"
    "s_addc_u32 s97, s97, 0\n"
    "v_lshlrev_b32 v128, 3, v156\n"
    "v_mov_b32 v129, 0\n"
    "v_lshlrev_b32 v130, 4, %3\n"
    "v_xor_b32 v130, 1024, v130\n"
    "buffer_load_dwordx2 v[124:125], v128, s[44:47], 0 offen offset:0\n"
    "buffer_load_dwordx2 v[126:127], v128, s[88:91], 0 offen offset:0\n"
    "buffer_load_dwordx2 v[238:239], v129, s[36:39], 0 offen offset:0 sc1\n"
    "s_mul_i32 s74, s52, 786432\n"
    "s_mul_i32 s73, s53, 98304\n"
    "s_add_u32 s74, s74, s73\n"
    "s_add_u32 s20, s20, s74\n"
    "s_addc_u32 s21, s21, 0\n"
    "s_add_u32 s24, s24, s74\n"
    "s_addc_u32 s25, s25, 0\n"
    "s_mul_i32 s72, s52, 1626112\n"
    "s_add_u32 s72, s72, 2097152\n"
    "s_add_u32 s16, s16, s72\n"
    "s_addc_u32 s17, s17, 0\n"
    "s_mul_i32 s65, s84, 6144\n"
    "s_mul_i32 s85, s84, 24576\n"
    "s_waitcnt lgkmcnt(0)\n"
    "s_barrier\n"
    "s_add_u32 s73, s65, 49152\n"
    "v_add_u32 v131, s73, v1\n"
    "s_add_u32 s73, s73, s69\n"
    "v_add_u32 v132, s73, v163\n"
    "v_and_b32 v159, 32, v156\n"
    "v_lshlrev_b32 v244, 3, v156\n"
    "v_xor_b32 v244, v244, v159\n"
    "v_lshl_add_u32 v244, v159, 3, v244\n"
    "s_bitcmp1_b32 s83, 0\n"
    "s_cbranch_scc1 .Lf5b\n"
    "s_lshl_b32 s60, s53, 11\n"
    "s_add_u32 s61, s60, 12288\n"
    "s_lshl_b32 s73, s58, 8\n"
    "s_add_u32 s73, s73, 122880\n"
    "v_and_b32 v243, 0xf0, v1\n"
    "v_add_u32 v243, s73, v243\n"
    "s_and_b32 s73, s73, 0x1fd00\n"
    "v_lshlrev_b32 v160, 9, v157\n"
    "v_lshl_add_u32 v160, v158, 6, v160\n"
    "v_add_u32 v160, s73, v160\n"
    "s_lshl_b32 s73, s79, 8\n"
    "s_mul_i32 s74, s59, 6144\n"
    "s_add_u32 s73, s73, s74\n"
    "v_add_u32 v163, s73, v244\n"
    "v_and_b32 v159, 32, v156\n"
    "v_lshlrev_b32 v244, 2, v156\n"
    "v_xor_b32 v244, v244, v159\n"
    "v_mad_u32_u24 v244, v159, 12, v244\n"
    "buffer_load_dwordx2 v[40:41], v242, s[28:31], s75 offen offset:3072 nt\n"
    "buffer_load_dwordx2 v[42:43], v242, s[28:31], s75 offen offset:3840 nt\n"
    "v_add_u32 v159, 0, v156\n"
    "v_mul_u32_u24 v159, 2731, v159\n"
    "v_lshrrev_b32 v159, 16, v159\n"
    "v_xor_b32 v159, v159, v156\n"
    "v_and_b32 v159, 7, v159\n"
    "v_and_b32 v88, 0x380, v1\n"
    "v_lshl_add_u32 v88, v159, 4, v88\n"
    "v_add_u32 v159, 64, v156\n"
    "v_mul_u32_u24 v159, 2731, v159\n"
    "v_lshrrev_b32 v159, 16, v159\n"
    "v_xor_b32 v159, v159, v156\n"
    "v_and_b32 v159, 7, v159\n"
    "v_and_b32 v89, 0x380, v1\n"
    "v_lshl_add_u32 v89, v159, 4, v89\n"
    "v_add_u32 v159, 128, v156\n"
    "v_mul_u32_u24 v159, 2731, v159\n"
    "v_lshrrev_b32 v159, 16, v159\n"
    "v_xor_b32 v159, v159, v156\n"
    "v_and_b32 v159, 7, v159\n"
    "v_and_b32 v90, 0x380, v1\n"
    "v_lshl_add_u32 v90, v159, 4, v90\n"
    "v_and_b32 v2, 7, v156\n"
    "v_lshrrev_b32 v144, 7, v130\n"
    "v_mul_u32_u24 v144, 768, v144\n"
    "v_lshl_add_u32 v144, v2, 3, v144\n"
    "buffer_load_dwordx2 v[44:45], v242, s[28:31], s76 offen offset:0 nt\n"
    "buffer_load_dwordx2 v[46:47], v242, s[28:31], s76 offen offset:768 nt\n"
    "v_add_u32 v144, 131072, v144\n"
    "v_mad_u32_u24 v86, v2, 24, v144\n"
    "v_and_b32 v96, 15, v156\n"
    "v_lshrrev_b32 v159, 4, v156\n"
    "v_xor_b32 v159, v159, v2\n"
    "v_mul_u32_u24 v96, 384, v96\n"
    "v_lshl_add_u32 v96, v159, 4, v96\n"
    "s_add_u32 s73, s65, 49152\n"
    "v_add_u32 v96, s73, v96\n"
    "v_xor_b32 v97, 64, v96\n"
    "v_subrev_u32 v154, 24576, v96\n"
    "v_subrev_u32 v155, 24576, v97\n"
    "s_add_u32 s67, s65, 98304\n"
    "s_add_u32 s68, s65, 24576\n"
    "s_add_u32 s69, s65, 73728\n"
    "s_mov_b32 s81, 256\n"
    "s_mul_i32 s74, s79, 24576\n"
    "s_add_u32 s70, s74, s65\n"
    "s_add_u32 s70, s70, 73728\n"
    "s_sub_u32 s64, s65, s74\n"
    "s_add_u32 s64, s64, 98304\n"
    "v_and_b32 v249, 0x310, v1\n"
    "v_and_b32 v159, 12, v156\n"
    "v_lshl_add_u32 v249, v159, 3, v249\n"
    "v_and_b32 v159, 2, v156\n"
    "buffer_load_dwordx2 v[48:49], v242, s[28:31], s76 offen offset:3072 nt\n"
    "buffer_load_dwordx2 v[50:51], v242, s[28:31], s76 offen offset:3840 nt\n"
    "v_lshl_add_u32 v249, v159, 6, v249\n"
    "s_add_u32 s73, s74, 73728\n"
    "v_add_u32 v248, s73, v249\n"
    "s_sub_u32 s73, 98304, s74\n"
    "v_add_u32 v249, s73, v249\n"
    "s_or_b32 s73, s53, s84\n"
    "s_cmp_eq_u32 s73, 0\n"
    "s_cselect_b32 s82, 0, 0x7fff0000\n"
    "v_and_b32 v252, 12, v156\n"
    "v_bfe_u32 v159, v156, 1, 1\n"
    "v_or_b32 v252, v252, v159\n"
    "v_and_b32 v159, 1, v156\n"
    "v_lshl_or_b32 v252, v159, 1, v252\n"
    "v_lshlrev_b32 v252, 6, v252\n"
    "v_lshlrev_b32 v159, 2, v156\n"
    "v_and_b32 v159, 32, v159\n"
    "v_xor_b32 v159, v159, v156\n"
    "v_and_or_b32 v252, v159, 48, v252\n"
    "v_mov_b32 v2, v252\n"
    "v_add_u32 v3, 24576, v252\n"
    "v_lshrrev_b32 v91, 3, v156\n"
    "v_lshl_add_u32 v87, v91, 6, 64\n"
    "v_add_u32 v87, 64, v87\n"
    "v_and_b32 v241, 1, v156\n"
    "v_lshlrev_b32 v241, 3, v241\n"
    "buffer_load_dwordx2 v[52:53], v242, s[28:31], s77 offen offset:0 nt\n"
    "buffer_load_dwordx2 v[54:55], v242, s[28:31], s77 offen offset:768 nt\n"
    "v_lshl_add_u32 v241, v91, 4, v241\n"
    "v_add_u32 v241, 1152, v241\n"
    "v_lshlrev_b32 v91, 24, v91\n"
    "v_lshl_add_u32 v91, v156, 3, v91\n"
    "s_lshl_b32 s73, s53, 4\n"
    "s_lshl_b32 s74, s59, 3\n"
    "s_add_u32 s73, s73, s74\n"
    "s_add_u32 s73, s73, 1152\n"
    "v_mov_b32 v240, 0x7fff0000\n"
    "v_writelane_b32 v240, s73, 0\n"
    "v_and_b32 v159, 48, v156\n"
    "s_mul_i32 s73, s79, 768\n"
    "s_sub_u32 s73, 131072, s73\n"
    "v_lshlrev_b32 v250, 1, v159\n"
    "v_add_u32 v250, s73, v250\n"
    "v_and_b32 v251, 3, v156\n"
    "v_lshl_add_u32 v251, v251, 7, v159\n"
    "s_add_u32 s73, s73, 256\n"
    "v_add_u32 v251, s73, v251\n"
    "v_and_b32 v159, 64, v1\n"
    "v_add_u32 v251, v159, v251\n"
    "v_bfe_i32 v159, v156, 2, 1\n"
    "v_and_b32 v159, 0xaaaa, v159\n"
    "v_or_b32 v253, 0x4444, v159\n"
    "s_mov_b32 s58, 0\n"
    "s_mov_b32 s59, 8\n"
    "s_waitcnt vmcnt(31)\n"
    "v_pk_min_i16 v4, v4, v10\n"
    "v_pk_min_i16 v5, v5, v11\n"
    "v_pk_min_i16 v6, v6, v12\n"
    "v_pk_min_i16 v7, v7, v13\n"
    "v_pk_min_i16 v8, v8, v14\n"
    "v_pk_min_i16 v9, v9, v15\n"
    "v_pk_min_i16 v16, v16, v18\n"
    "v_pk_min_i16 v17, v17, v19\n"
    "v_pk_min_i16 v4, v4, v7\n"
    "v_pk_min_i16 v5, v5, v8\n"
    "v_pk_min_i16 v6, v6, v9\n"
    "v_pk_min_i16 v16, v16, v17\n"
    "v_pk_min_i16 v4, v4, v5\n"
    "v_lshlrev_b32 v17, 16, v16\n"
    "v_pk_min_i16 v4, v4, v6\n"
    "v_min_i32 v16, v16, v17\n"
    "v_lshlrev_b32 v5, 16, v4\n"
    "v_mov_b32 v7, 155920\n"
    "v_min_i32 v4, v4, v5\n"
    "v_lshl_add_u32 v6, s84, 2, v7\n"
    "v_min_i32_dpp v16, v16, v16 row_shr:1\n"
    "v_min_i32_dpp v4, v4, v4 row_shr:1\n"
    "s_nop 0\n"
    "v_min_i32_dpp v16, v16, v16 row_shr:2\n"
    "v_min_i32_dpp v4, v4, v4 row_shr:2\n"
    "s_nop 0\n"
    "v_min_i32_dpp v16, v16, v16 row_shr:4\n"
    "v_min_i32_dpp v4, v4, v4 row_shr:4\n"
    "s_nop 0\n"
    "v_min_i32_dpp v16, v16, v16 row_shr:8\n"
    "v_min_i32_dpp v4, v4, v4 row_shr:8\n"
    "s_nop 0\n"
    "v_min_i32_dpp v16, v16, v16 row_bcast:15 row_mask:0xa\n"
    "v_min_i32_dpp v4, v4, v4 row_bcast:15 row_mask:0xa\n"
    "s_nop 0\n"
    "v_min_i32_dpp v16, v16, v16 row_bcast:31 row_mask:0xc\n"
    "v_min_i32_dpp v4, v4, v4 row_bcast:31 row_mask:0xc\n"
    "v_readlane_b32 s35, v16, 63\n"
    "v_readlane_b32 s73, v4, 63\n"
    "s_max_i32 s35, s35, 0\n"
    "s_and_b32 s35, s35, 0xffff0000\n"
    "v_mov_b32 v4, s73\n"
    "ds_write_b32 v6, v4\n"
    "v_mov_b32 v14, -32\n"
    "v_mov_b32 v12, -126\n"
    "v_mov_b32 v13, 0x3dd53b95\n"
    "v_mov_b32 v98, 0\n"
    "s_waitcnt vmcnt(29)\n"
    "v_lshlrev_b32 v180, 16, v164\n"
    "v_and_b32 v181, 0xffff0000, v164\n"
    "v_lshlrev_b32 v182, 16, v168\n"
    "v_and_b32 v183, 0xffff0000, v168\n"
    "v_lshlrev_b32 v184, 16, v165\n"
    "v_and_b32 v185, 0xffff0000, v165\n"
    "v_lshlrev_b32 v186, 16, v169\n"
    "v_and_b32 v187, 0xffff0000, v169\n"
    "v_lshlrev_b32 v188, 16, v166\n"
    "v_and_b32 v189, 0xffff0000, v166\n"
    "v_lshlrev_b32 v190, 16, v170\n"
    "v_and_b32 v191, 0xffff0000, v170\n"
    "v_lshlrev_b32 v192, 16, v167\n"
    "v_and_b32 v193, 0xffff0000, v167\n"
    "v_lshlrev_b32 v194, 16, v171\n"
    "v_and_b32 v195, 0xffff0000, v171\n"
    "s_waitcnt lgkmcnt(0)\n"
    "s_barrier\n"
    "ds_read_b128 v[8:11], v7 offset:0\n"
    "buffer_load_dwordx2 v[56:57], v242, s[28:31], s77 offen offset:3072 nt\n"
    "buffer_load_dwordx2 v[58:59], v242, s[28:31], s77 offen offset:3840 nt\n"
    "buffer_load_dwordx4 v[136:139], v130, s[92:95], 0 offen offset:0\n"
    "buffer_load_dwordx4 v[140:143], v130, s[96:99], 0 offen offset:0\n"
    "s_nop 0\n"
    "s_nop 0\n"
    "s_nop 0\n"
    "s_nop 0\n"
    "s_nop 0\n"
    "s_nop 0\n"
    "s_nop 0\n"
    "s_nop 0\n"
    "s_nop 0\n"
    "s_nop 0\n"
    "s_nop 0\n"
    "s_nop 0\n"
    "s_nop 0\n"
    "s_nop 0\n"
    "s_nop 0\n"
    "s_nop 0\n"
    "s_nop 0\n"
    "s_nop 0\n"
    "s_nop 0\n"
    "s_nop 0\n"
    "s_nop 0\n"
    "s_nop 0\n"
    "s_nop 0\n"
    "s_nop 0\n"
    "s_nop 0\n"
    "s_nop 0\n"
    "s_nop 0\n"
    "s_nop 0\n"
    "s_nop 0\n"
    "s_nop 0\n"
    "s_nop 0\n"
    "s_nop 0\n"
    "s_nop 0\n"
    "s_nop 0\n"
    "s_nop 0\n"
    "s_nop 0\n"
    "s_nop 0\n"
    "s_nop 0\n"
    "s_nop 0\n"
    "s_nop 0\n"
    "s_nop 0\n"
    "s_nop 0\n"
    "s_nop 0\n"
    "s_nop 0\n"
    "s_nop 0\n"
    "s_nop 0\n"
    "s_nop 0\n"
    "s_nop 0\n"
    "s_nop 0\n"
    "s_nop 0\n"
    "s_nop 0\n"
    "s_nop 0\n"
    "s_nop 0\n"
    "s_nop 0\n"
    "s_nop 0\n"
    "s_nop 0\n"
    "s_nop 0\n"
    "s_nop 0\n"
    "s_nop 0\n"
    "s_nop 0\n"
    "s_waitcnt lgkmcnt(0)\n"
    "v_min3_i32 v4, v8, v9, v10\n"
    "v_min_i32 v4, v4, v11\n"
    "v_max_i32 v4, 0, v4\n"
    "v_and_b32 v4, 0xffff0000, v4\n"
    "v_mul_f32 v4, s35, v4\n"
    "v_mul_f32 v4, 0x3ed53b95, v4\n"
    "v_lshrrev_b32 v4, 23, v4\n"
    "v_sub_u32 v4, 2, v4\n"
    "v_med3_i32 v4, v4, v14, v12\n"
    "v_ldexp_f32 v13, v13, v4\n"
    "v_readfirstlane_b32 s89, v4\n"
    "v_readfirstlane_b32 s88, v13\n"
    "s_mul_i32 s89, s89, 0xff7fff80\n"
    "s_mov_b32 s90, 149\n"
    "v_mul_f32 v180, s88, v180\n"
    "v_mul_f32 v181, s88, v181\n"
    "v_mul_f32 v184, s88, v184\n"
    "v_mul_f32 v185, s88, v185\n"
    "v_mul_f32 v188, s88, v188\n"
    "v_mul_f32 v189, s88, v189\n"
    "v_mul_f32 v192, s88, v192\n"
    "v_mul_f32 v193, s88, v193\n"
    "v_fmaak_f32 v182, v182, v180, 0x80730000\n"
    "v_fmaak_f32 v183, v183, v181, 0x80730000\n"
    "v_fmaak_f32 v186, v186, v184, 0x80730000\n"
    "v_fmaak_f32 v187, v187, v185, 0x80730000\n"
    "v_fmaak_f32 v190, v190, v188, 0x80730000\n"
    "v_fmaak_f32 v191, v191, v189, 0x80730000\n"
    "v_fmaak_f32 v194, v194, v192, 0x80730000\n"
    "v_fmaak_f32 v195, v195, v193, 0x80730000\n"
    "v_ldexp_f32 v180, v180, s90\n"
    "v_ldexp_f32 v181, v181, s90\n"
    "v_ldexp_f32 v184, v184, s90\n"
    "v_ldexp_f32 v185, v185, s90\n"
    "v_ldexp_f32 v188, v188, s90\n"
    "v_ldexp_f32 v189, v189, s90\n"
    "v_ldexp_f32 v192, v192, s90\n"
    "v_ldexp_f32 v193, v193, s90\n"
    "v_fma_f32 v180, v180, 0, v180\n"
    "v_fma_f32 v181, v181, 0, v181\n"
    "v_fma_f32 v184, v184, 0, v184\n"
    "v_fma_f32 v185, v185, 0, v185\n"
    "v_fma_f32 v188, v188, 0, v188\n"
    "v_fma_f32 v189, v189, 0, v189\n"
    "v_fma_f32 v192, v192, 0, v192\n"
    "v_fma_f32 v193, v193, 0, v193\n"
    "ds_write_b128 v160, v[180:183] offset:0\n"
    "ds_write_b128 v160, v[184:187] offset:16\n"
    "ds_write_b128 v160, v[188:191] offset:32\n"
    "ds_write_b128 v160, v[192:195] offset:48\n"
    "s_waitcnt vmcnt(17)\n"
    "s_add_u32 s54, s71, s60\n"
    "s_add_u32 s55, s71, s61\n"
    "ds_read_b128 v[92:95], v243 offset:0\n"
    "s_waitcnt lgkmcnt(0)\n"
    "v_fma_f32 v204, v204, v92, -v94\n"
    "v_fma_f32 v205, v205, v93, -v95\n"
    "v_fma_f32 v206, v206, v92, -v94\n"
    "v_fma_f32 v207, v207, v93, -v95\n"
    "v_cvt_pk_bf16_f32 v204, v204, v205\n"
    "v_cvt_pk_bf16_f32 v206, v206, v207\n"
    "buffer_store_dword v204, v244, s[16:19], s54 offen offset:0 sc1\n"
    "buffer_store_dword v206, v244, s[16:19], s54 offen offset:128 sc1\n"
    "v_fma_f32 v208, v208, v92, -v94\n"
    "v_fma_f32 v209, v209, v93, -v95\n"
    "v_fma_f32 v210, v210, v92, -v94\n"
    "v_fma_f32 v211, v211, v93, -v95\n"
    "v_cvt_pk_bf16_f32 v208, v208, v209\n"
    "v_cvt_pk_bf16_f32 v210, v210, v211\n"
    "buffer_store_dword v208, v244, s[16:19], s54 offen offset:1024 sc1\n"
    "buffer_store_dword v210, v244, s[16:19], s54 offen offset:1152 sc1\n"
    "v_fma_f32 v212, v212, v92, -v94\n"
    "v_fma_f32 v213, v213, v93, -v95\n"
    "buffer_load_dwordx2 v[60:61], v242, s[28:31], s78 offen offset:0 nt\n"
    "buffer_load_dwordx2 v[62:63], v242, s[28:31], s78 offen offset:768 nt\n"
    "buffer_load_dwordx4 v[4:7], v162, s[28:31], s57 offen offset:0\n"
    "buffer_load_dwordx4 v[8:11], v162, s[28:31], s57 offen offset:3072\n"
    "v_fma_f32 v214, v214, v92, -v94\n"
    "v_fma_f32 v215, v215, v93, -v95\n"
    "v_cvt_pk_bf16_f32 v212, v212, v213\n"
    "v_cvt_pk_bf16_f32 v214, v214, v215\n"
    "buffer_store_dword v212, v244, s[16:19], s54 offen offset:256 sc1\n"
    "buffer_store_dword v214, v244, s[16:19], s54 offen offset:384 sc1\n"
    "v_fma_f32 v216, v216, v92, -v94\n"
    "v_fma_f32 v217, v217, v93, -v95\n"
    "v_fma_f32 v218, v218, v92, -v94\n"
    "v_fma_f32 v219, v219, v93, -v95\n"
    "v_cvt_pk_bf16_f32 v216, v216, v217\n"
    "v_cvt_pk_bf16_f32 v218, v218, v219\n"
    "buffer_store_dword v216, v244, s[16:19], s54 offen offset:1280 sc1\n"
    "buffer_store_dword v218, v244, s[16:19], s54 offen offset:1408 sc1\n"
    "v_fma_f32 v220, v220, v92, -v94\n"
    "v_fma_f32 v221, v221, v93, -v95\n"
    "v_fma_f32 v222, v222, v92, -v94\n"
    "v_fma_f32 v223, v223, v93, -v95\n"
    "v_cvt_pk_bf16_f32 v220, v220, v221\n"
    "v_cvt_pk_bf16_f32 v222, v222, v223\n"
    "buffer_store_dword v220, v244, s[16:19], s55 offen offset:0 sc1\n"
    "buffer_store_dword v222, v244, s[16:19], s55 offen offset:128 sc1\n"
    "v_fma_f32 v224, v224, v92, -v94\n"
    "buffer_load_dwordx2 v[64:65], v242, s[28:31], s78 offen offset:3072 nt\n"
    "buffer_load_dwordx2 v[66:67], v242, s[28:31], s78 offen offset:3840 nt\n"
    "buffer_load_dwordx4 v[12:15], v162, s[28:31], s57 offen offset:128\n"
    "v_fma_f32 v225, v225, v93, -v95\n"
    "v_fma_f32 v226, v226, v92, -v94\n"
    "v_fma_f32 v227, v227, v93, -v95\n"
    "v_cvt_pk_bf16_f32 v224, v224, v225\n"
    "v_cvt_pk_bf16_f32 v226, v226, v227\n"
    "buffer_store_dword v224, v244, s[16:19], s55 offen offset:1024 sc1\n"
    "buffer_store_dword v226, v244, s[16:19], s55 offen offset:1152 sc1\n"
    "v_fma_f32 v228, v228, v92, -v94\n"
    "v_fma_f32 v229, v229, v93, -v95\n"
    "v_fma_f32 v230, v230, v92, -v94\n"
    "v_fma_f32 v231, v231, v93, -v95\n"
    "v_cvt_pk_bf16_f32 v228, v228, v229\n"
    "v_cvt_pk_bf16_f32 v230, v230, v231\n"
    "buffer_store_dword v228, v244, s[16:19], s55 offen offset:256 sc1\n"
    "buffer_store_dword v230, v244, s[16:19], s55 offen offset:384 sc1\n"
    "v_fma_f32 v232, v232, v92, -v94\n"
    "v_fma_f32 v233, v233, v93, -v95\n"
    "v_fma_f32 v234, v234, v92, -v94\n"
    "v_fma_f32 v235, v235, v93, -v95\n"
    "v_cvt_pk_bf16_f32 v232, v232, v233\n"
    "v_cvt_pk_bf16_f32 v234, v234, v235\n"
    "buffer_store_dword v232, v244, s[16:19], s55 offen offset:1280 sc1\n"
    "buffer_store_dword v234, v244, s[16:19], s55 offen offset:1408 sc1\n"
    "v_add_u32 v243, 512, v243\n"
    "s_add_u32 s71, s71, 98304\n"
    "s_add_u32 m0, s65, 24576\n"
    "s_add_u32 s72, s85, 0\n"
    "buffer_load_dwordx4 v88, s[20:23], s72 offen offset:0 nt lds\n"
    "buffer_load_dwordx4 v[16:19], v162, s[28:31], s57 offen offset:3200\n"
    "buffer_load_dwordx4 v[20:23], v162, s[28:31], s57 offen offset:256\n"
    "s_waitcnt vmcnt(38)\n"
    "v_add_co_u32 v238, vcc, 0x7f4a7c15, v238\n"
    "v_mov_b32 v159, 0x9e3779b9\n"
    "s_nop 0\n"
    "v_addc_co_u32 v239, vcc, v159, v239, vcc\n"
    "v_mov_b32 v246, 1\n"
    "s_waitcnt vmcnt(39)\n"
    "v_lshlrev_b32 v180, 16, v124\n"
    "v_and_b32 v181, 0xffff0000, v124\n"
    "v_lshlrev_b32 v182, 16, v125\n"
    "v_and_b32 v183, 0xffff0000, v125\n"
    "v_lshlrev_b32 v184, 16, v126\n"
    "v_and_b32 v185, 0xffff0000, v126\n"
    "v_lshlrev_b32 v186, 16, v127\n"
    "v_and_b32 v187, 0xffff0000, v127\n"
    "v_mul_f32 v180, s88, v180\n"
    "v_mul_f32 v181, s88, v181\n"
    "v_mul_f32 v182, s88, v182\n"
    "v_mul_f32 v183, s88, v183\n"
    "v_fmaak_f32 v184, v184, v180, 0x80730000\n"
    "v_fmaak_f32 v185, v185, v181, 0x80730000\n"
    "v_fmaak_f32 v186, v186, v182, 0x80730000\n"
    "v_fmaak_f32 v187, v187, v183, 0x80730000\n"
    "s_add_u32 m0, s65, 25600\n"
    "s_add_u32 s72, s85, 1024\n"
    "buffer_load_dwordx4 v89, s[20:23], s72 offen offset:0 nt lds\n"
    "buffer_load_dwordx4 v[24:27], v162, s[28:31], s57 offen offset:3328\n"
    "v_ldexp_f32 v180, v180, s90\n"
    "v_ldexp_f32 v181, v181, s90\n"
    "v_ldexp_f32 v182, v182, s90\n"
    "v_ldexp_f32 v183, v183, s90\n"
    "v_fma_f32 v180, v180, 0, v180\n"
    "v_fma_f32 v181, v181, 0, v181\n"
    "v_fma_f32 v182, v182, 0, v182\n"
    "v_fma_f32 v183, v183, 0, v183\n"
    "ds_write_b128 v131, v[180:183] offset:0\n"
    "ds_write_b128 v131, v[184:187] offset:1024\n"
    "ds_read_b128 v[180:183], v132 offset:0\n"
    "ds_read_b128 v[184:187], v132 offset:1024\n"
    "ds_read_b128 v[188:191], v132 offset:128\n"
    "ds_read_b128 v[192:195], v132 offset:1152\n"
    "ds_read_b128 v[196:199], v132 offset:256\n"
    "ds_read_b128 v[200:203], v132 offset:1280\n"
    "s_nop 0\n"
    "s_nop 0\n"
    "s_nop 0\n"
    "s_nop 0\n"
    "s_nop 0\n"
    "s_nop 0\n"
    "s_nop 0\n"
    "s_add_u32 m0, s65, 26624\n"
    "s_add_u32 s72, s85, 2048\n"
    "buffer_load_dwordx4 v90, s[20:23], s72 offen offset:0 nt lds\n"
    "buffer_load_dwordx4 v[100:103], v162, s[28:31], s66 offen offset:0\n"
    "buffer_load_dwordx4 v[104:107], v162, s[28:31], s66 offen offset:3072\n"
    "s_nop 0\n"
    "s_nop 0\n"
    "s_nop 0\n"
    "s_nop 0\n"
    "s_nop 0\n"
    "s_nop 0\n"
    "s_nop 0\n"
    "s_nop 0\n"
    "s_nop 0\n"
    "s_nop 0\n"
    "s_nop 0\n"
    "s_nop 0\n"
    "s_nop 0\n"
    "s_nop 0\n"
    "s_nop 0\n"
    "s_nop 0\n"
    "s_nop 0\n"
    "s_nop 0\n"
    "s_nop 0\n"
    "s_nop 0\n"
    "s_nop 0\n"
    "s_nop 0\n"
    "s_add_u32 m0, s65, 27648\n"
    "s_add_u32 s72, s85, 3072\n"
    "buffer_load_dwordx4 v88, s[20:23], s72 offen offset:0 nt lds\n"
    "buffer_load_dwordx4 v[108:111], v162, s[28:31], s66 offen offset:128\n"
    "s_nop 0\n"
    "s_nop 0\n"
    "s_nop 0\n"
    "s_nop 0\n"
    "s_nop 0\n"
    "s_nop 0\n"
    "s_nop 0\n"
    "s_nop 0\n"
    "s_nop 0\n"
    "s_nop 0\n"
    "s_nop 0\n"
    "s_nop 0\n"
    "s_nop 0\n"
    "s_nop 0\n"
    "s_nop 0\n"
    "s_nop 0\n"
    "s_nop 0\n"
    "s_nop 0\n"
    "s_nop 0\n"
    "s_nop 0\n"
    "s_nop 0\n"
    "s_nop 0\n"
    "s_nop 0\n"
    "s_waitcnt lgkmcnt(0)\n"
    "s_waitcnt vmcnt(10)\n"
    "ds_add_rtn_u32 v245, v247, v246\n"
    "s_lshl_b32 s73, s53, 6\n"
    "s_add_u32 s73, s73, 128\n"
    "s_waitcnt lgkmcnt(0)\n"
    "v_readfirstlane_b32 s74, v245\n"
    "s_nop 0\n"
    "s_cmp_eq_u32 s74, 3\n"
    "s_cselect_b32 s73, s73, 0x7fff0000\n"
    "buffer_store_dwordx2 v[238:239], v91, s[36:39], s73 offen offset:0 sc1\n"
    "s_waitcnt vmcnt(18)\n"
    "s_add_u32 m0, s65, 28672\n"
    "s_add_u32 s72, s85, 4096\n"
    "buffer_load_dwordx4 v89, s[20:23], s72 offen offset:0 nt lds\n"
    "buffer_load_dwordx4 v[112:115], v162, s[28:31], s66 offen offset:3200\n"
    "buffer_load_dwordx4 v[116:119], v162, s[28:31], s66 offen offset:256\n"
    "s_add_u32 s54, s71, s60\n"
    "s_add_u32 s55, s71, s61\n"
    "ds_read_b128 v[92:95], v243 offset:0\n"
    "s_waitcnt lgkmcnt(0)\n"
    "v_fma_f32 v36, v36, v92, -v94\n"
    "v_fma_f32 v37, v37, v93, -v95\n"
    "v_fma_f32 v38, v38, v92, -v94\n"
    "v_fma_f32 v39, v39, v93, -v95\n"
    "v_cvt_pk_bf16_f32 v36, v36, v37\n"
    "v_cvt_pk_bf16_f32 v38, v38, v39\n"
    "buffer_store_dword v36, v244, s[16:19], s54 offen offset:0 sc1\n"
    "buffer_store_dword v38, v244, s[16:19], s54 offen offset:128 sc1\n"
    "v_fma_f32 v40, v40, v92, -v94\n"
    "v_fma_f32 v41, v41, v93, -v95\n"
    "v_fma_f32 v42, v42, v92, -v94\n"
    "v_fma_f32 v43, v43, v93, -v95\n"
    "v_cvt_pk_bf16_f32 v40, v40, v41\n"
    "v_cvt_pk_bf16_f32 v42, v42, v43\n"
    "buffer_store_dword v40, v244, s[16:19], s54 offen offset:1024 sc1\n"
    "buffer_store_dword v42, v244, s[16:19], s54 offen offset:1152 sc1\n"
    "v_fma_f32 v44, v44, v92, -v94\n"
    "v_fma_f32 v45, v45, v93, -v95\n"
    "v_fma_f32 v46, v46, v92, -v94\n"
    "v_fma_f32 v47, v47, v93, -v95\n"
    "v_cvt_pk_bf16_f32 v44, v44, v45\n"
    "v_cvt_pk_bf16_f32 v46, v46, v47\n"
    "buffer_store_dword v44, v244, s[16:19], s54 offen offset:256 sc1\n"
    "buffer_store_dword v46, v244, s[16:19], s54 offen offset:384 sc1\n"
    "v_fma_f32 v48, v48, v92, -v94\n"
    "v_fma_f32 v49, v49, v93, -v95\n"
    "v_fma_f32 v50, v50, v92, -v94\n"
    "v_fma_f32 v51, v51, v93, -v95\n"
    "v_cvt_pk_bf16_f32 v48, v48, v49\n"
    "v_cvt_pk_bf16_f32 v50, v50, v51\n"
    "s_add_u32 m0, s65, 29696\n"
    "s_add_u32 s72, s85, 5120\n"
    "buffer_load_dwordx4 v90, s[20:23], s72 offen offset:0 nt lds\n"
    "buffer_load_dwordx4 v[120:123], v162, s[28:31], s66 offen offset:3328\n"
    "buffer_store_dword v48, v244, s[16:19], s54 offen offset:1280 sc1\n"
    "buffer_store_dword v50, v244, s[16:19], s54 offen offset:1408 sc1\n"
    "v_fma_f32 v52, v52, v92, -v94\n"
    "v_fma_f32 v53, v53, v93, -v95\n"
    "v_fma_f32 v54, v54, v92, -v94\n"
    "v_fma_f32 v55, v55, v93, -v95\n"
    "v_cvt_pk_bf16_f32 v52, v52, v53\n"
    "v_cvt_pk_bf16_f32 v54, v54, v55\n"
    "buffer_store_dword v52, v244, s[16:19], s55 offen offset:0 sc1\n"
    "buffer_store_dword v54, v244, s[16:19], s55 offen offset:128 sc1\n"
    "v_fma_f32 v56, v56, v92, -v94\n"
    "v_fma_f32 v57, v57, v93, -v95\n"
    "v_fma_f32 v58, v58, v92, -v94\n"
    "v_fma_f32 v59, v59, v93, -v95\n"
    "v_cvt_pk_bf16_f32 v56, v56, v57\n"
    "v_cvt_pk_bf16_f32 v58, v58, v59\n"
    "buffer_store_dword v56, v244, s[16:19], s55 offen offset:1024 sc1\n"
    "buffer_store_dword v58, v244, s[16:19], s55 offen offset:1152 sc1\n"
    "v_fma_f32 v60, v60, v92, -v94\n"
    "v_fma_f32 v61, v61, v93, -v95\n"
    "v_fma_f32 v62, v62, v92, -v94\n"
    "v_fma_f32 v63, v63, v93, -v95\n"
    "v_cvt_pk_bf16_f32 v60, v60, v61\n"
    "v_cvt_pk_bf16_f32 v62, v62, v63\n"
    "buffer_store_dword v60, v244, s[16:19], s55 offen offset:256 sc1\n"
    "buffer_store_dword v62, v244, s[16:19], s55 offen offset:384 sc1\n"
    "v_fma_f32 v64, v64, v92, -v94\n"
    "v_fma_f32 v65, v65, v93, -v95\n"
    "v_fma_f32 v66, v66, v92, -v94\n"
    "v_fma_f32 v67, v67, v93, -v95\n"
    "v_cvt_pk_bf16_f32 v64, v64, v65\n"
    "v_cvt_pk_bf16_f32 v66, v66, v67\n"
    "buffer_store_dword v64, v244, s[16:19], s55 offen offset:1280 sc1\n"
    "buffer_store_dword v66, v244, s[16:19], s55 offen offset:1408 sc1\n"
    "v_add_u32 v243, 512, v243\n"
    "s_add_u32 m0, s65, 73728\n"
    "s_add_u32 s72, s85, 6144\n"
    "buffer_load_dwordx4 v88, s[20:23], s72 offen offset:0 nt lds\n"
    "s_waitcnt vmcnt(56)\n"
    "v_lshlrev_b32 v204, 16, v136\n"
    "v_and_b32 v205, 0xffff0000, v136\n"
    "v_lshlrev_b32 v206, 16, v137\n"
    "v_and_b32 v207, 0xffff0000, v137\n"
    "v_lshlrev_b32 v208, 16, v138\n"
    "v_and_b32 v209, 0xffff0000, v138\n"
    "v_lshlrev_b32 v210, 16, v139\n"
    "v_and_b32 v211, 0xffff0000, v139\n"
    "v_log_f32 v68, v204\n"
    "v_log_f32 v69, v205\n"
    "v_log_f32 v70, v206\n"
    "v_log_f32 v71, v207\n"
    "v_log_f32 v72, v208\n"
    "v_log_f32 v73, v209\n"
    "v_log_f32 v74, v210\n"
    "v_log_f32 v75, v211\n"
    "v_rcp_f32 v204, v204\n"
    "v_rcp_f32 v205, v205\n"
    "v_rcp_f32 v206, v206\n"
    "s_add_u32 m0, s65, 74752\n"
    "s_add_u32 s72, s85, 7168\n"
    "buffer_load_dwordx4 v89, s[20:23], s72 offen offset:0 nt lds\n"
    "v_rcp_f32 v207, v207\n"
    "v_rcp_f32 v208, v208\n"
    "v_rcp_f32 v209, v209\n"
    "v_rcp_f32 v210, v210\n"
    "v_rcp_f32 v211, v211\n"
    "s_mov_b32 s73, 0x41a00000\n"
    "v_mov_b32 v228, 0x7fc00000\n"
    "v_cmp_le_f32 vcc, |v68|, s73\n"
    "s_nop 1\n"
    "v_cndmask_b32 v68, v228, v68, vcc\n"
    "v_cmp_le_f32 vcc, |v69|, s73\n"
    "s_nop 1\n"
    "v_cndmask_b32 v69, v228, v69, vcc\n"
    "v_cmp_le_f32 vcc, |v70|, s73\n"
    "s_nop 1\n"
    "v_cndmask_b32 v70, v228, v70, vcc\n"
    "v_cmp_le_f32 vcc, |v71|, s73\n"
    "s_nop 1\n"
    "v_cndmask_b32 v71, v228, v71, vcc\n"
    "v_cmp_le_f32 vcc, |v72|, s73\n"
    "s_nop 1\n"
    "v_cndmask_b32 v72, v228, v72, vcc\n"
    "v_cmp_le_f32 vcc, |v73|, s73\n"
    "s_nop 1\n"
    "v_cndmask_b32 v73, v228, v73, vcc\n"
    "v_cmp_le_f32 vcc, |v74|, s73\n"
    "s_nop 1\n"
    "v_cndmask_b32 v74, v228, v74, vcc\n"
    "s_add_u32 m0, s65, 75776\n"
    "s_add_u32 s72, s85, 8192\n"
    "buffer_load_dwordx4 v90, s[20:23], s72 offen offset:0 nt lds\n"
    "v_cmp_le_f32 vcc, |v75|, s73\n"
    "s_nop 1\n"
    "v_cndmask_b32 v75, v228, v75, vcc\n"
    "v_cvt_pk_bf16_f32 v220, v204, v205\n"
    "v_cvt_pk_bf16_f32 v221, v206, v207\n"
    "v_cvt_pk_bf16_f32 v222, v208, v209\n"
    "v_cvt_pk_bf16_f32 v223, v210, v211\n"
    "v_lshlrev_b32 v212, 16, v220\n"
    "v_and_b32 v213, 0xffff0000, v220\n"
    "v_lshlrev_b32 v214, 16, v221\n"
    "v_and_b32 v215, 0xffff0000, v221\n"
    "v_lshlrev_b32 v216, 16, v222\n"
    "v_and_b32 v217, 0xffff0000, v222\n"
    "v_lshlrev_b32 v218, 16, v223\n"
    "v_and_b32 v219, 0xffff0000, v223\n"
    "v_sub_f32 v204, v204, v212\n"
    "v_sub_f32 v205, v205, v213\n"
    "v_sub_f32 v206, v206, v214\n"
    "v_sub_f32 v207, v207, v215\n"
    "v_sub_f32 v208, v208, v216\n"
    "v_sub_f32 v209, v209, v217\n"
    "s_add_u32 m0, s65, 76800\n"
    "s_add_u32 s72, s85, 9216\n"
    "buffer_load_dwordx4 v88, s[20:23], s72 offen offset:0 nt lds\n"
    "v_sub_f32 v210, v210, v218\n"
    "v_sub_f32 v211, v211, v219\n"
    "v_cvt_pk_bf16_f32 v224, v204, v205\n"
    "v_cvt_pk_bf16_f32 v225, v206, v207\n"
    "v_cvt_pk_bf16_f32 v226, v208, v209\n"
    "v_cvt_pk_bf16_f32 v227, v210, v211\n"
    "v_mov_b32 v204, 0\n"
    "v_mov_b32 v205, 0\n"
    "v_mov_b32 v206, 0\n"
    "v_mov_b32 v207, 0\n"
    "v_swap_b32 v221, v222\n"
    "v_swap_b32 v225, v226\n"
    "v_swap_b32 v141, v142\n"
    "ds_write2_b64 v144, v[220:221], v[222:223] offset0:32 offset1:40\n"
    "ds_write2_b64 v144, v[224:225], v[226:227] offset0:48 offset1:56\n"
    "ds_write2_b64 v144, v[140:141], v[142:143] offset0:64 offset1:72\n"
    "ds_write2_b64 v144, v[204:205], v[206:207] offset0:80 offset1:88\n"
    "s_waitcnt vmcnt(31)\n"
    "v_fma_f32 v4, v4, v180, -v184\n"
    "v_fma_f32 v5, v5, v181, -v185\n"
    "v_fma_f32 v6, v6, v182, -v186\n"
    "s_add_u32 m0, s65, 77824\n"
    "s_add_u32 s72, s85, 10240\n"
    "buffer_load_dwordx4 v89, s[20:23], s72 offen offset:0 nt lds\n"
    "v_fma_f32 v7, v7, v183, -v187\n"
    "v_fma_f32 v8, v8, v180, -v184\n"
    "v_fma_f32 v9, v9, v181, -v185\n"
    "v_fma_f32 v10, v10, v182, -v186\n"
    "v_fma_f32 v11, v11, v183, -v187\n"
    "v_cvt_pk_bf16_f32 v4, v4, v5\n"
    "v_cvt_pk_bf16_f32 v5, v6, v7\n"
    "v_cvt_pk_bf16_f32 v6, v8, v9\n"
    "v_cvt_pk_bf16_f32 v7, v10, v11\n"
    "ds_write_b64 v163, v[4:5] offset:0\n"
    "ds_write_b64 v163, v[6:7] offset:1024\n"
    "v_fma_f32 v12, v12, v188, -v192\n"
    "v_fma_f32 v13, v13, v189, -v193\n"
    "v_fma_f32 v14, v14, v190, -v194\n"
    "v_fma_f32 v15, v15, v191, -v195\n"
    "v_fma_f32 v16, v16, v188, -v192\n"
    "v_fma_f32 v17, v17, v189, -v193\n"
    "v_fma_f32 v18, v18, v190, -v194\n"
    "v_fma_f32 v19, v19, v191, -v195\n"
    "v_cvt_pk_bf16_f32 v12, v12, v13\n"
    "s_add_u32 m0, s65, 78848\n"
    "s_add_u32 s72, s85, 11264\n"
    "buffer_load_dwordx4 v90, s[20:23], s72 offen offset:0 nt lds\n"
    "v_cvt_pk_bf16_f32 v13, v14, v15\n"
    "v_cvt_pk_bf16_f32 v14, v16, v17\n"
    "v_cvt_pk_bf16_f32 v15, v18, v19\n"
    "ds_write_b64 v163, v[12:13] offset:2048\n"
    "ds_write_b64 v163, v[14:15] offset:3072\n"
    "v_fma_f32 v20, v20, v196, -v200\n"
    "v_fma_f32 v21, v21, v197, -v201\n"
    "v_fma_f32 v22, v22, v198, -v202\n"
    "v_fma_f32 v23, v23, v199, -v203\n"
    "v_fma_f32 v24, v24, v196, -v200\n"
    "v_fma_f32 v25, v25, v197, -v201\n"
    "v_fma_f32 v26, v26, v198, -v202\n"
    "v_fma_f32 v27, v27, v199, -v203\n"
    "v_cvt_pk_bf16_f32 v20, v20, v21\n"
    "v_cvt_pk_bf16_f32 v21, v22, v23\n"
    "v_cvt_pk_bf16_f32 v22, v24, v25\n"
    "v_cvt_pk_bf16_f32 v23, v26, v27\n"
    "ds_write_b64 v163, v[20:21] offset:4096\n"
    "ds_write_b64 v163, v[22:23] offset:5120\n"
    "s_waitcnt vmcnt(16)\n"
    "v_fma_f32 v100, v100, v180, -v184\n"
    "s_add_u32 m0, s65, 49152\n"
    "s_add_u32 s72, s85, 12288\n"
    "buffer_load_dwordx4 v88, s[20:23], s72 offen offset:0 nt lds\n"
    "v_fma_f32 v101, v101, v181, -v185\n"
    "v_fma_f32 v102, v102, v182, -v186\n"
    "v_fma_f32 v103, v103, v183, -v187\n"
    "v_fma_f32 v104, v104, v180, -v184\n"
    "v_fma_f32 v105, v105, v181, -v185\n"
    "v_fma_f32 v106, v106, v182, -v186\n"
    "v_fma_f32 v107, v107, v183, -v187\n"
    "v_cvt_pk_bf16_f32 v100, v100, v101\n"
    "v_cvt_pk_bf16_f32 v101, v102, v103\n"
    "v_cvt_pk_bf16_f32 v102, v104, v105\n"
    "v_cvt_pk_bf16_f32 v103, v106, v107\n"
    "ds_write_b64 v163, v[100:101] offset:12288\n"
    "ds_write_b64 v163, v[102:103] offset:13312\n"
    "v_fma_f32 v108, v108, v188, -v192\n"
    "v_fma_f32 v109, v109, v189, -v193\n"
    "v_fma_f32 v110, v110, v190, -v194\n"
    "v_fma_f32 v111, v111, v191, -v195\n"
    "v_fma_f32 v112, v112, v188, -v192\n"
    "v_fma_f32 v113, v113, v189, -v193\n"
    "v_fma_f32 v114, v114, v190, -v194\n"
    "s_add_u32 m0, s65, 50176\n"
    "s_add_u32 s72, s85, 13312\n"
    "buffer_load_dwordx4 v89, s[20:23], s72 offen offset:0 nt lds\n"
    "v_fma_f32 v115, v115, v191, -v195\n"
    "v_cvt_pk_bf16_f32 v108, v108, v109\n"
    "v_cvt_pk_bf16_f32 v109, v110, v111\n"
    "v_cvt_pk_bf16_f32 v110, v112, v113\n"
    "v_cvt_pk_bf16_f32 v111, v114, v115\n"
    "ds_write_b64 v163, v[108:109] offset:14336\n"
    "ds_write_b64 v163, v[110:111] offset:15360\n"
    "v_fma_f32 v116, v116, v196, -v200\n"
    "v_fma_f32 v117, v117, v197, -v201\n"
    "v_fma_f32 v118, v118, v198, -v202\n"
    "v_fma_f32 v119, v119, v199, -v203\n"
    "v_fma_f32 v120, v120, v196, -v200\n"
    "v_fma_f32 v121, v121, v197, -v201\n"
    "v_fma_f32 v122, v122, v198, -v202\n"
    "v_fma_f32 v123, v123, v199, -v203\n"
    "v_cvt_pk_bf16_f32 v116, v116, v117\n"
    "v_cvt_pk_bf16_f32 v117, v118, v119\n"
    "v_cvt_pk_bf16_f32 v118, v120, v121\n"
    "v_cvt_pk_bf16_f32 v119, v122, v123\n"
    "ds_write_b64 v163, v[116:117] offset:16384\n"
    "ds_write_b64 v163, v[118:119] offset:17408\n"
    "s_waitcnt vmcnt(19)\n"
    "ds_read_b128 v[36:39], v154 offset:0\n"
    "ds_read_b128 v[40:43], v155 offset:0\n"
    "ds_read_b128 v[44:47], v154 offset:128\n"
    "ds_read_b128 v[48:51], v155 offset:128\n"
    "ds_read_b128 v[52:55], v154 offset:256\n"
    "ds_read_b128 v[56:59], v155 offset:256\n"
    "s_waitcnt lgkmcnt(0)\n"
    "s_barrier\n"
    "ds_read_b128 a[112:115], v252 offset:0\n"
    "ds_read_b128 a[116:119], v252 offset:2048\n"
    "ds_read_b128 a[120:123], v252 offset:4096\n"
    "ds_read_b128 a[124:127], v252 offset:6144\n"
    "ds_read_b128 a[128:131], v252 offset:8192\n"
    "ds_read_b128 a[132:135], v252 offset:10240\n"
    "ds_read_b128 a[136:139], v252 offset:1024\n"
    "ds_read_b128 a[140:143], v252 offset:3072\n"
    "ds_read_b128 a[144:147], v252 offset:5120\n"
    "ds_read_b128 a[148:151], v252 offset:7168\n"
    "ds_read_b128 a[152:155], v252 offset:9216\n"
    "ds_read_b128 a[156:159], v252 offset:11264\n"
    "ds_read_b128 a[0:3], v252 offset:12288\n"
    "ds_read_b128 a[4:7], v252 offset:14336\n"
    "ds_read_b128 a[8:11], v252 offset:16384\n"
    "ds_read_b128 a[12:15], v252 offset:18432\n"
    "ds_read_b128 a[16:19], v252 offset:20480\n"
    "ds_read_b128 a[20:23], v252 offset:22528\n"
    "ds_read_b128 a[24:27], v252 offset:13312\n"
    "ds_read_b128 a[28:31], v252 offset:15360\n"
    "ds_read_b128 a[32:35], v252 offset:17408\n"
    "ds_read_b128 a[36:39], v252 offset:19456\n"
    "ds_read_b128 a[40:43], v252 offset:21504\n"
    "ds_read_b128 a[44:47], v252 offset:23552\n"
    "s_add_u32 m0, s65, 51200\n"
    "s_add_u32 s72, s85, 14336\n"
    "buffer_load_dwordx4 v90, s[20:23], s72 offen offset:0 nt lds\n"
    "s_add_u32 m0, s65, 52224\n"
    "s_add_u32 s72, s85, 15360\n"
    "buffer_load_dwordx4 v88, s[20:23], s72 offen offset:0 nt lds\n"
    "v_pk_maximum3_f16 v98, v36, v37, v98\n"
    "v_pk_maximum3_f16 v98, v38, v39, v98\n"
    "v_pk_add_u16 v36, s89, v36 clamp\n"
    "v_pk_add_u16 v37, s89, v37 clamp\n"
    "v_pk_add_u16 v38, s89, v38 clamp\n"
    "v_pk_add_u16 v39, s89, v39 clamp\n"
    "v_pk_maximum3_f16 v98, v40, v41, v98\n"
    "v_pk_maximum3_f16 v98, v42, v43, v98\n"
    "v_pk_add_u16 v40, s89, v40 clamp\n"
    "v_pk_add_u16 v41, s89, v41 clamp\n"
    "v_pk_add_u16 v42, s89, v42 clamp\n"
    "v_pk_add_u16 v43, s89, v43 clamp\n"
    "v_pk_maximum3_f16 v98, v44, v45, v98\n"
    "v_pk_maximum3_f16 v98, v46, v47, v98\n"
    "v_pk_add_u16 v44, s89, v44 clamp\n"
    "v_pk_add_u16 v45, s89, v45 clamp\n"
    "v_pk_add_u16 v46, s89, v46 clamp\n"
    "v_pk_add_u16 v47, s89, v47 clamp\n"
    "v_pk_maximum3_f16 v98, v48, v49, v98\n"
    "v_pk_maximum3_f16 v98, v50, v51, v98\n"
    "v_pk_add_u16 v48, s89, v48 clamp\n"
    "v_pk_add_u16 v49, s89, v49 clamp\n"
    "v_pk_add_u16 v50, s89, v50 clamp\n"
    "v_pk_add_u16 v51, s89, v51 clamp\n"
    "v_pk_maximum3_f16 v98, v52, v53, v98\n"
    "v_pk_maximum3_f16 v98, v54, v55, v98\n"
    "v_pk_add_u16 v52, s89, v52 clamp\n"
    "v_pk_add_u16 v53, s89, v53 clamp\n"
    "v_pk_add_u16 v54, s89, v54 clamp\n"
    "v_pk_add_u16 v55, s89, v55 clamp\n"
    "v_pk_maximum3_f16 v98, v56, v57, v98\n"
    "v_pk_maximum3_f16 v98, v58, v59, v98\n"
    "v_pk_add_u16 v56, s89, v56 clamp\n"
    "v_pk_add_u16 v57, s89, v57 clamp\n"
    "v_pk_add_u16 v58, s89, v58 clamp\n"
    "v_pk_add_u16 v59, s89, v59 clamp\n"
    "v_mov_b32 v108, 0\n"
    "v_mov_b32 v109, 0\n"
    "v_mov_b32 v110, 0\n"
    "v_mov_b32 v111, 0\n"
    "v_mov_b32 v112, 0\n"
    "v_mov_b32 v113, 0\n"
    "v_mov_b32 v114, 0\n"
    "v_mov_b32 v115, 0\n"
    "v_mov_b32 v116, 0\n"
    "v_mov_b32 v117, 0\n"
    "v_mov_b32 v118, 0\n"
    "v_mov_b32 v119, 0\n"
    "v_mov_b32 v120, 0\n"
    "v_mov_b32 v121, 0\n"
    "v_mov_b32 v122, 0\n"
    "v_mov_b32 v123, 0\n"
    "s_mul_i32 s73, s79, 768\n"
    "v_add_u32 v78, s73, v250\n"
    "v_mov_b32 v79, 155936\n"
    "s_waitcnt lgkmcnt(15)\n"
    "v_mfma_f32_16x16x32_bf16 v[4:7], a[112:115], v[36:39], 0\n"
    "s_waitcnt lgkmcnt(15)\n"
    "v_mfma_f32_16x16x32_bf16 v[4:7], a[116:119], v[40:43], v[4:7]\n"
    "s_waitcnt lgkmcnt(15)\n"
    "v_mfma_f32_16x16x32_bf16 v[4:7], a[120:123], v[44:47], v[4:7]\n"
    "s_waitcnt lgkmcnt(15)\n"
    "v_mfma_f32_16x16x32_bf16 v[4:7], a[124:127], v[48:51], v[4:7]\n"
    "s_waitcnt lgkmcnt(15)\n"
    "v_mfma_f32_16x16x32_bf16 v[4:7], a[128:131], v[52:55], v[4:7]\n"
    "s_waitcnt lgkmcnt(15)\n"
    "v_mfma_f32_16x16x32_bf16 v[4:7], a[132:135], v[56:59], v[4:7]\n"
    "s_waitcnt lgkmcnt(15)\n"
    "v_mfma_f32_16x16x32_bf16 v[20:23], a[136:139], v[36:39], 0\n"
    "v_accvgpr_write_b32 a192, v36\n"
    "v_accvgpr_write_b32 a193, v37\n"
    "v_accvgpr_write_b32 a194, v38\n"
    "v_accvgpr_write_b32 a195, v39\n"
    "s_waitcnt lgkmcnt(15)\n"
    "v_mfma_f32_16x16x32_bf16 v[20:23], a[140:143], v[40:43], v[20:23]\n"
    "v_accvgpr_write_b32 a196, v40\n"
    "v_accvgpr_write_b32 a197, v41\n"
    "v_accvgpr_write_b32 a198, v42\n"
    "v_accvgpr_write_b32 a199, v43\n"
    "s_waitcnt lgkmcnt(15)\n"
    "v_mfma_f32_16x16x32_bf16 v[20:23], a[144:147], v[44:47], v[20:23]\n"
    "v_accvgpr_write_b32 a200, v44\n"
    "v_accvgpr_write_b32 a201, v45\n"
    "v_accvgpr_write_b32 a202, v46\n"
    "v_accvgpr_write_b32 a203, v47\n"
    "s_waitcnt lgkmcnt(14)\n"
    "v_mfma_f32_16x16x32_bf16 v[20:23], a[148:151], v[48:51], v[20:23]\n"
    "v_accvgpr_write_b32 a204, v48\n"
    "v_accvgpr_write_b32 a205, v49\n"
    "v_accvgpr_write_b32 a206, v50\n"
    "v_accvgpr_write_b32 a207, v51\n"
    "s_waitcnt lgkmcnt(13)\n"
    "v_mfma_f32_16x16x32_bf16 v[20:23], a[152:155], v[52:55], v[20:23]\n"
    "v_accvgpr_write_b32 a208, v52\n"
    "v_accvgpr_write_b32 a209, v53\n"
    "v_accvgpr_write_b32 a210, v54\n"
    "v_accvgpr_write_b32 a211, v55\n"
    "s_waitcnt lgkmcnt(12)\n"
    "v_mfma_f32_16x16x32_bf16 v[20:23], a[156:159], v[56:59], v[20:23]\n"
    "v_accvgpr_write_b32 a212, v56\n"
    "v_accvgpr_write_b32 a213, v57\n"
    "v_accvgpr_write_b32 a214, v58\n"
    "v_accvgpr_write_b32 a215, v59\n"
    "s_add_u32 m0, s65, 53248\n"
    "s_add_u32 s72, s85, 16384\n"
    "buffer_load_dwordx4 v89, s[20:23], s72 offen offset:0 nt lds\n"
    "buffer_load_dwordx2 v[236:237], v87, s[36:39], 0 offen offset:0 sc1\n"
    "ds_read_b128 v[16:19], v78 offset:0\n"
    "ds_read_b128 v[32:35], v78 offset:16\n"
    "ds_read_b128 v[48:51], v78 offset:128\n"
    "ds_read_b128 v[64:67], v78 offset:144\n"
    "ds_read_b128 v[100:103], v78 offset:768\n"
    "ds_read_b128 v[104:107], v78 offset:784\n"
    "s_add_u32 m0, s65, 54272\n"
    "s_add_u32 s72, s85, 17408\n"
    "buffer_load_dwordx4 v90, s[20:23], s72 offen offset:0 nt lds\n"
    "s_add_u32 m0, s65, 98304\n"
    "s_add_u32 s72, s85, 18432\n"
    "buffer_load_dwordx4 v88, s[20:23], s72 offen offset:0 nt lds\n"
    "s_waitcnt lgkmcnt(0)\n"
    "s_barrier\n"
    "ds_read_b128 v[80:83], v79 offset:0\n"
    "s_waitcnt vmcnt(14)\n"
    "buffer_store_dwordx2 v[238:239], v240, s[36:39], s80 offen offset:0 sc1\n"
    "s_lshl_b32 s73, s79, 8\n"
    "s_add_u32 s80, s80, s73\n"
    "s_mul_i32 s73, s79, 98304\n"
    "s_add_u32 s71, s71, s73\n"
    "s_add_u32 m0, s65, 99328\n"
    "s_add_u32 s72, s85, 19456\n"
    "buffer_load_dwordx4 v89, s[20:23], s72 offen offset:0 nt lds\n"
    "s_waitcnt lgkmcnt(0)\n"
    "v_max3_f32 v76, v80, v81, v82\n"
    "v_max_f32 v76, v76, v83\n"
    "v_add_f32 v76, 0xc2b00000, v76\n"
    "s_add_u32 m0, s65, 100352\n"
    "s_add_u32 s72, s85, 20480\n"
    "buffer_load_dwordx4 v90, s[20:23], s72 offen offset:0 nt lds\n"
    "s_add_u32 m0, s65, 101376\n"
    "s_add_u32 s72, s85, 21504\n"
    "buffer_load_dwordx4 v88, s[20:23], s72 offen offset:0 nt lds\n"
    "s_add_u32 m0, s65, 102400\n"
    "s_add_u32 s72, s85, 22528\n"
    "buffer_load_dwordx4 v89, s[20:23], s72 offen offset:0 nt lds\n"
    "s_add_u32 m0, s65, 103424\n"
    "s_add_u32 s72, s85, 23552\n"
    "buffer_load_dwordx4 v90, s[20:23], s72 offen offset:0 nt lds\n"
    "v_sub_f32 v48, v48, v76\n"
    "v_sub_f32 v51, v51, v76\n"
    "v_sub_f32 v49, v49, v76\n"
    "v_sub_f32 v50, v50, v76\n"
    "v_sub_f32 v66, v66, v76\n"
    "v_sub_f32 v65, v65, v76\n"
    "v_mfma_f32_16x16x32_bf16 v[36:39], a[0:3], a[192:195], v[48:51]\n"
    "s_waitcnt vmcnt(14)\n"
    "ds_read_b128 v[200:203], v97 offset:24832\n"
    "v_sub_f32 v67, v67, v76\n"
    "v_mfma_f32_16x16x32_bf16 v[36:39], a[4:7], a[196:199], v[36:39]\n"
    "v_sub_f32 v33, v33, v76\n"
    "v_sub_f32 v32, v32, v76\n"
    "v_mfma_f32_16x16x32_bf16 v[36:39], a[8:11], a[200:203], v[36:39]\n"
    "ds_read_b128 v[184:187], v97 offset:24576\n"
    "ds_read_b128 v[196:199], v96 offset:24832\n"
    "ds_read_b128 v[192:195], v97 offset:24704\n"
    "v_mfma_f32_16x16x32_bf16 v[36:39], a[12:15], a[204:207], v[36:39]\n"
    "v_sub_f32 v64, v64, v76\n"
    "v_sub_f32 v35, v35, v76\n"
    "v_sub_f32 v19, v19, v76\n"
    "v_mfma_f32_16x16x32_bf16 v[52:55], a[24:27], a[192:195], v[64:67]\n"
    "v_add_f32 v21, v21, v33\n"
    "v_sub_f32 v16, v16, v76\n"
    "v_mfma_f32_16x16x32_bf16 v[36:39], a[16:19], a[208:211], v[36:39]\n"
    "v_sub_f32 v18, v18, v76\n"
    "v_add_f32 v20, v20, v32\n"
    "v_sub_f32 v17, v17, v76\n"
    "v_mfma_f32_16x16x32_bf16 v[52:55], a[28:31], a[196:199], v[52:55]\n"
    "ds_read_b128 v[188:191], v96 offset:24704\n"
    "ds_read_b128 v[180:183], v96 offset:24576\n"
    "v_add_f32 v6, v6, v18\n"
    "v_sub_f32 v34, v34, v76\n"
    "v_mfma_f32_16x16x32_bf16 v[36:39], a[20:23], a[212:215], v[36:39]\n"
    "s_waitcnt lgkmcnt(5)\n"
    "v_pk_maximum3_f16 v98, v200, v201, v98\n"
    "v_exp_f32 v20, v20\n"
    "v_pk_maximum3_f16 v98, v202, v203, v98\n"
    "v_pk_add_u16 v203, s89, v203 clamp\n"
    "v_pk_add_u16 v200, s89, v200 clamp\n"
    "v_mfma_f32_16x16x32_bf16 v[52:55], a[32:35], a[200:203], v[52:55]\n"
    "s_waitcnt lgkmcnt(3)\n"
    "v_pk_maximum3_f16 v98, v196, v197, v98\n"
    "v_pk_maximum3_f16 v98, v198, v199, v98\n"
    "v_mfma_f32_16x16x32_bf16 v[52:55], a[36:39], a[204:207], v[52:55]\n"
    "v_exp_f32 v21, v21\n"
    "v_pk_add_u16 v202, s89, v202 clamp\n"
    "v_pk_add_u16 v201, s89, v201 clamp\n"
    "v_mfma_f32_16x16x32_bf16 v[52:55], a[40:43], a[208:211], v[52:55]\n"
    "v_add_f32 v22, v22, v34\n"
    "v_add_f32 v7, v7, v19\n"
    "v_mfma_f32_16x16x32_bf16 v[24:27], a[156:159], v[200:203], v[32:35]\n"
    "s_waitcnt lgkmcnt(2)\n"
    "v_pk_maximum3_f16 v98, v192, v193, v98\n"
    "v_pk_maximum3_f16 v98, v194, v195, v98\n"
    "v_pk_add_u16 v193, s89, v193 clamp\n"
    "v_pk_add_u16 v198, s89, v198 clamp\n"
    "v_add_f32 v5, v5, v17\n"
    "v_mfma_f32_16x16x32_bf16 v[52:55], a[44:47], a[212:215], v[52:55]\n"
    "s_waitcnt lgkmcnt(1)\n"
    "v_pk_add_u16 v194, s89, v194 clamp\n"
    "v_sub_f32 v69, v69, v76\n"
    "v_sub_f32 v73, v73, v76\n"
    "v_pk_maximum3_f16 v98, v188, v189, v98\n"
    "v_pk_maximum3_f16 v98, v190, v191, v98\n"
    "v_pk_add_u16 v196, s89, v196 clamp\n"
    "v_pk_maximum3_f16 v98, v184, v185, v98\n"
    "v_pk_add_u16 v197, s89, v197 clamp\n"
    "v_pk_maximum3_f16 v98, v186, v187, v98\n"
    "v_mfma_f32_16x16x32_bf16 v[8:11], a[132:135], v[200:203], v[16:19]\n"
    "v_sub_f32 v75, v75, v76\n"
    "v_pk_add_u16 v199, s89, v199 clamp\n"
    "v_sub_f32 v101, v101, v76\n"
    "v_exp_f32 v54, v54\n"
    "v_mfma_f32_16x16x32_bf16 v[24:27], a[152:155], v[196:199], v[24:27]\n"
    "s_waitcnt lgkmcnt(0)\n"
    "v_pk_add_u16 v188, s89, v188 clamp\n"
    "v_pk_add_u16 v184, s89, v184 clamp\n"
    "v_sub_f32 v70, v70, v76\n"
    "v_pk_maximum3_f16 v98, v180, v181, v98\n"
    "v_pk_add_u16 v186, s89, v186 clamp\n"
    "v_mfma_f32_16x16x32_bf16 v[56:59], a[44:47], v[200:203], v[64:67]\n"
    "v_pk_add_u16 v189, s89, v189 clamp\n"
    "v_pk_add_u16 v195, s89, v195 clamp\n"
    "v_pk_add_u16 v192, s89, v192 clamp\n"
    "v_pk_add_u16 v185, s89, v185 clamp\n"
    "v_pk_maximum3_f16 v98, v182, v183, v98\n"
    "v_exp_f32 v55, v55\n"
    "v_sub_f32 v68, v68, v76\n"
    "v_mfma_f32_16x16x32_bf16 v[56:59], a[40:43], v[196:199], v[56:59]\n"
    "v_exp_f32 v52, v52\n"
    "v_pk_add_u16 v190, s89, v190 clamp\n"
    "v_mfma_f32_16x16x32_bf16 v[8:11], a[128:131], v[196:199], v[8:11]\n"
    "v_accvgpr_write_b32 a231, v195\n"
    "v_sub_f32 v102, v102, v76\n"
    "v_sub_f32 v71, v71, v76\n"
    "v_sub_f32 v107, v107, v76\n"
    "v_mfma_f32_16x16x32_bf16 v[40:43], a[20:23], v[200:203], v[48:51]\n"
    "v_sub_f32 v74, v74, v76\n"
    "v_add_f32 v23, v23, v35\n"
    "v_mfma_f32_16x16x32_bf16 v[40:43], a[16:19], v[196:199], v[40:43]\n"
    "v_pk_add_u16 v183, s89, v183 clamp\n"
    "v_accvgpr_write_b32 a228, v192\n"
    "v_mfma_f32_16x16x32_bf16 v[56:59], a[36:39], v[192:195], v[56:59]\n"
    "v_add_f32 v4, v4, v16\n"
    "v_exp_f32 v6, v6\n"
    "v_mfma_f32_16x16x32_bf16 v[24:27], a[148:151], v[192:195], v[24:27]\n"
    "v_sub_f32 v72, v72, v76\n"
    "v_pk_add_u16 v187, s89, v187 clamp\n"
    "v_pk_add_u16 v191, s89, v191 clamp\n"
    "v_mfma_f32_16x16x32_bf16 v[8:11], a[124:127], v[192:195], v[8:11]\n"
    "v_exp_f32 v53, v53\n"
    "v_accvgpr_write_b32 a226, v190\n"
    "v_mfma_f32_16x16x32_bf16 v[8:11], a[120:123], v[188:191], v[8:11]\n"
    "v_accvgpr_write_b32 a221, v185\n"
    "v_exp_f32 v39, v39\n"
    "v_exp_f32 v22, v22\n"
    "v_pk_add_u16 v181, s89, v181 clamp\n"
    "v_mfma_f32_16x16x32_bf16 v[56:59], a[32:35], v[188:191], v[56:59]\n"
    "v_exp_f32 v36, v36\n"
    "v_pk_add_u16 v182, s89, v182 clamp\n"
    "v_exp_f32 v23, v23\n"
    "v_mfma_f32_16x16x32_bf16 v[24:27], a[144:147], v[188:191], v[24:27]\n"
    "v_accvgpr_write_b32 a237, v201\n"
    "v_pk_add_u16 v180, s89, v180 clamp\n"
    "v_mfma_f32_16x16x32_bf16 v[40:43], a[12:15], v[192:195], v[40:43]\n"
    "v_accvgpr_write_b32 a239, v203\n"
    "v_accvgpr_write_b32 a223, v187\n"
    "v_exp_f32 v7, v7\n"
    "v_accvgpr_write_b32 a233, v197\n"
    "v_mfma_f32_16x16x32_bf16 v[8:11], a[116:119], v[184:187], v[8:11]\n"
    "v_accvgpr_write_b32 a232, v196\n"
    "v_accvgpr_write_b32 a235, v199\n"
    "v_mfma_f32_16x16x32_bf16 v[40:43], a[8:11], v[188:191], v[40:43]\n"
    "ds_write_b128 v86, v[68:71] offset:0\n"
    "v_mfma_f32_16x16x32_bf16 v[40:43], a[4:7], v[184:187], v[40:43]\n"
    "v_sub_f32 v106, v106, v76\n"
    "v_accvgpr_write_b32 a234, v198\n"
    "v_mfma_f32_16x16x32_bf16 v[8:11], a[112:115], v[180:183], v[8:11]\n"
    "v_cvt_pk_bf16_f32 v70, v20, v21\n"
    "v_exp_f32 v37, v37\n"
    "v_cvt_pk_bf16_f32 v69, v6, v7\n"
    "v_accvgpr_write_b32 a218, v182\n"
    "v_mfma_f32_16x16x32_bf16 v[40:43], a[0:3], v[180:183], v[40:43]\n"
    "ds_write_b128 v86, v[72:75] offset:16\n"
    "v_accvgpr_write_b32 a230, v194\n"
    "v_mfma_f32_16x16x32_bf16 v[24:27], a[140:143], v[184:187], v[24:27]\n"
    "v_exp_f32 v10, v10\n"
    "v_mfma_f32_16x16x32_bf16 v[56:59], a[28:31], v[184:187], v[56:59]\n"
    "v_cvt_pk_bf16_f32 v72, v36, v37\n"
    "v_sub_f32 v103, v103, v76\n"
    "v_mfma_f32_16x16x32_bf16 v[56:59], a[24:27], v[180:183], v[56:59]\n"
    "v_sub_f32 v104, v104, v76\n"
    "v_accvgpr_write_b32 a217, v181\n"
    "v_exp_f32 v4, v4\n"
    "v_sub_f32 v100, v100, v76\n"
    "v_accvgpr_write_b32 a227, v191\n"
    "v_exp_f32 v11, v11\n"
    "v_exp_f32 v5, v5\n"
    "v_exp_f32 v38, v38\n"
    "v_exp_f32 v41, v41\n"
    "v_exp_f32 v9, v9\n"
    "v_exp_f32 v59, v59\n"
    "v_accvgpr_write_b32 a220, v184\n"
    "v_accvgpr_write_b32 a238, v202\n"
    "v_accvgpr_write_b32 a236, v200\n"
    "v_exp_f32 v43, v43\n"
    "v_cvt_pk_bf16_f32 v77, v10, v11\n"
    "v_exp_f32 v8, v8\n"
    "v_cvt_pk_bf16_f32 v75, v54, v55\n"
    "v_mfma_f32_16x16x32_bf16 v[24:27], a[136:139], v[180:183], v[24:27]\n"
    "v_cvt_pk_bf16_f32 v71, v22, v23\n"
    "v_sub_f32 v105, v105, v76\n"
    "v_accvgpr_write_b32 a216, v180\n"
    "v_exp_f32 v58, v58\n"
    "v_cvt_pk_bf16_f32 v73, v38, v39\n"
    "v_exp_f32 v40, v40\n"
    "v_accvgpr_write_b32 a222, v186\n"
    "v_accvgpr_write_b32 a225, v189\n"
    "v_exp_f32 v24, v24\n"
    "v_exp_f32 v57, v57\n"
    "v_cvt_pk_bf16_f32 v68, v4, v5\n"
    "v_cvt_pk_bf16_f32 v80, v40, v41\n"
    "v_exp_f32 v27, v27\n"
    "v_exp_f32 v25, v25\n"
    "v_cvt_pk_bf16_f32 v76, v8, v9\n"
    "v_accvgpr_write_b32 a224, v188\n"
    "v_accvgpr_write_b32 a219, v183\n"
    "v_exp_f32 v26, v26\n"
    "v_exp_f32 v42, v42\n"
    "v_cvt_pk_bf16_f32 v78, v24, v25\n"
    "v_exp_f32 v56, v56\n"
    "v_cvt_pk_bf16_f32 v74, v52, v53\n"
    "v_accvgpr_write_b32 a229, v193\n"
    "v_cvt_pk_bf16_f32 v79, v26, v27\n"
    "v_cvt_pk_bf16_f32 v82, v56, v57\n"
    "v_cvt_pk_bf16_f32 v83, v58, v59\n"
    "v_cvt_pk_bf16_f32 v81, v42, v43\n"
    "s_waitcnt vmcnt(8)\n"
    "v_cmp_eq_u64 s[54:55], v[236:237], v[238:239]\n"
    "s_nop 0\n"
    "s_cmp_eq_u64 s[54:55], -1\n"
    "s_cbranch_scc0 .LpollAK\n"
    ".LpollAKr:\n"
    "s_add_u32 m0, s65, 24576\n"
    "s_add_u32 s72, s65, 49152\n"
    "buffer_load_dwordx4 v1, s[16:19], s72 offen offset:0 sc1 lds\n"
    "s_add_u32 m0, s65, 25600\n"
    "s_add_u32 s72, s65, 50176\n"
    "buffer_load_dwordx4 v1, s[16:19], s72 offen offset:0 sc1 lds\n"
    "s_add_u32 m0, s65, 26624\n"
    "s_add_u32 s72, s65, 51200\n"
    "buffer_load_dwordx4 v1, s[16:19], s72 offen offset:0 sc1 lds\n"
    "s_add_u32 m0, s65, 27648\n"
    "s_add_u32 s72, s65, 52224\n"
    "buffer_load_dwordx4 v1, s[16:19], s72 offen offset:0 sc1 lds\n"
    "s_add_u32 m0, s65, 28672\n"
    "s_add_u32 s72, s65, 53248\n"
    "buffer_load_dwordx4 v1, s[16:19], s72 offen offset:0 sc1 lds\n"
    "s_add_u32 m0, s65, 29696\n"
    "s_add_u32 s72, s65, 54272\n"
    "buffer_load_dwordx4 v1, s[16:19], s72 offen offset:0 sc1 lds\n"
    "s_waitcnt vmcnt(13)\n"
    "ds_read_b128 v[156:159], v96 offset:0\n"
    "ds_read_b128 v[172:175], v96 offset:256\n"
    "ds_read_b128 v[168:171], v97 offset:128\n"
    "ds_read_b128 v[176:179], v97 offset:256\n"
    "ds_read_b128 v[160:163], v97 offset:0\n"
    "ds_read_b128 v[164:167], v96 offset:128\n"
    "s_waitcnt lgkmcnt(5)\n"
    "v_pk_maximum3_f16 v98, v156, v157, v98\n"
    "v_pk_maximum3_f16 v98, v158, v159, v98\n"
    "v_pk_add_u16 v157, s89, v157 clamp\n"
    "v_pk_add_u16 v158, s89, v158 clamp\n"
    "v_pk_add_u16 v156, s89, v156 clamp\n"
    "v_pk_add_u16 v159, s89, v159 clamp\n"
    "s_waitcnt lgkmcnt(1)\n"
    "v_pk_maximum3_f16 v98, v160, v161, v98\n"
    "s_add_u32 m0, s65, 73728\n"
    "s_add_u32 s72, s65, 73728\n"
    "buffer_load_dwordx4 v1, s[16:19], s72 offen offset:0 sc1 lds\n"
    "v_mfma_f32_16x16x32_bf16 v[28:31], a[136:139], v[156:159], v[32:35]\n"
    "s_waitcnt lgkmcnt(0)\n"
    "v_pk_maximum3_f16 v98, v162, v163, v98\n"
    "v_pk_maximum3_f16 v98, v164, v165, v98\n"
    "v_mfma_f32_16x16x32_bf16 v[12:15], a[112:115], v[156:159], v[16:19]\n"
    "v_pk_add_u16 v161, s89, v161 clamp\n"
    "v_pk_maximum3_f16 v98, v166, v167, v98\n"
    "v_pk_add_u16 v160, s89, v160 clamp\n"
    "v_pk_add_u16 v162, s89, v162 clamp\n"
    "v_mfma_f32_16x16x32_bf16 v[60:63], a[24:27], v[156:159], v[64:67]\n"
    "v_pk_maximum3_f16 v98, v168, v169, v98\n"
    "v_pk_add_u16 v163, s89, v163 clamp\n"
    "v_mfma_f32_16x16x32_bf16 v[44:47], a[0:3], v[156:159], v[48:51]\n"
    "v_pk_maximum3_f16 v98, v170, v171, v98\n"
    "v_pk_add_u16 v164, s89, v164 clamp\n"
    "v_mfma_f32_16x16x32_bf16 v[12:15], a[116:119], v[160:163], v[12:15]\n"
    "v_pk_add_u16 v165, s89, v165 clamp\n"
    "s_add_u32 m0, s65, 74752\n"
    "s_add_u32 s72, s65, 74752\n"
    "buffer_load_dwordx4 v1, s[16:19], s72 offen offset:0 sc1 lds\n"
    "v_pk_add_u16 v169, s89, v169 clamp\n"
    "v_pk_add_u16 v168, s89, v168 clamp\n"
    "v_mfma_f32_16x16x32_bf16 v[60:63], a[28:31], v[160:163], v[60:63]\n"
    "v_pk_maximum3_f16 v98, v172, v173, v98\n"
    "v_pk_add_u16 v170, s89, v170 clamp\n"
    "v_mfma_f32_16x16x32_bf16 v[28:31], a[140:143], v[160:163], v[28:31]\n"
    "v_pk_maximum3_f16 v98, v174, v175, v98\n"
    "v_pk_add_u16 v166, s89, v166 clamp\n"
    "v_pk_add_u16 v167, s89, v167 clamp\n"
    "v_mfma_f32_16x16x32_bf16 v[44:47], a[4:7], v[160:163], v[44:47]\n"
    "v_pk_maximum3_f16 v98, v176, v177, v98\n"
    "v_accvgpr_write_b32 a251, v167\n"
    "v_mfma_f32_16x16x32_bf16 v[44:47], a[8:11], v[164:167], v[44:47]\n"
    "v_pk_maximum3_f16 v98, v178, v179, v98\n"
    "v_pk_add_u16 v176, s89, v176 clamp\n"
    "v_mfma_f32_16x16x32_bf16 v[60:63], a[32:35], v[164:167], v[60:63]\n"
    "v_pk_add_u16 v171, s89, v171 clamp\n"
    "s_add_u32 m0, s65, 75776\n"
    "s_add_u32 s72, s65, 75776\n"
    "buffer_load_dwordx4 v1, s[16:19], s72 offen offset:0 sc1 lds\n"
    "v_pk_add_u16 v174, s89, v174 clamp\n"
    "v_mfma_f32_16x16x32_bf16 v[28:31], a[144:147], v[164:167], v[28:31]\n"
    "v_pk_add_u16 v173, s89, v173 clamp\n"
    "v_pk_add_u16 v172, s89, v172 clamp\n"
    "v_mfma_f32_16x16x32_bf16 v[12:15], a[120:123], v[164:167], v[12:15]\n"
    "v_accvgpr_write_b32 a254, v170\n"
    "v_pk_add_u16 v178, s89, v178 clamp\n"
    "v_mfma_f32_16x16x32_bf16 v[28:31], a[148:151], v[168:171], v[28:31]\n"
    "v_accvgpr_write_b32 a243, v159\n"
    "v_accvgpr_write_b32 a162, v174\n"
    "v_mfma_f32_16x16x32_bf16 v[12:15], a[124:127], v[168:171], v[12:15]\n"
    "v_pk_add_u16 v179, s89, v179 clamp\n"
    "v_pk_add_u16 v175, s89, v175 clamp\n"
    "v_mfma_f32_16x16x32_bf16 v[44:47], a[12:15], v[168:171], v[44:47]\n"
    "v_accvgpr_write_b32 a166, v178\n"
    "v_accvgpr_write_b32 a161, v173\n"
    "v_accvgpr_write_b32 a245, v161\n"
    "s_add_u32 m0, s65, 76800\n"
    "s_add_u32 s72, s65, 76800\n"
    "buffer_load_dwordx4 v1, s[16:19], s72 offen offset:0 sc1 lds\n"
    "v_mfma_f32_16x16x32_bf16 v[28:31], a[152:155], v[172:175], v[28:31]\n"
    "v_accvgpr_write_b32 a163, v175\n"
    "v_pk_add_u16 v177, s89, v177 clamp\n"
    "v_mfma_f32_16x16x32_bf16 v[44:47], a[16:19], v[172:175], v[44:47]\n"
    "v_accvgpr_write_b32 a160, v172\n"
    "v_accvgpr_write_b32 a252, v168\n"
    "v_mfma_f32_16x16x32_bf16 v[28:31], a[156:159], v[176:179], v[28:31]\n"
    "v_accvgpr_write_b32 a249, v165\n"
    "v_accvgpr_write_b32 a255, v171\n"
    "v_mfma_f32_16x16x32_bf16 v[12:15], a[128:131], v[172:175], v[12:15]\n"
    "v_accvgpr_write_b32 a247, v163\n"
    "v_accvgpr_write_b32 a164, v176\n"
    "v_mfma_f32_16x16x32_bf16 v[60:63], a[36:39], v[168:171], v[60:63]\n"
    "v_accvgpr_write_b32 a242, v158\n"
    "v_accvgpr_write_b32 a250, v166\n"
    "v_mfma_f32_16x16x32_bf16 v[44:47], a[20:23], v[176:179], v[44:47]\n"
    "v_accvgpr_write_b32 a241, v157\n"
    "s_add_u32 m0, s65, 77824\n"
    "s_add_u32 s72, s65, 77824\n"
    "buffer_load_dwordx4 v1, s[16:19], s72 offen offset:0 sc1 lds\n"
    "v_accvgpr_write_b32 a248, v164\n"
    "v_mfma_f32_16x16x32_bf16 v[12:15], a[132:135], v[176:179], v[12:15]\n"
    "v_accvgpr_write_b32 a246, v162\n"
    "v_exp_f32 v29, v29\n"
    "v_mfma_f32_16x16x32_bf16 v[60:63], a[40:43], v[172:175], v[60:63]\n"
    "v_exp_f32 v28, v28\n"
    "v_mfma_f32_16x16x32_bf16 v[60:63], a[44:47], v[176:179], v[60:63]\n"
    "v_exp_f32 v31, v31\n"
    "v_exp_f32 v30, v30\n"
    "v_exp_f32 v45, v45\n"
    "v_cvt_pk_bf16_f32 v86, v28, v29\n"
    "v_exp_f32 v47, v47\n"
    "v_accvgpr_write_b32 a240, v156\n"
    "v_exp_f32 v12, v12\n"
    "v_exp_f32 v14, v14\n"
    "v_accvgpr_write_b32 a167, v179\n"
    "v_exp_f32 v15, v15\n"
    "s_add_u32 m0, s65, 78848\n"
    "s_add_u32 s72, s65, 78848\n"
    "buffer_load_dwordx4 v1, s[16:19], s72 offen offset:0 sc1 lds\n"
    "v_accvgpr_write_b32 a253, v169\n"
    "v_exp_f32 v63, v63\n"
    "v_exp_f32 v13, v13\n"
    "v_exp_f32 v44, v44\n"
    "v_cvt_pk_bf16_f32 v84, v12, v13\n"
    "v_cvt_pk_bf16_f32 v87, v30, v31\n"
    "v_exp_f32 v46, v46\n"
    "v_exp_f32 v61, v61\n"
    "v_accvgpr_write_b32 a165, v177\n"
    "v_exp_f32 v60, v60\n"
    "v_exp_f32 v62, v62\n"
    "v_cvt_pk_bf16_f32 v90, v60, v61\n"
    "v_accvgpr_write_b32 a244, v160\n"
    "v_cvt_pk_bf16_f32 v85, v14, v15\n"
    "v_cvt_pk_bf16_f32 v89, v46, v47\n"
    "v_cvt_pk_bf16_f32 v88, v44, v45\n"
    "v_cvt_pk_bf16_f32 v91, v62, v63\n"
    "s_bitcmp1_b32 s83, 1\n"
    "s_cbranch_scc1 .Lf3b\n"
    "s_mov_b32 s66, 1\n"
    "s_add_u32 s68, s68, 24576\n"
    "s_add_u32 s81, s81, 128\n"
    "s_add_u32 s80, s80, 256\n"
    "s_add_u32 s71, s71, 98304\n"
    "s_add_u32 s75, s71, s63\n"
    "s_add_u32 s76, s75, 6144\n"
    "s_add_u32 s77, s75, 24576\n"
    "s_add_u32 s78, s75, 30720\n"
    "s_branch .LhKP\n"
    "s_branch .Lf3e\n"
    ".Lf3b:\n"
    "s_add_u32 s75, s71, s63\n"
    "s_add_u32 s76, s75, 6144\n"
    "s_add_u32 s77, s75, 24576\n"
    "s_add_u32 s78, s75, 30720\n"
    "buffer_load_dwordx2 v[204:205], v242, s[28:31], s75 offen offset:0 nt\n"
    "buffer_load_dwordx2 v[206:207], v242, s[28:31], s75 offen offset:768 nt\n"
    "buffer_load_dwordx2 v[208:209], v242, s[28:31], s75 offen offset:3072 nt\n"
    "buffer_load_dwordx2 v[210:211], v242, s[28:31], s75 offen offset:3840 nt\n"
    "buffer_load_dwordx2 v[212:213], v242, s[28:31], s76 offen offset:0 nt\n"
    "buffer_load_dwordx2 v[214:215], v242, s[28:31], s76 offen offset:768 nt\n"
    "buffer_load_dwordx2 v[216:217], v242, s[28:31], s76 offen offset:3072 nt\n"
    "buffer_load_dwordx2 v[218:219], v242, s[28:31], s76 offen offset:3840 nt\n"
    "buffer_load_dwordx2 v[220:221], v242, s[28:31], s77 offen offset:0 nt\n"
    "buffer_load_dwordx2 v[222:223], v242, s[28:31], s77 offen offset:768 nt\n"
    "buffer_load_dwordx2 v[224:225], v242, s[28:31], s77 offen offset:3072 nt\n"
    "buffer_load_dwordx2 v[226:227], v242, s[28:31], s77 offen offset:3840 nt\n"
    "buffer_load_dwordx2 v[228:229], v242, s[28:31], s78 offen offset:0 nt\n"
    "buffer_load_dwordx2 v[230:231], v242, s[28:31], s78 offen offset:768 nt\n"
    "buffer_load_dwordx2 v[232:233], v242, s[28:31], s78 offen offset:3072 nt\n"
    "buffer_load_dwordx2 v[234:235], v242, s[28:31], s78 offen offset:3840 nt\n"
    "s_mov_b32 s66, 1\n"
    "s_add_u32 s68, s68, 24576\n"
    "s_add_u32 s81, s81, 128\n"
    "s_branch .LhKN\n"
    ".Lf3e:\n"
    "s_nop 4\n"
    "s_branch .Lf5e\n"
    ".Lf5b:\n"
    "s_lshl_b32 s73, s79, 8\n"
    "s_mul_i32 s74, s59, 6144\n"
    "s_add_u32 s73, s73, s74\n"
    "v_add_u32 v163, s73, v244\n"
    "s_lshr_b32 s73, s53, 2\n"
    "s_mul_i32 s73, s73, 12288\n"
    "s_and_b32 s74, s53, 3\n"
    "s_lshl_b32 s74, s74, 8\n"
    "s_add_u32 s73, s73, s74\n"
    "s_cmp_eq_u32 s59, 0\n"
    "s_cselect_b32 s73, 0x7f000000, s73\n"
    "s_add_u32 s60, s73, 24576\n"
    "s_add_u32 s61, s73, 28672\n"
    "s_add_u32 s62, s73, 32768\n"
    "v_mov_b32 v244, v243\n"
    "v_mov_b32 v243, 0\n"
    "buffer_load_dwordx4 v[40:43], v242, s[40:43], s75 offen offset:768 nt\n"
    "v_add_u32 v159, 0, v156\n"
    "v_mul_u32_u24 v159, 2731, v159\n"
    "v_lshrrev_b32 v159, 16, v159\n"
    "v_xor_b32 v159, v159, v156\n"
    "v_and_b32 v159, 7, v159\n"
    "v_and_b32 v88, 0x380, v1\n"
    "v_lshl_add_u32 v88, v159, 4, v88\n"
    "v_add_u32 v159, 64, v156\n"
    "v_mul_u32_u24 v159, 2731, v159\n"
    "v_lshrrev_b32 v159, 16, v159\n"
    "v_xor_b32 v159, v159, v156\n"
    "v_and_b32 v159, 7, v159\n"
    "v_and_b32 v89, 0x380, v1\n"
    "v_lshl_add_u32 v89, v159, 4, v89\n"
    "v_add_u32 v159, 128, v156\n"
    "v_mul_u32_u24 v159, 2731, v159\n"
    "v_lshrrev_b32 v159, 16, v159\n"
    "v_xor_b32 v159, v159, v156\n"
    "v_and_b32 v159, 7, v159\n"
    "v_and_b32 v90, 0x380, v1\n"
    "v_lshl_add_u32 v90, v159, 4, v90\n"
    "v_and_b32 v2, 7, v156\n"
    "v_lshrrev_b32 v144, 7, v130\n"
    "v_mul_u32_u24 v144, 768, v144\n"
    "v_lshl_add_u32 v144, v2, 3, v144\n"
    "buffer_load_dwordx4 v[44:47], v242, s[40:43], s75 offen offset:256 nt\n"
    "v_add_u32 v144, 131072, v144\n"
    "v_mad_u32_u24 v86, v2, 24, v144\n"
    "v_and_b32 v96, 15, v156\n"
    "v_lshrrev_b32 v159, 4, v156\n"
    "v_xor_b32 v159, v159, v2\n"
    "v_mul_u32_u24 v96, 384, v96\n"
    "v_lshl_add_u32 v96, v159, 4, v96\n"
    "s_add_u32 s73, s65, 49152\n"
    "v_add_u32 v96, s73, v96\n"
    "v_xor_b32 v97, 64, v96\n"
    "v_subrev_u32 v154, 24576, v96\n"
    "v_subrev_u32 v155, 24576, v97\n"
    "s_add_u32 s67, s65, 98304\n"
    "s_add_u32 s68, s65, 24576\n"
    "s_add_u32 s69, s65, 73728\n"
    "s_mov_b32 s81, 256\n"
    "s_mul_i32 s74, s79, 24576\n"
    "s_add_u32 s70, s74, s65\n"
    "s_add_u32 s70, s70, 73728\n"
    "s_sub_u32 s64, s65, s74\n"
    "s_add_u32 s64, s64, 98304\n"
    "v_and_b32 v249, 0x310, v1\n"
    "v_and_b32 v159, 12, v156\n"
    "v_lshl_add_u32 v249, v159, 3, v249\n"
    "v_and_b32 v159, 2, v156\n"
    "buffer_load_dwordx4 v[48:51], v242, s[40:43], s75 offen offset:1024 nt\n"
    "v_lshl_add_u32 v249, v159, 6, v249\n"
    "s_add_u32 s73, s74, 73728\n"
    "v_add_u32 v248, s73, v249\n"
    "s_sub_u32 s73, 98304, s74\n"
    "v_add_u32 v249, s73, v249\n"
    "s_or_b32 s73, s53, s84\n"
    "s_cmp_eq_u32 s73, 0\n"
    "s_cselect_b32 s82, 0, 0x7fff0000\n"
    "v_and_b32 v252, 12, v156\n"
    "v_bfe_u32 v159, v156, 1, 1\n"
    "v_or_b32 v252, v252, v159\n"
    "v_and_b32 v159, 1, v156\n"
    "v_lshl_or_b32 v252, v159, 1, v252\n"
    "v_lshlrev_b32 v252, 6, v252\n"
    "v_lshlrev_b32 v159, 2, v156\n"
    "v_and_b32 v159, 32, v159\n"
    "v_xor_b32 v159, v159, v156\n"
    "v_and_or_b32 v252, v159, 48, v252\n"
    "v_mov_b32 v2, v252\n"
    "v_add_u32 v3, 24576, v252\n"
    "v_lshrrev_b32 v91, 3, v156\n"
    "v_lshl_add_u32 v87, v91, 6, 64\n"
    "v_add_u32 v87, 64, v87\n"
    "v_and_b32 v241, 1, v156\n"
    "v_lshlrev_b32 v241, 3, v241\n"
    "buffer_load_dwordx4 v[52:55], v242, s[40:43], s75 offen offset:512 nt\n"
    "v_lshl_add_u32 v241, v91, 4, v241\n"
    "v_add_u32 v241, 1152, v241\n"
    "v_lshlrev_b32 v91, 24, v91\n"
    "v_lshl_add_u32 v91, v156, 3, v91\n"
    "s_lshl_b32 s73, s53, 4\n"
    "s_lshl_b32 s74, s59, 3\n"
    "s_add_u32 s73, s73, s74\n"
    "s_add_u32 s73, s73, 1152\n"
    "v_mov_b32 v240, 0x7fff0000\n"
    "v_writelane_b32 v240, s73, 0\n"
    "v_and_b32 v159, 48, v156\n"
    "s_mul_i32 s73, s79, 768\n"
    "s_sub_u32 s73, 131072, s73\n"
    "v_lshlrev_b32 v250, 1, v159\n"
    "v_add_u32 v250, s73, v250\n"
    "v_and_b32 v251, 3, v156\n"
    "v_lshl_add_u32 v251, v251, 7, v159\n"
    "s_add_u32 s73, s73, 256\n"
    "v_add_u32 v251, s73, v251\n"
    "v_and_b32 v159, 64, v1\n"
    "v_add_u32 v251, v159, v251\n"
    "v_bfe_i32 v159, v156, 2, 1\n"
    "v_and_b32 v159, 0xaaaa, v159\n"
    "v_or_b32 v253, 0x4444, v159\n"
    "s_mov_b32 s58, 0\n"
    "s_mov_b32 s59, 8\n"
    "s_waitcnt vmcnt(14)\n"
    "v_pk_min_i16 v4, v4, v10\n"
    "v_pk_min_i16 v5, v5, v11\n"
    "v_pk_min_i16 v6, v6, v12\n"
    "v_pk_min_i16 v7, v7, v13\n"
    "v_pk_min_i16 v8, v8, v14\n"
    "v_pk_min_i16 v9, v9, v15\n"
    "v_pk_min_i16 v16, v16, v18\n"
    "v_pk_min_i16 v17, v17, v19\n"
    "v_pk_min_i16 v4, v4, v7\n"
    "v_pk_min_i16 v5, v5, v8\n"
    "v_pk_min_i16 v6, v6, v9\n"
    "v_pk_min_i16 v16, v16, v17\n"
    "v_pk_min_i16 v4, v4, v5\n"
    "v_lshlrev_b32 v17, 16, v16\n"
    "v_pk_min_i16 v4, v4, v6\n"
    "v_min_i32 v16, v16, v17\n"
    "v_lshlrev_b32 v5, 16, v4\n"
    "v_mov_b32 v7, 155920\n"
    "v_min_i32 v4, v4, v5\n"
    "v_lshl_add_u32 v6, s84, 2, v7\n"
    "v_min_i32_dpp v16, v16, v16 row_shr:1\n"
    "v_min_i32_dpp v4, v4, v4 row_shr:1\n"
    "s_nop 0\n"
    "v_min_i32_dpp v16, v16, v16 row_shr:2\n"
    "v_min_i32_dpp v4, v4, v4 row_shr:2\n"
    "s_nop 0\n"
    "v_min_i32_dpp v16, v16, v16 row_shr:4\n"
    "v_min_i32_dpp v4, v4, v4 row_shr:4\n"
    "s_nop 0\n"
    "v_min_i32_dpp v16, v16, v16 row_shr:8\n"
    "v_min_i32_dpp v4, v4, v4 row_shr:8\n"
    "s_nop 0\n"
    "v_min_i32_dpp v16, v16, v16 row_bcast:15 row_mask:0xa\n"
    "v_min_i32_dpp v4, v4, v4 row_bcast:15 row_mask:0xa\n"
    "s_nop 0\n"
    "v_min_i32_dpp v16, v16, v16 row_bcast:31 row_mask:0xc\n"
    "v_min_i32_dpp v4, v4, v4 row_bcast:31 row_mask:0xc\n"
    "v_readlane_b32 s35, v16, 63\n"
    "v_readlane_b32 s73, v4, 63\n"
    "s_max_i32 s35, s35, 0\n"
    "s_and_b32 s35, s35, 0xffff0000\n"
    "v_mov_b32 v4, s73\n"
    "ds_write_b32 v6, v4\n"
    "v_mov_b32 v14, -32\n"
    "v_mov_b32 v12, -126\n"
    "v_mov_b32 v13, 0x3dd53b95\n"
    "v_mov_b32 v98, 0\n"
    "s_waitcnt lgkmcnt(0)\n"
    "s_barrier\n"
    "ds_read_b128 v[8:11], v7 offset:0\n"
    "buffer_load_dwordx4 v[56:59], v242, s[40:43], s75 offen offset:1280 nt\n"
    "buffer_load_dwordx4 v[136:139], v130, s[92:95], 0 offen offset:0\n"
    "buffer_load_dwordx4 v[140:143], v130, s[96:99], 0 offen offset:0\n"
    "s_nop 0\n"
    "s_nop 0\n"
    "s_nop 0\n"
    "s_nop 0\n"
    "s_nop 0\n"
    "s_nop 0\n"
    "s_nop 0\n"
    "s_nop 0\n"
    "s_nop 0\n"
    "s_nop 0\n"
    "s_nop 0\n"
    "s_nop 0\n"
    "s_nop 0\n"
    "s_nop 0\n"
    "s_nop 0\n"
    "s_nop 0\n"
    "s_nop 0\n"
    "s_nop 0\n"
    "s_nop 0\n"
    "s_nop 0\n"
    "s_nop 0\n"
    "s_nop 0\n"
    "s_nop 0\n"
    "s_nop 0\n"
    "s_nop 0\n"
    "s_nop 0\n"
    "s_nop 0\n"
    "s_nop 0\n"
    "s_nop 0\n"
    "s_nop 0\n"
    "s_nop 0\n"
    "s_nop 0\n"
    "s_nop 0\n"
    "s_nop 0\n"
    "s_nop 0\n"
    "s_nop 0\n"
    "s_nop 0\n"
    "s_nop 0\n"
    "s_nop 0\n"
    "s_nop 0\n"
    "s_nop 0\n"
    "s_nop 0\n"
    "s_nop 0\n"
    "s_nop 0\n"
    "s_nop 0\n"
    "s_nop 0\n"
    "s_nop 0\n"
    "s_nop 0\n"
    "s_nop 0\n"
    "s_nop 0\n"
    "s_nop 0\n"
    "s_nop 0\n"
    "s_nop 0\n"
    "s_nop 0\n"
    "s_nop 0\n"
    "s_nop 0\n"
    "s_nop 0\n"
    "s_nop 0\n"
    "s_nop 0\n"
    "s_nop 0\n"
    "s_waitcnt lgkmcnt(0)\n"
    "v_min3_i32 v4, v8, v9, v10\n"
    "v_min_i32 v4, v4, v11\n"
    "v_max_i32 v4, 0, v4\n"
    "v_and_b32 v4, 0xffff0000, v4\n"
    "v_mul_f32 v4, s35, v4\n"
    "v_mul_f32 v4, 0x3ed53b95, v4\n"
    "v_lshrrev_b32 v4, 23, v4\n"
    "v_sub_u32 v4, 2, v4\n"
    "v_med3_i32 v4, v4, v14, v12\n"
    "v_ldexp_f32 v13, v13, v4\n"
    "v_readfirstlane_b32 s89, v4\n"
    "v_readfirstlane_b32 s88, v13\n"
    "s_mul_i32 s89, s89, 0xff7fff80\n"
    "s_mov_b32 s90, 149\n"
    "s_waitcnt vmcnt(11)\n"
    "s_add_u32 s54, s71, s60\n"
    "s_add_u32 s55, s71, s61\n"
    "s_add_u32 s56, s71, s62\n"
    "v_cvt_pk_u16_u32 v204, v204, v208\n"
    "v_cvt_pk_u16_u32 v205, v205, v209\n"
    "v_cvt_pk_u16_u32 v206, v206, v210\n"
    "v_cvt_pk_u16_u32 v207, v207, v211\n"
    "v_or3_b32 v243, v204, v205, v243\n"
    "v_or3_b32 v243, v206, v207, v243\n"
    "buffer_store_dword v204, v244, s[16:19], s54 offen offset:0 sc1\n"
    "buffer_store_dword v205, v244, s[16:19], s54 offen offset:1024 sc1\n"
    "buffer_load_dwordx4 v[4:7], v162, s[28:31], s57 offen offset:0\n"
    "buffer_load_dwordx4 v[8:11], v162, s[28:31], s57 offen offset:3072\n"
    "buffer_store_dword v206, v244, s[16:19], s54 offen offset:2048 sc1\n"
    "buffer_store_dword v207, v244, s[16:19], s54 offen offset:3072 sc1\n"
    "v_cvt_pk_u16_u32 v212, v212, v216\n"
    "v_cvt_pk_u16_u32 v213, v213, v217\n"
    "v_cvt_pk_u16_u32 v214, v214, v218\n"
    "v_cvt_pk_u16_u32 v215, v215, v219\n"
    "v_or3_b32 v243, v212, v213, v243\n"
    "v_or3_b32 v243, v214, v215, v243\n"
    "buffer_store_dword v212, v244, s[16:19], s55 offen offset:0 sc1\n"
    "buffer_store_dword v213, v244, s[16:19], s55 offen offset:1024 sc1\n"
    "buffer_store_dword v214, v244, s[16:19], s55 offen offset:2048 sc1\n"
    "buffer_load_dwordx4 v[12:15], v162, s[28:31], s57 offen offset:128\n"
    "buffer_store_dword v215, v244, s[16:19], s55 offen offset:3072 sc1\n"
    "v_cvt_pk_u16_u32 v220, v220, v224\n"
    "v_cvt_pk_u16_u32 v221, v221, v225\n"
    "v_cvt_pk_u16_u32 v222, v222, v226\n"
    "v_cvt_pk_u16_u32 v223, v223, v227\n"
    "v_or3_b32 v243, v220, v221, v243\n"
    "v_or3_b32 v243, v222, v223, v243\n"
    "buffer_store_dword v220, v244, s[16:19], s56 offen offset:0 sc1\n"
    "buffer_store_dword v221, v244, s[16:19], s56 offen offset:1024 sc1\n"
    "buffer_store_dword v222, v244, s[16:19], s56 offen offset:2048 sc1\n"
    "buffer_store_dword v223, v244, s[16:19], s56 offen offset:3072 sc1\n"
    "s_add_u32 s71, s71, 98304\n"
    "s_add_u32 m0, s65, 24576\n"
    "s_add_u32 s72, s85, 0\n"
    "buffer_load_dwordx4 v88, s[20:23], s72 offen offset:0 nt lds\n"
    "buffer_load_dwordx4 v[16:19], v162, s[28:31], s57 offen offset:3200\n"
    "buffer_load_dwordx4 v[20:23], v162, s[28:31], s57 offen offset:256\n"
    "s_waitcnt vmcnt(25)\n"
    "v_add_co_u32 v238, vcc, 0x7f4a7c15, v238\n"
    "v_mov_b32 v159, 0x9e3779b9\n"
    "s_nop 0\n"
    "v_addc_co_u32 v239, vcc, v159, v239, vcc\n"
    "v_mov_b32 v246, 1\n"
    "s_waitcnt vmcnt(26)\n"
    "v_lshlrev_b32 v180, 16, v124\n"
    "v_and_b32 v181, 0xffff0000, v124\n"
    "v_lshlrev_b32 v182, 16, v125\n"
    "v_and_b32 v183, 0xffff0000, v125\n"
    "v_lshlrev_b32 v184, 16, v126\n"
    "v_and_b32 v185, 0xffff0000, v126\n"
    "v_lshlrev_b32 v186, 16, v127\n"
    "v_and_b32 v187, 0xffff0000, v127\n"
    "v_mul_f32 v180, s88, v180\n"
    "v_mul_f32 v181, s88, v181\n"
    "v_mul_f32 v182, s88, v182\n"
    "v_mul_f32 v183, s88, v183\n"
    "v_fmaak_f32 v184, v184, v180, 0x80730000\n"
    "v_fmaak_f32 v185, v185, v181, 0x80730000\n"
    "v_fmaak_f32 v186, v186, v182, 0x80730000\n"
    "v_fmaak_f32 v187, v187, v183, 0x80730000\n"
    "s_add_u32 m0, s65, 25600\n"
    "s_add_u32 s72, s85, 1024\n"
    "buffer_load_dwordx4 v89, s[20:23], s72 offen offset:0 nt lds\n"
    "buffer_load_dwordx4 v[24:27], v162, s[28:31], s57 offen offset:3328\n"
    "v_ldexp_f32 v180, v180, s90\n"
    "v_ldexp_f32 v181, v181, s90\n"
    "v_ldexp_f32 v182, v182, s90\n"
    "v_ldexp_f32 v183, v183, s90\n"
    "v_fma_f32 v180, v180, 0, v180\n"
    "v_fma_f32 v181, v181, 0, v181\n"
    "v_fma_f32 v182, v182, 0, v182\n"
    "v_fma_f32 v183, v183, 0, v183\n"
    "ds_write_b128 v131, v[180:183] offset:0\n"
    "ds_write_b128 v131, v[184:187] offset:1024\n"
    "ds_read_b128 v[180:183], v132 offset:0\n"
    "ds_read_b128 v[184:187], v132 offset:1024\n"
    "ds_read_b128 v[188:191], v132 offset:128\n"
    "ds_read_b128 v[192:195], v132 offset:1152\n"
    "ds_read_b128 v[196:199], v132 offset:256\n"
    "ds_read_b128 v[200:203], v132 offset:1280\n"
    "s_nop 0\n"
    "s_nop 0\n"
    "s_nop 0\n"
    "s_nop 0\n"
    "s_nop 0\n"
    "s_nop 0\n"
    "s_nop 0\n"
    "s_add_u32 m0, s65, 26624\n"
    "s_add_u32 s72, s85, 2048\n"
    "buffer_load_dwordx4 v90, s[20:23], s72 offen offset:0 nt lds\n"
    "buffer_load_dwordx4 v[100:103], v162, s[28:31], s66 offen offset:0\n"
    "buffer_load_dwordx4 v[104:107], v162, s[28:31], s66 offen offset:3072\n"
    "s_nop 0\n"
    "s_nop 0\n"
    "s_nop 0\n"
    "s_nop 0\n"
    "s_nop 0\n"
    "s_nop 0\n"
    "s_nop 0\n"
    "s_nop 0\n"
    "s_nop 0\n"
    "s_nop 0\n"
    "s_nop 0\n"
    "s_nop 0\n"
    "s_nop 0\n"
    "s_nop 0\n"
    "s_nop 0\n"
    "s_nop 0\n"
    "s_nop 0\n"
    "s_nop 0\n"
    "s_nop 0\n"
    "s_nop 0\n"
    "s_nop 0\n"
    "s_nop 0\n"
    "s_add_u32 m0, s65, 27648\n"
    "s_add_u32 s72, s85, 3072\n"
    "buffer_load_dwordx4 v88, s[20:23], s72 offen offset:0 nt lds\n"
    "buffer_load_dwordx4 v[108:111], v162, s[28:31], s66 offen offset:128\n"
    "s_nop 0\n"
    "s_nop 0\n"
    "s_nop 0\n"
    "s_nop 0\n"
    "s_nop 0\n"
    "s_nop 0\n"
    "s_nop 0\n"
    "s_nop 0\n"
    "s_nop 0\n"
    "s_nop 0\n"
    "s_nop 0\n"
    "s_nop 0\n"
    "s_nop 0\n"
    "s_nop 0\n"
    "s_nop 0\n"
    "s_nop 0\n"
    "s_nop 0\n"
    "s_nop 0\n"
    "s_nop 0\n"
    "s_nop 0\n"
    "s_nop 0\n"
    "s_nop 0\n"
    "s_nop 0\n"
    "s_waitcnt lgkmcnt(0)\n"
    "s_waitcnt vmcnt(10)\n"
    "ds_add_rtn_u32 v245, v247, v246\n"
    "s_lshl_b32 s73, s53, 6\n"
    "s_add_u32 s73, s73, 128\n"
    "s_waitcnt lgkmcnt(0)\n"
    "v_readfirstlane_b32 s74, v245\n"
    "s_nop 0\n"
    "s_cmp_eq_u32 s74, 3\n"
    "s_cselect_b32 s73, s73, 0x7fff0000\n"
    "buffer_store_dwordx2 v[238:239], v91, s[36:39], s73 offen offset:0 sc1\n"
    "s_waitcnt vmcnt(28)\n"
    "s_add_u32 m0, s65, 28672\n"
    "s_add_u32 s72, s85, 4096\n"
    "buffer_load_dwordx4 v89, s[20:23], s72 offen offset:0 nt lds\n"
    "buffer_load_dwordx4 v[112:115], v162, s[28:31], s66 offen offset:3200\n"
    "buffer_load_dwordx4 v[116:119], v162, s[28:31], s66 offen offset:256\n"
    "s_add_u32 s54, s71, s60\n"
    "s_add_u32 s55, s71, s61\n"
    "s_add_u32 s56, s71, s62\n"
    "v_cvt_pk_u16_u32 v36, v36, v40\n"
    "v_cvt_pk_u16_u32 v37, v37, v41\n"
    "v_cvt_pk_u16_u32 v38, v38, v42\n"
    "v_cvt_pk_u16_u32 v39, v39, v43\n"
    "v_or3_b32 v243, v36, v37, v243\n"
    "v_or3_b32 v243, v38, v39, v243\n"
    "buffer_store_dword v36, v244, s[16:19], s54 offen offset:0 sc1\n"
    "buffer_store_dword v37, v244, s[16:19], s54 offen offset:1024 sc1\n"
    "buffer_store_dword v38, v244, s[16:19], s54 offen offset:2048 sc1\n"
    "buffer_store_dword v39, v244, s[16:19], s54 offen offset:3072 sc1\n"
    "v_cvt_pk_u16_u32 v44, v44, v48\n"
    "v_cvt_pk_u16_u32 v45, v45, v49\n"
    "v_cvt_pk_u16_u32 v46, v46, v50\n"
    "s_add_u32 m0, s65, 29696\n"
    "s_add_u32 s72, s85, 5120\n"
    "buffer_load_dwordx4 v90, s[20:23], s72 offen offset:0 nt lds\n"
    "buffer_load_dwordx4 v[120:123], v162, s[28:31], s66 offen offset:3328\n"
    "v_cvt_pk_u16_u32 v47, v47, v51\n"
    "v_or3_b32 v243, v44, v45, v243\n"
    "v_or3_b32 v243, v46, v47, v243\n"
    "buffer_store_dword v44, v244, s[16:19], s55 offen offset:0 sc1\n"
    "buffer_store_dword v45, v244, s[16:19], s55 offen offset:1024 sc1\n"
    "buffer_store_dword v46, v244, s[16:19], s55 offen offset:2048 sc1\n"
    "buffer_store_dword v47, v244, s[16:19], s55 offen offset:3072 sc1\n"
    "v_cvt_pk_u16_u32 v52, v52, v56\n"
    "v_cvt_pk_u16_u32 v53, v53, v57\n"
    "v_cvt_pk_u16_u32 v54, v54, v58\n"
    "v_cvt_pk_u16_u32 v55, v55, v59\n"
    "v_or3_b32 v243, v52, v53, v243\n"
    "v_or3_b32 v243, v54, v55, v243\n"
    "buffer_store_dword v52, v244, s[16:19], s56 offen offset:0 sc1\n"
    "buffer_store_dword v53, v244, s[16:19], s56 offen offset:1024 sc1\n"
    "buffer_store_dword v54, v244, s[16:19], s56 offen offset:2048 sc1\n"
    "buffer_store_dword v55, v244, s[16:19], s56 offen offset:3072 sc1\n"
    "s_add_u32 m0, s65, 73728\n"
    "s_add_u32 s72, s85, 6144\n"
    "buffer_load_dwordx4 v88, s[20:23], s72 offen offset:0 nt lds\n"
    "s_waitcnt vmcnt(44)\n"
    "v_lshlrev_b32 v204, 16, v136\n"
    "v_and_b32 v205, 0xffff0000, v136\n"
    "v_lshlrev_b32 v206, 16, v137\n"
    "v_and_b32 v207, 0xffff0000, v137\n"
    "v_lshlrev_b32 v208, 16, v138\n"
    "v_and_b32 v209, 0xffff0000, v138\n"
    "v_lshlrev_b32 v210, 16, v139\n"
    "v_and_b32 v211, 0xffff0000, v139\n"
    "v_log_f32 v68, v204\n"
    "v_log_f32 v69, v205\n"
    "v_log_f32 v70, v206\n"
    "v_log_f32 v71, v207\n"
    "v_log_f32 v72, v208\n"
    "v_log_f32 v73, v209\n"
    "v_log_f32 v74, v210\n"
    "v_log_f32 v75, v211\n"
    "v_rcp_f32 v204, v204\n"
    "v_rcp_f32 v205, v205\n"
    "v_rcp_f32 v206, v206\n"
    "s_add_u32 m0, s65, 74752\n"
    "s_add_u32 s72, s85, 7168\n"
    "buffer_load_dwordx4 v89, s[20:23], s72 offen offset:0 nt lds\n"
    "v_rcp_f32 v207, v207\n"
    "v_rcp_f32 v208, v208\n"
    "v_rcp_f32 v209, v209\n"
    "v_rcp_f32 v210, v210\n"
    "v_rcp_f32 v211, v211\n"
    "s_mov_b32 s73, 0x41a00000\n"
    "v_mov_b32 v228, 0x7fc00000\n"
    "v_cmp_le_f32 vcc, |v68|, s73\n"
    "s_nop 1\n"
    "v_cndmask_b32 v68, v228, v68, vcc\n"
    "v_cmp_le_f32 vcc, |v69|, s73\n"
    "s_nop 1\n"
    "v_cndmask_b32 v69, v228, v69, vcc\n"
    "v_cmp_le_f32 vcc, |v70|, s73\n"
    "s_nop 1\n"
    "v_cndmask_b32 v70, v228, v70, vcc\n"
    "v_cmp_le_f32 vcc, |v71|, s73\n"
    "s_nop 1\n"
    "v_cndmask_b32 v71, v228, v71, vcc\n"
    "v_cmp_le_f32 vcc, |v72|, s73\n"
    "s_nop 1\n"
    "v_cndmask_b32 v72, v228, v72, vcc\n"
    "v_cmp_le_f32 vcc, |v73|, s73\n"
    "s_nop 1\n"
    "v_cndmask_b32 v73, v228, v73, vcc\n"
    "v_cmp_le_f32 vcc, |v74|, s73\n"
    "s_nop 1\n"
    "v_cndmask_b32 v74, v228, v74, vcc\n"
    "s_add_u32 m0, s65, 75776\n"
    "s_add_u32 s72, s85, 8192\n"
    "buffer_load_dwordx4 v90, s[20:23], s72 offen offset:0 nt lds\n"
    "v_cmp_le_f32 vcc, |v75|, s73\n"
    "s_nop 1\n"
    "v_cndmask_b32 v75, v228, v75, vcc\n"
    "ds_write_b128 v86, v[68:71] offset:0\n"
    "ds_write_b128 v86, v[72:75] offset:16\n"
    "v_cvt_pk_bf16_f32 v220, v204, v205\n"
    "v_cvt_pk_bf16_f32 v221, v206, v207\n"
    "v_cvt_pk_bf16_f32 v222, v208, v209\n"
    "v_cvt_pk_bf16_f32 v223, v210, v211\n"
    "v_lshlrev_b32 v212, 16, v220\n"
    "v_and_b32 v213, 0xffff0000, v220\n"
    "v_lshlrev_b32 v214, 16, v221\n"
    "v_and_b32 v215, 0xffff0000, v221\n"
    "v_lshlrev_b32 v216, 16, v222\n"
    "v_and_b32 v217, 0xffff0000, v222\n"
    "v_lshlrev_b32 v218, 16, v223\n"
    "v_and_b32 v219, 0xffff0000, v223\n"
    "v_sub_f32 v204, v204, v212\n"
    "v_sub_f32 v205, v205, v213\n"
    "v_sub_f32 v206, v206, v214\n"
    "v_sub_f32 v207, v207, v215\n"
    "v_sub_f32 v208, v208, v216\n"
    "s_add_u32 m0, s65, 76800\n"
    "s_add_u32 s72, s85, 9216\n"
    "buffer_load_dwordx4 v88, s[20:23], s72 offen offset:0 nt lds\n"
    "v_sub_f32 v209, v209, v217\n"
    "v_sub_f32 v210, v210, v218\n"
    "v_sub_f32 v211, v211, v219\n"
    "v_cvt_pk_bf16_f32 v224, v204, v205\n"
    "v_cvt_pk_bf16_f32 v225, v206, v207\n"
    "v_cvt_pk_bf16_f32 v226, v208, v209\n"
    "v_cvt_pk_bf16_f32 v227, v210, v211\n"
    "v_mov_b32 v204, 0\n"
    "v_mov_b32 v205, 0\n"
    "v_mov_b32 v206, 0\n"
    "v_mov_b32 v207, 0\n"
    "v_swap_b32 v221, v222\n"
    "v_swap_b32 v225, v226\n"
    "v_swap_b32 v141, v142\n"
    "ds_write2_b64 v144, v[220:221], v[222:223] offset0:32 offset1:40\n"
    "ds_write2_b64 v144, v[224:225], v[226:227] offset0:48 offset1:56\n"
    "ds_write2_b64 v144, v[140:141], v[142:143] offset0:64 offset1:72\n"
    "ds_write2_b64 v144, v[204:205], v[206:207] offset0:80 offset1:88\n"
    "s_waitcnt vmcnt(27)\n"
    "v_fma_f32 v4, v4, v180, -v184\n"
    "v_fma_f32 v5, v5, v181, -v185\n"
    "s_add_u32 m0, s65, 77824\n"
    "s_add_u32 s72, s85, 10240\n"
    "buffer_load_dwordx4 v89, s[20:23], s72 offen offset:0 nt lds\n"
    "v_fma_f32 v6, v6, v182, -v186\n"
    "v_fma_f32 v7, v7, v183, -v187\n"
    "v_fma_f32 v8, v8, v180, -v184\n"
    "v_fma_f32 v9, v9, v181, -v185\n"
    "v_fma_f32 v10, v10, v182, -v186\n"
    "v_fma_f32 v11, v11, v183, -v187\n"
    "v_cvt_pk_bf16_f32 v4, v4, v5\n"
    "v_cvt_pk_bf16_f32 v5, v6, v7\n"
    "v_cvt_pk_bf16_f32 v6, v8, v9\n"
    "v_cvt_pk_bf16_f32 v7, v10, v11\n"
    "ds_write_b64 v163, v[4:5] offset:0\n"
    "ds_write_b64 v163, v[6:7] offset:1024\n"
    "v_fma_f32 v12, v12, v188, -v192\n"
    "v_fma_f32 v13, v13, v189, -v193\n"
    "v_fma_f32 v14, v14, v190, -v194\n"
    "v_fma_f32 v15, v15, v191, -v195\n"
    "v_fma_f32 v16, v16, v188, -v192\n"
    "v_fma_f32 v17, v17, v189, -v193\n"
    "v_fma_f32 v18, v18, v190, -v194\n"
    "v_fma_f32 v19, v19, v191, -v195\n"
    "s_add_u32 m0, s65, 78848\n"
    "s_add_u32 s72, s85, 11264\n"
    "buffer_load_dwordx4 v90, s[20:23], s72 offen offset:0 nt lds\n"
    "v_cvt_pk_bf16_f32 v12, v12, v13\n"
    "v_cvt_pk_bf16_f32 v13, v14, v15\n"
    "v_cvt_pk_bf16_f32 v14, v16, v17\n"
    "v_cvt_pk_bf16_f32 v15, v18, v19\n"
    "ds_write_b64 v163, v[12:13] offset:2048\n"
    "ds_write_b64 v163, v[14:15] offset:3072\n"
    "v_fma_f32 v20, v20, v196, -v200\n"
    "v_fma_f32 v21, v21, v197, -v201\n"
    "v_fma_f32 v22, v22, v198, -v202\n"
    "v_fma_f32 v23, v23, v199, -v203\n"
    "v_fma_f32 v24, v24, v196, -v200\n"
    "v_fma_f32 v25, v25, v197, -v201\n"
    "v_fma_f32 v26, v26, v198, -v202\n"
    "v_fma_f32 v27, v27, v199, -v203\n"
    "v_cvt_pk_bf16_f32 v20, v20, v21\n"
    "v_cvt_pk_bf16_f32 v21, v22, v23\n"
    "v_cvt_pk_bf16_f32 v22, v24, v25\n"
    "v_cvt_pk_bf16_f32 v23, v26, v27\n"
    "ds_write_b64 v163, v[20:21] offset:4096\n"
    "ds_write_b64 v163, v[22:23] offset:5120\n"
    "s_waitcnt vmcnt(14)\n"
    "s_add_u32 m0, s65, 49152\n"
    "s_add_u32 s72, s85, 12288\n"
    "buffer_load_dwordx4 v88, s[20:23], s72 offen offset:0 nt lds\n"
    "v_fma_f32 v100, v100, v180, -v184\n"
    "v_fma_f32 v101, v101, v181, -v185\n"
    "v_fma_f32 v102, v102, v182, -v186\n"
    "v_fma_f32 v103, v103, v183, -v187\n"
    "v_fma_f32 v104, v104, v180, -v184\n"
    "v_fma_f32 v105, v105, v181, -v185\n"
    "v_fma_f32 v106, v106, v182, -v186\n"
    "v_fma_f32 v107, v107, v183, -v187\n"
    "v_cvt_pk_bf16_f32 v100, v100, v101\n"
    "v_cvt_pk_bf16_f32 v101, v102, v103\n"
    "v_cvt_pk_bf16_f32 v102, v104, v105\n"
    "v_cvt_pk_bf16_f32 v103, v106, v107\n"
    "ds_write_b64 v163, v[100:101] offset:12288\n"
    "ds_write_b64 v163, v[102:103] offset:13312\n"
    "v_fma_f32 v108, v108, v188, -v192\n"
    "v_fma_f32 v109, v109, v189, -v193\n"
    "v_fma_f32 v110, v110, v190, -v194\n"
    "v_fma_f32 v111, v111, v191, -v195\n"
    "v_fma_f32 v112, v112, v188, -v192\n"
    "v_fma_f32 v113, v113, v189, -v193\n"
    "v_fma_f32 v114, v114, v190, -v194\n"
    "s_add_u32 m0, s65, 50176\n"
    "s_add_u32 s72, s85, 13312\n"
    "buffer_load_dwordx4 v89, s[20:23], s72 offen offset:0 nt lds\n"
    "v_fma_f32 v115, v115, v191, -v195\n"
    "v_cvt_pk_bf16_f32 v108, v108, v109\n"
    "v_cvt_pk_bf16_f32 v109, v110, v111\n"
    "v_cvt_pk_bf16_f32 v110, v112, v113\n"
    "v_cvt_pk_bf16_f32 v111, v114, v115\n"
    "ds_write_b64 v163, v[108:109] offset:14336\n"
    "ds_write_b64 v163, v[110:111] offset:15360\n"
    "v_fma_f32 v116, v116, v196, -v200\n"
    "v_fma_f32 v117, v117, v197, -v201\n"
    "v_fma_f32 v118, v118, v198, -v202\n"
    "v_fma_f32 v119, v119, v199, -v203\n"
    "v_fma_f32 v120, v120, v196, -v200\n"
    "v_fma_f32 v121, v121, v197, -v201\n"
    "v_fma_f32 v122, v122, v198, -v202\n"
    "v_fma_f32 v123, v123, v199, -v203\n"
    "v_cvt_pk_bf16_f32 v116, v116, v117\n"
    "v_cvt_pk_bf16_f32 v117, v118, v119\n"
    "v_cvt_pk_bf16_f32 v118, v120, v121\n"
    "v_cvt_pk_bf16_f32 v119, v122, v123\n"
    "ds_write_b64 v163, v[116:117] offset:16384\n"
    "ds_write_b64 v163, v[118:119] offset:17408\n"
    "s_waitcnt vmcnt(17)\n"
    "ds_read_b128 v[36:39], v154 offset:0\n"
    "ds_read_b128 v[40:43], v155 offset:0\n"
    "ds_read_b128 v[44:47], v154 offset:128\n"
    "ds_read_b128 v[48:51], v155 offset:128\n"
    "ds_read_b128 v[52:55], v154 offset:256\n"
    "ds_read_b128 v[56:59], v155 offset:256\n"
    "s_mov_b64 exec, 0\n"
    "s_bitcmp1_b32 s83, 1\n"
    "s_cbranch_scc1 .LwV\n"
    "s_cmp_eq_u32 0, 0\n"
    "s_branch .LwK\n"
    ".Lw2:\n"
    "s_bitcmp1_b32 s83, 1\n"
    "s_cbranch_scc1 .Lwp1\n"
    "s_branch .Lwp0\n"
    ".Lw3:\n"
    "s_mov_b64 exec, -1\n"
    "s_waitcnt lgkmcnt(5)\n"
    "v_pk_maximum3_f16 v98, v36, v37, v98\n"
    "v_pk_maximum3_f16 v98, v38, v39, v98\n"
    "v_pk_add_u16 v36, s89, v36 clamp\n"
    "v_pk_add_u16 v37, s89, v37 clamp\n"
    "v_pk_add_u16 v38, s89, v38 clamp\n"
    "v_pk_add_u16 v39, s89, v39 clamp\n"
    "s_waitcnt lgkmcnt(4)\n"
    "v_pk_maximum3_f16 v98, v40, v41, v98\n"
    "v_pk_maximum3_f16 v98, v42, v43, v98\n"
    "v_pk_add_u16 v40, s89, v40 clamp\n"
    "v_pk_add_u16 v41, s89, v41 clamp\n"
    "v_pk_add_u16 v42, s89, v42 clamp\n"
    "v_pk_add_u16 v43, s89, v43 clamp\n"
    "s_waitcnt lgkmcnt(3)\n"
    "v_pk_maximum3_f16 v98, v44, v45, v98\n"
    "v_pk_maximum3_f16 v98, v46, v47, v98\n"
    "v_pk_add_u16 v44, s89, v44 clamp\n"
    "v_pk_add_u16 v45, s89, v45 clamp\n"
    "v_pk_add_u16 v46, s89, v46 clamp\n"
    "v_pk_add_u16 v47, s89, v47 clamp\n"
    "s_waitcnt lgkmcnt(2)\n"
    "v_pk_maximum3_f16 v98, v48, v49, v98\n"
    "v_pk_maximum3_f16 v98, v50, v51, v98\n"
    "v_pk_add_u16 v48, s89, v48 clamp\n"
    "v_pk_add_u16 v49, s89, v49 clamp\n"
    "v_pk_add_u16 v50, s89, v50 clamp\n"
    "v_pk_add_u16 v51, s89, v51 clamp\n"
    "s_waitcnt lgkmcnt(1)\n"
    "v_pk_maximum3_f16 v98, v52, v53, v98\n"
    "v_pk_maximum3_f16 v98, v54, v55, v98\n"
    "v_pk_add_u16 v52, s89, v52 clamp\n"
    "v_pk_add_u16 v53, s89, v53 clamp\n"
    "v_pk_add_u16 v54, s89, v54 clamp\n"
    "v_pk_add_u16 v55, s89, v55 clamp\n"
    "s_waitcnt lgkmcnt(0)\n"
    "v_pk_maximum3_f16 v98, v56, v57, v98\n"
    "v_pk_maximum3_f16 v98, v58, v59, v98\n"
    "v_pk_add_u16 v56, s89, v56 clamp\n"
    "v_pk_add_u16 v57, s89, v57 clamp\n"
    "v_pk_add_u16 v58, s89, v58 clamp\n"
    "v_pk_add_u16 v59, s89, v59 clamp\n"
    "v_mov_b32 v108, 0\n"
    "v_mov_b32 v109, 0\n"
    "v_mov_b32 v110, 0\n"
    "v_mov_b32 v111, 0\n"
    "v_mov_b32 v112, 0\n"
    "v_mov_b32 v113, 0\n"
    "v_mov_b32 v114, 0\n"
    "v_mov_b32 v115, 0\n"
    "v_mov_b32 v116, 0\n"
    "v_mov_b32 v117, 0\n"
    "v_mov_b32 v118, 0\n"
    "v_mov_b32 v119, 0\n"
    "v_mov_b32 v120, 0\n"
    "v_mov_b32 v121, 0\n"
    "v_mov_b32 v122, 0\n"
    "v_mov_b32 v123, 0\n"
    "s_mul_i32 s73, s79, 768\n"
    "v_add_u32 v78, s73, v250\n"
    "v_mov_b32 v79, 155936\n"
    "s_lshl2_add_u32 s74, s84, 155936\n"
    "v_mov_b32 v77, 155952\n"
    "v_writelane_b32 v77, s74, 63\n"
    "s_waitcnt lgkmcnt(0)\n"
    "s_barrier\n"
    "ds_read_b128 a[112:115], v252 offset:0\n"
    "ds_read_b128 a[116:119], v252 offset:2048\n"
    "ds_read_b128 a[120:123], v252 offset:4096\n"
    "ds_read_b128 a[124:127], v252 offset:6144\n"
    "ds_read_b128 a[128:131], v252 offset:8192\n"
    "ds_read_b128 a[132:135], v252 offset:10240\n"
    "ds_read_b128 a[136:139], v252 offset:1024\n"
    "ds_read_b128 a[140:143], v252 offset:3072\n"
    "ds_read_b128 a[144:147], v252 offset:5120\n"
    "ds_read_b128 a[148:151], v252 offset:7168\n"
    "ds_read_b128 a[152:155], v252 offset:9216\n"
    "ds_read_b128 a[156:159], v252 offset:11264\n"
    "s_add_u32 m0, s65, 51200\n"
    "s_add_u32 s72, s85, 14336\n"
    "buffer_load_dwordx4 v90, s[20:23], s72 offen offset:0 nt lds\n"
    "s_add_u32 m0, s65, 52224\n"
    "s_add_u32 s72, s85, 15360\n"
    "buffer_load_dwordx4 v88, s[20:23], s72 offen offset:0 nt lds\n"
    "s_waitcnt lgkmcnt(11)\n"
    "v_mfma_f32_16x16x32_bf16 v[4:7], a[112:115], v[36:39], 0\n"
    "s_waitcnt lgkmcnt(10)\n"
    "v_mfma_f32_16x16x32_bf16 v[4:7], a[116:119], v[40:43], v[4:7]\n"
    "s_waitcnt lgkmcnt(9)\n"
    "v_mfma_f32_16x16x32_bf16 v[4:7], a[120:123], v[44:47], v[4:7]\n"
    "s_waitcnt lgkmcnt(8)\n"
    "v_mfma_f32_16x16x32_bf16 v[4:7], a[124:127], v[48:51], v[4:7]\n"
    "s_waitcnt lgkmcnt(7)\n"
    "v_mfma_f32_16x16x32_bf16 v[4:7], a[128:131], v[52:55], v[4:7]\n"
    "s_waitcnt lgkmcnt(6)\n"
    "v_mfma_f32_16x16x32_bf16 v[4:7], a[132:135], v[56:59], v[4:7]\n"
    "s_waitcnt lgkmcnt(5)\n"
    "v_mfma_f32_16x16x32_bf16 v[20:23], a[136:139], v[36:39], 0\n"
    "v_accvgpr_write_b32 a192, v36\n"
    "v_accvgpr_write_b32 a193, v37\n"
    "v_accvgpr_write_b32 a194, v38\n"
    "v_accvgpr_write_b32 a195, v39\n"
    "s_waitcnt lgkmcnt(4)\n"
    "v_mfma_f32_16x16x32_bf16 v[20:23], a[140:143], v[40:43], v[20:23]\n"
    "v_accvgpr_write_b32 a196, v40\n"
    "v_accvgpr_write_b32 a197, v41\n"
    "v_accvgpr_write_b32 a198, v42\n"
    "v_accvgpr_write_b32 a199, v43\n"
    "s_waitcnt lgkmcnt(3)\n"
    "v_mfma_f32_16x16x32_bf16 v[20:23], a[144:147], v[44:47], v[20:23]\n"
    "v_accvgpr_write_b32 a200, v44\n"
    "v_accvgpr_write_b32 a201, v45\n"
    "v_accvgpr_write_b32 a202, v46\n"
    "v_accvgpr_write_b32 a203, v47\n"
    "s_waitcnt lgkmcnt(2)\n"
    "v_mfma_f32_16x16x32_bf16 v[20:23], a[148:151], v[48:51], v[20:23]\n"
    "v_accvgpr_write_b32 a204, v48\n"
    "v_accvgpr_write_b32 a205, v49\n"
    "v_accvgpr_write_b32 a206, v50\n"
    "v_accvgpr_write_b32 a207, v51\n"
    "s_waitcnt lgkmcnt(1)\n"
    "v_mfma_f32_16x16x32_bf16 v[20:23], a[152:155], v[52:55], v[20:23]\n"
    "v_accvgpr_write_b32 a208, v52\n"
    "v_accvgpr_write_b32 a209, v53\n"
    "v_accvgpr_write_b32 a210, v54\n"
    "v_accvgpr_write_b32 a211, v55\n"
    "s_waitcnt lgkmcnt(0)\n"
    "v_mfma_f32_16x16x32_bf16 v[20:23], a[156:159], v[56:59], v[20:23]\n"
    "v_accvgpr_write_b32 a212, v56\n"
    "v_accvgpr_write_b32 a213, v57\n"
    "v_accvgpr_write_b32 a214, v58\n"
    "v_accvgpr_write_b32 a215, v59\n"
    "s_add_u32 m0, s65, 53248\n"
    "s_add_u32 s72, s85, 16384\n"
    "buffer_load_dwordx4 v89, s[20:23], s72 offen offset:0 nt lds\n"
    "buffer_load_dwordx2 v[236:237], v87, s[36:39], 0 offen offset:0 sc1\n"
    "ds_read_b128 v[16:19], v78 offset:0\n"
    "ds_read_b128 v[32:35], v78 offset:16\n"
    "ds_read_b128 v[48:51], v78 offset:128\n"
    "ds_read_b128 v[64:67], v78 offset:144\n"
    "ds_read_b128 v[100:103], v78 offset:768\n"
    "ds_read_b128 v[104:107], v78 offset:784\n"
    "v_max3_f32 v76, v4, v5, v6\n"
    "ds_read_b128 a[0:3], v252 offset:12288\n"
    "v_max3_f32 v76, v76, v7, v20\n"
    "ds_read_b128 a[4:7], v252 offset:14336\n"
    "v_max3_f32 v76, v76, v21, v22\n"
    "ds_read_b128 a[8:11], v252 offset:16384\n"
    "v_max_f32 v76, v76, v23\n"
    "ds_read_b128 a[12:15], v252 offset:18432\n"
    "s_nop 0\n"
    "v_max_f32_dpp v76, v76, v76 row_shr:1\n"
    "ds_read_b128 a[16:19], v252 offset:20480\n"
    "ds_read_b128 a[20:23], v252 offset:22528\n"
    "v_max_f32_dpp v76, v76, v76 row_shr:2\n"
    "ds_read_b128 a[24:27], v252 offset:13312\n"
    "s_nop 0\n"
    "v_max_f32_dpp v76, v76, v76 row_shr:4\n"
    "ds_read_b128 a[28:31], v252 offset:15360\n"
    "s_nop 0\n"
    "v_max_f32_dpp v76, v76, v76 row_shr:8\n"
    "ds_read_b128 a[32:35], v252 offset:17408\n"
    "s_nop 0\n"
    "v_max_f32_dpp v76, v76, v76 row_bcast:15 row_mask:0xa\n"
    "ds_read_b128 a[36:39], v252 offset:19456\n"
    "s_nop 0\n"
    "v_max_f32_dpp v76, v76, v76 row_bcast:31 row_mask:0xc\n"
    "ds_read_b128 a[40:43], v252 offset:21504\n"
    "ds_read_b128 a[44:47], v252 offset:23552\n"
    "ds_write_b32 v77, v76\n"
    "s_add_u32 m0, s65, 54272\n"
    "s_add_u32 s72, s85, 17408\n"
    "buffer_load_dwordx4 v90, s[20:23], s72 offen offset:0 nt lds\n"
    "s_add_u32 m0, s65, 98304\n"
    "s_add_u32 s72, s85, 18432\n"
    "buffer_load_dwordx4 v88, s[20:23], s72 offen offset:0 nt lds\n"
    "s_waitcnt lgkmcnt(0)\n"
    "s_barrier\n"
    "ds_read_b128 v[80:83], v79 offset:0\n"
    "s_waitcnt vmcnt(14)\n"
    "buffer_store_dwordx2 v[238:239], v240, s[36:39], s80 offen offset:0 sc1\n"
    "s_lshl_b32 s73, s79, 8\n"
    "s_add_u32 s80, s80, s73\n"
    "s_mul_i32 s73, s79, 98304\n"
    "s_add_u32 s71, s71, s73\n"
    "s_add_u32 m0, s65, 99328\n"
    "s_add_u32 s72, s85, 19456\n"
    "buffer_load_dwordx4 v89, s[20:23], s72 offen offset:0 nt lds\n"
    "s_waitcnt lgkmcnt(0)\n"
    "v_max3_f32 v76, v80, v81, v82\n"
    "v_max_f32 v76, v76, v83\n"
    "v_add_f32 v76, 0xc2b00000, v76\n"
    "s_add_u32 m0, s65, 100352\n"
    "s_add_u32 s72, s85, 20480\n"
    "buffer_load_dwordx4 v90, s[20:23], s72 offen offset:0 nt lds\n"
    "s_add_u32 m0, s65, 101376\n"
    "s_add_u32 s72, s85, 21504\n"
    "buffer_load_dwordx4 v88, s[20:23], s72 offen offset:0 nt lds\n"
    "s_add_u32 m0, s65, 102400\n"
    "s_add_u32 s72, s85, 22528\n"
    "buffer_load_dwordx4 v89, s[20:23], s72 offen offset:0 nt lds\n"
    "s_add_u32 m0, s65, 103424\n"
    "s_add_u32 s72, s85, 23552\n"
    "buffer_load_dwordx4 v90, s[20:23], s72 offen offset:0 nt lds\n"
    "v_sub_f32 v48, v48, v76\n"
    "v_sub_f32 v51, v51, v76\n"
    "v_sub_f32 v49, v49, v76\n"
    "v_sub_f32 v50, v50, v76\n"
    "v_sub_f32 v66, v66, v76\n"
    "v_sub_f32 v65, v65, v76\n"
    "v_mfma_f32_16x16x32_bf16 v[36:39], a[0:3], a[192:195], v[48:51]\n"
    "s_waitcnt vmcnt(14)\n"
    "ds_read_b128 v[200:203], v97 offset:24832\n"
    "v_sub_f32 v67, v67, v76\n"
    "v_mfma_f32_16x16x32_bf16 v[36:39], a[4:7], a[196:199], v[36:39]\n"
    "v_sub_f32 v33, v33, v76\n"
    "v_sub_f32 v32, v32, v76\n"
    "v_mfma_f32_16x16x32_bf16 v[36:39], a[8:11], a[200:203], v[36:39]\n"
    "ds_read_b128 v[184:187], v97 offset:24576\n"
    "ds_read_b128 v[196:199], v96 offset:24832\n"
    "ds_read_b128 v[192:195], v97 offset:24704\n"
    "v_mfma_f32_16x16x32_bf16 v[36:39], a[12:15], a[204:207], v[36:39]\n"
    "v_sub_f32 v64, v64, v76\n"
    "v_sub_f32 v35, v35, v76\n"
    "v_sub_f32 v19, v19, v76\n"
    "v_mfma_f32_16x16x32_bf16 v[52:55], a[24:27], a[192:195], v[64:67]\n"
    "v_add_f32 v21, v21, v33\n"
    "v_sub_f32 v16, v16, v76\n"
    "v_mfma_f32_16x16x32_bf16 v[36:39], a[16:19], a[208:211], v[36:39]\n"
    "v_sub_f32 v18, v18, v76\n"
    "v_add_f32 v20, v20, v32\n"
    "v_sub_f32 v17, v17, v76\n"
    "v_mfma_f32_16x16x32_bf16 v[52:55], a[28:31], a[196:199], v[52:55]\n"
    "ds_read_b128 v[188:191], v96 offset:24704\n"
    "ds_read_b128 v[180:183], v96 offset:24576\n"
    "v_add_f32 v6, v6, v18\n"
    "v_sub_f32 v34, v34, v76\n"
    "v_mfma_f32_16x16x32_bf16 v[36:39], a[20:23], a[212:215], v[36:39]\n"
    "s_waitcnt lgkmcnt(5)\n"
    "v_pk_maximum3_f16 v98, v200, v201, v98\n"
    "v_exp_f32 v20, v20\n"
    "v_pk_maximum3_f16 v98, v202, v203, v98\n"
    "v_pk_add_u16 v203, s89, v203 clamp\n"
    "v_pk_add_u16 v200, s89, v200 clamp\n"
    "v_mfma_f32_16x16x32_bf16 v[52:55], a[32:35], a[200:203], v[52:55]\n"
    "s_waitcnt lgkmcnt(3)\n"
    "v_pk_maximum3_f16 v98, v196, v197, v98\n"
    "v_pk_maximum3_f16 v98, v198, v199, v98\n"
    "v_mfma_f32_16x16x32_bf16 v[52:55], a[36:39], a[204:207], v[52:55]\n"
    "v_exp_f32 v21, v21\n"
    "v_pk_add_u16 v202, s89, v202 clamp\n"
    "v_pk_add_u16 v201, s89, v201 clamp\n"
    "v_mfma_f32_16x16x32_bf16 v[52:55], a[40:43], a[208:211], v[52:55]\n"
    "v_add_f32 v22, v22, v34\n"
    "v_add_f32 v7, v7, v19\n"
    "v_mfma_f32_16x16x32_bf16 v[24:27], a[156:159], v[200:203], v[32:35]\n"
    "s_waitcnt lgkmcnt(2)\n"
    "v_pk_maximum3_f16 v98, v192, v193, v98\n"
    "v_pk_maximum3_f16 v98, v194, v195, v98\n"
    "v_pk_add_u16 v193, s89, v193 clamp\n"
    "v_pk_add_u16 v198, s89, v198 clamp\n"
    "v_add_f32 v5, v5, v17\n"
    "v_mfma_f32_16x16x32_bf16 v[52:55], a[44:47], a[212:215], v[52:55]\n"
    "s_waitcnt lgkmcnt(1)\n"
    "v_pk_add_u16 v194, s89, v194 clamp\n"
    "v_sub_f32 v69, v69, v76\n"
    "v_sub_f32 v73, v73, v76\n"
    "v_pk_maximum3_f16 v98, v188, v189, v98\n"
    "v_pk_maximum3_f16 v98, v190, v191, v98\n"
    "v_pk_add_u16 v196, s89, v196 clamp\n"
    "v_pk_maximum3_f16 v98, v184, v185, v98\n"
    "v_pk_add_u16 v197, s89, v197 clamp\n"
    "v_pk_maximum3_f16 v98, v186, v187, v98\n"
    "v_mfma_f32_16x16x32_bf16 v[8:11], a[132:135], v[200:203], v[16:19]\n"
    "v_sub_f32 v75, v75, v76\n"
    "v_pk_add_u16 v199, s89, v199 clamp\n"
    "v_sub_f32 v101, v101, v76\n"
    "v_exp_f32 v54, v54\n"
    "v_mfma_f32_16x16x32_bf16 v[24:27], a[152:155], v[196:199], v[24:27]\n"
    "s_waitcnt lgkmcnt(0)\n"
    "v_pk_add_u16 v188, s89, v188 clamp\n"
    "v_pk_add_u16 v184, s89, v184 clamp\n"
    "v_sub_f32 v70, v70, v76\n"
    "v_pk_maximum3_f16 v98, v180, v181, v98\n"
    "v_pk_add_u16 v186, s89, v186 clamp\n"
    "v_mfma_f32_16x16x32_bf16 v[56:59], a[44:47], v[200:203], v[64:67]\n"
    "v_pk_add_u16 v189, s89, v189 clamp\n"
    "v_pk_add_u16 v195, s89, v195 clamp\n"
    "v_pk_add_u16 v192, s89, v192 clamp\n"
    "v_pk_add_u16 v185, s89, v185 clamp\n"
    "v_pk_maximum3_f16 v98, v182, v183, v98\n"
    "v_exp_f32 v55, v55\n"
    "v_sub_f32 v68, v68, v76\n"
    "v_mfma_f32_16x16x32_bf16 v[56:59], a[40:43], v[196:199], v[56:59]\n"
    "v_exp_f32 v52, v52\n"
    "v_pk_add_u16 v190, s89, v190 clamp\n"
    "v_mfma_f32_16x16x32_bf16 v[8:11], a[128:131], v[196:199], v[8:11]\n"
    "v_accvgpr_write_b32 a231, v195\n"
    "v_sub_f32 v102, v102, v76\n"
    "v_sub_f32 v71, v71, v76\n"
    "v_sub_f32 v107, v107, v76\n"
    "v_mfma_f32_16x16x32_bf16 v[40:43], a[20:23], v[200:203], v[48:51]\n"
    "v_sub_f32 v74, v74, v76\n"
    "v_add_f32 v23, v23, v35\n"
    "v_mfma_f32_16x16x32_bf16 v[40:43], a[16:19], v[196:199], v[40:43]\n"
    "v_pk_add_u16 v183, s89, v183 clamp\n"
    "v_accvgpr_write_b32 a228, v192\n"
    "v_mfma_f32_16x16x32_bf16 v[56:59], a[36:39], v[192:195], v[56:59]\n"
    "v_add_f32 v4, v4, v16\n"
    "v_exp_f32 v6, v6\n"
    "v_mfma_f32_16x16x32_bf16 v[24:27], a[148:151], v[192:195], v[24:27]\n"
    "v_sub_f32 v72, v72, v76\n"
    "v_pk_add_u16 v187, s89, v187 clamp\n"
    "v_pk_add_u16 v191, s89, v191 clamp\n"
    "v_mfma_f32_16x16x32_bf16 v[8:11], a[124:127], v[192:195], v[8:11]\n"
    "v_exp_f32 v53, v53\n"
    "v_accvgpr_write_b32 a226, v190\n"
    "v_mfma_f32_16x16x32_bf16 v[8:11], a[120:123], v[188:191], v[8:11]\n"
    "v_accvgpr_write_b32 a221, v185\n"
    "v_exp_f32 v39, v39\n"
    "v_exp_f32 v22, v22\n"
    "v_pk_add_u16 v181, s89, v181 clamp\n"
    "v_mfma_f32_16x16x32_bf16 v[56:59], a[32:35], v[188:191], v[56:59]\n"
    "v_exp_f32 v36, v36\n"
    "v_pk_add_u16 v182, s89, v182 clamp\n"
    "v_exp_f32 v23, v23\n"
    "v_mfma_f32_16x16x32_bf16 v[24:27], a[144:147], v[188:191], v[24:27]\n"
    "v_accvgpr_write_b32 a237, v201\n"
    "v_pk_add_u16 v180, s89, v180 clamp\n"
    "v_mfma_f32_16x16x32_bf16 v[40:43], a[12:15], v[192:195], v[40:43]\n"
    "v_accvgpr_write_b32 a239, v203\n"
    "v_accvgpr_write_b32 a223, v187\n"
    "v_exp_f32 v7, v7\n"
    "v_accvgpr_write_b32 a233, v197\n"
    "v_mfma_f32_16x16x32_bf16 v[8:11], a[116:119], v[184:187], v[8:11]\n"
    "v_accvgpr_write_b32 a232, v196\n"
    "v_accvgpr_write_b32 a235, v199\n"
    "v_mfma_f32_16x16x32_bf16 v[40:43], a[8:11], v[188:191], v[40:43]\n"
    "ds_write_b128 v86, v[68:71] offset:0\n"
    "v_mfma_f32_16x16x32_bf16 v[40:43], a[4:7], v[184:187], v[40:43]\n"
    "v_sub_f32 v106, v106, v76\n"
    "v_accvgpr_write_b32 a234, v198\n"
    "v_mfma_f32_16x16x32_bf16 v[8:11], a[112:115], v[180:183], v[8:11]\n"
    "v_cvt_pk_bf16_f32 v70, v20, v21\n"
    "v_exp_f32 v37, v37\n"
    "v_cvt_pk_bf16_f32 v69, v6, v7\n"
    "v_accvgpr_write_b32 a218, v182\n"
    "v_mfma_f32_16x16x32_bf16 v[40:43], a[0:3], v[180:183], v[40:43]\n"
    "ds_write_b128 v86, v[72:75] offset:16\n"
    "v_accvgpr_write_b32 a230, v194\n"
    "v_mfma_f32_16x16x32_bf16 v[24:27], a[140:143], v[184:187], v[24:27]\n"
    "v_exp_f32 v10, v10\n"
    "v_mfma_f32_16x16x32_bf16 v[56:59], a[28:31], v[184:187], v[56:59]\n"
    "v_cvt_pk_bf16_f32 v72, v36, v37\n"
    "v_sub_f32 v103, v103, v76\n"
    "v_mfma_f32_16x16x32_bf16 v[56:59], a[24:27], v[180:183], v[56:59]\n"
    "v_sub_f32 v104, v104, v76\n"
    "v_accvgpr_write_b32 a217, v181\n"
    "v_exp_f32 v4, v4\n"
    "v_sub_f32 v100, v100, v76\n"
    "v_accvgpr_write_b32 a227, v191\n"
    "v_exp_f32 v11, v11\n"
    "v_exp_f32 v5, v5\n"
    "v_exp_f32 v38, v38\n"
    "v_exp_f32 v41, v41\n"
    "v_exp_f32 v9, v9\n"
    "v_exp_f32 v59, v59\n"
    "v_accvgpr_write_b32 a220, v184\n"
    "v_accvgpr_write_b32 a238, v202\n"
    "v_accvgpr_write_b32 a236, v200\n"
    "v_exp_f32 v43, v43\n"
    "v_cvt_pk_bf16_f32 v77, v10, v11\n"
    "v_exp_f32 v8, v8\n"
    "v_cvt_pk_bf16_f32 v75, v54, v55\n"
    "v_mfma_f32_16x16x32_bf16 v[24:27], a[136:139], v[180:183], v[24:27]\n"
    "v_cvt_pk_bf16_f32 v71, v22, v23\n"
    "v_sub_f32 v105, v105, v76\n"
    "v_accvgpr_write_b32 a216, v180\n"
    "v_exp_f32 v58, v58\n"
    "v_cvt_pk_bf16_f32 v73, v38, v39\n"
    "v_exp_f32 v40, v40\n"
    "v_accvgpr_write_b32 a222, v186\n"
    "v_accvgpr_write_b32 a225, v189\n"
    "v_exp_f32 v24, v24\n"
    "v_exp_f32 v57, v57\n"
    "v_cvt_pk_bf16_f32 v68, v4, v5\n"
    "v_cvt_pk_bf16_f32 v80, v40, v41\n"
    "v_exp_f32 v27, v27\n"
    "v_exp_f32 v25, v25\n"
    "v_cvt_pk_bf16_f32 v76, v8, v9\n"
    "v_accvgpr_write_b32 a224, v188\n"
    "v_accvgpr_write_b32 a219, v183\n"
    "v_exp_f32 v26, v26\n"
    "v_exp_f32 v42, v42\n"
    "v_cvt_pk_bf16_f32 v78, v24, v25\n"
    "v_exp_f32 v56, v56\n"
    "v_cvt_pk_bf16_f32 v74, v52, v53\n"
    "v_accvgpr_write_b32 a229, v193\n"
    "v_cvt_pk_bf16_f32 v79, v26, v27\n"
    "v_cvt_pk_bf16_f32 v82, v56, v57\n"
    "v_cvt_pk_bf16_f32 v83, v58, v59\n"
    "v_cvt_pk_bf16_f32 v81, v42, v43\n"
    "s_waitcnt vmcnt(8)\n"
    "v_cmp_eq_u64 s[54:55], v[236:237], v[238:239]\n"
    "s_nop 0\n"
    "s_cmp_eq_u64 s[54:55], -1\n"
    "s_cbranch_scc0 .LpollAV\n"
    ".LpollAVr:\n"
    "s_add_u32 m0, s65, 24576\n"
    "s_add_u32 s72, s65, 49152\n"
    "buffer_load_dwordx4 v1, s[16:19], s72 offen offset:0 sc1 lds\n"
    "s_add_u32 m0, s65, 25600\n"
    "s_add_u32 s72, s65, 50176\n"
    "buffer_load_dwordx4 v1, s[16:19], s72 offen offset:0 sc1 lds\n"
    "s_add_u32 m0, s65, 26624\n"
    "s_add_u32 s72, s65, 51200\n"
    "buffer_load_dwordx4 v1, s[16:19], s72 offen offset:0 sc1 lds\n"
    "s_add_u32 m0, s65, 27648\n"
    "s_add_u32 s72, s65, 52224\n"
    "buffer_load_dwordx4 v1, s[16:19], s72 offen offset:0 sc1 lds\n"
    "s_add_u32 m0, s65, 28672\n"
    "s_add_u32 s72, s65, 53248\n"
    "buffer_load_dwordx4 v1, s[16:19], s72 offen offset:0 sc1 lds\n"
    "s_add_u32 m0, s65, 29696\n"
    "s_add_u32 s72, s65, 54272\n"
    "buffer_load_dwordx4 v1, s[16:19], s72 offen offset:0 sc1 lds\n"
    "s_waitcnt vmcnt(13)\n"
    "ds_read_b128 v[156:159], v96 offset:0\n"
    "ds_read_b128 v[172:175], v96 offset:256\n"
    "ds_read_b128 v[168:171], v97 offset:128\n"
    "ds_read_b128 v[176:179], v97 offset:256\n"
    "ds_read_b128 v[160:163], v97 offset:0\n"
    "ds_read_b128 v[164:167], v96 offset:128\n"
    "s_waitcnt lgkmcnt(5)\n"
    "v_pk_maximum3_f16 v98, v156, v157, v98\n"
    "v_pk_maximum3_f16 v98, v158, v159, v98\n"
    "v_pk_add_u16 v157, s89, v157 clamp\n"
    "v_pk_add_u16 v158, s89, v158 clamp\n"
    "v_pk_add_u16 v156, s89, v156 clamp\n"
    "v_pk_add_u16 v159, s89, v159 clamp\n"
    "s_waitcnt lgkmcnt(1)\n"
    "v_pk_maximum3_f16 v98, v160, v161, v98\n"
    "s_add_u32 m0, s65, 73728\n"
    "s_add_u32 s72, s65, 73728\n"
    "buffer_load_dwordx4 v1, s[16:19], s72 offen offset:0 sc1 lds\n"
    "v_mfma_f32_16x16x32_bf16 v[28:31], a[136:139], v[156:159], v[32:35]\n"
    "s_waitcnt lgkmcnt(0)\n"
    "v_pk_maximum3_f16 v98, v162, v163, v98\n"
    "v_pk_maximum3_f16 v98, v164, v165, v98\n"
    "v_mfma_f32_16x16x32_bf16 v[12:15], a[112:115], v[156:159], v[16:19]\n"
    "v_pk_add_u16 v161, s89, v161 clamp\n"
    "v_pk_maximum3_f16 v98, v166, v167, v98\n"
    "v_pk_add_u16 v160, s89, v160 clamp\n"
    "v_pk_add_u16 v162, s89, v162 clamp\n"
    "v_mfma_f32_16x16x32_bf16 v[60:63], a[24:27], v[156:159], v[64:67]\n"
    "v_pk_maximum3_f16 v98, v168, v169, v98\n"
    "v_pk_add_u16 v163, s89, v163 clamp\n"
    "v_mfma_f32_16x16x32_bf16 v[44:47], a[0:3], v[156:159], v[48:51]\n"
    "v_pk_maximum3_f16 v98, v170, v171, v98\n"
    "v_pk_add_u16 v164, s89, v164 clamp\n"
    "v_mfma_f32_16x16x32_bf16 v[12:15], a[116:119], v[160:163], v[12:15]\n"
    "v_pk_add_u16 v165, s89, v165 clamp\n"
    "s_add_u32 m0, s65, 74752\n"
    "s_add_u32 s72, s65, 74752\n"
    "buffer_load_dwordx4 v1, s[16:19], s72 offen offset:0 sc1 lds\n"
    "v_pk_add_u16 v169, s89, v169 clamp\n"
    "v_pk_add_u16 v168, s89, v168 clamp\n"
    "v_mfma_f32_16x16x32_bf16 v[60:63], a[28:31], v[160:163], v[60:63]\n"
    "v_pk_maximum3_f16 v98, v172, v173, v98\n"
    "v_pk_add_u16 v170, s89, v170 clamp\n"
    "v_mfma_f32_16x16x32_bf16 v[28:31], a[140:143], v[160:163], v[28:31]\n"
    "v_pk_maximum3_f16 v98, v174, v175, v98\n"
    "v_pk_add_u16 v166, s89, v166 clamp\n"
    "v_pk_add_u16 v167, s89, v167 clamp\n"
    "v_mfma_f32_16x16x32_bf16 v[44:47], a[4:7], v[160:163], v[44:47]\n"
    "v_pk_maximum3_f16 v98, v176, v177, v98\n"
    "v_accvgpr_write_b32 a251, v167\n"
    "v_mfma_f32_16x16x32_bf16 v[44:47], a[8:11], v[164:167], v[44:47]\n"
    "v_pk_maximum3_f16 v98, v178, v179, v98\n"
    "v_pk_add_u16 v176, s89, v176 clamp\n"
    "v_mfma_f32_16x16x32_bf16 v[60:63], a[32:35], v[164:167], v[60:63]\n"
    "v_pk_add_u16 v171, s89, v171 clamp\n"
    "s_add_u32 m0, s65, 75776\n"
    "s_add_u32 s72, s65, 75776\n"
    "buffer_load_dwordx4 v1, s[16:19], s72 offen offset:0 sc1 lds\n"
    "v_pk_add_u16 v174, s89, v174 clamp\n"
    "v_mfma_f32_16x16x32_bf16 v[28:31], a[144:147], v[164:167], v[28:31]\n"
    "v_pk_add_u16 v173, s89, v173 clamp\n"
    "v_pk_add_u16 v172, s89, v172 clamp\n"
    "v_mfma_f32_16x16x32_bf16 v[12:15], a[120:123], v[164:167], v[12:15]\n"
    "v_accvgpr_write_b32 a254, v170\n"
    "v_pk_add_u16 v178, s89, v178 clamp\n"
    "v_mfma_f32_16x16x32_bf16 v[28:31], a[148:151], v[168:171], v[28:31]\n"
    "v_accvgpr_write_b32 a243, v159\n"
    "v_accvgpr_write_b32 a162, v174\n"
    "v_mfma_f32_16x16x32_bf16 v[12:15], a[124:127], v[168:171], v[12:15]\n"
    "v_pk_add_u16 v179, s89, v179 clamp\n"
    "v_pk_add_u16 v175, s89, v175 clamp\n"
    "v_mfma_f32_16x16x32_bf16 v[44:47], a[12:15], v[168:171], v[44:47]\n"
    "v_accvgpr_write_b32 a166, v178\n"
    "v_accvgpr_write_b32 a161, v173\n"
    "v_accvgpr_write_b32 a245, v161\n"
    "s_add_u32 m0, s65, 76800\n"
    "s_add_u32 s72, s65, 76800\n"
    "buffer_load_dwordx4 v1, s[16:19], s72 offen offset:0 sc1 lds\n"
    "v_mfma_f32_16x16x32_bf16 v[28:31], a[152:155], v[172:175], v[28:31]\n"
    "v_accvgpr_write_b32 a163, v175\n"
    "v_pk_add_u16 v177, s89, v177 clamp\n"
    "v_mfma_f32_16x16x32_bf16 v[44:47], a[16:19], v[172:175], v[44:47]\n"
    "v_accvgpr_write_b32 a160, v172\n"
    "v_accvgpr_write_b32 a252, v168\n"
    "v_mfma_f32_16x16x32_bf16 v[28:31], a[156:159], v[176:179], v[28:31]\n"
    "v_accvgpr_write_b32 a249, v165\n"
    "v_accvgpr_write_b32 a255, v171\n"
    "v_mfma_f32_16x16x32_bf16 v[12:15], a[128:131], v[172:175], v[12:15]\n"
    "v_accvgpr_write_b32 a247, v163\n"
    "v_accvgpr_write_b32 a164, v176\n"
    "v_mfma_f32_16x16x32_bf16 v[60:63], a[36:39], v[168:171], v[60:63]\n"
    "v_accvgpr_write_b32 a242, v158\n"
    "v_accvgpr_write_b32 a250, v166\n"
    "v_mfma_f32_16x16x32_bf16 v[44:47], a[20:23], v[176:179], v[44:47]\n"
    "v_accvgpr_write_b32 a241, v157\n"
    "s_add_u32 m0, s65, 77824\n"
    "s_add_u32 s72, s65, 77824\n"
    "buffer_load_dwordx4 v1, s[16:19], s72 offen offset:0 sc1 lds\n"
    "v_accvgpr_write_b32 a248, v164\n"
    "v_mfma_f32_16x16x32_bf16 v[12:15], a[132:135], v[176:179], v[12:15]\n"
    "v_accvgpr_write_b32 a246, v162\n"
    "v_exp_f32 v29, v29\n"
    "v_mfma_f32_16x16x32_bf16 v[60:63], a[40:43], v[172:175], v[60:63]\n"
    "v_exp_f32 v28, v28\n"
    "v_mfma_f32_16x16x32_bf16 v[60:63], a[44:47], v[176:179], v[60:63]\n"
    "v_exp_f32 v31, v31\n"
    "v_exp_f32 v30, v30\n"
    "v_exp_f32 v45, v45\n"
    "v_cvt_pk_bf16_f32 v86, v28, v29\n"
    "v_exp_f32 v47, v47\n"
    "v_accvgpr_write_b32 a240, v156\n"
    "v_exp_f32 v12, v12\n"
    "v_exp_f32 v14, v14\n"
    "v_accvgpr_write_b32 a167, v179\n"
    "v_exp_f32 v15, v15\n"
    "s_add_u32 m0, s65, 78848\n"
    "s_add_u32 s72, s65, 78848\n"
    "buffer_load_dwordx4 v1, s[16:19], s72 offen offset:0 sc1 lds\n"
    "v_accvgpr_write_b32 a253, v169\n"
    "v_exp_f32 v63, v63\n"
    "v_exp_f32 v13, v13\n"
    "v_exp_f32 v44, v44\n"
    "v_cvt_pk_bf16_f32 v84, v12, v13\n"
    "v_cvt_pk_bf16_f32 v87, v30, v31\n"
    "v_exp_f32 v46, v46\n"
    "v_exp_f32 v61, v61\n"
    "v_accvgpr_write_b32 a165, v177\n"
    "v_exp_f32 v60, v60\n"
    "v_exp_f32 v62, v62\n"
    "v_cvt_pk_bf16_f32 v90, v60, v61\n"
    "v_accvgpr_write_b32 a244, v160\n"
    "v_cvt_pk_bf16_f32 v85, v14, v15\n"
    "v_cvt_pk_bf16_f32 v89, v46, v47\n"
    "v_cvt_pk_bf16_f32 v88, v44, v45\n"
    "v_cvt_pk_bf16_f32 v91, v62, v63\n"
    "s_bitcmp1_b32 s83, 1\n"
    "s_cbranch_scc1 .Lf4b\n"
    "s_lshl_b32 s73, s53, 1\n"
    "s_add_u32 s73, s73, s79\n"
    "s_mul_i32 s74, s73, 6144\n"
    "s_add_u32 s74, s74, 1425408\n"
    "s_lshl_b32 s73, s73, 8\n"
    "s_add_u32 s73, s73, 1557504\n"
    "s_mov_b64 s[20:21], s[16:17]\n"
    "s_mov_b32 s44, 98304\n"
    "s_mov_b32 s22, 1572864\n"
    "s_bitcmp1_b32 s84, 0\n"
    "s_cselect_b32 s52, s71, s73\n"
    "s_cselect_b32 s71, s71, s74\n"
    "s_cselect_b32 s63, s63, 0\n"
    "s_cselect_b32 s45, s44, 12288\n"
    "s_cselect_b32 s44, s44, 24576\n"
    "s_cselect_b32 s42, 1523712, s42\n"
    "s_cselect_b32 s22, s22, s18\n"
    "s_cselect_b32 s60, s60, 24576\n"
    "s_cselect_b32 s61, s61, 28672\n"
    "s_cselect_b32 s62, s62, 32768\n"
    "s_mov_b32 s66, 1\n"
    "s_add_u32 s68, s68, 24576\n"
    "s_add_u32 s81, s81, 128\n"
    "s_add_u32 s80, s80, 256\n"
    "s_add_u32 s71, s71, s44\n"
    "s_add_u32 s52, s52, s45\n"
    "s_add_u32 s75, s71, s63\n"
    "s_branch .LhVP\n"
    "s_branch .Lf4e\n"
    ".Lf4b:\n"
    "s_add_u32 s75, s71, s63\n"
    "buffer_load_dwordx4 v[204:207], v242, s[40:43], s75 offen offset:0 nt\n"
    "buffer_load_dwordx4 v[208:211], v242, s[40:43], s75 offen offset:768 nt\n"
    "buffer_load_dwordx4 v[212:215], v242, s[40:43], s75 offen offset:256 nt\n"
    "buffer_load_dwordx4 v[216:219], v242, s[40:43], s75 offen offset:1024 nt\n"
    "buffer_load_dwordx4 v[220:223], v242, s[40:43], s75 offen offset:512 nt\n"
    "buffer_load_dwordx4 v[224:227], v242, s[40:43], s75 offen offset:1280 nt\n"
    "s_lshl_b32 s73, s53, 1\n"
    "s_add_u32 s73, s73, s79\n"
    "s_mul_i32 s74, s73, 6144\n"
    "s_add_u32 s74, s74, 1425408\n"
    "s_lshl_b32 s73, s73, 8\n"
    "s_add_u32 s73, s73, 1557504\n"
    "s_mov_b64 s[20:21], s[16:17]\n"
    "s_mov_b32 s44, 98304\n"
    "s_mov_b32 s22, 1572864\n"
    "s_bitcmp1_b32 s84, 0\n"
    "s_cselect_b32 s52, s71, s73\n"
    "s_cselect_b32 s71, s71, s74\n"
    "s_cselect_b32 s63, s63, 0\n"
    "s_cselect_b32 s45, s44, 12288\n"
    "s_cselect_b32 s44, s44, 24576\n"
    "s_cselect_b32 s42, 1523712, s42\n"
    "s_cselect_b32 s22, s22, s18\n"
    "s_cselect_b32 s60, s60, 24576\n"
    "s_cselect_b32 s61, s61, 28672\n"
    "s_cselect_b32 s62, s62, 32768\n"
    "s_mov_b32 s66, 1\n"
    "s_add_u32 s68, s68, 24576\n"
    "s_add_u32 s81, s81, 128\n"
    "s_branch .LhVN\n"
    ".Lf4e:\n"
    "s_nop 4\n"
    ".Lf5e:\n"
    "s_nop 4\n"
    ".fill ((0x20000 - (. - _Z8attn_fwdPKtPKiS2_S0_S0_S0_S0_PcPt)) & 4095) / 4, 4, 0xBF800000\n"
    ".LhKP:\n"
    "s_waitcnt vmcnt(12)\n"
    "ds_read_b128 v[52:55], v251 offset:0\n"
    "ds_read_b128 v[192:195], v97 offset:49280\n"
    "ds_read_b128 v[196:199], v96 offset:49408\n"
    "ds_read_b128 v[200:203], v97 offset:49408\n"
    "s_add_u32 s83, s67, 4096\n"
    "ds_read_b128 v[188:191], v96 offset:49280\n"
    "ds_read_b128 v[180:183], v96 offset:49152\n"
    "s_nop 0\n"
    "ds_read_b128 v[184:187], v97 offset:49152\n"
    "s_waitcnt lgkmcnt(6)\n"
    "v_smfmac_f32_16x16x64_bf16 v[108:111], v[52:55], v[68:75], v253\n"
    "s_waitcnt lgkmcnt(3)\n"
    "v_pk_maximum3_f16 v98, v200, v201, v98\n"
    "v_pk_maximum3_f16 v98, v202, v203, v98\n"
    "v_pk_maximum3_f16 v98, v196, v197, v98\n"
    "v_pk_add_u16 v203, s89, v203 clamp\n"
    "v_pk_maximum3_f16 v98, v198, v199, v98\n"
    "v_pk_add_u16 v200, s89, v200 clamp\n"
    "v_pk_add_u16 v202, s89, v202 clamp\n"
    "v_pk_add_u16 v201, s89, v201 clamp\n"
    "v_pk_maximum3_f16 v98, v192, v193, v98\n"
    "v_pk_maximum3_f16 v98, v194, v195, v98\n"
    "v_mfma_f32_16x16x32_bf16 v[16:19], a[132:135], v[200:203], v[16:19]\n"
    "s_add_u32 m0, s68, .Lend-.Lend2+(0)\n"
    "v_pk_add_u16 v199, s89, v199 clamp\n"
    "v_pk_add_u16 v197, s89, v197 clamp\n"
    "v_mfma_f32_16x16x32_bf16 v[64:67], a[44:47], v[200:203], v[64:67]\n"
    "buffer_load_dwordx4 v1, s[16:19], s67 offen offset:0 sc1 lds\n"
    "v_pk_add_u16 v198, s89, v198 clamp\n"
    "v_mfma_f32_16x16x32_bf16 v[32:35], a[156:159], v[200:203], v[32:35]\n"
    "v_pk_add_u16 v196, s89, v196 clamp\n"
    "v_accvgpr_write_b32 a191, v203\n"
    "v_mfma_f32_16x16x32_bf16 v[48:51], a[20:23], v[200:203], v[48:51]\n"
    "s_waitcnt lgkmcnt(2)\n"
    "v_pk_maximum3_f16 v98, v188, v189, v98\n"
    "v_pk_add_u16 v195, s89, v195 clamp\n"
    "v_mfma_f32_16x16x32_bf16 v[64:67], a[40:43], v[196:199], v[64:67]\n"
    "v_pk_add_u16 v192, s89, v192 clamp\n"
    "v_pk_add_u16 v193, s89, v193 clamp\n"
    "v_mfma_f32_16x16x32_bf16 v[32:35], a[152:155], v[196:199], v[32:35]\n"
    "v_pk_maximum3_f16 v98, v190, v191, v98\n"
    "v_pk_add_u16 v194, s89, v194 clamp\n"
    "v_smfmac_f32_16x16x64_bf16 v[116:119], v[52:55], v[84:91], v253\n"
    "v_pk_add_u16 v191, s89, v191 clamp\n"
    "v_mfma_f32_16x16x32_bf16 v[48:51], a[16:19], v[196:199], v[48:51]\n"
    "buffer_load_dwordx4 v1, s[16:19], s67 offen offset:1024 sc1 lds\n"
    "v_mfma_f32_16x16x32_bf16 v[16:19], a[128:131], v[196:199], v[16:19]\n"
    "s_waitcnt lgkmcnt(0)\n"
    "v_pk_maximum3_f16 v98, v184, v185, v98\n"
    "v_pk_add_u16 v189, s89, v189 clamp\n"
    "v_mfma_f32_16x16x32_bf16 v[48:51], a[12:15], v[192:195], v[48:51]\n"
    "v_pk_add_u16 v190, s89, v190 clamp\n"
    "v_pk_add_u16 v184, s89, v184 clamp\n"
    "v_accvgpr_write_b32 a188, v200\n"
    "v_mfma_f32_16x16x32_bf16 v[64:67], a[36:39], v[192:195], v[64:67]\n"
    "v_pk_maximum3_f16 v98, v186, v187, v98\n"
    "v_pk_add_u16 v188, s89, v188 clamp\n"
    "v_mfma_f32_16x16x32_bf16 v[32:35], a[148:151], v[192:195], v[32:35]\n"
    "v_pk_add_u16 v187, s89, v187 clamp\n"
    "v_pk_maximum3_f16 v98, v180, v181, v98\n"
    "v_mfma_f32_16x16x32_bf16 v[32:35], a[144:147], v[188:191], v[32:35]\n"
    "v_pk_maximum3_f16 v98, v182, v183, v98\n"
    "v_pk_add_u16 v185, s89, v185 clamp\n"
    "v_mfma_f32_16x16x32_bf16 v[16:19], a[124:127], v[192:195], v[16:19]\n"
    "buffer_load_dwordx4 v1, s[16:19], s67 offen offset:2048 sc1 lds\n"
    "v_mfma_f32_16x16x32_bf16 v[16:19], a[120:123], v[188:191], v[16:19]\n"
    "s_waitcnt vmcnt(9)\n"
    "v_pk_add_u16 v186, s89, v186 clamp\n"
    "v_pk_add_u16 v181, s89, v181 clamp\n"
    "v_mfma_f32_16x16x32_bf16 v[64:67], a[32:35], v[188:191], v[64:67]\n"
    "s_barrier\n"
    "ds_read_b128 a[48:51], v252 offset:24576\n"
    "v_mfma_f32_16x16x32_bf16 v[48:51], a[8:11], v[188:191], v[48:51]\n"
    "ds_read_b128 a[72:75], v252 offset:25600\n"
    "ds_read_b128 a[92:95], v252 offset:35840\n"
    "v_mfma_f32_16x16x32_bf16 v[32:35], a[140:143], v[184:187], v[32:35]\n"
    "v_pk_add_u16 v183, s89, v183 clamp\n"
    "v_pk_add_u16 v180, s89, v180 clamp\n"
    "v_pk_add_u16 v182, s89, v182 clamp\n"
    "v_mfma_f32_16x16x32_bf16 v[16:19], a[116:119], v[184:187], v[16:19]\n"
    "ds_read_b128 a[84:87], v252 offset:31744\n"
    "ds_read_b128 a[52:55], v252 offset:26624\n"
    "v_mfma_f32_16x16x32_bf16 v[32:35], a[136:139], v[180:183], v[32:35]\n"
    "buffer_load_dwordx4 v1, s[16:19], s67 offen offset:3072 sc1 lds\n"
    "v_mfma_f32_16x16x32_bf16 v[48:51], a[4:7], v[184:187], v[48:51]\n"
    "ds_read_b128 a[88:91], v252 offset:33792\n"
    "ds_read_b128 a[56:59], v252 offset:28672\n"
    "v_mfma_f32_16x16x32_bf16 v[64:67], a[28:31], v[184:187], v[64:67]\n"
    "v_accvgpr_write_b32 a170, v182\n"
    "v_accvgpr_write_b32 a177, v189\n"
    "v_mfma_f32_16x16x32_bf16 v[16:19], a[112:115], v[180:183], v[16:19]\n"
    "s_add_u32 m0, s68, 4096\n"
    "ds_read_b128 a[76:79], v252 offset:27648\n"
    "ds_read_b128 a[68:71], v252 offset:34816\n"
    "ds_read_b128 a[60:63], v252 offset:30720\n"
    "v_mfma_f32_16x16x32_bf16 v[48:51], a[0:3], v[180:183], v[48:51]\n"
    "s_waitcnt lgkmcnt(9)\n"
    "ds_read_b128 a[80:83], v252 offset:29696\n"
    "v_accvgpr_write_b32 a179, v191\n"
    "v_mfma_f32_16x16x32_bf16 v[4:7], a[48:51], a[192:195], v[100:103]\n"
    "s_waitcnt lgkmcnt(8)\n"
    "v_pk_add_u16 v98, s89, v98\n"
    "v_accvgpr_write_b32 a174, v186\n"
    "v_mfma_f32_16x16x32_bf16 v[24:27], a[92:95], a[236:239], v[104:107]\n"
    "buffer_load_dwordx4 v1, s[16:19], s83 offen offset:0 sc1 lds\n"
    "v_mfma_f32_16x16x32_bf16 v[12:15], a[48:51], a[240:243], v[100:103]\n"
    "v_accvgpr_write_b32 a186, v198\n"
    "v_accvgpr_write_b32 a180, v192\n"
    "v_mfma_f32_16x16x32_bf16 v[20:23], a[72:75], a[192:195], v[104:107]\n"
    "v_accvgpr_write_b32 a181, v193\n"
    "v_accvgpr_write_b32 a172, v184\n"
    "v_mfma_f32_16x16x32_bf16 v[28:31], a[72:75], a[240:243], v[104:107]\n"
    "s_waitcnt lgkmcnt(6)\n"
    "v_exp_f32 v33, v33\n"
    "v_mfma_f32_16x16x32_bf16 v[4:7], a[52:55], a[196:199], v[4:7]\n"
    "ds_read_b128 a[64:67], v252 offset:32768\n"
    "v_accvgpr_write_b32 a184, v196\n"
    "v_mfma_f32_16x16x32_bf16 v[12:15], a[52:55], a[244:247], v[12:15]\n"
    "s_waitcnt lgkmcnt(6)\n"
    "v_accvgpr_write_b32 a168, v180\n"
    "v_accvgpr_write_b32 a182, v194\n"
    "v_mfma_f32_16x16x32_bf16 v[24:27], a[88:91], a[232:235], v[24:27]\n"
    "s_waitcnt lgkmcnt(3)\n"
    "v_accvgpr_write_b32 a183, v195\n"
    "v_accvgpr_write_b32 a190, v202\n"
    "v_mfma_f32_16x16x32_bf16 v[8:11], a[68:71], a[236:239], v[100:103]\n"
    "buffer_load_dwordx4 v1, s[16:19], s83 offen offset:1024 sc1 lds\n"
    "v_mfma_f32_16x16x32_bf16 v[4:7], a[56:59], a[200:203], v[4:7]\n"
    "v_accvgpr_write_b32 a173, v185\n"
    "v_accvgpr_write_b32 a171, v183\n"
    "v_mfma_f32_16x16x32_bf16 v[12:15], a[56:59], a[248:251], v[12:15]\n"
    "s_add_u32 m0, s64, 0\n"
    "v_accvgpr_write_b32 a176, v188\n"
    "v_accvgpr_write_b32 a189, v201\n"
    "v_accvgpr_write_b32 a169, v181\n"
    "v_mfma_f32_16x16x32_bf16 v[64:67], a[24:27], v[180:183], v[64:67]\n"
    "v_accvgpr_write_b32 a175, v187\n"
    "v_and_b32 v98, 0x80008000, v98\n"
    "v_mfma_f32_16x16x32_bf16 v[20:23], a[76:79], a[196:199], v[20:23]\n"
    "s_waitcnt lgkmcnt(0)\n"
    "v_cmp_ne_u32 s[34:35], 0, v98\n"
    "v_accvgpr_write_b32 a187, v199\n"
    "v_mfma_f32_16x16x32_bf16 v[8:11], a[64:67], a[232:235], v[8:11]\n"
    "s_add_u32 s83, s67, 24576\n"
    "v_accvgpr_write_b32 a185, v197\n"
    "v_accvgpr_write_b32 a178, v190\n"
    "v_mfma_f32_16x16x32_bf16 v[24:27], a[84:87], a[228:231], v[24:27]\n"
    "buffer_load_dwordx4 v1, s[16:19], s83 offen offset:0 sc1 lds\n"
    "v_mfma_f32_16x16x32_bf16 v[12:15], a[60:63], a[252:255], v[12:15]\n"
    "v_exp_f32 v32, v32\n"
    "v_mfma_f32_16x16x32_bf16 v[4:7], a[60:63], a[204:207], v[4:7]\n"
    "v_exp_f32 v18, v18\n"
    "v_mfma_f32_16x16x32_bf16 v[12:15], a[64:67], a[160:163], v[12:15]\n"
    "v_exp_f32 v48, v48\n"
    "v_mfma_f32_16x16x32_bf16 v[4:7], a[64:67], a[208:211], v[4:7]\n"
    "v_exp_f32 v19, v19\n"
    "v_mfma_f32_16x16x32_bf16 v[4:7], a[68:71], a[212:215], v[4:7]\n"
    "v_cvt_pk_bf16_f32 v93, v18, v19\n"
    "v_exp_f32 v17, v17\n"
    "v_mfma_f32_16x16x32_bf16 v[28:31], a[76:79], a[244:247], v[28:31]\n"
    "buffer_load_dwordx4 v1, s[16:19], s83 offen offset:1024 sc1 lds\n"
    "v_mfma_f32_16x16x32_bf16 v[24:27], a[80:83], a[224:227], v[24:27]\n"
    "v_exp_f32 v35, v35\n"
    "v_mfma_f32_16x16x32_bf16 v[20:23], a[80:83], a[200:203], v[20:23]\n"
    "v_exp_f32 v16, v16\n"
    "v_mfma_f32_16x16x32_bf16 v[8:11], a[60:63], a[228:231], v[8:11]\n"
    "v_exp_f32 v34, v34\n"
    "v_mfma_f32_16x16x32_bf16 v[20:23], a[84:87], a[204:207], v[20:23]\n"
    "v_cvt_pk_bf16_f32 v95, v34, v35\n"
    "v_mfma_f32_16x16x32_bf16 v[8:11], a[56:59], a[224:227], v[8:11]\n"
    "v_exp_f32 v66, v66\n"
    "v_mfma_f32_16x16x32_bf16 v[24:27], a[76:79], a[220:223], v[24:27]\n"
    "v_cvt_pk_bf16_f32 v94, v32, v33\n"
    "v_mfma_f32_16x16x32_bf16 v[8:11], a[52:55], a[220:223], v[8:11]\n"
    "buffer_load_dwordx4 v1, s[16:19], s83 offen offset:2048 sc1 lds\n"
    "v_mfma_f32_16x16x32_bf16 v[32:35], a[92:95], a[188:191], v[104:107]\n"
    "v_exp_f32 v49, v49\n"
    "v_mfma_f32_16x16x32_bf16 v[12:15], a[68:71], a[164:167], v[12:15]\n"
    "v_exp_f32 v67, v67\n"
    "v_mfma_f32_16x16x32_bf16 v[32:35], a[88:91], a[184:187], v[32:35]\n"
    "s_waitcnt vmcnt(9)\n"
    "s_barrier\n"
    "ds_read_b128 a[116:119], v248 offset:12288\n"
    "v_mfma_f32_16x16x32_bf16 v[8:11], a[48:51], a[216:219], v[8:11]\n"
    "ds_read_b128 a[144:147], v248 offset:4096\n"
    "v_cvt_pk_bf16_f32 v99, v66, v67\n"
    "v_mfma_f32_16x16x32_bf16 v[32:35], a[84:87], a[180:183], v[32:35]\n"
    "ds_read_b128 a[124:127], v248 offset:13312\n"
    "v_cvt_pk_bf16_f32 v92, v16, v17\n"
    "v_mfma_f32_16x16x32_bf16 v[16:19], a[68:71], a[188:191], v[100:103]\n"
    "buffer_load_dwordx4 v1, s[16:19], s83 offen offset:3072 sc1 lds\n"
    "v_mfma_f32_16x16x32_bf16 v[20:23], a[88:91], a[208:211], v[20:23]\n"
    "s_add_u32 s83, s67, 28672\n"
    "ds_read_b128 a[136:139], v248 offset:3072\n"
    "ds_read_b128 a[140:143], v248 offset:15360\n"
    "v_mfma_f32_16x16x32_bf16 v[32:35], a[80:83], a[176:179], v[32:35]\n"
    "ds_read_b128 a[120:123], v248 offset:1024\n"
    "ds_read_b128 a[148:151], v248 offset:16384\n"
    "v_mfma_f32_16x16x32_bf16 v[28:31], a[80:83], a[248:251], v[28:31]\n"
    "v_exp_f32 v64, v64\n"
    "v_mfma_f32_16x16x32_bf16 v[28:31], a[84:87], a[252:255], v[28:31]\n"
    "s_add_u32 m0, s64, 4096\n"
    "v_exp_f32 v50, v50\n"
    "v_mfma_f32_16x16x32_bf16 v[16:19], a[64:67], a[184:187], v[16:19]\n"
    "v_cvt_pk_bf16_f32 v96, v48, v49\n"
    "v_mfma_f32_16x16x32_bf16 v[16:19], a[60:63], a[180:183], v[16:19]\n"
    "ds_read_b128 a[152:155], v248 offset:5120\n"
    "ds_read_b128 a[132:135], v248 offset:14336\n"
    "v_mfma_f32_16x16x32_bf16 v[32:35], a[76:79], a[172:175], v[32:35]\n"
    "s_waitcnt lgkmcnt(7)\n"
    "buffer_load_dwordx4 v1, s[16:19], s83 offen offset:0 sc1 lds\n"
    "v_mfma_f32_16x16x32_bf16 a[32:35], a[144:147], v[92:95], 0\n"
    "ds_read_b128 a[128:131], v248 offset:2048\n"
    "v_smfmac_f32_16x16x64_bf16 v[112:115], v[52:55], v[76:83], v253\n"
    "v_mfma_f32_16x16x32_bf16 v[24:27], a[72:75], a[216:219], v[24:27]\n"
    "s_waitcnt lgkmcnt(5)\n"
    "v_exp_f32 v65, v65\n"
    "v_mfma_f32_16x16x32_bf16 v[152:155], a[140:143], v[80:83], 0\n"
    "s_waitcnt lgkmcnt(4)\n"
    "v_mfma_f32_16x16x32_bf16 v[144:147], a[120:123], v[76:79], 0\n"
    "v_exp_f32 v51, v51\n"
    "v_mfma_f32_16x16x32_bf16 v[16:19], a[56:59], a[176:179], v[16:19]\n"
    "s_waitcnt lgkmcnt(3)\n"
    "buffer_load_dwordx4 v1, s[16:19], s83 offen offset:1024 sc1 lds\n"
    "v_mfma_f32_16x16x32_bf16 v[188:191], a[148:151], v[72:75], 0\n"
    "s_waitcnt lgkmcnt(1)\n"
    "v_cvt_pk_bf16_f32 v97, v50, v51\n"
    "v_cvt_pk_bf16_f32 v98, v64, v65\n"
    "v_mfma_f32_16x16x32_bf16 v[148:151], a[132:135], v[80:83], 0\n"
    "s_waitcnt lgkmcnt(0)\n"
    "ds_read_b128 a[112:115], v248 offset:0\n"
    "v_mfma_f32_16x16x32_bf16 v[148:151], a[128:131], v[76:79], v[148:151]\n"
    "v_exp_f32 v4, v4\n"
    "v_mfma_f32_16x16x32_bf16 v[180:183], a[132:135], v[96:99], 0\n"
    "v_mfma_f32_16x16x32_bf16 v[176:179], a[120:123], v[92:95], 0\n"
    "v_exp_f32 v14, v14\n"
    "v_mfma_f32_16x16x32_bf16 v[28:31], a[88:91], a[160:163], v[28:31]\n"
    "v_exp_f32 v10, v10\n"
    "v_mfma_f32_16x16x32_bf16 v[164:167], a[128:131], v[84:87], 0\n"
    "buffer_load_dwordx2 v[204:205], v242, s[28:31], s75 offen offset:0 nt\n"
    "v_mfma_f32_16x16x32_bf16 v[176:179], a[124:127], v[96:99], v[176:179]\n"
    "v_mfma_f32_16x16x32_bf16 v[20:23], a[92:95], a[212:215], v[20:23]\n"
    "buffer_load_dwordx2 v[206:207], v242, s[28:31], s75 offen offset:768 nt\n"
    "v_mfma_f32_16x16x32_bf16 v[16:19], a[52:55], a[172:175], v[16:19]\n"
    "v_mfma_f32_16x16x32_bf16 v[132:135], a[128:131], v[68:71], 0\n"
    "buffer_load_dwordx2 v[208:209], v242, s[28:31], s75 offen offset:3072 nt\n"
    "v_mfma_f32_16x16x32_bf16 v[136:139], a[136:139], v[68:71], 0\n"
    "v_mfma_f32_16x16x32_bf16 v[16:19], a[48:51], a[168:171], v[16:19]\n"
    "buffer_load_dwordx2 v[210:211], v242, s[28:31], s75 offen offset:3840 nt\n"
    "v_mfma_f32_16x16x32_bf16 v[180:183], a[128:131], v[92:95], v[180:183]\n"
    "v_mfma_f32_16x16x32_bf16 v[184:187], a[140:143], v[96:99], 0\n"
    "s_waitcnt lgkmcnt(0)\n"
    "buffer_load_dwordx2 v[212:213], v242, s[28:31], s76 offen offset:0 nt\n"
    "v_mfma_f32_16x16x32_bf16 v[156:159], a[112:115], v[84:87], 0\n"
    "v_mfma_f32_16x16x32_bf16 a[16:19], a[148:151], v[88:91], 0\n"
    "buffer_load_dwordx2 v[214:215], v242, s[28:31], s76 offen offset:768 nt\n"
    "v_mfma_f32_16x16x32_bf16 v[168:171], a[136:139], v[84:87], 0\n"
    "v_exp_f32 v22, v22\n"
    "v_mfma_f32_16x16x32_bf16 v[152:155], a[136:139], v[76:79], v[152:155]\n"
    "buffer_load_dwordx2 v[216:217], v242, s[28:31], s76 offen offset:3072 nt\n"
    "v_mfma_f32_16x16x32_bf16 v[160:163], a[124:127], v[88:91], 0\n"
    "v_mfma_f32_16x16x32_bf16 v[140:143], a[116:119], v[80:83], 0\n"
    "buffer_load_dwordx2 v[218:219], v242, s[28:31], s76 offen offset:3840 nt\n"
    "v_mfma_f32_16x16x32_bf16 v[132:135], a[132:135], v[72:75], v[132:135]\n"
    "ds_read_b128 v[100:103], v250 offset:896\n"
    "ds_read_b128 a[128:131], v248 offset:8192\n"
    "v_mfma_f32_16x16x32_bf16 a[16:19], a[144:147], v[84:87], a[16:19]\n"
    "buffer_load_dwordx2 v[220:221], v242, s[28:31], s77 offen offset:0 nt\n"
    "v_mfma_f32_16x16x32_bf16 v[140:143], a[112:115], v[76:79], v[140:143]\n"
    "v_mfma_f32_16x16x32_bf16 v[128:131], a[124:127], v[72:75], 0\n"
    "buffer_load_dwordx2 v[222:223], v242, s[28:31], s77 offen offset:768 nt\n"
    "v_mfma_f32_16x16x32_bf16 a[0:3], a[144:147], v[76:79], 0\n"
    "v_exp_f32 v8, v8\n"
    "v_mfma_f32_16x16x32_bf16 a[20:23], a[152:155], v[84:87], 0\n"
    "buffer_load_dwordx2 v[224:225], v242, s[28:31], s77 offen offset:3072 nt\n"
    "v_mfma_f32_16x16x32_bf16 v[188:191], a[144:147], v[68:71], v[188:191]\n"
    "v_mfma_f32_16x16x32_bf16 v[144:147], a[124:127], v[80:83], v[144:147]\n"
    "buffer_load_dwordx2 v[226:227], v242, s[28:31], s77 offen offset:3840 nt\n"
    "v_mfma_f32_16x16x32_bf16 a[0:3], a[148:151], v[80:83], a[0:3]\n"
    "v_exp_f32 v15, v15\n"
    "v_mfma_f32_16x16x32_bf16 v[172:175], a[116:119], v[96:99], 0\n"
    "s_waitcnt lgkmcnt(0)\n"
    "buffer_load_dwordx2 v[228:229], v242, s[28:31], s78 offen offset:0 nt\n"
    "v_mfma_f32_16x16x32_bf16 a[48:51], a[128:131], v[68:71], 0\n"
    "ds_read_b128 a[156:159], v248 offset:17408\n"
    "v_mfma_f32_16x16x32_bf16 v[164:167], a[132:135], v[88:91], v[164:167]\n"
    "buffer_load_dwordx2 v[230:231], v242, s[28:31], s78 offen offset:768 nt\n"
    "v_mfma_f32_16x16x32_bf16 v[124:127], a[112:115], v[68:71], 0\n"
    "v_mfma_f32_16x16x32_bf16 v[172:175], a[112:115], v[92:95], v[172:175]\n"
    "buffer_load_dwordx2 v[232:233], v242, s[28:31], s78 offen offset:3072 nt\n"
    "v_mfma_f32_16x16x32_bf16 a[32:35], a[148:151], v[96:99], a[32:35]\n"
    "v_mfma_f32_16x16x32_bf16 v[128:131], a[120:123], v[68:71], v[128:131]\n"
    "buffer_load_dwordx2 v[234:235], v242, s[28:31], s78 offen offset:3840 nt\n"
    "v_mfma_f32_16x16x32_bf16 v[192:195], a[152:155], v[68:71], 0\n"
    "ds_read_b128 a[132:135], v248 offset:20480\n"
    "ds_read_b128 a[112:115], v248 offset:6144\n"
    "v_mfma_f32_16x16x32_bf16 v[160:163], a[120:123], v[84:87], v[160:163]\n"
    "s_waitcnt lgkmcnt(2)\n"
    "v_mfma_f32_16x16x32_bf16 a[36:39], a[156:159], v[96:99], 0\n"
    "ds_read_b128 a[144:147], v248 offset:10240\n"
    "ds_read_b128 a[124:127], v248 offset:19456\n"
    "v_mfma_f32_16x16x32_bf16 v[32:35], a[72:75], a[168:171], v[32:35]\n"
    "v_exp_f32 v7, v7\n"
    "v_mfma_f32_16x16x32_bf16 v[192:195], a[156:159], v[72:75], v[192:195]\n"
    "v_exp_f32 v21, v21\n"
    "v_mfma_f32_16x16x32_bf16 v[184:187], a[136:139], v[92:95], v[184:187]\n"
    "ds_read_b128 a[120:123], v248 offset:7168\n"
    "v_mfma_f32_16x16x32_bf16 v[28:31], a[92:95], a[164:167], v[28:31]\n"
    "v_mfma_f32_16x16x32_bf16 v[168:171], a[140:143], v[88:91], v[168:171]\n"
    "ds_read_b128 a[148:151], v248 offset:22528\n"
    "ds_read_b128 a[136:139], v248 offset:9216\n"
    "v_mfma_f32_16x16x32_bf16 a[4:7], a[156:159], v[80:83], 0\n"
    "v_exp_f32 v33, v33\n"
    "v_mfma_f32_16x16x32_bf16 a[36:39], a[152:155], v[92:95], a[36:39]\n"
    "v_mfma_f32_16x16x32_bf16 v[124:127], a[116:119], v[72:75], v[124:127]\n"
    "v_exp_f32 v16, v16\n"
    "v_mfma_f32_16x16x32_bf16 a[4:7], a[152:155], v[76:79], a[4:7]\n"
    "v_exp_f32 v5, v5\n"
    "v_mfma_f32_16x16x32_bf16 v[136:139], a[140:143], v[72:75], v[136:139]\n"
    "ds_read_b128 a[140:143], v248 offset:21504\n"
    "ds_read_b128 a[152:155], v248 offset:11264\n"
    "v_mfma_f32_16x16x32_bf16 v[156:159], a[116:119], v[88:91], v[156:159]\n"
    "v_smfmac_f32_16x16x64_bf16 v[120:123], v[52:55], v[92:99], v253\n"
    "s_waitcnt lgkmcnt(8)\n"
    "v_mfma_f32_16x16x32_bf16 a[64:67], a[132:135], v[80:83], 0\n"
    "s_waitcnt lgkmcnt(6)\n"
    "v_mfma_f32_16x16x32_bf16 a[104:107], a[144:147], v[92:95], 0\n"
    "s_waitcnt lgkmcnt(2)\n"
    "v_exp_f32 v20, v20\n"
    "v_mfma_f32_16x16x32_bf16 a[52:55], a[136:139], v[68:71], 0\n"
    "ds_read_b128 a[116:119], v248 offset:18432\n"
    "v_mfma_f32_16x16x32_bf16 a[20:23], a[156:159], v[88:91], a[20:23]\n"
    "ds_read_b128 a[156:159], v248 offset:23552\n"
    "v_mfma_f32_16x16x32_bf16 v[196:199], a[112:115], v[68:71], 0\n"
    "buffer_load_dwordx2 v[236:237], v241, s[36:39], s81 offen offset:0 sc1\n"
    "v_mfma_f32_16x16x32_bf16 v[200:203], a[124:127], v[72:75], 0\n"
    "s_waitcnt lgkmcnt(2)\n"
    "v_exp_f32 v27, v27\n"
    "v_mfma_f32_16x16x32_bf16 a[60:63], a[152:155], v[68:71], 0\n"
    "v_mfma_f32_16x16x32_bf16 a[96:99], a[132:135], v[96:99], 0\n"
    "v_exp_f32 v17, v17\n"
    "v_mfma_f32_16x16x32_bf16 a[72:75], a[144:147], v[76:79], 0\n"
    "v_exp_f32 v24, v24\n"
    "v_mfma_f32_16x16x32_bf16 a[24:27], a[112:115], v[84:87], 0\n"
    "v_exp_f32 v35, v35\n"
    "v_mfma_f32_16x16x32_bf16 a[64:67], a[128:131], v[76:79], a[64:67]\n"
    "v_mfma_f32_16x16x32_bf16 a[80:83], a[128:131], v[84:87], 0\n"
    "s_add_u32 s67, s67, 49152\n"
    "v_exp_f32 v32, v32\n"
    "v_mfma_f32_16x16x32_bf16 a[12:15], a[120:123], v[76:79], 0\n"
    "v_mfma_f32_16x16x32_bf16 a[104:107], a[148:151], v[96:99], a[104:107]\n"
    "v_mov_b32 v2, v3\n"
    "v_mfma_f32_16x16x32_bf16 a[28:31], a[124:127], v[88:91], 0\n"
    "v_mfma_f32_16x16x32_bf16 a[28:31], a[120:123], v[84:87], a[28:31]\n"
    "v_mfma_f32_16x16x32_bf16 a[92:95], a[152:155], v[84:87], 0\n"
    "v_mfma_f32_16x16x32_bf16 v[200:203], a[120:123], v[68:71], v[200:203]\n"
    "s_waitcnt lgkmcnt(0)\n"
    "v_exp_f32 v12, v12\n"
    "v_mfma_f32_16x16x32_bf16 a[108:111], a[156:159], v[96:99], 0\n"
    "v_exp_f32 v18, v18\n"
    "v_mfma_f32_16x16x32_bf16 a[80:83], a[132:135], v[88:91], a[80:83]\n"
    "v_mfma_f32_16x16x32_bf16 a[44:47], a[120:123], v[92:95], 0\n"
    "v_exp_f32 v29, v29\n"
    "v_mfma_f32_16x16x32_bf16 a[12:15], a[124:127], v[80:83], a[12:15]\n"
    "ds_read_b128 a[120:123], v3 offset:16384\n"
    "v_mfma_f32_16x16x32_bf16 a[44:47], a[124:127], v[96:99], a[44:47]\n"
    "v_mfma_f32_16x16x32_bf16 a[56:59], a[148:151], v[72:75], 0\n"
    "v_exp_f32 v28, v28\n"
    "v_mfma_f32_16x16x32_bf16 a[96:99], a[128:131], v[92:95], a[96:99]\n"
    "v_exp_f32 v19, v19\n"
    "v_mfma_f32_16x16x32_bf16 a[40:43], a[116:119], v[96:99], 0\n"
    "v_exp_f32 v31, v31\n"
    "v_mfma_f32_16x16x32_bf16 a[60:63], a[156:159], v[72:75], a[60:63]\n"
    "v_exp_f32 v26, v26\n"
    "v_mfma_f32_16x16x32_bf16 a[72:75], a[148:151], v[80:83], a[72:75]\n"
    "v_mfma_f32_16x16x32_bf16 a[8:11], a[116:119], v[80:83], 0\n"
    "v_exp_f32 v6, v6\n"
    "v_mfma_f32_16x16x32_bf16 a[68:71], a[140:143], v[80:83], 0\n"
    "v_exp_f32 v25, v25\n"
    "v_mfma_f32_16x16x32_bf16 a[48:51], a[132:135], v[72:75], a[48:51]\n"
    "ds_read_b128 a[124:127], v3 offset:18432\n"
    "v_mfma_f32_16x16x32_bf16 a[40:43], a[112:115], v[92:95], a[40:43]\n"
    "ds_read_b128 a[132:135], v3 offset:22528\n"
    "v_mfma_f32_16x16x32_bf16 a[100:103], a[140:143], v[96:99], 0\n"
    "v_mfma_f32_16x16x32_bf16 v[196:199], a[116:119], v[72:75], v[196:199]\n"
    "v_exp_f32 v30, v30\n"
    "v_mfma_f32_16x16x32_bf16 a[108:111], a[152:155], v[92:95], a[108:111]\n"
    "v_mfma_f32_16x16x32_bf16 a[88:91], a[148:151], v[88:91], 0\n"
    "s_waitcnt vmcnt(17)\n"
    "v_exp_f32 v11, v11\n"
    "v_mfma_f32_16x16x32_bf16 a[8:11], a[112:115], v[76:79], a[8:11]\n"
    "ds_read_b128 a[128:131], v3 offset:20480\n"
    "ds_read_b128 a[112:115], v3 offset:12288\n"
    "v_mfma_f32_16x16x32_bf16 a[76:79], a[156:159], v[80:83], 0\n"
    "ds_add_u32 v247, v246\n"
    "v_mfma_f32_16x16x32_bf16 a[24:27], a[116:119], v[88:91], a[24:27]\n"
    "ds_read_b128 a[116:119], v3 offset:14336\n"
    "v_mfma_f32_16x16x32_bf16 a[56:59], a[144:147], v[68:71], a[56:59]\n"
    "v_exp_f32 v9, v9\n"
    "v_mfma_f32_16x16x32_bf16 a[84:87], a[136:139], v[84:87], 0\n"
    "v_exp_f32 v23, v23\n"
    "v_mfma_f32_16x16x32_bf16 a[88:91], a[144:147], v[84:87], a[88:91]\n"
    "v_add_u32 v3, 49152, v252\n"
    "v_exp_f32 v34, v34\n"
    "v_mfma_f32_16x16x32_bf16 a[84:87], a[140:143], v[88:91], a[84:87]\n"
    "v_mfma_f32_16x16x32_bf16 a[100:103], a[136:139], v[92:95], a[100:103]\n"
    "v_exp_f32 v13, v13\n"
    "v_mfma_f32_16x16x32_bf16 a[76:79], a[152:155], v[76:79], a[76:79]\n"
    "v_mfma_f32_16x16x32_bf16 a[68:71], a[136:139], v[76:79], a[68:71]\n"
    "s_waitcnt lgkmcnt(0)\n"
    "v_mfma_f32_16x16x32_bf16 a[92:95], a[156:159], v[88:91], a[92:95]\n"
    "v_mfma_f32_16x16x32_bf16 a[52:55], a[140:143], v[72:75], a[52:55]\n"
    "s_branch .LNK\n"
    ".fill ((0x20000 - (. - _Z8attn_fwdPKtPKiS2_S0_S0_S0_S0_PcPt)) & 4095) / 4, 4, 0xBF800000\n"
    ".LhKN:\n"
    "s_waitcnt vmcnt(28)\n"
    "ds_read_b128 v[52:55], v251 offset:768\n"
    "ds_read_b128 v[180:183], v96 offset:49152\n"
    "ds_read_b128 v[200:203], v97 offset:49408\n"
    "ds_read_b128 v[184:187], v97 offset:49152\n"
    "s_add_u32 m0, s68, 0\n"
    "ds_read_b128 v[196:199], v96 offset:49408\n"
    "ds_read_b128 v[188:191], v96 offset:49280\n"
    "ds_read_b128 v[192:195], v97 offset:49280\n"
    "s_waitcnt lgkmcnt(6)\n"
    "v_smfmac_f32_16x16x64_bf16 v[108:111], v[52:55], v[68:75], v253\n"
    "s_waitcnt lgkmcnt(4)\n"
    "v_pk_maximum3_f16 v98, v200, v201, v98\n"
    "v_pk_maximum3_f16 v98, v202, v203, v98\n"
    "v_pk_add_u16 v200, s89, v200 clamp\n"
    "v_pk_add_u16 v203, s89, v203 clamp\n"
    "v_smfmac_f32_16x16x64_bf16 v[116:119], v[52:55], v[84:91], v253\n"
    "s_waitcnt lgkmcnt(2)\n"
    "s_nop 0\n"
    "v_pk_add_u16 v202, s89, v202 clamp\n"
    "v_pk_add_u16 v201, s89, v201 clamp\n"
    "v_pk_maximum3_f16 v98, v196, v197, v98\n"
    "v_pk_maximum3_f16 v98, v198, v199, v98\n"
    "v_mfma_f32_16x16x32_bf16 v[64:67], a[44:47], v[200:203], v[64:67]\n"
    "v_pk_add_u16 v198, s89, v198 clamp\n"
    "v_pk_add_u16 v196, s89, v196 clamp\n"
    "v_mfma_f32_16x16x32_bf16 v[48:51], a[20:23], v[200:203], v[48:51]\n"
    "v_pk_add_u16 v199, s89, v199 clamp\n"
    "v_pk_add_u16 v197, s89, v197 clamp\n"
    "v_mfma_f32_16x16x32_bf16 v[32:35], a[156:159], v[200:203], v[32:35]\n"
    "s_waitcnt lgkmcnt(0)\n"
    "v_pk_maximum3_f16 v98, v192, v193, v98\n"
    "v_pk_maximum3_f16 v98, v194, v195, v98\n"
    "v_mfma_f32_16x16x32_bf16 v[64:67], a[40:43], v[196:199], v[64:67]\n"
    "v_pk_maximum3_f16 v98, v188, v189, v98\n"
    "v_pk_add_u16 v193, s89, v193 clamp\n"
    "v_mfma_f32_16x16x32_bf16 v[48:51], a[16:19], v[196:199], v[48:51]\n"
    "buffer_load_dwordx4 v1, s[16:19], s67 offen offset:0 sc1 lds\n"
    "v_mfma_f32_16x16x32_bf16 v[16:19], a[132:135], v[200:203], v[16:19]\n"
    "v_pk_add_u16 v194, s89, v194 clamp\n"
    "v_pk_maximum3_f16 v98, v190, v191, v98\n"
    "v_pk_add_u16 v192, s89, v192 clamp\n"
    "v_smfmac_f32_16x16x64_bf16 v[112:115], v[52:55], v[76:83], v253\n"
    "v_pk_add_u16 v191, s89, v191 clamp\n"
    "v_mfma_f32_16x16x32_bf16 v[16:19], a[128:131], v[196:199], v[16:19]\n"
    "v_pk_add_u16 v195, s89, v195 clamp\n"
    "v_pk_maximum3_f16 v98, v184, v185, v98\n"
    "v_mfma_f32_16x16x32_bf16 v[32:35], a[152:155], v[196:199], v[32:35]\n"
    "v_pk_add_u16 v185, s89, v185 clamp\n"
    "v_pk_add_u16 v188, s89, v188 clamp\n"
    "v_mfma_f32_16x16x32_bf16 v[64:67], a[36:39], v[192:195], v[64:67]\n"
    "v_pk_add_u16 v189, s89, v189 clamp\n"
    "v_pk_maximum3_f16 v98, v186, v187, v98\n"
    "v_mfma_f32_16x16x32_bf16 v[32:35], a[148:151], v[192:195], v[32:35]\n"
    "buffer_load_dwordx4 v1, s[16:19], s67 offen offset:1024 sc1 lds\n"
    "v_mfma_f32_16x16x32_bf16 v[16:19], a[124:127], v[192:195], v[16:19]\n"
    "v_pk_add_u16 v190, s89, v190 clamp\n"
    "v_pk_add_u16 v184, s89, v184 clamp\n"
    "v_pk_maximum3_f16 v98, v180, v181, v98\n"
    "v_mfma_f32_16x16x32_bf16 v[32:35], a[144:147], v[188:191], v[32:35]\n"
    "v_pk_add_u16 v187, s89, v187 clamp\n"
    "v_pk_add_u16 v186, s89, v186 clamp\n"
    "v_mfma_f32_16x16x32_bf16 v[48:51], a[12:15], v[192:195], v[48:51]\n"
    "s_waitcnt vmcnt(24)\n"
    "v_pk_maximum3_f16 v98, v182, v183, v98\n"
    "v_pk_add_u16 v182, s89, v182 clamp\n"
    "v_mfma_f32_16x16x32_bf16 v[48:51], a[8:11], v[188:191], v[48:51]\n"
    "s_barrier\n"
    "v_pk_add_u16 v180, s89, v180 clamp\n"
    "v_mfma_f32_16x16x32_bf16 v[64:67], a[32:35], v[188:191], v[64:67]\n"
    "ds_read_b128 a[72:75], v252 offset:25600\n"
    "v_pk_add_u16 v183, s89, v183 clamp\n"
    "v_mfma_f32_16x16x32_bf16 v[32:35], a[140:143], v[184:187], v[32:35]\n"
    "ds_read_b128 a[48:51], v252 offset:24576\n"
    "v_pk_add_u16 v181, s89, v181 clamp\n"
    "v_mfma_f32_16x16x32_bf16 v[16:19], a[120:123], v[188:191], v[16:19]\n"
    "buffer_load_dwordx4 v1, s[16:19], s67 offen offset:2048 sc1 lds\n"
    "v_mfma_f32_16x16x32_bf16 v[32:35], a[136:139], v[180:183], v[32:35]\n"
    "v_accvgpr_write_b32 a187, v199\n"
    "v_accvgpr_write_b32 a186, v198\n"
    "v_mfma_f32_16x16x32_bf16 v[64:67], a[28:31], v[184:187], v[64:67]\n"
    "ds_read_b128 a[92:95], v252 offset:35840\n"
    "ds_read_b128 a[64:67], v252 offset:32768\n"
    "v_mfma_f32_16x16x32_bf16 v[48:51], a[4:7], v[184:187], v[48:51]\n"
    "s_add_u32 s83, s67, 4096\n"
    "ds_read_b128 a[68:71], v252 offset:34816\n"
    "ds_read_b128 a[88:91], v252 offset:33792\n"
    "v_mfma_f32_16x16x32_bf16 v[16:19], a[116:119], v[184:187], v[16:19]\n"
    "v_accvgpr_write_b32 a178, v190\n"
    "v_accvgpr_write_b32 a173, v185\n"
    "v_mfma_f32_16x16x32_bf16 v[64:67], a[24:27], v[180:183], v[64:67]\n"
    "ds_read_b128 a[52:55], v252 offset:26624\n"
    "ds_read_b128 a[56:59], v252 offset:28672\n"
    "v_mfma_f32_16x16x32_bf16 v[48:51], a[0:3], v[180:183], v[48:51]\n"
    "ds_read_b128 a[84:87], v252 offset:31744\n"
    "v_accvgpr_write_b32 a177, v189\n"
    "v_mfma_f32_16x16x32_bf16 v[16:19], a[112:115], v[180:183], v[16:19]\n"
    "s_waitcnt lgkmcnt(8)\n"
    "buffer_load_dwordx4 v1, s[16:19], s67 offen offset:3072 sc1 lds\n"
    "v_mfma_f32_16x16x32_bf16 v[28:31], a[72:75], a[240:243], v[104:107]\n"
    "s_waitcnt lgkmcnt(7)\n"
    "ds_read_b128 a[60:63], v252 offset:30720\n"
    "v_accvgpr_write_b32 a170, v182\n"
    "v_mfma_f32_16x16x32_bf16 v[4:7], a[48:51], a[192:195], v[100:103]\n"
    "ds_read_b128 a[76:79], v252 offset:27648\n"
    "ds_read_b128 a[80:83], v252 offset:29696\n"
    "v_mfma_f32_16x16x32_bf16 v[12:15], a[48:51], a[240:243], v[100:103]\n"
    "s_waitcnt lgkmcnt(9)\n"
    "v_exp_f32 v16, v16\n"
    "v_mfma_f32_16x16x32_bf16 v[24:27], a[92:95], a[236:239], v[104:107]\n"
    "s_waitcnt lgkmcnt(6)\n"
    "v_accvgpr_write_b32 a183, v195\n"
    "v_accvgpr_write_b32 a190, v202\n"
    "v_mfma_f32_16x16x32_bf16 v[24:27], a[88:91], a[232:235], v[24:27]\n"
    "v_exp_f32 v17, v17\n"
    "v_mfma_f32_16x16x32_bf16 v[20:23], a[72:75], a[192:195], v[104:107]\n"
    "s_waitcnt lgkmcnt(5)\n"
    "v_accvgpr_write_b32 a172, v184\n"
    "v_accvgpr_write_b32 a168, v180\n"
    "v_mfma_f32_16x16x32_bf16 v[4:7], a[52:55], a[196:199], v[4:7]\n"
    "s_add_u32 m0, s68, 4096\n"
    "v_exp_f32 v34, v34\n"
    "v_mfma_f32_16x16x32_bf16 v[12:15], a[52:55], a[244:247], v[12:15]\n"
    "buffer_load_dwordx4 v1, s[16:19], s83 offen offset:0 sc1 lds\n"
    "v_mfma_f32_16x16x32_bf16 v[8:11], a[68:71], a[236:239], v[100:103]\n"
    "s_waitcnt lgkmcnt(4)\n"
    "v_accvgpr_write_b32 a182, v194\n"
    "v_accvgpr_write_b32 a189, v201\n"
    "v_mfma_f32_16x16x32_bf16 v[4:7], a[56:59], a[200:203], v[4:7]\n"
    "s_waitcnt lgkmcnt(2)\n"
    "v_accvgpr_write_b32 a175, v187\n"
    "v_accvgpr_write_b32 a180, v192\n"
    "v_mfma_f32_16x16x32_bf16 v[4:7], a[60:63], a[204:207], v[4:7]\n"
    "s_waitcnt lgkmcnt(1)\n"
    "v_accvgpr_write_b32 a176, v188\n"
    "v_pk_add_u16 v98, s89, v98\n"
    "v_mfma_f32_16x16x32_bf16 v[28:31], a[76:79], a[244:247], v[28:31]\n"
    "v_accvgpr_write_b32 a188, v200\n"
    "v_accvgpr_write_b32 a191, v203\n"
    "v_mfma_f32_16x16x32_bf16 v[20:23], a[76:79], a[196:199], v[20:23]\n"
    "s_waitcnt lgkmcnt(0)\n"
    "v_accvgpr_write_b32 a184, v196\n"
    "v_mfma_f32_16x16x32_bf16 v[20:23], a[80:83], a[200:203], v[20:23]\n"
    "v_accvgpr_write_b32 a181, v193\n"
    "v_mfma_f32_16x16x32_bf16 v[12:15], a[56:59], a[248:251], v[12:15]\n"
    "buffer_load_dwordx4 v1, s[16:19], s83 offen offset:1024 sc1 lds\n"
    "v_mfma_f32_16x16x32_bf16 v[20:23], a[84:87], a[204:207], v[20:23]\n"
    "v_accvgpr_write_b32 a171, v183\n"
    "v_accvgpr_write_b32 a185, v197\n"
    "v_mfma_f32_16x16x32_bf16 v[4:7], a[64:67], a[208:211], v[4:7]\n"
    "s_add_u32 m0, s70, 0\n"
    "v_accvgpr_write_b32 a179, v191\n"
    "v_accvgpr_write_b32 a169, v181\n"
    "v_mfma_f32_16x16x32_bf16 v[4:7], a[68:71], a[212:215], v[4:7]\n"
    "s_add_u32 s83, s67, 24576\n"
    "v_mfma_f32_16x16x32_bf16 v[28:31], a[80:83], a[248:251], v[28:31]\n"
    "v_exp_f32 v19, v19\n"
    "v_mfma_f32_16x16x32_bf16 v[8:11], a[64:67], a[232:235], v[8:11]\n"
    "v_accvgpr_write_b32 a174, v186\n"
    "v_mfma_f32_16x16x32_bf16 v[24:27], a[84:87], a[228:231], v[24:27]\n"
    "v_exp_f32 v33, v33\n"
    "v_mfma_f32_16x16x32_bf16 v[20:23], a[88:91], a[208:211], v[20:23]\n"
    "buffer_load_dwordx4 v1, s[16:19], s83 offen offset:0 sc1 lds\n"
    "v_mfma_f32_16x16x32_bf16 v[28:31], a[84:87], a[252:255], v[28:31]\n"
    "v_exp_f32 v32, v32\n"
    "v_mfma_f32_16x16x32_bf16 v[28:31], a[88:91], a[160:163], v[28:31]\n"
    "v_exp_f32 v18, v18\n"
    "v_mfma_f32_16x16x32_bf16 v[24:27], a[80:83], a[224:227], v[24:27]\n"
    "v_exp_f32 v49, v49\n"
    "v_mfma_f32_16x16x32_bf16 v[8:11], a[60:63], a[228:231], v[8:11]\n"
    "v_exp_f32 v35, v35\n"
    "v_mfma_f32_16x16x32_bf16 v[24:27], a[76:79], a[220:223], v[24:27]\n"
    "v_cvt_pk_bf16_f32 v92, v16, v17\n"
    "v_cvt_pk_bf16_f32 v93, v18, v19\n"
    "v_mfma_f32_16x16x32_bf16 v[12:15], a[60:63], a[252:255], v[12:15]\n"
    "v_and_b32 v98, 0x80008000, v98\n"
    "v_cvt_pk_bf16_f32 v94, v32, v33\n"
    "v_mfma_f32_16x16x32_bf16 v[24:27], a[72:75], a[216:219], v[24:27]\n"
    "buffer_load_dwordx4 v1, s[16:19], s83 offen offset:1024 sc1 lds\n"
    "v_mfma_f32_16x16x32_bf16 v[16:19], a[68:71], a[188:191], v[100:103]\n"
    "v_cvt_pk_bf16_f32 v95, v34, v35\n"
    "v_mfma_f32_16x16x32_bf16 v[16:19], a[64:67], a[184:187], v[16:19]\n"
    "s_waitcnt vmcnt(24)\n"
    "s_barrier\n"
    "v_mfma_f32_16x16x32_bf16 v[8:11], a[56:59], a[224:227], v[8:11]\n"
    "ds_read_b128 a[124:127], v249 offset:13312\n"
    "ds_read_b128 a[120:123], v249 offset:1024\n"
    "v_mfma_f32_16x16x32_bf16 v[32:35], a[92:95], a[188:191], v[104:107]\n"
    "ds_read_b128 a[116:119], v249 offset:12288\n"
    "ds_read_b128 a[112:115], v249 offset:0\n"
    "v_mfma_f32_16x16x32_bf16 v[32:35], a[88:91], a[184:187], v[32:35]\n"
    "ds_read_b128 a[132:135], v249 offset:14336\n"
    "ds_read_b128 a[144:147], v249 offset:4096\n"
    "v_mfma_f32_16x16x32_bf16 v[8:11], a[52:55], a[220:223], v[8:11]\n"
    "s_add_u32 s76, s71, s60\n"
    "v_exp_f32 v51, v51\n"
    "v_mfma_f32_16x16x32_bf16 v[12:15], a[64:67], a[160:163], v[12:15]\n"
    "buffer_load_dwordx4 v1, s[16:19], s83 offen offset:2048 sc1 lds\n"
    "v_mfma_f32_16x16x32_bf16 v[28:31], a[92:95], a[164:167], v[28:31]\n"
    "v_exp_f32 v66, v66\n"
    "v_mfma_f32_16x16x32_bf16 v[16:19], a[60:63], a[180:183], v[16:19]\n"
    "v_exp_f32 v50, v50\n"
    "v_mfma_f32_16x16x32_bf16 v[12:15], a[68:71], a[164:167], v[12:15]\n"
    "v_exp_f32 v67, v67\n"
    "v_mfma_f32_16x16x32_bf16 v[8:11], a[48:51], a[216:219], v[8:11]\n"
    "s_add_u32 s77, s71, s61\n"
    "ds_read_b128 a[156:159], v249 offset:17408\n"
    "s_waitcnt lgkmcnt(6)\n"
    "v_mfma_f32_16x16x32_bf16 v[128:131], a[124:127], v[72:75], 0\n"
    "ds_read_b128 a[136:139], v249 offset:3072\n"
    "v_mfma_f32_16x16x32_bf16 v[160:163], a[124:127], v[88:91], 0\n"
    "s_waitcnt lgkmcnt(4)\n"
    "ds_read_b128 a[148:151], v249 offset:16384\n"
    "ds_read_b128 a[128:131], v249 offset:2048\n"
    "v_mfma_f32_16x16x32_bf16 v[156:159], a[112:115], v[84:87], 0\n"
    "buffer_load_dwordx4 v1, s[16:19], s83 offen offset:3072 sc1 lds\n"
    "v_mfma_f32_16x16x32_bf16 v[16:19], a[56:59], a[176:179], v[16:19]\n"
    "s_add_u32 s83, s67, 28672\n"
    "ds_read_b128 a[152:155], v249 offset:5120\n"
    "v_mfma_f32_16x16x32_bf16 v[16:19], a[52:55], a[172:175], v[16:19]\n"
    "s_add_u32 m0, s70, 4096\n"
    "v_exp_f32 v7, v7\n"
    "v_mfma_f32_16x16x32_bf16 v[20:23], a[92:95], a[212:215], v[20:23]\n"
    "v_mfma_f32_16x16x32_bf16 v[156:159], a[116:119], v[88:91], v[156:159]\n"
    "v_exp_f32 v48, v48\n"
    "v_mfma_f32_16x16x32_bf16 v[32:35], a[84:87], a[180:183], v[32:35]\n"
    "s_waitcnt lgkmcnt(6)\n"
    "v_exp_f32 v65, v65\n"
    "v_mfma_f32_16x16x32_bf16 v[148:151], a[132:135], v[80:83], 0\n"
    "ds_read_b128 v[36:39], v243 offset:0\n"
    "v_cmp_ne_u32 s[34:35], 0, v98\n"
    "v_mfma_f32_16x16x32_bf16 v[124:127], a[112:115], v[68:71], 0\n"
    "buffer_load_dwordx4 v1, s[16:19], s83 offen offset:0 sc1 lds\n"
    "v_mfma_f32_16x16x32_bf16 v[176:179], a[120:123], v[92:95], 0\n"
    "s_waitcnt lgkmcnt(3)\n"
    "v_exp_f32 v10, v10\n"
    "v_mfma_f32_16x16x32_bf16 a[16:19], a[148:151], v[88:91], 0\n"
    "v_exp_f32 v64, v64\n"
    "v_mfma_f32_16x16x32_bf16 v[136:139], a[136:139], v[68:71], 0\n"
    "v_exp_f32 v4, v4\n"
    "v_mfma_f32_16x16x32_bf16 v[32:35], a[80:83], a[176:179], v[32:35]\n"
    "ds_read_b128 a[140:143], v249 offset:15360\n"
    "v_add_u32 v251, 1536, v251\n"
    "v_mfma_f32_16x16x32_bf16 v[140:143], a[116:119], v[80:83], 0\n"
    "v_cvt_pk_bf16_f32 v97, v50, v51\n"
    "v_mfma_f32_16x16x32_bf16 v[16:19], a[48:51], a[168:171], v[16:19]\n"
    "s_waitcnt lgkmcnt(3)\n"
    "v_cvt_pk_bf16_f32 v98, v64, v65\n"
    "v_cvt_pk_bf16_f32 v99, v66, v67\n"
    "v_mfma_f32_16x16x32_bf16 v[164:167], a[128:131], v[84:87], 0\n"
    "buffer_load_dwordx4 v1, s[16:19], s83 offen offset:1024 sc1 lds\n"
    "v_mfma_f32_16x16x32_bf16 a[4:7], a[156:159], v[80:83], 0\n"
    "v_exp_f32 v27, v27\n"
    "v_mfma_f32_16x16x32_bf16 v[128:131], a[120:123], v[68:71], v[128:131]\n"
    "s_waitcnt lgkmcnt(2)\n"
    "v_mfma_f32_16x16x32_bf16 v[192:195], a[152:155], v[68:71], 0\n"
    "v_exp_f32 v26, v26\n"
    "v_mfma_f32_16x16x32_bf16 a[32:35], a[144:147], v[92:95], 0\n"
    "ds_read_b128 v[100:103], v250 offset:1664\n"
    "s_waitcnt lgkmcnt(1)\n"
    "v_mfma_f32_16x16x32_bf16 v[152:155], a[140:143], v[80:83], 0\n"
    "v_exp_f32 v22, v22\n"
    "v_mfma_f32_16x16x32_bf16 v[32:35], a[76:79], a[172:175], v[32:35]\n"
    "v_cvt_pk_bf16_f32 v96, v48, v49\n"
    "v_mfma_f32_16x16x32_bf16 v[144:147], a[120:123], v[76:79], 0\n"
    "s_waitcnt vmcnt(12)\n"
    "v_fma_f32 v216, v216, v36, -v38\n"
    "v_fma_f32 v229, v229, v37, -v39\n"
    "v_mfma_f32_16x16x32_bf16 v[160:163], a[120:123], v[84:87], v[160:163]\n"
    "v_fma_f32 v206, v206, v36, -v38\n"
    "v_fma_f32 v213, v213, v37, -v39\n"
    "v_mfma_f32_16x16x32_bf16 v[172:175], a[116:119], v[96:99], 0\n"
    "v_fma_f32 v214, v214, v36, -v38\n"
    "v_mfma_f32_16x16x32_bf16 v[124:127], a[116:119], v[72:75], v[124:127]\n"
    "v_fma_f32 v207, v207, v37, -v39\n"
    "v_fma_f32 v205, v205, v37, -v39\n"
    "v_mfma_f32_16x16x32_bf16 v[176:179], a[124:127], v[96:99], v[176:179]\n"
    "ds_read_b128 a[116:119], v249 offset:18432\n"
    "v_fma_f32 v209, v209, v37, -v39\n"
    "v_mfma_f32_16x16x32_bf16 v[144:147], a[124:127], v[80:83], v[144:147]\n"
    "v_fma_f32 v215, v215, v37, -v39\n"
    "v_fma_f32 v217, v217, v37, -v39\n"
    "v_mfma_f32_16x16x32_bf16 a[4:7], a[152:155], v[76:79], a[4:7]\n"
    "v_exp_f32 v13, v13\n"
    "v_mfma_f32_16x16x32_bf16 v[140:143], a[112:115], v[76:79], v[140:143]\n"
    "v_fma_f32 v212, v212, v36, -v38\n"
    "v_mfma_f32_16x16x32_bf16 v[184:187], a[140:143], v[96:99], 0\n"
    "v_cvt_pk_bf16_f32 v216, v216, v217\n"
    "v_fma_f32 v223, v223, v37, -v39\n"
    "v_mfma_f32_16x16x32_bf16 v[164:167], a[132:135], v[88:91], v[164:167]\n"
    "v_fma_f32 v222, v222, v36, -v38\n"
    "v_fma_f32 v231, v231, v37, -v39\n"
    "v_mfma_f32_16x16x32_bf16 v[188:191], a[148:151], v[72:75], 0\n"
    "v_exp_f32 v21, v21\n"
    "v_mfma_f32_16x16x32_bf16 v[172:175], a[112:115], v[92:95], v[172:175]\n"
    "v_fma_f32 v235, v235, v37, -v39\n"
    "v_fma_f32 v211, v211, v37, -v39\n"
    "v_mfma_f32_16x16x32_bf16 v[132:135], a[128:131], v[68:71], 0\n"
    "ds_read_b128 a[112:115], v249 offset:6144\n"
    "v_mfma_f32_16x16x32_bf16 a[32:35], a[148:151], v[96:99], a[32:35]\n"
    "v_exp_f32 v5, v5\n"
    "v_mfma_f32_16x16x32_bf16 v[180:183], a[132:135], v[96:99], 0\n"
    "ds_read_b128 a[120:123], v249 offset:7168\n"
    "v_cvt_pk_bf16_f32 v206, v206, v207\n"
    "v_mfma_f32_16x16x32_bf16 v[132:135], a[132:135], v[72:75], v[132:135]\n"
    "ds_read_b128 a[124:127], v249 offset:19456\n"
    "v_fma_f32 v221, v221, v37, -v39\n"
    "v_mfma_f32_16x16x32_bf16 v[180:183], a[128:131], v[92:95], v[180:183]\n"
    "ds_read_b128 a[132:135], v249 offset:20480\n"
    "v_fma_f32 v233, v233, v37, -v39\n"
    "v_mfma_f32_16x16x32_bf16 a[0:3], a[144:147], v[76:79], 0\n"
    "v_exp_f32 v12, v12\n"
    "v_mfma_f32_16x16x32_bf16 a[0:3], a[148:151], v[80:83], a[0:3]\n"
    "s_waitcnt lgkmcnt(4)\n"
    "v_fma_f32 v204, v204, v36, -v38\n"
    "v_fma_f32 v218, v218, v36, -v38\n"
    "v_mfma_f32_16x16x32_bf16 a[8:11], a[116:119], v[80:83], 0\n"
    "v_cvt_pk_bf16_f32 v212, v212, v213\n"
    "v_fma_f32 v219, v219, v37, -v39\n"
    "v_mfma_f32_16x16x32_bf16 a[20:23], a[152:155], v[84:87], 0\n"
    "v_cvt_pk_bf16_f32 v204, v204, v205\n"
    "v_fma_f32 v220, v220, v36, -v38\n"
    "v_mfma_f32_16x16x32_bf16 a[36:39], a[156:159], v[96:99], 0\n"
    "v_mfma_f32_16x16x32_bf16 a[16:19], a[144:147], v[84:87], a[16:19]\n"
    "s_waitcnt lgkmcnt(2)\n"
    "buffer_store_dword v204, v244, s[16:19], s76 offen offset:0 sc1\n"
    "v_exp_f32 v16, v16\n"
    "v_mfma_f32_16x16x32_bf16 a[12:15], a[120:123], v[76:79], 0\n"
    "v_mfma_f32_16x16x32_bf16 v[32:35], a[72:75], a[168:171], v[32:35]\n"
    "v_fma_f32 v234, v234, v36, -v38\n"
    "v_fma_f32 v208, v208, v36, -v38\n"
    "v_mfma_f32_16x16x32_bf16 a[36:39], a[152:155], v[92:95], a[36:39]\n"
    "buffer_store_dword v206, v244, s[16:19], s76 offen offset:128 sc1\n"
    "v_fma_f32 v225, v225, v37, -v39\n"
    "v_fma_f32 v224, v224, v36, -v38\n"
    "v_mfma_f32_16x16x32_bf16 v[168:171], a[136:139], v[84:87], 0\n"
    "s_waitcnt lgkmcnt(1)\n"
    "v_cvt_pk_bf16_f32 v208, v208, v209\n"
    "v_fma_f32 v230, v230, v36, -v38\n"
    "v_mfma_f32_16x16x32_bf16 v[200:203], a[124:127], v[72:75], 0\n"
    "v_fma_f32 v226, v226, v36, -v38\n"
    "v_fma_f32 v210, v210, v36, -v38\n"
    "v_mfma_f32_16x16x32_bf16 a[44:47], a[120:123], v[92:95], 0\n"
    "buffer_store_dword v208, v244, s[16:19], s76 offen offset:1024 sc1\n"
    "v_smfmac_f32_16x16x64_bf16 v[120:123], v[52:55], v[92:99], v253\n"
    "v_cvt_pk_bf16_f32 v210, v210, v211\n"
    "v_mfma_f32_16x16x32_bf16 v[188:191], a[144:147], v[68:71], v[188:191]\n"
    "buffer_store_dword v210, v244, s[16:19], s76 offen offset:1152 sc1\n"
    "v_mfma_f32_16x16x32_bf16 v[148:151], a[128:131], v[76:79], v[148:151]\n"
    "v_cvt_pk_bf16_f32 v214, v214, v215\n"
    "v_fma_f32 v227, v227, v37, -v39\n"
    "v_mfma_f32_16x16x32_bf16 a[8:11], a[112:115], v[76:79], a[8:11]\n"
    "ds_read_b128 a[144:147], v249 offset:10240\n"
    "ds_read_b128 a[128:131], v249 offset:8192\n"
    "v_mfma_f32_16x16x32_bf16 a[28:31], a[124:127], v[88:91], 0\n"
    "v_exp_f32 v24, v24\n"
    "v_mfma_f32_16x16x32_bf16 a[24:27], a[112:115], v[84:87], 0\n"
    "v_mov_b32 v2, v3\n"
    "v_cvt_pk_bf16_f32 v222, v222, v223\n"
    "v_mfma_f32_16x16x32_bf16 a[40:43], a[116:119], v[96:99], 0\n"
    "ds_read_b128 a[148:151], v249 offset:22528\n"
    "v_fma_f32 v232, v232, v36, -v38\n"
    "v_mfma_f32_16x16x32_bf16 v[200:203], a[120:123], v[68:71], v[200:203]\n"
    "buffer_store_dword v212, v244, s[16:19], s76 offen offset:256 sc1\n"
    "v_cvt_pk_bf16_f32 v224, v224, v225\n"
    "v_mfma_f32_16x16x32_bf16 v[152:155], a[136:139], v[76:79], v[152:155]\n"
    "s_waitcnt lgkmcnt(3)\n"
    "v_exp_f32 v14, v14\n"
    "v_mfma_f32_16x16x32_bf16 a[96:99], a[132:135], v[96:99], 0\n"
    "v_exp_f32 v31, v31\n"
    "v_mfma_f32_16x16x32_bf16 v[196:199], a[112:115], v[68:71], 0\n"
    "s_waitcnt lgkmcnt(2)\n"
    "buffer_store_dword v214, v244, s[16:19], s76 offen offset:384 sc1\n"
    "v_exp_f32 v6, v6\n"
    "v_mfma_f32_16x16x32_bf16 a[104:107], a[144:147], v[92:95], 0\n"
    "v_exp_f32 v18, v18\n"
    "v_mfma_f32_16x16x32_bf16 a[28:31], a[120:123], v[84:87], a[28:31]\n"
    "v_exp_f32 v33, v33\n"
    "v_mfma_f32_16x16x32_bf16 a[64:67], a[132:135], v[80:83], 0\n"
    "v_cvt_pk_bf16_f32 v230, v230, v231\n"
    "v_mfma_f32_16x16x32_bf16 v[168:171], a[140:143], v[88:91], v[168:171]\n"
    "s_waitcnt lgkmcnt(1)\n"
    "buffer_load_dwordx2 v[236:237], v241, s[36:39], s81 offen offset:0 sc1\n"
    "v_mfma_f32_16x16x32_bf16 a[48:51], a[128:131], v[68:71], 0\n"
    "v_exp_f32 v19, v19\n"
    "v_mfma_f32_16x16x32_bf16 v[136:139], a[140:143], v[72:75], v[136:139]\n"
    "s_waitcnt lgkmcnt(0)\n"
    "buffer_store_dword v216, v244, s[16:19], s76 offen offset:1280 sc1\n"
    "v_mfma_f32_16x16x32_bf16 a[104:107], a[148:151], v[96:99], a[104:107]\n"
    "v_exp_f32 v29, v29\n"
    "v_mfma_f32_16x16x32_bf16 a[88:91], a[148:151], v[88:91], 0\n"
    "v_fma_f32 v228, v228, v36, -v38\n"
    "v_cvt_pk_bf16_f32 v226, v226, v227\n"
    "v_mfma_f32_16x16x32_bf16 v[196:199], a[116:119], v[72:75], v[196:199]\n"
    "v_cvt_pk_bf16_f32 v218, v218, v219\n"
    "v_cvt_pk_bf16_f32 v220, v220, v221\n"
    "v_mfma_f32_16x16x32_bf16 v[184:187], a[136:139], v[92:95], v[184:187]\n"
    "buffer_store_dword v218, v244, s[16:19], s76 offen offset:1408 sc1\n"
    "ds_read_b128 a[140:143], v249 offset:21504\n"
    "v_mfma_f32_16x16x32_bf16 a[24:27], a[116:119], v[88:91], a[24:27]\n"
    "v_exp_f32 v32, v32\n"
    "v_mfma_f32_16x16x32_bf16 v[192:195], a[156:159], v[72:75], v[192:195]\n"
    "ds_read_b128 a[152:155], v249 offset:11264\n"
    "v_cvt_pk_bf16_f32 v232, v232, v233\n"
    "v_mfma_f32_16x16x32_bf16 a[20:23], a[156:159], v[88:91], a[20:23]\n"
    "v_exp_f32 v9, v9\n"
    "v_mfma_f32_16x16x32_bf16 a[80:83], a[128:131], v[84:87], 0\n"
    "v_exp_f32 v11, v11\n"
    "v_mfma_f32_16x16x32_bf16 a[12:15], a[124:127], v[80:83], a[12:15]\n"
    "buffer_store_dword v220, v244, s[16:19], s77 offen offset:0 sc1\n"
    "ds_read_b128 a[156:159], v249 offset:23552\n"
    "v_mfma_f32_16x16x32_bf16 a[72:75], a[144:147], v[76:79], 0\n"
    "v_exp_f32 v28, v28\n"
    "v_mfma_f32_16x16x32_bf16 a[64:67], a[128:131], v[76:79], a[64:67]\n"
    "buffer_store_dword v222, v244, s[16:19], s77 offen offset:128 sc1\n"
    "v_mfma_f32_16x16x32_bf16 a[80:83], a[132:135], v[88:91], a[80:83]\n"
    "v_mfma_f32_16x16x32_bf16 a[88:91], a[144:147], v[84:87], a[88:91]\n"
    "v_exp_f32_e64 v30, v30\n"
    "v_mfma_f32_16x16x32_bf16 a[56:59], a[148:151], v[72:75], 0\n"
    "v_add_u32 v243, 512, v243\n"
    "v_cvt_pk_bf16_f32 v228, v228, v229\n"
    "v_mfma_f32_16x16x32_bf16 a[56:59], a[144:147], v[68:71], a[56:59]\n"
    "s_add_u32 s67, s67, 49152\n"
    "buffer_store_dword v224, v244, s[16:19], s77 offen offset:1024 sc1\n"
    "v_mfma_f32_16x16x32_bf16 a[48:51], a[132:135], v[72:75], a[48:51]\n"
    "v_exp_f32 v20, v20\n"
    "v_mfma_f32_16x16x32_bf16 a[44:47], a[124:127], v[96:99], a[44:47]\n"
    "s_waitcnt lgkmcnt(0)\n"
    "buffer_store_dword v226, v244, s[16:19], s77 offen offset:1152 sc1\n"
    "ds_read_b128 a[136:139], v249 offset:9216\n"
    "v_mfma_f32_16x16x32_bf16 a[108:111], a[156:159], v[96:99], 0\n"
    "v_mfma_f32_16x16x32_bf16 a[96:99], a[128:131], v[92:95], a[96:99]\n"
    "v_exp_f32 v15, v15\n"
    "v_mfma_f32_16x16x32_bf16 a[100:103], a[140:143], v[96:99], 0\n"
    "buffer_store_dword v228, v244, s[16:19], s77 offen offset:256 sc1\n"
    "v_exp_f32 v35, v35\n"
    "v_mfma_f32_16x16x32_bf16 a[68:71], a[140:143], v[80:83], 0\n"
    "ds_read_b128 a[120:123], v3 offset:16384\n"
    "ds_read_b128 a[116:119], v3 offset:14336\n"
    "v_mfma_f32_16x16x32_bf16 a[40:43], a[112:115], v[92:95], a[40:43]\n"
    "v_exp_f32 v8, v8\n"
    "v_mfma_f32_16x16x32_bf16 a[76:79], a[156:159], v[80:83], 0\n"
    "buffer_store_dword v230, v244, s[16:19], s77 offen offset:384 sc1\n"
    "v_mfma_f32_16x16x32_bf16 a[76:79], a[152:155], v[76:79], a[76:79]\n"
    "v_exp_f32 v34, v34\n"
    "v_mfma_f32_16x16x32_bf16 a[60:63], a[152:155], v[68:71], 0\n"
    "s_waitcnt lgkmcnt(2)\n"
    "ds_read_b128 a[124:127], v3 offset:18432\n"
    "ds_read_b128 a[128:131], v3 offset:20480\n"
    "v_mfma_f32_16x16x32_bf16 a[92:95], a[152:155], v[84:87], 0\n"
    "s_waitcnt vmcnt(15)\n"
    "ds_add_u32 v247, v246\n"
    "v_mfma_f32_16x16x32_bf16 a[108:111], a[152:155], v[92:95], a[108:111]\n"
    "ds_read_b128 a[132:135], v3 offset:22528\n"
    "v_add_u32 v250, 1536, v250\n"
    "v_mfma_f32_16x16x32_bf16 a[52:55], a[136:139], v[68:71], 0\n"
    "ds_read_b128 a[112:115], v3 offset:12288\n"
    "v_mfma_f32_16x16x32_bf16 a[84:87], a[136:139], v[84:87], 0\n"
    "v_add_u32 v3, 49152, v252\n"
    "v_mfma_f32_16x16x32_bf16 a[72:75], a[148:151], v[80:83], a[72:75]\n"
    "buffer_store_dword v232, v244, s[16:19], s77 offen offset:1280 sc1\n"
    "v_cvt_pk_bf16_f32 v234, v234, v235\n"
    "v_mfma_f32_16x16x32_bf16 a[100:103], a[136:139], v[92:95], a[100:103]\n"
    "v_exp_f32 v17, v17\n"
    "v_mfma_f32_16x16x32_bf16 a[84:87], a[140:143], v[88:91], a[84:87]\n"
    "v_exp_f32 v23, v23\n"
    "v_mfma_f32_16x16x32_bf16 a[60:63], a[156:159], v[72:75], a[60:63]\n"
    "buffer_store_dword v234, v244, s[16:19], s77 offen offset:1408 sc1\n"
    "v_mfma_f32_16x16x32_bf16 a[52:55], a[140:143], v[72:75], a[52:55]\n"
    "v_exp_f32 v25, v25\n"
    "v_mfma_f32_16x16x32_bf16 a[68:71], a[136:139], v[76:79], a[68:71]\n"
    "s_waitcnt lgkmcnt(0)\n"
    "v_mfma_f32_16x16x32_bf16 a[92:95], a[156:159], v[88:91], a[92:95]\n"
    "s_branch .LPK\n"
    ".fill ((0x20000 - (. - _Z8attn_fwdPKtPKiS2_S0_S0_S0_S0_PcPt)) & 4095) / 4, 4, 0xBF800000\n"
    ".LhVP:\n"
    "s_waitcnt vmcnt(12)\n"
    "ds_read_b128 v[52:55], v251 offset:0\n"
    "s_add_u32 s83, s67, 4096\n"
    "ds_read_b128 v[200:203], v97 offset:49408\n"
    "ds_read_b128 v[196:199], v96 offset:49408\n"
    "v_mov_b32 v2, v3\n"
    "ds_read_b128 v[192:195], v97 offset:49280\n"
    "ds_read_b128 v[188:191], v96 offset:49280\n"
    "ds_read_b128 v[184:187], v97 offset:49152\n"
    "s_add_u32 m0, s68, .Lend-.Lend2+(0)\n"
    "ds_read_b128 v[180:183], v96 offset:49152\n"
    "s_waitcnt lgkmcnt(6)\n"
    "v_smfmac_f32_16x16x64_bf16 v[108:111], v[52:55], v[68:75], v253\n"
    "s_waitcnt lgkmcnt(4)\n"
    "v_pk_maximum3_f16 v98, v200, v201, v98\n"
    "v_pk_maximum3_f16 v98, v202, v203, v98\n"
    "v_pk_add_u16 v203, s89, v203 clamp\n"
    "v_pk_add_u16 v201, s89, v201 clamp\n"
    "v_pk_add_u16 v200, s89, v200 clamp\n"
    "v_pk_add_u16 v202, s89, v202 clamp\n"
    "v_pk_maximum3_f16 v98, v196, v197, v98\n"
    "v_smfmac_f32_16x16x64_bf16 v[116:119], v[52:55], v[84:91], v253\n"
    "v_pk_maximum3_f16 v98, v198, v199, v98\n"
    "v_mfma_f32_16x16x32_bf16 v[64:67], a[44:47], v[200:203], v[64:67]\n"
    "s_waitcnt lgkmcnt(3)\n"
    "v_pk_add_u16 v197, s89, v197 clamp\n"
    "v_pk_maximum3_f16 v98, v192, v193, v98\n"
    "v_mfma_f32_16x16x32_bf16 v[48:51], a[20:23], v[200:203], v[48:51]\n"
    "buffer_load_dwordx4 v1, s[16:19], s67 offen offset:0 sc1 lds\n"
    "v_mfma_f32_16x16x32_bf16 v[16:19], a[132:135], v[200:203], v[16:19]\n"
    "v_pk_add_u16 v199, s89, v199 clamp\n"
    "v_pk_add_u16 v198, s89, v198 clamp\n"
    "v_smfmac_f32_16x16x64_bf16 v[112:115], v[52:55], v[76:83], v253\n"
    "v_pk_add_u16 v196, s89, v196 clamp\n"
    "v_mfma_f32_16x16x32_bf16 v[32:35], a[156:159], v[200:203], v[32:35]\n"
    "s_waitcnt lgkmcnt(2)\n"
    "v_pk_add_u16 v193, s89, v193 clamp\n"
    "v_pk_maximum3_f16 v98, v194, v195, v98\n"
    "v_pk_maximum3_f16 v98, v188, v189, v98\n"
    "v_mfma_f32_16x16x32_bf16 v[32:35], a[152:155], v[196:199], v[32:35]\n"
    "v_pk_maximum3_f16 v98, v190, v191, v98\n"
    "v_pk_add_u16 v192, s89, v192 clamp\n"
    "v_mfma_f32_16x16x32_bf16 v[16:19], a[128:131], v[196:199], v[16:19]\n"
    "v_pk_add_u16 v194, s89, v194 clamp\n"
    "v_pk_add_u16 v195, s89, v195 clamp\n"
    "v_mfma_f32_16x16x32_bf16 v[48:51], a[16:19], v[196:199], v[48:51]\n"
    "buffer_load_dwordx4 v1, s[16:19], s67 offen offset:1024 sc1 lds\n"
    "v_mfma_f32_16x16x32_bf16 v[48:51], a[12:15], v[192:195], v[48:51]\n"
    "s_waitcnt lgkmcnt(1)\n"
    "v_pk_maximum3_f16 v98, v184, v185, v98\n"
    "v_pk_add_u16 v188, s89, v188 clamp\n"
    "v_pk_maximum3_f16 v98, v186, v187, v98\n"
    "v_mfma_f32_16x16x32_bf16 v[32:35], a[148:151], v[192:195], v[32:35]\n"
    "v_pk_add_u16 v190, s89, v190 clamp\n"
    "v_accvgpr_write_b32 a184, v196\n"
    "v_mfma_f32_16x16x32_bf16 v[16:19], a[124:127], v[192:195], v[16:19]\n"
    "v_pk_add_u16 v189, s89, v189 clamp\n"
    "v_pk_add_u16 v191, s89, v191 clamp\n"
    "v_mfma_f32_16x16x32_bf16 v[64:67], a[40:43], v[196:199], v[64:67]\n"
    "v_pk_add_u16 v186, s89, v186 clamp\n"
    "v_pk_add_u16 v187, s89, v187 clamp\n"
    "v_mfma_f32_16x16x32_bf16 v[32:35], a[144:147], v[188:191], v[32:35]\n"
    "s_waitcnt lgkmcnt(0)\n"
    "v_pk_add_u16 v185, s89, v185 clamp\n"
    "v_pk_maximum3_f16 v98, v180, v181, v98\n"
    "v_mfma_f32_16x16x32_bf16 v[48:51], a[8:11], v[188:191], v[48:51]\n"
    "s_waitcnt vmcnt(8)\n"
    "buffer_load_dwordx4 v1, s[16:19], s67 offen offset:2048 sc1 lds\n"
    "v_mfma_f32_16x16x32_bf16 v[64:67], a[36:39], v[192:195], v[64:67]\n"
    "s_barrier\n"
    "v_pk_add_u16 v184, s89, v184 clamp\n"
    "v_mfma_f32_16x16x32_bf16 v[16:19], a[120:123], v[188:191], v[16:19]\n"
    "ds_read_b128 a[48:51], v252 offset:24576\n"
    "ds_read_b128 a[68:71], v252 offset:34816\n"
    "v_mfma_f32_16x16x32_bf16 v[32:35], a[140:143], v[184:187], v[32:35]\n"
    "ds_read_b128 a[76:79], v252 offset:27648\n"
    "ds_read_b128 a[72:75], v252 offset:25600\n"
    "v_mfma_f32_16x16x32_bf16 v[48:51], a[4:7], v[184:187], v[48:51]\n"
    "ds_read_b128 a[92:95], v252 offset:35840\n"
    "v_pk_maximum3_f16 v98, v182, v183, v98\n"
    "v_mfma_f32_16x16x32_bf16 v[64:67], a[32:35], v[188:191], v[64:67]\n"
    "v_pk_add_u16 v182, s89, v182 clamp\n"
    "v_pk_add_u16 v181, s89, v181 clamp\n"
    "v_mfma_f32_16x16x32_bf16 v[64:67], a[28:31], v[184:187], v[64:67]\n"
    "v_pk_add_u16 v183, s89, v183 clamp\n"
    "v_pk_add_u16 v180, s89, v180 clamp\n"
    "v_mfma_f32_16x16x32_bf16 v[16:19], a[116:119], v[184:187], v[16:19]\n"
    "buffer_load_dwordx4 v1, s[16:19], s67 offen offset:3072 sc1 lds\n"
    "v_mfma_f32_16x16x32_bf16 v[32:35], a[136:139], v[180:183], v[32:35]\n"
    "ds_read_b128 a[64:67], v252 offset:32768\n"
    "v_accvgpr_write_b32 a176, v188\n"
    "v_mfma_f32_16x16x32_bf16 v[16:19], a[112:115], v[180:183], v[16:19]\n"
    "v_accvgpr_write_b32 a175, v187\n"
    "s_waitcnt lgkmcnt(5)\n"
    "v_mfma_f32_16x16x32_bf16 v[12:15], a[48:51], a[240:243], v[100:103]\n"
    "s_waitcnt lgkmcnt(4)\n"
    "ds_read_b128 a[56:59], v252 offset:28672\n"
    "v_accvgpr_write_b32 a177, v189\n"
    "v_mfma_f32_16x16x32_bf16 v[48:51], a[0:3], v[180:183], v[48:51]\n"
    "s_add_u32 m0, s68, 4096\n"
    "ds_read_b128 a[80:83], v252 offset:29696\n"
    "ds_read_b128 a[88:91], v252 offset:33792\n"
    "v_mfma_f32_16x16x32_bf16 v[8:11], a[68:71], a[236:239], v[100:103]\n"
    "v_accvgpr_write_b32 a188, v200\n"
    "v_accvgpr_write_b32 a190, v202\n"
    "v_mfma_f32_16x16x32_bf16 v[4:7], a[48:51], a[192:195], v[100:103]\n"
    "s_waitcnt lgkmcnt(5)\n"
    "ds_read_b128 a[60:63], v252 offset:30720\n"
    "ds_read_b128 a[52:55], v252 offset:26624\n"
    "v_mfma_f32_16x16x32_bf16 v[20:23], a[72:75], a[192:195], v[104:107]\n"
    "buffer_load_dwordx4 v1, s[16:19], s83 offen offset:0 sc1 lds\n"
    "v_mfma_f32_16x16x32_bf16 v[28:31], a[72:75], a[240:243], v[104:107]\n"
    "s_waitcnt lgkmcnt(6)\n"
    "v_accvgpr_write_b32 a171, v183\n"
    "v_accvgpr_write_b32 a186, v198\n"
    "v_mfma_f32_16x16x32_bf16 v[24:27], a[92:95], a[236:239], v[104:107]\n"
    "v_exp_f32 v18, v18\n"
    "v_mfma_f32_16x16x32_bf16 v[64:67], a[24:27], v[180:183], v[64:67]\n"
    "v_accvgpr_write_b32 a174, v186\n"
    "v_accvgpr_write_b32 a178, v190\n"
    "v_mfma_f32_16x16x32_bf16 v[20:23], a[76:79], a[196:199], v[20:23]\n"
    "v_accvgpr_write_b32 a191, v203\n"
    "v_accvgpr_write_b32 a179, v191\n"
    "v_mfma_f32_16x16x32_bf16 v[28:31], a[76:79], a[244:247], v[28:31]\n"
    "s_waitcnt lgkmcnt(5)\n"
    "v_accvgpr_write_b32 a180, v192\n"
    "v_mfma_f32_16x16x32_bf16 v[8:11], a[64:67], a[232:235], v[8:11]\n"
    "s_waitcnt lgkmcnt(3)\n"
    "ds_read_b128 a[84:87], v252 offset:31744\n"
    "v_accvgpr_write_b32 a187, v199\n"
    "v_mfma_f32_16x16x32_bf16 v[28:31], a[80:83], a[248:251], v[28:31]\n"
    "v_accvgpr_write_b32 a169, v181\n"
    "v_accvgpr_write_b32 a173, v185\n"
    "v_mfma_f32_16x16x32_bf16 v[20:23], a[80:83], a[200:203], v[20:23]\n"
    "s_waitcnt lgkmcnt(1)\n"
    "buffer_load_dwordx4 v1, s[16:19], s83 offen offset:1024 sc1 lds\n"
    "v_mfma_f32_16x16x32_bf16 v[4:7], a[52:55], a[196:199], v[4:7]\n"
    "v_exp_f32 v16, v16\n"
    "v_mfma_f32_16x16x32_bf16 v[24:27], a[88:91], a[232:235], v[24:27]\n"
    "v_accvgpr_write_b32 a168, v180\n"
    "v_accvgpr_write_b32 a182, v194\n"
    "v_mfma_f32_16x16x32_bf16 v[8:11], a[60:63], a[228:231], v[8:11]\n"
    "v_accvgpr_write_b32 a181, v193\n"
    "v_accvgpr_write_b32 a189, v201\n"
    "v_mfma_f32_16x16x32_bf16 v[8:11], a[56:59], a[224:227], v[8:11]\n"
    "s_add_u32 m0, s64, 0\n"
    "v_accvgpr_write_b32 a170, v182\n"
    "v_accvgpr_write_b32 a172, v184\n"
    "v_mfma_f32_16x16x32_bf16 v[4:7], a[56:59], a[200:203], v[4:7]\n"
    "v_exp_f32 v32, v32\n"
    "v_mfma_f32_16x16x32_bf16 v[4:7], a[60:63], a[204:207], v[4:7]\n"
    "s_waitcnt lgkmcnt(0)\n"
    "v_accvgpr_write_b32 a183, v195\n"
    "v_accvgpr_write_b32 a185, v197\n"
    "v_mfma_f32_16x16x32_bf16 v[28:31], a[84:87], a[252:255], v[28:31]\n"
    "s_add_u32 s83, s67, 24576\n"
    "buffer_load_dwordx4 v1, s[16:19], s83 offen offset:0 sc1 lds\n"
    "v_mfma_f32_16x16x32_bf16 v[12:15], a[52:55], a[244:247], v[12:15]\n"
    "v_exp_f32 v17, v17\n"
    "v_mfma_f32_16x16x32_bf16 v[12:15], a[56:59], a[248:251], v[12:15]\n"
    "v_exp_f32 v19, v19\n"
    "v_mfma_f32_16x16x32_bf16 v[8:11], a[52:55], a[220:223], v[8:11]\n"
    "v_exp_f32 v35, v35\n"
    "v_mfma_f32_16x16x32_bf16 v[20:23], a[84:87], a[204:207], v[20:23]\n"
    "v_exp_f32 v51, v51\n"
    "v_mfma_f32_16x16x32_bf16 v[12:15], a[60:63], a[252:255], v[12:15]\n"
    "v_exp_f32 v65, v65\n"
    "v_mfma_f32_16x16x32_bf16 v[4:7], a[64:67], a[208:211], v[4:7]\n"
    "v_mfma_f32_16x16x32_bf16 v[12:15], a[64:67], a[160:163], v[12:15]\n"
    "buffer_load_dwordx4 v1, s[16:19], s83 offen offset:1024 sc1 lds\n"
    "v_mfma_f32_16x16x32_bf16 v[4:7], a[68:71], a[212:215], v[4:7]\n"
    "v_cvt_pk_bf16_f32 v93, v18, v19\n"
    "v_mfma_f32_16x16x32_bf16 v[24:27], a[84:87], a[228:231], v[24:27]\n"
    "v_exp_f32 v34, v34\n"
    "v_mfma_f32_16x16x32_bf16 v[8:11], a[48:51], a[216:219], v[8:11]\n"
    "v_exp_f32 v33, v33\n"
    "v_mfma_f32_16x16x32_bf16 v[12:15], a[68:71], a[164:167], v[12:15]\n"
    "v_cvt_pk_bf16_f32 v92, v16, v17\n"
    "v_mfma_f32_16x16x32_bf16 v[28:31], a[88:91], a[160:163], v[28:31]\n"
    "s_waitcnt vmcnt(8)\n"
    "v_exp_f32 v48, v48\n"
    "v_mfma_f32_16x16x32_bf16 v[16:19], a[68:71], a[188:191], v[100:103]\n"
    "s_barrier\n"
    "ds_read_b128 a[120:123], v248 offset:1024\n"
    "v_mfma_f32_16x16x32_bf16 v[24:27], a[80:83], a[224:227], v[24:27]\n"
    "buffer_load_dwordx4 v1, s[16:19], s83 offen offset:2048 sc1 lds\n"
    "v_mfma_f32_16x16x32_bf16 v[16:19], a[64:67], a[184:187], v[16:19]\n"
    "ds_read_b128 a[124:127], v248 offset:13312\n"
    "v_cvt_pk_bf16_f32 v94, v32, v33\n"
    "v_mfma_f32_16x16x32_bf16 v[16:19], a[60:63], a[180:183], v[16:19]\n"
    "ds_read_b128 a[136:139], v248 offset:3072\n"
    "ds_read_b128 a[128:131], v248 offset:2048\n"
    "v_mfma_f32_16x16x32_bf16 v[24:27], a[76:79], a[220:223], v[24:27]\n"
    "v_exp_f32 v50, v50\n"
    "v_mfma_f32_16x16x32_bf16 v[20:23], a[88:91], a[208:211], v[20:23]\n"
    "v_cvt_pk_bf16_f32 v97, v50, v51\n"
    "v_cvt_pk_bf16_f32 v95, v34, v35\n"
    "v_mfma_f32_16x16x32_bf16 v[32:35], a[92:95], a[188:191], v[104:107]\n"
    "v_exp_f32 v67, v67\n"
    "v_mfma_f32_16x16x32_bf16 v[16:19], a[56:59], a[176:179], v[16:19]\n"
    "v_mfma_f32_16x16x32_bf16 v[32:35], a[88:91], a[184:187], v[32:35]\n"
    "buffer_load_dwordx4 v1, s[16:19], s83 offen offset:3072 sc1 lds\n"
    "v_mfma_f32_16x16x32_bf16 v[20:23], a[92:95], a[212:215], v[20:23]\n"
    "v_pk_add_u16 v98, s89, v98\n"
    "s_waitcnt lgkmcnt(2)\n"
    "v_mfma_f32_16x16x32_bf16 v[160:163], a[124:127], v[88:91], 0\n"
    "ds_read_b128 a[132:135], v248 offset:14336\n"
    "s_waitcnt lgkmcnt(2)\n"
    "v_mfma_f32_16x16x32_bf16 v[136:139], a[136:139], v[68:71], 0\n"
    "ds_read_b128 a[144:147], v248 offset:4096\n"
    "v_mfma_f32_16x16x32_bf16 v[128:131], a[124:127], v[72:75], 0\n"
    "s_add_u32 s83, s67, 28672\n"
    "ds_read_b128 a[148:151], v248 offset:16384\n"
    "ds_read_b128 a[112:115], v248 offset:0\n"
    "v_mfma_f32_16x16x32_bf16 v[144:147], a[120:123], v[76:79], 0\n"
    "v_exp_f32 v64, v64\n"
    "v_mfma_f32_16x16x32_bf16 v[32:35], a[84:87], a[180:183], v[32:35]\n"
    "s_add_u32 m0, s64, 4096\n"
    "ds_read_b128 a[152:155], v248 offset:5120\n"
    "v_mfma_f32_16x16x32_bf16 v[16:19], a[52:55], a[172:175], v[16:19]\n"
    "buffer_load_dwordx4 v1, s[16:19], s83 offen offset:0 sc1 lds\n"
    "v_mfma_f32_16x16x32_bf16 v[128:131], a[120:123], v[68:71], v[128:131]\n"
    "v_exp_f32 v66, v66\n"
    "v_mfma_f32_16x16x32_bf16 v[176:179], a[120:123], v[92:95], 0\n"
    "s_waitcnt lgkmcnt(5)\n"
    "ds_read_b128 a[116:119], v248 offset:12288\n"
    "v_cvt_pk_bf16_f32 v99, v66, v67\n"
    "v_mfma_f32_16x16x32_bf16 v[132:135], a[128:131], v[68:71], 0\n"
    "v_exp_f32 v49, v49\n"
    "v_mfma_f32_16x16x32_bf16 v[28:31], a[92:95], a[164:167], v[28:31]\n"
    "s_waitcnt lgkmcnt(5)\n"
    "v_cvt_pk_bf16_f32 v96, v48, v49\n"
    "v_mfma_f32_16x16x32_bf16 v[148:151], a[132:135], v[80:83], 0\n"
    "v_and_b32 v98, 0x80008000, v98\n"
    "v_mfma_f32_16x16x32_bf16 v[164:167], a[128:131], v[84:87], 0\n"
    "v_exp_f32 v4, v4\n"
    "v_mfma_f32_16x16x32_bf16 v[24:27], a[72:75], a[216:219], v[24:27]\n"
    "buffer_load_dwordx4 v1, s[16:19], s83 offen offset:1024 sc1 lds\n"
    "v_mfma_f32_16x16x32_bf16 v[32:35], a[80:83], a[176:179], v[32:35]\n"
    "v_exp_f32 v12, v12\n"
    "v_mfma_f32_16x16x32_bf16 v[144:147], a[124:127], v[80:83], v[144:147]\n"
    "v_exp_f32 v31, v31\n"
    "v_mfma_f32_16x16x32_bf16 v[160:163], a[120:123], v[84:87], v[160:163]\n"
    "s_waitcnt lgkmcnt(2)\n"
    "v_mfma_f32_16x16x32_bf16 v[124:127], a[112:115], v[68:71], 0\n"
    "v_exp_f32 v27, v27\n"
    "v_mfma_f32_16x16x32_bf16 v[188:191], a[148:151], v[72:75], 0\n"
    "ds_read_b128 a[140:143], v248 offset:15360\n"
    "v_mfma_f32_16x16x32_bf16 v[16:19], a[48:51], a[168:171], v[16:19]\n"
    "s_waitcnt lgkmcnt(2)\n"
    "ds_read_b128 a[120:123], v248 offset:7168\n"
    "v_cmp_ne_u32 s[34:35], 0, v98\n"
    "v_mfma_f32_16x16x32_bf16 a[20:23], a[152:155], v[84:87], 0\n"
    "s_waitcnt lgkmcnt(2)\n"
    "v_exp_f32 v29, v29\n"
    "v_mfma_f32_16x16x32_bf16 v[140:143], a[116:119], v[80:83], 0\n"
    "buffer_load_dwordx4 v[204:207], v242, s[40:43], s75 offen offset:0 nt\n"
    "v_mfma_f32_16x16x32_bf16 v[188:191], a[144:147], v[68:71], v[188:191]\n"
    "v_cvt_pk_bf16_f32 v98, v64, v65\n"
    "v_mfma_f32_16x16x32_bf16 v[124:127], a[116:119], v[72:75], v[124:127]\n"
    "ds_read_b128 a[156:159], v248 offset:17408\n"
    "v_mfma_f32_16x16x32_bf16 v[132:135], a[132:135], v[72:75], v[132:135]\n"
    "v_exp_f32 v5, v5\n"
    "v_mfma_f32_16x16x32_bf16 v[192:195], a[152:155], v[68:71], 0\n"
    "buffer_load_dwordx4 v[208:211], v242, s[40:43], s75 offen offset:768 nt\n"
    "v_mfma_f32_16x16x32_bf16 v[176:179], a[124:127], v[96:99], v[176:179]\n"
    "ds_read_b128 a[124:127], v248 offset:19456\n"
    "v_mfma_f32_16x16x32_bf16 v[140:143], a[112:115], v[76:79], v[140:143]\n"
    "v_exp_f32 v26, v26\n"
    "v_mfma_f32_16x16x32_bf16 v[172:175], a[116:119], v[96:99], 0\n"
    "s_waitcnt lgkmcnt(2)\n"
    "buffer_load_dwordx4 v[212:215], v242, s[40:43], s75 offen offset:256 nt\n"
    "v_mfma_f32_16x16x32_bf16 a[12:15], a[120:123], v[76:79], 0\n"
    "v_exp_f32 v20, v20\n"
    "v_mfma_f32_16x16x32_bf16 a[0:3], a[144:147], v[76:79], 0\n"
    "v_mfma_f32_16x16x32_bf16 v[152:155], a[140:143], v[80:83], 0\n"
    "v_mfma_f32_16x16x32_bf16 v[172:175], a[112:115], v[92:95], v[172:175]\n"
    "v_exp_f32 v16, v16\n"
    "v_mfma_f32_16x16x32_bf16 a[16:19], a[148:151], v[88:91], 0\n"
    "v_exp_f32 v17, v17\n"
    "v_mfma_f32_16x16x32_bf16 v[164:167], a[132:135], v[88:91], v[164:167]\n"
    "v_exp_f32 v24, v24\n"
    "v_mfma_f32_16x16x32_bf16 v[152:155], a[136:139], v[76:79], v[152:155]\n"
    "buffer_load_dwordx4 v[216:219], v242, s[40:43], s75 offen offset:1024 nt\n"
    "v_mfma_f32_16x16x32_bf16 v[180:183], a[132:135], v[96:99], 0\n"
    "v_exp_f32 v19, v19\n"
    "v_mfma_f32_16x16x32_bf16 v[156:159], a[112:115], v[84:87], 0\n"
    "v_exp_f32 v14, v14\n"
    "v_mfma_f32_16x16x32_bf16 a[44:47], a[120:123], v[92:95], 0\n"
    "v_exp_f32 v9, v9\n"
    "v_mfma_f32_16x16x32_bf16 a[32:35], a[144:147], v[92:95], 0\n"
    "s_waitcnt lgkmcnt(1)\n"
    "buffer_load_dwordx4 v[220:223], v242, s[40:43], s75 offen offset:512 nt\n"
    "v_mfma_f32_16x16x32_bf16 a[36:39], a[156:159], v[96:99], 0\n"
    "v_mfma_f32_16x16x32_bf16 a[0:3], a[148:151], v[80:83], a[0:3]\n"
    "buffer_load_dwordx4 v[224:227], v242, s[40:43], s75 offen offset:1280 nt\n"
    "v_mfma_f32_16x16x32_bf16 a[32:35], a[148:151], v[96:99], a[32:35]\n"
    "ds_read_b128 a[132:135], v248 offset:20480\n"
    "v_mfma_f32_16x16x32_bf16 v[148:151], a[128:131], v[76:79], v[148:151]\n"
    "v_exp_f32 v25, v25\n"
    "v_mfma_f32_16x16x32_bf16 a[4:7], a[156:159], v[80:83], 0\n"
    "ds_read_b128 a[112:115], v248 offset:6144\n"
    "v_mfma_f32_16x16x32_bf16 v[180:183], a[128:131], v[92:95], v[180:183]\n"
    "ds_read_b128 a[148:151], v248 offset:22528\n"
    "v_mfma_f32_16x16x32_bf16 v[184:187], a[140:143], v[96:99], 0\n"
    "v_mfma_f32_16x16x32_bf16 v[156:159], a[116:119], v[88:91], v[156:159]\n"
    "v_exp_f32 v10, v10\n"
    "v_mfma_f32_16x16x32_bf16 v[136:139], a[140:143], v[72:75], v[136:139]\n"
    "s_waitcnt lgkmcnt(3)\n"
    "v_mfma_f32_16x16x32_bf16 v[200:203], a[124:127], v[72:75], 0\n"
    "ds_read_b128 a[116:119], v248 offset:18432\n"
    "ds_read_b128 a[128:131], v248 offset:8192\n"
    "v_mfma_f32_16x16x32_bf16 a[20:23], a[156:159], v[88:91], a[20:23]\n"
    "v_exp_f32 v7, v7\n"
    "v_mfma_f32_16x16x32_bf16 a[4:7], a[152:155], v[76:79], a[4:7]\n"
    "v_mfma_f32_16x16x32_bf16 v[192:195], a[156:159], v[72:75], v[192:195]\n"
    "v_exp_f32 v21, v21\n"
    "v_mfma_f32_16x16x32_bf16 a[36:39], a[152:155], v[92:95], a[36:39]\n"
    "ds_read_b128 a[156:159], v248 offset:23552\n"
    "v_mfma_f32_16x16x32_bf16 v[184:187], a[136:139], v[92:95], v[184:187]\n"
    "s_waitcnt lgkmcnt(5)\n"
    "v_mfma_f32_16x16x32_bf16 a[64:67], a[132:135], v[80:83], 0\n"
    "v_mfma_f32_16x16x32_bf16 v[168:171], a[136:139], v[84:87], 0\n"
    "s_waitcnt lgkmcnt(3)\n"
    "v_mfma_f32_16x16x32_bf16 a[88:91], a[148:151], v[88:91], 0\n"
    "v_mfma_f32_16x16x32_bf16 a[16:19], a[144:147], v[84:87], a[16:19]\n"
    "s_waitcnt lgkmcnt(2)\n"
    "ds_read_b128 a[152:155], v248 offset:11264\n"
    "v_mfma_f32_16x16x32_bf16 a[8:11], a[116:119], v[80:83], 0\n"
    "v_mfma_f32_16x16x32_bf16 v[168:171], a[140:143], v[88:91], v[168:171]\n"
    "v_exp_f32 v15, v15\n"
    "v_mfma_f32_16x16x32_bf16 a[28:31], a[124:127], v[88:91], 0\n"
    "s_waitcnt lgkmcnt(2)\n"
    "v_exp_f32 v11, v11\n"
    "v_mfma_f32_16x16x32_bf16 a[48:51], a[128:131], v[68:71], 0\n"
    "v_mfma_f32_16x16x32_bf16 a[56:59], a[148:151], v[72:75], 0\n"
    "v_mfma_f32_16x16x32_bf16 a[28:31], a[120:123], v[84:87], a[28:31]\n"
    "v_mfma_f32_16x16x32_bf16 a[8:11], a[112:115], v[76:79], a[8:11]\n"
    "buffer_load_dwordx2 v[236:237], v241, s[36:39], s81 offen offset:0 sc1\n"
    "v_mfma_f32_16x16x32_bf16 v[200:203], a[120:123], v[68:71], v[200:203]\n"
    "v_exp_f32 v28, v28\n"
    "v_mfma_f32_16x16x32_bf16 v[196:199], a[112:115], v[68:71], 0\n"
    "s_waitcnt lgkmcnt(1)\n"
    "v_mfma_f32_16x16x32_bf16 a[108:111], a[156:159], v[96:99], 0\n"
    "v_mfma_f32_16x16x32_bf16 v[32:35], a[76:79], a[172:175], v[32:35]\n"
    "ds_read_b128 a[136:139], v248 offset:9216\n"
    "ds_read_b128 a[140:143], v248 offset:21504\n"
    "v_mfma_f32_16x16x32_bf16 a[80:83], a[128:131], v[84:87], 0\n"
    "v_mfma_f32_16x16x32_bf16 a[24:27], a[112:115], v[84:87], 0\n"
    "v_mfma_f32_16x16x32_bf16 a[12:15], a[124:127], v[80:83], a[12:15]\n"
    "v_exp_f32 v23, v23\n"
    "v_mfma_f32_16x16x32_bf16 v[32:35], a[72:75], a[168:171], v[32:35]\n"
    "ds_read_b128 a[120:123], v3 offset:16384\n"
    "ds_read_b128 a[144:147], v248 offset:10240\n"
    "v_mfma_f32_16x16x32_bf16 a[40:43], a[116:119], v[96:99], 0\n"
    "v_exp_f32 v22, v22\n"
    "v_mfma_f32_16x16x32_bf16 a[80:83], a[132:135], v[88:91], a[80:83]\n"
    "v_mfma_f32_16x16x32_bf16 a[76:79], a[156:159], v[80:83], 0\n"
    "v_mfma_f32_16x16x32_bf16 a[40:43], a[112:115], v[92:95], a[40:43]\n"
    "s_waitcnt lgkmcnt(3)\n"
    "v_exp_f32 v32, v32\n"
    "v_mfma_f32_16x16x32_bf16 a[52:55], a[136:139], v[68:71], 0\n"
    "v_exp_f32 v33, v33\n"
    "v_mfma_f32_16x16x32_bf16 a[48:51], a[132:135], v[72:75], a[48:51]\n"
    "v_mfma_f32_16x16x32_bf16 a[92:95], a[152:155], v[84:87], 0\n"
    "s_waitcnt lgkmcnt(2)\n"
    "v_mfma_f32_16x16x32_bf16 a[68:71], a[140:143], v[80:83], 0\n"
    "v_mfma_f32_16x16x32_bf16 a[100:103], a[140:143], v[96:99], 0\n"
    "s_waitcnt lgkmcnt(0)\n"
    "ds_read_b128 v[100:103], v250 offset:896\n"
    "v_mfma_f32_16x16x32_bf16 a[88:91], a[144:147], v[84:87], a[88:91]\n"
    "v_mfma_f32_16x16x32_bf16 a[24:27], a[116:119], v[88:91], a[24:27]\n"
    "v_mfma_f32_16x16x32_bf16 a[84:87], a[136:139], v[84:87], 0\n"
    "ds_read_b128 a[112:115], v3 offset:12288\n"
    "v_mfma_f32_16x16x32_bf16 a[96:99], a[132:135], v[96:99], 0\n"
    "v_exp_f32 v34, v34\n"
    "v_mfma_f32_16x16x32_bf16 v[196:199], a[116:119], v[72:75], v[196:199]\n"
    "v_exp_f32 v6, v6\n"
    "v_mfma_f32_16x16x32_bf16 a[44:47], a[124:127], v[96:99], a[44:47]\n"
    "v_mfma_f32_16x16x32_bf16 a[108:111], a[152:155], v[92:95], a[108:111]\n"
    "v_exp_f32 v8, v8\n"
    "v_mfma_f32_16x16x32_bf16 a[76:79], a[152:155], v[76:79], a[76:79]\n"
    "v_mfma_f32_16x16x32_bf16 a[104:107], a[144:147], v[92:95], 0\n"
    "s_add_u32 s67, s67, 49152\n"
    "v_mfma_f32_16x16x32_bf16 a[56:59], a[144:147], v[68:71], a[56:59]\n"
    "v_exp_f32 v30, v30\n"
    "v_mfma_f32_16x16x32_bf16 a[96:99], a[128:131], v[92:95], a[96:99]\n"
    "ds_read_b128 a[124:127], v3 offset:18432\n"
    "v_mfma_f32_16x16x32_bf16 a[72:75], a[144:147], v[76:79], 0\n"
    "ds_read_b128 a[132:135], v3 offset:22528\n"
    "s_waitcnt vmcnt(7)\n"
    "v_mfma_f32_16x16x32_bf16 a[104:107], a[148:151], v[96:99], a[104:107]\n"
    "ds_add_u32 v247, v246\n"
    "v_mfma_f32_16x16x32_bf16 a[64:67], a[128:131], v[76:79], a[64:67]\n"
    "ds_read_b128 a[116:119], v3 offset:14336\n"
    "ds_read_b128 a[128:131], v3 offset:20480\n"
    "v_smfmac_f32_16x16x64_bf16 v[120:123], v[52:55], v[92:99], v253\n"
    "v_add_u32 v3, 49152, v252\n"
    "v_mfma_f32_16x16x32_bf16 a[60:63], a[152:155], v[68:71], 0\n"
    "v_mfma_f32_16x16x32_bf16 a[52:55], a[140:143], v[72:75], a[52:55]\n"
    "v_mfma_f32_16x16x32_bf16 a[84:87], a[140:143], v[88:91], a[84:87]\n"
    "v_exp_f32 v13, v13\n"
    "v_mfma_f32_16x16x32_bf16 a[60:63], a[156:159], v[72:75], a[60:63]\n"
    "v_exp_f32 v35, v35\n"
    "v_mfma_f32_16x16x32_bf16 a[92:95], a[156:159], v[88:91], a[92:95]\n"
    "v_exp_f32 v18, v18\n"
    "v_mfma_f32_16x16x32_bf16 a[72:75], a[148:151], v[80:83], a[72:75]\n"
    "v_mfma_f32_16x16x32_bf16 a[100:103], a[136:139], v[92:95], a[100:103]\n"
    "s_waitcnt lgkmcnt(0)\n"
    "v_mfma_f32_16x16x32_bf16 a[68:71], a[136:139], v[76:79], a[68:71]\n"
    "s_branch .LNV\n"
    ".fill ((0x20000 - (. - _Z8attn_fwdPKtPKiS2_S0_S0_S0_S0_PcPt)) & 4095) / 4, 4, 0xBF800000\n"
    ".LhVN:\n"
    "s_waitcnt vmcnt(18)\n"
    "ds_read_b128 v[52:55], v251 offset:768\n"
    "ds_read_b128 v[184:187], v97 offset:49152\n"
    "ds_read_b128 v[196:199], v96 offset:49408\n"
    "ds_read_b128 v[200:203], v97 offset:49408\n"
    "ds_read_b128 v[188:191], v96 offset:49280\n"
    "ds_read_b128 v[180:183], v96 offset:49152\n"
    "v_mov_b32 v2, v3\n"
    "ds_read_b128 v[192:195], v97 offset:49280\n"
    "s_add_u32 m0, s68, .Lend-.Lend2+(0)\n"
    "s_waitcnt lgkmcnt(6)\n"
    "v_smfmac_f32_16x16x64_bf16 v[108:111], v[52:55], v[68:75], v253\n"
    "s_waitcnt lgkmcnt(3)\n"
    "v_pk_maximum3_f16 v98, v200, v201, v98\n"
    "v_pk_maximum3_f16 v98, v202, v203, v98\n"
    "v_pk_add_u16 v200, s89, v200 clamp\n"
    "v_smfmac_f32_16x16x64_bf16 v[116:119], v[52:55], v[84:91], v253\n"
    "s_add_u32 s83, s67, 4096\n"
    "v_pk_add_u16 v201, s89, v201 clamp\n"
    "v_pk_add_u16 v202, s89, v202 clamp\n"
    "v_pk_add_u16 v203, s89, v203 clamp\n"
    "v_pk_maximum3_f16 v98, v196, v197, v98\n"
    "v_pk_maximum3_f16 v98, v198, v199, v98\n"
    "v_mfma_f32_16x16x32_bf16 v[48:51], a[20:23], v[200:203], v[48:51]\n"
    "v_pk_add_u16 v197, s89, v197 clamp\n"
    "v_pk_add_u16 v198, s89, v198 clamp\n"
    "v_mfma_f32_16x16x32_bf16 v[64:67], a[44:47], v[200:203], v[64:67]\n"
    "buffer_load_dwordx4 v1, s[16:19], s67 offen offset:0 sc1 lds\n"
    "v_mfma_f32_16x16x32_bf16 v[16:19], a[132:135], v[200:203], v[16:19]\n"
    "v_pk_add_u16 v199, s89, v199 clamp\n"
    "v_pk_add_u16 v196, s89, v196 clamp\n"
    "v_mfma_f32_16x16x32_bf16 v[32:35], a[156:159], v[200:203], v[32:35]\n"
    "s_waitcnt lgkmcnt(0)\n"
    "v_pk_maximum3_f16 v98, v192, v193, v98\n"
    "v_pk_maximum3_f16 v98, v194, v195, v98\n"
    "v_pk_add_u16 v194, s89, v194 clamp\n"
    "v_mfma_f32_16x16x32_bf16 v[32:35], a[152:155], v[196:199], v[32:35]\n"
    "v_pk_add_u16 v192, s89, v192 clamp\n"
    "v_pk_add_u16 v193, s89, v193 clamp\n"
    "v_mfma_f32_16x16x32_bf16 v[64:67], a[40:43], v[196:199], v[64:67]\n"
    "v_pk_maximum3_f16 v98, v188, v189, v98\n"
    "v_pk_add_u16 v195, s89, v195 clamp\n"
    "v_mfma_f32_16x16x32_bf16 v[16:19], a[128:131], v[196:199], v[16:19]\n"
    "v_pk_add_u16 v188, s89, v188 clamp\n"
    "v_pk_maximum3_f16 v98, v190, v191, v98\n"
    "v_mfma_f32_16x16x32_bf16 v[32:35], a[148:151], v[192:195], v[32:35]\n"
    "buffer_load_dwordx4 v1, s[16:19], s67 offen offset:1024 sc1 lds\n"
    "v_mfma_f32_16x16x32_bf16 v[64:67], a[36:39], v[192:195], v[64:67]\n"
    "v_pk_maximum3_f16 v98, v184, v185, v98\n"
    "v_pk_add_u16 v189, s89, v189 clamp\n"
    "v_mfma_f32_16x16x32_bf16 v[48:51], a[16:19], v[196:199], v[48:51]\n"
    "v_pk_add_u16 v185, s89, v185 clamp\n"
    "v_pk_add_u16 v191, s89, v191 clamp\n"
    "v_pk_add_u16 v190, s89, v190 clamp\n"
    "v_mfma_f32_16x16x32_bf16 v[48:51], a[12:15], v[192:195], v[48:51]\n"
    "v_pk_maximum3_f16 v98, v186, v187, v98\n"
    "v_pk_add_u16 v186, s89, v186 clamp\n"
    "v_mfma_f32_16x16x32_bf16 v[16:19], a[124:127], v[192:195], v[16:19]\n"
    "v_pk_maximum3_f16 v98, v180, v181, v98\n"
    "v_pk_add_u16 v187, s89, v187 clamp\n"
    "v_mfma_f32_16x16x32_bf16 v[16:19], a[120:123], v[188:191], v[16:19]\n"
    "v_pk_add_u16 v180, s89, v180 clamp\n"
    "v_pk_add_u16 v184, s89, v184 clamp\n"
    "v_mfma_f32_16x16x32_bf16 v[64:67], a[32:35], v[188:191], v[64:67]\n"
    "v_accvgpr_write_b32 a177, v189\n"
    "v_accvgpr_write_b32 a190, v202\n"
    "v_mfma_f32_16x16x32_bf16 v[32:35], a[144:147], v[188:191], v[32:35]\n"
    "s_waitcnt vmcnt(14)\n"
    "buffer_load_dwordx4 v1, s[16:19], s67 offen offset:2048 sc1 lds\n"
    "v_smfmac_f32_16x16x64_bf16 v[112:115], v[52:55], v[76:83], v253\n"
    "s_barrier\n"
    "ds_read_b128 a[48:51], v252 offset:24576\n"
    "v_mfma_f32_16x16x32_bf16 v[48:51], a[8:11], v[188:191], v[48:51]\n"
    "ds_read_b128 a[68:71], v252 offset:34816\n"
    "ds_read_b128 a[72:75], v252 offset:25600\n"
    "v_mfma_f32_16x16x32_bf16 v[32:35], a[140:143], v[184:187], v[32:35]\n"
    "v_pk_add_u16 v181, s89, v181 clamp\n"
    "v_accvgpr_write_b32 a188, v200\n"
    "v_mfma_f32_16x16x32_bf16 v[48:51], a[4:7], v[184:187], v[48:51]\n"
    "v_pk_maximum3_f16 v98, v182, v183, v98\n"
    "v_accvgpr_write_b32 a176, v188\n"
    "v_mfma_f32_16x16x32_bf16 v[64:67], a[28:31], v[184:187], v[64:67]\n"
    "v_pk_add_u16 v183, s89, v183 clamp\n"
    "v_pk_add_u16 v182, s89, v182 clamp\n"
    "v_mfma_f32_16x16x32_bf16 v[16:19], a[116:119], v[184:187], v[16:19]\n"
    "ds_read_b128 a[52:55], v252 offset:26624\n"
    "ds_read_b128 a[76:79], v252 offset:27648\n"
    "v_mfma_f32_16x16x32_bf16 v[48:51], a[0:3], v[180:183], v[48:51]\n"
    "buffer_load_dwordx4 v1, s[16:19], s67 offen offset:3072 sc1 lds\n"
    "v_mfma_f32_16x16x32_bf16 v[32:35], a[136:139], v[180:183], v[32:35]\n"
    "ds_read_b128 a[92:95], v252 offset:35840\n"
    "ds_read_b128 a[88:91], v252 offset:33792\n"
    "v_mfma_f32_16x16x32_bf16 v[16:19], a[112:115], v[180:183], v[16:19]\n"
    "s_waitcnt lgkmcnt(6)\n"
    "v_accvgpr_write_b32 a180, v192\n"
    "v_accvgpr_write_b32 a181, v193\n"
    "v_mfma_f32_16x16x32_bf16 v[4:7], a[48:51], a[192:195], v[100:103]\n"
    "s_waitcnt lgkmcnt(4)\n"
    "ds_read_b128 a[60:63], v252 offset:30720\n"
    "ds_read_b128 a[56:59], v252 offset:28672\n"
    "v_mfma_f32_16x16x32_bf16 v[20:23], a[72:75], a[192:195], v[104:107]\n"
    "ds_read_b128 a[64:67], v252 offset:32768\n"
    "v_accvgpr_write_b32 a191, v203\n"
    "v_mfma_f32_16x16x32_bf16 v[28:31], a[72:75], a[240:243], v[104:107]\n"
    "s_add_u32 m0, s68, 4096\n"
    "v_accvgpr_write_b32 a175, v187\n"
    "v_accvgpr_write_b32 a182, v194\n"
    "v_mfma_f32_16x16x32_bf16 v[12:15], a[48:51], a[240:243], v[100:103]\n"
    "v_accvgpr_write_b32 a178, v190\n"
    "v_accvgpr_write_b32 a171, v183\n"
    "v_mfma_f32_16x16x32_bf16 v[8:11], a[68:71], a[236:239], v[100:103]\n"
    "buffer_load_dwordx4 v1, s[16:19], s83 offen offset:0 sc1 lds\n"
    "v_mfma_f32_16x16x32_bf16 v[64:67], a[24:27], v[180:183], v[64:67]\n"
    "s_waitcnt lgkmcnt(6)\n"
    "ds_read_b128 a[80:83], v252 offset:29696\n"
    "v_accvgpr_write_b32 a172, v184\n"
    "v_mfma_f32_16x16x32_bf16 v[4:7], a[52:55], a[196:199], v[4:7]\n"
    "s_waitcnt lgkmcnt(5)\n"
    "v_exp_f32 v16, v16\n"
    "v_mfma_f32_16x16x32_bf16 v[24:27], a[92:95], a[236:239], v[104:107]\n"
    "s_waitcnt lgkmcnt(4)\n"
    "v_accvgpr_write_b32 a174, v186\n"
    "v_accvgpr_write_b32 a169, v181\n"
    "v_mfma_f32_16x16x32_bf16 v[24:27], a[88:91], a[232:235], v[24:27]\n"
    "s_waitcnt lgkmcnt(2)\n"
    "ds_read_b128 a[84:87], v252 offset:31744\n"
    "v_mfma_f32_16x16x32_bf16 v[4:7], a[56:59], a[200:203], v[4:7]\n"
    "s_waitcnt lgkmcnt(2)\n"
    "v_accvgpr_write_b32 a189, v201\n"
    "v_accvgpr_write_b32 a179, v191\n"
    "v_mfma_f32_16x16x32_bf16 v[8:11], a[64:67], a[232:235], v[8:11]\n"
    "v_exp_f32 v17, v17\n"
    "v_mfma_f32_16x16x32_bf16 v[28:31], a[76:79], a[244:247], v[28:31]\n"
    "buffer_load_dwordx4 v1, s[16:19], s83 offen offset:1024 sc1 lds\n"
    "v_mfma_f32_16x16x32_bf16 v[20:23], a[76:79], a[196:199], v[20:23]\n"
    "v_accvgpr_write_b32 a184, v196\n"
    "v_accvgpr_write_b32 a170, v182\n"
    "v_mfma_f32_16x16x32_bf16 v[12:15], a[52:55], a[244:247], v[12:15]\n"
    "v_accvgpr_write_b32 a185, v197\n"
    "v_accvgpr_write_b32 a186, v198\n"
    "v_mfma_f32_16x16x32_bf16 v[12:15], a[56:59], a[248:251], v[12:15]\n"
    "v_cvt_pk_bf16_f32 v92, v16, v17\n"
    "v_mfma_f32_16x16x32_bf16 v[12:15], a[60:63], a[252:255], v[12:15]\n"
    "s_add_u32 m0, s70, 0\n"
    "s_add_u32 s83, s67, 24576\n"
    "v_accvgpr_write_b32 a168, v180\n"
    "v_mfma_f32_16x16x32_bf16 v[12:15], a[64:67], a[160:163], v[12:15]\n"
    "s_waitcnt lgkmcnt(1)\n"
    "v_accvgpr_write_b32 a183, v195\n"
    "v_pk_add_u16 v98, s89, v98\n"
    "v_mfma_f32_16x16x32_bf16 v[28:31], a[80:83], a[248:251], v[28:31]\n"
    "s_waitcnt lgkmcnt(0)\n"
    "v_accvgpr_write_b32 a173, v185\n"
    "v_accvgpr_write_b32 a187, v199\n"
    "v_mfma_f32_16x16x32_bf16 v[28:31], a[84:87], a[252:255], v[28:31]\n"
    "buffer_load_dwordx4 v1, s[16:19], s83 offen offset:0 sc1 lds\n"
    "v_mfma_f32_16x16x32_bf16 v[4:7], a[60:63], a[204:207], v[4:7]\n"
    "v_exp_f32 v18, v18\n"
    "v_mfma_f32_16x16x32_bf16 v[8:11], a[60:63], a[228:231], v[8:11]\n"
    "v_exp_f32 v19, v19\n"
    "v_mfma_f32_16x16x32_bf16 v[8:11], a[56:59], a[224:227], v[8:11]\n"
    "v_exp_f32 v32, v32\n"
    "v_mfma_f32_16x16x32_bf16 v[8:11], a[52:55], a[220:223], v[8:11]\n"
    "v_exp_f32 v35, v35\n"
    "v_mfma_f32_16x16x32_bf16 v[28:31], a[88:91], a[160:163], v[28:31]\n"
    "v_cvt_pk_bf16_f32 v93, v18, v19\n"
    "v_mfma_f32_16x16x32_bf16 v[24:27], a[84:87], a[228:231], v[24:27]\n"
    "v_exp_f32 v34, v34\n"
    "v_mfma_f32_16x16x32_bf16 v[20:23], a[80:83], a[200:203], v[20:23]\n"
    "v_exp_f32 v67, v67\n"
    "v_mfma_f32_16x16x32_bf16 v[16:19], a[68:71], a[188:191], v[100:103]\n"
    "buffer_load_dwordx4 v1, s[16:19], s83 offen offset:1024 sc1 lds\n"
    "v_mfma_f32_16x16x32_bf16 v[4:7], a[64:67], a[208:211], v[4:7]\n"
    "v_exp_f32 v33, v33\n"
    "v_mfma_f32_16x16x32_bf16 v[20:23], a[84:87], a[204:207], v[20:23]\n"
    "v_mfma_f32_16x16x32_bf16 v[20:23], a[88:91], a[208:211], v[20:23]\n"
    "v_mfma_f32_16x16x32_bf16 v[24:27], a[80:83], a[224:227], v[24:27]\n"
    "v_cvt_pk_bf16_f32 v95, v34, v35\n"
    "v_cvt_pk_bf16_f32 v94, v32, v33\n"
    "v_mfma_f32_16x16x32_bf16 v[32:35], a[92:95], a[188:191], v[104:107]\n"
    "s_waitcnt vmcnt(14)\n"
    "v_exp_f32 v66, v66\n"
    "v_mfma_f32_16x16x32_bf16 v[16:19], a[64:67], a[184:187], v[16:19]\n"
    "s_barrier\n"
    "ds_read_b128 a[112:115], v249 offset:0\n"
    "v_mfma_f32_16x16x32_bf16 v[32:35], a[88:91], a[184:187], v[32:35]\n"
    "v_exp_f32 v51, v51\n"
    "v_mfma_f32_16x16x32_bf16 v[32:35], a[84:87], a[180:183], v[32:35]\n"
    "buffer_load_dwordx4 v1, s[16:19], s83 offen offset:2048 sc1 lds\n"
    "v_mfma_f32_16x16x32_bf16 v[16:19], a[60:63], a[180:183], v[16:19]\n"
    "ds_read_b128 a[144:147], v249 offset:4096\n"
    "ds_read_b128 a[140:143], v249 offset:15360\n"
    "v_mfma_f32_16x16x32_bf16 v[16:19], a[56:59], a[176:179], v[16:19]\n"
    "ds_read_b128 a[152:155], v249 offset:5120\n"
    "ds_read_b128 a[132:135], v249 offset:14336\n"
    "v_mfma_f32_16x16x32_bf16 v[4:7], a[68:71], a[212:215], v[4:7]\n"
    "ds_read_b128 a[120:123], v249 offset:1024\n"
    "ds_read_b128 a[128:131], v249 offset:2048\n"
    "v_mfma_f32_16x16x32_bf16 v[8:11], a[48:51], a[216:219], v[8:11]\n"
    "ds_read_b128 a[124:127], v249 offset:13312\n"
    "ds_read_b128 a[136:139], v249 offset:3072\n"
    "v_mfma_f32_16x16x32_bf16 v[24:27], a[76:79], a[220:223], v[24:27]\n"
    "v_exp_f32 v65, v65\n"
    "v_mfma_f32_16x16x32_bf16 v[12:15], a[68:71], a[164:167], v[12:15]\n"
    "ds_read_b128 a[116:119], v249 offset:12288\n"
    "v_mfma_f32_16x16x32_bf16 v[32:35], a[80:83], a[176:179], v[32:35]\n"
    "buffer_load_dwordx4 v1, s[16:19], s83 offen offset:3072 sc1 lds\n"
    "v_mfma_f32_16x16x32_bf16 v[20:23], a[92:95], a[212:215], v[20:23]\n"
    "v_mfma_f32_16x16x32_bf16 v[16:19], a[52:55], a[172:175], v[16:19]\n"
    "s_waitcnt lgkmcnt(7)\n"
    "v_exp_f32 v50, v50\n"
    "v_mfma_f32_16x16x32_bf16 v[152:155], a[140:143], v[80:83], 0\n"
    "v_and_b32 v98, 0x80008000, v98\n"
    "v_mfma_f32_16x16x32_bf16 v[24:27], a[72:75], a[216:219], v[24:27]\n"
    "s_add_u32 m0, s70, 4096\n"
    "s_add_u32 s83, s67, 28672\n"
    "s_waitcnt lgkmcnt(2)\n"
    "v_mfma_f32_16x16x32_bf16 v[128:131], a[124:127], v[72:75], 0\n"
    "ds_read_b128 a[148:151], v249 offset:16384\n"
    "v_mfma_f32_16x16x32_bf16 v[148:151], a[132:135], v[80:83], 0\n"
    "s_add_u32 s76, s52, s60\n"
    "v_mfma_f32_16x16x32_bf16 v[28:31], a[92:95], a[164:167], v[28:31]\n"
    "buffer_load_dwordx4 v1, s[16:19], s83 offen offset:0 sc1 lds\n"
    "v_mfma_f32_16x16x32_bf16 v[160:163], a[124:127], v[88:91], 0\n"
    "s_waitcnt lgkmcnt(2)\n"
    "v_exp_f32 v48, v48\n"
    "v_mfma_f32_16x16x32_bf16 v[136:139], a[136:139], v[68:71], 0\n"
    "v_exp_f32 v49, v49\n"
    "v_mfma_f32_16x16x32_bf16 a[32:35], a[144:147], v[92:95], 0\n"
    "v_exp_f32 v27, v27\n"
    "v_mfma_f32_16x16x32_bf16 v[136:139], a[140:143], v[72:75], v[136:139]\n"
    "v_cmp_ne_u32 s[34:35], 0, v98\n"
    "v_mfma_f32_16x16x32_bf16 v[156:159], a[112:115], v[84:87], 0\n"
    "v_mfma_f32_16x16x32_bf16 v[132:135], a[128:131], v[68:71], 0\n"
    "v_cvt_pk_bf16_f32 v96, v48, v49\n"
    "v_mfma_f32_16x16x32_bf16 v[144:147], a[120:123], v[76:79], 0\n"
    "buffer_load_dwordx4 v1, s[16:19], s83 offen offset:1024 sc1 lds\n"
    "v_mfma_f32_16x16x32_bf16 v[16:19], a[48:51], a[168:171], v[16:19]\n"
    "s_waitcnt lgkmcnt(0)\n"
    "v_exp_f32 v20, v20\n"
    "v_mfma_f32_16x16x32_bf16 a[16:19], a[148:151], v[88:91], 0\n"
    "v_exp_f32 v64, v64\n"
    "v_mfma_f32_16x16x32_bf16 v[176:179], a[120:123], v[92:95], 0\n"
    "v_mfma_f32_16x16x32_bf16 v[124:127], a[112:115], v[68:71], 0\n"
    "v_exp_f32 v4, v4\n"
    "v_mfma_f32_16x16x32_bf16 v[156:159], a[116:119], v[88:91], v[156:159]\n"
    "v_exp_f32 v7, v7\n"
    "v_mfma_f32_16x16x32_bf16 v[188:191], a[148:151], v[72:75], 0\n"
    "v_exp_f32 v10, v10\n"
    "v_mfma_f32_16x16x32_bf16 v[32:35], a[76:79], a[172:175], v[32:35]\n"
    "v_mfma_f32_16x16x32_bf16 v[192:195], a[152:155], v[68:71], 0\n"
    "s_add_u32 s78, s52, s62\n"
    "v_exp_f32 v11, v11\n"
    "v_mfma_f32_16x16x32_bf16 v[124:127], a[116:119], v[72:75], v[124:127]\n"
    "s_waitcnt vmcnt(12)\n"
    "v_cvt_pk_bf16_f32 v97, v50, v51\n"
    "v_cvt_pk_bf16_f32 v99, v66, v67\n"
    "v_mfma_f32_16x16x32_bf16 v[164:167], a[128:131], v[84:87], 0\n"
    "v_exp_f32 v19, v19\n"
    "v_mfma_f32_16x16x32_bf16 v[160:163], a[120:123], v[84:87], v[160:163]\n"
    "v_cvt_pk_u16_u32 v204, v204, v208\n"
    "v_cvt_pk_bf16_f32 v98, v64, v65\n"
    "v_mfma_f32_16x16x32_bf16 a[20:23], a[152:155], v[84:87], 0\n"
    "v_cvt_pk_u16_u32 v214, v214, v218\n"
    "v_mfma_f32_16x16x32_bf16 v[148:151], a[128:131], v[76:79], v[148:151]\n"
    "v_cvt_pk_u16_u32 v207, v207, v211\n"
    "v_cvt_pk_u16_u32 v205, v205, v209\n"
    "v_mfma_f32_16x16x32_bf16 v[128:131], a[120:123], v[68:71], v[128:131]\n"
    "ds_read_b128 a[156:159], v249 offset:17408\n"
    "v_mfma_f32_16x16x32_bf16 v[172:175], a[116:119], v[96:99], 0\n"
    "s_add_u32 s77, s52, s61\n"
    "v_exp_f32 v25, v25\n"
    "v_mfma_f32_16x16x32_bf16 v[132:135], a[132:135], v[72:75], v[132:135]\n"
    "v_mfma_f32_16x16x32_bf16 v[144:147], a[124:127], v[80:83], v[144:147]\n"
    "v_mfma_f32_16x16x32_bf16 v[180:183], a[132:135], v[96:99], 0\n"
    "v_exp_f32 v17, v17\n"
    "v_mfma_f32_16x16x32_bf16 v[168:171], a[136:139], v[84:87], 0\n"
    "v_exp_f32 v9, v9\n"
    "v_mfma_f32_16x16x32_bf16 v[164:167], a[132:135], v[88:91], v[164:167]\n"
    "v_mfma_f32_16x16x32_bf16 v[152:155], a[136:139], v[76:79], v[152:155]\n"
    "v_exp_f32 v18, v18\n"
    "v_mfma_f32_16x16x32_bf16 v[176:179], a[124:127], v[96:99], v[176:179]\n"
    "ds_read_b128 a[132:135], v249 offset:20480\n"
    "v_mfma_f32_16x16x32_bf16 v[140:143], a[116:119], v[80:83], 0\n"
    "v_exp_f32 v31, v31\n"
    "v_mfma_f32_16x16x32_bf16 v[188:191], a[144:147], v[68:71], v[188:191]\n"
    "v_mfma_f32_16x16x32_bf16 v[168:171], a[140:143], v[88:91], v[168:171]\n"
    "v_exp_f32 v12, v12\n"
    "v_mfma_f32_16x16x32_bf16 v[180:183], a[128:131], v[92:95], v[180:183]\n"
    "v_cvt_pk_u16_u32 v212, v212, v216\n"
    "v_cvt_pk_u16_u32 v223, v223, v227\n"
    "v_mfma_f32_16x16x32_bf16 v[140:143], a[112:115], v[76:79], v[140:143]\n"
    "s_waitcnt lgkmcnt(1)\n"
    "v_exp_f32 v21, v21\n"
    "v_mfma_f32_16x16x32_bf16 a[36:39], a[156:159], v[96:99], 0\n"
    "ds_read_b128 a[128:131], v249 offset:8192\n"
    "v_cvt_pk_u16_u32 v222, v222, v226\n"
    "v_mfma_f32_16x16x32_bf16 v[172:175], a[112:115], v[92:95], v[172:175]\n"
    "v_or3_b32 v243, v204, v205, v243\n"
    "s_waitcnt lgkmcnt(1)\n"
    "v_mfma_f32_16x16x32_bf16 a[96:99], a[132:135], v[96:99], 0\n"
    "v_mfma_f32_16x16x32_bf16 a[20:23], a[156:159], v[88:91], a[20:23]\n"
    "ds_read_b128 a[116:119], v249 offset:18432\n"
    "v_mfma_f32_16x16x32_bf16 a[16:19], a[144:147], v[84:87], a[16:19]\n"
    "v_exp_f32 v15, v15\n"
    "v_mfma_f32_16x16x32_bf16 a[0:3], a[144:147], v[76:79], 0\n"
    "ds_read_b128 a[120:123], v249 offset:7168\n"
    "v_mfma_f32_16x16x32_bf16 v[184:187], a[140:143], v[96:99], 0\n"
    "v_exp_f32 v26, v26\n"
    "v_mfma_f32_16x16x32_bf16 a[64:67], a[132:135], v[80:83], 0\n"
    "v_cvt_pk_u16_u32 v206, v206, v210\n"
    "v_mfma_f32_16x16x32_bf16 a[36:39], a[152:155], v[92:95], a[36:39]\n"
    "ds_read_b128 a[124:127], v249 offset:19456\n"
    "ds_read_b128 a[140:143], v249 offset:21504\n"
    "v_mfma_f32_16x16x32_bf16 a[0:3], a[148:151], v[80:83], a[0:3]\n"
    "v_cvt_pk_u16_u32 v220, v220, v224\n"
    "v_cvt_pk_u16_u32 v221, v221, v225\n"
    "v_mfma_f32_16x16x32_bf16 v[184:187], a[136:139], v[92:95], v[184:187]\n"
    "s_waitcnt lgkmcnt(4)\n"
    "v_or3_b32 v243, v206, v207, v243\n"
    "v_cvt_pk_u16_u32 v213, v213, v217\n"
    "v_mfma_f32_16x16x32_bf16 a[80:83], a[128:131], v[84:87], 0\n"
    "s_add_u32 s67, s67, 49152\n"
    "ds_read_b128 a[136:139], v249 offset:9216\n"
    "ds_read_b128 a[144:147], v249 offset:10240\n"
    "v_mfma_f32_16x16x32_bf16 a[64:67], a[128:131], v[76:79], a[64:67]\n"
    "v_exp_f32 v23, v23\n"
    "v_mfma_f32_16x16x32_bf16 a[32:35], a[148:151], v[96:99], a[32:35]\n"
    "v_mfma_f32_16x16x32_bf16 a[4:7], a[156:159], v[80:83], 0\n"
    "v_mfma_f32_16x16x32_bf16 a[4:7], a[152:155], v[76:79], a[4:7]\n"
    "v_or3_b32 v243, v212, v213, v243\n"
    "v_cvt_pk_u16_u32 v215, v215, v219\n"
    "v_mfma_f32_16x16x32_bf16 v[192:195], a[156:159], v[72:75], v[192:195]\n"
    "v_exp_f32 v28, v28\n"
    "v_mfma_f32_16x16x32_bf16 a[96:99], a[128:131], v[92:95], a[96:99]\n"
    "s_waitcnt lgkmcnt(3)\n"
    "ds_read_b128 a[112:115], v249 offset:6144\n"
    "ds_read_b128 a[148:151], v249 offset:22528\n"
    "v_mfma_f32_16x16x32_bf16 v[200:203], a[124:127], v[72:75], 0\n"
    "v_exp_f32 v16, v16\n"
    "v_mfma_f32_16x16x32_bf16 a[12:15], a[120:123], v[76:79], 0\n"
    "buffer_load_dwordx2 v[236:237], v241, s[36:39], s81 offen offset:0 sc1\n"
    "v_mfma_f32_16x16x32_bf16 v[200:203], a[120:123], v[68:71], v[200:203]\n"
    "s_waitcnt lgkmcnt(4)\n"
    "v_add_u32 v251, 1536, v251\n"
    "v_mfma_f32_16x16x32_bf16 a[100:103], a[140:143], v[96:99], 0\n"
    "v_exp_f32 v29, v29\n"
    "v_mfma_f32_16x16x32_bf16 a[28:31], a[124:127], v[88:91], 0\n"
    "buffer_store_dword v204, v244, s[20:23], s76 offen offset:0 sc1\n"
    "v_exp_f32 v30, v30\n"
    "v_mfma_f32_16x16x32_bf16 v[32:35], a[72:75], a[168:171], v[32:35]\n"
    "v_mfma_f32_16x16x32_bf16 a[40:43], a[116:119], v[96:99], 0\n"
    "v_exp_f32 v13, v13\n"
    "v_mfma_f32_16x16x32_bf16 a[68:71], a[140:143], v[80:83], 0\n"
    "s_waitcnt lgkmcnt(3)\n"
    "v_mfma_f32_16x16x32_bf16 a[52:55], a[136:139], v[68:71], 0\n"
    "buffer_store_dword v205, v244, s[20:23], s76 offen offset:1024 sc1\n"
    "v_exp_f32 v22, v22\n"
    "v_mfma_f32_16x16x32_bf16 a[84:87], a[136:139], v[84:87], 0\n"
    "v_exp_f32 v24, v24\n"
    "v_mfma_f32_16x16x32_bf16 a[12:15], a[124:127], v[80:83], a[12:15]\n"
    "ds_read_b128 a[156:159], v249 offset:23552\n"
    "v_mfma_f32_16x16x32_bf16 a[44:47], a[120:123], v[92:95], 0\n"
    "v_exp_f32 v35, v35\n"
    "v_mfma_f32_16x16x32_bf16 a[80:83], a[132:135], v[88:91], a[80:83]\n"
    "buffer_store_dword v206, v244, s[20:23], s76 offen offset:2048 sc1\n"
    "ds_read_b128 v[100:103], v250 offset:1664\n"
    "v_mfma_f32_16x16x32_bf16 a[84:87], a[140:143], v[88:91], a[84:87]\n"
    "v_mfma_f32_16x16x32_bf16 a[8:11], a[116:119], v[80:83], 0\n"
    "s_waitcnt lgkmcnt(2)\n"
    "buffer_store_dword v207, v244, s[20:23], s76 offen offset:3072 sc1\n"
    "v_or3_b32 v243, v214, v215, v243\n"
    "v_or3_b32 v243, v220, v221, v243\n"
    "v_mfma_f32_16x16x32_bf16 a[88:91], a[148:151], v[88:91], 0\n"
    "v_mfma_f32_16x16x32_bf16 a[44:47], a[124:127], v[96:99], a[44:47]\n"
    "buffer_store_dword v212, v244, s[20:23], s77 offen offset:0 sc1\n"
    "v_mfma_f32_16x16x32_bf16 a[28:31], a[120:123], v[84:87], a[28:31]\n"
    "v_mfma_f32_16x16x32_bf16 a[24:27], a[112:115], v[84:87], 0\n"
    "ds_read_b128 a[152:155], v249 offset:11264\n"
    "v_mfma_f32_16x16x32_bf16 a[40:43], a[112:115], v[92:95], a[40:43]\n"
    "buffer_store_dword v213, v244, s[20:23], s77 offen offset:1024 sc1\n"
    "v_exp_f32 v6, v6\n"
    "v_mfma_f32_16x16x32_bf16 a[48:51], a[128:131], v[68:71], 0\n"
    "s_waitcnt lgkmcnt(2)\n"
    "v_exp_f32 v8, v8\n"
    "v_mfma_f32_16x16x32_bf16 a[76:79], a[156:159], v[80:83], 0\n"
    "buffer_store_dword v214, v244, s[20:23], s77 offen offset:2048 sc1\n"
    "v_mfma_f32_16x16x32_bf16 a[104:107], a[144:147], v[92:95], 0\n"
    "v_exp_f32 v34, v34\n"
    "v_mfma_f32_16x16x32_bf16 a[52:55], a[140:143], v[72:75], a[52:55]\n"
    "buffer_store_dword v215, v244, s[20:23], s77 offen offset:3072 sc1\n"
    "v_exp_f32 v14, v14\n"
    "v_mfma_f32_16x16x32_bf16 a[48:51], a[132:135], v[72:75], a[48:51]\n"
    "v_mfma_f32_16x16x32_bf16 a[24:27], a[116:119], v[88:91], a[24:27]\n"
    "buffer_store_dword v220, v244, s[20:23], s78 offen offset:0 sc1\n"
    "ds_read_b128 a[120:123], v3 offset:16384\n"
    "v_mfma_f32_16x16x32_bf16 v[196:199], a[112:115], v[68:71], 0\n"
    "v_mfma_f32_16x16x32_bf16 v[196:199], a[116:119], v[72:75], v[196:199]\n"
    "v_mfma_f32_16x16x32_bf16 a[8:11], a[112:115], v[76:79], a[8:11]\n"
    "s_waitcnt lgkmcnt(1)\n"
    "ds_read_b128 a[132:135], v3 offset:22528\n"
    "v_mfma_f32_16x16x32_bf16 a[76:79], a[152:155], v[76:79], a[76:79]\n"
    "buffer_store_dword v221, v244, s[20:23], s78 offen offset:1024 sc1\n"
    "v_mfma_f32_16x16x32_bf16 a[108:111], a[156:159], v[96:99], 0\n"
    "ds_read_b128 a[124:127], v3 offset:18432\n"
    "ds_read_b128 a[128:131], v3 offset:20480\n"
    "v_mfma_f32_16x16x32_bf16 a[92:95], a[152:155], v[84:87], 0\n"
    "v_or3_b32 v243, v222, v223, v243\n"
    "v_mfma_f32_16x16x32_bf16 a[92:95], a[156:159], v[88:91], a[92:95]\n"
    "ds_read_b128 a[112:115], v3 offset:12288\n"
    "ds_read_b128 a[116:119], v3 offset:14336\n"
    "v_mfma_f32_16x16x32_bf16 a[72:75], a[144:147], v[76:79], 0\n"
    "s_waitcnt vmcnt(11)\n"
    "ds_add_u32 v247, v246\n"
    "v_mfma_f32_16x16x32_bf16 a[56:59], a[148:151], v[72:75], 0\n"
    "v_exp_f32 v32, v32\n"
    "v_mfma_f32_16x16x32_bf16 a[60:63], a[152:155], v[68:71], 0\n"
    "v_and_b32 v208, 0xff00ff00, v243\n"
    "v_add_u32 v250, 1536, v250\n"
    "v_mfma_f32_16x16x32_bf16 a[68:71], a[136:139], v[76:79], a[68:71]\n"
    "v_exp_f32 v33, v33\n"
    "v_mfma_f32_16x16x32_bf16 a[88:91], a[144:147], v[84:87], a[88:91]\n"
    "buffer_store_dword v222, v244, s[20:23], s78 offen offset:2048 sc1\n"
    "v_exp_f32 v5, v5\n"
    "v_mfma_f32_16x16x32_bf16 a[72:75], a[148:151], v[80:83], a[72:75]\n"
    "v_cmp_ne_u32 vcc, 0, v208\n"
    "v_mfma_f32_16x16x32_bf16 a[104:107], a[148:151], v[96:99], a[104:107]\n"
    "v_mfma_f32_16x16x32_bf16 a[108:111], a[152:155], v[92:95], a[108:111]\n"
    "buffer_store_dword v223, v244, s[20:23], s78 offen offset:3072 sc1\n"
    "v_add_u32 v3, 49152, v252\n"
    "v_mfma_f32_16x16x32_bf16 a[56:59], a[144:147], v[68:71], a[56:59]\n"
    "s_waitcnt lgkmcnt(1)\n"
    "v_cndmask_b32 v208, 2.0, v241, vcc\n"
    "v_mfma_f32_16x16x32_bf16 a[60:63], a[156:159], v[72:75], a[60:63]\n"
    "v_mfma_f32_16x16x32_bf16 a[100:103], a[136:139], v[92:95], a[100:103]\n"
    "buffer_store_dwordx2 v[238:239], v208, s[36:39], 0 offen offset:0 sc1\n"
    "v_smfmac_f32_16x16x64_bf16 v[120:123], v[52:55], v[92:99], v253\n"
    "s_branch .LPV\n"
    ".fill ((0x20000 - (.LwKe - .LwK) - (. - _Z8attn_fwdPKtPKiS2_S0_S0_S0_S0_PcPt)) & 4095) / 4, 4, 0xBF800000\n"
    ".LwK:\n"
    "s_branch .LwKe\n"
    ".LPK:\n"
    "v_mfma_f32_16x16x32_bf16 v[36:39], a[112:115], a[192:195], v[100:103]\n"
    "s_add_u32 s71, s71, 98304\n"
    "ds_read_b128 a[136:139], v2 offset:13312\n"
    "v_cvt_pk_bf16_f32 v68, v4, v5\n"
    "v_mfma_f32_16x16x32_bf16 v[36:39], a[116:119], a[196:199], v[36:39]\n"
    "s_add_u32 s75, s71, s63\n"
    "ds_read_b128 a[140:143], v2 offset:15360\n"
    "v_cvt_pk_bf16_f32 v69, v6, v7\n"
    "v_mfma_f32_16x16x32_bf16 v[36:39], a[120:123], a[200:203], v[36:39]\n"
    "s_add_u32 s76, s75, 6144\n"
    "ds_read_b128 a[144:147], v2 offset:17408\n"
    "v_cvt_pk_bf16_f32 v76, v8, v9\n"
    "s_nop 0\n"
    "v_mfma_f32_16x16x32_bf16 v[36:39], a[124:127], a[204:207], v[36:39]\n"
    "s_add_u32 s77, s75, 24576\n"
    "ds_read_b128 a[148:151], v2 offset:19456\n"
    "v_cvt_pk_bf16_f32 v77, v10, v11\n"
    "v_mfma_f32_16x16x32_bf16 v[36:39], a[128:131], a[208:211], v[36:39]\n"
    "s_add_u32 s78, s75, 30720\n"
    "ds_read_b128 a[152:155], v2 offset:21504\n"
    "v_cvt_pk_bf16_f32 v84, v12, v13\n"
    "v_mfma_f32_16x16x32_bf16 v[36:39], a[132:135], a[212:215], v[36:39]\n"
    "ds_read_b128 a[156:159], v2 offset:23552\n"
    "v_cvt_pk_bf16_f32 v85, v14, v15\n"
    "v_mfma_f32_16x16x32_bf16 v[40:43], a[132:135], a[236:239], v[100:103]\n"
    "ds_read_b128 v[104:107], v250 offset:144\n"
    "v_cvt_pk_bf16_f32 v92, v16, v17\n"
    "v_mfma_f32_16x16x32_bf16 v[40:43], a[128:131], a[232:235], v[40:43]\n"
    "s_add_u32 s68, s68, 24576\n"
    "v_cvt_pk_bf16_f32 v93, v18, v19\n"
    "v_cvt_pk_bf16_f32 v70, v20, v21\n"
    "v_mfma_f32_16x16x32_bf16 v[40:43], a[124:127], a[228:231], v[40:43]\n"
    "s_cmp_ge_u32 s68, s69\n"
    "s_cselect_b32 s68, s65, s68\n"
    "v_cvt_pk_bf16_f32 v71, v22, v23\n"
    "v_mfma_f32_16x16x32_bf16 v[40:43], a[120:123], a[224:227], v[40:43]\n"
    "v_cvt_pk_bf16_f32 v78, v24, v25\n"
    "v_cvt_pk_bf16_f32 v79, v26, v27\n"
    "v_mfma_f32_16x16x32_bf16 v[40:43], a[116:119], a[220:223], v[40:43]\n"
    "v_cvt_pk_bf16_f32 v86, v28, v29\n"
    "v_cvt_pk_bf16_f32 v87, v30, v31\n"
    "v_mfma_f32_16x16x32_bf16 v[40:43], a[112:115], a[216:219], v[40:43]\n"
    "v_cvt_pk_bf16_f32 v94, v32, v33\n"
    "v_cvt_pk_bf16_f32 v95, v34, v35\n"
    "v_mfma_f32_16x16x32_bf16 v[44:47], a[112:115], a[240:243], v[100:103]\n"
    "v_exp_f32 v36, v36\n"
    "v_mfma_f32_16x16x32_bf16 v[44:47], a[116:119], a[244:247], v[44:47]\n"
    "v_exp_f32 v37, v37\n"
    "v_mfma_f32_16x16x32_bf16 v[44:47], a[120:123], a[248:251], v[44:47]\n"
    "v_exp_f32 v38, v38\n"
    "v_mfma_f32_16x16x32_bf16 v[44:47], a[124:127], a[252:255], v[44:47]\n"
    "v_exp_f32 v39, v39\n"
    "v_mfma_f32_16x16x32_bf16 v[44:47], a[128:131], a[160:163], v[44:47]\n"
    "v_cvt_pk_bf16_f32 v72, v36, v37\n"
    "v_cvt_pk_bf16_f32 v73, v38, v39\n"
    "v_mfma_f32_16x16x32_bf16 v[44:47], a[132:135], a[164:167], v[44:47]\n"
    "v_exp_f32 v40, v40\n"
    "v_mfma_f32_16x16x32_bf16 v[48:51], a[132:135], a[188:191], v[100:103]\n"
    "ds_read_b32 v245, v247\n"
    "v_mfma_f32_16x16x32_bf16 v[48:51], a[128:131], a[184:187], v[48:51]\n"
    "s_add_u32 m0, s68, 0\n"
    "v_exp_f32 v41, v41\n"
    "v_mfma_f32_16x16x32_bf16 v[48:51], a[124:127], a[180:183], v[48:51]\n"
    "v_exp_f32 v42, v42\n"
    "v_mfma_f32_16x16x32_bf16 v[48:51], a[120:123], a[176:179], v[48:51]\n"
    "v_exp_f32 v43, v43\n"
    "v_mfma_f32_16x16x32_bf16 v[48:51], a[116:119], a[172:175], v[48:51]\n"
    "s_waitcnt vmcnt(10)\n"
    "v_cmp_eq_u64 s[54:55], v[236:237], v[238:239]\n"
    "v_cvt_pk_bf16_f32 v80, v40, v41\n"
    "v_mfma_f32_16x16x32_bf16 v[48:51], a[112:115], a[168:171], v[48:51]\n"
    "s_waitcnt lgkmcnt(1)\n"
    "v_cvt_pk_bf16_f32 v81, v42, v43\n"
    "v_mfma_f32_16x16x32_bf16 v[52:55], a[136:139], a[192:195], v[104:107]\n"
    "s_waitcnt lgkmcnt(0)\n"
    "v_cmp_le_u32 s[56:57], s59, v245\n"
    "v_mfma_f32_16x16x32_bf16 v[52:55], a[140:143], a[196:199], v[52:55]\n"
    "v_exp_f32 v44, v44\n"
    "v_mfma_f32_16x16x32_bf16 v[52:55], a[144:147], a[200:203], v[52:55]\n"
    "s_and_b64 s[54:55], s[54:55], s[56:57]\n"
    "s_cmp_eq_u64 s[54:55], -1\n"
    "v_mfma_f32_16x16x32_bf16 v[52:55], a[148:151], a[204:207], v[52:55]\n"
    "s_cbranch_scc0 .Lpoll1\n"
    ".Lpoll1r:\n"
    "v_mfma_f32_16x16x32_bf16 v[52:55], a[152:155], a[208:211], v[52:55]\n"
    "buffer_load_dwordx4 v1, s[16:19], s67 offen offset:0 sc1 lds\n"
    "v_mfma_f32_16x16x32_bf16 v[52:55], a[156:159], a[212:215], v[52:55]\n"
    "ds_read_b128 a[112:115], v3 offset:0\n"
    "v_mfma_f32_16x16x32_bf16 v[56:59], a[156:159], a[236:239], v[104:107]\n"
    "ds_read_b128 a[116:119], v3 offset:2048\n"
    "v_mfma_f32_16x16x32_bf16 v[56:59], a[152:155], a[232:235], v[56:59]\n"
    "ds_read_b128 a[120:123], v3 offset:4096\n"
    "v_mfma_f32_16x16x32_bf16 v[56:59], a[148:151], a[228:231], v[56:59]\n"
    "buffer_load_dwordx4 v1, s[16:19], s67 offen offset:1024 sc1 lds\n"
    "v_mfma_f32_16x16x32_bf16 v[56:59], a[144:147], a[224:227], v[56:59]\n"
    "ds_read_b128 a[124:127], v3 offset:6144\n"
    "v_mfma_f32_16x16x32_bf16 v[56:59], a[140:143], a[220:223], v[56:59]\n"
    "ds_read_b128 a[128:131], v3 offset:8192\n"
    "v_mfma_f32_16x16x32_bf16 v[56:59], a[136:139], a[216:219], v[56:59]\n"
    "ds_read_b128 a[132:135], v3 offset:10240\n"
    "v_mfma_f32_16x16x32_bf16 v[60:63], a[136:139], a[240:243], v[104:107]\n"
    "buffer_load_dwordx4 v1, s[16:19], s67 offen offset:2048 sc1 lds\n"
    "v_mfma_f32_16x16x32_bf16 v[60:63], a[140:143], a[244:247], v[60:63]\n"
    "ds_read_b128 v[100:103], v250 offset:768\n"
    "v_mfma_f32_16x16x32_bf16 v[60:63], a[144:147], a[248:251], v[60:63]\n"
    "v_exp_f32 v52, v52\n"
    "v_mfma_f32_16x16x32_bf16 v[60:63], a[148:151], a[252:255], v[60:63]\n"
    "v_exp_f32 v53, v53\n"
    "v_mfma_f32_16x16x32_bf16 v[60:63], a[152:155], a[160:163], v[60:63]\n"
    "buffer_load_dwordx4 v1, s[16:19], s67 offen offset:3072 sc1 lds\n"
    "v_mfma_f32_16x16x32_bf16 v[60:63], a[156:159], a[164:167], v[60:63]\n"
    "s_add_u32 m0, s68, 4096\n"
    "v_exp_f32 v54, v54\n"
    "v_mfma_f32_16x16x32_bf16 v[64:67], a[156:159], a[188:191], v[104:107]\n"
    "s_add_u32 s83, s67, 4096\n"
    "v_exp_f32 v55, v55\n"
    "v_mfma_f32_16x16x32_bf16 v[64:67], a[152:155], a[184:187], v[64:67]\n"
    "v_cvt_pk_bf16_f32 v74, v52, v53\n"
    "v_cvt_pk_bf16_f32 v75, v54, v55\n"
    "v_mfma_f32_16x16x32_bf16 v[64:67], a[148:151], a[180:183], v[64:67]\n"
    "buffer_load_dwordx4 v1, s[16:19], s83 offen offset:0 sc1 lds\n"
    "v_mfma_f32_16x16x32_bf16 v[64:67], a[144:147], a[176:179], v[64:67]\n"
    "v_exp_f32 v56, v56\n"
    "v_mfma_f32_16x16x32_bf16 v[64:67], a[140:143], a[172:175], v[64:67]\n"
    "v_exp_f32 v57, v57\n"
    "v_mfma_f32_16x16x32_bf16 v[64:67], a[136:139], a[168:171], v[64:67]\n"
    "s_waitcnt lgkmcnt(0)\n"
    "v_exp_f32 v58, v58\n"
    "v_mfma_f32_16x16x32_bf16 v[4:7], a[112:115], a[192:195], v[100:103]\n"
    "buffer_load_dwordx4 v1, s[16:19], s83 offen offset:1024 sc1 lds\n"
    "v_mfma_f32_16x16x32_bf16 v[4:7], a[116:119], a[196:199], v[4:7]\n"
    "s_add_u32 m0, s64, 0\n"
    "ds_read_b128 a[136:139], v3 offset:1024\n"
    "v_cvt_pk_bf16_f32 v82, v56, v57\n"
    "v_mfma_f32_16x16x32_bf16 v[4:7], a[120:123], a[200:203], v[4:7]\n"
    "s_add_u32 s83, s67, 24576\n"
    "ds_read_b128 a[140:143], v3 offset:3072\n"
    "v_mfma_f32_16x16x32_bf16 v[4:7], a[124:127], a[204:207], v[4:7]\n"
    "ds_read_b128 a[144:147], v3 offset:5120\n"
    "v_mfma_f32_16x16x32_bf16 v[4:7], a[128:131], a[208:211], v[4:7]\n"
    "buffer_load_dwordx4 v1, s[16:19], s83 offen offset:0 sc1 lds\n"
    "v_mfma_f32_16x16x32_bf16 v[4:7], a[132:135], a[212:215], v[4:7]\n"
    "ds_read_b128 a[148:151], v3 offset:7168\n"
    "v_mfma_f32_16x16x32_bf16 v[8:11], a[132:135], a[236:239], v[100:103]\n"
    "ds_read_b128 a[152:155], v3 offset:9216\n"
    "v_mfma_f32_16x16x32_bf16 v[8:11], a[128:131], a[232:235], v[8:11]\n"
    "ds_read_b128 a[156:159], v3 offset:11264\n"
    "v_mfma_f32_16x16x32_bf16 v[8:11], a[124:127], a[228:231], v[8:11]\n"
    "buffer_load_dwordx4 v1, s[16:19], s83 offen offset:1024 sc1 lds\n"
    "v_mfma_f32_16x16x32_bf16 v[8:11], a[120:123], a[224:227], v[8:11]\n"
    "ds_read_b128 v[104:107], v250 offset:784\n"
    "v_mfma_f32_16x16x32_bf16 v[8:11], a[116:119], a[220:223], v[8:11]\n"
    "v_exp_f32 v59, v59\n"
    "v_mfma_f32_16x16x32_bf16 v[8:11], a[112:115], a[216:219], v[8:11]\n"
    "v_cvt_pk_bf16_f32 v83, v58, v59\n"
    "v_mfma_f32_16x16x32_bf16 v[12:15], a[112:115], a[240:243], v[100:103]\n"
    "buffer_load_dwordx4 v1, s[16:19], s83 offen offset:2048 sc1 lds\n"
    "v_mfma_f32_16x16x32_bf16 v[12:15], a[116:119], a[244:247], v[12:15]\n"
    "v_exp_f32 v45, v45\n"
    "v_mfma_f32_16x16x32_bf16 v[12:15], a[120:123], a[248:251], v[12:15]\n"
    "v_exp_f32 v46, v46\n"
    "v_mfma_f32_16x16x32_bf16 v[12:15], a[124:127], a[252:255], v[12:15]\n"
    "v_exp_f32 v47, v47\n"
    "v_mfma_f32_16x16x32_bf16 v[12:15], a[128:131], a[160:163], v[12:15]\n"
    "buffer_load_dwordx4 v1, s[16:19], s83 offen offset:3072 sc1 lds\n"
    "v_mfma_f32_16x16x32_bf16 v[12:15], a[132:135], a[164:167], v[12:15]\n"
    "s_add_u32 m0, s64, 4096\n"
    "v_exp_f32 v60, v60\n"
    "v_mfma_f32_16x16x32_bf16 v[16:19], a[132:135], a[188:191], v[100:103]\n"
    "s_add_u32 s83, s67, 28672\n"
    "v_exp_f32 v61, v61\n"
    "v_mfma_f32_16x16x32_bf16 v[16:19], a[128:131], a[184:187], v[16:19]\n"
    "v_exp_f32 v62, v62\n"
    "v_mfma_f32_16x16x32_bf16 v[16:19], a[124:127], a[180:183], v[16:19]\n"
    "buffer_load_dwordx4 v1, s[16:19], s83 offen offset:0 sc1 lds\n"
    "v_mfma_f32_16x16x32_bf16 v[16:19], a[120:123], a[176:179], v[16:19]\n"
    "v_exp_f32 v63, v63\n"
    "v_mfma_f32_16x16x32_bf16 v[16:19], a[116:119], a[172:175], v[16:19]\n"
    "v_cvt_pk_bf16_f32 v88, v44, v45\n"
    "v_cvt_pk_bf16_f32 v89, v46, v47\n"
    "v_mfma_f32_16x16x32_bf16 v[16:19], a[112:115], a[168:171], v[16:19]\n"
    "s_waitcnt lgkmcnt(0)\n"
    "v_cvt_pk_bf16_f32 v90, v60, v61\n"
    "v_cvt_pk_bf16_f32 v91, v62, v63\n"
    "v_mfma_f32_16x16x32_bf16 v[20:23], a[136:139], a[192:195], v[104:107]\n"
    "buffer_load_dwordx4 v1, s[16:19], s83 offen offset:1024 sc1 lds\n"
    "v_mfma_f32_16x16x32_bf16 v[20:23], a[140:143], a[196:199], v[20:23]\n"
    "s_add_u32 s67, s67, 49152\n"
    "ds_read_b128 a[112:115], v248 offset:0\n"
    "v_mfma_f32_16x16x32_bf16 v[20:23], a[144:147], a[200:203], v[20:23]\n"
    "s_add_u32 s59, s59, 4\n"
    "ds_read_b128 a[116:119], v248 offset:12288\n"
    "v_mfma_f32_16x16x32_bf16 v[20:23], a[148:151], a[204:207], v[20:23]\n"
    "s_waitcnt vmcnt(12)\n"
    "ds_read_b128 a[120:123], v248 offset:1024\n"
    "v_mfma_f32_16x16x32_bf16 v[20:23], a[152:155], a[208:211], v[20:23]\n"
    "buffer_store_dwordx2 v[238:239], v240, s[36:39], s80 offen offset:0 sc1\n"
    "v_mfma_f32_16x16x32_bf16 v[20:23], a[156:159], a[212:215], v[20:23]\n"
    "buffer_load_dwordx2 v[204:205], v242, s[28:31], s75 offen offset:0 nt\n"
    "v_mfma_f32_16x16x32_bf16 v[24:27], a[156:159], a[236:239], v[104:107]\n"
    "s_add_u32 s80, s80, 256\n"
    "ds_read_b128 a[124:127], v248 offset:13312\n"
    "v_mfma_f32_16x16x32_bf16 v[24:27], a[152:155], a[232:235], v[24:27]\n"
    "buffer_load_dwordx2 v[206:207], v242, s[28:31], s75 offen offset:768 nt\n"
    "v_mfma_f32_16x16x32_bf16 v[24:27], a[148:151], a[228:231], v[24:27]\n"
    "ds_read_b128 a[128:131], v248 offset:2048\n"
    "v_mfma_f32_16x16x32_bf16 v[24:27], a[144:147], a[224:227], v[24:27]\n"
    "buffer_load_dwordx2 v[208:209], v242, s[28:31], s75 offen offset:3072 nt\n"
    "v_mfma_f32_16x16x32_bf16 v[24:27], a[140:143], a[220:223], v[24:27]\n"
    "ds_read_b128 a[132:135], v248 offset:14336\n"
    "v_mfma_f32_16x16x32_bf16 v[24:27], a[136:139], a[216:219], v[24:27]\n"
    "buffer_load_dwordx2 v[210:211], v242, s[28:31], s75 offen offset:3840 nt\n"
    "v_mfma_f32_16x16x32_bf16 v[28:31], a[136:139], a[240:243], v[104:107]\n"
    "v_exp_f32 v48, v48\n"
    "v_mfma_f32_16x16x32_bf16 v[28:31], a[140:143], a[244:247], v[28:31]\n"
    "buffer_load_dwordx2 v[212:213], v242, s[28:31], s76 offen offset:0 nt\n"
    "v_mfma_f32_16x16x32_bf16 v[28:31], a[144:147], a[248:251], v[28:31]\n"
    "v_exp_f32 v49, v49\n"
    "v_mfma_f32_16x16x32_bf16 v[28:31], a[148:151], a[252:255], v[28:31]\n"
    "buffer_load_dwordx2 v[214:215], v242, s[28:31], s76 offen offset:768 nt\n"
    "v_mfma_f32_16x16x32_bf16 v[28:31], a[152:155], a[160:163], v[28:31]\n"
    "v_exp_f32 v50, v50\n"
    "v_mfma_f32_16x16x32_bf16 v[28:31], a[156:159], a[164:167], v[28:31]\n"
    "buffer_load_dwordx2 v[216:217], v242, s[28:31], s76 offen offset:3072 nt\n"
    "v_mfma_f32_16x16x32_bf16 v[32:35], a[156:159], a[188:191], v[104:107]\n"
    "v_exp_f32 v51, v51\n"
    "v_mfma_f32_16x16x32_bf16 v[32:35], a[152:155], a[184:187], v[32:35]\n"
    "buffer_load_dwordx2 v[218:219], v242, s[28:31], s76 offen offset:3840 nt\n"
    "v_mfma_f32_16x16x32_bf16 v[32:35], a[148:151], a[180:183], v[32:35]\n"
    "v_exp_f32 v64, v64\n"
    "v_mfma_f32_16x16x32_bf16 v[32:35], a[144:147], a[176:179], v[32:35]\n"
    "buffer_load_dwordx2 v[220:221], v242, s[28:31], s77 offen offset:0 nt\n"
    "v_mfma_f32_16x16x32_bf16 v[32:35], a[140:143], a[172:175], v[32:35]\n"
    "v_exp_f32 v65, v65\n"
    "v_mfma_f32_16x16x32_bf16 v[32:35], a[136:139], a[168:171], v[32:35]\n"
    "s_waitcnt lgkmcnt(0)\n"
    "v_exp_f32 v66, v66\n"
    "v_mfma_f32_16x16x32_bf16 v[124:127], a[112:115], v[68:71], v[124:127]\n"
    "buffer_load_dwordx2 v[222:223], v242, s[28:31], s77 offen offset:768 nt\n"
    "v_mfma_f32_16x16x32_bf16 v[124:127], a[116:119], v[72:75], v[124:127]\n"
    "ds_read_b128 a[136:139], v248 offset:3072\n"
    "v_cvt_pk_bf16_f32 v96, v48, v49\n"
    "v_mfma_f32_16x16x32_bf16 v[128:131], a[124:127], v[72:75], v[128:131]\n"
    "buffer_load_dwordx2 v[224:225], v242, s[28:31], s77 offen offset:3072 nt\n"
    "v_mfma_f32_16x16x32_bf16 v[128:131], a[120:123], v[68:71], v[128:131]\n"
    "ds_read_b128 a[140:143], v248 offset:15360\n"
    "v_cvt_pk_bf16_f32 v97, v50, v51\n"
    "v_mfma_f32_16x16x32_bf16 v[132:135], a[128:131], v[68:71], v[132:135]\n"
    "buffer_load_dwordx2 v[226:227], v242, s[28:31], s77 offen offset:3840 nt\n"
    "v_mfma_f32_16x16x32_bf16 v[132:135], a[132:135], v[72:75], v[132:135]\n"
    "ds_read_b128 a[144:147], v248 offset:4096\n"
    "v_cvt_pk_bf16_f32 v98, v64, v65\n"
    "v_mfma_f32_16x16x32_bf16 v[148:151], a[132:135], v[80:83], v[148:151]\n"
    "buffer_load_dwordx2 v[228:229], v242, s[28:31], s78 offen offset:0 nt\n"
    "v_mfma_f32_16x16x32_bf16 v[148:151], a[128:131], v[76:79], v[148:151]\n"
    "ds_read_b128 a[148:151], v248 offset:16384\n"
    "v_mfma_f32_16x16x32_bf16 v[144:147], a[120:123], v[76:79], v[144:147]\n"
    "buffer_load_dwordx2 v[230:231], v242, s[28:31], s78 offen offset:768 nt\n"
    "v_mfma_f32_16x16x32_bf16 v[144:147], a[124:127], v[80:83], v[144:147]\n"
    "ds_read_b128 a[152:155], v248 offset:5120\n"
    "v_mfma_f32_16x16x32_bf16 v[140:143], a[116:119], v[80:83], v[140:143]\n"
    "buffer_load_dwordx2 v[232:233], v242, s[28:31], s78 offen offset:3072 nt\n"
    "v_mfma_f32_16x16x32_bf16 v[140:143], a[112:115], v[76:79], v[140:143]\n"
    "ds_read_b128 a[156:159], v248 offset:17408\n"
    "v_mfma_f32_16x16x32_bf16 v[156:159], a[112:115], v[84:87], v[156:159]\n"
    "buffer_load_dwordx2 v[234:235], v242, s[28:31], s78 offen offset:3840 nt\n"
    "v_mfma_f32_16x16x32_bf16 v[156:159], a[116:119], v[88:91], v[156:159]\n"
    "ds_read_b128 v[104:107], v251 offset:0\n"
    "v_mfma_f32_16x16x32_bf16 v[160:163], a[124:127], v[88:91], v[160:163]\n"
    "v_exp_f32 v67, v67\n"
    "v_mfma_f32_16x16x32_bf16 v[160:163], a[120:123], v[84:87], v[160:163]\n"
    "v_cvt_pk_bf16_f32 v99, v66, v67\n"
    "v_mfma_f32_16x16x32_bf16 v[164:167], a[128:131], v[84:87], v[164:167]\n"
    "v_exp_f32 v4, v4\n"
    "v_mfma_f32_16x16x32_bf16 v[164:167], a[132:135], v[88:91], v[164:167]\n"
    "v_exp_f32 v5, v5\n"
    "v_mfma_f32_16x16x32_bf16 v[180:183], a[132:135], v[96:99], v[180:183]\n"
    "v_exp_f32 v6, v6\n"
    "v_mfma_f32_16x16x32_bf16 v[180:183], a[128:131], v[92:95], v[180:183]\n"
    "v_exp_f32 v7, v7\n"
    "v_mfma_f32_16x16x32_bf16 v[176:179], a[120:123], v[92:95], v[176:179]\n"
    "v_exp_f32 v8, v8\n"
    "v_mfma_f32_16x16x32_bf16 v[176:179], a[124:127], v[96:99], v[176:179]\n"
    "v_exp_f32 v9, v9\n"
    "v_mfma_f32_16x16x32_bf16 v[172:175], a[116:119], v[96:99], v[172:175]\n"
    "v_exp_f32 v10, v10\n"
    "v_mfma_f32_16x16x32_bf16 v[172:175], a[112:115], v[92:95], v[172:175]\n"
    "s_waitcnt lgkmcnt(0)\n"
    "v_exp_f32 v11, v11\n"
    "v_mfma_f32_16x16x32_bf16 v[136:139], a[136:139], v[68:71], v[136:139]\n"
    "ds_read_b128 a[112:115], v248 offset:6144\n"
    "v_mfma_f32_16x16x32_bf16 v[136:139], a[140:143], v[72:75], v[136:139]\n"
    "ds_read_b128 a[116:119], v248 offset:18432\n"
    "v_mfma_f32_16x16x32_bf16 v[188:191], a[148:151], v[72:75], v[188:191]\n"
    "ds_read_b128 a[120:123], v248 offset:7168\n"
    "v_mfma_f32_16x16x32_bf16 v[188:191], a[144:147], v[68:71], v[188:191]\n"
    "ds_read_b128 a[124:127], v248 offset:19456\n"
    "v_mfma_f32_16x16x32_bf16 v[192:195], a[152:155], v[68:71], v[192:195]\n"
    "ds_read_b128 a[128:131], v248 offset:8192\n"
    "v_mfma_f32_16x16x32_bf16 v[192:195], a[156:159], v[72:75], v[192:195]\n"
    "ds_read_b128 a[132:135], v248 offset:20480\n"
    "v_smfmac_f32_16x16x64_bf16 v[108:111], v[104:107], v[68:75], v253\n"
    "s_add_u32 s81, s81, 128\n"
    "s_cmp_gt_u32 s81, 4096\n"
    "s_cselect_b32 s81, 0, s81\n"
    "v_smfmac_f32_16x16x64_bf16 v[112:115], v[104:107], v[76:83], v253\n"
    "v_mfma_f32_16x16x32_bf16 a[4:7], a[156:159], v[80:83], a[4:7]\n"
    "v_exp_f32 v12, v12\n"
    "v_mfma_f32_16x16x32_bf16 a[4:7], a[152:155], v[76:79], a[4:7]\n"
    "v_exp_f32 v13, v13\n"
    "v_mfma_f32_16x16x32_bf16 a[0:3], a[144:147], v[76:79], a[0:3]\n"
    "v_exp_f32 v14, v14\n"
    "v_mfma_f32_16x16x32_bf16 a[0:3], a[148:151], v[80:83], a[0:3]\n"
    "v_exp_f32 v15, v15\n"
    "v_mfma_f32_16x16x32_bf16 v[152:155], a[140:143], v[80:83], v[152:155]\n"
    "v_exp_f32 v16, v16\n"
    "v_mfma_f32_16x16x32_bf16 v[152:155], a[136:139], v[76:79], v[152:155]\n"
    "v_mfma_f32_16x16x32_bf16 v[168:171], a[136:139], v[84:87], v[168:171]\n"
    "v_exp_f32 v17, v17\n"
    "v_mfma_f32_16x16x32_bf16 v[168:171], a[140:143], v[88:91], v[168:171]\n"
    "v_mfma_f32_16x16x32_bf16 a[16:19], a[148:151], v[88:91], a[16:19]\n"
    "v_mfma_f32_16x16x32_bf16 a[16:19], a[144:147], v[84:87], a[16:19]\n"
    "v_exp_f32 v18, v18\n"
    "v_mfma_f32_16x16x32_bf16 a[20:23], a[152:155], v[84:87], a[20:23]\n"
    "v_mfma_f32_16x16x32_bf16 a[20:23], a[156:159], v[88:91], a[20:23]\n"
    "v_smfmac_f32_16x16x64_bf16 v[116:119], v[104:107], v[84:91], v253\n"
    "v_smfmac_f32_16x16x64_bf16 v[120:123], v[104:107], v[92:99], v253\n"
    "v_mfma_f32_16x16x32_bf16 a[36:39], a[156:159], v[96:99], a[36:39]\n"
    "v_exp_f32 v19, v19\n"
    "v_mfma_f32_16x16x32_bf16 a[36:39], a[152:155], v[92:95], a[36:39]\n"
    "v_exp_f32 v20, v20\n"
    "v_mfma_f32_16x16x32_bf16 a[32:35], a[144:147], v[92:95], a[32:35]\n"
    "v_mfma_f32_16x16x32_bf16 a[32:35], a[148:151], v[96:99], a[32:35]\n"
    "v_exp_f32 v21, v21\n"
    "v_mfma_f32_16x16x32_bf16 v[184:187], a[140:143], v[96:99], v[184:187]\n"
    "v_mfma_f32_16x16x32_bf16 v[184:187], a[136:139], v[92:95], v[184:187]\n"
    "s_waitcnt lgkmcnt(0)\n"
    "v_mfma_f32_16x16x32_bf16 v[196:199], a[112:115], v[68:71], v[196:199]\n"
    "ds_read_b128 a[136:139], v248 offset:9216\n"
    "v_mfma_f32_16x16x32_bf16 v[196:199], a[116:119], v[72:75], v[196:199]\n"
    "ds_read_b128 a[140:143], v248 offset:21504\n"
    "v_mfma_f32_16x16x32_bf16 v[200:203], a[124:127], v[72:75], v[200:203]\n"
    "ds_read_b128 a[144:147], v248 offset:10240\n"
    "v_mfma_f32_16x16x32_bf16 v[200:203], a[120:123], v[68:71], v[200:203]\n"
    "ds_read_b128 a[148:151], v248 offset:22528\n"
    "v_mfma_f32_16x16x32_bf16 a[48:51], a[128:131], v[68:71], a[48:51]\n"
    "buffer_load_dwordx2 v[236:237], v241, s[36:39], s81 offen offset:0 sc1\n"
    "v_mfma_f32_16x16x32_bf16 a[48:51], a[132:135], v[72:75], a[48:51]\n"
    "ds_read_b128 a[152:155], v248 offset:11264\n"
    "v_mfma_f32_16x16x32_bf16 a[64:67], a[132:135], v[80:83], a[64:67]\n"
    "ds_read_b128 a[156:159], v248 offset:23552\n"
    "v_mfma_f32_16x16x32_bf16 a[64:67], a[128:131], v[76:79], a[64:67]\n"
    "v_exp_f32 v22, v22\n"
    "v_mfma_f32_16x16x32_bf16 a[12:15], a[120:123], v[76:79], a[12:15]\n"
    "v_exp_f32 v23, v23\n"
    "v_mfma_f32_16x16x32_bf16 a[12:15], a[124:127], v[80:83], a[12:15]\n"
    "v_exp_f32 v24, v24\n"
    "v_mfma_f32_16x16x32_bf16 a[8:11], a[116:119], v[80:83], a[8:11]\n"
    "v_exp_f32 v25, v25\n"
    "v_mfma_f32_16x16x32_bf16 a[8:11], a[112:115], v[76:79], a[8:11]\n"
    "v_exp_f32 v26, v26\n"
    "v_mfma_f32_16x16x32_bf16 a[24:27], a[112:115], v[84:87], a[24:27]\n"
    "v_mfma_f32_16x16x32_bf16 a[24:27], a[116:119], v[88:91], a[24:27]\n"
    "v_exp_f32 v27, v27\n"
    "v_mfma_f32_16x16x32_bf16 a[28:31], a[124:127], v[88:91], a[28:31]\n"
    "v_mfma_f32_16x16x32_bf16 a[28:31], a[120:123], v[84:87], a[28:31]\n"
    "v_mfma_f32_16x16x32_bf16 a[80:83], a[128:131], v[84:87], a[80:83]\n"
    "v_exp_f32 v28, v28\n"
    "v_mfma_f32_16x16x32_bf16 a[80:83], a[132:135], v[88:91], a[80:83]\n"
    "v_mfma_f32_16x16x32_bf16 a[96:99], a[132:135], v[96:99], a[96:99]\n"
    "v_mfma_f32_16x16x32_bf16 a[96:99], a[128:131], v[92:95], a[96:99]\n"
    "v_exp_f32 v29, v29\n"
    "v_mfma_f32_16x16x32_bf16 a[44:47], a[120:123], v[92:95], a[44:47]\n"
    "v_mfma_f32_16x16x32_bf16 a[44:47], a[124:127], v[96:99], a[44:47]\n"
    "v_exp_f32 v30, v30\n"
    "v_mfma_f32_16x16x32_bf16 a[40:43], a[116:119], v[96:99], a[40:43]\n"
    "v_mfma_f32_16x16x32_bf16 a[40:43], a[112:115], v[92:95], a[40:43]\n"
    "s_waitcnt lgkmcnt(0)\n"
    "v_mfma_f32_16x16x32_bf16 a[52:55], a[136:139], v[68:71], a[52:55]\n"
    "ds_read_b128 a[112:115], v3 offset:12288\n"
    "v_mfma_f32_16x16x32_bf16 a[52:55], a[140:143], v[72:75], a[52:55]\n"
    "ds_read_b128 a[116:119], v3 offset:14336\n"
    "v_mfma_f32_16x16x32_bf16 a[56:59], a[148:151], v[72:75], a[56:59]\n"
    "ds_read_b128 a[120:123], v3 offset:16384\n"
    "v_mfma_f32_16x16x32_bf16 a[56:59], a[144:147], v[68:71], a[56:59]\n"
    "ds_read_b128 a[124:127], v3 offset:18432\n"
    "v_mfma_f32_16x16x32_bf16 a[60:63], a[152:155], v[68:71], a[60:63]\n"
    "ds_read_b128 a[128:131], v3 offset:20480\n"
    "v_mfma_f32_16x16x32_bf16 a[60:63], a[156:159], v[72:75], a[60:63]\n"
    "ds_read_b128 a[132:135], v3 offset:22528\n"
    "v_mfma_f32_16x16x32_bf16 a[76:79], a[156:159], v[80:83], a[76:79]\n"
    "ds_read_b128 v[100:103], v250 offset:896\n"
    "v_mfma_f32_16x16x32_bf16 a[76:79], a[152:155], v[76:79], a[76:79]\n"
    "v_exp_f32 v31, v31\n"
    "v_mfma_f32_16x16x32_bf16 a[72:75], a[144:147], v[76:79], a[72:75]\n"
    "v_exp_f32 v32, v32\n"
    "v_mfma_f32_16x16x32_bf16 a[72:75], a[148:151], v[80:83], a[72:75]\n"
    "v_exp_f32 v33, v33\n"
    "v_mfma_f32_16x16x32_bf16 a[68:71], a[140:143], v[80:83], a[68:71]\n"
    "v_exp_f32 v34, v34\n"
    "v_mfma_f32_16x16x32_bf16 a[68:71], a[136:139], v[76:79], a[68:71]\n"
    "v_exp_f32 v35, v35\n"
    "v_mfma_f32_16x16x32_bf16 a[84:87], a[136:139], v[84:87], a[84:87]\n"
    "v_mfma_f32_16x16x32_bf16 a[84:87], a[140:143], v[88:91], a[84:87]\n"
    "v_mfma_f32_16x16x32_bf16 a[88:91], a[148:151], v[88:91], a[88:91]\n"
    "s_waitcnt vmcnt(18)\n"
    "ds_add_u32 v247, v246\n"
    "v_mfma_f32_16x16x32_bf16 a[88:91], a[144:147], v[84:87], a[88:91]\n"
    "v_mfma_f32_16x16x32_bf16 a[92:95], a[152:155], v[84:87], a[92:95]\n"
    "v_mfma_f32_16x16x32_bf16 a[92:95], a[156:159], v[88:91], a[92:95]\n"
    "v_mfma_f32_16x16x32_bf16 a[108:111], a[156:159], v[96:99], a[108:111]\n"
    "v_mfma_f32_16x16x32_bf16 a[108:111], a[152:155], v[92:95], a[108:111]\n"
    "v_mfma_f32_16x16x32_bf16 a[104:107], a[144:147], v[92:95], a[104:107]\n"
    "s_sub_u32 s74, s68, s65\n"
    "v_mfma_f32_16x16x32_bf16 a[104:107], a[148:151], v[96:99], a[104:107]\n"
    "s_add_u32 s66, s66, 1\n"
    "v_mov_b32 v2, v3\n"
    "v_add_u32_e64 v3, s74, v252\n"
    "v_mfma_f32_16x16x32_bf16 a[100:103], a[140:143], v[96:99], a[100:103]\n"
    "s_waitcnt lgkmcnt(1)\n"
    "s_cmp_eq_u32 s66, .Lend-.Lend2+(29)\n"
    "v_mfma_f32_16x16x32_bf16 a[100:103], a[136:139], v[92:95], a[100:103]\n"
    ".LwKe:\n"
    "s_cbranch_scc1 .LtailP\n"
    ".LNK:\n"
    "v_mfma_f32_16x16x32_bf16 v[36:39], a[112:115], a[192:195], v[100:103]\n"
    "ds_read_b128 a[136:139], v2 offset:13312\n"
    "v_cvt_pk_bf16_f32 v68, v4, v5\n"
    "v_mfma_f32_16x16x32_bf16 v[36:39], a[116:119], a[196:199], v[36:39]\n"
    "ds_read_b128 a[140:143], v2 offset:15360\n"
    "v_mfma_f32_16x16x32_bf16 v[36:39], a[120:123], a[200:203], v[36:39]\n"
    "ds_read_b128 a[144:147], v2 offset:17408\n"
    "v_mfma_f32_16x16x32_bf16 v[36:39], a[124:127], a[204:207], v[36:39]\n"
    "ds_read_b128 a[148:151], v2 offset:19456\n"
    "v_cvt_pk_bf16_f32 v69, v6, v7\n"
    "v_mfma_f32_16x16x32_bf16 v[36:39], a[128:131], a[208:211], v[36:39]\n"
    "ds_read_b128 a[152:155], v2 offset:21504\n"
    "v_mfma_f32_16x16x32_bf16 v[36:39], a[132:135], a[212:215], v[36:39]\n"
    "ds_read_b128 a[156:159], v2 offset:23552\n"
    "v_mfma_f32_16x16x32_bf16 v[40:43], a[132:135], a[236:239], v[100:103]\n"
    "ds_read_b128 v[104:107], v250 offset:912\n"
    "v_mfma_f32_16x16x32_bf16 v[40:43], a[128:131], a[232:235], v[40:43]\n"
    "s_add_u32 s68, s68, 24576\n"
    "v_cvt_pk_bf16_f32 v76, v8, v9\n"
    "v_mfma_f32_16x16x32_bf16 v[40:43], a[124:127], a[228:231], v[40:43]\n"
    "s_cmp_ge_u32 s68, s69\n"
    "s_cselect_b32 s68, s65, s68\n"
    "v_mfma_f32_16x16x32_bf16 v[40:43], a[120:123], a[224:227], v[40:43]\n"
    "v_exp_f32 v36, v36\n"
    "v_mfma_f32_16x16x32_bf16 v[40:43], a[116:119], a[220:223], v[40:43]\n"
    "v_exp_f32 v37, v37\n"
    "v_mfma_f32_16x16x32_bf16 v[40:43], a[112:115], a[216:219], v[40:43]\n"
    "v_cvt_pk_bf16_f32 v77, v10, v11\n"
    "v_cvt_pk_bf16_f32 v72, v36, v37\n"
    "v_mfma_f32_16x16x32_bf16 v[44:47], a[112:115], a[240:243], v[100:103]\n"
    "v_mfma_f32_16x16x32_bf16 v[44:47], a[116:119], a[244:247], v[44:47]\n"
    "v_exp_f32 v38, v38\n"
    "v_mfma_f32_16x16x32_bf16 v[44:47], a[120:123], a[248:251], v[44:47]\n"
    "v_mfma_f32_16x16x32_bf16 v[44:47], a[124:127], a[252:255], v[44:47]\n"
    "v_cvt_pk_bf16_f32 v84, v12, v13\n"
    "v_mfma_f32_16x16x32_bf16 v[44:47], a[128:131], a[160:163], v[44:47]\n"
    "v_exp_f32 v39, v39\n"
    "v_mfma_f32_16x16x32_bf16 v[44:47], a[132:135], a[164:167], v[44:47]\n"
    "v_cvt_pk_bf16_f32 v73, v38, v39\n"
    "v_mfma_f32_16x16x32_bf16 v[48:51], a[132:135], a[188:191], v[100:103]\n"
    "ds_read_b32 v245, v247\n"
    "v_cvt_pk_bf16_f32 v85, v14, v15\n"
    "v_mfma_f32_16x16x32_bf16 v[48:51], a[128:131], a[184:187], v[48:51]\n"
    "s_add_u32 m0, s68, 0\n"
    "v_exp_f32 v40, v40\n"
    "v_mfma_f32_16x16x32_bf16 v[48:51], a[124:127], a[180:183], v[48:51]\n"
    "v_mfma_f32_16x16x32_bf16 v[48:51], a[120:123], a[176:179], v[48:51]\n"
    "v_mfma_f32_16x16x32_bf16 v[48:51], a[116:119], a[172:175], v[48:51]\n"
    "s_waitcnt vmcnt(0)\n"
    "v_cmp_eq_u64 s[54:55], v[236:237], v[238:239]\n"
    "v_cvt_pk_bf16_f32 v92, v16, v17\n"
    "v_mfma_f32_16x16x32_bf16 v[48:51], a[112:115], a[168:171], v[48:51]\n"
    "s_waitcnt lgkmcnt(1)\n"
    "v_exp_f32 v41, v41\n"
    "v_mfma_f32_16x16x32_bf16 v[52:55], a[136:139], a[192:195], v[104:107]\n"
    "s_waitcnt lgkmcnt(0)\n"
    "v_cmp_le_u32 s[56:57], s59, v245\n"
    "v_cvt_pk_bf16_f32 v80, v40, v41\n"
    "v_mfma_f32_16x16x32_bf16 v[52:55], a[140:143], a[196:199], v[52:55]\n"
    "s_waitcnt vmcnt(1)\n"
    "v_exp_f32 v42, v42\n"
    "v_mfma_f32_16x16x32_bf16 v[52:55], a[144:147], a[200:203], v[52:55]\n"
    "s_and_b64 s[54:55], s[54:55], s[56:57]\n"
    "s_cmp_eq_u64 s[54:55], -1\n"
    "v_mfma_f32_16x16x32_bf16 v[52:55], a[148:151], a[204:207], v[52:55]\n"
    "s_cbranch_scc0 .Lpoll2\n"
    ".Lpoll2r:\n"
    "v_mfma_f32_16x16x32_bf16 v[52:55], a[152:155], a[208:211], v[52:55]\n"
    "buffer_load_dwordx4 v1, s[16:19], s67 offen offset:0 sc1 lds\n"
    "v_mfma_f32_16x16x32_bf16 v[52:55], a[156:159], a[212:215], v[52:55]\n"
    "s_add_u32 s76, s71, s60\n"
    "ds_read_b128 a[112:115], v3 offset:0\n"
    "v_cvt_pk_bf16_f32 v93, v18, v19\n"
    "v_mfma_f32_16x16x32_bf16 v[56:59], a[156:159], a[236:239], v[104:107]\n"
    "s_add_u32 s77, s71, s61\n"
    "ds_read_b128 a[116:119], v3 offset:2048\n"
    "v_cvt_pk_bf16_f32 v70, v20, v21\n"
    "v_mfma_f32_16x16x32_bf16 v[56:59], a[152:155], a[232:235], v[56:59]\n"
    "ds_read_b128 a[120:123], v3 offset:4096\n"
    "v_mfma_f32_16x16x32_bf16 v[56:59], a[148:151], a[228:231], v[56:59]\n"
    "buffer_load_dwordx4 v1, s[16:19], s67 offen offset:1024 sc1 lds\n"
    "v_mfma_f32_16x16x32_bf16 v[56:59], a[144:147], a[224:227], v[56:59]\n"
    "ds_read_b128 a[124:127], v3 offset:6144\n"
    "v_cvt_pk_bf16_f32 v71, v22, v23\n"
    "v_mfma_f32_16x16x32_bf16 v[56:59], a[140:143], a[220:223], v[56:59]\n"
    "ds_read_b128 a[128:131], v3 offset:8192\n"
    "v_mfma_f32_16x16x32_bf16 v[56:59], a[136:139], a[216:219], v[56:59]\n"
    "ds_read_b128 a[132:135], v3 offset:10240\n"
    "v_mfma_f32_16x16x32_bf16 v[60:63], a[136:139], a[240:243], v[104:107]\n"
    "buffer_load_dwordx4 v1, s[16:19], s67 offen offset:2048 sc1 lds\n"
    "v_mfma_f32_16x16x32_bf16 v[60:63], a[140:143], a[244:247], v[60:63]\n"
    "ds_read_b128 v[100:103], v250 offset:1536\n"
    "v_cvt_pk_bf16_f32 v78, v24, v25\n"
    "v_mfma_f32_16x16x32_bf16 v[60:63], a[144:147], a[248:251], v[60:63]\n"
    "v_exp_f32 v43, v43\n"
    "v_mfma_f32_16x16x32_bf16 v[60:63], a[148:151], a[252:255], v[60:63]\n"
    "ds_read_b128 v[36:39], v243 offset:0\n"
    "v_cvt_pk_bf16_f32 v81, v42, v43\n"
    "v_mfma_f32_16x16x32_bf16 v[60:63], a[152:155], a[160:163], v[60:63]\n"
    "buffer_load_dwordx4 v1, s[16:19], s67 offen offset:3072 sc1 lds\n"
    "v_mfma_f32_16x16x32_bf16 v[60:63], a[156:159], a[164:167], v[60:63]\n"
    "s_add_u32 m0, s68, 4096\n"
    "v_cvt_pk_bf16_f32 v79, v26, v27\n"
    "v_add_u32 v243, 512, v243\n"
    "v_mfma_f32_16x16x32_bf16 v[64:67], a[156:159], a[188:191], v[104:107]\n"
    "s_add_u32 s83, s67, 4096\n"
    "v_exp_f32 v44, v44\n"
    "v_mfma_f32_16x16x32_bf16 v[64:67], a[152:155], a[184:187], v[64:67]\n"
    "v_exp_f32 v45, v45\n"
    "v_mfma_f32_16x16x32_bf16 v[64:67], a[148:151], a[180:183], v[64:67]\n"
    "buffer_load_dwordx4 v1, s[16:19], s83 offen offset:0 sc1 lds\n"
    "v_mfma_f32_16x16x32_bf16 v[64:67], a[144:147], a[176:179], v[64:67]\n"
    "v_cvt_pk_bf16_f32 v86, v28, v29\n"
    "v_cvt_pk_bf16_f32 v88, v44, v45\n"
    "v_mfma_f32_16x16x32_bf16 v[64:67], a[140:143], a[172:175], v[64:67]\n"
    "v_exp_f32 v46, v46\n"
    "v_mfma_f32_16x16x32_bf16 v[64:67], a[136:139], a[168:171], v[64:67]\n"
    "s_waitcnt lgkmcnt(1)\n"
    "v_exp_f32 v47, v47\n"
    "v_mfma_f32_16x16x32_bf16 v[4:7], a[112:115], a[192:195], v[100:103]\n"
    "buffer_load_dwordx4 v1, s[16:19], s83 offen offset:1024 sc1 lds\n"
    "v_mfma_f32_16x16x32_bf16 v[4:7], a[116:119], a[196:199], v[4:7]\n"
    "s_add_u32 m0, s70, .Lend-.Lend2+(0)\n"
    "ds_read_b128 a[136:139], v3 offset:1024\n"
    "v_cvt_pk_bf16_f32 v87, v30, v31\n"
    "v_mfma_f32_16x16x32_bf16 v[4:7], a[120:123], a[200:203], v[4:7]\n"
    "s_add_u32 s83, s67, 24576\n"
    "ds_read_b128 a[140:143], v3 offset:3072\n"
    "v_cvt_pk_bf16_f32 v89, v46, v47\n"
    "v_mfma_f32_16x16x32_bf16 v[4:7], a[124:127], a[204:207], v[4:7]\n"
    "s_waitcnt lgkmcnt(2)\n"
    "ds_read_b128 a[144:147], v3 offset:5120\n"
    "v_fma_f32 v204, v204, v36, -v38\n"
    "v_mfma_f32_16x16x32_bf16 v[4:7], a[128:131], a[208:211], v[4:7]\n"
    "buffer_load_dwordx4 v1, s[16:19], s83 offen offset:0 sc1 lds\n"
    "v_mfma_f32_16x16x32_bf16 v[4:7], a[132:135], a[212:215], v[4:7]\n"
    "ds_read_b128 a[148:151], v3 offset:7168\n"
    "v_cvt_pk_bf16_f32 v94, v32, v33\n"
    "v_mfma_f32_16x16x32_bf16 v[8:11], a[132:135], a[236:239], v[100:103]\n"
    "ds_read_b128 a[152:155], v3 offset:9216\n"
    "v_fma_f32 v205, v205, v37, -v39\n"
    "v_mfma_f32_16x16x32_bf16 v[8:11], a[128:131], a[232:235], v[8:11]\n"
    "ds_read_b128 a[156:159], v3 offset:11264\n"
    "v_fma_f32 v206, v206, v36, -v38\n"
    "v_mfma_f32_16x16x32_bf16 v[8:11], a[124:127], a[228:231], v[8:11]\n"
    "buffer_load_dwordx4 v1, s[16:19], s83 offen offset:1024 sc1 lds\n"
    "v_mfma_f32_16x16x32_bf16 v[8:11], a[120:123], a[224:227], v[8:11]\n"
    "ds_read_b128 v[104:107], v250 offset:1552\n"
    "v_cvt_pk_bf16_f32 v95, v34, v35\n"
    "v_mfma_f32_16x16x32_bf16 v[8:11], a[116:119], a[220:223], v[8:11]\n"
    "v_exp_f32 v52, v52\n"
    "v_mfma_f32_16x16x32_bf16 v[8:11], a[112:115], a[216:219], v[8:11]\n"
    "v_exp_f32 v53, v53\n"
    "v_mfma_f32_16x16x32_bf16 v[12:15], a[112:115], a[240:243], v[100:103]\n"
    "buffer_load_dwordx4 v1, s[16:19], s83 offen offset:2048 sc1 lds\n"
    "v_mfma_f32_16x16x32_bf16 v[12:15], a[116:119], a[244:247], v[12:15]\n"
    "v_exp_f32 v54, v54\n"
    "v_mfma_f32_16x16x32_bf16 v[12:15], a[120:123], a[248:251], v[12:15]\n"
    "v_exp_f32 v55, v55\n"
    "v_mfma_f32_16x16x32_bf16 v[12:15], a[124:127], a[252:255], v[12:15]\n"
    "v_cvt_pk_bf16_f32 v74, v52, v53\n"
    "v_cvt_pk_bf16_f32 v75, v54, v55\n"
    "v_mfma_f32_16x16x32_bf16 v[12:15], a[128:131], a[160:163], v[12:15]\n"
    "buffer_load_dwordx4 v1, s[16:19], s83 offen offset:3072 sc1 lds\n"
    "v_mfma_f32_16x16x32_bf16 v[12:15], a[132:135], a[164:167], v[12:15]\n"
    "s_add_u32 m0, s70, 4096\n"
    "v_exp_f32 v56, v56\n"
    "v_mfma_f32_16x16x32_bf16 v[16:19], a[132:135], a[188:191], v[100:103]\n"
    "s_add_u32 s83, s67, 28672\n"
    "v_exp_f32 v48, v48\n"
    "v_mfma_f32_16x16x32_bf16 v[16:19], a[128:131], a[184:187], v[16:19]\n"
    "v_exp_f32 v49, v49\n"
    "v_mfma_f32_16x16x32_bf16 v[16:19], a[124:127], a[180:183], v[16:19]\n"
    "buffer_load_dwordx4 v1, s[16:19], s83 offen offset:0 sc1 lds\n"
    "v_mfma_f32_16x16x32_bf16 v[16:19], a[120:123], a[176:179], v[16:19]\n"
    "v_exp_f32 v57, v57\n"
    "v_mfma_f32_16x16x32_bf16 v[16:19], a[116:119], a[172:175], v[16:19]\n"
    "v_cvt_pk_bf16_f32 v82, v56, v57\n"
    "v_cvt_pk_bf16_f32 v96, v48, v49\n"
    "v_mfma_f32_16x16x32_bf16 v[16:19], a[112:115], a[168:171], v[16:19]\n"
    "s_waitcnt lgkmcnt(0)\n"
    "v_exp_f32 v58, v58\n"
    "v_mfma_f32_16x16x32_bf16 v[20:23], a[136:139], a[192:195], v[104:107]\n"
    "buffer_load_dwordx4 v1, s[16:19], s83 offen offset:1024 sc1 lds\n"
    "v_mfma_f32_16x16x32_bf16 v[20:23], a[140:143], a[196:199], v[20:23]\n"
    "s_add_u32 s67, s67, 49152\n"
    "ds_read_b128 a[112:115], v249 offset:0\n"
    "v_fma_f32 v207, v207, v37, -v39\n"
    "v_mfma_f32_16x16x32_bf16 v[20:23], a[144:147], a[200:203], v[20:23]\n"
    "s_add_u32 s59, s59, 4\n"
    "ds_read_b128 a[116:119], v249 offset:12288\n"
    "v_cvt_pk_bf16_f32 v204, v204, v205\n"
    "v_mfma_f32_16x16x32_bf16 v[20:23], a[148:151], a[204:207], v[20:23]\n"
    "ds_read_b128 a[120:123], v249 offset:1024\n"
    "v_cvt_pk_bf16_f32 v206, v206, v207\n"
    "v_mfma_f32_16x16x32_bf16 v[20:23], a[152:155], a[208:211], v[20:23]\n"
    "ds_read_b128 a[124:127], v249 offset:13312\n"
    "v_fma_f32 v208, v208, v36, -v38\n"
    "v_mfma_f32_16x16x32_bf16 v[20:23], a[156:159], a[212:215], v[20:23]\n"
    "ds_read_b128 a[128:131], v249 offset:2048\n"
    "v_fma_f32 v209, v209, v37, -v39\n"
    "v_mfma_f32_16x16x32_bf16 v[24:27], a[156:159], a[236:239], v[104:107]\n"
    "ds_read_b128 a[132:135], v249 offset:14336\n"
    "v_fma_f32 v210, v210, v36, -v38\n"
    "v_mfma_f32_16x16x32_bf16 v[24:27], a[152:155], a[232:235], v[24:27]\n"
    "v_exp_f32 v59, v59\n"
    "v_mfma_f32_16x16x32_bf16 v[24:27], a[148:151], a[228:231], v[24:27]\n"
    "v_cvt_pk_bf16_f32 v83, v58, v59\n"
    "v_fma_f32 v211, v211, v37, -v39\n"
    "v_mfma_f32_16x16x32_bf16 v[24:27], a[144:147], a[224:227], v[24:27]\n"
    "v_exp_f32 v60, v60\n"
    "v_mfma_f32_16x16x32_bf16 v[24:27], a[140:143], a[220:223], v[24:27]\n"
    "v_exp_f32 v61, v61\n"
    "v_mfma_f32_16x16x32_bf16 v[24:27], a[136:139], a[216:219], v[24:27]\n"
    "v_exp_f32 v62, v62\n"
    "v_mfma_f32_16x16x32_bf16 v[28:31], a[136:139], a[240:243], v[104:107]\n"
    "v_cvt_pk_bf16_f32 v90, v60, v61\n"
    "v_cvt_pk_bf16_f32 v208, v208, v209\n"
    "v_mfma_f32_16x16x32_bf16 v[28:31], a[140:143], a[244:247], v[28:31]\n"
    "v_exp_f32 v63, v63\n"
    "v_mfma_f32_16x16x32_bf16 v[28:31], a[144:147], a[248:251], v[28:31]\n"
    "v_cvt_pk_bf16_f32 v91, v62, v63\n"
    "v_cvt_pk_bf16_f32 v210, v210, v211\n"
    "v_mfma_f32_16x16x32_bf16 v[28:31], a[148:151], a[252:255], v[28:31]\n"
    "v_exp_f32 v50, v50\n"
    "v_mfma_f32_16x16x32_bf16 v[28:31], a[152:155], a[160:163], v[28:31]\n"
    "v_exp_f32 v51, v51\n"
    "v_mfma_f32_16x16x32_bf16 v[28:31], a[156:159], a[164:167], v[28:31]\n"
    "v_exp_f32 v64, v64\n"
    "v_mfma_f32_16x16x32_bf16 v[32:35], a[156:159], a[188:191], v[104:107]\n"
    "v_cvt_pk_bf16_f32 v97, v50, v51\n"
    "v_fma_f32 v212, v212, v36, -v38\n"
    "v_mfma_f32_16x16x32_bf16 v[32:35], a[152:155], a[184:187], v[32:35]\n"
    "v_exp_f32 v65, v65\n"
    "v_mfma_f32_16x16x32_bf16 v[32:35], a[148:151], a[180:183], v[32:35]\n"
    "v_cvt_pk_bf16_f32 v98, v64, v65\n"
    "v_fma_f32 v213, v213, v37, -v39\n"
    "v_mfma_f32_16x16x32_bf16 v[32:35], a[144:147], a[176:179], v[32:35]\n"
    "buffer_store_dword v204, v244, s[16:19], s76 offen offset:0 sc1\n"
    "v_mfma_f32_16x16x32_bf16 v[32:35], a[140:143], a[172:175], v[32:35]\n"
    "v_exp_f32 v66, v66\n"
    "v_mfma_f32_16x16x32_bf16 v[32:35], a[136:139], a[168:171], v[32:35]\n"
    "s_waitcnt lgkmcnt(0)\n"
    "v_fma_f32 v214, v214, v36, -v38\n"
    "v_fma_f32 v215, v215, v37, -v39\n"
    "v_mfma_f32_16x16x32_bf16 v[124:127], a[112:115], v[68:71], v[124:127]\n"
    "ds_read_b128 a[136:139], v249 offset:3072\n"
    "v_cvt_pk_bf16_f32 v212, v212, v213\n"
    "v_mfma_f32_16x16x32_bf16 v[124:127], a[116:119], v[72:75], v[124:127]\n"
    "ds_read_b128 a[140:143], v249 offset:15360\n"
    "v_cvt_pk_bf16_f32 v214, v214, v215\n"
    "v_mfma_f32_16x16x32_bf16 v[128:131], a[124:127], v[72:75], v[128:131]\n"
    "ds_read_b128 a[144:147], v249 offset:4096\n"
    "v_fma_f32 v216, v216, v36, -v38\n"
    "v_mfma_f32_16x16x32_bf16 v[128:131], a[120:123], v[68:71], v[128:131]\n"
    "ds_read_b128 a[148:151], v249 offset:16384\n"
    "v_fma_f32 v217, v217, v37, -v39\n"
    "v_mfma_f32_16x16x32_bf16 v[132:135], a[128:131], v[68:71], v[132:135]\n"
    "ds_read_b128 a[152:155], v249 offset:5120\n"
    "v_fma_f32 v218, v218, v36, -v38\n"
    "v_mfma_f32_16x16x32_bf16 v[132:135], a[132:135], v[72:75], v[132:135]\n"
    "ds_read_b128 a[156:159], v249 offset:17408\n"
    "v_fma_f32 v219, v219, v37, -v39\n"
    "v_mfma_f32_16x16x32_bf16 v[148:151], a[132:135], v[80:83], v[148:151]\n"
    "ds_read_b128 v[104:107], v251 offset:768\n"
    "v_cvt_pk_bf16_f32 v216, v216, v217\n"
    "v_mfma_f32_16x16x32_bf16 v[148:151], a[128:131], v[76:79], v[148:151]\n"
    "v_exp_f32 v67, v67\n"
    "v_mfma_f32_16x16x32_bf16 v[144:147], a[120:123], v[76:79], v[144:147]\n"
    "v_cvt_pk_bf16_f32 v99, v66, v67\n"
    "v_cvt_pk_bf16_f32 v218, v218, v219\n"
    "v_mfma_f32_16x16x32_bf16 v[144:147], a[124:127], v[80:83], v[144:147]\n"
    "buffer_store_dword v206, v244, s[16:19], s76 offen offset:128 sc1\n"
    "v_mfma_f32_16x16x32_bf16 v[140:143], a[116:119], v[80:83], v[140:143]\n"
    "v_exp_f32 v4, v4\n"
    "v_mfma_f32_16x16x32_bf16 v[140:143], a[112:115], v[76:79], v[140:143]\n"
    "buffer_store_dword v208, v244, s[16:19], s76 offen offset:1024 sc1\n"
    "v_mfma_f32_16x16x32_bf16 v[156:159], a[112:115], v[84:87], v[156:159]\n"
    "v_fma_f32 v220, v220, v36, -v38\n"
    "v_fma_f32 v221, v221, v37, -v39\n"
    "v_mfma_f32_16x16x32_bf16 v[156:159], a[116:119], v[88:91], v[156:159]\n"
    "buffer_store_dword v210, v244, s[16:19], s76 offen offset:1152 sc1\n"
    "v_mfma_f32_16x16x32_bf16 v[160:163], a[124:127], v[88:91], v[160:163]\n"
    "v_exp_f32 v5, v5\n"
    "v_mfma_f32_16x16x32_bf16 v[160:163], a[120:123], v[84:87], v[160:163]\n"
    "buffer_store_dword v212, v244, s[16:19], s76 offen offset:256 sc1\n"
    "v_mfma_f32_16x16x32_bf16 v[164:167], a[128:131], v[84:87], v[164:167]\n"
    "v_exp_f32 v6, v6\n"
    "v_mfma_f32_16x16x32_bf16 v[164:167], a[132:135], v[88:91], v[164:167]\n"
    "buffer_store_dword v214, v244, s[16:19], s76 offen offset:384 sc1\n"
    "v_mfma_f32_16x16x32_bf16 v[180:183], a[132:135], v[96:99], v[180:183]\n"
    "v_fma_f32 v222, v222, v36, -v38\n"
    "v_fma_f32 v223, v223, v37, -v39\n"
    "v_mfma_f32_16x16x32_bf16 v[180:183], a[128:131], v[92:95], v[180:183]\n"
    "v_exp_f32 v7, v7\n"
    "v_mfma_f32_16x16x32_bf16 v[176:179], a[120:123], v[92:95], v[176:179]\n"
    "v_exp_f32 v8, v8\n"
    "v_mfma_f32_16x16x32_bf16 v[176:179], a[124:127], v[96:99], v[176:179]\n"
    "v_cvt_pk_bf16_f32 v220, v220, v221\n"
    "v_cvt_pk_bf16_f32 v222, v222, v223\n"
    "v_mfma_f32_16x16x32_bf16 v[172:175], a[116:119], v[96:99], v[172:175]\n"
    "v_exp_f32 v9, v9\n"
    "v_mfma_f32_16x16x32_bf16 v[172:175], a[112:115], v[92:95], v[172:175]\n"
    "s_waitcnt lgkmcnt(0)\n"
    "v_exp_f32 v10, v10\n"
    "v_mfma_f32_16x16x32_bf16 v[136:139], a[136:139], v[68:71], v[136:139]\n"
    "ds_read_b128 a[112:115], v249 offset:6144\n"
    "v_fma_f32 v224, v224, v36, -v38\n"
    "v_mfma_f32_16x16x32_bf16 v[136:139], a[140:143], v[72:75], v[136:139]\n"
    "ds_read_b128 a[116:119], v249 offset:18432\n"
    "v_fma_f32 v225, v225, v37, -v39\n"
    "v_mfma_f32_16x16x32_bf16 v[188:191], a[148:151], v[72:75], v[188:191]\n"
    "ds_read_b128 a[120:123], v249 offset:7168\n"
    "v_fma_f32 v226, v226, v36, -v38\n"
    "v_mfma_f32_16x16x32_bf16 v[188:191], a[144:147], v[68:71], v[188:191]\n"
    "ds_read_b128 a[124:127], v249 offset:19456\n"
    "v_fma_f32 v227, v227, v37, -v39\n"
    "v_mfma_f32_16x16x32_bf16 v[192:195], a[152:155], v[68:71], v[192:195]\n"
    "ds_read_b128 a[128:131], v249 offset:8192\n"
    "v_cvt_pk_bf16_f32 v224, v224, v225\n"
    "v_mfma_f32_16x16x32_bf16 v[192:195], a[156:159], v[72:75], v[192:195]\n"
    "ds_read_b128 a[132:135], v249 offset:20480\n"
    "v_smfmac_f32_16x16x64_bf16 v[108:111], v[104:107], v[68:75], v253\n"
    "s_add_u32 s81, s81, 128\n"
    "s_cmp_gt_u32 s81, 4096\n"
    "s_cselect_b32 s81, 0, s81\n"
    "v_smfmac_f32_16x16x64_bf16 v[112:115], v[104:107], v[76:83], v253\n"
    "v_mfma_f32_16x16x32_bf16 a[4:7], a[156:159], v[80:83], a[4:7]\n"
    "v_exp_f32 v11, v11\n"
    "v_mfma_f32_16x16x32_bf16 a[4:7], a[152:155], v[76:79], a[4:7]\n"
    "v_exp_f32 v12, v12\n"
    "v_mfma_f32_16x16x32_bf16 a[0:3], a[144:147], v[76:79], a[0:3]\n"
    "v_exp_f32 v13, v13\n"
    "v_mfma_f32_16x16x32_bf16 a[0:3], a[148:151], v[80:83], a[0:3]\n"
    "v_exp_f32 v14, v14\n"
    "v_mfma_f32_16x16x32_bf16 v[152:155], a[140:143], v[80:83], v[152:155]\n"
    "v_exp_f32 v15, v15\n"
    "v_mfma_f32_16x16x32_bf16 v[152:155], a[136:139], v[76:79], v[152:155]\n"
    "v_cvt_pk_bf16_f32 v226, v226, v227\n"
    "v_fma_f32 v228, v228, v36, -v38\n"
    "v_mfma_f32_16x16x32_bf16 v[168:171], a[136:139], v[84:87], v[168:171]\n"
    "v_exp_f32 v16, v16\n"
    "v_mfma_f32_16x16x32_bf16 v[168:171], a[140:143], v[88:91], v[168:171]\n"
    "v_exp_f32 v17, v17\n"
    "v_mfma_f32_16x16x32_bf16 a[16:19], a[148:151], v[88:91], a[16:19]\n"
    "v_fma_f32 v229, v229, v37, -v39\n"
    "v_fma_f32 v230, v230, v36, -v38\n"
    "v_mfma_f32_16x16x32_bf16 a[16:19], a[144:147], v[84:87], a[16:19]\n"
    "v_exp_f32 v18, v18\n"
    "v_mfma_f32_16x16x32_bf16 a[20:23], a[152:155], v[84:87], a[20:23]\n"
    "v_fma_f32 v231, v231, v37, -v39\n"
    "v_mfma_f32_16x16x32_bf16 a[20:23], a[156:159], v[88:91], a[20:23]\n"
    "v_smfmac_f32_16x16x64_bf16 v[116:119], v[104:107], v[84:91], v253\n"
    "v_smfmac_f32_16x16x64_bf16 v[120:123], v[104:107], v[92:99], v253\n"
    "v_mfma_f32_16x16x32_bf16 a[36:39], a[156:159], v[96:99], a[36:39]\n"
    "v_exp_f32 v19, v19\n"
    "v_mfma_f32_16x16x32_bf16 a[36:39], a[152:155], v[92:95], a[36:39]\n"
    "v_exp_f32 v20, v20\n"
    "v_mfma_f32_16x16x32_bf16 a[32:35], a[144:147], v[92:95], a[32:35]\n"
    "v_cvt_pk_bf16_f32 v228, v228, v229\n"
    "v_cvt_pk_bf16_f32 v230, v230, v231\n"
    "v_mfma_f32_16x16x32_bf16 a[32:35], a[148:151], v[96:99], a[32:35]\n"
    "v_exp_f32 v21, v21\n"
    "v_mfma_f32_16x16x32_bf16 v[184:187], a[140:143], v[96:99], v[184:187]\n"
    "v_fma_f32 v232, v232, v36, -v38\n"
    "v_mfma_f32_16x16x32_bf16 v[184:187], a[136:139], v[92:95], v[184:187]\n"
    "s_waitcnt lgkmcnt(0)\n"
    "v_mfma_f32_16x16x32_bf16 v[196:199], a[112:115], v[68:71], v[196:199]\n"
    "ds_read_b128 a[136:139], v249 offset:9216\n"
    "v_fma_f32 v233, v233, v37, -v39\n"
    "v_mfma_f32_16x16x32_bf16 v[196:199], a[116:119], v[72:75], v[196:199]\n"
    "ds_read_b128 a[140:143], v249 offset:21504\n"
    "v_mfma_f32_16x16x32_bf16 v[200:203], a[124:127], v[72:75], v[200:203]\n"
    "ds_read_b128 a[144:147], v249 offset:10240\n"
    "v_mfma_f32_16x16x32_bf16 v[200:203], a[120:123], v[68:71], v[200:203]\n"
    "ds_read_b128 a[148:151], v249 offset:22528\n"
    "v_fma_f32 v234, v234, v36, -v38\n"
    "v_mfma_f32_16x16x32_bf16 a[48:51], a[128:131], v[68:71], a[48:51]\n"
    "buffer_load_dwordx2 v[236:237], v241, s[36:39], s81 offen offset:0 sc1\n"
    "v_mfma_f32_16x16x32_bf16 a[48:51], a[132:135], v[72:75], a[48:51]\n"
    "ds_read_b128 a[152:155], v249 offset:11264\n"
    "v_fma_f32 v235, v235, v37, -v39\n"
    "v_mfma_f32_16x16x32_bf16 a[64:67], a[132:135], v[80:83], a[64:67]\n"
    "ds_read_b128 a[156:159], v249 offset:23552\n"
    "v_mfma_f32_16x16x32_bf16 a[64:67], a[128:131], v[76:79], a[64:67]\n"
    "buffer_store_dword v216, v244, s[16:19], s76 offen offset:1280 sc1\n"
    "v_mfma_f32_16x16x32_bf16 a[12:15], a[120:123], v[76:79], a[12:15]\n"
    "v_exp_f32 v22, v22\n"
    "v_mfma_f32_16x16x32_bf16 a[12:15], a[124:127], v[80:83], a[12:15]\n"
    "buffer_store_dword v218, v244, s[16:19], s76 offen offset:1408 sc1\n"
    "v_mfma_f32_16x16x32_bf16 a[8:11], a[116:119], v[80:83], a[8:11]\n"
    "v_exp_f32 v23, v23\n"
    "v_mfma_f32_16x16x32_bf16 a[8:11], a[112:115], v[76:79], a[8:11]\n"
    "buffer_store_dword v220, v244, s[16:19], s77 offen offset:0 sc1\n"
    "v_mfma_f32_16x16x32_bf16 a[24:27], a[112:115], v[84:87], a[24:27]\n"
    "v_exp_f32 v24, v24\n"
    "v_mfma_f32_16x16x32_bf16 a[24:27], a[116:119], v[88:91], a[24:27]\n"
    "buffer_store_dword v222, v244, s[16:19], s77 offen offset:128 sc1\n"
    "v_mfma_f32_16x16x32_bf16 a[28:31], a[124:127], v[88:91], a[28:31]\n"
    "v_exp_f32 v25, v25\n"
    "v_mfma_f32_16x16x32_bf16 a[28:31], a[120:123], v[84:87], a[28:31]\n"
    "buffer_store_dword v224, v244, s[16:19], s77 offen offset:1024 sc1\n"
    "v_mfma_f32_16x16x32_bf16 a[80:83], a[128:131], v[84:87], a[80:83]\n"
    "v_exp_f32 v26, v26\n"
    "v_mfma_f32_16x16x32_bf16 a[80:83], a[132:135], v[88:91], a[80:83]\n"
    "buffer_store_dword v226, v244, s[16:19], s77 offen offset:1152 sc1\n"
    "v_mfma_f32_16x16x32_bf16 a[96:99], a[132:135], v[96:99], a[96:99]\n"
    "v_cvt_pk_bf16_f32 v232, v232, v233\n"
    "v_cvt_pk_bf16_f32 v234, v234, v235\n"
    "v_mfma_f32_16x16x32_bf16 a[96:99], a[128:131], v[92:95], a[96:99]\n"
    "buffer_store_dword v228, v244, s[16:19], s77 offen offset:256 sc1\n"
    "v_mfma_f32_16x16x32_bf16 a[44:47], a[120:123], v[92:95], a[44:47]\n"
    "v_exp_f32 v27, v27\n"
    "v_mfma_f32_16x16x32_bf16 a[44:47], a[124:127], v[96:99], a[44:47]\n"
    "buffer_store_dword v230, v244, s[16:19], s77 offen offset:384 sc1\n"
    "v_mfma_f32_16x16x32_bf16 a[40:43], a[116:119], v[96:99], a[40:43]\n"
    "v_exp_f32 v28, v28\n"
    "v_mfma_f32_16x16x32_bf16 a[40:43], a[112:115], v[92:95], a[40:43]\n"
    "s_waitcnt lgkmcnt(0)\n"
    "v_exp_f32 v29, v29\n"
    "v_mfma_f32_16x16x32_bf16 a[52:55], a[136:139], v[68:71], a[52:55]\n"
    "ds_read_b128 a[112:115], v3 offset:12288\n"
    "v_mfma_f32_16x16x32_bf16 a[52:55], a[140:143], v[72:75], a[52:55]\n"
    "ds_read_b128 a[116:119], v3 offset:14336\n"
    "v_mfma_f32_16x16x32_bf16 a[56:59], a[148:151], v[72:75], a[56:59]\n"
    "ds_read_b128 a[120:123], v3 offset:16384\n"
    "v_mfma_f32_16x16x32_bf16 a[56:59], a[144:147], v[68:71], a[56:59]\n"
    "ds_read_b128 a[124:127], v3 offset:18432\n"
    "v_mfma_f32_16x16x32_bf16 a[60:63], a[152:155], v[68:71], a[60:63]\n"
    "ds_read_b128 a[128:131], v3 offset:20480\n"
    "v_mfma_f32_16x16x32_bf16 a[60:63], a[156:159], v[72:75], a[60:63]\n"
    "ds_read_b128 a[132:135], v3 offset:22528\n"
    "v_mfma_f32_16x16x32_bf16 a[76:79], a[156:159], v[80:83], a[76:79]\n"
    "ds_read_b128 v[100:103], v250 offset:1664\n"
    "v_mfma_f32_16x16x32_bf16 a[76:79], a[152:155], v[76:79], a[76:79]\n"
    "buffer_store_dword v232, v244, s[16:19], s77 offen offset:1280 sc1\n"
    "v_mfma_f32_16x16x32_bf16 a[72:75], a[144:147], v[76:79], a[72:75]\n"
    "v_exp_f32 v30, v30\n"
    "v_mfma_f32_16x16x32_bf16 a[72:75], a[148:151], v[80:83], a[72:75]\n"
    "buffer_store_dword v234, v244, s[16:19], s77 offen offset:1408 sc1\n"
    "v_mfma_f32_16x16x32_bf16 a[68:71], a[140:143], v[80:83], a[68:71]\n"
    "v_exp_f32 v31, v31\n"
    "v_mfma_f32_16x16x32_bf16 a[68:71], a[136:139], v[76:79], a[68:71]\n"
    "v_exp_f32 v32, v32\n"
    "v_mfma_f32_16x16x32_bf16 a[84:87], a[136:139], v[84:87], a[84:87]\n"
    "v_exp_f32 v33, v33\n"
    "v_mfma_f32_16x16x32_bf16 a[84:87], a[140:143], v[88:91], a[84:87]\n"
    "v_exp_f32 v34, v34\n"
    "v_mfma_f32_16x16x32_bf16 a[88:91], a[148:151], v[88:91], a[88:91]\n"
    "s_waitcnt vmcnt(17)\n"
    "ds_add_u32 v247, v246\n"
    "v_mfma_f32_16x16x32_bf16 a[88:91], a[144:147], v[84:87], a[88:91]\n"
    "v_exp_f32 v35, v35\n"
    "v_mfma_f32_16x16x32_bf16 a[92:95], a[152:155], v[84:87], a[92:95]\n"
    "v_mfma_f32_16x16x32_bf16 a[92:95], a[156:159], v[88:91], a[92:95]\n"
    "v_mfma_f32_16x16x32_bf16 a[108:111], a[156:159], v[96:99], a[108:111]\n"
    "v_mfma_f32_16x16x32_bf16 a[108:111], a[152:155], v[92:95], a[108:111]\n"
    "v_mfma_f32_16x16x32_bf16 a[104:107], a[144:147], v[92:95], a[104:107]\n"
    "s_sub_u32 s74, s68, s65\n"
    "v_add_u32 v250, 1536, v250\n"
    "v_add_u32 v251, 1536, v251\n"
    "v_mfma_f32_16x16x32_bf16 a[104:107], a[148:151], v[96:99], a[104:107]\n"
    "s_add_u32 s66, s66, 1\n"
    "v_mov_b32 v2, v3\n"
    "v_add_u32 v3, s74, v252\n"
    "v_mfma_f32_16x16x32_bf16 a[100:103], a[140:143], v[96:99], a[100:103]\n"
    "s_waitcnt lgkmcnt(1)\n"
    "s_cmp_lt_u32 s66, 29\n"
    "v_mfma_f32_16x16x32_bf16 a[100:103], a[136:139], v[92:95], a[100:103]\n"
    "s_cbranch_scc1 .LPK\n"
    "s_branch .Ltail\n"
    "s_nop 0\n"
    ".LpollAK:\n"
    "s_sleep 1\n"
    "buffer_load_dwordx2 v[236:237], v87, s[36:39], 0 offen offset:0 sc1\n"
    "s_waitcnt vmcnt(0)\n"
    "v_cmp_eq_u64 s[54:55], v[236:237], v[238:239]\n"
    "s_add_u32 s58, s58, 1\n"
    "s_cmp_eq_u64 s[54:55], -1\n"
    "s_cbranch_scc1 .LpollAKr\n"
    "s_cmp_lt_u32 s58, 0x80000\n"
    "s_cbranch_scc1 .LpollAK\n"
    "s_branch .LpollAKr\n"
    "s_nop 0\n"
    "s_nop 0\n"
    "s_nop 0\n"
    ".LpollAV:\n"
    "s_sleep 1\n"
    "buffer_load_dwordx2 v[236:237], v87, s[36:39], 0 offen offset:0 sc1\n"
    "s_waitcnt vmcnt(0)\n"
    "v_cmp_eq_u64 s[54:55], v[236:237], v[238:239]\n"
    "s_add_u32 s58, s58, 1\n"
    "s_cmp_eq_u64 s[54:55], -1\n"
    "s_cbranch_scc1 .LpollAVr\n"
    "s_cmp_lt_u32 s58, 0x80000\n"
    "s_cbranch_scc1 .LpollAV\n"
    "s_branch .LpollAVr\n"
    "s_nop 0\n"
    "s_nop 0\n"
    "s_nop 0\n"
    "s_nop 0\n"
    "s_nop 0\n"
    "s_nop 0\n"
    "s_nop 0\n"
    ".Lpoll1:\n"
    "s_sleep 1\n"
    "v_cmp_eq_u64 s[54:55], v[236:237], v[238:239]\n"
    "s_nop 1\n"
    "s_cmp_eq_u64 s[54:55], -1\n"
    "s_cbranch_scc1 .Lpoll1l\n"
    "buffer_load_dwordx2 v[236:237], v241, s[36:39], s81 offen offset:0 sc1\n"
    "s_waitcnt vmcnt(0)\n"
    "v_cmp_eq_u64 s[54:55], v[236:237], v[238:239]\n"
    ".Lpoll1l:\n"
    "ds_read_b32 v245, v247\n"
    "s_waitcnt lgkmcnt(0)\n"
    "v_cmp_le_u32 s[56:57], s59, v245\n"
    "s_add_u32 s58, s58, 1\n"
    "s_nop 1\n"
    "s_and_b64 s[54:55], s[54:55], s[56:57]\n"
    "s_cmp_eq_u64 s[54:55], -1\n"
    "s_cbranch_scc1 .Lpoll1r\n"
    "s_cmp_lt_u32 s58, 0x80000\n"
    "s_cbranch_scc1 .Lpoll1\n"
    "s_branch .Lpoll1r\n"
    "s_nop 0\n"
    "s_nop 0\n"
    "s_nop 0\n"
    "s_nop 0\n"
    "s_nop 0\n"
    "s_nop 0\n"
    "s_nop 0\n"
    ".Lpoll2:\n"
    "s_sleep 1\n"
    "v_cmp_eq_u64 s[54:55], v[236:237], v[238:239]\n"
    "s_nop 1\n"
    "s_cmp_eq_u64 s[54:55], -1\n"
    "s_cbranch_scc1 .Lpoll2l\n"
    "buffer_load_dwordx2 v[236:237], v241, s[36:39], s81 offen offset:0 sc1\n"
    "s_waitcnt vmcnt(0)\n"
    "v_cmp_eq_u64 s[54:55], v[236:237], v[238:239]\n"
    ".Lpoll2l:\n"
    "ds_read_b32 v245, v247\n"
    "s_waitcnt lgkmcnt(0)\n"
    "v_cmp_le_u32 s[56:57], s59, v245\n"
    "s_add_u32 s58, s58, 1\n"
    "s_nop 1\n"
    "s_and_b64 s[54:55], s[54:55], s[56:57]\n"
    "s_cmp_eq_u64 s[54:55], -1\n"
    "s_cbranch_scc1 .Lpoll2r\n"
    "s_cmp_lt_u32 s58, 0x80000\n"
    "s_cbranch_scc1 .Lpoll2\n"
    "s_branch .Lpoll2r\n"
    "s_nop 0\n"
    "s_nop 0\n"
    "s_nop 0\n"
    "s_nop 0\n"
    "s_nop 0\n"
    "s_nop 0\n"
    "s_nop 0\n"
    ".Lpoll3:\n"
    "s_sleep 1\n"
    "v_cmp_eq_u64 s[54:55], v[236:237], v[238:239]\n"
    "s_nop 1\n"
    "s_cmp_eq_u64 s[54:55], -1\n"
    "s_cbranch_scc1 .Lpoll3l\n"
    "buffer_load_dwordx2 v[236:237], v241, s[36:39], s81 offen offset:0 sc1\n"
    "s_waitcnt vmcnt(0)\n"
    "v_cmp_eq_u64 s[54:55], v[236:237], v[238:239]\n"
    ".Lpoll3l:\n"
    "ds_read_b32 v245, v247\n"
    "s_waitcnt lgkmcnt(0)\n"
    "v_cmp_le_u32 s[56:57], s59, v245\n"
    "s_add_u32 s58, s58, 1\n"
    "s_nop 1\n"
    "s_and_b64 s[54:55], s[54:55], s[56:57]\n"
    "s_cmp_eq_u64 s[54:55], -1\n"
    "s_cbranch_scc1 .Lpoll3r\n"
    "s_cmp_lt_u32 s58, 0x80000\n"
    "s_cbranch_scc1 .Lpoll3\n"
    "s_branch .Lpoll3r\n"
    "s_nop 0\n"
    "s_nop 0\n"
    "s_nop 0\n"
    "s_nop 0\n"
    "s_nop 0\n"
    "s_nop 0\n"
    "s_nop 0\n"
    ".Lpoll4:\n"
    "s_sleep 1\n"
    "v_cmp_eq_u64 s[54:55], v[236:237], v[238:239]\n"
    "s_nop 1\n"
    "s_cmp_eq_u64 s[54:55], -1\n"
    "s_cbranch_scc1 .Lpoll4l\n"
    "buffer_load_dwordx2 v[236:237], v241, s[36:39], s81 offen offset:0 sc1\n"
    "s_waitcnt vmcnt(0)\n"
    "v_cmp_eq_u64 s[54:55], v[236:237], v[238:239]\n"
    ".Lpoll4l:\n"
    "ds_read_b32 v245, v247\n"
    "s_waitcnt lgkmcnt(0)\n"
    "v_cmp_le_u32 s[56:57], s59, v245\n"
    "s_add_u32 s58, s58, 1\n"
    "s_nop 1\n"
    "s_and_b64 s[54:55], s[54:55], s[56:57]\n"
    "s_cmp_eq_u64 s[54:55], -1\n"
    "s_cbranch_scc1 .Lpoll4r\n"
    "s_cmp_lt_u32 s58, 0x80000\n"
    "s_cbranch_scc1 .Lpoll4\n"
    "s_branch .Lpoll4r\n"
    "s_nop 0\n"
    "s_nop 0\n"
    "s_nop 0\n"
    "s_nop 0\n"
    "s_nop 0\n"
    "s_nop 0\n"
    "s_nop 0\n"
    ".Lpt0:\n"
    "s_sleep 1\n"
    "v_cmp_eq_u64 s[54:55], v[236:237], v[238:239]\n"
    "s_nop 1\n"
    "s_cmp_eq_u64 s[54:55], -1\n"
    "s_cbranch_scc1 .Lpt0l\n"
    "buffer_load_dwordx2 v[236:237], v241, s[36:39], s81 offen offset:0 sc1\n"
    "s_waitcnt vmcnt(0)\n"
    "v_cmp_eq_u64 s[54:55], v[236:237], v[238:239]\n"
    ".Lpt0l:\n"
    "ds_read_b32 v245, v247\n"
    "s_waitcnt lgkmcnt(0)\n"
    "v_cmp_le_u32 s[56:57], s59, v245\n"
    "s_add_u32 s58, s58, 1\n"
    "s_nop 1\n"
    "s_and_b64 s[54:55], s[54:55], s[56:57]\n"
    "s_cmp_eq_u64 s[54:55], -1\n"
    "s_cbranch_scc1 .Lpt0r\n"
    "s_cmp_lt_u32 s58, 0x80000\n"
    "s_cbranch_scc1 .Lpt0\n"
    "s_branch .Lpt0r\n"
    "s_nop 0\n"
    "s_nop 0\n"
    "s_nop 0\n"
    ".Lpt1:\n"
    "s_sleep 1\n"
    "ds_read_b32 v245, v247\n"
    "s_waitcnt lgkmcnt(0)\n"
    "v_cmp_le_u32 s[56:57], s59, v245\n"
    "s_add_u32 s58, s58, 1\n"
    "s_nop 1\n"
    "s_cmp_eq_u64 s[56:57], -1\n"
    "s_cbranch_scc1 .Lpt1r\n"
    "s_cmp_lt_u32 s58, 0x80000\n"
    "s_cbranch_scc1 .Lpt1\n"
    "s_branch .Lpt1r\n"
    "s_nop 0\n"
    "s_nop 0\n"
    "s_nop 0\n"
    ".Lpt4:\n"
    "s_sleep 1\n"
    "ds_read_b32 v245, v247\n"
    "s_waitcnt lgkmcnt(0)\n"
    "v_cmp_le_u32 s[56:57], s59, v245\n"
    "s_add_u32 s58, s58, 1\n"
    "s_nop 1\n"
    "s_cmp_eq_u64 s[56:57], -1\n"
    "s_cbranch_scc1 .Lpt4r\n"
    "s_cmp_lt_u32 s58, 0x80000\n"
    "s_cbranch_scc1 .Lpt4\n"
    "s_branch .Lpt4r\n"
    "s_nop 0\n"
    ".Lpt3:\n"
    "s_sleep 1\n"
    "ds_read_b32 v245, v247\n"
    "s_waitcnt lgkmcnt(0)\n"
    "v_cmp_le_u32 s[56:57], s59, v245\n"
    "s_add_u32 s58, s58, 1\n"
    "s_nop 1\n"
    "s_cmp_eq_u64 s[56:57], -1\n"
    "s_cbranch_scc1 .Lpt3r\n"
    "s_cmp_lt_u32 s58, 0x80000\n"
    "s_cbranch_scc1 .Lpt3\n"
    "s_branch .Lpt3r\n"
    ".fill ((0x20000 - (.LwVe - .LwV) - (. - _Z8attn_fwdPKtPKiS2_S0_S0_S0_S0_PcPt)) & 4095) / 4, 4, 0xBF800000\n"
    ".LwV:\n"
    "s_branch .LwVe\n"
    ".LPV:\n"
    "v_mfma_f32_16x16x32_bf16 v[36:39], a[112:115], a[192:195], v[100:103]\n"
    "s_add_u32 s71, s71, s44\n"
    "ds_read_b128 a[136:139], v2 offset:13312\n"
    "v_cvt_pk_bf16_f32 v68, v4, v5\n"
    "v_mfma_f32_16x16x32_bf16 v[36:39], a[116:119], a[196:199], v[36:39]\n"
    "s_add_u32 s75, s71, s63\n"
    "ds_read_b128 a[140:143], v2 offset:15360\n"
    "v_cvt_pk_bf16_f32 v69, v6, v7\n"
    "v_mfma_f32_16x16x32_bf16 v[36:39], a[120:123], a[200:203], v[36:39]\n"
    "ds_read_b128 a[144:147], v2 offset:17408\n"
    "v_cvt_pk_bf16_f32 v76, v8, v9\n"
    "v_mfma_f32_16x16x32_bf16 v[36:39], a[124:127], a[204:207], v[36:39]\n"
    "ds_read_b128 a[148:151], v2 offset:19456\n"
    "v_cvt_pk_bf16_f32 v77, v10, v11\n"
    "v_mfma_f32_16x16x32_bf16 v[36:39], a[128:131], a[208:211], v[36:39]\n"
    "ds_read_b128 a[152:155], v2 offset:21504\n"
    "v_cvt_pk_bf16_f32 v84, v12, v13\n"
    "v_mfma_f32_16x16x32_bf16 v[36:39], a[132:135], a[212:215], v[36:39]\n"
    "ds_read_b128 a[156:159], v2 offset:23552\n"
    "v_cvt_pk_bf16_f32 v85, v14, v15\n"
    "v_mfma_f32_16x16x32_bf16 v[40:43], a[132:135], a[236:239], v[100:103]\n"
    "ds_read_b128 v[104:107], v250 offset:144\n"
    "v_cvt_pk_bf16_f32 v92, v16, v17\n"
    "v_mfma_f32_16x16x32_bf16 v[40:43], a[128:131], a[232:235], v[40:43]\n"
    "s_add_u32 s68, s68, 24576\n"
    "v_cvt_pk_bf16_f32 v93, v18, v19\n"
    "v_cvt_pk_bf16_f32 v70, v20, v21\n"
    "v_mfma_f32_16x16x32_bf16 v[40:43], a[124:127], a[228:231], v[40:43]\n"
    "s_cmp_ge_u32 s68, s69\n"
    "s_cselect_b32 s68, s65, s68\n"
    "v_cvt_pk_bf16_f32 v71, v22, v23\n"
    "v_mfma_f32_16x16x32_bf16 v[40:43], a[120:123], a[224:227], v[40:43]\n"
    "v_cvt_pk_bf16_f32 v78, v24, v25\n"
    "v_cvt_pk_bf16_f32 v79, v26, v27\n"
    "v_mfma_f32_16x16x32_bf16 v[40:43], a[116:119], a[220:223], v[40:43]\n"
    "v_cvt_pk_bf16_f32 v86, v28, v29\n"
    "v_cvt_pk_bf16_f32 v87, v30, v31\n"
    "v_mfma_f32_16x16x32_bf16 v[40:43], a[112:115], a[216:219], v[40:43]\n"
    "v_cvt_pk_bf16_f32 v94, v32, v33\n"
    "v_cvt_pk_bf16_f32 v95, v34, v35\n"
    "v_mfma_f32_16x16x32_bf16 v[44:47], a[112:115], a[240:243], v[100:103]\n"
    "v_exp_f32 v36, v36\n"
    "v_mfma_f32_16x16x32_bf16 v[44:47], a[116:119], a[244:247], v[44:47]\n"
    "v_exp_f32 v37, v37\n"
    "v_mfma_f32_16x16x32_bf16 v[44:47], a[120:123], a[248:251], v[44:47]\n"
    "v_exp_f32 v38, v38\n"
    "v_mfma_f32_16x16x32_bf16 v[44:47], a[124:127], a[252:255], v[44:47]\n"
    "v_exp_f32 v39, v39\n"
    "v_mfma_f32_16x16x32_bf16 v[44:47], a[128:131], a[160:163], v[44:47]\n"
    "v_cvt_pk_bf16_f32 v72, v36, v37\n"
    "v_cvt_pk_bf16_f32 v73, v38, v39\n"
    "v_mfma_f32_16x16x32_bf16 v[44:47], a[132:135], a[164:167], v[44:47]\n"
    "v_exp_f32 v40, v40\n"
    "v_mfma_f32_16x16x32_bf16 v[48:51], a[132:135], a[188:191], v[100:103]\n"
    "ds_read_b32 v245, v247\n"
    "v_mfma_f32_16x16x32_bf16 v[48:51], a[128:131], a[184:187], v[48:51]\n"
    "s_add_u32 m0, s68, 0\n"
    "v_exp_f32 v41, v41\n"
    "v_mfma_f32_16x16x32_bf16 v[48:51], a[124:127], a[180:183], v[48:51]\n"
    "v_exp_f32 v42, v42\n"
    "v_mfma_f32_16x16x32_bf16 v[48:51], a[120:123], a[176:179], v[48:51]\n"
    "v_exp_f32 v43, v43\n"
    "v_mfma_f32_16x16x32_bf16 v[48:51], a[116:119], a[172:175], v[48:51]\n"
    "s_waitcnt vmcnt(13)\n"
    "v_cmp_eq_u64 s[54:55], v[236:237], v[238:239]\n"
    "v_cvt_pk_bf16_f32 v80, v40, v41\n"
    "v_mfma_f32_16x16x32_bf16 v[48:51], a[112:115], a[168:171], v[48:51]\n"
    "s_waitcnt lgkmcnt(1)\n"
    "v_cvt_pk_bf16_f32 v81, v42, v43\n"
    "v_mfma_f32_16x16x32_bf16 v[52:55], a[136:139], a[192:195], v[104:107]\n"
    "s_waitcnt lgkmcnt(0)\n"
    "v_cmp_le_u32 s[56:57], s59, v245\n"
    "v_mfma_f32_16x16x32_bf16 v[52:55], a[140:143], a[196:199], v[52:55]\n"
    "v_exp_f32 v44, v44\n"
    "v_mfma_f32_16x16x32_bf16 v[52:55], a[144:147], a[200:203], v[52:55]\n"
    "s_and_b64 s[54:55], s[54:55], s[56:57]\n"
    "s_cmp_eq_u64 s[54:55], -1\n"
    "v_mfma_f32_16x16x32_bf16 v[52:55], a[148:151], a[204:207], v[52:55]\n"
    "s_cbranch_scc0 .Lpoll3\n"
    ".Lpoll3r:\n"
    "v_mfma_f32_16x16x32_bf16 v[52:55], a[152:155], a[208:211], v[52:55]\n"
    "buffer_load_dwordx4 v1, s[16:19], s67 offen offset:0 sc1 lds\n"
    "v_mfma_f32_16x16x32_bf16 v[52:55], a[156:159], a[212:215], v[52:55]\n"
    "ds_read_b128 a[112:115], v3 offset:0\n"
    "v_mfma_f32_16x16x32_bf16 v[56:59], a[156:159], a[236:239], v[104:107]\n"
    "ds_read_b128 a[116:119], v3 offset:2048\n"
    "v_mfma_f32_16x16x32_bf16 v[56:59], a[152:155], a[232:235], v[56:59]\n"
    "ds_read_b128 a[120:123], v3 offset:4096\n"
    "v_mfma_f32_16x16x32_bf16 v[56:59], a[148:151], a[228:231], v[56:59]\n"
    "buffer_load_dwordx4 v1, s[16:19], s67 offen offset:1024 sc1 lds\n"
    "v_mfma_f32_16x16x32_bf16 v[56:59], a[144:147], a[224:227], v[56:59]\n"
    "ds_read_b128 a[124:127], v3 offset:6144\n"
    "v_mfma_f32_16x16x32_bf16 v[56:59], a[140:143], a[220:223], v[56:59]\n"
    "ds_read_b128 a[128:131], v3 offset:8192\n"
    "v_mfma_f32_16x16x32_bf16 v[56:59], a[136:139], a[216:219], v[56:59]\n"
    "ds_read_b128 a[132:135], v3 offset:10240\n"
    "v_mfma_f32_16x16x32_bf16 v[60:63], a[136:139], a[240:243], v[104:107]\n"
    "buffer_load_dwordx4 v1, s[16:19], s67 offen offset:2048 sc1 lds\n"
    "v_mfma_f32_16x16x32_bf16 v[60:63], a[140:143], a[244:247], v[60:63]\n"
    "ds_read_b128 v[100:103], v250 offset:768\n"
    "v_mfma_f32_16x16x32_bf16 v[60:63], a[144:147], a[248:251], v[60:63]\n"
    "v_exp_f32 v52, v52\n"
    "v_mfma_f32_16x16x32_bf16 v[60:63], a[148:151], a[252:255], v[60:63]\n"
    "v_exp_f32 v53, v53\n"
    "v_mfma_f32_16x16x32_bf16 v[60:63], a[152:155], a[160:163], v[60:63]\n"
    "buffer_load_dwordx4 v1, s[16:19], s67 offen offset:3072 sc1 lds\n"
    "v_mfma_f32_16x16x32_bf16 v[60:63], a[156:159], a[164:167], v[60:63]\n"
    "s_add_u32 m0, s68, 4096\n"
    "v_exp_f32 v54, v54\n"
    "v_mfma_f32_16x16x32_bf16 v[64:67], a[156:159], a[188:191], v[104:107]\n"
    "s_add_u32 s83, s67, 4096\n"
    "v_exp_f32 v55, v55\n"
    "v_mfma_f32_16x16x32_bf16 v[64:67], a[152:155], a[184:187], v[64:67]\n"
    "v_cvt_pk_bf16_f32 v74, v52, v53\n"
    "v_cvt_pk_bf16_f32 v75, v54, v55\n"
    "v_mfma_f32_16x16x32_bf16 v[64:67], a[148:151], a[180:183], v[64:67]\n"
    "buffer_load_dwordx4 v1, s[16:19], s83 offen offset:0 sc1 lds\n"
    "v_mfma_f32_16x16x32_bf16 v[64:67], a[144:147], a[176:179], v[64:67]\n"
    "v_exp_f32 v56, v56\n"
    "v_mfma_f32_16x16x32_bf16 v[64:67], a[140:143], a[172:175], v[64:67]\n"
    "v_exp_f32 v57, v57\n"
    "v_mfma_f32_16x16x32_bf16 v[64:67], a[136:139], a[168:171], v[64:67]\n"
    "s_waitcnt lgkmcnt(0)\n"
    "v_exp_f32 v58, v58\n"
    "v_mfma_f32_16x16x32_bf16 v[4:7], a[112:115], a[192:195], v[100:103]\n"
    "buffer_load_dwordx4 v1, s[16:19], s83 offen offset:1024 sc1 lds\n"
    "v_mfma_f32_16x16x32_bf16 v[4:7], a[116:119], a[196:199], v[4:7]\n"
    "s_add_u32 m0, s64, 0\n"
    "ds_read_b128 a[136:139], v3 offset:1024\n"
    "v_cvt_pk_bf16_f32 v82, v56, v57\n"
    "v_mfma_f32_16x16x32_bf16 v[4:7], a[120:123], a[200:203], v[4:7]\n"
    "s_add_u32 s83, s67, 24576\n"
    "ds_read_b128 a[140:143], v3 offset:3072\n"
    "v_mfma_f32_16x16x32_bf16 v[4:7], a[124:127], a[204:207], v[4:7]\n"
    "ds_read_b128 a[144:147], v3 offset:5120\n"
    "v_mfma_f32_16x16x32_bf16 v[4:7], a[128:131], a[208:211], v[4:7]\n"
    "buffer_load_dwordx4 v1, s[16:19], s83 offen offset:0 sc1 lds\n"
    "v_mfma_f32_16x16x32_bf16 v[4:7], a[132:135], a[212:215], v[4:7]\n"
    "ds_read_b128 a[148:151], v3 offset:7168\n"
    "v_mfma_f32_16x16x32_bf16 v[8:11], a[132:135], a[236:239], v[100:103]\n"
    "ds_read_b128 a[152:155], v3 offset:9216\n"
    "v_mfma_f32_16x16x32_bf16 v[8:11], a[128:131], a[232:235], v[8:11]\n"
    "ds_read_b128 a[156:159], v3 offset:11264\n"
    "v_mfma_f32_16x16x32_bf16 v[8:11], a[124:127], a[228:231], v[8:11]\n"
    "buffer_load_dwordx4 v1, s[16:19], s83 offen offset:1024 sc1 lds\n"
    "v_mfma_f32_16x16x32_bf16 v[8:11], a[120:123], a[224:227], v[8:11]\n"
    "ds_read_b128 v[104:107], v250 offset:784\n"
    "v_mfma_f32_16x16x32_bf16 v[8:11], a[116:119], a[220:223], v[8:11]\n"
    "v_exp_f32 v59, v59\n"
    "v_mfma_f32_16x16x32_bf16 v[8:11], a[112:115], a[216:219], v[8:11]\n"
    "v_cvt_pk_bf16_f32 v83, v58, v59\n"
    "v_mfma_f32_16x16x32_bf16 v[12:15], a[112:115], a[240:243], v[100:103]\n"
    "buffer_load_dwordx4 v1, s[16:19], s83 offen offset:2048 sc1 lds\n"
    "v_mfma_f32_16x16x32_bf16 v[12:15], a[116:119], a[244:247], v[12:15]\n"
    "v_exp_f32 v45, v45\n"
    "v_mfma_f32_16x16x32_bf16 v[12:15], a[120:123], a[248:251], v[12:15]\n"
    "v_exp_f32 v46, v46\n"
    "v_mfma_f32_16x16x32_bf16 v[12:15], a[124:127], a[252:255], v[12:15]\n"
    "v_exp_f32 v47, v47\n"
    "v_mfma_f32_16x16x32_bf16 v[12:15], a[128:131], a[160:163], v[12:15]\n"
    "buffer_load_dwordx4 v1, s[16:19], s83 offen offset:3072 sc1 lds\n"
    "v_mfma_f32_16x16x32_bf16 v[12:15], a[132:135], a[164:167], v[12:15]\n"
    "s_add_u32 m0, s64, 4096\n"
    "v_exp_f32 v60, v60\n"
    "v_mfma_f32_16x16x32_bf16 v[16:19], a[132:135], a[188:191], v[100:103]\n"
    "s_add_u32 s83, s67, 28672\n"
    "v_exp_f32 v61, v61\n"
    "v_mfma_f32_16x16x32_bf16 v[16:19], a[128:131], a[184:187], v[16:19]\n"
    "v_exp_f32 v62, v62\n"
    "v_mfma_f32_16x16x32_bf16 v[16:19], a[124:127], a[180:183], v[16:19]\n"
    "buffer_load_dwordx4 v1, s[16:19], s83 offen offset:0 sc1 lds\n"
    "v_mfma_f32_16x16x32_bf16 v[16:19], a[120:123], a[176:179], v[16:19]\n"
    "v_exp_f32 v63, v63\n"
    "v_mfma_f32_16x16x32_bf16 v[16:19], a[116:119], a[172:175], v[16:19]\n"
    "v_cvt_pk_bf16_f32 v88, v44, v45\n"
    "v_cvt_pk_bf16_f32 v89, v46, v47\n"
    "v_mfma_f32_16x16x32_bf16 v[16:19], a[112:115], a[168:171], v[16:19]\n"
    "s_waitcnt lgkmcnt(0)\n"
    "v_cvt_pk_bf16_f32 v90, v60, v61\n"
    "v_cvt_pk_bf16_f32 v91, v62, v63\n"
    "v_mfma_f32_16x16x32_bf16 v[20:23], a[136:139], a[192:195], v[104:107]\n"
    "buffer_load_dwordx4 v1, s[16:19], s83 offen offset:1024 sc1 lds\n"
    "v_mfma_f32_16x16x32_bf16 v[20:23], a[140:143], a[196:199], v[20:23]\n"
    "s_add_u32 s67, s67, 49152\n"
    "ds_read_b128 a[112:115], v248 offset:0\n"
    "v_mfma_f32_16x16x32_bf16 v[20:23], a[144:147], a[200:203], v[20:23]\n"
    "s_add_u32 s59, s59, 4\n"
    "ds_read_b128 a[116:119], v248 offset:12288\n"
    "v_mfma_f32_16x16x32_bf16 v[20:23], a[148:151], a[204:207], v[20:23]\n"
    "s_waitcnt vmcnt(12)\n"
    "ds_read_b128 a[120:123], v248 offset:1024\n"
    "v_mfma_f32_16x16x32_bf16 v[20:23], a[152:155], a[208:211], v[20:23]\n"
    "buffer_store_dwordx2 v[238:239], v240, s[36:39], s80 offen offset:0 sc1\n"
    "v_mfma_f32_16x16x32_bf16 v[20:23], a[156:159], a[212:215], v[20:23]\n"
    "buffer_load_dwordx4 v[204:207], v242, s[40:43], s75 offen offset:0 nt\n"
    "v_mfma_f32_16x16x32_bf16 v[24:27], a[156:159], a[236:239], v[104:107]\n"
    "s_add_u32 s80, s80, 256\n"
    "ds_read_b128 a[124:127], v248 offset:13312\n"
    "v_mfma_f32_16x16x32_bf16 v[24:27], a[152:155], a[232:235], v[24:27]\n"
    "buffer_load_dwordx4 v[208:211], v242, s[40:43], s75 offen offset:768 nt\n"
    "v_mfma_f32_16x16x32_bf16 v[24:27], a[148:151], a[228:231], v[24:27]\n"
    "ds_read_b128 a[128:131], v248 offset:2048\n"
    "v_mfma_f32_16x16x32_bf16 v[24:27], a[144:147], a[224:227], v[24:27]\n"
    "buffer_load_dwordx4 v[212:215], v242, s[40:43], s75 offen offset:256 nt\n"
    "v_mfma_f32_16x16x32_bf16 v[24:27], a[140:143], a[220:223], v[24:27]\n"
    "ds_read_b128 a[132:135], v248 offset:14336\n"
    "v_mfma_f32_16x16x32_bf16 v[24:27], a[136:139], a[216:219], v[24:27]\n"
    "buffer_load_dwordx4 v[216:219], v242, s[40:43], s75 offen offset:1024 nt\n"
    "v_mfma_f32_16x16x32_bf16 v[28:31], a[136:139], a[240:243], v[104:107]\n"
    "v_exp_f32 v48, v48\n"
    "v_mfma_f32_16x16x32_bf16 v[28:31], a[140:143], a[244:247], v[28:31]\n"
    "buffer_load_dwordx4 v[220:223], v242, s[40:43], s75 offen offset:512 nt\n"
    "v_mfma_f32_16x16x32_bf16 v[28:31], a[144:147], a[248:251], v[28:31]\n"
    "v_exp_f32 v49, v49\n"
    "v_mfma_f32_16x16x32_bf16 v[28:31], a[148:151], a[252:255], v[28:31]\n"
    "buffer_load_dwordx4 v[224:227], v242, s[40:43], s75 offen offset:1280 nt\n"
    "v_mfma_f32_16x16x32_bf16 v[28:31], a[152:155], a[160:163], v[28:31]\n"
    "v_exp_f32 v50, v50\n"
    "v_mfma_f32_16x16x32_bf16 v[28:31], a[156:159], a[164:167], v[28:31]\n"
    "v_exp_f32 v51, v51\n"
    "v_mfma_f32_16x16x32_bf16 v[32:35], a[156:159], a[188:191], v[104:107]\n"
    "v_exp_f32 v64, v64\n"
    "v_mfma_f32_16x16x32_bf16 v[32:35], a[152:155], a[184:187], v[32:35]\n"
    "v_exp_f32 v65, v65\n"
    "v_mfma_f32_16x16x32_bf16 v[32:35], a[148:151], a[180:183], v[32:35]\n"
    "v_exp_f32 v66, v66\n"
    "v_mfma_f32_16x16x32_bf16 v[32:35], a[144:147], a[176:179], v[32:35]\n"
    "v_exp_f32 v67, v67\n"
    "v_mfma_f32_16x16x32_bf16 v[32:35], a[140:143], a[172:175], v[32:35]\n"
    "v_cvt_pk_bf16_f32 v96, v48, v49\n"
    "v_cvt_pk_bf16_f32 v97, v50, v51\n"
    "v_mfma_f32_16x16x32_bf16 v[32:35], a[136:139], a[168:171], v[32:35]\n"
    "s_waitcnt lgkmcnt(0)\n"
    "v_cvt_pk_bf16_f32 v98, v64, v65\n"
    "v_cvt_pk_bf16_f32 v99, v66, v67\n"
    "v_mfma_f32_16x16x32_bf16 v[124:127], a[112:115], v[68:71], v[124:127]\n"
    "ds_read_b128 a[136:139], v248 offset:3072\n"
    "v_mfma_f32_16x16x32_bf16 v[124:127], a[116:119], v[72:75], v[124:127]\n"
    "ds_read_b128 a[140:143], v248 offset:15360\n"
    "v_mfma_f32_16x16x32_bf16 v[128:131], a[124:127], v[72:75], v[128:131]\n"
    "ds_read_b128 a[144:147], v248 offset:4096\n"
    "v_mfma_f32_16x16x32_bf16 v[128:131], a[120:123], v[68:71], v[128:131]\n"
    "ds_read_b128 a[148:151], v248 offset:16384\n"
    "v_mfma_f32_16x16x32_bf16 v[132:135], a[128:131], v[68:71], v[132:135]\n"
    "ds_read_b128 a[152:155], v248 offset:5120\n"
    "v_mfma_f32_16x16x32_bf16 v[132:135], a[132:135], v[72:75], v[132:135]\n"
    "ds_read_b128 a[156:159], v248 offset:17408\n"
    "v_mfma_f32_16x16x32_bf16 v[148:151], a[132:135], v[80:83], v[148:151]\n"
    "ds_read_b128 v[104:107], v251 offset:0\n"
    "v_mfma_f32_16x16x32_bf16 v[148:151], a[128:131], v[76:79], v[148:151]\n"
    "v_exp_f32 v4, v4\n"
    "v_mfma_f32_16x16x32_bf16 v[144:147], a[120:123], v[76:79], v[144:147]\n"
    "v_exp_f32 v5, v5\n"
    "v_mfma_f32_16x16x32_bf16 v[144:147], a[124:127], v[80:83], v[144:147]\n"
    "v_exp_f32 v6, v6\n"
    "v_mfma_f32_16x16x32_bf16 v[140:143], a[116:119], v[80:83], v[140:143]\n"
    "v_mfma_f32_16x16x32_bf16 v[140:143], a[112:115], v[76:79], v[140:143]\n"
    "v_mfma_f32_16x16x32_bf16 v[156:159], a[112:115], v[84:87], v[156:159]\n"
    "v_exp_f32 v7, v7\n"
    "v_mfma_f32_16x16x32_bf16 v[156:159], a[116:119], v[88:91], v[156:159]\n"
    "v_mfma_f32_16x16x32_bf16 v[160:163], a[124:127], v[88:91], v[160:163]\n"
    "v_exp_f32 v8, v8\n"
    "v_mfma_f32_16x16x32_bf16 v[160:163], a[120:123], v[84:87], v[160:163]\n"
    "v_mfma_f32_16x16x32_bf16 v[164:167], a[128:131], v[84:87], v[164:167]\n"
    "v_mfma_f32_16x16x32_bf16 v[164:167], a[132:135], v[88:91], v[164:167]\n"
    "v_exp_f32 v9, v9\n"
    "v_mfma_f32_16x16x32_bf16 v[180:183], a[132:135], v[96:99], v[180:183]\n"
    "v_mfma_f32_16x16x32_bf16 v[180:183], a[128:131], v[92:95], v[180:183]\n"
    "v_mfma_f32_16x16x32_bf16 v[176:179], a[120:123], v[92:95], v[176:179]\n"
    "v_exp_f32 v10, v10\n"
    "v_mfma_f32_16x16x32_bf16 v[176:179], a[124:127], v[96:99], v[176:179]\n"
    "v_mfma_f32_16x16x32_bf16 v[172:175], a[116:119], v[96:99], v[172:175]\n"
    "v_exp_f32 v11, v11\n"
    "v_mfma_f32_16x16x32_bf16 v[172:175], a[112:115], v[92:95], v[172:175]\n"
    "s_waitcnt lgkmcnt(0)\n"
    "v_mfma_f32_16x16x32_bf16 v[136:139], a[136:139], v[68:71], v[136:139]\n"
    "ds_read_b128 a[112:115], v248 offset:6144\n"
    "v_mfma_f32_16x16x32_bf16 v[136:139], a[140:143], v[72:75], v[136:139]\n"
    "ds_read_b128 a[116:119], v248 offset:18432\n"
    "v_mfma_f32_16x16x32_bf16 v[188:191], a[148:151], v[72:75], v[188:191]\n"
    "ds_read_b128 a[120:123], v248 offset:7168\n"
    "v_mfma_f32_16x16x32_bf16 v[188:191], a[144:147], v[68:71], v[188:191]\n"
    "ds_read_b128 a[124:127], v248 offset:19456\n"
    "v_mfma_f32_16x16x32_bf16 v[192:195], a[152:155], v[68:71], v[192:195]\n"
    "ds_read_b128 a[128:131], v248 offset:8192\n"
    "v_mfma_f32_16x16x32_bf16 v[192:195], a[156:159], v[72:75], v[192:195]\n"
    "ds_read_b128 a[132:135], v248 offset:20480\n"
    "v_smfmac_f32_16x16x64_bf16 v[108:111], v[104:107], v[68:75], v253\n"
    "s_add_u32 s81, s81, 128\n"
    "s_cmp_gt_u32 s81, 4096\n"
    "s_cselect_b32 s81, 0, s81\n"
    "v_smfmac_f32_16x16x64_bf16 v[112:115], v[104:107], v[76:83], v253\n"
    "v_mfma_f32_16x16x32_bf16 a[4:7], a[156:159], v[80:83], a[4:7]\n"
    "v_exp_f32 v12, v12\n"
    "v_mfma_f32_16x16x32_bf16 a[4:7], a[152:155], v[76:79], a[4:7]\n"
    "v_exp_f32 v13, v13\n"
    "v_mfma_f32_16x16x32_bf16 a[0:3], a[144:147], v[76:79], a[0:3]\n"
    "v_exp_f32 v14, v14\n"
    "v_mfma_f32_16x16x32_bf16 a[0:3], a[148:151], v[80:83], a[0:3]\n"
    "v_exp_f32 v15, v15\n"
    "v_mfma_f32_16x16x32_bf16 v[152:155], a[140:143], v[80:83], v[152:155]\n"
    "v_exp_f32 v16, v16\n"
    "v_mfma_f32_16x16x32_bf16 v[152:155], a[136:139], v[76:79], v[152:155]\n"
    "v_mfma_f32_16x16x32_bf16 v[168:171], a[136:139], v[84:87], v[168:171]\n"
    "v_exp_f32 v17, v17\n"
    "v_mfma_f32_16x16x32_bf16 v[168:171], a[140:143], v[88:91], v[168:171]\n"
    "v_mfma_f32_16x16x32_bf16 a[16:19], a[148:151], v[88:91], a[16:19]\n"
    "v_mfma_f32_16x16x32_bf16 a[16:19], a[144:147], v[84:87], a[16:19]\n"
    "v_exp_f32 v18, v18\n"
    "v_mfma_f32_16x16x32_bf16 a[20:23], a[152:155], v[84:87], a[20:23]\n"
    "v_mfma_f32_16x16x32_bf16 a[20:23], a[156:159], v[88:91], a[20:23]\n"
    "v_smfmac_f32_16x16x64_bf16 v[116:119], v[104:107], v[84:91], v253\n"
    "s_add_u32 s52, s52, s45\n"
    "v_smfmac_f32_16x16x64_bf16 v[120:123], v[104:107], v[92:99], v253\n"
    "v_mfma_f32_16x16x32_bf16 a[36:39], a[156:159], v[96:99], a[36:39]\n"
    "v_exp_f32 v19, v19\n"
    "v_mfma_f32_16x16x32_bf16 a[36:39], a[152:155], v[92:95], a[36:39]\n"
    "v_exp_f32 v20, v20\n"
    "v_mfma_f32_16x16x32_bf16 a[32:35], a[144:147], v[92:95], a[32:35]\n"
    "v_mfma_f32_16x16x32_bf16 a[32:35], a[148:151], v[96:99], a[32:35]\n"
    "v_exp_f32 v21, v21\n"
    "v_mfma_f32_16x16x32_bf16 v[184:187], a[140:143], v[96:99], v[184:187]\n"
    "v_mfma_f32_16x16x32_bf16 v[184:187], a[136:139], v[92:95], v[184:187]\n"
    "s_waitcnt lgkmcnt(0)\n"
    "v_mfma_f32_16x16x32_bf16 v[196:199], a[112:115], v[68:71], v[196:199]\n"
    "ds_read_b128 a[136:139], v248 offset:9216\n"
    "v_mfma_f32_16x16x32_bf16 v[196:199], a[116:119], v[72:75], v[196:199]\n"
    "ds_read_b128 a[140:143], v248 offset:21504\n"
    "v_mfma_f32_16x16x32_bf16 v[200:203], a[124:127], v[72:75], v[200:203]\n"
    "ds_read_b128 a[144:147], v248 offset:10240\n"
    "v_mfma_f32_16x16x32_bf16 v[200:203], a[120:123], v[68:71], v[200:203]\n"
    "ds_read_b128 a[148:151], v248 offset:22528\n"
    "v_mfma_f32_16x16x32_bf16 a[48:51], a[128:131], v[68:71], a[48:51]\n"
    "buffer_load_dwordx2 v[236:237], v241, s[36:39], s81 offen offset:0 sc1\n"
    "v_mfma_f32_16x16x32_bf16 a[48:51], a[132:135], v[72:75], a[48:51]\n"
    "ds_read_b128 a[152:155], v248 offset:11264\n"
    "v_mfma_f32_16x16x32_bf16 a[64:67], a[132:135], v[80:83], a[64:67]\n"
    "ds_read_b128 a[156:159], v248 offset:23552\n"
    "v_mfma_f32_16x16x32_bf16 a[64:67], a[128:131], v[76:79], a[64:67]\n"
    "v_exp_f32 v22, v22\n"
    "v_mfma_f32_16x16x32_bf16 a[12:15], a[120:123], v[76:79], a[12:15]\n"
    "v_exp_f32 v23, v23\n"
    "v_mfma_f32_16x16x32_bf16 a[12:15], a[124:127], v[80:83], a[12:15]\n"
    "v_exp_f32 v24, v24\n"
    "v_mfma_f32_16x16x32_bf16 a[8:11], a[116:119], v[80:83], a[8:11]\n"
    "v_exp_f32 v25, v25\n"
    "v_mfma_f32_16x16x32_bf16 a[8:11], a[112:115], v[76:79], a[8:11]\n"
    "v_exp_f32 v26, v26\n"
    "v_mfma_f32_16x16x32_bf16 a[24:27], a[112:115], v[84:87], a[24:27]\n"
    "v_mfma_f32_16x16x32_bf16 a[24:27], a[116:119], v[88:91], a[24:27]\n"
    "v_exp_f32 v27, v27\n"
    "v_mfma_f32_16x16x32_bf16 a[28:31], a[124:127], v[88:91], a[28:31]\n"
    "v_mfma_f32_16x16x32_bf16 a[28:31], a[120:123], v[84:87], a[28:31]\n"
    "v_mfma_f32_16x16x32_bf16 a[80:83], a[128:131], v[84:87], a[80:83]\n"
    "v_exp_f32 v28, v28\n"
    "v_mfma_f32_16x16x32_bf16 a[80:83], a[132:135], v[88:91], a[80:83]\n"
    "v_mfma_f32_16x16x32_bf16 a[96:99], a[132:135], v[96:99], a[96:99]\n"
    "v_mfma_f32_16x16x32_bf16 a[96:99], a[128:131], v[92:95], a[96:99]\n"
    "v_exp_f32 v29, v29\n"
    "v_mfma_f32_16x16x32_bf16 a[44:47], a[120:123], v[92:95], a[44:47]\n"
    "v_mfma_f32_16x16x32_bf16 a[44:47], a[124:127], v[96:99], a[44:47]\n"
    "v_exp_f32 v30, v30\n"
    "v_mfma_f32_16x16x32_bf16 a[40:43], a[116:119], v[96:99], a[40:43]\n"
    "v_mfma_f32_16x16x32_bf16 a[40:43], a[112:115], v[92:95], a[40:43]\n"
    "s_waitcnt lgkmcnt(0)\n"
    "v_mfma_f32_16x16x32_bf16 a[52:55], a[136:139], v[68:71], a[52:55]\n"
    "ds_read_b128 a[112:115], v3 offset:12288\n"
    "v_mfma_f32_16x16x32_bf16 a[52:55], a[140:143], v[72:75], a[52:55]\n"
    "ds_read_b128 a[116:119], v3 offset:14336\n"
    "v_mfma_f32_16x16x32_bf16 a[56:59], a[148:151], v[72:75], a[56:59]\n"
    "ds_read_b128 a[120:123], v3 offset:16384\n"
    "v_mfma_f32_16x16x32_bf16 a[56:59], a[144:147], v[68:71], a[56:59]\n"
    "ds_read_b128 a[124:127], v3 offset:18432\n"
    "v_mfma_f32_16x16x32_bf16 a[60:63], a[152:155], v[68:71], a[60:63]\n"
    "ds_read_b128 a[128:131], v3 offset:20480\n"
    "v_mfma_f32_16x16x32_bf16 a[60:63], a[156:159], v[72:75], a[60:63]\n"
    "ds_read_b128 a[132:135], v3 offset:22528\n"
    "v_mfma_f32_16x16x32_bf16 a[76:79], a[156:159], v[80:83], a[76:79]\n"
    "ds_read_b128 v[100:103], v250 offset:896\n"
    "v_mfma_f32_16x16x32_bf16 a[76:79], a[152:155], v[76:79], a[76:79]\n"
    "v_exp_f32 v31, v31\n"
    "v_mfma_f32_16x16x32_bf16 a[72:75], a[144:147], v[76:79], a[72:75]\n"
    "v_exp_f32 v32, v32\n"
    "v_mfma_f32_16x16x32_bf16 a[72:75], a[148:151], v[80:83], a[72:75]\n"
    "v_exp_f32 v33, v33\n"
    "v_mfma_f32_16x16x32_bf16 a[68:71], a[140:143], v[80:83], a[68:71]\n"
    "v_exp_f32 v34, v34\n"
    "v_mfma_f32_16x16x32_bf16 a[68:71], a[136:139], v[76:79], a[68:71]\n"
    "v_exp_f32_e64 v35, v35\n"
    "v_mfma_f32_16x16x32_bf16 a[84:87], a[136:139], v[84:87], a[84:87]\n"
    "v_mfma_f32_16x16x32_bf16 a[84:87], a[140:143], v[88:91], a[84:87]\n"
    "v_mfma_f32_16x16x32_bf16 a[88:91], a[148:151], v[88:91], a[88:91]\n"
    "s_waitcnt vmcnt(8)\n"
    "ds_add_u32 v247, v246\n"
    "v_mfma_f32_16x16x32_bf16 a[88:91], a[144:147], v[84:87], a[88:91]\n"
    "v_mfma_f32_16x16x32_bf16 a[92:95], a[152:155], v[84:87], a[92:95]\n"
    "v_mfma_f32_16x16x32_bf16 a[92:95], a[156:159], v[88:91], a[92:95]\n"
    "v_mfma_f32_16x16x32_bf16 a[108:111], a[156:159], v[96:99], a[108:111]\n"
    "v_mfma_f32_16x16x32_bf16 a[108:111], a[152:155], v[92:95], a[108:111]\n"
    "v_mfma_f32_16x16x32_bf16 a[104:107], a[144:147], v[92:95], a[104:107]\n"
    "s_sub_u32 s74, s68, s65\n"
    "v_mfma_f32_16x16x32_bf16 a[104:107], a[148:151], v[96:99], a[104:107]\n"
    "s_add_u32 s66, s66, .Lend-.Lend2+(1)\n"
    "v_mov_b32_e64 v2, v3\n"
    "v_add_u32_e64 v3, s74, v252\n"
    "v_mfma_f32_16x16x32_bf16 a[100:103], a[140:143], v[96:99], a[100:103]\n"
    "s_waitcnt lgkmcnt(1)\n"
    "s_cmp_eq_u32 s66, .Lend-.Lend2+(29)\n"
    "v_mfma_f32_16x16x32_bf16 a[100:103], a[136:139], v[92:95], a[100:103]\n"
    ".LwVe:\n"
    "s_cbranch_scc1 .LtailP\n"
    ".LNV:\n"
    "v_mfma_f32_16x16x32_bf16 v[36:39], a[112:115], a[192:195], v[100:103]\n"
    "ds_read_b128 a[136:139], v2 offset:13312\n"
    "v_cvt_pk_bf16_f32 v68, v4, v5\n"
    "v_mfma_f32_16x16x32_bf16 v[36:39], a[116:119], a[196:199], v[36:39]\n"
    "ds_read_b128 a[140:143], v2 offset:15360\n"
    "v_mfma_f32_16x16x32_bf16 v[36:39], a[120:123], a[200:203], v[36:39]\n"
    "ds_read_b128 a[144:147], v2 offset:17408\n"
    "v_mfma_f32_16x16x32_bf16 v[36:39], a[124:127], a[204:207], v[36:39]\n"
    "ds_read_b128 a[148:151], v2 offset:19456\n"
    "v_cvt_pk_bf16_f32 v69, v6, v7\n"
    "v_mfma_f32_16x16x32_bf16 v[36:39], a[128:131], a[208:211], v[36:39]\n"
    "ds_read_b128 a[152:155], v2 offset:21504\n"
    "v_mfma_f32_16x16x32_bf16 v[36:39], a[132:135], a[212:215], v[36:39]\n"
    "ds_read_b128 a[156:159], v2 offset:23552\n"
    "v_mfma_f32_16x16x32_bf16 v[40:43], a[132:135], a[236:239], v[100:103]\n"
    "ds_read_b128 v[104:107], v250 offset:912\n"
    "v_mfma_f32_16x16x32_bf16 v[40:43], a[128:131], a[232:235], v[40:43]\n"
    "s_add_u32 s68, s68, 24576\n"
    "v_cvt_pk_bf16_f32 v76, v8, v9\n"
    "v_mfma_f32_16x16x32_bf16 v[40:43], a[124:127], a[228:231], v[40:43]\n"
    "s_cmp_ge_u32 s68, s69\n"
    "s_cselect_b32 s68, s65, s68\n"
    "v_mfma_f32_16x16x32_bf16 v[40:43], a[120:123], a[224:227], v[40:43]\n"
    "v_exp_f32 v36, v36\n"
    "v_mfma_f32_16x16x32_bf16 v[40:43], a[116:119], a[220:223], v[40:43]\n"
    "v_exp_f32 v37, v37\n"
    "v_mfma_f32_16x16x32_bf16 v[40:43], a[112:115], a[216:219], v[40:43]\n"
    "v_cvt_pk_bf16_f32 v77, v10, v11\n"
    "v_cvt_pk_bf16_f32 v72, v36, v37\n"
    "v_mfma_f32_16x16x32_bf16 v[44:47], a[112:115], a[240:243], v[100:103]\n"
    "v_mfma_f32_16x16x32_bf16 v[44:47], a[116:119], a[244:247], v[44:47]\n"
    "v_exp_f32 v38, v38\n"
    "v_mfma_f32_16x16x32_bf16 v[44:47], a[120:123], a[248:251], v[44:47]\n"
    "v_mfma_f32_16x16x32_bf16 v[44:47], a[124:127], a[252:255], v[44:47]\n"
    "v_cvt_pk_bf16_f32 v84, v12, v13\n"
    "v_mfma_f32_16x16x32_bf16 v[44:47], a[128:131], a[160:163], v[44:47]\n"
    "v_exp_f32 v39, v39\n"
    "v_mfma_f32_16x16x32_bf16 v[44:47], a[132:135], a[164:167], v[44:47]\n"
    "v_cvt_pk_bf16_f32 v73, v38, v39\n"
    "v_mfma_f32_16x16x32_bf16 v[48:51], a[132:135], a[188:191], v[100:103]\n"
    "ds_read_b32 v245, v247\n"
    "v_cvt_pk_bf16_f32 v85, v14, v15\n"
    "v_mfma_f32_16x16x32_bf16 v[48:51], a[128:131], a[184:187], v[48:51]\n"
    "s_add_u32 m0, s68, 0\n"
    "v_exp_f32 v40, v40\n"
    "v_mfma_f32_16x16x32_bf16 v[48:51], a[124:127], a[180:183], v[48:51]\n"
    "v_mfma_f32_16x16x32_bf16 v[48:51], a[120:123], a[176:179], v[48:51]\n"
    "v_mfma_f32_16x16x32_bf16 v[48:51], a[116:119], a[172:175], v[48:51]\n"
    "s_waitcnt vmcnt(0)\n"
    "v_cmp_eq_u64 s[54:55], v[236:237], v[238:239]\n"
    "v_cvt_pk_bf16_f32 v92, v16, v17\n"
    "v_mfma_f32_16x16x32_bf16 v[48:51], a[112:115], a[168:171], v[48:51]\n"
    "s_waitcnt lgkmcnt(1)\n"
    "v_exp_f32 v41, v41\n"
    "v_mfma_f32_16x16x32_bf16 v[52:55], a[136:139], a[192:195], v[104:107]\n"
    "s_waitcnt lgkmcnt(0)\n"
    "v_cmp_le_u32 s[56:57], s59, v245\n"
    "v_cvt_pk_bf16_f32 v80, v40, v41\n"
    "v_mfma_f32_16x16x32_bf16 v[52:55], a[140:143], a[196:199], v[52:55]\n"
    "s_waitcnt vmcnt(1)\n"
    "v_exp_f32 v42, v42\n"
    "v_mfma_f32_16x16x32_bf16 v[52:55], a[144:147], a[200:203], v[52:55]\n"
    "s_and_b64 s[54:55], s[54:55], s[56:57]\n"
    "s_cmp_eq_u64 s[54:55], -1\n"
    "v_mfma_f32_16x16x32_bf16 v[52:55], a[148:151], a[204:207], v[52:55]\n"
    "s_cbranch_scc0 .Lpoll4\n"
    ".Lpoll4r:\n"
    "v_mfma_f32_16x16x32_bf16 v[52:55], a[152:155], a[208:211], v[52:55]\n"
    "buffer_load_dwordx4 v1, s[16:19], s67 offen offset:0 sc1 lds\n"
    "v_mfma_f32_16x16x32_bf16 v[52:55], a[156:159], a[212:215], v[52:55]\n"
    "s_add_u32 s76, s52, s60\n"
    "ds_read_b128 a[112:115], v3 offset:0\n"
    "v_cvt_pk_bf16_f32 v93, v18, v19\n"
    "v_mfma_f32_16x16x32_bf16 v[56:59], a[156:159], a[236:239], v[104:107]\n"
    "s_add_u32 s77, s52, s61\n"
    "ds_read_b128 a[116:119], v3 offset:2048\n"
    "v_cvt_pk_bf16_f32 v70, v20, v21\n"
    "v_mfma_f32_16x16x32_bf16 v[56:59], a[152:155], a[232:235], v[56:59]\n"
    "ds_read_b128 a[120:123], v3 offset:4096\n"
    "v_mfma_f32_16x16x32_bf16 v[56:59], a[148:151], a[228:231], v[56:59]\n"
    "buffer_load_dwordx4 v1, s[16:19], s67 offen offset:1024 sc1 lds\n"
    "v_mfma_f32_16x16x32_bf16 v[56:59], a[144:147], a[224:227], v[56:59]\n"
    "ds_read_b128 a[124:127], v3 offset:6144\n"
    "v_cvt_pk_bf16_f32 v71, v22, v23\n"
    "v_mfma_f32_16x16x32_bf16 v[56:59], a[140:143], a[220:223], v[56:59]\n"
    "s_add_u32 s78, s52, s62\n"
    "ds_read_b128 a[128:131], v3 offset:8192\n"
    "v_mfma_f32_16x16x32_bf16 v[56:59], a[136:139], a[216:219], v[56:59]\n"
    "ds_read_b128 a[132:135], v3 offset:10240\n"
    "v_mfma_f32_16x16x32_bf16 v[60:63], a[136:139], a[240:243], v[104:107]\n"
    "buffer_load_dwordx4 v1, s[16:19], s67 offen offset:2048 sc1 lds\n"
    "v_mfma_f32_16x16x32_bf16 v[60:63], a[140:143], a[244:247], v[60:63]\n"
    "ds_read_b128 v[100:103], v250 offset:1536\n"
    "v_cvt_pk_bf16_f32 v78, v24, v25\n"
    "v_mfma_f32_16x16x32_bf16 v[60:63], a[144:147], a[248:251], v[60:63]\n"
    "v_exp_f32 v43, v43\n"
    "v_mfma_f32_16x16x32_bf16 v[60:63], a[148:151], a[252:255], v[60:63]\n"
    "v_cvt_pk_bf16_f32 v81, v42, v43\n"
    "v_cvt_pk_u16_u32 v204, v204, v208\n"
    "v_mfma_f32_16x16x32_bf16 v[60:63], a[152:155], a[160:163], v[60:63]\n"
    "buffer_load_dwordx4 v1, s[16:19], s67 offen offset:3072 sc1 lds\n"
    "v_mfma_f32_16x16x32_bf16 v[60:63], a[156:159], a[164:167], v[60:63]\n"
    "s_add_u32 m0, s68, 4096\n"
    "v_cvt_pk_bf16_f32 v79, v26, v27\n"
    "v_mfma_f32_16x16x32_bf16 v[64:67], a[156:159], a[188:191], v[104:107]\n"
    "s_add_u32 s83, s67, 4096\n"
    "v_exp_f32 v44, v44\n"
    "v_mfma_f32_16x16x32_bf16 v[64:67], a[152:155], a[184:187], v[64:67]\n"
    "v_exp_f32 v45, v45\n"
    "v_mfma_f32_16x16x32_bf16 v[64:67], a[148:151], a[180:183], v[64:67]\n"
    "buffer_load_dwordx4 v1, s[16:19], s83 offen offset:0 sc1 lds\n"
    "v_mfma_f32_16x16x32_bf16 v[64:67], a[144:147], a[176:179], v[64:67]\n"
    "v_cvt_pk_bf16_f32 v86, v28, v29\n"
    "v_cvt_pk_bf16_f32 v88, v44, v45\n"
    "v_mfma_f32_16x16x32_bf16 v[64:67], a[140:143], a[172:175], v[64:67]\n"
    "v_exp_f32 v46, v46\n"
    "v_mfma_f32_16x16x32_bf16 v[64:67], a[136:139], a[168:171], v[64:67]\n"
    "s_waitcnt lgkmcnt(0)\n"
    "v_exp_f32 v47, v47\n"
    "v_mfma_f32_16x16x32_bf16 v[4:7], a[112:115], a[192:195], v[100:103]\n"
    "buffer_load_dwordx4 v1, s[16:19], s83 offen offset:1024 sc1 lds\n"
    "v_mfma_f32_16x16x32_bf16 v[4:7], a[116:119], a[196:199], v[4:7]\n"
    "s_add_u32 m0, s70, 0\n"
    "ds_read_b128 a[136:139], v3 offset:1024\n"
    "v_cvt_pk_bf16_f32 v87, v30, v31\n"
    "v_mfma_f32_16x16x32_bf16 v[4:7], a[120:123], a[200:203], v[4:7]\n"
    "s_add_u32 s83, s67, 24576\n"
    "ds_read_b128 a[140:143], v3 offset:3072\n"
    "v_cvt_pk_bf16_f32 v89, v46, v47\n"
    "v_mfma_f32_16x16x32_bf16 v[4:7], a[124:127], a[204:207], v[4:7]\n"
    "ds_read_b128 a[144:147], v3 offset:5120\n"
    "v_cvt_pk_u16_u32 v205, v205, v209\n"
    "v_mfma_f32_16x16x32_bf16 v[4:7], a[128:131], a[208:211], v[4:7]\n"
    "buffer_load_dwordx4 v1, s[16:19], s83 offen offset:0 sc1 lds\n"
    "v_mfma_f32_16x16x32_bf16 v[4:7], a[132:135], a[212:215], v[4:7]\n"
    "ds_read_b128 a[148:151], v3 offset:7168\n"
    "v_cvt_pk_bf16_f32 v94, v32, v33\n"
    "v_mfma_f32_16x16x32_bf16 v[8:11], a[132:135], a[236:239], v[100:103]\n"
    "ds_read_b128 a[152:155], v3 offset:9216\n"
    "v_cvt_pk_u16_u32 v206, v206, v210\n"
    "v_mfma_f32_16x16x32_bf16 v[8:11], a[128:131], a[232:235], v[8:11]\n"
    "ds_read_b128 a[156:159], v3 offset:11264\n"
    "v_cvt_pk_u16_u32 v207, v207, v211\n"
    "v_mfma_f32_16x16x32_bf16 v[8:11], a[124:127], a[228:231], v[8:11]\n"
    "buffer_load_dwordx4 v1, s[16:19], s83 offen offset:1024 sc1 lds\n"
    "v_mfma_f32_16x16x32_bf16 v[8:11], a[120:123], a[224:227], v[8:11]\n"
    "ds_read_b128 v[104:107], v250 offset:1552\n"
    "v_cvt_pk_bf16_f32 v95, v34, v35\n"
    "v_mfma_f32_16x16x32_bf16 v[8:11], a[116:119], a[220:223], v[8:11]\n"
    "v_exp_f32 v52, v52\n"
    "v_mfma_f32_16x16x32_bf16 v[8:11], a[112:115], a[216:219], v[8:11]\n"
    "v_exp_f32 v53, v53\n"
    "v_mfma_f32_16x16x32_bf16 v[12:15], a[112:115], a[240:243], v[100:103]\n"
    "buffer_load_dwordx4 v1, s[16:19], s83 offen offset:2048 sc1 lds\n"
    "v_mfma_f32_16x16x32_bf16 v[12:15], a[116:119], a[244:247], v[12:15]\n"
    "v_exp_f32 v54, v54\n"
    "v_mfma_f32_16x16x32_bf16 v[12:15], a[120:123], a[248:251], v[12:15]\n"
    "v_exp_f32 v55, v55\n"
    "v_mfma_f32_16x16x32_bf16 v[12:15], a[124:127], a[252:255], v[12:15]\n"
    "v_cvt_pk_bf16_f32 v74, v52, v53\n"
    "v_cvt_pk_bf16_f32 v75, v54, v55\n"
    "v_mfma_f32_16x16x32_bf16 v[12:15], a[128:131], a[160:163], v[12:15]\n"
    "buffer_load_dwordx4 v1, s[16:19], s83 offen offset:3072 sc1 lds\n"
    "v_mfma_f32_16x16x32_bf16 v[12:15], a[132:135], a[164:167], v[12:15]\n"
    "s_add_u32 m0, s70, 4096\n"
    "v_exp_f32 v56, v56\n"
    "v_mfma_f32_16x16x32_bf16 v[16:19], a[132:135], a[188:191], v[100:103]\n"
    "s_add_u32 s83, s67, 28672\n"
    "v_exp_f32 v48, v48\n"
    "v_mfma_f32_16x16x32_bf16 v[16:19], a[128:131], a[184:187], v[16:19]\n"
    "v_exp_f32 v49, v49\n"
    "v_mfma_f32_16x16x32_bf16 v[16:19], a[124:127], a[180:183], v[16:19]\n"
    "buffer_load_dwordx4 v1, s[16:19], s83 offen offset:0 sc1 lds\n"
    "v_mfma_f32_16x16x32_bf16 v[16:19], a[120:123], a[176:179], v[16:19]\n"
    "v_exp_f32 v57, v57\n"
    "v_mfma_f32_16x16x32_bf16 v[16:19], a[116:119], a[172:175], v[16:19]\n"
    "v_cvt_pk_bf16_f32 v82, v56, v57\n"
    "v_cvt_pk_bf16_f32 v96, v48, v49\n"
    "v_mfma_f32_16x16x32_bf16 v[16:19], a[112:115], a[168:171], v[16:19]\n"
    "s_waitcnt lgkmcnt(0)\n"
    "v_exp_f32 v58, v58\n"
    "v_mfma_f32_16x16x32_bf16 v[20:23], a[136:139], a[192:195], v[104:107]\n"
    "buffer_load_dwordx4 v1, s[16:19], s83 offen offset:1024 sc1 lds\n"
    "v_mfma_f32_16x16x32_bf16 v[20:23], a[140:143], a[196:199], v[20:23]\n"
    "s_add_u32 s67, s67, 49152\n"
    "ds_read_b128 a[112:115], v249 offset:0\n"
    "v_or3_b32 v243, v204, v205, v243\n"
    "v_mfma_f32_16x16x32_bf16 v[20:23], a[144:147], a[200:203], v[20:23]\n"
    "s_add_u32 s59, s59, 4\n"
    "ds_read_b128 a[116:119], v249 offset:12288\n"
    "v_or3_b32 v243, v206, v207, v243\n"
    "v_mfma_f32_16x16x32_bf16 v[20:23], a[148:151], a[204:207], v[20:23]\n"
    "ds_read_b128 a[120:123], v249 offset:1024\n"
    "v_cvt_pk_u16_u32 v212, v212, v216\n"
    "v_mfma_f32_16x16x32_bf16 v[20:23], a[152:155], a[208:211], v[20:23]\n"
    "ds_read_b128 a[124:127], v249 offset:13312\n"
    "v_cvt_pk_u16_u32 v213, v213, v217\n"
    "v_mfma_f32_16x16x32_bf16 v[20:23], a[156:159], a[212:215], v[20:23]\n"
    "ds_read_b128 a[128:131], v249 offset:2048\n"
    "v_mfma_f32_16x16x32_bf16 v[24:27], a[156:159], a[236:239], v[104:107]\n"
    "ds_read_b128 a[132:135], v249 offset:14336\n"
    "v_mfma_f32_16x16x32_bf16 v[24:27], a[152:155], a[232:235], v[24:27]\n"
    "v_exp_f32 v59, v59\n"
    "v_mfma_f32_16x16x32_bf16 v[24:27], a[148:151], a[228:231], v[24:27]\n"
    "v_cvt_pk_bf16_f32 v83, v58, v59\n"
    "v_cvt_pk_u16_u32 v214, v214, v218\n"
    "v_mfma_f32_16x16x32_bf16 v[24:27], a[144:147], a[224:227], v[24:27]\n"
    "v_exp_f32 v60, v60\n"
    "v_mfma_f32_16x16x32_bf16 v[24:27], a[140:143], a[220:223], v[24:27]\n"
    "v_exp_f32 v61, v61\n"
    "v_mfma_f32_16x16x32_bf16 v[24:27], a[136:139], a[216:219], v[24:27]\n"
    "v_exp_f32 v62, v62\n"
    "v_mfma_f32_16x16x32_bf16 v[28:31], a[136:139], a[240:243], v[104:107]\n"
    "v_cvt_pk_bf16_f32 v90, v60, v61\n"
    "v_mfma_f32_16x16x32_bf16 v[28:31], a[140:143], a[244:247], v[28:31]\n"
    "v_exp_f32 v63, v63\n"
    "v_mfma_f32_16x16x32_bf16 v[28:31], a[144:147], a[248:251], v[28:31]\n"
    "v_cvt_pk_bf16_f32 v91, v62, v63\n"
    "v_cvt_pk_u16_u32 v215, v215, v219\n"
    "v_mfma_f32_16x16x32_bf16 v[28:31], a[148:151], a[252:255], v[28:31]\n"
    "v_exp_f32 v50, v50\n"
    "v_mfma_f32_16x16x32_bf16 v[28:31], a[152:155], a[160:163], v[28:31]\n"
    "v_exp_f32 v51, v51\n"
    "v_mfma_f32_16x16x32_bf16 v[28:31], a[156:159], a[164:167], v[28:31]\n"
    "v_exp_f32 v64, v64\n"
    "v_mfma_f32_16x16x32_bf16 v[32:35], a[156:159], a[188:191], v[104:107]\n"
    "v_cvt_pk_bf16_f32 v97, v50, v51\n"
    "v_mfma_f32_16x16x32_bf16 v[32:35], a[152:155], a[184:187], v[32:35]\n"
    "v_exp_f32 v65, v65\n"
    "v_mfma_f32_16x16x32_bf16 v[32:35], a[148:151], a[180:183], v[32:35]\n"
    "v_cvt_pk_bf16_f32 v98, v64, v65\n"
    "v_or3_b32 v243, v212, v213, v243\n"
    "v_mfma_f32_16x16x32_bf16 v[32:35], a[144:147], a[176:179], v[32:35]\n"
    "v_mfma_f32_16x16x32_bf16 v[32:35], a[140:143], a[172:175], v[32:35]\n"
    "v_exp_f32 v66, v66\n"
    "v_mfma_f32_16x16x32_bf16 v[32:35], a[136:139], a[168:171], v[32:35]\n"
    "s_waitcnt lgkmcnt(0)\n"
    "v_mfma_f32_16x16x32_bf16 v[124:127], a[112:115], v[68:71], v[124:127]\n"
    "ds_read_b128 a[136:139], v249 offset:3072\n"
    "v_or3_b32 v243, v214, v215, v243\n"
    "v_mfma_f32_16x16x32_bf16 v[124:127], a[116:119], v[72:75], v[124:127]\n"
    "ds_read_b128 a[140:143], v249 offset:15360\n"
    "v_mfma_f32_16x16x32_bf16 v[128:131], a[124:127], v[72:75], v[128:131]\n"
    "ds_read_b128 a[144:147], v249 offset:4096\n"
    "v_mfma_f32_16x16x32_bf16 v[128:131], a[120:123], v[68:71], v[128:131]\n"
    "ds_read_b128 a[148:151], v249 offset:16384\n"
    "v_mfma_f32_16x16x32_bf16 v[132:135], a[128:131], v[68:71], v[132:135]\n"
    "ds_read_b128 a[152:155], v249 offset:5120\n"
    "v_mfma_f32_16x16x32_bf16 v[132:135], a[132:135], v[72:75], v[132:135]\n"
    "ds_read_b128 a[156:159], v249 offset:17408\n"
    "v_cvt_pk_u16_u32 v220, v220, v224\n"
    "v_mfma_f32_16x16x32_bf16 v[148:151], a[132:135], v[80:83], v[148:151]\n"
    "ds_read_b128 v[104:107], v251 offset:768\n"
    "v_mfma_f32_16x16x32_bf16 v[148:151], a[128:131], v[76:79], v[148:151]\n"
    "v_exp_f32 v67, v67\n"
    "v_mfma_f32_16x16x32_bf16 v[144:147], a[120:123], v[76:79], v[144:147]\n"
    "v_cvt_pk_bf16_f32 v99, v66, v67\n"
    "v_mfma_f32_16x16x32_bf16 v[144:147], a[124:127], v[80:83], v[144:147]\n"
    "v_exp_f32 v4, v4\n"
    "v_mfma_f32_16x16x32_bf16 v[140:143], a[116:119], v[80:83], v[140:143]\n"
    "v_exp_f32 v5, v5\n"
    "v_mfma_f32_16x16x32_bf16 v[140:143], a[112:115], v[76:79], v[140:143]\n"
    "v_exp_f32 v6, v6\n"
    "v_mfma_f32_16x16x32_bf16 v[156:159], a[112:115], v[84:87], v[156:159]\n"
    "v_exp_f32 v7, v7\n"
    "v_mfma_f32_16x16x32_bf16 v[156:159], a[116:119], v[88:91], v[156:159]\n"
    "v_cvt_pk_u16_u32 v221, v221, v225\n"
    "v_mfma_f32_16x16x32_bf16 v[160:163], a[124:127], v[88:91], v[160:163]\n"
    "v_exp_f32 v8, v8\n"
    "v_mfma_f32_16x16x32_bf16 v[160:163], a[120:123], v[84:87], v[160:163]\n"
    "v_cvt_pk_u16_u32 v222, v222, v226\n"
    "v_mfma_f32_16x16x32_bf16 v[164:167], a[128:131], v[84:87], v[164:167]\n"
    "v_mfma_f32_16x16x32_bf16 v[164:167], a[132:135], v[88:91], v[164:167]\n"
    "v_exp_f32 v9, v9\n"
    "v_mfma_f32_16x16x32_bf16 v[180:183], a[132:135], v[96:99], v[180:183]\n"
    "v_mfma_f32_16x16x32_bf16 v[180:183], a[128:131], v[92:95], v[180:183]\n"
    "v_mfma_f32_16x16x32_bf16 v[176:179], a[120:123], v[92:95], v[176:179]\n"
    "v_exp_f32 v10, v10\n"
    "v_mfma_f32_16x16x32_bf16 v[176:179], a[124:127], v[96:99], v[176:179]\n"
    "v_cvt_pk_u16_u32 v223, v223, v227\n"
    "v_mfma_f32_16x16x32_bf16 v[172:175], a[116:119], v[96:99], v[172:175]\n"
    "v_exp_f32 v11, v11\n"
    "v_mfma_f32_16x16x32_bf16 v[172:175], a[112:115], v[92:95], v[172:175]\n"
    "s_waitcnt lgkmcnt(0)\n"
    "v_mfma_f32_16x16x32_bf16 v[136:139], a[136:139], v[68:71], v[136:139]\n"
    "ds_read_b128 a[112:115], v249 offset:6144\n"
    "v_mfma_f32_16x16x32_bf16 v[136:139], a[140:143], v[72:75], v[136:139]\n"
    "ds_read_b128 a[116:119], v249 offset:18432\n"
    "v_mfma_f32_16x16x32_bf16 v[188:191], a[148:151], v[72:75], v[188:191]\n"
    "ds_read_b128 a[120:123], v249 offset:7168\n"
    "v_or3_b32 v243, v220, v221, v243\n"
    "v_mfma_f32_16x16x32_bf16 v[188:191], a[144:147], v[68:71], v[188:191]\n"
    "ds_read_b128 a[124:127], v249 offset:19456\n"
    "v_mfma_f32_16x16x32_bf16 v[192:195], a[152:155], v[68:71], v[192:195]\n"
    "ds_read_b128 a[128:131], v249 offset:8192\n"
    "v_mfma_f32_16x16x32_bf16 v[192:195], a[156:159], v[72:75], v[192:195]\n"
    "ds_read_b128 a[132:135], v249 offset:20480\n"
    "v_smfmac_f32_16x16x64_bf16 v[108:111], v[104:107], v[68:75], v253\n"
    "s_add_u32 s81, s81, 128\n"
    "s_cmp_gt_u32 s81, 4096\n"
    "s_cselect_b32 s81, 0, s81\n"
    "v_smfmac_f32_16x16x64_bf16 v[112:115], v[104:107], v[76:83], v253\n"
    "v_mfma_f32_16x16x32_bf16 a[4:7], a[156:159], v[80:83], a[4:7]\n"
    "v_exp_f32 v12, v12\n"
    "v_mfma_f32_16x16x32_bf16 a[4:7], a[152:155], v[76:79], a[4:7]\n"
    "v_exp_f32 v13, v13\n"
    "v_mfma_f32_16x16x32_bf16 a[0:3], a[144:147], v[76:79], a[0:3]\n"
    "v_exp_f32 v14, v14\n"
    "v_mfma_f32_16x16x32_bf16 a[0:3], a[148:151], v[80:83], a[0:3]\n"
    "v_exp_f32 v15, v15\n"
    "v_mfma_f32_16x16x32_bf16 v[152:155], a[140:143], v[80:83], v[152:155]\n"
    "v_or3_b32 v243, v222, v223, v243\n"
    "v_and_b32 v208, 0xff00ff00, v243\n"
    "v_mfma_f32_16x16x32_bf16 v[152:155], a[136:139], v[76:79], v[152:155]\n"
    "v_exp_f32 v16, v16\n"
    "v_mfma_f32_16x16x32_bf16 v[168:171], a[136:139], v[84:87], v[168:171]\n"
    "v_exp_f32 v17, v17\n"
    "v_mfma_f32_16x16x32_bf16 v[168:171], a[140:143], v[88:91], v[168:171]\n"
    "v_mfma_f32_16x16x32_bf16 a[16:19], a[148:151], v[88:91], a[16:19]\n"
    "v_mfma_f32_16x16x32_bf16 a[16:19], a[144:147], v[84:87], a[16:19]\n"
    "v_exp_f32 v18, v18\n"
    "v_mfma_f32_16x16x32_bf16 a[20:23], a[152:155], v[84:87], a[20:23]\n"
    "v_cmp_ne_u32 vcc, 0, v208\n"
    "v_mfma_f32_16x16x32_bf16 a[20:23], a[156:159], v[88:91], a[20:23]\n"
    "v_smfmac_f32_16x16x64_bf16 v[116:119], v[104:107], v[84:91], v253\n"
    "v_smfmac_f32_16x16x64_bf16 v[120:123], v[104:107], v[92:99], v253\n"
    "v_mfma_f32_16x16x32_bf16 a[36:39], a[156:159], v[96:99], a[36:39]\n"
    "v_exp_f32 v19, v19\n"
    "v_mfma_f32_16x16x32_bf16 a[36:39], a[152:155], v[92:95], a[36:39]\n"
    "v_exp_f32 v20, v20\n"
    "v_mfma_f32_16x16x32_bf16 a[32:35], a[144:147], v[92:95], a[32:35]\n"
    "v_cndmask_b32 v208, 2.0, v241, vcc\n"
    "v_mfma_f32_16x16x32_bf16 a[32:35], a[148:151], v[96:99], a[32:35]\n"
    "v_exp_f32 v21, v21\n"
    "v_mfma_f32_16x16x32_bf16 v[184:187], a[140:143], v[96:99], v[184:187]\n"
    "v_mfma_f32_16x16x32_bf16 v[184:187], a[136:139], v[92:95], v[184:187]\n"
    "s_waitcnt lgkmcnt(0)\n"
    "v_mfma_f32_16x16x32_bf16 v[196:199], a[112:115], v[68:71], v[196:199]\n"
    "ds_read_b128 a[136:139], v249 offset:9216\n"
    "v_mfma_f32_16x16x32_bf16 v[196:199], a[116:119], v[72:75], v[196:199]\n"
    "ds_read_b128 a[140:143], v249 offset:21504\n"
    "v_mfma_f32_16x16x32_bf16 v[200:203], a[124:127], v[72:75], v[200:203]\n"
    "ds_read_b128 a[144:147], v249 offset:10240\n"
    "v_mfma_f32_16x16x32_bf16 v[200:203], a[120:123], v[68:71], v[200:203]\n"
    "ds_read_b128 a[148:151], v249 offset:22528\n"
    "v_mfma_f32_16x16x32_bf16 a[48:51], a[128:131], v[68:71], a[48:51]\n"
    "buffer_load_dwordx2 v[236:237], v241, s[36:39], s81 offen offset:0 sc1\n"
    "v_mfma_f32_16x16x32_bf16 a[48:51], a[132:135], v[72:75], a[48:51]\n"
    "ds_read_b128 a[152:155], v249 offset:11264\n"
    "v_mfma_f32_16x16x32_bf16 a[64:67], a[132:135], v[80:83], a[64:67]\n"
    "ds_read_b128 a[156:159], v249 offset:23552\n"
    "v_mfma_f32_16x16x32_bf16 a[64:67], a[128:131], v[76:79], a[64:67]\n"
    "buffer_store_dword v204, v244, s[20:23], s76 offen offset:0 sc1\n"
    "v_mfma_f32_16x16x32_bf16 a[12:15], a[120:123], v[76:79], a[12:15]\n"
    "v_exp_f32 v22, v22\n"
    "v_mfma_f32_16x16x32_bf16 a[12:15], a[124:127], v[80:83], a[12:15]\n"
    "buffer_store_dword v205, v244, s[20:23], s76 offen offset:1024 sc1\n"
    "v_mfma_f32_16x16x32_bf16 a[8:11], a[116:119], v[80:83], a[8:11]\n"
    "v_exp_f32 v23, v23\n"
    "v_mfma_f32_16x16x32_bf16 a[8:11], a[112:115], v[76:79], a[8:11]\n"
    "buffer_store_dword v206, v244, s[20:23], s76 offen offset:2048 sc1\n"
    "v_mfma_f32_16x16x32_bf16 a[24:27], a[112:115], v[84:87], a[24:27]\n"
    "v_exp_f32 v24, v24\n"
    "v_mfma_f32_16x16x32_bf16 a[24:27], a[116:119], v[88:91], a[24:27]\n"
    "buffer_store_dword v207, v244, s[20:23], s76 offen offset:3072 sc1\n"
    "v_mfma_f32_16x16x32_bf16 a[28:31], a[124:127], v[88:91], a[28:31]\n"
    "v_exp_f32 v25, v25\n"
    "v_mfma_f32_16x16x32_bf16 a[28:31], a[120:123], v[84:87], a[28:31]\n"
    "buffer_store_dword v212, v244, s[20:23], s77 offen offset:0 sc1\n"
    "v_mfma_f32_16x16x32_bf16 a[80:83], a[128:131], v[84:87], a[80:83]\n"
    "v_exp_f32 v26, v26\n"
    "v_mfma_f32_16x16x32_bf16 a[80:83], a[132:135], v[88:91], a[80:83]\n"
    "buffer_store_dword v213, v244, s[20:23], s77 offen offset:1024 sc1\n"
    "v_mfma_f32_16x16x32_bf16 a[96:99], a[132:135], v[96:99], a[96:99]\n"
    "v_exp_f32 v27, v27\n"
    "v_mfma_f32_16x16x32_bf16 a[96:99], a[128:131], v[92:95], a[96:99]\n"
    "buffer_store_dword v214, v244, s[20:23], s77 offen offset:2048 sc1\n"
    "v_mfma_f32_16x16x32_bf16 a[44:47], a[120:123], v[92:95], a[44:47]\n"
    "v_exp_f32 v28, v28\n"
    "v_mfma_f32_16x16x32_bf16 a[44:47], a[124:127], v[96:99], a[44:47]\n"
    "buffer_store_dword v215, v244, s[20:23], s77 offen offset:3072 sc1\n"
    "v_mfma_f32_16x16x32_bf16 a[40:43], a[116:119], v[96:99], a[40:43]\n"
    "v_exp_f32 v29, v29\n"
    "v_mfma_f32_16x16x32_bf16 a[40:43], a[112:115], v[92:95], a[40:43]\n"
    "s_waitcnt lgkmcnt(0)\n"
    "v_exp_f32 v30, v30\n"
    "v_mfma_f32_16x16x32_bf16 a[52:55], a[136:139], v[68:71], a[52:55]\n"
    "ds_read_b128 a[112:115], v3 offset:12288\n"
    "v_mfma_f32_16x16x32_bf16 a[52:55], a[140:143], v[72:75], a[52:55]\n"
    "ds_read_b128 a[116:119], v3 offset:14336\n"
    "v_mfma_f32_16x16x32_bf16 a[56:59], a[148:151], v[72:75], a[56:59]\n"
    "ds_read_b128 a[120:123], v3 offset:16384\n"
    "v_mfma_f32_16x16x32_bf16 a[56:59], a[144:147], v[68:71], a[56:59]\n"
    "ds_read_b128 a[124:127], v3 offset:18432\n"
    "v_mfma_f32_16x16x32_bf16 a[60:63], a[152:155], v[68:71], a[60:63]\n"
    "ds_read_b128 a[128:131], v3 offset:20480\n"
    "v_mfma_f32_16x16x32_bf16 a[60:63], a[156:159], v[72:75], a[60:63]\n"
    "ds_read_b128 a[132:135], v3 offset:22528\n"
    "v_mfma_f32_16x16x32_bf16 a[76:79], a[156:159], v[80:83], a[76:79]\n"
    "ds_read_b128 v[100:103], v250 offset:1664\n"
    "v_mfma_f32_16x16x32_bf16 a[76:79], a[152:155], v[76:79], a[76:79]\n"
    "buffer_store_dword v220, v244, s[20:23], s78 offen offset:0 sc1\n"
    "v_mfma_f32_16x16x32_bf16 a[72:75], a[144:147], v[76:79], a[72:75]\n"
    "v_exp_f32 v31, v31\n"
    "v_mfma_f32_16x16x32_bf16 a[72:75], a[148:151], v[80:83], a[72:75]\n"
    "buffer_store_dword v221, v244, s[20:23], s78 offen offset:1024 sc1\n"
    "v_mfma_f32_16x16x32_bf16 a[68:71], a[140:143], v[80:83], a[68:71]\n"
    "v_exp_f32 v32, v32\n"
    "v_mfma_f32_16x16x32_bf16 a[68:71], a[136:139], v[76:79], a[68:71]\n"
    "buffer_store_dword v222, v244, s[20:23], s78 offen offset:2048 sc1\n"
    "v_mfma_f32_16x16x32_bf16 a[84:87], a[136:139], v[84:87], a[84:87]\n"
    "v_exp_f32 v33, v33\n"
    "v_mfma_f32_16x16x32_bf16 a[84:87], a[140:143], v[88:91], a[84:87]\n"
    "buffer_store_dword v223, v244, s[20:23], s78 offen offset:3072 sc1\n"
    "v_mfma_f32_16x16x32_bf16 a[88:91], a[148:151], v[88:91], a[88:91]\n"
    "s_waitcnt vmcnt(13)\n"
    "ds_add_u32 v247, v246\n"
    "v_mfma_f32_16x16x32_bf16 a[88:91], a[144:147], v[84:87], a[88:91]\n"
    "buffer_store_dwordx2 v[238:239], v208, s[36:39], 0 offen offset:0 sc1\n"
    "v_mfma_f32_16x16x32_bf16 a[92:95], a[152:155], v[84:87], a[92:95]\n"
    "v_exp_f32 v34, v34\n"
    "v_mfma_f32_16x16x32_bf16 a[92:95], a[156:159], v[88:91], a[92:95]\n"
    "v_exp_f32 v35, v35\n"
    "v_mfma_f32_16x16x32_bf16 a[108:111], a[156:159], v[96:99], a[108:111]\n"
    "v_mfma_f32_16x16x32_bf16 a[108:111], a[152:155], v[92:95], a[108:111]\n"
    "v_mfma_f32_16x16x32_bf16 a[104:107], a[144:147], v[92:95], a[104:107]\n"
    "s_sub_u32 s74, s68, s65\n"
    "v_add_u32 v250, 1536, v250\n"
    "v_add_u32 v251, 1536, v251\n"
    "v_mfma_f32_16x16x32_bf16 a[104:107], a[148:151], v[96:99], a[104:107]\n"
    "s_add_u32 s66, s66, 1\n"
    "v_mov_b32 v2, v3\n"
    "v_add_u32 v3, s74, v252\n"
    "v_mfma_f32_16x16x32_bf16 a[100:103], a[140:143], v[96:99], a[100:103]\n"
    "s_waitcnt lgkmcnt(1)\n"
    "s_cmp_lt_u32 s66, 29\n"
    "v_mfma_f32_16x16x32_bf16 a[100:103], a[136:139], v[92:95], a[100:103]\n"
    "s_cbranch_scc1 .LPV\n"
    "s_branch .Ltail\n"
    "s_nop 0\n"
    "s_nop 0\n"
    "s_nop 0\n"
    "s_nop 0\n"
    "s_nop 0\n"
    "s_nop 0\n"
    ".LtailP:\n"
    "s_cbranch_execz .Lw2\n"
    "v_swap_b32 v248, v249\n"
    "s_mov_b32 s64, s70\n"
    "v_add_u32 v250, 768, v250\n"
    "v_add_u32 v251, 768, v251\n"
    ".Ltail:\n"
    "v_mfma_f32_16x16x32_bf16 v[36:39], a[112:115], a[192:195], v[100:103]\n"
    "v_cvt_pk_bf16_f32 v93, v18, v19\n"
    "v_cvt_pk_bf16_f32 v84, v12, v13\n"
    "v_mfma_f32_16x16x32_bf16 v[36:39], a[116:119], a[196:199], v[36:39]\n"
    "ds_read_b128 a[136:139], v2 offset:13312\n"
    "ds_read_b128 a[140:143], v2 offset:15360\n"
    "v_mfma_f32_16x16x32_bf16 v[36:39], a[120:123], a[200:203], v[36:39]\n"
    "s_mul_i32 s75, s84, 24576\n"
    "v_mfma_f32_16x16x32_bf16 v[36:39], a[124:127], a[204:207], v[36:39]\n"
    "v_cvt_pk_bf16_f32 v77, v10, v11\n"
    "v_subrev_u32 v249, 24576, v249\n"
    "v_mfma_f32_16x16x32_bf16 v[36:39], a[128:131], a[208:211], v[36:39]\n"
    "ds_read_b128 a[152:155], v2 offset:21504\n"
    "v_mfma_f32_16x16x32_bf16 v[40:43], a[132:135], a[236:239], v[100:103]\n"
    "v_cvt_pk_bf16_f32 v69, v6, v7\n"
    "v_lshlrev_b32 v240, 16, v1\n"
    "v_mfma_f32_16x16x32_bf16 v[36:39], a[132:135], a[212:215], v[36:39]\n"
    "ds_read_b128 a[156:159], v2 offset:23552\n"
    "ds_read_b128 v[104:107], v250 offset:144\n"
    "v_mfma_f32_16x16x32_bf16 v[48:51], a[132:135], a[188:191], v[100:103]\n"
    "s_add_u32 s76, s75, 6144\n"
    "ds_read_b128 a[144:147], v2 offset:17408\n"
    "v_and_b32 v3, 0xf0, v1\n"
    "v_mfma_f32_16x16x32_bf16 v[48:51], a[128:131], a[184:187], v[48:51]\n"
    "ds_read_b128 a[148:151], v2 offset:19456\n"
    "s_add_u32 s77, s76, 6144\n"
    "v_mfma_f32_16x16x32_bf16 v[40:43], a[128:131], a[232:235], v[40:43]\n"
    "v_exp_f32 v37, v37\n"
    "v_mfma_f32_16x16x32_bf16 v[48:51], a[124:127], a[180:183], v[48:51]\n"
    "v_exp_f32 v38, v38\n"
    "v_mfma_f32_16x16x32_bf16 v[48:51], a[120:123], a[176:179], v[48:51]\n"
    "s_add_u32 s78, s77, 6144\n"
    "v_cvt_pk_bf16_f32 v76, v8, v9\n"
    "v_cvt_pk_bf16_f32 v68, v4, v5\n"
    "v_mfma_f32_16x16x32_bf16 v[40:43], a[124:127], a[228:231], v[40:43]\n"
    "v_cvt_pk_bf16_f32 v85, v14, v15\n"
    "v_cvt_pk_bf16_f32 v71, v22, v23\n"
    "v_mfma_f32_16x16x32_bf16 v[48:51], a[116:119], a[172:175], v[48:51]\n"
    "v_and_b32 v2, 0x70, v1\n"
    "v_cvt_pk_bf16_f32 v94, v32, v33\n"
    "v_mfma_f32_16x16x32_bf16 v[44:47], a[112:115], a[240:243], v[100:103]\n"
    "v_cvt_pk_bf16_f32 v95, v34, v35\n"
    "v_cvt_pk_bf16_f32 v86, v28, v29\n"
    "v_mfma_f32_16x16x32_bf16 v[48:51], a[112:115], a[168:171], v[48:51]\n"
    "v_exp_f32 v39, v39\n"
    "v_mfma_f32_16x16x32_bf16 v[44:47], a[116:119], a[244:247], v[44:47]\n"
    "s_waitcnt lgkmcnt(2)\n"
    "v_cvt_pk_bf16_f32 v70, v20, v21\n"
    "v_cvt_pk_bf16_f32 v92, v16, v17\n"
    "v_mfma_f32_16x16x32_bf16 v[60:63], a[136:139], a[240:243], v[104:107]\n"
    "v_exp_f32 v36, v36\n"
    "v_mfma_f32_16x16x32_bf16 v[56:59], a[156:159], a[236:239], v[104:107]\n"
    "s_add_u32 s72, s65, 122880\n"
    "ds_read_b32 v245, v247\n"
    "v_mfma_f32_16x16x32_bf16 v[40:43], a[120:123], a[224:227], v[40:43]\n"
    "v_exp_f32 v51, v51\n"
    "v_mfma_f32_16x16x32_bf16 v[60:63], a[140:143], a[244:247], v[60:63]\n"
    "v_exp_f32 v48, v48\n"
    "v_mfma_f32_16x16x32_bf16 v[44:47], a[120:123], a[248:251], v[44:47]\n"
    "v_exp_f32 v50, v50\n"
    "v_mfma_f32_16x16x32_bf16 v[40:43], a[116:119], a[220:223], v[40:43]\n"
    "s_waitcnt vmcnt(0)\n"
    "v_exp_f32 v49, v49\n"
    "v_mfma_f32_16x16x32_bf16 v[44:47], a[124:127], a[252:255], v[44:47]\n"
    "ds_read_b128 v[232:235], v251 offset:0\n"
    "ds_read_b128 v[100:103], v250 offset:768\n"
    "v_mfma_f32_16x16x32_bf16 v[52:55], a[136:139], a[192:195], v[104:107]\n"
    "v_cvt_pk_bf16_f32 v79, v26, v27\n"
    "v_lshrrev_b32 v224, 4, v1\n"
    "v_mfma_f32_16x16x32_bf16 v[64:67], a[156:159], a[188:191], v[104:107]\n"
    "v_lshl_add_u32 v3, v3, 3, s72\n"
    "v_cmp_eq_u64 s[54:55], v[236:237], v[238:239]\n"
    "v_mfma_f32_16x16x32_bf16 v[52:55], a[140:143], a[196:199], v[52:55]\n"
    "s_waitcnt lgkmcnt(2)\n"
    "v_cvt_pk_bf16_f32 v78, v24, v25\n"
    "v_cmp_le_u32 s[56:57], s59, v245\n"
    "v_mfma_f32_16x16x32_bf16 v[40:43], a[112:115], a[216:219], v[40:43]\n"
    "s_and_b64 s[54:55], s[54:55], s[56:57]\n"
    "v_and_b32 v224, 48, v224\n"
    "s_cmp_eq_u64 s[54:55], -1\n"
    "v_mfma_f32_16x16x32_bf16 v[60:63], a[144:147], a[248:251], v[60:63]\n"
    "s_cbranch_scc0 .Lpt0\n"
    ".Lpt0r:\n"
    "v_mfma_f32_16x16x32_bf16 v[44:47], a[128:131], a[160:163], v[44:47]\n"
    "s_add_u32 s81, s81, 128\n"
    "ds_read_b128 a[116:119], v252 offset:2048\n"
    "s_add_u32 m0, s65, 24576\n"
    "v_mfma_f32_16x16x32_bf16 v[52:55], a[144:147], a[200:203], v[52:55]\n"
    "s_add_u32 s83, s67, .Lend-.Lend2+(0)\n"
    "buffer_load_dwordx4 v1, s[16:19], s83 offen offset:0 sc1 lds\n"
    "v_mfma_f32_16x16x32_bf16 v[60:63], a[148:151], a[252:255], v[60:63]\n"
    "ds_read_b128 a[120:123], v252 offset:4096\n"
    "ds_read_b128 a[128:131], v252 offset:8192\n"
    "v_mfma_f32_16x16x32_bf16 v[56:59], a[152:155], a[232:235], v[56:59]\n"
    "buffer_load_dwordx4 v1, s[16:19], s83 offen offset:1024 sc1 lds\n"
    "v_mfma_f32_16x16x32_bf16 v[60:63], a[152:155], a[160:163], v[60:63]\n"
    "ds_read_b128 v[104:107], v250 offset:784\n"
    "ds_read_b128 a[112:115], v252 offset:0\n"
    "v_mfma_f32_16x16x32_bf16 v[44:47], a[132:135], a[164:167], v[44:47]\n"
    "buffer_load_dwordx4 v1, s[16:19], s83 offen offset:2048 sc1 lds\n"
    "v_mfma_f32_16x16x32_bf16 v[52:55], a[148:151], a[204:207], v[52:55]\n"
    "ds_read_b128 a[132:135], v252 offset:10240\n"
    "v_mfma_f32_16x16x32_bf16 v[52:55], a[152:155], a[208:211], v[52:55]\n"
    "buffer_load_dwordx4 v1, s[16:19], s83 offen offset:3072 sc1 lds\n"
    "v_mfma_f32_16x16x32_bf16 v[60:63], a[156:159], a[164:167], v[60:63]\n"
    "s_add_u32 m0, s65, 28672\n"
    "v_exp_f32_e64 v40, v40\n"
    "v_mfma_f32_16x16x32_bf16 v[56:59], a[148:151], a[228:231], v[56:59]\n"
    "s_add_u32 s83, s67, 4096\n"
    "buffer_load_dwordx4 v1, s[16:19], s83 offen offset:0 sc1 lds\n"
    "v_mfma_f32_16x16x32_bf16 v[56:59], a[144:147], a[224:227], v[56:59]\n"
    "ds_read_b128 a[124:127], v252 offset:6144\n"
    "v_cvt_pk_bf16_f32 v72, v36, v37\n"
    "v_mfma_f32_16x16x32_bf16 v[56:59], a[140:143], a[220:223], v[56:59]\n"
    "buffer_load_dwordx4 v1, s[16:19], s83 offen offset:1024 sc1 lds\n"
    "v_mfma_f32_16x16x32_bf16 v[52:55], a[156:159], a[212:215], v[52:55]\n"
    "v_exp_f32 v45, v45\n"
    "v_mfma_f32_16x16x32_bf16 v[56:59], a[136:139], a[216:219], v[56:59]\n"
    "v_exp_f32 v62, v62\n"
    "v_mfma_f32_16x16x32_bf16 v[64:67], a[152:155], a[184:187], v[64:67]\n"
    "s_waitcnt lgkmcnt(2)\n"
    "v_xor_b32 v224, v224, v2\n"
    "v_cvt_pk_bf16_f32 v87, v30, v31\n"
    "v_mfma_f32_16x16x32_bf16 v[12:15], a[112:115], a[240:243], v[100:103]\n"
    "s_waitcnt lgkmcnt(1)\n"
    "ds_read_b128 a[156:159], v252 offset:11264\n"
    "ds_read_b128 a[152:155], v252 offset:9216\n"
    "v_mfma_f32_16x16x32_bf16 v[16:19], a[132:135], a[188:191], v[100:103]\n"
    ".Lwp0:\n"
    "s_cbranch_execz .Lw3\n"
    "s_add_u32 m0, s65, 73728\n"
    "v_exp_f32 v56, v56\n"
    "v_mfma_f32_16x16x32_bf16 v[64:67], a[148:151], a[180:183], v[64:67]\n"
    "v_exp_f32 v58, v58\n"
    "v_mfma_f32_16x16x32_bf16 v[4:7], a[112:115], a[192:195], v[100:103]\n"
    "s_add_u32 s83, s67, 24576\n"
    "v_exp_f32 v61, v61\n"
    "v_mfma_f32_16x16x32_bf16 v[16:19], a[128:131], a[184:187], v[16:19]\n"
    "v_exp_f32 v53, v53\n"
    "v_mfma_f32_16x16x32_bf16 v[64:67], a[144:147], a[176:179], v[64:67]\n"
    "v_exp_f32 v54, v54\n"
    "v_mfma_f32_16x16x32_bf16 v[64:67], a[140:143], a[172:175], v[64:67]\n"
    "v_exp_f32 v52, v52\n"
    "v_mfma_f32_16x16x32_bf16 v[8:11], a[132:135], a[236:239], v[100:103]\n"
    "s_waitcnt lgkmcnt(2)\n"
    "v_exp_f32 v63, v63\n"
    "v_mfma_f32_16x16x32_bf16 v[16:19], a[124:127], a[180:183], v[16:19]\n"
    "v_exp_f32 v41, v41\n"
    "v_mfma_f32_16x16x32_bf16 v[64:67], a[136:139], a[168:171], v[64:67]\n"
    "v_exp_f32 v43, v43\n"
    "v_mfma_f32_16x16x32_bf16 v[8:11], a[128:131], a[232:235], v[8:11]\n"
    "ds_read_b128 a[144:147], v252 offset:5120\n"
    "ds_read_b128 a[148:151], v252 offset:7168\n"
    "v_mfma_f32_16x16x32_bf16 v[12:15], a[116:119], a[244:247], v[12:15]\n"
    "v_exp_f32 v44, v44\n"
    "v_mfma_f32_16x16x32_bf16 v[4:7], a[116:119], a[196:199], v[4:7]\n"
    "v_exp_f32 v55, v55\n"
    "v_mfma_f32_16x16x32_bf16 v[16:19], a[120:123], a[176:179], v[16:19]\n"
    "ds_read_b128 a[136:139], v252 offset:1024\n"
    "v_cvt_pk_bf16_f32 v80, v40, v41\n"
    "v_mfma_f32_16x16x32_bf16 v[12:15], a[120:123], a[248:251], v[12:15]\n"
    "buffer_load_dwordx4 v1, s[16:19], s83 offen offset:0 sc1 lds\n"
    "v_mfma_f32_16x16x32_bf16 v[4:7], a[120:123], a[200:203], v[4:7]\n"
    "v_exp_f32 v42, v42\n"
    "v_mfma_f32_16x16x32_bf16 v[16:19], a[116:119], a[172:175], v[16:19]\n"
    "v_exp_f32 v66, v66\n"
    "v_mfma_f32_16x16x32_bf16 v[4:7], a[124:127], a[204:207], v[4:7]\n"
    "v_exp_f32 v59, v59\n"
    "v_mfma_f32_16x16x32_bf16 v[4:7], a[128:131], a[208:211], v[4:7]\n"
    "buffer_load_dwordx4 v1, s[16:19], s83 offen offset:1024 sc1 lds\n"
    "v_mfma_f32_16x16x32_bf16 v[4:7], a[132:135], a[212:215], v[4:7]\n"
    "v_exp_f32 v57, v57\n"
    "v_mfma_f32_16x16x32_bf16 v[12:15], a[124:127], a[252:255], v[12:15]\n"
    "ds_read_b128 v[100:103], v250 offset:896\n"
    "ds_read_b128 a[140:143], v252 offset:3072\n"
    "v_mfma_f32_16x16x32_bf16 v[12:15], a[128:131], a[160:163], v[12:15]\n"
    "v_exp_f32 v65, v65\n"
    "v_mfma_f32_16x16x32_bf16 v[8:11], a[124:127], a[228:231], v[8:11]\n"
    "v_exp_f32 v47, v47\n"
    "v_mfma_f32_16x16x32_bf16 v[12:15], a[132:135], a[164:167], v[12:15]\n"
    "s_waitcnt lgkmcnt(6)\n"
    "buffer_load_dwordx4 v1, s[16:19], s83 offen offset:2048 sc1 lds\n"
    "v_mfma_f32_16x16x32_bf16 v[32:35], a[156:159], a[188:191], v[104:107]\n"
    "v_exp_f32 v60, v60\n"
    "v_mfma_f32_16x16x32_bf16 v[24:27], a[156:159], a[236:239], v[104:107]\n"
    "s_waitcnt lgkmcnt(2)\n"
    "v_cvt_pk_bf16_f32 v81, v42, v43\n"
    "v_mfma_f32_16x16x32_bf16 v[28:31], a[136:139], a[240:243], v[104:107]\n"
    "v_cvt_pk_bf16_f32 v96, v48, v49\n"
    "v_cvt_pk_bf16_f32 v90, v60, v61\n"
    "v_mfma_f32_16x16x32_bf16 v[8:11], a[120:123], a[224:227], v[8:11]\n"
    "v_exp_f32 v7, v7\n"
    "v_mfma_f32_16x16x32_bf16 v[24:27], a[152:155], a[232:235], v[24:27]\n"
    "v_exp_f32 v67, v67\n"
    "v_mfma_f32_16x16x32_bf16 v[8:11], a[116:119], a[220:223], v[8:11]\n"
    "ds_read_b128 a[124:127], v252 offset:18432\n"
    "v_mfma_f32_16x16x32_bf16 v[16:19], a[112:115], a[168:171], v[16:19]\n"
    "buffer_load_dwordx4 v1, s[16:19], s83 offen offset:3072 sc1 lds\n"
    "v_mfma_f32_16x16x32_bf16 v[32:35], a[152:155], a[184:187], v[32:35]\n"
    "s_waitcnt lgkmcnt(1)\n"
    "ds_read_b128 a[132:135], v252 offset:22528\n"
    "v_cvt_pk_bf16_f32 v75, v54, v55\n"
    "v_mfma_f32_16x16x32_bf16 v[24:27], a[148:151], a[228:231], v[24:27]\n"
    "s_add_u32 m0, s65, 77824\n"
    "v_cvt_pk_bf16_f32 v74, v52, v53\n"
    "v_cvt_pk_bf16_f32 v97, v50, v51\n"
    "v_mfma_f32_16x16x32_bf16 v[20:23], a[136:139], a[192:195], v[104:107]\n"
    "s_add_u32 s83, s67, 28672\n"
    "v_exp_f32 v6, v6\n"
    "v_mfma_f32_16x16x32_bf16 v[20:23], a[140:143], a[196:199], v[20:23]\n"
    "v_cvt_pk_bf16_f32 v99, v66, v67\n"
    "v_exp_f32 v14, v14\n"
    "v_mfma_f32_16x16x32_bf16 v[24:27], a[144:147], a[224:227], v[24:27]\n"
    "ds_read_b128 a[128:131], v252 offset:20480\n"
    "ds_read_b128 v[104:107], v250 offset:912\n"
    "v_mfma_f32_16x16x32_bf16 v[8:11], a[112:115], a[216:219], v[8:11]\n"
    "v_exp_f32 v46, v46\n"
    "v_mfma_f32_16x16x32_bf16 v[28:31], a[140:143], a[244:247], v[28:31]\n"
    "v_exp_f32 v13, v13\n"
    "v_mfma_f32_16x16x32_bf16 v[32:35], a[148:151], a[180:183], v[32:35]\n"
    "s_waitcnt lgkmcnt(2)\n"
    "v_exp_f32 v64, v64\n"
    "v_mfma_f32_16x16x32_bf16 v[40:43], a[132:135], a[236:239], v[100:103]\n"
    "ds_read_b128 a[120:123], v252 offset:16384\n"
    "v_cvt_pk_bf16_f32 v91, v62, v63\n"
    "v_mfma_f32_16x16x32_bf16 v[32:35], a[144:147], a[176:179], v[32:35]\n"
    "ds_read_b128 a[112:115], v252 offset:12288\n"
    "v_cvt_pk_bf16_f32 v73, v38, v39\n"
    "v_mfma_f32_16x16x32_bf16 v[28:31], a[144:147], a[248:251], v[28:31]\n"
    "v_exp_f32 v11, v11\n"
    "v_mfma_f32_16x16x32_bf16 v[32:35], a[140:143], a[172:175], v[32:35]\n"
    "v_cvt_pk_bf16_f32 v88, v44, v45\n"
    "v_cvt_pk_bf16_f32 v89, v46, v47\n"
    "v_mfma_f32_16x16x32_bf16 v[32:35], a[136:139], a[168:171], v[32:35]\n"
    "buffer_load_dwordx4 v1, s[16:19], s83 offen offset:0 sc1 lds\n"
    "v_mfma_f32_16x16x32_bf16 v[24:27], a[140:143], a[220:223], v[24:27]\n"
    "v_exp_f32 v9, v9\n"
    "v_mfma_f32_16x16x32_bf16 v[20:23], a[144:147], a[200:203], v[20:23]\n"
    "buffer_load_dwordx4 v1, s[16:19], s83 offen offset:1024 sc1 lds\n"
    "v_mfma_f32_16x16x32_bf16 v[20:23], a[148:151], a[204:207], v[20:23]\n"
    "v_exp_f32 v15, v15\n"
    "v_mfma_f32_16x16x32_bf16 v[20:23], a[152:155], a[208:211], v[20:23]\n"
    "v_exp_f32 v34, v34\n"
    "v_mfma_f32_16x16x32_bf16 v[20:23], a[156:159], a[212:215], v[20:23]\n"
    "v_exp_f32 v10, v10\n"
    "v_mfma_f32_16x16x32_bf16 v[28:31], a[148:151], a[252:255], v[28:31]\n"
    "s_add_u32 s59, s59, 4\n"
    "ds_read_b128 a[140:143], v252 offset:15360\n"
    "ds_read_b128 a[116:119], v252 offset:14336\n"
    "v_mfma_f32_16x16x32_bf16 v[28:31], a[152:155], a[160:163], v[28:31]\n"
    "s_waitcnt lgkmcnt(5)\n"
    "v_exp_f32 v4, v4\n"
    "v_mfma_f32_16x16x32_bf16 v[40:43], a[128:131], a[232:235], v[40:43]\n"
    "v_exp_f32 v12, v12\n"
    "v_mfma_f32_16x16x32_bf16 v[24:27], a[136:139], a[216:219], v[24:27]\n"
    "ds_read_b128 a[136:139], v252 offset:13312\n"
    "v_add_u32 v3, v3, v224\n"
    "v_mfma_f32_16x16x32_bf16 v[48:51], a[132:135], a[188:191], v[100:103]\n"
    "v_exp_f32 v23, v23\n"
    "v_smfmac_f32_16x16x64_bf16 v[116:119], v[232:235], v[84:91], v253\n"
    "ds_read_b128 a[152:155], v252 offset:21504\n"
    "v_mfma_f32_16x16x32_bf16 v[28:31], a[156:159], a[164:167], v[28:31]\n"
    "s_waitcnt lgkmcnt(5)\n"
    "v_exp_f32 v5, v5\n"
    "v_mfma_f32_16x16x32_bf16 v[40:43], a[124:127], a[228:231], v[40:43]\n"
    "s_mov_b32 s74, 0x47000000\n"
    "v_exp_f32 v20, v20\n"
    "v_mfma_f32_16x16x32_bf16 v[40:43], a[120:123], a[224:227], v[40:43]\n"
    "s_waitcnt lgkmcnt(4)\n"
    "ds_read_b128 a[156:159], v252 offset:23552\n"
    "v_cvt_pk_bf16_f32 v204, v4, v5\n"
    "v_mfma_f32_16x16x32_bf16 v[36:39], a[112:115], a[192:195], v[100:103]\n"
    "s_waitcnt lgkmcnt(3)\n"
    "v_exp_f32 v32, v32\n"
    "v_mfma_f32_16x16x32_bf16 v[36:39], a[116:119], a[196:199], v[36:39]\n"
    "v_cvt_pk_bf16_f32 v220, v12, v13\n"
    "v_cvt_pk_bf16_f32 v82, v56, v57\n"
    "v_mfma_f32_16x16x32_bf16 v[40:43], a[116:119], a[220:223], v[40:43]\n"
    "v_exp_f32 v16, v16\n"
    "v_mfma_f32_16x16x32_bf16 v[40:43], a[112:115], a[216:219], v[40:43]\n"
    "s_waitcnt lgkmcnt(2)\n"
    "v_exp_f32 v28, v28\n"
    "v_mfma_f32_16x16x32_bf16 v[52:55], a[136:139], a[192:195], v[104:107]\n"
    "v_exp_f32 v33, v33\n"
    "v_mfma_f32_16x16x32_bf16 v[48:51], a[128:131], a[184:187], v[48:51]\n"
    "v_exp_f32 v8, v8\n"
    "v_mfma_f32_16x16x32_bf16 v[44:47], a[112:115], a[240:243], v[100:103]\n"
    "v_exp_f32 v17, v17\n"
    "v_mfma_f32_16x16x32_bf16 v[60:63], a[136:139], a[240:243], v[104:107]\n"
    "v_cvt_pk_bf16_f32 v98, v64, v65\n"
    "v_cvt_pk_bf16_f32 v83, v58, v59\n"
    "v_mfma_f32_16x16x32_bf16 v[48:51], a[124:127], a[180:183], v[48:51]\n"
    "v_exp_f32 v35, v35\n"
    "v_mfma_f32_16x16x32_bf16 v[44:47], a[116:119], a[244:247], v[44:47]\n"
    "v_exp_f32 v41, v41\n"
    "v_mfma_f32_16x16x32_bf16 v[44:47], a[120:123], a[248:251], v[44:47]\n"
    "v_exp_f32 v29, v29\n"
    "v_mfma_f32_16x16x32_bf16 v[60:63], a[140:143], a[244:247], v[60:63]\n"
    "v_exp_f32 v31, v31\n"
    "v_mfma_f32_16x16x32_bf16 v[36:39], a[120:123], a[200:203], v[36:39]\n"
    "v_lshrrev_b32 v224, 3, v1\n"
    "v_cvt_pk_bf16_f32 v205, v6, v7\n"
    "v_mfma_f32_16x16x32_bf16 v[48:51], a[120:123], a[176:179], v[48:51]\n"
    "s_waitcnt vmcnt(6)\n"
    "ds_add_u32 v247, v246\n"
    "v_mfma_f32_16x16x32_bf16 v[48:51], a[116:119], a[172:175], v[48:51]\n"
    "s_waitcnt lgkmcnt(1)\n"
    "ds_read_b128 a[144:147], v252 offset:17408\n"
    "ds_read_b128 a[148:151], v252 offset:19456\n"
    "v_mfma_f32_16x16x32_bf16 v[56:59], a[156:159], a[236:239], v[104:107]\n"
    "v_exp_f32 v25, v25\n"
    "v_cvt_pk_bf16_f32 v228, v16, v17\n"
    "v_mfma_f32_16x16x32_bf16 v[52:55], a[140:143], a[196:199], v[52:55]\n"
    "v_exp_f32 v18, v18\n"
    "v_mfma_f32_16x16x32_bf16 v[44:47], a[124:127], a[252:255], v[44:47]\n"
    "v_exp_f32 v19, v19\n"
    "v_mfma_f32_16x16x32_bf16 v[64:67], a[156:159], a[188:191], v[104:107]\n"
    "v_exp_f32 v43, v43\n"
    "v_mfma_f32_16x16x32_bf16 v[48:51], a[112:115], a[168:171], v[48:51]\n"
    "v_exp_f32 v27, v27\n"
    "v_mfma_f32_16x16x32_bf16 v[36:39], a[124:127], a[204:207], v[36:39]\n"
    "v_exp_f32 v30, v30\n"
    "v_mfma_f32_16x16x32_bf16 v[64:67], a[152:155], a[184:187], v[64:67]\n"
    "v_exp_f32 v22, v22\n"
    "v_mfma_f32_16x16x32_bf16 v[36:39], a[128:131], a[208:211], v[36:39]\n"
    "v_exp_f32 v42, v42\n"
    "v_mfma_f32_16x16x32_bf16 v[44:47], a[128:131], a[160:163], v[44:47]\n"
    "s_waitcnt lgkmcnt(1)\n"
    "v_exp_f32 v26, v26\n"
    "v_mfma_f32_16x16x32_bf16 v[60:63], a[144:147], a[248:251], v[60:63]\n"
    "s_waitcnt lgkmcnt(0)\n"
    "v_exp_f32 v40, v40\n"
    "v_mfma_f32_16x16x32_bf16 v[64:67], a[148:151], a[180:183], v[64:67]\n"
    "ds_read_b128 v[100:103], v250 offset:1536\n"
    "ds_read_b32 v245, v247\n"
    "v_mfma_f32_16x16x32_bf16 v[56:59], a[152:155], a[232:235], v[56:59]\n"
    "v_cvt_pk_bf16_f32 v216, v40, v41\n"
    "v_cvt_pk_bf16_f32 v231, v34, v35\n"
    "v_mfma_f32_16x16x32_bf16 v[64:67], a[144:147], a[176:179], v[64:67]\n"
    "v_exp_f32 v21, v21\n"
    "v_smfmac_f32_16x16x64_bf16 v[120:123], v[232:235], v[92:99], v253\n"
    "v_cvt_pk_bf16_f32 v221, v14, v15\n"
    "v_mfma_f32_16x16x32_bf16 v[44:47], a[132:135], a[164:167], v[44:47]\n"
    "v_cvt_pk_bf16_f32 v207, v22, v23\n"
    "v_cvt_pk_bf16_f32 v229, v18, v19\n"
    "v_mfma_f32_16x16x32_bf16 v[64:67], a[140:143], a[172:175], v[64:67]\n"
    "v_cvt_pk_bf16_f32 v206, v20, v21\n"
    "v_cvt_pk_bf16_f32 v212, v8, v9\n"
    "v_mfma_f32_16x16x32_bf16 v[52:55], a[144:147], a[200:203], v[52:55]\n"
    "v_exp_f32 v50, v50\n"
    "v_mfma_f32_16x16x32_bf16 v[60:63], a[148:151], a[252:255], v[60:63]\n"
    "v_exp_f32 v24, v24\n"
    "v_mfma_f32_16x16x32_bf16 v[36:39], a[132:135], a[212:215], v[36:39]\n"
    "s_waitcnt lgkmcnt(0)\n"
    "v_cmp_le_u32 s[56:57], s59, v245\n"
    "v_cvt_pk_bf16_f32 v213, v10, v11\n"
    "v_smfmac_f32_16x16x64_bf16 v[108:111], v[232:235], v[68:75], v253\n"
    "s_cmp_eq_u64 s[56:57], -1\n"
    "v_cvt_pk_bf16_f32 v214, v24, v25\n"
    "v_smfmac_f32_16x16x64_bf16 v[112:115], v[232:235], v[76:83], v253\n"
    "s_cbranch_scc0 .Lpt1\n"
    ".Lpt1r:\n"
    "v_mfma_f32_16x16x32_bf16 v[52:55], a[148:151], a[204:207], v[52:55]\n"
    "s_add_u32 m0, s65, 49152\n"
    "ds_read_b128 a[128:131], v252 offset:32768\n"
    "ds_read_b128 a[124:127], v252 offset:30720\n"
    "v_mfma_f32_16x16x32_bf16 v[60:63], a[152:155], a[160:163], v[60:63]\n"
    "ds_read_b128 a[112:115], v252 offset:24576\n"
    "v_cvt_pk_bf16_f32 v222, v28, v29\n"
    "v_mfma_f32_16x16x32_bf16 v[60:63], a[156:159], a[164:167], v[60:63]\n"
    "s_add_u32 s83, s67, 73728\n"
    "ds_read_b128 a[132:135], v252 offset:34816\n"
    "v_cvt_pk_bf16_f32 v215, v26, v27\n"
    "v_mfma_f32_16x16x32_bf16 v[56:59], a[148:151], a[228:231], v[56:59]\n"
    "buffer_load_dwordx4 v1, s[16:19], s83 offen offset:0 sc1 lds\n"
    "v_mfma_f32_16x16x32_bf16 v[56:59], a[144:147], a[224:227], v[56:59]\n"
    "ds_read_b128 a[116:119], v252 offset:26624\n"
    "v_cvt_pk_bf16_f32 v223, v30, v31\n"
    "v_mfma_f32_16x16x32_bf16 v[56:59], a[140:143], a[220:223], v[56:59]\n"
    "v_exp_f32 v38, v38\n"
    "v_exp_f32 v47, v47\n"
    "v_mfma_f32_16x16x32_bf16 v[52:55], a[152:155], a[208:211], v[52:55]\n"
    "ds_read_b128 a[120:123], v252 offset:28672\n"
    "ds_read_b128 v[104:107], v250 offset:1552\n"
    "v_mfma_f32_16x16x32_bf16 v[64:67], a[136:139], a[168:171], v[64:67]\n"
    "buffer_load_dwordx4 v1, s[16:19], s83 offen offset:1024 sc1 lds\n"
    "v_mfma_f32_16x16x32_bf16 v[56:59], a[136:139], a[216:219], v[56:59]\n"
    "v_exp_f32 v37, v37\n"
    "v_mfma_f32_16x16x32_bf16 v[52:55], a[156:159], a[212:215], v[52:55]\n"
    "s_waitcnt lgkmcnt(4)\n"
    "buffer_load_dwordx4 v1, s[16:19], s83 offen offset:2048 sc1 lds\n"
    "v_mfma_f32_16x16x32_bf16 v[4:7], a[112:115], a[192:195], v[100:103]\n"
    "ds_read_b128 a[140:143], v252 offset:27648\n"
    "v_cvt_pk_bf16_f32 v230, v32, v33\n"
    "v_mfma_f32_16x16x32_bf16 v[20:23], a[112:115], a[240:243], v[100:103]\n"
    "s_waitcnt lgkmcnt(3)\n"
    "buffer_load_dwordx4 v1, s[16:19], s83 offen offset:3072 sc1 lds\n"
    "v_mfma_f32_16x16x32_bf16 v[20:23], a[116:119], a[244:247], v[20:23]\n"
    "s_add_u32 m0, s65, 53248\n"
    "v_exp_f32 v58, v58\n"
    "v_mfma_f32_16x16x32_bf16 v[28:31], a[132:135], a[188:191], v[100:103]\n"
    "s_add_u32 s83, s67, 77824\n"
    "v_exp_f32 v44, v44\n"
    "v_mfma_f32_16x16x32_bf16 v[28:31], a[128:131], a[184:187], v[28:31]\n"
    "ds_read_b128 a[148:151], v252 offset:31744\n"
    "ds_read_b128 a[152:155], v252 offset:33792\n"
    "v_mfma_f32_16x16x32_bf16 v[12:15], a[132:135], a[236:239], v[100:103]\n"
    "ds_read_b128 a[156:159], v252 offset:35840\n"
    "v_and_b32 v224, 0x70, v224\n"
    "v_mfma_f32_16x16x32_bf16 v[12:15], a[128:131], a[232:235], v[12:15]\n"
    "buffer_load_dwordx4 v1, s[16:19], s83 offen offset:0 sc1 lds\n"
    "v_mfma_f32_16x16x32_bf16 v[28:31], a[124:127], a[180:183], v[28:31]\n"
    "v_exp_f32 v55, v55\n"
    "v_mfma_f32_16x16x32_bf16 v[4:7], a[116:119], a[196:199], v[4:7]\n"
    "s_waitcnt lgkmcnt(5)\n"
    "buffer_load_dwordx4 v1, s[16:19], s83 offen offset:1024 sc1 lds\n"
    "v_mfma_f32_16x16x32_bf16 v[28:31], a[120:123], a[176:179], v[28:31]\n"
    "v_exp_f32 v65, v65\n"
    "v_mfma_f32_16x16x32_bf16 v[28:31], a[116:119], a[172:175], v[28:31]\n"
    "v_exp_f32 v61, v61\n"
    "v_mfma_f32_16x16x32_bf16 v[12:15], a[124:127], a[228:231], v[12:15]\n"
    "v_exp_f32 v53, v53\n"
    "v_mfma_f32_16x16x32_bf16 v[4:7], a[120:123], a[200:203], v[4:7]\n"
    "buffer_load_dwordx2 v[236:237], v241, s[36:39], 0 offen offset:0 sc1\n"
    "v_mfma_f32_16x16x32_bf16 v[20:23], a[120:123], a[248:251], v[20:23]\n"
    "s_waitcnt lgkmcnt(0)\n"
    "v_exp_f32 v67, v67\n"
    "v_mfma_f32_16x16x32_bf16 v[16:19], a[156:159], a[236:239], v[104:107]\n"
    "ds_read_b128 a[136:139], v252 offset:25600\n"
    "ds_read_b128 v[100:103], v250 offset:1664\n"
    "v_mfma_f32_16x16x32_bf16 v[4:7], a[124:127], a[204:207], v[4:7]\n"
    "v_exp_f32 v36, v36\n"
    "v_mfma_f32_16x16x32_bf16 v[28:31], a[112:115], a[168:171], v[28:31]\n"
    "s_waitcnt vmcnt(7)\n"
    "buffer_store_dwordx2 v[238:239], v240, s[36:39], s82 offen offset:0 sc1\n"
    "v_exp_f32 v46, v46\n"
    "v_mfma_f32_16x16x32_bf16 v[32:35], a[156:159], a[188:191], v[104:107]\n"
    "v_exp_f32 v60, v60\n"
    "v_mfma_f32_16x16x32_bf16 v[12:15], a[120:123], a[224:227], v[12:15]\n"
    "v_exp_f32 v64, v64\n"
    "v_mfma_f32_16x16x32_bf16 v[4:7], a[128:131], a[208:211], v[4:7]\n"
    "v_exp_f32 v66, v66\n"
    "v_mfma_f32_16x16x32_bf16 v[12:15], a[116:119], a[220:223], v[12:15]\n"
    "v_exp_f32 v49, v49\n"
    "v_mfma_f32_16x16x32_bf16 v[20:23], a[124:127], a[252:255], v[20:23]\n"
    "ds_read_b128 a[116:119], v252 offset:38912\n"
    "v_cvt_pk_bf16_f32 v235, v66, v67\n"
    "v_mfma_f32_16x16x32_bf16 v[32:35], a[152:155], a[184:187], v[32:35]\n"
    "ds_read_b128 a[120:123], v252 offset:40960\n"
    "v_cvt_pk_bf16_f32 v208, v36, v37\n"
    "v_mfma_f32_16x16x32_bf16 v[12:15], a[112:115], a[216:219], v[12:15]\n"
    "s_mov_b32 s73, 0x7f800000\n"
    "ds_read_b128 a[144:147], v252 offset:29696\n"
    "ds_read_b128 a[112:115], v252 offset:36864\n"
    "v_mfma_f32_16x16x32_bf16 v[4:7], a[132:135], a[212:215], v[4:7]\n"
    "s_waitcnt lgkmcnt(5)\n"
    "ds_add_u32 v247, v246\n"
    "v_mfma_f32_16x16x32_bf16 v[24:27], a[136:139], a[240:243], v[104:107]\n"
    "v_exp_f32 v39, v39\n"
    "v_mfma_f32_16x16x32_bf16 v[16:19], a[152:155], a[232:235], v[16:19]\n"
    "v_cvt_pk_bf16_f32 v226, v60, v61\n"
    "v_cvt_pk_bf16_f32 v234, v64, v65\n"
    "v_mfma_f32_16x16x32_bf16 v[8:11], a[136:139], a[192:195], v[104:107]\n"
    "v_exp_f32 v48, v48\n"
    "v_mfma_f32_16x16x32_bf16 v[24:27], a[140:143], a[244:247], v[24:27]\n"
    "v_exp_f32 v56, v56\n"
    "v_mfma_f32_16x16x32_bf16 v[20:23], a[128:131], a[160:163], v[20:23]\n"
    "ds_read_b128 v[240:243], v251 offset:768\n"
    "v_cvt_pk_bf16_f32 v209, v38, v39\n"
    "v_mfma_f32_16x16x32_bf16 v[16:19], a[148:151], a[228:231], v[16:19]\n"
    "v_exp_f32 v62, v62\n"
    "v_mfma_f32_16x16x32_bf16 v[20:23], a[132:135], a[164:167], v[20:23]\n"
    "v_exp_f32 v63, v63\n"
    "v_mfma_f32_16x16x32_bf16 v[8:11], a[140:143], a[196:199], v[8:11]\n"
    "s_waitcnt lgkmcnt(3)\n"
    "ds_read_b128 a[128:131], v252 offset:45056\n"
    "ds_read_b128 a[132:135], v252 offset:47104\n"
    "v_mfma_f32_16x16x32_bf16 v[24:27], a[144:147], a[248:251], v[24:27]\n"
    "v_exp_f32 v12, v12\n"
    "v_mfma_f32_16x16x32_bf16 v[8:11], a[144:147], a[200:203], v[8:11]\n"
    "v_exp_f32 v13, v13\n"
    "v_mfma_f32_16x16x32_bf16 v[24:27], a[148:151], a[252:255], v[24:27]\n"
    "v_exp_f32 v4, v4\n"
    "v_mfma_f32_16x16x32_bf16 v[24:27], a[152:155], a[160:163], v[24:27]\n"
    "v_exp_f32 v59, v59\n"
    "v_mfma_f32_16x16x32_bf16 v[16:19], a[144:147], a[224:227], v[16:19]\n"
    "s_mov_b32 s79, 133\n"
    "v_exp_f32 v5, v5\n"
    "v_mfma_f32_16x16x32_bf16 v[16:19], a[140:143], a[220:223], v[16:19]\n"
    "v_exp_f32 v54, v54\n"
    "v_mfma_f32_16x16x32_bf16 v[8:11], a[148:151], a[204:207], v[8:11]\n"
    "v_exp_f32 v45, v45\n"
    "v_mfma_f32_16x16x32_bf16 v[16:19], a[136:139], a[216:219], v[16:19]\n"
    "ds_read_b128 a[124:127], v252 offset:43008\n"
    "ds_read_b32 v245, v247\n"
    "v_mfma_f32_16x16x32_bf16 v[32:35], a[148:151], a[180:183], v[32:35]\n"
    "v_exp_f32 v6, v6\n"
    "v_mfma_f32_16x16x32_bf16 v[8:11], a[152:155], a[208:211], v[8:11]\n"
    "ds_read_b128 a[152:155], v252 offset:46080\n"
    "v_cvt_pk_bf16_f32 v217, v42, v43\n"
    "v_mfma_f32_16x16x32_bf16 v[8:11], a[156:159], a[212:215], v[8:11]\n"
    "v_exp_f32 v19, v19\n"
    "v_mfma_f32_16x16x32_bf16 v[24:27], a[156:159], a[164:167], v[24:27]\n"
    "v_exp_f32 v51, v51\n"
    "v_mfma_f32_16x16x32_bf16 v[32:35], a[144:147], a[176:179], v[32:35]\n"
    "s_waitcnt lgkmcnt(7)\n"
    "v_cvt_pk_bf16_f32 v219, v58, v59\n"
    "v_cvt_pk_bf16_f32 v233, v50, v51\n"
    "v_mfma_f32_16x16x32_bf16 v[36:39], a[112:115], a[192:195], v[100:103]\n"
    "s_waitcnt lgkmcnt(3)\n"
    "v_exp_f32 v21, v21\n"
    "v_mfma_f32_16x16x32_bf16 v[40:43], a[132:135], a[236:239], v[100:103]\n"
    "ds_read_b128 a[156:159], v252 offset:48128\n"
    "v_cvt_pk_bf16_f32 v227, v62, v63\n"
    "v_mfma_f32_16x16x32_bf16 v[36:39], a[116:119], a[196:199], v[36:39]\n"
    "s_add_u32 s59, s59, 4\n"
    "v_exp_f32 v28, v28\n"
    "v_mfma_f32_16x16x32_bf16 v[32:35], a[140:143], a[172:175], v[32:35]\n"
    "v_exp_f32 v52, v52\n"
    "v_mfma_f32_16x16x32_bf16 v[32:35], a[136:139], a[168:171], v[32:35]\n"
    "v_cvt_pk_bf16_f32 v232, v48, v49\n"
    "v_mad_u32_u24 v244, v224, 24, v2\n"
    "v_mfma_f32_16x16x32_bf16 v[48:51], a[132:135], a[188:191], v[100:103]\n"
    "ds_read_b128 a[136:139], v252 offset:37888\n"
    "ds_read_b128 v[104:107], v250 offset:1680\n"
    "v_mfma_f32_16x16x32_bf16 v[36:39], a[120:123], a[200:203], v[36:39]\n"
    "s_waitcnt lgkmcnt(5)\n"
    "v_exp_f32 v57, v57\n"
    "v_mfma_f32_16x16x32_bf16 v[36:39], a[124:127], a[204:207], v[36:39]\n"
    "v_cvt_pk_bf16_f32 v225, v46, v47\n"
    "v_cvt_pk_bf16_f32 v210, v52, v53\n"
    "v_mfma_f32_16x16x32_bf16 v[36:39], a[128:131], a[208:211], v[36:39]\n"
    "v_cvt_pk_bf16_f32 v224, v44, v45\n"
    "v_cvt_pk_bf16_f32 v211, v54, v55\n"
    "v_mfma_f32_16x16x32_bf16 v[40:43], a[128:131], a[232:235], v[40:43]\n"
    "ds_read_b128 a[144:147], v252 offset:41984\n"
    "ds_read_b128 a[140:143], v252 offset:39936\n"
    "v_mfma_f32_16x16x32_bf16 v[44:47], a[112:115], a[240:243], v[100:103]\n"
    "v_exp_f32 v20, v20\n"
    "v_mfma_f32_16x16x32_bf16 v[40:43], a[124:127], a[228:231], v[40:43]\n"
    "v_cvt_pk_bf16_f32 v12, v12, v13\n"
    "v_cvt_pk_bf16_f32 v218, v56, v57\n"
    "v_mfma_f32_16x16x32_bf16 v[36:39], a[132:135], a[212:215], v[36:39]\n"
    "v_exp_f32 v32, v32\n"
    "v_mfma_f32_16x16x32_bf16 v[40:43], a[120:123], a[224:227], v[40:43]\n"
    "v_exp_f32 v30, v30\n"
    "v_smfmac_f32_16x16x64_bf16 v[120:123], v[240:243], v[228:235], v253\n"
    "ds_read_b128 a[148:151], v252 offset:44032\n"
    "v_mfma_f32_16x16x32_bf16 v[44:47], a[116:119], a[244:247], v[44:47]\n"
    "s_waitcnt lgkmcnt(3)\n"
    "v_exp_f32 v26, v26\n"
    "v_mfma_f32_16x16x32_bf16 v[52:55], a[136:139], a[192:195], v[104:107]\n"
    "v_exp_f32 v15, v15\n"
    "v_mfma_f32_16x16x32_bf16 v[56:59], a[156:159], a[236:239], v[104:107]\n"
    "s_waitcnt lgkmcnt(1)\n"
    "v_cvt_pk_bf16_f32 v4, v4, v5\n"
    "v_cmp_le_u32 s[56:57], s59, v245\n"
    "v_smfmac_f32_16x16x64_bf16 v[108:111], v[240:243], v[204:211], v253\n"
    "s_cmp_eq_u64 s[56:57], -1\n"
    "v_lshrrev_b32 v252, 3, v1\n"
    "v_mfma_f32_16x16x32_bf16 v[52:55], a[140:143], a[196:199], v[52:55]\n"
    "v_exp_f32 v7, v7\n"
    "v_mfma_f32_16x16x32_bf16 v[60:63], a[136:139], a[240:243], v[104:107]\n"
    "v_exp_f32 v17, v17\n"
    "v_mfma_f32_16x16x32_bf16 v[60:63], a[140:143], a[244:247], v[60:63]\n"
    "v_exp_f32 v14, v14\n"
    "v_mfma_f32_16x16x32_bf16 v[56:59], a[152:155], a[232:235], v[56:59]\n"
    "v_exp_f32 v39, v39\n"
    "v_mfma_f32_16x16x32_bf16 v[64:67], a[156:159], a[188:191], v[104:107]\n"
    "v_exp_f32 v11, v11\n"
    "v_mfma_f32_16x16x32_bf16 v[60:63], a[144:147], a[248:251], v[60:63]\n"
    "v_exp_f32 v22, v22\n"
    "v_mfma_f32_16x16x32_bf16 v[52:55], a[144:147], a[200:203], v[52:55]\n"
    "s_waitcnt lgkmcnt(0)\n"
    "v_exp_f32 v27, v27\n"
    "v_mfma_f32_16x16x32_bf16 v[52:55], a[148:151], a[204:207], v[52:55]\n"
    "v_exp_f32 v8, v8\n"
    "v_mfma_f32_16x16x32_bf16 v[52:55], a[152:155], a[208:211], v[52:55]\n"
    "v_exp_f32 v9, v9\n"
    "v_mfma_f32_16x16x32_bf16 v[56:59], a[148:151], a[228:231], v[56:59]\n"
    "ds_read_b128 a[204:207], v248 offset:13312\n"
    "v_cvt_pk_bf16_f32 v5, v6, v7\n"
    "v_mfma_f32_16x16x32_bf16 v[64:67], a[152:155], a[184:187], v[64:67]\n"
    "v_exp_f32 v16, v16\n"
    "v_mfma_f32_16x16x32_bf16 v[56:59], a[144:147], a[224:227], v[56:59]\n"
    "ds_read_b128 a[208:211], v248 offset:2048\n"
    "ds_read_b128 a[196:199], v248 offset:12288\n"
    "v_mfma_f32_16x16x32_bf16 v[40:43], a[116:119], a[220:223], v[40:43]\n"
    "v_exp_f32_e64 v38, v38\n"
    "v_mfma_f32_16x16x32_bf16 v[52:55], a[156:159], a[212:215], v[52:55]\n"
    "v_exp_f32_e64 v37, v37\n"
    "v_smfmac_f32_16x16x64_bf16 v[116:119], v[240:243], v[220:227], v253\n"
    "v_cvt_pk_bf16_f32 v6, v8, v9\n"
    "v_smfmac_f32_16x16x64_bf16 v[112:115], v[240:243], v[212:219], v253\n"
    "s_cbranch_scc0 .Lpt4\n"
    ".Lpt4r:\n"
    "v_mfma_f32_16x16x32_bf16 v[44:47], a[120:123], a[248:251], v[44:47]\n"
    "v_exp_f32 v10, v10\n"
    "v_mfma_f32_16x16x32_bf16 v[40:43], a[112:115], a[216:219], v[40:43]\n"
    "v_exp_f32 v25, v25\n"
    "v_mfma_f32_16x16x32_bf16 v[48:51], a[128:131], a[184:187], v[48:51]\n"
    "v_exp_f32 v54, v54\n"
    "v_mfma_f32_16x16x32_bf16 v[56:59], a[140:143], a[220:223], v[56:59]\n"
    "s_waitcnt lgkmcnt(1)\n"
    "ds_read_b128 a[212:215], v248 offset:14336\n"
    "v_cvt_pk_bf16_f32 v20, v20, v21\n"
    "v_mfma_f32_16x16x32_bf16 v[148:151], a[208:211], v[76:79], v[148:151]\n"
    "s_waitcnt vmcnt(0)\n"
    "ds_add_u32 v247, v246\n"
    "v_mfma_f32_16x16x32_bf16 v[64:67], a[148:151], a[180:183], v[64:67]\n"
    "ds_read_b128 a[192:195], v248 offset:0\n"
    "ds_read_b128 a[200:203], v248 offset:1024\n"
    "v_mfma_f32_16x16x32_bf16 v[48:51], a[124:127], a[180:183], v[48:51]\n"
    "v_exp_f32 v52, v52\n"
    "v_mfma_f32_16x16x32_bf16 v[128:131], a[204:207], v[72:75], v[128:131]\n"
    "v_exp_f32 v41, v41\n"
    "v_mfma_f32_16x16x32_bf16 v[144:147], a[204:207], v[80:83], v[144:147]\n"
    "v_exp_f32 v36, v36\n"
    "v_mfma_f32_16x16x32_bf16 v[44:47], a[124:127], a[252:255], v[44:47]\n"
    "v_exp_f32 v24, v24\n"
    "v_mfma_f32_16x16x32_bf16 v[60:63], a[148:151], a[252:255], v[60:63]\n"
    "v_exp_f32 v23, v23\n"
    "v_mfma_f32_16x16x32_bf16 v[44:47], a[128:131], a[160:163], v[44:47]\n"
    "v_exp_f32 v55, v55\n"
    "v_mfma_f32_16x16x32_bf16 v[48:51], a[120:123], a[176:179], v[48:51]\n"
    "v_exp_f32 v29, v29\n"
    "v_mfma_f32_16x16x32_bf16 v[64:67], a[144:147], a[176:179], v[64:67]\n"
    "ds_read_b128 a[236:239], v249 offset:37888\n"
    "v_cvt_pk_bf16_f32 v9, v38, v39\n"
    "v_mfma_f32_16x16x32_bf16 v[56:59], a[136:139], a[216:219], v[56:59]\n"
    "ds_read_b128 a[220:223], v248 offset:15360\n"
    "ds_read_b128 a[244:247], v249 offset:38912\n"
    "v_mfma_f32_16x16x32_bf16 v[48:51], a[116:119], a[172:175], v[48:51]\n"
    "s_waitcnt lgkmcnt(6)\n"
    "ds_read_b128 a[232:235], v249 offset:25600\n"
    "ds_read_b128 a[240:243], v249 offset:26624\n"
    "v_mfma_f32_16x16x32_bf16 v[64:67], a[140:143], a[172:175], v[64:67]\n"
    "s_add_u32 s59, s59, 4\n"
    "v_cvt_pk_bf16_f32 v7, v10, v11\n"
    "v_cvt_pk_bf16_f32 v13, v14, v15\n"
    "v_mfma_f32_16x16x32_bf16 v[148:151], a[212:215], v[80:83], v[148:151]\n"
    "v_exp_f32 v53, v53\n"
    "v_mfma_f32_16x16x32_bf16 v[44:47], a[132:135], a[164:167], v[44:47]\n"
    "ds_read_b32 v245, v247\n"
    "v_cvt_pk_bf16_f32 v11, v54, v55\n"
    "v_mfma_f32_16x16x32_bf16 v[60:63], a[152:155], a[160:163], v[60:63]\n"
    "ds_read_b128 v[240:243], v251 offset:1536\n"
    "ds_read_b128 a[216:219], v248 offset:3072\n"
    "v_mfma_f32_16x16x32_bf16 v[64:67], a[136:139], a[168:171], v[64:67]\n"
    "s_waitcnt lgkmcnt(8)\n"
    "v_cvt_pk_bf16_f32 v8, v36, v37\n"
    "v_cvt_pk_bf16_f32 v10, v52, v53\n"
    "v_mfma_f32_16x16x32_bf16 v[128:131], a[200:203], v[68:71], v[128:131]\n"
    "v_exp_f32 v43, v43\n"
    "v_mfma_f32_16x16x32_bf16 v[60:63], a[156:159], a[164:167], v[60:63]\n"
    "v_exp_f32 v45, v45\n"
    "v_mfma_f32_16x16x32_bf16 v[140:143], a[192:195], v[76:79], v[140:143]\n"
    "ds_read_b128 a[252:255], v249 offset:39936\n"
    "ds_read_b128 a[224:227], v249 offset:24576\n"
    "v_mfma_f32_16x16x32_bf16 v[124:127], a[192:195], v[68:71], v[124:127]\n"
    "s_waitcnt lgkmcnt(9)\n"
    "v_cvt_pk_bf16_f32 v14, v16, v17\n"
    "v_cvt_pk_bf16_f32 v21, v22, v23\n"
    "v_mfma_f32_16x16x32_bf16 v[128:131], a[236:239], v[208:211], v[128:131]\n"
    "v_exp_f32 v58, v58\n"
    "v_mfma_f32_16x16x32_bf16 v[140:143], a[196:199], v[80:83], v[140:143]\n"
    "s_waitcnt lgkmcnt(4)\n"
    "ds_read_b128 a[228:231], v249 offset:36864\n"
    "v_cmp_le_u32 s[56:57], s59, v245\n"
    "v_mfma_f32_16x16x32_bf16 v[124:127], a[196:199], v[72:75], v[124:127]\n"
    "v_exp_f32 v62, v62\n"
    "v_mfma_f32_16x16x32_bf16 v[148:151], a[240:243], v[212:215], v[148:151]\n"
    "s_cmp_eq_u64 s[56:57], -1\n"
    "v_exp_f32 v40, v40\n"
    "v_mfma_f32_16x16x32_bf16 v[132:135], a[208:211], v[68:71], v[132:135]\n"
    "s_waitcnt lgkmcnt(4)\n"
    "v_exp_f32 v42, v42\n"
    "v_smfmac_f32_16x16x64_bf16 v[108:111], v[240:243], v[4:11], v253\n"
    "s_cbranch_scc0 .Lpt3\n"
    ".Lpt3r:\n"
    "v_mfma_f32_16x16x32_bf16 v[48:51], a[112:115], a[168:171], v[48:51]\n"
    "s_waitcnt lgkmcnt(1)\n"
    "ds_read_b128 a[176:179], v249 offset:2048\n"
    "ds_read_b128 a[168:171], v249 offset:1024\n"
    "v_mfma_f32_16x16x32_bf16 v[124:127], a[224:227], v[204:207], v[124:127]\n"
    "v_exp_f32 v18, v18\n"
    "v_mfma_f32_16x16x32_bf16 v[136:139], a[220:223], v[72:75], v[136:139]\n"
    "v_cvt_pk_bf16_f32 v16, v40, v41\n"
    "v_cvt_pk_bf16_f32 v15, v18, v19\n"
    "v_mfma_f32_16x16x32_bf16 v[152:155], a[220:223], v[80:83], v[152:155]\n"
    "s_waitcnt lgkmcnt(2)\n"
    "v_exp_f32 v61, v61\n"
    "v_mfma_f32_16x16x32_bf16 v[124:127], a[228:231], v[208:211], v[124:127]\n"
    "v_exp_f32 v63, v63\n"
    "v_mov_b32 v111, v110\n"
    "v_mfma_f32_16x16x32_bf16 v[152:155], a[216:219], v[76:79], v[152:155]\n"
    "v_add_f32 v108, v108, v109\n"
    "v_cvt_pk_bf16_f32 v22, v24, v25\n"
    "v_mfma_f32_16x16x32_bf16 v[132:135], a[212:215], v[72:75], v[132:135]\n"
    "ds_read_b128 a[188:191], v249 offset:15360\n"
    "ds_read_b128 a[248:251], v249 offset:27648\n"
    "v_mfma_f32_16x16x32_bf16 v[136:139], a[216:219], v[68:71], v[136:139]\n"
    "ds_read_b128 a[172:175], v249 offset:13312\n"
    "ds_read_b128 a[180:183], v249 offset:14336\n"
    "v_mfma_f32_16x16x32_bf16 v[144:147], a[200:203], v[76:79], v[144:147]\n"
    "v_exp_f32 v35, v35\n"
    "v_mfma_f32_16x16x32_bf16 v[132:135], a[240:243], v[204:207], v[132:135]\n"
    "ds_read_b128 a[160:163], v249 offset:0\n"
    "v_mov_b32 v109, v108\n"
    "v_mfma_f32_16x16x32_bf16 v[136:139], a[252:255], v[208:211], v[136:139]\n"
    "ds_read_b128 a[164:167], v249 offset:12288\n"
    "ds_read_b128 a[184:187], v249 offset:3072\n"
    "v_mfma_f32_16x16x32_bf16 v[132:135], a[244:247], v[208:211], v[132:135]\n"
    "s_waitcnt lgkmcnt(8)\n"
    "v_exp_f32 v46, v46\n"
    "v_mfma_f32_16x16x32_bf16 v[132:135], a[176:179], v[4:7], v[132:135]\n"
    "v_exp_f32 v59, v59\n"
    "v_mfma_f32_16x16x32_bf16 v[144:147], a[236:239], v[216:219], v[144:147]\n"
    "v_exp_f32 v67, v67\n"
    "v_mfma_f32_16x16x32_bf16 v[144:147], a[232:235], v[212:215], v[144:147]\n"
    "v_permlane16_swap_b32 v108, v109\n"
    "v_mfma_f32_16x16x32_bf16 v[128:131], a[232:235], v[204:207], v[128:131]\n"
    "s_waitcnt lgkmcnt(3)\n"
    "v_exp_f32 v44, v44\n"
    "v_mfma_f32_16x16x32_bf16 v[132:135], a[180:183], v[8:11], v[132:135]\n"
    "v_cvt_pk_bf16_f32 v23, v26, v27\n"
    "v_cvt_pk_bf16_f32 v27, v62, v63\n"
    "v_mfma_f32_16x16x32_bf16 v[136:139], a[248:251], v[204:207], v[136:139]\n"
    "s_waitcnt lgkmcnt(2)\n"
    "v_exp_f32 v57, v57\n"
    "v_mfma_f32_16x16x32_bf16 v[124:127], a[160:163], v[4:7], v[124:127]\n"
    "v_exp_f32 v56, v56\n"
    "v_mfma_f32_16x16x32_bf16 v[140:143], a[224:227], v[212:215], v[140:143]\n"
    "s_waitcnt lgkmcnt(1)\n"
    "v_and_b32 v252, 0x70, v252\n"
    "v_add_f32 v108, v108, v109\n"
    "v_mfma_f32_16x16x32_bf16 v[124:127], a[164:167], v[8:11], v[124:127]\n"
    "v_permlane16_swap_b32 v110, v111\n"
    "v_mfma_f32_16x16x32_bf16 v[128:131], a[172:175], v[8:11], v[128:131]\n"
    "v_add_f32 v110, v110, v111\n"
    "v_cvt_pk_bf16_f32 v18, v56, v57\n"
    "v_mfma_f32_16x16x32_bf16 v[128:131], a[168:171], v[4:7], v[128:131]\n"
    ".Lwp1:\n"
    "s_cbranch_execz .Lwp2\n"
    "v_rcp_f32 v109, v108\n"
    "v_mfma_f32_16x16x32_bf16 v[136:139], a[188:191], v[8:11], v[136:139]\n"
    "s_waitcnt lgkmcnt(0)\n"
    "v_exp_f32 v60, v60\n"
    "v_mfma_f32_16x16x32_bf16 v[136:139], a[184:187], v[4:7], v[136:139]\n"
    "v_mul_f32 v111, -v110, v109\n"
    "v_cvt_pk_bf16_f32 v19, v58, v59\n"
    "v_cvt_pk_bf16_f32 v17, v42, v43\n"
    "v_xor_b32 v252, v252, v1\n"
    "v_ldexp_f32 v109, v109, s79\n"
    "v_pk_fma_f32 v[134:135], v[134:135], v[108:109], v[110:111] op_sel:[0,1,1] op_sel_hi:[1,1,1]\n"
    "v_pk_fma_f32 v[124:125], v[124:125], v[108:109], v[110:111] op_sel:[0,1,1] op_sel_hi:[1,1,1]\n"
    "v_pk_fma_f32 v[126:127], v[126:127], v[108:109], v[110:111] op_sel:[0,1,1] op_sel_hi:[1,1,1]\n"
    "v_pk_fma_f32 v[128:129], v[128:129], v[108:109], v[110:111] op_sel:[0,1,1] op_sel_hi:[1,1,1]\n"
    "v_pk_fma_f32 v[132:133], v[132:133], v[108:109], v[110:111] op_sel:[0,1,1] op_sel_hi:[1,1,1]\n"
    "v_pk_fma_f32 v[130:131], v[130:131], v[108:109], v[110:111] op_sel:[0,1,1] op_sel_hi:[1,1,1]\n"
    "v_pk_fma_f32 v[138:139], v[138:139], v[108:109], v[110:111] op_sel:[0,1,1] op_sel_hi:[1,1,1]\n"
    "v_pk_fma_f32 v[136:137], v[136:137], v[108:109], v[110:111] op_sel:[0,1,1] op_sel_hi:[1,1,1]\n"
    "v_cvt_pk_bf16_f32 v124, v124, v128\n"
    "v_cvt_pk_bf16_f32 v128, v126, v130\n"
    "v_mfma_f32_16x16x32_bf16 v[152:155], a[252:255], v[216:219], v[152:155]\n"
    "v_cvt_pk_bf16_f32 v130, v127, v131\n"
    "v_cvt_pk_bf16_f32 v126, v125, v129\n"
    "v_cvt_pk_bf16_f32 v127, v133, v137\n"
    "v_mfma_f32_16x16x32_bf16 v[148:151], a[244:247], v[216:219], v[148:151]\n"
    "v_add_u32 v252, s72, v252\n"
    "v_cvt_pk_bf16_f32 v125, v132, v136\n"
    "v_mfma_f32_16x16x32_bf16 v[140:143], a[228:231], v[216:219], v[140:143]\n"
    "ds_write_b128 v3, v[124:127] offset:0\n"
    "v_smfmac_f32_16x16x64_bf16 v[112:115], v[240:243], v[12:19], v253\n"
    "v_cvt_pk_bf16_f32 v129, v134, v138\n"
    "v_cvt_pk_bf16_f32 v26, v60, v61\n"
    "v_mfma_f32_16x16x32_bf16 v[148:151], a[176:179], v[12:15], v[148:151]\n"
    "v_exp_f32 v51, v51\n"
    "v_mfma_f32_16x16x32_bf16 v[140:143], a[160:163], v[12:15], v[140:143]\n"
    "v_cvt_pk_bf16_f32 v131, v135, v139\n"
    "v_xor_b32 v2, 64, v3\n"
    "v_mfma_f32_16x16x32_bf16 v[156:159], a[192:195], v[84:87], v[156:159]\n"
    "ds_write_b128 v2, v[128:131] offset:0\n"
    "v_mfma_f32_16x16x32_bf16 v[156:159], a[196:199], v[88:91], v[156:159]\n"
    "v_exp_f32 v47, v47\n"
    "v_mfma_f32_16x16x32_bf16 v[152:155], a[248:251], v[212:215], v[152:155]\n"
    "v_cvt_pk_bf16_f32 v24, v44, v45\n"
    "v_cvt_pk_bf16_f32 v25, v46, v47\n"
    "v_mfma_f32_16x16x32_bf16 v[156:159], a[224:227], v[220:223], v[156:159]\n"
    "ds_read_b128 v[132:135], v252 offset:0\n"
    "v_add_f32 v112, v112, v113\n"
    "v_mfma_f32_16x16x32_bf16 v[140:143], a[164:167], v[16:19], v[140:143]\n"
    "v_exp_f32 v34, v34\n"
    "v_mfma_f32_16x16x32_bf16 v[144:147], a[172:175], v[16:19], v[144:147]\n"
    "v_mov_b32 v113, v112\n"
    "v_mov_b32 v115, v114\n"
    "v_mfma_f32_16x16x32_bf16 v[144:147], a[168:171], v[12:15], v[144:147]\n"
    "ds_read_b128 v[136:139], v252 offset:1024\n"
    "v_permlane16_swap_b32 v112, v113\n"
    "v_mfma_f32_16x16x32_bf16 v[164:167], a[208:211], v[84:87], v[164:167]\n"
    "v_permlane16_swap_b32 v114, v115\n"
    "v_mfma_f32_16x16x32_bf16 v[152:155], a[188:191], v[16:19], v[152:155]\n"
    "v_exp_f32_e64 v48, v48\n"
    "v_mfma_f32_16x16x32_bf16 v[148:151], a[180:183], v[16:19], v[148:151]\n"
    "v_add_f32 v112, v112, v113\n"
    "v_cvt_pk_bf16_f32 v28, v28, v29\n"
    "v_mfma_f32_16x16x32_bf16 v[152:155], a[184:187], v[12:15], v[152:155]\n"
    "v_rcp_f32 v113, v112\n"
    "v_smfmac_f32_16x16x64_bf16 v[116:119], v[240:243], v[20:27], v253\n"
    "s_waitcnt lgkmcnt(1)\n"
    "buffer_store_dwordx4 v[132:135], v244, s[24:27], s75 offen offset:0 sc1\n"
    "v_add_f32 v114, v114, v115\n"
    "v_mul_f32 v115, -v114, v113\n"
    "v_ldexp_f32 v113, v113, s79\n"
    "v_pk_fma_f32 v[142:143], v[142:143], v[112:113], v[114:115] op_sel:[0,1,1] op_sel_hi:[1,1,1]\n"
    "v_pk_fma_f32 v[144:145], v[144:145], v[112:113], v[114:115] op_sel:[0,1,1] op_sel_hi:[1,1,1]\n"
    "v_pk_fma_f32 v[140:141], v[140:141], v[112:113], v[114:115] op_sel:[0,1,1] op_sel_hi:[1,1,1]\n"
    "v_pk_fma_f32 v[146:147], v[146:147], v[112:113], v[114:115] op_sel:[0,1,1] op_sel_hi:[1,1,1]\n"
    "v_pk_fma_f32 v[150:151], v[150:151], v[112:113], v[114:115] op_sel:[0,1,1] op_sel_hi:[1,1,1]\n"
    "v_add_f32_e64 v116, v116, v117\n"
    "v_pk_fma_f32 v[152:153], v[152:153], v[112:113], v[114:115] op_sel:[0,1,1] op_sel_hi:[1,1,1]\n"
    "v_pk_fma_f32 v[148:149], v[148:149], v[112:113], v[114:115] op_sel:[0,1,1] op_sel_hi:[1,1,1]\n"
    "v_pk_fma_f32 v[154:155], v[154:155], v[112:113], v[114:115] op_sel:[0,1,1] op_sel_hi:[1,1,1]\n"
    "v_cvt_pk_bf16_f32 v140, v140, v144\n"
    "v_cvt_pk_bf16_f32 v144, v142, v146\n"
    "v_mfma_f32_16x16x32_bf16 v[160:163], a[204:207], v[88:91], v[160:163]\n"
    "v_cvt_pk_bf16_f32 v146, v143, v147\n"
    "v_cvt_pk_bf16_f32 v142, v141, v145\n"
    "v_mfma_f32_16x16x32_bf16 v[156:159], a[228:231], v[224:227], v[156:159]\n"
    "v_cvt_pk_bf16_f32 v141, v148, v152\n"
    "v_cvt_pk_bf16_f32 v143, v149, v153\n"
    "v_mfma_f32_16x16x32_bf16 v[156:159], a[160:163], v[20:23], v[156:159]\n"
    "s_waitcnt lgkmcnt(0)\n"
    "buffer_store_dwordx4 v[136:139], v244, s[24:27], s75 offen offset:3072 sc1\n"
    "ds_write_b128 v3, v[140:143] offset:2048\n"
    "v_mfma_f32_16x16x32_bf16 v[172:175], a[192:195], v[92:95], v[172:175]\n"
    "v_mov_b32 v119, v118\n"
    "v_cvt_pk_bf16_f32 v145, v150, v154\n"
    "v_mfma_f32_16x16x32_bf16 v[156:159], a[164:167], v[24:27], v[156:159]\n"
    "v_cvt_pk_bf16_f32 v147, v151, v155\n"
    "v_mov_b32 v117, v116\n"
    "v_mfma_f32_16x16x32_bf16 v[180:183], a[208:211], v[92:95], v[180:183]\n"
    "ds_write_b128 v2, v[144:147] offset:2048\n"
    "v_mfma_f32_16x16x32_bf16 v[160:163], a[200:203], v[84:87], v[160:163]\n"
    "v_permlane16_swap_b32 v118, v119\n"
    "v_mfma_f32_16x16x32_bf16 v[168:171], a[220:223], v[88:91], v[168:171]\n"
    "v_exp_f32 v33, v33\n"
    "v_mfma_f32_16x16x32_bf16 v[172:175], a[196:199], v[96:99], v[172:175]\n"
    "ds_read_b128 v[148:151], v252 offset:2048\n"
    "ds_read_b128 v[152:155], v252 offset:3072\n"
    "v_mfma_f32_16x16x32_bf16 v[180:183], a[212:215], v[96:99], v[180:183]\n"
    "v_exp_f32 v49, v49\n"
    "v_mfma_f32_16x16x32_bf16 v[160:163], a[236:239], v[224:227], v[160:163]\n"
    "v_permlane16_swap_b32 v116, v117\n"
    "v_mfma_f32_16x16x32_bf16 v[168:171], a[216:219], v[84:87], v[168:171]\n"
    "v_exp_f32 v31, v31\n"
    "v_mfma_f32_16x16x32_bf16 v[176:179], a[204:207], v[96:99], v[176:179]\n"
    "v_exp_f32 v66, v66\n"
    "v_mfma_f32_16x16x32_bf16 v[164:167], a[212:215], v[88:91], v[164:167]\n"
    "ds_read_b128 a[204:207], v248 offset:17408\n"
    "v_add_f32 v118, v118, v119\n"
    "v_mfma_f32_16x16x32_bf16 v[160:163], a[232:235], v[220:223], v[160:163]\n"
    "v_cvt_pk_bf16_f32 v29, v30, v31\n"
    "v_add_f32 v116, v116, v117\n"
    "v_mfma_f32_16x16x32_bf16 v[168:171], a[252:255], v[224:227], v[168:171]\n"
    "v_rcp_f32 v117, v116\n"
    "v_mfma_f32_16x16x32_bf16 v[160:163], a[172:175], v[24:27], v[160:163]\n"
    "s_waitcnt lgkmcnt(1)\n"
    "buffer_store_dwordx4 v[152:155], v244, s[24:27], s76 offen offset:3072 sc1\n"
    "v_exp_f32 v50, v50\n"
    "v_exp_f32 v64, v64\n"
    "v_mfma_f32_16x16x32_bf16 v[176:179], a[200:203], v[92:95], v[176:179]\n"
    "ds_read_b128 a[196:199], v248 offset:16384\n"
    "v_cvt_pk_bf16_f32 v31, v34, v35\n"
    "v_mfma_f32_16x16x32_bf16 v[160:163], a[168:171], v[20:23], v[160:163]\n"
    "v_mul_f32 v119, -v118, v117\n"
    "v_cvt_pk_bf16_f32 v30, v32, v33\n"
    "v_mfma_f32_16x16x32_bf16 v[164:167], a[240:243], v[220:223], v[164:167]\n"
    "buffer_store_dwordx4 v[148:151], v244, s[24:27], s76 offen offset:0 sc1\n"
    "v_cvt_pk_bf16_f32 v35, v66, v67\n"
    "v_ldexp_f32 v117, v117, s79\n"
    "v_exp_f32 v65, v65\n"
    "v_mfma_f32_16x16x32_bf16 v[184:187], a[220:223], v[96:99], v[184:187]\n"
    "v_fma_f32 v156, v156, v117, v119\n"
    "v_fma_f32 v157, v157, v117, v119\n"
    "v_mfma_f32_16x16x32_bf16 v[168:171], a[248:251], v[220:223], v[168:171]\n"
    "v_fma_f32 v158, v158, v117, v119\n"
    "v_fma_f32 v159, v159, v117, v119\n"
    "v_mfma_f32_16x16x32_bf16 v[164:167], a[244:247], v[224:227], v[164:167]\n"
    "v_fma_f32 v160, v160, v117, v119\n"
    "v_fma_f32 v161, v161, v117, v119\n"
    "v_mfma_f32_16x16x32_bf16 v[168:171], a[188:191], v[24:27], v[168:171]\n"
    "v_fma_f32 v162, v162, v117, v119\n"
    "v_fma_f32 v163, v163, v117, v119\n"
    "v_mfma_f32_16x16x32_bf16 v[172:175], a[224:227], v[228:231], v[172:175]\n"
    "ds_read_b128 a[224:227], v249 offset:28672\n"
    "v_cvt_pk_bf16_f32 v34, v64, v65\n"
    "v_mfma_f32_16x16x32_bf16 v[168:171], a[184:187], v[20:23], v[168:171]\n"
    "v_cvt_pk_bf16_f32 v156, v156, v160\n"
    "v_cvt_pk_bf16_f32 v160, v158, v162\n"
    "v_mfma_f32_16x16x32_bf16 v[164:167], a[176:179], v[20:23], v[164:167]\n"
    "v_cvt_pk_bf16_f32 v162, v159, v163\n"
    "v_cvt_pk_bf16_f32 v32, v48, v49\n"
    "v_mfma_f32_16x16x32_bf16 v[164:167], a[180:183], v[24:27], v[164:167]\n"
    "ds_read_b128 a[192:195], v248 offset:4096\n"
    "v_cvt_pk_bf16_f32 v158, v157, v161\n"
    "v_mfma_f32_16x16x32_bf16 v[180:183], a[240:243], v[228:231], v[180:183]\n"
    "v_cvt_pk_bf16_f32 v33, v50, v51\n"
    "v_fma_f32 v168, v168, v117, v119\n"
    "v_fma_f32 v169, v169, v117, v119\n"
    "v_mfma_f32_16x16x32_bf16 v[184:187], a[216:219], v[92:95], v[184:187]\n"
    "v_fma_f32 v170, v170, v117, v119\n"
    "v_fma_f32 v171, v171, v117, v119\n"
    "v_smfmac_f32_16x16x64_bf16 v[120:123], v[240:243], v[28:35], v253\n"
    "ds_read_b128 a[220:223], v248 offset:19456\n"
    "v_mfma_f32_16x16x32_bf16 v[184:187], a[252:255], v[232:235], v[184:187]\n"
    "v_fma_f32 v164, v164, v117, v119\n"
    "v_fma_f32 v165, v165, v117, v119\n"
    "v_mfma_f32_16x16x32_bf16 v[172:175], a[228:231], v[232:235], v[172:175]\n"
    "v_cvt_pk_bf16_f32 v157, v164, v168\n"
    "v_cvt_pk_bf16_f32 v159, v165, v169\n"
    "v_mfma_f32_16x16x32_bf16 v[176:179], a[236:239], v[232:235], v[176:179]\n"
    "ds_write_b128 v3, v[156:159] offset:4096\n"
    "v_mfma_f32_16x16x32_bf16 v[180:183], a[244:247], v[232:235], v[180:183]\n"
    "s_waitcnt lgkmcnt(2)\n"
    "v_fma_f32 v166, v166, v117, v119\n"
    "v_fma_f32 v167, v167, v117, v119\n"
    "v_mfma_f32_16x16x32_bf16 v[188:191], a[192:195], v[68:71], v[188:191]\n"
    "v_cvt_pk_bf16_f32 v163, v167, v171\n"
    "v_cvt_pk_bf16_f32 v161, v166, v170\n"
    "v_mfma_f32_16x16x32_bf16 v[184:187], a[248:251], v[228:231], v[184:187]\n"
    "ds_write_b128 v2, v[160:163] offset:4096\n"
    "v_mfma_f32_16x16x32_bf16 v[192:195], a[204:207], v[72:75], v[192:195]\n"
    "v_add_f32_e64 v120, v120, v121\n"
    "v_mov_b32_e64 v121, v120\n"
    "v_mfma_f32_16x16x32_bf16 v[188:191], a[196:199], v[72:75], v[188:191]\n"
    "s_nop 0\n"
    "v_permlane16_swap_b32 v120, v121\n"
    "v_mfma_f32_16x16x32_bf16 v[176:179], a[232:235], v[228:231], v[176:179]\n"
    "s_waitcnt lgkmcnt(2)\n"
    "ds_read_b128 v[164:167], v252 offset:4096\n"
    "ds_read_b128 a[228:231], v249 offset:40960\n"
    "v_mfma_f32_16x16x32_bf16 v[200:203], a[220:223], v[72:75], v[200:203]\n"
    "ds_read_b128 a[212:215], v248 offset:18432\n"
    "ds_read_b128 v[168:171], v252 offset:5120\n"
    "v_mfma_f32_16x16x32_bf16 v[180:183], a[176:179], v[28:31], v[180:183]\n"
    "ds_read_b128 a[132:135], v249 offset:18432\n"
    "v_mov_b32 v123, v122\n"
    "v_mfma_f32_16x16x32_bf16 v[176:179], a[172:175], v[32:35], v[176:179]\n"
    "ds_read_b128 a[208:211], v248 offset:6144\n"
    "ds_read_b128 a[216:219], v248 offset:7168\n"
    "v_mfma_f32_16x16x32_bf16 v[188:191], a[224:227], v[204:207], v[188:191]\n"
    "ds_read_b128 a[248:251], v249 offset:31744\n"
    "ds_read_b128 a[236:239], v249 offset:41984\n"
    "v_mfma_f32_16x16x32_bf16 v[184:187], a[188:191], v[32:35], v[184:187]\n"
    "v_permlane16_swap_b32_e64 v122, v123\n"
    "v_mfma_f32_16x16x32_bf16 v[172:175], a[160:163], v[28:31], v[172:175]\n"
    "ds_read_b128 a[140:143], v249 offset:19456\n"
    "ds_read_b128 a[200:203], v248 offset:5120\n"
    "v_mfma_f32_16x16x32_bf16 v[180:183], a[180:183], v[32:35], v[180:183]\n"
    "ds_read_b128 a[136:139], v249 offset:7168\n"
    "ds_read_b128 a[124:127], v249 offset:17408\n"
    "v_mfma_f32_16x16x32_bf16 v[184:187], a[184:187], v[28:31], v[184:187]\n"
    "s_waitcnt lgkmcnt(12)\n"
    "buffer_store_dwordx4 v[164:167], v244, s[24:27], s77 offen offset:0 sc1\n"
    "ds_read_b128 a[240:243], v249 offset:30720\n"
    "ds_read_b128 a[252:255], v249 offset:44032\n"
    "v_add_f32 v122, v122, v123\n"
    "v_add_f32 v120, v120, v121\n"
    "v_mfma_f32_16x16x32_bf16 v[176:179], a[168:171], v[28:31], v[176:179]\n"
    "ds_read_b128 a[112:115], v249 offset:4096\n"
    "ds_read_b128 a[128:131], v249 offset:6144\n"
    "v_mfma_f32_16x16x32_bf16 v[172:175], a[164:167], v[32:35], v[172:175]\n"
    "s_waitcnt lgkmcnt(11)\n"
    "v_rcp_f32 v121, v120\n"
    "v_mfma_f32_16x16x32_bf16 v[196:199], a[208:211], v[68:71], v[196:199]\n"
    "ds_read_b128 a[244:247], v249 offset:43008\n"
    "v_mul_f32 v123, -v122, v121\n"
    "v_mfma_f32_16x16x32_bf16 v[188:191], a[228:231], v[208:211], v[188:191]\n"
    "buffer_store_dwordx4 v[168:171], v244, s[24:27], s77 offen offset:3072 sc1\n"
    "ds_read_b128 a[232:235], v249 offset:29696\n"
    "ds_read_b128 a[116:119], v249 offset:16384\n"
    "ds_read_b128 a[120:123], v249 offset:5120\n"
    "v_ldexp_f32 v121, v121, s79\n"
    "v_mfma_f32_16x16x32_bf16 v[196:199], a[212:215], v[72:75], v[196:199]\n"
    "s_waitcnt lgkmcnt(10)\n"
    "v_fma_f32 v174, v174, v121, v123\n"
    "v_fma_f32 v175, v175, v121, v123\n"
    "v_mfma_f32_16x16x32_bf16 v[192:195], a[200:203], v[68:71], v[192:195]\n"
    "v_fma_f32 v176, v176, v121, v123\n"
    "v_fma_f32 v177, v177, v121, v123\n"
    "v_mfma_f32_16x16x32_bf16 v[200:203], a[216:219], v[68:71], v[200:203]\n"
    "s_waitcnt lgkmcnt(7)\n"
    "v_fma_f32 v186, v186, v121, v123\n"
    "v_fma_f32 v187, v187, v121, v123\n"
    "v_mfma_f32_16x16x32_bf16 v[196:199], a[240:243], v[204:207], v[196:199]\n"
    "s_waitcnt lgkmcnt(6)\n"
    "v_fma_f32 v182, v182, v121, v123\n"
    "v_fma_f32 v183, v183, v121, v123\n"
    "v_mfma_f32_16x16x32_bf16 v[200:203], a[252:255], v[208:211], v[200:203]\n"
    "v_fma_f32 v178, v178, v121, v123\n"
    "v_fma_f32 v179, v179, v121, v123\n"
    "v_mfma_f32_16x16x32_bf16 v[200:203], a[248:251], v[204:207], v[200:203]\n"
    "s_waitcnt lgkmcnt(3)\n"
    "v_fma_f32 v172, v172, v121, v123\n"
    "v_fma_f32 v173, v173, v121, v123\n"
    "v_mfma_f32_16x16x32_bf16 v[196:199], a[244:247], v[208:211], v[196:199]\n"
    "v_cvt_pk_bf16_f32 v172, v172, v176\n"
    "v_cvt_pk_bf16_f32 v176, v174, v178\n"
    "v_mfma_f32_16x16x32_bf16 v[192:195], a[236:239], v[208:211], v[192:195]\n"
    "s_waitcnt lgkmcnt(2)\n"
    "v_cvt_pk_bf16_f32 v174, v173, v177\n"
    "v_cvt_pk_bf16_f32 v178, v175, v179\n"
    "v_mfma_f32_16x16x32_bf16 v[192:195], a[232:235], v[204:207], v[192:195]\n"
    "v_cvt_pk_bf16_f32 v177, v182, v186\n"
    "v_cvt_pk_bf16_f32 v179, v183, v187\n"
    "v_mfma_f32_16x16x32_bf16 v[196:199], a[128:131], v[4:7], v[196:199]\n"
    "v_fma_f32 v180, v180, v121, v123\n"
    "v_fma_f32 v181, v181, v121, v123\n"
    "v_mfma_f32_16x16x32_bf16 v[188:191], a[112:115], v[4:7], v[188:191]\n"
    "s_waitcnt lgkmcnt(1)\n"
    "ds_write_b128 v2, v[176:179] offset:0\n"
    "v_mfma_f32_16x16x32_bf16 v[188:191], a[116:119], v[8:11], v[188:191]\n"
    "v_fma_f32 v184, v184, v121, v123\n"
    "v_fma_f32 v185, v185, v121, v123\n"
    "v_mfma_f32_16x16x32_bf16 v[192:195], a[124:127], v[8:11], v[192:195]\n"
    "v_cvt_pk_bf16_f32 v175, v181, v185\n"
    "v_cvt_pk_bf16_f32 v173, v180, v184\n"
    "v_mfma_f32_16x16x32_bf16 v[196:199], a[132:135], v[8:11], v[196:199]\n"
    "ds_write_b128 v3, v[172:175] offset:0\n"
    "v_mfma_f32_16x16x32_bf16 a[4:7], a[204:207], v[80:83], a[4:7]\n"
    "v_fma_f32 v188, v188, v109, v111\n"
    "v_fma_f32 v189, v189, v109, v111\n"
    "v_mfma_f32_16x16x32_bf16 a[0:3], a[192:195], v[76:79], a[0:3]\n"
    "s_waitcnt lgkmcnt(2)\n"
    "v_fma_f32 v190, v190, v109, v111\n"
    "v_fma_f32 v191, v191, v109, v111\n"
    "v_mfma_f32_16x16x32_bf16 v[192:195], a[120:123], v[4:7], v[192:195]\n"
    "ds_read_b128 v[180:183], v252 offset:0\n"
    "ds_read_b128 v[184:187], v252 offset:1024\n"
    "v_mfma_f32_16x16x32_bf16 v[200:203], a[140:143], v[8:11], v[200:203]\n"
    "v_fma_f32 v198, v198, v109, v111\n"
    "v_fma_f32 v199, v199, v109, v111\n"
    "v_mfma_f32_16x16x32_bf16 a[0:3], a[196:199], v[80:83], a[0:3]\n"
    "v_fma_f32 v196, v196, v109, v111\n"
    "v_fma_f32 v197, v197, v109, v111\n"
    "v_mfma_f32_16x16x32_bf16 a[12:15], a[220:223], v[80:83], a[12:15]\n"
    "v_fma_f32 v192, v192, v109, v111\n"
    "v_fma_f32 v193, v193, v109, v111\n"
    "v_mfma_f32_16x16x32_bf16 a[4:7], a[200:203], v[76:79], a[4:7]\n"
    "ds_read_b128 a[164:167], v249 offset:20480\n"
    "ds_read_b128 a[180:183], v249 offset:22528\n"
    "v_mfma_f32_16x16x32_bf16 v[200:203], a[136:139], v[4:7], v[200:203]\n"
    "v_fma_f32 v194, v194, v109, v111\n"
    "v_fma_f32 v195, v195, v109, v111\n"
    "v_mfma_f32_16x16x32_bf16 a[0:3], a[224:227], v[212:215], a[0:3]\n"
    "ds_read_b128 a[172:175], v249 offset:21504\n"
    "v_mfma_f32_16x16x32_bf16 a[4:7], a[236:239], v[216:219], a[4:7]\n"
    "v_cvt_pk_bf16_f32 v188, v188, v192\n"
    "v_cvt_pk_bf16_f32 v192, v190, v194\n"
    "v_mfma_f32_16x16x32_bf16 a[0:3], a[228:231], v[216:219], a[0:3]\n"
    "s_waitcnt lgkmcnt(4)\n"
    "buffer_store_dwordx4 v[180:183], v244, s[24:27], s78 offen offset:0 sc1\n"
    "v_cvt_pk_bf16_f32 v190, v189, v193\n"
    "v_cvt_pk_bf16_f32 v194, v191, v195\n"
    "v_fma_f32 v200, v200, v109, v111\n"
    "v_fma_f32 v201, v201, v109, v111\n"
    "v_mfma_f32_16x16x32_bf16 a[0:3], a[112:115], v[12:15], a[0:3]\n"
    "v_cvt_pk_bf16_f32 v189, v196, v200\n"
    "v_cvt_pk_bf16_f32 v191, v197, v201\n"
    "v_mfma_f32_16x16x32_bf16 a[0:3], a[116:119], v[16:19], a[0:3]\n"
    "ds_read_b128 a[176:179], v249 offset:10240\n"
    "v_mfma_f32_16x16x32_bf16 a[4:7], a[232:235], v[212:215], a[4:7]\n"
    "s_waitcnt lgkmcnt(4)\n"
    "buffer_store_dwordx4 v[184:187], v244, s[24:27], s78 offen offset:3072 sc1\n"
    "ds_write_b128 v3, v[188:191] offset:2048\n"
    "v_mfma_f32_16x16x32_bf16 a[8:11], a[208:211], v[76:79], a[8:11]\n"
    "v_fma_f32 v202, v202, v109, v111\n"
    "v_fma_f32 v203, v203, v109, v111\n"
    "v_mfma_f32_16x16x32_bf16 a[12:15], a[216:219], v[76:79], a[12:15]\n"
    "v_cvt_pk_bf16_f32 v195, v199, v203\n"
    "v_cvt_pk_bf16_f32 v193, v198, v202\n"
    "v_mfma_f32_16x16x32_bf16 a[12:15], a[252:255], v[216:219], a[12:15]\n"
    "ds_write_b128 v2, v[192:195] offset:2048\n"
    "v_mfma_f32_16x16x32_bf16 a[4:7], a[124:127], v[16:19], a[4:7]\n"
    "v_accvgpr_read_b32 v40, a1\n"
    "v_mfma_f32_16x16x32_bf16 a[20:23], a[204:207], v[88:91], a[20:23]\n"
    "v_accvgpr_read_b32 v56, a3\n"
    "v_mov_b32_e64 v250, 0\n"
    "v_mfma_f32_16x16x32_bf16 a[4:7], a[120:123], v[12:15], a[4:7]\n"
    "ds_read_b128 v[200:203], v252 offset:3072\n"
    "ds_read_b128 v[196:199], v252 offset:2048\n"
    "v_mfma_f32_16x16x32_bf16 a[8:11], a[212:215], v[80:83], a[8:11]\n"
    "ds_read_b128 a[188:191], v249 offset:23552\n"
    "v_mfma_f32_16x16x32_bf16 a[8:11], a[240:243], v[212:215], a[8:11]\n"
    "v_accvgpr_read_b32 v36, a0\n"
    "v_accvgpr_read_b32 v52, a2\n"
    "v_mfma_f32_16x16x32_bf16 a[8:11], a[244:247], v[216:219], a[8:11]\n"
    "v_accvgpr_read_b32 v41, a5\n"
    "v_accvgpr_read_b32 v57, a7\n"
    "v_mfma_f32_16x16x32_bf16 a[8:11], a[128:131], v[12:15], a[8:11]\n"
    "v_accvgpr_read_b32 v37, a4\n"
    "v_accvgpr_read_b32 v53, a6\n"
    "v_mfma_f32_16x16x32_bf16 a[8:11], a[132:135], v[16:19], a[8:11]\n"
    "v_fma_f32 v40, v40, v113, v115\n"
    "v_fma_f32 v41, v41, v113, v115\n"
    "v_mfma_f32_16x16x32_bf16 a[12:15], a[248:251], v[212:215], a[12:15]\n"
    "ds_read_b128 a[160:163], v249 offset:8192\n"
    "v_mfma_f32_16x16x32_bf16 a[12:15], a[140:143], v[16:19], a[12:15]\n"
    "v_fma_f32 v52, v52, v113, v115\n"
    "v_fma_f32 v53, v53, v113, v115\n"
    "v_mfma_f32_16x16x32_bf16 a[12:15], a[136:139], v[12:15], a[12:15]\n"
    "s_waitcnt lgkmcnt(3)\n"
    "buffer_store_dwordx4 v[200:203], v244, s[24:27], s75 offen offset:3200 sc1\n"
    "v_cvt_pk_bf16_f32 v52, v52, v53\n"
    "v_fma_f32 v36, v36, v113, v115\n"
    "v_fma_f32 v37, v37, v113, v115\n"
    "v_accvgpr_read_b32 v38, a8\n"
    "v_mfma_f32_16x16x32_bf16 a[16:19], a[192:195], v[84:87], a[16:19]\n"
    "ds_read_b128 a[168:171], v249 offset:9216\n"
    "v_accvgpr_read_b32 v42, a9\n"
    "v_mfma_f32_16x16x32_bf16 a[16:19], a[196:199], v[88:91], a[16:19]\n"
    "v_accvgpr_read_b32 v54, a10\n"
    "v_accvgpr_read_b32 v39, a12\n"
    "v_mfma_f32_16x16x32_bf16 a[20:23], a[200:203], v[84:87], a[20:23]\n"
    "v_accvgpr_read_b32 v55, a14\n"
    "v_accvgpr_read_b32 v58, a11\n"
    "v_mfma_f32_16x16x32_bf16 a[16:19], a[224:227], v[220:223], a[16:19]\n"
    "v_accvgpr_read_b32 v59, a15\n"
    "v_cvt_pk_bf16_f32 v36, v36, v37\n"
    "v_mfma_f32_16x16x32_bf16 a[20:23], a[236:239], v[224:227], a[20:23]\n"
    "v_fma_f32 v38, v38, v113, v115\n"
    "v_fma_f32 v39, v39, v113, v115\n"
    "v_mfma_f32_16x16x32_bf16 a[24:27], a[208:211], v[84:87], a[24:27]\n"
    "v_fma_f32 v54, v54, v113, v115\n"
    "v_fma_f32 v55, v55, v113, v115\n"
    "v_mfma_f32_16x16x32_bf16 a[20:23], a[232:235], v[220:223], a[20:23]\n"
    "v_accvgpr_read_b32 v43, a13\n"
    "v_cvt_pk_bf16_f32 v53, v54, v55\n"
    "v_mfma_f32_16x16x32_bf16 a[20:23], a[124:127], v[24:27], a[20:23]\n"
    "v_fma_f32 v42, v42, v113, v115\n"
    "v_fma_f32 v43, v43, v113, v115\n"
    "v_mfma_f32_16x16x32_bf16 a[16:19], a[228:231], v[224:227], a[16:19]\n"
    "ds_read_b128 a[184:187], v249 offset:11264\n"
    "v_cvt_pk_bf16_f32 v37, v38, v39\n"
    "v_mfma_f32_16x16x32_bf16 a[20:23], a[120:123], v[20:23], a[20:23]\n"
    "v_cvt_pk_bf16_f32 v39, v42, v43\n"
    "v_cvt_pk_bf16_f32 v38, v40, v41\n"
    "v_mfma_f32_16x16x32_bf16 a[28:31], a[220:223], v[88:91], a[28:31]\n"
    "s_waitcnt lgkmcnt(4)\n"
    "buffer_store_dwordx4 v[196:199], v244, s[24:27], s75 offen offset:128 sc1\n"
    "ds_write_b128 v3, v[36:39] offset:4096\n"
    "v_mfma_f32_16x16x32_bf16 a[16:19], a[112:115], v[20:23], a[16:19]\n"
    "v_fma_f32 v58, v58, v113, v115\n"
    "v_fma_f32 v59, v59, v113, v115\n"
    "v_mfma_f32_16x16x32_bf16 a[16:19], a[116:119], v[24:27], a[16:19]\n"
    "v_fma_f32 v56, v56, v113, v115\n"
    "v_fma_f32 v57, v57, v113, v115\n"
    "v_cvt_pk_bf16_f32 v55, v58, v59\n"
    "v_mfma_f32_16x16x32_bf16 a[28:31], a[216:219], v[84:87], a[28:31]\n"
    "v_accvgpr_read_b32 v49, a21\n"
    "v_cvt_pk_bf16_f32 v54, v56, v57\n"
    "v_mfma_f32_16x16x32_bf16 a[24:27], a[212:215], v[88:91], a[24:27]\n"
    "ds_write_b128 v2, v[52:55] offset:4096\n"
    "v_mfma_f32_16x16x32_bf16 a[32:35], a[192:195], v[92:95], a[32:35]\n"
    "v_accvgpr_read_b32 v64, a19\n"
    "v_accvgpr_read_b32 v45, a20\n"
    "v_mfma_f32_16x16x32_bf16 a[24:27], a[240:243], v[220:223], a[24:27]\n"
    "v_accvgpr_read_b32 v44, a16\n"
    "v_accvgpr_read_b32 v65, a23\n"
    "v_mfma_f32_16x16x32_bf16 a[24:27], a[244:247], v[224:227], a[24:27]\n"
    "ds_read_b128 v[56:59], v252 offset:5120\n"
    "v_accvgpr_read_b32 v48, a17\n"
    "v_mfma_f32_16x16x32_bf16 a[28:31], a[252:255], v[224:227], a[28:31]\n"
    "v_fma_f32 v64, v64, v117, v119\n"
    "v_fma_f32 v65, v65, v117, v119\n"
    "v_mfma_f32_16x16x32_bf16 a[24:27], a[128:131], v[20:23], a[24:27]\n"
    "ds_read_b128 v[40:43], v252 offset:4096\n"
    "v_accvgpr_read_b32 v60, a18\n"
    "v_mfma_f32_16x16x32_bf16 a[24:27], a[132:135], v[24:27], a[24:27]\n"
    "v_fma_f32 v44, v44, v117, v119\n"
    "v_fma_f32 v45, v45, v117, v119\n"
    "v_mfma_f32_16x16x32_bf16 a[28:31], a[248:251], v[220:223], a[28:31]\n"
    "ds_read_b128 a[192:195], v248 offset:8192\n"
    "v_accvgpr_read_b32 v61, a22\n"
    "v_mfma_f32_16x16x32_bf16 a[28:31], a[140:143], v[24:27], a[28:31]\n"
    "v_fma_f32 v48, v48, v117, v119\n"
    "v_fma_f32 v49, v49, v117, v119\n"
    "v_mfma_f32_16x16x32_bf16 a[28:31], a[136:139], v[20:23], a[28:31]\n"
    "v_fma_f32 v60, v60, v117, v119\n"
    "v_fma_f32 v61, v61, v117, v119\n"
    "v_mfma_f32_16x16x32_bf16 a[40:43], a[208:211], v[92:95], a[40:43]\n"
    "v_accvgpr_read_b32 v62, a26\n"
    "v_accvgpr_read_b32 v66, a27\n"
    "v_mfma_f32_16x16x32_bf16 a[36:39], a[204:207], v[96:99], a[36:39]\n"
    "s_waitcnt lgkmcnt(2)\n"
    "buffer_store_dwordx4 v[56:59], v244, s[24:27], s76 offen offset:3200 sc1\n"
    "v_accvgpr_read_b32 v46, a24\n"
    "v_accvgpr_read_b32 v63, a30\n"
    "v_accvgpr_read_b32 v47, a28\n"
    "v_accvgpr_read_b32 v51, a29\n"
    "v_mfma_f32_16x16x32_bf16 a[40:43], a[212:215], v[96:99], a[40:43]\n"
    "v_fma_f32 v62, v62, v117, v119\n"
    "v_fma_f32 v63, v63, v117, v119\n"
    "v_mfma_f32_16x16x32_bf16 a[44:47], a[220:223], v[96:99], a[44:47]\n"
    "v_accvgpr_read_b32 v50, a25\n"
    "v_cvt_pk_bf16_f32 v44, v44, v45\n"
    "v_cvt_pk_bf16_f32 v60, v60, v61\n"
    "v_mfma_f32_16x16x32_bf16 a[36:39], a[200:203], v[92:95], a[36:39]\n"
    "v_fma_f32 v50, v50, v117, v119\n"
    "v_fma_f32 v51, v51, v117, v119\n"
    "v_mfma_f32_16x16x32_bf16 a[44:47], a[216:219], v[92:95], a[44:47]\n"
    "v_fma_f32 v46, v46, v117, v119\n"
    "v_fma_f32 v47, v47, v117, v119\n"
    "v_mfma_f32_16x16x32_bf16 a[36:39], a[236:239], v[232:235], a[36:39]\n"
    "v_cvt_pk_bf16_f32 v61, v62, v63\n"
    "v_cvt_pk_bf16_f32 v45, v46, v47\n"
    "v_mfma_f32_16x16x32_bf16 a[40:43], a[240:243], v[228:231], a[40:43]\n"
    "v_accvgpr_read_b32 v67, a31\n"
    "v_cvt_pk_bf16_f32 v47, v50, v51\n"
    "v_mfma_f32_16x16x32_bf16 a[36:39], a[232:235], v[228:231], a[36:39]\n"
    "ds_read_b128 a[208:211], v248 offset:10240\n"
    "v_cvt_pk_bf16_f32 v46, v48, v49\n"
    "v_mfma_f32_16x16x32_bf16 a[36:39], a[124:127], v[32:35], a[36:39]\n"
    "s_waitcnt lgkmcnt(2)\n"
    "buffer_store_dwordx4 v[40:43], v244, s[24:27], s76 offen offset:128 sc1\n"
    "ds_write_b128 v3, v[44:47] offset:0\n"
    "v_mfma_f32_16x16x32_bf16 a[36:39], a[120:123], v[28:31], a[36:39]\n"
    "v_fma_f32 v66, v66, v117, v119\n"
    "v_fma_f32 v67, v67, v117, v119\n"
    "v_mfma_f32_16x16x32_bf16 a[40:43], a[244:247], v[232:235], a[40:43]\n"
    "v_cvt_pk_bf16_f32 v62, v64, v65\n"
    "v_cvt_pk_bf16_f32 v63, v66, v67\n"
    "v_mfma_f32_16x16x32_bf16 a[44:47], a[252:255], v[232:235], a[44:47]\n"
    "ds_write_b128 v2, v[60:63] offset:0\n"
    "v_mfma_f32_16x16x32_bf16 a[32:35], a[196:199], v[96:99], a[32:35]\n"
    "v_accvgpr_read_b32 v125, a36\n"
    "v_fma_f32 v250, |v110|, 4.0, v250\n"
    "v_mfma_f32_16x16x32_bf16 a[44:47], a[248:251], v[228:231], a[44:47]\n"
    "v_fma_f32 v250, v108, 4.0, v250\n"
    "v_accvgpr_read_b32 v137, a39\n"
    "v_mfma_f32_16x16x32_bf16 a[32:35], a[224:227], v[228:231], a[32:35]\n"
    "ds_read_b128 a[236:239], v249 offset:46080\n"
    "ds_read_b128 v[48:51], v252 offset:0\n"
    "ds_read_b128 a[212:215], v248 offset:22528\n"
    "v_mfma_f32_16x16x32_bf16 a[32:35], a[228:231], v[232:235], a[32:35]\n"
    "ds_read_b128 a[224:227], v249 offset:32768\n"
    "v_accvgpr_read_b32 v133, a38\n"
    "v_mfma_f32_16x16x32_bf16 a[40:43], a[128:131], v[28:31], a[40:43]\n"
    "ds_read_b128 a[204:207], v248 offset:21504\n"
    "v_mfma_f32_16x16x32_bf16 a[44:47], a[140:143], v[32:35], a[44:47]\n"
    "ds_read_b128 v[64:67], v252 offset:1024\n"
    "ds_read_b128 a[240:243], v249 offset:34816\n"
    "v_mfma_f32_16x16x32_bf16 a[32:35], a[112:115], v[28:31], a[32:35]\n"
    "ds_read_b128 a[216:219], v248 offset:11264\n"
    "ds_read_b128 a[196:199], v248 offset:20480\n"
    "v_mfma_f32_16x16x32_bf16 a[32:35], a[116:119], v[32:35], a[32:35]\n"
    "s_waitcnt lgkmcnt(12)\n"
    "ds_read_b128 a[244:247], v249 offset:47104\n"
    "ds_read_b128 a[200:203], v248 offset:9216\n"
    "v_mfma_f32_16x16x32_bf16 a[48:51], a[192:195], v[68:71], a[48:51]\n"
    "ds_read_b128 a[232:235], v249 offset:33792\n"
    "ds_read_b128 a[252:255], v249 offset:48128\n"
    "v_mfma_f32_16x16x32_bf16 a[40:43], a[132:135], v[32:35], a[40:43]\n"
    "s_nop 0\n"
    "v_accvgpr_read_b32 v124, a32\n"
    "v_mfma_f32_16x16x32_bf16 a[44:47], a[136:139], v[28:31], a[44:47]\n"
    "s_waitcnt lgkmcnt(11)\n"
    "buffer_store_dwordx4 v[48:51], v244, s[24:27], s77 offen offset:128 sc1\n"
    "v_accvgpr_read_b32 v128, a33\n"
    "v_accvgpr_read_b32 v132, a34\n"
    "v_fma_f32 v132, v132, v121, v123\n"
    "v_fma_f32 v133, v133, v121, v123\n"
    "v_accvgpr_read_b32 v134, a42\n"
    "v_mfma_f32_16x16x32_bf16 a[56:59], a[208:211], v[68:71], a[56:59]\n"
    "s_waitcnt lgkmcnt(8)\n"
    "v_accvgpr_read_b32 v129, a37\n"
    "v_accvgpr_read_b32 v131, a45\n"
    "v_mfma_f32_16x16x32_bf16 a[52:55], a[204:207], v[72:75], a[52:55]\n"
    "v_accvgpr_read_b32 v130, a41\n"
    "v_accvgpr_read_b32 v138, a43\n"
    "v_mfma_f32_16x16x32_bf16 a[56:59], a[212:215], v[72:75], a[56:59]\n"
    "s_waitcnt lgkmcnt(4)\n"
    "v_accvgpr_read_b32 v135, a46\n"
    "v_fma_f32 v124, v124, v121, v123\n"
    "v_fma_f32 v125, v125, v121, v123\n"
    "v_mfma_f32_16x16x32_bf16 a[48:51], a[196:199], v[72:75], a[48:51]\n"
    "s_waitcnt lgkmcnt(2)\n"
    "ds_read_b128 a[228:231], v249 offset:45056\n"
    "ds_read_b128 a[248:251], v249 offset:35840\n"
    "v_mfma_f32_16x16x32_bf16 a[52:55], a[200:203], v[68:71], a[52:55]\n"
    ".Lwp2:\n"
    "s_cbranch_execz .Lw3\n"
    "v_fma_f32 v130, v130, v121, v123\n"
    "v_fma_f32 v131, v131, v121, v123\n"
    "v_mfma_f32_16x16x32_bf16 a[56:59], a[240:243], v[204:207], a[56:59]\n"
    "v_accvgpr_read_b32 v126, a40\n"
    "v_fma_f32 v134, v134, v121, v123\n"
    "v_fma_f32 v135, v135, v121, v123\n"
    "v_mfma_f32_16x16x32_bf16 a[48:51], a[224:227], v[204:207], a[48:51]\n"
    "ds_read_b128 a[220:223], v248 offset:23552\n"
    "v_accvgpr_read_b32 v139, a47\n"
    "v_mfma_f32_16x16x32_bf16 a[56:59], a[244:247], v[208:211], a[56:59]\n"
    "buffer_store_dwordx4 v[64:67], v244, s[24:27], s77 offen offset:3200 sc1\n"
    "v_accvgpr_read_b32 v136, a35\n"
    "v_accvgpr_read_b32 v127, a44\n"
    "v_fma_f32 v138, v138, v121, v123\n"
    "v_fma_f32 v139, v139, v121, v123\n"
    "v_mfma_f32_16x16x32_bf16 a[56:59], a[176:179], v[4:7], a[56:59]\n"
    "v_fma_f32 v126, v126, v121, v123\n"
    "v_fma_f32 v127, v127, v121, v123\n"
    "v_mfma_f32_16x16x32_bf16 a[56:59], a[180:183], v[8:11], a[56:59]\n"
    "v_cvt_pk_bf16_f32 v124, v124, v125\n"
    "v_cvt_pk_bf16_f32 v125, v126, v127\n"
    "v_mfma_f32_16x16x32_bf16 a[52:55], a[236:239], v[208:211], a[52:55]\n"
    "s_waitcnt lgkmcnt(2)\n"
    "v_fma_f32 v128, v128, v121, v123\n"
    "v_fma_f32 v129, v129, v121, v123\n"
    "v_mfma_f32_16x16x32_bf16 a[48:51], a[228:231], v[208:211], a[48:51]\n"
    "v_cvt_pk_bf16_f32 v132, v132, v133\n"
    "v_cvt_pk_bf16_f32 v126, v128, v129\n"
    "v_mfma_f32_16x16x32_bf16 a[52:55], a[232:235], v[204:207], a[52:55]\n"
    "v_cvt_pk_bf16_f32 v133, v134, v135\n"
    "v_cvt_pk_bf16_f32 v127, v130, v131\n"
    "v_mfma_f32_16x16x32_bf16 a[52:55], a[172:175], v[8:11], a[52:55]\n"
    "s_waitcnt lgkmcnt(0)\n"
    "ds_write_b128 v3, v[124:127] offset:2048\n"
    "v_mfma_f32_16x16x32_bf16 a[60:63], a[220:223], v[72:75], a[60:63]\n"
    "v_fma_f32 v136, v136, v121, v123\n"
    "v_fma_f32 v137, v137, v121, v123\n"
    "v_mfma_f32_16x16x32_bf16 a[52:55], a[168:171], v[4:7], a[52:55]\n"
    "v_cvt_pk_bf16_f32 v135, v138, v139\n"
    "v_cvt_pk_bf16_f32 v134, v136, v137\n"
    "v_mfma_f32_16x16x32_bf16 a[60:63], a[216:219], v[68:71], a[60:63]\n"
    "ds_write_b128 v2, v[132:135] offset:2048\n"
    "v_mfma_f32_16x16x32_bf16 a[48:51], a[160:163], v[4:7], a[48:51]\n"
    "v_accvgpr_read_b32 v146, a57\n"
    "v_mfma_f32_16x16x32_bf16 a[48:51], a[164:167], v[8:11], a[48:51]\n"
    "ds_read_b128 v[128:131], v252 offset:2048\n"
    "v_mfma_f32_16x16x32_bf16 a[60:63], a[252:255], v[208:211], a[60:63]\n"
    "v_accvgpr_read_b32 v141, a52\n"
    "v_accvgpr_read_b32 v153, a55\n"
    "v_mfma_f32_16x16x32_bf16 a[64:67], a[192:195], v[76:79], a[64:67]\n"
    "v_accvgpr_read_b32 v149, a54\n"
    "v_accvgpr_read_b32 v150, a58\n"
    "v_mfma_f32_16x16x32_bf16 a[60:63], a[248:251], v[204:207], a[60:63]\n"
    "ds_read_b128 v[136:139], v252 offset:3072\n"
    "v_accvgpr_read_b32 v142, a56\n"
    "v_mfma_f32_16x16x32_bf16 a[60:63], a[188:191], v[8:11], a[60:63]\n"
    "v_accvgpr_read_b32 v145, a53\n"
    "v_accvgpr_read_b32 v148, a50\n"
    "v_mfma_f32_16x16x32_bf16 a[60:63], a[184:187], v[4:7], a[60:63]\n"
    "v_accvgpr_read_b32 v154, a59\n"
    "v_accvgpr_read_b32 v140, a48\n"
    "v_mfma_f32_16x16x32_bf16 a[64:67], a[196:199], v[80:83], a[64:67]\n"
    "v_fma_f32 v140, v140, v109, v111\n"
    "v_fma_f32 v141, v141, v109, v111\n"
    "v_mfma_f32_16x16x32_bf16 a[68:71], a[204:207], v[80:83], a[68:71]\n"
    "s_nop 1\n"
    "v_accvgpr_read_b32 v143, a60\n"
    "v_mfma_f32_16x16x32_bf16 a[68:71], a[200:203], v[76:79], a[68:71]\n"
    "s_waitcnt lgkmcnt(1)\n"
    "buffer_store_dwordx4 v[128:131], v244, s[24:27], s78 offen offset:128 sc1\n"
    "v_fma_f32 v148, v148, v109, v111\n"
    "v_fma_f32 v149, v149, v109, v111\n"
    "v_accvgpr_read_b32 v144, a49\n"
    "v_cvt_pk_bf16_f32 v148, v148, v149\n"
    "v_mfma_f32_16x16x32_bf16 a[76:79], a[220:223], v[80:83], a[76:79]\n"
    "v_cvt_pk_bf16_f32 v140, v140, v141\n"
    "v_accvgpr_read_b32 v152, a51\n"
    "v_mfma_f32_16x16x32_bf16 a[76:79], a[216:219], v[76:79], a[76:79]\n"
    "v_fma_f32 v142, v142, v109, v111\n"
    "v_fma_f32 v143, v143, v109, v111\n"
    "v_mfma_f32_16x16x32_bf16 a[64:67], a[224:227], v[212:215], a[64:67]\n"
    "v_fma_f32 v144, v144, v109, v111\n"
    "v_fma_f32 v145, v145, v109, v111\n"
    "v_accvgpr_read_b32 v151, a62\n"
    "v_mfma_f32_16x16x32_bf16 a[68:71], a[236:239], v[216:219], a[68:71]\n"
    "v_accvgpr_read_b32 v155, a63\n"
    "s_nop 0\n"
    "v_mfma_f32_16x16x32_bf16 a[68:71], a[232:235], v[212:215], a[68:71]\n"
    "s_waitcnt lgkmcnt(0)\n"
    "buffer_store_dwordx4 v[136:139], v244, s[24:27], s78 offen offset:3200 sc1\n"
    "v_fma_f32 v150, v150, v109, v111\n"
    "v_fma_f32 v151, v151, v109, v111\n"
    "v_cvt_pk_bf16_f32 v141, v142, v143\n"
    "v_accvgpr_read_b32 v147, a61\n"
    "v_mfma_f32_16x16x32_bf16 a[72:75], a[208:211], v[76:79], a[72:75]\n"
    "v_fma_f32 v146, v146, v109, v111\n"
    "v_fma_f32 v147, v147, v109, v111\n"
    "v_mfma_f32_16x16x32_bf16 a[64:67], a[228:231], v[216:219], a[64:67]\n"
    "v_mfma_f32_16x16x32_bf16 a[68:71], a[172:175], v[16:19], a[68:71]\n"
    "v_fma_f32 v154, v154, v109, v111\n"
    "v_fma_f32 v155, v155, v109, v111\n"
    "v_mfma_f32_16x16x32_bf16 a[64:67], a[160:163], v[12:15], a[64:67]\n"
    "v_cvt_pk_bf16_f32 v149, v150, v151\n"
    "v_cvt_pk_bf16_f32 v142, v144, v145\n"
    "v_cvt_pk_bf16_f32 v143, v146, v147\n"
    "v_mfma_f32_16x16x32_bf16 a[68:71], a[168:171], v[12:15], a[68:71]\n"
    "ds_write_b128 v3, v[140:143] offset:4096\n"
    "v_mfma_f32_16x16x32_bf16 a[72:75], a[212:215], v[80:83], a[72:75]\n"
    "v_fma_f32 v152, v152, v109, v111\n"
    "v_fma_f32 v153, v153, v109, v111\n"
    "v_mfma_f32_16x16x32_bf16 a[72:75], a[240:243], v[212:215], a[72:75]\n"
    "v_cvt_pk_bf16_f32 v150, v152, v153\n"
    "v_cvt_pk_bf16_f32 v151, v154, v155\n"
    "v_mfma_f32_16x16x32_bf16 a[92:95], a[220:223], v[88:91], a[92:95]\n"
    "ds_write_b128 v2, v[148:151] offset:4096\n"
    "v_mfma_f32_16x16x32_bf16 a[92:95], a[216:219], v[84:87], a[92:95]\n"
    "v_accvgpr_read_b32 v161, a69\n"
    "v_fma_f32 v250, v109, s74, v250\n"
    "v_mfma_f32_16x16x32_bf16 a[64:67], a[164:167], v[16:19], a[64:67]\n"
    "v_fma_f32 v250, |v114|, 4.0, v250\n"
    "v_mfma_f32_16x16x32_bf16 a[72:75], a[244:247], v[216:219], a[72:75]\n"
    "ds_read_b128 v[144:147], v252 offset:4096\n"
    "v_mfma_f32_16x16x32_bf16 a[72:75], a[176:179], v[12:15], a[72:75]\n"
    "ds_read_b128 v[152:155], v252 offset:5120\n"
    "v_accvgpr_read_b32 v165, a70\n"
    "v_mfma_f32_16x16x32_bf16 a[72:75], a[180:183], v[16:19], a[72:75]\n"
    "s_nop 0\n"
    "v_accvgpr_read_b32 v156, a64\n"
    "v_accvgpr_read_b32 v160, a65\n"
    "v_mfma_f32_16x16x32_bf16 a[76:79], a[252:255], v[216:219], a[76:79]\n"
    "v_fma_f32 v160, v160, v113, v115\n"
    "v_fma_f32 v161, v161, v113, v115\n"
    "v_mfma_f32_16x16x32_bf16 a[84:87], a[204:207], v[88:91], a[84:87]\n"
    "s_nop 0\n"
    "v_accvgpr_read_b32 v162, a73\n"
    "v_accvgpr_read_b32 v169, a71\n"
    "v_mfma_f32_16x16x32_bf16 a[80:83], a[192:195], v[84:87], a[80:83]\n"
    "v_fma_f32 v250, v112, 4.0, v250\n"
    "s_nop 0\n"
    "v_mfma_f32_16x16x32_bf16 a[76:79], a[248:251], v[212:215], a[76:79]\n"
    "v_accvgpr_read_b32 v166, a74\n"
    "v_accvgpr_read_b32 v157, a68\n"
    "v_mfma_f32_16x16x32_bf16 a[76:79], a[188:191], v[16:19], a[76:79]\n"
    "v_accvgpr_read_b32 v164, a66\n"
    "v_accvgpr_read_b32 v168, a67\n"
    "v_mfma_f32_16x16x32_bf16 a[76:79], a[184:187], v[12:15], a[76:79]\n"
    "s_waitcnt lgkmcnt(1)\n"
    "buffer_store_dwordx4 v[144:147], v244, s[24:27], s75 offen offset:256 sc1\n"
    "v_fma_f32 v164, v164, v113, v115\n"
    "v_fma_f32 v165, v165, v113, v115\n"
    "v_accvgpr_read_b32 v158, a72\n"
    "v_fma_f32 v156, v156, v113, v115\n"
    "v_fma_f32 v157, v157, v113, v115\n"
    "v_mfma_f32_16x16x32_bf16 a[80:83], a[196:199], v[88:91], a[80:83]\n"
    "v_accvgpr_read_b32 v167, a78\n"
    "v_cvt_pk_bf16_f32 v156, v156, v157\n"
    "v_fma_f32 v250, v113, s74, v250\n"
    "v_mfma_f32_16x16x32_bf16 a[88:91], a[208:211], v[84:87], a[88:91]\n"
    "s_nop 0\n"
    "v_accvgpr_read_b32 v159, a76\n"
    "v_fma_f32 v166, v166, v113, v115\n"
    "v_fma_f32 v167, v167, v113, v115\n"
    "v_mfma_f32_16x16x32_bf16 a[84:87], a[200:203], v[84:87], a[84:87]\n"
    "s_waitcnt lgkmcnt(0)\n"
    "buffer_store_dwordx4 v[152:155], v244, s[24:27], s75 offen offset:3328 sc1\n"
    "v_fma_f32 v158, v158, v113, v115\n"
    "v_fma_f32 v159, v159, v113, v115\n"
    "v_accvgpr_read_b32 v170, a75\n"
    "v_accvgpr_read_b32 v171, a79\n"
    "v_mfma_f32_16x16x32_bf16 a[84:87], a[236:239], v[224:227], a[84:87]\n"
    "v_cvt_pk_bf16_f32 v157, v158, v159\n"
    "v_cvt_pk_bf16_f32 v158, v160, v161\n"
    "v_mfma_f32_16x16x32_bf16 a[84:87], a[232:235], v[220:223], a[84:87]\n"
    "v_cvt_pk_bf16_f32 v164, v164, v165\n"
    "v_accvgpr_read_b32 v163, a77\n"
    "v_mfma_f32_16x16x32_bf16 a[80:83], a[224:227], v[220:223], a[80:83]\n"
    "v_fma_f32 v162, v162, v113, v115\n"
    "v_fma_f32 v163, v163, v113, v115\n"
    "v_mfma_f32_16x16x32_bf16 a[88:91], a[212:215], v[88:91], a[88:91]\n"
    "v_cvt_pk_bf16_f32 v159, v162, v163\n"
    "v_cvt_pk_bf16_f32 v165, v166, v167\n"
    "v_mfma_f32_16x16x32_bf16 a[84:87], a[172:175], v[24:27], a[84:87]\n"
    "ds_write_b128 v3, v[156:159] offset:0\n"
    "v_mfma_f32_16x16x32_bf16 a[84:87], a[168:171], v[20:23], a[84:87]\n"
    "v_fma_f32 v168, v168, v113, v115\n"
    "v_fma_f32 v169, v169, v113, v115\n"
    "v_mfma_f32_16x16x32_bf16 a[92:95], a[252:255], v[224:227], a[92:95]\n"
    "v_fma_f32 v170, v170, v113, v115\n"
    "v_fma_f32 v171, v171, v113, v115\n"
    "v_fma_f32 v250, |v118|, 4.0, v250\n"
    "v_cvt_pk_bf16_f32 v167, v170, v171\n"
    "v_cvt_pk_bf16_f32 v166, v168, v169\n"
    "v_mfma_f32_16x16x32_bf16 a[80:83], a[228:231], v[224:227], a[80:83]\n"
    "ds_write_b128 v2, v[164:167] offset:0\n"
    "v_mfma_f32_16x16x32_bf16 a[80:83], a[160:163], v[20:23], a[80:83]\n"
    "v_mfma_f32_16x16x32_bf16 a[92:95], a[248:251], v[220:223], a[92:95]\n"
    "ds_read_b128 v[160:163], v252 offset:0\n"
    "v_mfma_f32_16x16x32_bf16 a[80:83], a[164:167], v[24:27], a[80:83]\n"
    "ds_read_b128 v[168:171], v252 offset:1024\n"
    "v_accvgpr_read_b32 v173, a84\n"
    "v_mfma_f32_16x16x32_bf16 a[92:95], a[188:191], v[24:27], a[92:95]\n"
    "v_accvgpr_read_b32 v177, a85\n"
    "v_mfma_f32_16x16x32_bf16 a[88:91], a[240:243], v[220:223], a[88:91]\n"
    "v_mfma_f32_16x16x32_bf16 a[88:91], a[244:247], v[224:227], a[88:91]\n"
    "s_nop 1\n"
    "v_accvgpr_read_b32 v176, a81\n"
    "v_mfma_f32_16x16x32_bf16 a[92:95], a[184:187], v[20:23], a[92:95]\n"
    "v_accvgpr_read_b32 v172, a80\n"
    "v_accvgpr_read_b32 v184, a83\n"
    "v_mfma_f32_16x16x32_bf16 a[88:91], a[176:179], v[20:23], a[88:91]\n"
    "v_fma_f32 v172, v172, v117, v119\n"
    "v_fma_f32 v173, v173, v117, v119\n"
    "v_mfma_f32_16x16x32_bf16 a[88:91], a[180:183], v[24:27], a[88:91]\n"
    "v_fma_f32 v176, v176, v117, v119\n"
    "v_fma_f32 v177, v177, v117, v119\n"
    "v_mfma_f32_16x16x32_bf16 a[96:99], a[192:195], v[92:95], a[96:99]\n"
    "s_waitcnt lgkmcnt(1)\n"
    "buffer_store_dwordx4 v[160:163], v244, s[24:27], s76 offen offset:256 sc1\n"
    "v_accvgpr_read_b32 v175, a92\n"
    "v_accvgpr_read_b32 v180, a82\n"
    "v_accvgpr_read_b32 v181, a86\n"
    "v_cvt_pk_bf16_f32 v172, v172, v173\n"
    "v_mfma_f32_16x16x32_bf16 a[108:111], a[220:223], v[96:99], a[108:111]\n"
    "v_accvgpr_read_b32 v179, a93\n"
    "v_accvgpr_read_b32 v174, a88\n"
    "v_mfma_f32_16x16x32_bf16 a[108:111], a[216:219], v[92:95], a[108:111]\n"
    "v_fma_f32 v250, v116, 4.0, v250\n"
    "v_accvgpr_read_b32 v183, a94\n"
    "v_mfma_f32_16x16x32_bf16 a[96:99], a[196:199], v[96:99], a[96:99]\n"
    "v_accvgpr_read_b32 v187, a95\n"
    "v_accvgpr_read_b32 v178, a89\n"
    "v_mfma_f32_16x16x32_bf16 a[100:103], a[204:207], v[96:99], a[100:103]\n"
    "v_mfma_f32_16x16x32_bf16 a[104:107], a[208:211], v[92:95], a[104:107]\n"
    "s_nop 0\n"
    "v_fma_f32 v174, v174, v117, v119\n"
    "v_fma_f32 v175, v175, v117, v119\n"
    "v_mfma_f32_16x16x32_bf16 a[108:111], a[252:255], v[232:235], a[108:111]\n"
    "s_waitcnt lgkmcnt(0)\n"
    "buffer_store_dwordx4 v[168:171], v244, s[24:27], s76 offen offset:3328 sc1\n"
    "v_cvt_pk_bf16_f32 v173, v174, v175\n"
    "v_accvgpr_read_b32 v182, a90\n"
    "v_accvgpr_read_b32 v185, a87\n"
    "v_cvt_pk_bf16_f32 v174, v176, v177\n"
    "v_pk_fma_f32 v[178:179], v[178:179], v[116:117], v[118:119] op_sel:[0,1,1] op_sel_hi:[1,1,1]\n"
    "v_accvgpr_read_b32 v186, a91\n"
    "v_cvt_pk_bf16_f32 v175, v178, v179\n"
    "v_fma_f32 v186, v186, v117, v119\n"
    "v_fma_f32 v187, v187, v117, v119\n"
    "v_pk_fma_f32 v[180:181], v[180:181], v[116:117], v[118:119] op_sel:[0,1,1] op_sel_hi:[1,1,1]\n"
    "v_cmp_eq_u64 s[56:57], v[236:237], v[238:239]\n"
    "v_fma_f32 v182, v182, v117, v119\n"
    "v_fma_f32 v183, v183, v117, v119\n"
    "v_mfma_f32_16x16x32_bf16 a[108:111], a[248:251], v[228:231], a[108:111]\n"
    "ds_write_b128 v3, v[172:175] offset:2048\n"
    "v_cvt_pk_bf16_f32 v180, v180, v181\n"
    "v_mfma_f32_16x16x32_bf16 a[96:99], a[224:227], v[228:231], a[96:99]\n"
    "v_fma_f32 v184, v184, v117, v119\n"
    "v_fma_f32 v185, v185, v117, v119\n"
    "v_cvt_pk_bf16_f32 v181, v182, v183\n"
    "v_mfma_f32_16x16x32_bf16 a[104:107], a[212:215], v[96:99], a[104:107]\n"
    "v_cvt_pk_bf16_f32 v182, v184, v185\n"
    "v_cvt_pk_bf16_f32 v183, v186, v187\n"
    "v_mfma_f32_16x16x32_bf16 a[96:99], a[228:231], v[232:235], a[96:99]\n"
    "ds_write_b128 v2, v[180:183] offset:2048\n"
    "v_mfma_f32_16x16x32_bf16 a[96:99], a[160:163], v[28:31], a[96:99]\n"
    "v_mfma_f32_16x16x32_bf16 a[104:107], a[240:243], v[228:231], a[104:107]\n"
    "ds_read_b128 v[176:179], v252 offset:2048\n"
    "v_mfma_f32_16x16x32_bf16 a[104:107], a[244:247], v[232:235], a[104:107]\n"
    "v_mfma_f32_16x16x32_bf16 a[100:103], a[200:203], v[92:95], a[100:103]\n"
    "v_mfma_f32_16x16x32_bf16 a[104:107], a[176:179], v[28:31], a[104:107]\n"
    "v_mfma_f32_16x16x32_bf16 a[104:107], a[180:183], v[32:35], a[104:107]\n"
    "v_mfma_f32_16x16x32_bf16 a[108:111], a[188:191], v[32:35], a[108:111]\n"
    "s_or_b64 s[56:57], s[56:57], s[34:35]\n"
    "ds_read_b128 v[184:187], v252 offset:3072\n"
    "v_mfma_f32_16x16x32_bf16 a[100:103], a[236:239], v[232:235], a[100:103]\n"
    "v_mfma_f32_16x16x32_bf16 a[96:99], a[164:167], v[32:35], a[96:99]\n"
    "v_mfma_f32_16x16x32_bf16 a[100:103], a[232:235], v[228:231], a[100:103]\n"
    "s_waitcnt lgkmcnt(1)\n"
    "buffer_store_dwordx4 v[176:179], v244, s[24:27], s77 offen offset:256 sc1\n"
    "v_accvgpr_read_b32 v198, a106\n"
    "v_fma_f32 v250, v117, s74, v250\n"
    "v_fma_f32 v250, |v122|, 4.0, v250\n"
    "v_accvgpr_read_b32 v194, a105\n"
    "s_nop 0\n"
    "v_accvgpr_read_b32 v196, a98\n"
    "v_mfma_f32_16x16x32_bf16 a[108:111], a[184:187], v[28:31], a[108:111]\n"
    "v_accvgpr_read_b32 v202, a107\n"
    "v_mfma_f32_16x16x32_bf16 a[100:103], a[172:175], v[32:35], a[100:103]\n"
    "s_nop 0\n"
    "v_mfma_f32_16x16x32_bf16 a[100:103], a[168:171], v[28:31], a[100:103]\n"
    "s_waitcnt lgkmcnt(0)\n"
    "buffer_store_dwordx4 v[184:187], v244, s[24:27], s77 offen offset:3328 sc1\n"
    "v_accvgpr_read_b32 v190, a104\n"
    "s_nop 1\n"
    "v_accvgpr_read_b32 v191, a108\n"
    "v_accvgpr_read_b32 v199, a110\n"
    "v_fma_f32 v250, v120, 4.0, v250\n"
    "v_accvgpr_read_b32 v197, a102\n"
    "v_accvgpr_read_b32 v200, a99\n"
    "v_accvgpr_read_b32 v195, a109\n"
    "v_accvgpr_read_b32 v188, a96\n"
    "v_pk_fma_f32 v[190:191], v[190:191], v[120:121], v[122:123] op_sel:[0,1,1] op_sel_hi:[1,1,1]\n"
    "v_accvgpr_read_b32 v201, a103\n"
    "v_accvgpr_read_b32 v203, a111\n"
    "v_accvgpr_read_b32 v189, a100\n"
    "v_pk_fma_f32 v[194:195], v[194:195], v[120:121], v[122:123] op_sel:[0,1,1] op_sel_hi:[1,1,1]\n"
    "v_fma_f32 v198, v198, v121, v123\n"
    "v_fma_f32 v199, v199, v121, v123\n"
    "v_fma_f32 v188, v188, v121, v123\n"
    "v_fma_f32 v189, v189, v121, v123\n"
    "v_accvgpr_read_b32 v192, a97\n"
    "v_cvt_pk_bf16_f32 v188, v188, v189\n"
    "v_accvgpr_read_b32 v193, a101\n"
    "v_cvt_pk_bf16_f32 v189, v190, v191\n"
    "v_cvt_pk_bf16_f32 v191, v194, v195\n"
    "v_fma_f32 v192, v192, v121, v123\n"
    "v_fma_f32 v193, v193, v121, v123\n"
    "v_cvt_pk_bf16_f32 v190, v192, v193\n"
    "ds_write_b128 v3, v[188:191] offset:4096\n"
    "v_pk_fma_f32 v[196:197], v[196:197], v[120:121], v[122:123] op_sel:[0,1,1] op_sel_hi:[1,1,1]\n"
    "v_cvt_pk_bf16_f32 v196, v196, v197\n"
    "v_cvt_pk_bf16_f32 v197, v198, v199\n"
    "v_pk_fma_f32 v[200:201], v[200:201], v[120:121], v[122:123] op_sel:[0,1,1] op_sel_hi:[1,1,1]\n"
    "v_fma_f32 v250, v121, s74, v250\n"
    "v_pk_fma_f32 v[202:203], v[202:203], v[120:121], v[122:123] op_sel:[0,1,1] op_sel_hi:[1,1,1]\n"
    "v_cvt_pk_bf16_f32 v199, v202, v203\n"
    "v_cvt_pk_bf16_f32 v198, v200, v201\n"
    "ds_write_b128 v2, v[196:199] offset:4096\n"
    "v_cmp_nlt_f32 s[54:55], |v250|, s73\n"
    "ds_read_b128 v[200:203], v252 offset:5120\n"
    "ds_read_b128 v[192:195], v252 offset:4096\n"
    "s_waitcnt lgkmcnt(1)\n"
    "buffer_store_dwordx4 v[200:203], v244, s[24:27], s78 offen offset:3328 sc1\n"
    "s_or_b64 s[54:55], s[54:55], s[56:57]\n"
    "s_waitcnt lgkmcnt(0)\n"
    "buffer_store_dwordx4 v[192:195], v244, s[24:27], s78 offen offset:256 sc1\n"
    "s_or_b32 %0, s54, s55\n"
    "s_cmp_eq_u32 %0, 0\n"
    "s_cbranch_scc1 .Lfin\n"
    "s_waitcnt vmcnt(0)\n"
    ".Lfin:\n"
    ".Lend:\n"
    ".Lend2:\n"
    : "=&s"(bad), "+s"(ka) : "s"(b), "v"(tid)
    : "memory", "vcc", "s16", "s17", "s18", "s19", "s20", "s21", "s22", "s23", "s24", "s25", "s26", "s27", "s28", "s29", "s30", "s31", "s34", "s35", "s36", "s37", "s38", "s39", "s40", "s41", "s42", "s43", "s44", "s45", "s46", "s47", "s48", "s49", "s50", "s51", "s52", "s53", "s54", "s55", "s56", "s57", "s58", "s59", "s60", "s61", "s62", "s63", "s64", "s65", "s66", "s67", "s68", "s69", "s70", "s71", "s72", "s73", "s74", "s75", "s76", "s77", "s78", "s79", "s80", "s81", "s82", "s83", "s84", "s85", "s86", "s87", "s88", "s89", "s90", "s91", "s92", "s93", "s94", "s95", "s96", "s97", "s98", "s99", "v1", "v2", "v3", "v4", "v5", "v6", "v7", "v8", "v9", "v10", "v11", "v12", "v13", "v14", "v15", "v16", "v17", "v18", "v19", "v20", "v21", "v22", "v23", "v24", "v25", "v26", "v27", "v28", "v29", "v30", "v31", "v32", "v33", "v34", "v35", "v36", "v37", "v38", "v39", "v40", "v41", "v42", "v43", "v44", "v45", "v46", "v47", "v48", "v49", "v50", "v51", "v52", "v53", "v54", "v55", "v56", "v57", "v58", "v59", "v60", "v61", "v62", "v63", "v64", "v65", "v66", "v67", "v68", "v69", "v70", "v71", "v72", "v73", "v74", "v75", "v76", "v77", "v78", "v79", "v80", "v81", "v82", "v83", "v84", "v85", "v86", "v87", "v88", "v89", "v90", "v91", "v92", "v93", "v94", "v95", "v96", "v97", "v98", "v99", "v100", "v101", "v102", "v103", "v104", "v105", "v106", "v107", "v108", "v109", "v110", "v111", "v112", "v113", "v114", "v115", "v116", "v117", "v118", "v119", "v120", "v121", "v122", "v123", "v124", "v125", "v126", "v127", "v128", "v129", "v130", "v131", "v132", "v133", "v134", "v135", "v136", "v137", "v138", "v139", "v140", "v141", "v142", "v143", "v144", "v145", "v146", "v147", "v148", "v149", "v150", "v151", "v152", "v153", "v154", "v155", "v156", "v157", "v158", "v159", "v160", "v161", "v162", "v163", "v164", "v165", "v166", "v167", "v168", "v169", "v170", "v171", "v172", "v173", "v174", "v175", "v176", "v177", "v178", "v179", "v180", "v181", "v182", "v183", "v184", "v185", "v186", "v187", "v188", "v189", "v190", "v191", "v192", "v193", "v194", "v195", "v196", "v197", "v198", "v199", "v200", "v201", "v202", "v203", "v204", "v205", "v206", "v207", "v208", "v209", "v210", "v211", "v212", "v213", "v214", "v215", "v216", "v217", "v218", "v219", "v220", "v221", "v222", "v223", "v224", "v225", "v226", "v227", "v228", "v229", "v230", "v231", "v232", "v233", "v234", "v235", "v236", "v237", "v238", "v239", "v240", "v241", "v242", "v243", "v244", "v245", "v246", "v247", "v248", "v249", "v250", "v251", "v252", "v253", "v254", "v255", "a0", "a1", "a2", "a3", "a4", "a5", "a6", "a7", "a8", "a9", "a10", "a11", "a12", "a13", "a14", "a15", "a16", "a17", "a18", "a19", "a20", "a21", "a22", "a23", "a24", "a25", "a26", "a27", "a28", "a29", "a30", "a31", "a32", "a33", "a34", "a35", "a36", "a37", "a38", "a39", "a40", "a41", "a42", "a43", "a44", "a45", "a46", "a47", "a48", "a49", "a50", "a51", "a52", "a53", "a54", "a55", "a56", "a57", "a58", "a59", "a60", "a61", "a62", "a63", "a64", "a65", "a66", "a67", "a68", "a69", "a70", "a71", "a72", "a73", "a74", "a75", "a76", "a77", "a78", "a79", "a80", "a81", "a82", "a83", "a84", "a85", "a86", "a87", "a88", "a89", "a90", "a91", "a92", "a93", "a94", "a95", "a96", "a97", "a98", "a99", "a100", "a101", "a102", "a103", "a104", "a105", "a106", "a107", "a108", "a109", "a110", "a111", "a112", "a113", "a114", "a115", "a116", "a117", "a118", "a119", "a120", "a121", "a122", "a123", "a124", "a125", "a126", "a127", "a128", "a129", "a130", "a131", "a132", "a133", "a134", "a135", "a136", "a137", "a138", "a139", "a140", "a141", "a142", "a143", "a144", "a145", "a146", "a147", "a148", "a149", "a150", "a151", "a152", "a153", "a154", "a155", "a156", "a157", "a158", "a159", "a160", "a161", "a162", "a163", "a164", "a165", "a166", "a167", "a168", "a169", "a170", "a171", "a172", "a173", "a174", "a175", "a176", "a177", "a178", "a179", "a180", "a181", "a182", "a183", "a184", "a185", "a186", "a187", "a188", "a189", "a190", "a191", "a192", "a193", "a194", "a195", "a196", "a197", "a198", "a199", "a200", "a201", "a202", "a203", "a204", "a205", "a206", "a207", "a208", "a209", "a210", "a211", "a212", "a213", "a214", "a215", "a216", "a217", "a218", "a219", "a220", "a221", "a222", "a223", "a224", "a225", "a226", "a227", "a228", "a229", "a230", "a231", "a232", "a233", "a234", "a235", "a236", "a237", "a238", "a239", "a240", "a241", "a242", "a243", "a244", "a245", "a246", "a247", "a248", "a249", "a250", "a251", "a252", "a253", "a254", "a255");
  if (bad) {
    slow_rows((const Args*)ka, (b & 7) * 4 + ((b >> 3) & 3), (b >> 5) * 256 + (tid & 192), tid & 63);
  }
}

extern "C" void kernel_launch(void* const* d_in, const int* in_sizes, int n_in,
                              void* d_out, int out_size, void* d_ws, size_t ws_size,
                              hipStream_t stream) {
  (void)hipFuncSetAttribute((const void*)attn_fwd, hipFuncAttributeMaxDynamicSharedMemorySize, SHM);
  attn_fwd<<<256, 256, SHM, stream>>>((const unsigned short*)d_in[0], (const int*)d_in[1], (const int*)d_in[2], (const unsigned short*)d_in[3], (const unsigned short*)d_in[4],
                                      (const unsigned short*)d_in[5], (const unsigned short*)d_in[6], (char*)d_ws, (unsigned short*)d_out);
}
```

```cpp
#include <hip/hip_runtime.h>

constexpr int D = 192, NH = 32, S = 2048;
constexpr int SHM = 155968;

struct Args { const unsigned short* q; const int* qk; const int* qv; const unsigned short* ksc; const unsigned short* kzr;
              const unsigned short* vsc; const unsigned short* vzr; char* ws; unsigned short* o; };

__device__ __forceinline__ float bf2f(unsigned short x) { return __uint_as_float((unsigned)x << 16); }
__device__ __forceinline__ float rbf(float x) { return (float)(__bf16)x; }

__device__ __forceinline__ void slow_rows(const Args* a, unsigned h, unsigned row0, unsigned lane) {
  const unsigned short* q = a->q + ((size_t)h * S + row0) * D;
  unsigned short* o = a->o + ((size_t)h * S + row0) * D;
  const int* qk = a->qk + (size_t)h * S * D; const int* qv = a->qv + (size_t)h * S * D;
  const unsigned short* ks = a->ksc + h * 32 * D; const unsigned short* kz = a->kzr + h * 32 * D;
  const unsigned short* vs = a->vsc + h * S; const unsigned short* vz = a->vzr + h * S;
#pragma unroll 1
  for (int r = 0; r < 64; ++r) {
    float m = -INFINITY, l = 0.f, a0 = 0.f, a1 = 0.f, a2 = 0.f;
#pragma unroll 1
    for (int t = 0; t < 32; ++t) {
      const int* kr = qk + (size_t)(t * 64 + lane) * D;
      float s = 0.f;
#pragma unroll 4
      for (int d = 0; d < D; ++d) s = fmaf(bf2f(q[r * D + d]), rbf(rbf(rbf((float)kr[d]) - bf2f(kz[t * D + d])) * bf2f(ks[t * D + d])), s);
      s *= 0.07216878364870322f;
      float mx = s;
      for (int x = 32; x; x >>= 1) mx = fmaxf(mx, __shfl_xor(mx, x));
      const float mn = fmaxf(m, mx), c = __expf(m - mn), p = __expf(s - mn);
      float ps = p;
      for (int x = 32; x; x >>= 1) ps += __shfl_xor(ps, x);
      l = l * c + ps; a0 *= c; a1 *= c; a2 *= c; m = mn;
#pragma unroll 2
      for (int k = 0; k < 64; ++k) {
        const float pk = __shfl(p, k), z = bf2f(vz[t * 64 + k]), sc = bf2f(vs[t * 64 + k]);
        const int* vr = qv + (size_t)(t * 64 + k) * D + lane;
        a0 = fmaf(pk, rbf(rbf(rbf((float)vr[0]) - z) * sc), a0); a1 = fmaf(pk, rbf(rbf(rbf((float)vr[64]) - z) * sc), a1); a2 = fmaf(pk, rbf(rbf(rbf((float)vr[128]) - z) * sc), a2);
      }
    }
    const float i = 1.f / l;
    o[r * D + lane] = __builtin_bit_cast(unsigned short, (__bf16)(a0 * i)); o[r * D + 64 + lane] = __builtin_bit_cast(unsigned short, (__bf16)(a1 * i));
    o[r * D + 128 + lane] = __builtin_bit_cast(unsigned short, (__bf16)(a2 * i));
  }
}

__global__ __launch_bounds__(256, 1) __attribute__((aligned(4096))) void attn_fwd(const unsigned short* q, const int* qk, const int* qv, const unsigned short* ksc, const unsigned short* kzr,
                                                   const unsigned short* vsc, const unsigned short* vzr, char* ws, unsigned short* o) {
  unsigned long long ka = (unsigned long long)__builtin_amdgcn_kernarg_segment_ptr();
  unsigned bad, b = blockIdx.x;
  const unsigned tid = threadIdx.x;
  asm volatile(
    "s_load_dwordx16 s[4:19], %1, 0\n"
    "s_load_dwordx2 s[24:25], %1, 64\n"
    "s_getpc_b64 s[54:55]\n"
    ".Lpc:\n"
    "s_and_b32 s72, %2, 24\n"
    "s_lshl_b32 s72, s72, 4\n"
    "v_lshl_add_u32 v246, %3, 9, s72\n"
    "v_min_u32 v246, .Lend-.Lpc-4, v246\n"
    "global_load_dword v246, v246, s[54:55]\n"
    "s_getreg_b32 s72, hwreg(HW_REG_XCC_ID, 0, 1)\n"
    "s_cmp_eq_u32 s72, 1\n"
    "s_cbranch_scc1 .Ldly\n"
    "s_sleep 38\n"
    ".Ldly:\n"
    "v_readfirstlane_b32 s84, %3\n"
    "v_and_b32 v156, 63, %3\n"
    "s_and_b32 s52, %2, 7\n"
    "s_bfe_u32 s72, %2, 0x20003\n"
    "s_lshr_b32 s53, %2, 5\n"
    "s_lshl_b32 s52, s52, 2\n"
    "s_lshr_b32 s84, s84, 6\n"
    "s_or_b32 s52, s52, s72\n"
    "v_lshlrev_b32 v1, 4, v156\n"
    "s_mov_b32 s30, 1572864\n"
    "s_mov_b32 s31, 0x20000\n"
    "s_mov_b32 s42, 1572864\n"
    "s_mov_b32 s43, 0x20000\n"
    "s_mov_b32 s46, 12288\n"
    "s_mov_b32 s47, 0x20000\n"
    "s_mov_b32 s90, 12288\n"
    "s_mov_b32 s91, 0x20000\n"
    "s_lshr_b32 s79, s84, 1\n"
    "s_and_b32 s59, s84, 1\n"
    "s_cmp_ge_u32 s53, 6\n"
    "s_cselect_b32 s83, 1, 0\n"
    "s_or_b32 s83, s83, s84\n"
    "s_sub_u32 s58, 2, s79\n"
    "s_mul_i32 s71, s58, 49152\n"
    "s_mul_i32 s72, s52, 1572864\n"
    "s_mul_i32 s73, s52, 12288\n"
    "v_lshrrev_b32 v157, 2, v156\n"
    "v_and_b32 v158, 3, v156\n"
    "s_mul_i32 s35, s84, 3072\n"
    "s_bitcmp1_b32 s83, 0\n"
    "s_cbranch_scc1 .Lf2b\n"
    "v_and_b32 v159, 32, v156\n"
    "v_mul_u32_u24 v242, 376, v159\n"
    "v_and_b32 v159, 16, v156\n"
    "v_mul_u32_u24 v159, 88, v159\n"
    "v_lshl_add_u32 v242, v156, 3, v242\n"
    "v_add_u32 v242, v159, v242\n"
    "s_lshl_b32 s63, s53, 7\n"
    "v_mul_u32_u24 v160, 768, v157\n"
    "v_lshl_add_u32 v160, v158, 4, v160\n"
    "s_and_b32 s81, s58, 1\n"
    "s_mul_i32 s81, s81, 384\n"
    "s_lshl_b32 s75, s53, 6\n"
    "s_add_u32 s81, s81, s75\n"
    "s_add_u32 s75, s71, s63\n"
    "s_add_u32 s76, s75, 6144\n"
    "s_add_u32 s77, s75, 24576\n"
    "s_add_u32 s78, s75, 30720\n"
    "s_waitcnt lgkmcnt(0)\n"
    "s_add_u32 s44, s10, s73\n"
    "s_addc_u32 s45, s11, 0\n"
    "s_add_u32 s88, s12, s73\n"
    "s_addc_u32 s89, s13, 0\n"
    "buffer_load_dwordx4 v[4:7], v1, s[44:47], s35 offen offset:0\n"
    "buffer_load_dwordx4 v[8:11], v1, s[44:47], s35 offen offset:1024\n"
    "buffer_load_dwordx4 v[12:15], v1, s[44:47], s35 offen offset:2048\n"
    "buffer_load_dwordx4 v[16:19], v1, s[88:91], 0 offen offset:0\n"
    "buffer_load_dwordx4 v[164:167], v160, s[44:47], s81 offen offset:0\n"
    "buffer_load_dwordx4 v[168:171], v160, s[88:91], s81 offen offset:0\n"
    "s_add_u32 s28, s6, s72\n"
    "s_addc_u32 s29, s7, 0\n"
    "buffer_load_dwordx2 v[204:205], v242, s[28:31], s75 offen offset:0 nt\n"
    "buffer_load_dwordx2 v[206:207], v242, s[28:31], s75 offen offset:768 nt\n"
    "buffer_load_dwordx2 v[208:209], v242, s[28:31], s75 offen offset:3072 nt\n"
    "buffer_load_dwordx2 v[210:211], v242, s[28:31], s75 offen offset:3840 nt\n"
    "buffer_load_dwordx2 v[212:213], v242, s[28:31], s76 offen offset:0 nt\n"
    "buffer_load_dwordx2 v[214:215], v242, s[28:31], s76 offen offset:768 nt\n"
    "buffer_load_dwordx2 v[216:217], v242, s[28:31], s76 offen offset:3072 nt\n"
    "buffer_load_dwordx2 v[218:219], v242, s[28:31], s76 offen offset:3840 nt\n"
    "buffer_load_dwordx2 v[220:221], v242, s[28:31], s77 offen offset:0 nt\n"
    "buffer_load_dwordx2 v[222:223], v242, s[28:31], s77 offen offset:768 nt\n"
    "buffer_load_dwordx2 v[224:225], v242, s[28:31], s77 offen offset:3072 nt\n"
    "buffer_load_dwordx2 v[226:227], v242, s[28:31], s77 offen offset:3840 nt\n"
    "buffer_load_dwordx2 v[228:229], v242, s[28:31], s78 offen offset:0 nt\n"
    "buffer_load_dwordx2 v[230:231], v242, s[28:31], s78 offen offset:768 nt\n"
    "buffer_load_dwordx2 v[232:233], v242, s[28:31], s78 offen offset:3072 nt\n"
    "buffer_load_dwordx2 v[234:235], v242, s[28:31], s78 offen offset:3840 nt\n"
    "s_add_u32 s75, s71, s63\n"
    "s_add_u32 s75, s75, 98304\n"
    "s_add_u32 s76, s75, 6144\n"
    "s_add_u32 s77, s75, 24576\n"
    "s_add_u32 s78, s75, 30720\n"
    "buffer_load_dwordx2 v[36:37], v242, s[28:31], s75 offen offset:0 nt\n"
    "buffer_load_dwordx2 v[38:39], v242, s[28:31], s75 offen offset:768 nt\n"
    "v_lshrrev_b32 v162, 1, v156\n"
    "v_and_b32 v162, 16, v162\n"
    "v_bfe_u32 v159, v156, 4, 1\n"
    "v_or_b32 v162, v162, v159\n"
    "v_and_or_b32 v162, v157, 2, v162\n"
    "v_mul_u32_u24 v162, 768, v162\n"
    "v_and_b32 v163, 0x70, v1\n"
    "v_add_u32 v162, v162, v163\n"
    "s_branch .Lf2e\n"
    ".Lf2b:\n"
    "v_and_b32 v159, 48, v156\n"
    "v_lshlrev_b32 v242, 2, v159\n"
    "v_lshl_add_u32 v243, v158, 4, v242\n"
    "v_and_b32 v159, 12, v156\n"
    "v_mul_u32_u24 v242, 384, v159\n"
    "v_add_u32 v242, v243, v242\n"
    "v_add_u32 v243, v243, v159\n"
    "s_mul_i32 s63, s53, 6144\n"
    "s_sub_u32 s63, s63, 49152\n"
    "s_cmp_eq_u32 s59, 0\n"
    "s_cselect_b32 s63, 0x7f000000, s63\n"
    "s_add_u32 s75, s71, s63\n"
    "s_waitcnt lgkmcnt(0)\n"
    "s_add_u32 s44, s10, s73\n"
    "s_addc_u32 s45, s11, 0\n"
    "s_add_u32 s88, s12, s73\n"
    "s_addc_u32 s89, s13, 0\n"
    "buffer_load_dwordx4 v[4:7], v1, s[44:47], s35 offen offset:0\n"
    "buffer_load_dwordx4 v[8:11], v1, s[44:47], s35 offen offset:1024\n"
    "buffer_load_dwordx4 v[12:15], v1, s[44:47], s35 offen offset:2048\n"
    "buffer_load_dwordx4 v[16:19], v1, s[88:91], 0 offen offset:0\n"
    "s_add_u32 s40, s8, s72\n"
    "s_addc_u32 s41, s9, 0\n"
    "buffer_load_dwordx4 v[204:207], v242, s[40:43], s75 offen offset:0 nt\n"
    "buffer_load_dwordx4 v[208:211], v242, s[40:43], s75 offen offset:768 nt\n"
    "buffer_load_dwordx4 v[212:215], v242, s[40:43], s75 offen offset:256 nt\n"
    "buffer_load_dwordx4 v[216:219], v242, s[40:43], s75 offen offset:1024 nt\n"
    "buffer_load_dwordx4 v[220:223], v242, s[40:43], s75 offen offset:512 nt\n"
    "buffer_load_dwordx4 v[224:227], v242, s[40:43], s75 offen offset:1280 nt\n"
    "s_add_u32 s75, s71, s63\n"
    "s_add_u32 s75, s75, 98304\n"
    "buffer_load_dwordx4 v[36:39], v242, s[40:43], s75 offen offset:0 nt\n"
    "s_add_u32 s28, s6, s72\n"
    "s_addc_u32 s29, s7, 0\n"
    "v_lshrrev_b32 v162, 1, v156\n"
    "v_and_b32 v162, 16, v162\n"
    "v_bfe_u32 v159, v156, 4, 1\n"
    "v_or_b32 v162, v162, v159\n"
    "v_and_or_b32 v162, v157, 2, v162\n"
    "v_mul_u32_u24 v162, 768, v162\n"
    "v_and_b32 v163, 0x70, v1\n"
    "v_add_u32 v162, v162, v163\n"
    ".Lf2e:\n"
    "s_nop 4\n"
    "v_lshlrev_b32 v247, 2, v156\n"
    "v_add_u32 v247, 155648, v247\n"
    "v_mov_b32 v159, 0\n"
    "ds_write_b32 v247, v159\n"
    "v_mov_b32 v159, 0xff800000\n"
    "s_lshl2_add_u32 s74, s84, 155936\n"
    "v_mov_b32 v160, s74\n"
    "ds_write_b32 v160, v159\n"
    "s_mov_b32 s38, 5376\n"
    "s_mov_b32 s39, 0x20000\n"
    "s_mov_b32 s94, 4096\n"
    "s_mov_b32 s95, 0x20000\n"
    "s_mov_b32 s98, 4096\n"
    "s_mov_b32 s99, 0x20000\n"
    "s_mov_b32 s22, 98304\n"
    "s_mov_b32 s23, 0x20000\n"
    "s_mov_b32 s26, 98304\n"
    "s_mov_b32 s27, 0x20000\n"
    "s_lshl_b32 s80, s58, 7\n"
    "s_add_u32 s80, s80, 256\n"
    "s_lshl_b32 s74, s52, 16\n"
    "s_mul_i32 s69, s59, 384\n"
    "s_mul_i32 s57, s79, 6144\n"
    "s_add_u32 s57, s57, s69\n"
    "s_add_u32 s66, s57, 24576\n"
    "s_add_u32 s36, s18, s74\n"
    "s_addc_u32 s37, s19, 0\n"
    "s_lshr_b32 s74, s74, 4\n"
    "s_add_u32 s92, s14, s74\n"
    "s_addc_u32 s93, s15, 0\n"
    "s_add_u32 s96, s16, s74\n"
    "s_addc_u32 s97, s17, 0\n"
    "v_lshlrev_b32 v128, 3, v156\n"
    "v_mov_b32 v129, 0\n"
    "v_lshlrev_b32 v130, 4, %3\n"
    "v_xor_b32 v130, 1024, v130\n"
    "buffer_load_dwordx2 v[124:125], v128, s[44:47], 0 offen offset:0\n"
    "buffer_load_dwordx2 v[126:127], v128, s[88:91], 0 offen offset:0\n"
    "buffer_load_dwordx2 v[238:239], v129, s[36:39], 0 offen offset:0 sc1\n"
    "s_mul_i32 s74, s52, 786432\n"
    "s_mul_i32 s73, s53, 98304\n"
    "s_add_u32 s74, s74, s73\n"
    "s_add_u32 s20, s4, s74\n"
    "s_addc_u32 s21, s5, 0\n"
    "s_add_u32 s24, s24, s74\n"
    "s_addc_u32 s25, s25, 0\n"
    "s_mul_i32 s72, s52, 1626112\n"
    "s_add_u32 s72, s72, 2097152\n"
    "s_add_u32 s16, s18, s72\n"
    "s_addc_u32 s17, s19, 0\n"
    "s_mov_b32 s18, 1622016\n"
    "s_mov_b32 s19, 0x20000\n"
    "s_mul_i32 s65, s84, 6144\n"
    "s_mul_i32 s85, s84, 24576\n"
    "s_waitcnt lgkmcnt(0)\n"
    "s_barrier\n"
    "s_add_u32 s73, s65, 49152\n"
    "v_add_u32 v131, s73, v1\n"
    "s_add_u32 s73, s73, s69\n"
    "v_add_u32 v132, s73, v163\n"
    "v_and_b32 v159, 32, v156\n"
    "v_lshlrev_b32 v244, 3, v156\n"
    "v_xor_b32 v244, v244, v159\n"
    "v_lshl_add_u32 v244, v159, 3, v244\n"
    "s_bitcmp1_b32 s83, 0\n"
    "s_cbranch_scc1 .Lf5b\n"
    "s_lshl_b32 s60, s53, 11\n"
    "s_add_u32 s61, s60, 12288\n"
    "s_lshl_b32 s73, s58, 8\n"
    "s_add_u32 s73, s73, 122880\n"
    "v_and_b32 v243, 0xf0, v1\n"
    "v_add_u32 v243, s73, v243\n"
    "s_and_b32 s73, s73, 0x1fd00\n"
    "v_lshlrev_b32 v160, 9, v157\n"
    "v_lshl_add_u32 v160, v158, 6, v160\n"
    "v_add_u32 v160, s73, v160\n"
    "s_lshl_b32 s73, s79, 8\n"
    "s_mul_i32 s74, s59, 6144\n"
    "s_add_u32 s73, s73, s74\n"
    "v_add_u32 v163, s73, v244\n"
    "v_and_b32 v159, 32, v156\n"
    "v_lshlrev_b32 v244, 2, v156\n"
    "v_xor_b32 v244, v244, v159\n"
    "v_mad_u32_u24 v244, v159, 12, v244\n"
    "buffer_load_dwordx2 v[40:41], v242, s[28:31], s75 offen offset:3072 nt\n"
    "buffer_load_dwordx2 v[42:43], v242, s[28:31], s75 offen offset:3840 nt\n"
    "v_add_u32 v159, 0, v156\n"
    "v_mul_u32_u24 v159, 2731, v159\n"
    "v_lshrrev_b32 v159, 16, v159\n"
    "v_xor_b32 v159, v159, v156\n"
    "v_and_b32 v159, 7, v159\n"
    "v_and_b32 v88, 0x380, v1\n"
    "v_lshl_add_u32 v88, v159, 4, v88\n"
    "v_add_u32 v159, 64, v156\n"
    "v_mul_u32_u24 v159, 2731, v159\n"
    "v_lshrrev_b32 v159, 16, v159\n"
    "v_xor_b32 v159, v159, v156\n"
    "v_and_b32 v159, 7, v159\n"
    "v_and_b32 v89, 0x380, v1\n"
    "v_lshl_add_u32 v89, v159, 4, v89\n"
    "v_add_u32 v159, 128, v156\n"
    "v_mul_u32_u24 v159, 2731, v159\n"
    "v_lshrrev_b32 v159, 16, v159\n"
    "v_xor_b32 v159, v159, v156\n"
    "v_and_b32 v159, 7, v159\n"
    "v_and_b32 v90, 0x380, v1\n"
    "v_lshl_add_u32 v90, v159, 4, v90\n"
    "v_and_b32 v2, 7, v156\n"
    "v_lshrrev_b32 v144, 7, v130\n"
    "v_mul_u32_u24 v144, 768, v144\n"
    "v_lshl_add_u32 v144, v2, 3, v144\n"
    "buffer_load_dwordx2 v[44:45], v242, s[28:31], s76 offen offset:0 nt\n"
    "buffer_load_dwordx2 v[46:47], v242, s[28:31], s76 offen offset:768 nt\n"
    "v_add_u32 v144, 131072, v144\n"
    "v_mad_u32_u24 v86, v2, 24, v144\n"
    "v_and_b32 v96, 15, v156\n"
    "v_lshrrev_b32 v159, 4, v156\n"
    "v_xor_b32 v159, v159, v2\n"
    "v_mul_u32_u24 v96, 384, v96\n"
    "v_lshl_add_u32 v96, v159, 4, v96\n"
    "s_add_u32 s73, s65, 49152\n"
    "v_add_u32 v96, s73, v96\n"
    "v_xor_b32 v97, 64, v96\n"
    "v_subrev_u32 v154, 24576, v96\n"
    "v_subrev_u32 v155, 24576, v97\n"
    "s_add_u32 s67, s65, 98304\n"
    "s_add_u32 s68, s65, 24576\n"
    "s_add_u32 s69, s65, 73728\n"
    "s_mov_b32 s81, 256\n"
    "s_mul_i32 s74, s79, 24576\n"
    "s_add_u32 s70, s74, s65\n"
    "s_add_u32 s70, s70, 73728\n"
    "s_sub_u32 s64, s65, s74\n"
    "s_add_u32 s64, s64, 98304\n"
    "v_and_b32 v249, 0x310, v1\n"
    "v_and_b32 v159, 12, v156\n"
    "v_lshl_add_u32 v249, v159, 3, v249\n"
    "v_and_b32 v159, 2, v156\n"
    "buffer_load_dwordx2 v[48:49], v242, s[28:31], s76 offen offset:3072 nt\n"
    "buffer_load_dwordx2 v[50:51], v242, s[28:31], s76 offen offset:3840 nt\n"
    "v_lshl_add_u32 v249, v159, 6, v249\n"
    "s_add_u32 s73, s74, 73728\n"
    "v_add_u32 v248, s73, v249\n"
    "s_sub_u32 s73, 98304, s74\n"
    "v_add_u32 v249, s73, v249\n"
    "s_or_b32 s73, s53, s84\n"
    "s_cmp_eq_u32 s73, 0\n"
    "s_cselect_b32 s82, 0, 0x7fff0000\n"
    "v_and_b32 v252, 12, v156\n"
    "v_bfe_u32 v159, v156, 1, 1\n"
    "v_or_b32 v252, v252, v159\n"
    "v_and_b32 v159, 1, v156\n"
    "v_lshl_or_b32 v252, v159, 1, v252\n"
    "v_lshlrev_b32 v252, 6, v252\n"
    "v_lshlrev_b32 v159, 2, v156\n"
    "v_and_b32 v159, 32, v159\n"
    "v_xor_b32 v159, v159, v156\n"
    "v_and_or_b32 v252, v159, 48, v252\n"
    "v_mov_b32 v2, v252\n"
    "v_add_u32 v3, 24576, v252\n"
    "v_lshrrev_b32 v91, 3, v156\n"
    "v_lshl_add_u32 v87, v91, 6, 64\n"
    "v_add_u32 v87, 64, v87\n"
    "v_and_b32 v241, 1, v156\n"
    "v_lshlrev_b32 v241, 3, v241\n"
    "buffer_load_dwordx2 v[52:53], v242, s[28:31], s77 offen offset:0 nt\n"
    "buffer_load_dwordx2 v[54:55], v242, s[28:31], s77 offen offset:768 nt\n"
    "v_lshl_add_u32 v241, v91, 4, v241\n"
    "v_add_u32 v241, 1152, v241\n"
    "v_lshlrev_b32 v91, 24, v91\n"
    "v_lshl_add_u32 v91, v156, 3, v91\n"
    "s_lshl_b32 s73, s53, 4\n"
    "s_lshl_b32 s74, s59, 3\n"
    "s_add_u32 s73, s73, s74\n"
    "s_add_u32 s73, s73, 1152\n"
    "v_mov_b32 v240, 0x7fff0000\n"
    "v_writelane_b32 v240, s73, 0\n"
    "v_and_b32 v159, 48, v156\n"
    "s_mul_i32 s73, s79, 768\n"
    "s_sub_u32 s73, 131072, s73\n"
    "v_lshlrev_b32 v250, 1, v159\n"
    "v_add_u32 v250, s73, v250\n"
    "v_and_b32 v251, 3, v156\n"
    "v_lshl_add_u32 v251, v251, 7, v159\n"
    "s_add_u32 s73, s73, 256\n"
    "v_add_u32 v251, s73, v251\n"
    "v_and_b32 v159, 64, v1\n"
    "v_add_u32 v251, v159, v251\n"
    "v_bfe_i32 v159, v156, 2, 1\n"
    "v_and_b32 v159, 0xaaaa, v159\n"
    "v_or_b32 v253, 0x4444, v159\n"
    "s_mov_b32 s58, 0\n"
    "s_mov_b32 s59, 8\n"
    "s_waitcnt vmcnt(31)\n"
    "v_pk_min_i16 v4, v4, v10\n"
    "v_pk_min_i16 v5, v5, v11\n"
    "v_pk_min_i16 v6, v6, v12\n"
    "v_pk_min_i16 v7, v7, v13\n"
    "v_pk_min_i16 v8, v8, v14\n"
    "v_pk_min_i16 v9, v9, v15\n"
    "v_pk_min_i16 v16, v16, v18\n"
    "v_pk_min_i16 v17, v17, v19\n"
    "v_pk_min_i16 v4, v4, v7\n"
    "v_pk_min_i16 v5, v5, v8\n"
    "v_pk_min_i16 v6, v6, v9\n"
    "v_pk_min_i16 v16, v16, v17\n"
    "v_pk_min_i16 v4, v4, v5\n"
    "v_lshlrev_b32 v17, 16, v16\n"
    "v_pk_min_i16 v4, v4, v6\n"
    "v_min_i32 v16, v16, v17\n"
    "v_lshlrev_b32 v5, 16, v4\n"
    "v_mov_b32 v7, 155920\n"
    "v_min_i32 v4, v4, v5\n"
    "v_lshl_add_u32 v6, s84, 2, v7\n"
    "v_min_i32_dpp v16, v16, v16 row_shr:1\n"
    "v_min_i32_dpp v4, v4, v4 row_shr:1\n"
    "s_nop 0\n"
    "v_min_i32_dpp v16, v16, v16 row_shr:2\n"
    "v_min_i32_dpp v4, v4, v4 row_shr:2\n"
    "s_nop 0\n"
    "v_min_i32_dpp v16, v16, v16 row_shr:4\n"
    "v_min_i32_dpp v4, v4, v4 row_shr:4\n"
    "s_nop 0\n"
    "v_min_i32_dpp v16, v16, v16 row_shr:8\n"
    "v_min_i32_dpp v4, v4, v4 row_shr:8\n"
    "s_nop 0\n"
    "v_min_i32_dpp v16, v16, v16 row_bcast:15 row_mask:0xa\n"
    "v_min_i32_dpp v4, v4, v4 row_bcast:15 row_mask:0xa\n"
    "s_nop 0\n"
    "v_min_i32_dpp v16, v16, v16 row_bcast:31 row_mask:0xc\n"
    "v_min_i32_dpp v4, v4, v4 row_bcast:31 row_mask:0xc\n"
    "v_readlane_b32 s35, v16, 63\n"
    "v_readlane_b32 s73, v4, 63\n"
    "s_max_i32 s35, s35, 0\n"
    "s_and_b32 s35, s35, 0xffff0000\n"
    "v_mov_b32 v4, s73\n"
    "ds_write_b32 v6, v4\n"
    "v_mov_b32 v14, -32\n"
    "v_mov_b32 v12, -126\n"
    "v_mov_b32 v13, 0x3dd53b95\n"
    "v_mov_b32 v98, 0\n"
    "s_waitcnt vmcnt(29)\n"
    "v_lshlrev_b32 v180, 16, v164\n"
    "v_and_b32 v181, 0xffff0000, v164\n"
    "v_lshlrev_b32 v182, 16, v168\n"
    "v_and_b32 v183, 0xffff0000, v168\n"
    "v_lshlrev_b32 v184, 16, v165\n"
    "v_and_b32 v185, 0xffff0000, v165\n"
    "v_lshlrev_b32 v186, 16, v169\n"
    "v_and_b32 v187, 0xffff0000, v169\n"
    "v_lshlrev_b32 v188, 16, v166\n"
    "v_and_b32 v189, 0xffff0000, v166\n"
    "v_lshlrev_b32 v190, 16, v170\n"
    "v_and_b32 v191, 0xffff0000, v170\n"
    "v_lshlrev_b32 v192, 16, v167\n"
    "v_and_b32 v193, 0xffff0000, v167\n"
    "v_lshlrev_b32 v194, 16, v171\n"
    "v_and_b32 v195, 0xffff0000, v171\n"
    "s_waitcnt lgkmcnt(0)\n"
    "s_barrier\n"
    "ds_read_b128 v[8:11], v7 offset:0\n"
    "buffer_load_dwordx2 v[56:57], v242, s[28:31], s77 offen offset:3072 nt\n"
    "buffer_load_dwordx2 v[58:59], v242, s[28:31], s77 offen offset:3840 nt\n"
    "buffer_load_dwordx4 v[136:139], v130, s[92:95], 0 offen offset:0\n"
    "buffer_load_dwordx4 v[140:143], v130, s[96:99], 0 offen offset:0\n"
    "s_nop 0\n"
    "s_nop 0\n"
    "s_nop 0\n"
    "s_nop 0\n"
    "s_nop 0\n"
    "s_nop 0\n"
    "s_nop 0\n"
    "s_nop 0\n"
    "s_nop 0\n"
    "s_nop 0\n"
    "s_nop 0\n"
    "s_nop 0\n"
    "s_nop 0\n"
    "s_nop 0\n"
    "s_nop 0\n"
    "s_nop 0\n"
    "s_nop 0\n"
    "s_nop 0\n"
    "s_nop 0\n"
    "s_nop 0\n"
    "s_nop 0\n"
    "s_nop 0\n"
    "s_nop 0\n"
    "s_nop 0\n"
    "s_nop 0\n"
    "s_nop 0\n"
    "s_nop 0\n"
    "s_nop 0\n"
    "s_nop 0\n"
    "s_nop 0\n"
    "s_nop 0\n"
    "s_nop 0\n"
    "s_nop 0\n"
    "s_nop 0\n"
    "s_nop 0\n"
    "s_nop 0\n"
    "s_nop 0\n"
    "s_nop 0\n"
    "s_nop 0\n"
    "s_nop 0\n"
    "s_nop 0\n"
    "s_nop 0\n"
    "s_nop 0\n"
    "s_nop 0\n"
    "s_nop 0\n"
    "s_nop 0\n"
    "s_nop 0\n"
    "s_nop 0\n"
    "s_nop 0\n"
    "s_nop 0\n"
    "s_nop 0\n"
    "s_nop 0\n"
    "s_nop 0\n"
    "s_nop 0\n"
    "s_nop 0\n"
    "s_nop 0\n"
    "s_nop 0\n"
    "s_nop 0\n"
    "s_nop 0\n"
    "s_nop 0\n"
    "s_waitcnt lgkmcnt(0)\n"
    "v_min3_i32 v4, v8, v9, v10\n"
    "v_min_i32 v4, v4, v11\n"
    "v_max_i32 v4, 0, v4\n"
    "v_and_b32 v4, 0xffff0000, v4\n"
    "v_mul_f32 v4, s35, v4\n"
    "v_mul_f32 v4, 0x3ed53b95, v4\n"
    "v_lshrrev_b32 v4, 23, v4\n"
    "v_sub_u32 v4, 2, v4\n"
    "v_med3_i32 v4, v4, v14, v12\n"
    "v_ldexp_f32 v13, v13, v4\n"
    "v_readfirstlane_b32 s89, v4\n"
    "v_readfirstlane_b32 s88, v13\n"
    "s_mul_i32 s89, s89, 0xff7fff80\n"
    "s_mov_b32 s90, 149\n"
    "v_mul_f32 v180, s88, v180\n"
    "v_mul_f32 v181, s88, v181\n"
    "v_mul_f32 v184, s88, v184\n"
    "v_mul_f32 v185, s88, v185\n"
    "v_mul_f32 v188, s88, v188\n"
    "v_mul_f32 v189, s88, v189\n"
    "v_mul_f32 v192, s88, v192\n"
    "v_mul_f32 v193, s88, v193\n"
    "v_fmaak_f32 v182, v182, v180, 0x80730000\n"
    "v_fmaak_f32 v183, v183, v181, 0x80730000\n"
    "v_fmaak_f32 v186, v186, v184, 0x80730000\n"
    "v_fmaak_f32 v187, v187, v185, 0x80730000\n"
    "v_fmaak_f32 v190, v190, v188, 0x80730000\n"
    "v_fmaak_f32 v191, v191, v189, 0x80730000\n"
    "v_fmaak_f32 v194, v194, v192, 0x80730000\n"
    "v_fmaak_f32 v195, v195, v193, 0x80730000\n"
    "v_ldexp_f32 v180, v180, s90\n"
    "v_ldexp_f32 v181, v181, s90\n"
    "v_ldexp_f32 v184, v184, s90\n"
    "v_ldexp_f32 v185, v185, s90\n"
    "v_ldexp_f32 v188, v188, s90\n"
    "v_ldexp_f32 v189, v189, s90\n"
    "v_ldexp_f32 v192, v192, s90\n"
    "v_ldexp_f32 v193, v193, s90\n"
    "v_fma_f32 v180, v180, 0, v180\n"
    "v_fma_f32 v181, v181, 0, v181\n"
    "v_fma_f32 v184, v184, 0, v184\n"
    "v_fma_f32 v185, v185, 0, v185\n"
    "v_fma_f32 v188, v188, 0, v188\n"
    "v_fma_f32 v189, v189, 0, v189\n"
    "v_fma_f32 v192, v192, 0, v192\n"
    "v_fma_f32 v193, v193, 0, v193\n"
    "ds_write_b128 v160, v[180:183] offset:0\n"
    "ds_write_b128 v160, v[184:187] offset:16\n"
    "ds_write_b128 v160, v[188:191] offset:32\n"
    "ds_write_b128 v160, v[192:195] offset:48\n"
    "s_waitcnt vmcnt(17)\n"
    "s_add_u32 s54, s71, s60\n"
    "s_add_u32 s55, s71, s61\n"
    "ds_read_b128 v[92:95], v243 offset:0\n"
    "s_waitcnt lgkmcnt(0)\n"
    "v_fma_f32 v204, v204, v92, -v94\n"
    "v_fma_f32 v205, v205, v93, -v95\n"
    "v_fma_f32 v206, v206, v92, -v94\n"
    "v_fma_f32 v207, v207, v93, -v95\n"
    "v_cvt_pk_bf16_f32 v204, v204, v205\n"
    "v_cvt_pk_bf16_f32 v206, v206, v207\n"
    "buffer_store_dword v204, v244, s[16:19], s54 offen offset:0 sc1\n"
    "buffer_store_dword v206, v244, s[16:19], s54 offen offset:128 sc1\n"
    "v_fma_f32 v208, v208, v92, -v94\n"
    "v_fma_f32 v209, v209, v93, -v95\n"
    "v_fma_f32 v210, v210, v92, -v94\n"
    "v_fma_f32 v211, v211, v93, -v95\n"
    "v_cvt_pk_bf16_f32 v208, v208, v209\n"
    "v_cvt_pk_bf16_f32 v210, v210, v211\n"
    "buffer_store_dword v208, v244, s[16:19], s54 offen offset:1024 sc1\n"
    "buffer_store_dword v210, v244, s[16:19], s54 offen offset:1152 sc1\n"
    "v_fma_f32 v212, v212, v92, -v94\n"
    "v_fma_f32 v213, v213, v93, -v95\n"
    "buffer_load_dwordx2 v[60:61], v242, s[28:31], s78 offen offset:0 nt\n"
    "buffer_load_dwordx2 v[62:63], v242, s[28:31], s78 offen offset:768 nt\n"
    "buffer_load_dwordx4 v[4:7], v162, s[28:31], s57 offen offset:0\n"
    "buffer_load_dwordx4 v[8:11], v162, s[28:31], s57 offen offset:3072\n"
    "v_fma_f32 v214, v214, v92, -v94\n"
    "v_fma_f32 v215, v215, v93, -v95\n"
    "v_cvt_pk_bf16_f32 v212, v212, v213\n"
    "v_cvt_pk_bf16_f32 v214, v214, v215\n"
    "buffer_store_dword v212, v244, s[16:19], s54 offen offset:256 sc1\n"
    "buffer_store_dword v214, v244, s[16:19], s54 offen offset:384 sc1\n"
    "v_fma_f32 v216, v216, v92, -v94\n"
    "v_fma_f32 v217, v217, v93, -v95\n"
    "v_fma_f32 v218, v218, v92, -v94\n"
    "v_fma_f32 v219, v219, v93, -v95\n"
    "v_cvt_pk_bf16_f32 v216, v216, v217\n"
    "v_cvt_pk_bf16_f32 v218, v218, v219\n"
    "buffer_store_dword v216, v244, s[16:19], s54 offen offset:1280 sc1\n"
    "buffer_store_dword v218, v244, s[16:19], s54 offen offset:1408 sc1\n"
    "v_fma_f32 v220, v220, v92, -v94\n"
    "v_fma_f32 v221, v221, v93, -v95\n"
    "v_fma_f32 v222, v222, v92, -v94\n"
    "v_fma_f32 v223, v223, v93, -v95\n"
    "v_cvt_pk_bf16_f32 v220, v220, v221\n"
    "v_cvt_pk_bf16_f32 v222, v222, v223\n"
    "buffer_store_dword v220, v244, s[16:19], s55 offen offset:0 sc1\n"
    "buffer_store_dword v222, v244, s[16:19], s55 offen offset:128 sc1\n"
    "v_fma_f32 v224, v224, v92, -v94\n"
    "buffer_load_dwordx2 v[64:65], v242, s[28:31], s78 offen offset:3072 nt\n"
    "buffer_load_dwordx2 v[66:67], v242, s[28:31], s78 offen offset:3840 nt\n"
    "buffer_load_dwordx4 v[12:15], v162, s[28:31], s57 offen offset:128\n"
    "v_fma_f32 v225, v225, v93, -v95\n"
    "v_fma_f32 v226, v226, v92, -v94\n"
    "v_fma_f32 v227, v227, v93, -v95\n"
    "v_cvt_pk_bf16_f32 v224, v224, v225\n"
    "v_cvt_pk_bf16_f32 v226, v226, v227\n"
    "buffer_store_dword v224, v244, s[16:19], s55 offen offset:1024 sc1\n"
    "buffer_store_dword v226, v244, s[16:19], s55 offen offset:1152 sc1\n"
    "v_fma_f32 v228, v228, v92, -v94\n"
    "v_fma_f32 v229, v229, v93, -v95\n"
    "v_fma_f32 v230, v230, v92, -v94\n"
    "v_fma_f32 v231, v231, v93, -v95\n"
    "v_cvt_pk_bf16_f32 v228, v228, v229\n"
    "v_cvt_pk_bf16_f32 v230, v230, v231\n"
    "buffer_store_dword v228, v244, s[16:19], s55 offen offset:256 sc1\n"
    "buffer_store_dword v230, v244, s[16:19], s55 offen offset:384 sc1\n"
    "v_fma_f32 v232, v232, v92, -v94\n"
    "v_fma_f32 v233, v233, v93, -v95\n"
    "v_fma_f32 v234, v234, v92, -v94\n"
    "v_fma_f32 v235, v235, v93, -v95\n"
    "v_cvt_pk_bf16_f32 v232, v232, v233\n"
    "v_cvt_pk_bf16_f32 v234, v234, v235\n"
    "buffer_store_dword v232, v244, s[16:19], s55 offen offset:1280 sc1\n"
    "buffer_store_dword v234, v244, s[16:19], s55 offen offset:1408 sc1\n"
    "v_add_u32 v243, 512, v243\n"
    "s_add_u32 s71, s71, 98304\n"
    "s_add_u32 m0, s65, 24576\n"
    "s_add_u32 s72, s85, 0\n"
    "buffer_load_dwordx4 v88, s[20:23], s72 offen offset:0 nt lds\n"
    "buffer_load_dwordx4 v[16:19], v162, s[28:31], s57 offen offset:3200\n"
    "buffer_load_dwordx4 v[20:23], v162, s[28:31], s57 offen offset:256\n"
    "s_waitcnt vmcnt(38)\n"
    "v_add_co_u32 v238, vcc, 0x7f4a7c15, v238\n"
    "v_mov_b32 v159, 0x9e3779b9\n"
    "s_nop 0\n"
    "v_addc_co_u32 v239, vcc, v159, v239, vcc\n"
    "v_mov_b32 v246, 1\n"
    "s_waitcnt vmcnt(39)\n"
    "v_lshlrev_b32 v180, 16, v124\n"
    "v_and_b32 v181, 0xffff0000, v124\n"
    "v_lshlrev_b32 v182, 16, v125\n"
    "v_and_b32 v183, 0xffff0000, v125\n"
    "v_lshlrev_b32 v184, 16, v126\n"
    "v_and_b32 v185, 0xffff0000, v126\n"
    "v_lshlrev_b32 v186, 16, v127\n"
    "v_and_b32 v187, 0xffff0000, v127\n"
    "v_mul_f32 v180, s88, v180\n"
    "v_mul_f32 v181, s88, v181\n"
    "v_mul_f32 v182, s88, v182\n"
    "v_mul_f32 v183, s88, v183\n"
    "v_fmaak_f32 v184, v184, v180, 0x80730000\n"
    "v_fmaak_f32 v185, v185, v181, 0x80730000\n"
    "v_fmaak_f32 v186, v186, v182, 0x80730000\n"
    "v_fmaak_f32 v187, v187, v183, 0x80730000\n"
    "s_add_u32 m0, s65, 25600\n"
    "s_add_u32 s72, s85, 1024\n"
    "buffer_load_dwordx4 v89, s[20:23], s72 offen offset:0 nt lds\n"
    "buffer_load_dwordx4 v[24:27], v162, s[28:31], s57 offen offset:3328\n"
    "v_ldexp_f32 v180, v180, s90\n"
    "v_ldexp_f32 v181, v181, s90\n"
    "v_ldexp_f32 v182, v182, s90\n"
    "v_ldexp_f32 v183, v183, s90\n"
    "v_fma_f32 v180, v180, 0, v180\n"
    "v_fma_f32 v181, v181, 0, v181\n"
    "v_fma_f32 v182, v182, 0, v182\n"
    "v_fma_f32 v183, v183, 0, v183\n"
    "ds_write_b128 v131, v[180:183] offset:0\n"
    "ds_write_b128 v131, v[184:187] offset:1024\n"
    "ds_read_b128 v[180:183], v132 offset:0\n"
    "ds_read_b128 v[184:187], v132 offset:1024\n"
    "ds_read_b128 v[188:191], v132 offset:128\n"
    "ds_read_b128 v[192:195], v132 offset:1152\n"
    "ds_read_b128 v[196:199], v132 offset:256\n"
    "ds_read_b128 v[200:203], v132 offset:1280\n"
    "s_nop 0\n"
    "s_nop 0\n"
    "s_nop 0\n"
    "s_nop 0\n"
    "s_nop 0\n"
    "s_nop 0\n"
    "s_nop 0\n"
    "s_add_u32 m0, s65, 26624\n"
    "s_add_u32 s72, s85, 2048\n"
    "buffer_load_dwordx4 v90, s[20:23], s72 offen offset:0 nt lds\n"
    "buffer_load_dwordx4 v[100:103], v162, s[28:31], s66 offen offset:0\n"
    "buffer_load_dwordx4 v[104:107], v162, s[28:31], s66 offen offset:3072\n"
    "s_nop 0\n"
    "s_nop 0\n"
    "s_nop 0\n"
    "s_nop 0\n"
    "s_nop 0\n"
    "s_nop 0\n"
    "s_nop 0\n"
    "s_nop 0\n"
    "s_nop 0\n"
    "s_nop 0\n"
    "s_nop 0\n"
    "s_nop 0\n"
    "s_nop 0\n"
    "s_nop 0\n"
    "s_nop 0\n"
    "s_nop 0\n"
    "s_nop 0\n"
    "s_nop 0\n"
    "s_nop 0\n"
    "s_nop 0\n"
    "s_nop 0\n"
    "s_nop 0\n"
    "s_add_u32 m0, s65, 27648\n"
    "s_add_u32 s72, s85, 3072\n"
    "buffer_load_dwordx4 v88, s[20:23], s72 offen offset:0 nt lds\n"
    "buffer_load_dwordx4 v[108:111], v162, s[28:31], s66 offen offset:128\n"
    "s_nop 0\n"
    "s_nop 0\n"
    "s_nop 0\n"
    "s_nop 0\n"
    "s_nop 0\n"
    "s_nop 0\n"
    "s_nop 0\n"
    "s_nop 0\n"
    "s_nop 0\n"
    "s_nop 0\n"
    "s_nop 0\n"
    "s_nop 0\n"
    "s_nop 0\n"
    "s_nop 0\n"
    "s_nop 0\n"
    "s_nop 0\n"
    "s_nop 0\n"
    "s_nop 0\n"
    "s_nop 0\n"
    "s_nop 0\n"
    "s_nop 0\n"
    "s_nop 0\n"
    "s_nop 0\n"
    "s_waitcnt lgkmcnt(0)\n"
    "s_waitcnt vmcnt(10)\n"
    "ds_add_rtn_u32 v245, v247, v246\n"
    "s_lshl_b32 s73, s53, 6\n"
    "s_add_u32 s73, s73, 128\n"
    "s_waitcnt lgkmcnt(0)\n"
    "v_readfirstlane_b32 s74, v245\n"
    "s_nop 0\n"
    "s_cmp_eq_u32 s74, 3\n"
    "s_cselect_b32 s73, s73, 0x7fff0000\n"
    "buffer_store_dwordx2 v[238:239], v91, s[36:39], s73 offen offset:0 sc1\n"
    "s_waitcnt vmcnt(18)\n"
    "s_add_u32 m0, s65, 28672\n"
    "s_add_u32 s72, s85, 4096\n"
    "buffer_load_dwordx4 v89, s[20:23], s72 offen offset:0 nt lds\n"
    "buffer_load_dwordx4 v[112:115], v162, s[28:31], s66 offen offset:3200\n"
    "buffer_load_dwordx4 v[116:119], v162, s[28:31], s66 offen offset:256\n"
    "s_add_u32 s54, s71, s60\n"
    "s_add_u32 s55, s71, s61\n"
    "ds_read_b128 v[92:95], v243 offset:0\n"
    "s_waitcnt lgkmcnt(0)\n"
    "v_fma_f32 v36, v36, v92, -v94\n"
    "v_fma_f32 v37, v37, v93, -v95\n"
    "v_fma_f32 v38, v38, v92, -v94\n"
    "v_fma_f32 v39, v39, v93, -v95\n"
    "v_cvt_pk_bf16_f32 v36, v36, v37\n"
    "v_cvt_pk_bf16_f32 v38, v38, v39\n"
    "buffer_store_dword v36, v244, s[16:19], s54 offen offset:0 sc1\n"
    "buffer_store_dword v38, v244, s[16:19], s54 offen offset:128 sc1\n"
    "v_fma_f32 v40, v40, v92, -v94\n"
    "v_fma_f32 v41, v41, v93, -v95\n"
    "v_fma_f32 v42, v42, v92, -v94\n"
    "v_fma_f32 v43, v43, v93, -v95\n"
    "v_cvt_pk_bf16_f32 v40, v40, v41\n"
    "v_cvt_pk_bf16_f32 v42, v42, v43\n"
    "buffer_store_dword v40, v244, s[16:19], s54 offen offset:1024 sc1\n"
    "buffer_store_dword v42, v244, s[16:19], s54 offen offset:1152 sc1\n"
    "v_fma_f32 v44, v44, v92, -v94\n"
    "v_fma_f32 v45, v45, v93, -v95\n"
    "v_fma_f32 v46, v46, v92, -v94\n"
    "v_fma_f32 v47, v47, v93, -v95\n"
    "v_cvt_pk_bf16_f32 v44, v44, v45\n"
    "v_cvt_pk_bf16_f32 v46, v46, v47\n"
    "buffer_store_dword v44, v244, s[16:19], s54 offen offset:256 sc1\n"
    "buffer_store_dword v46, v244, s[16:19], s54 offen offset:384 sc1\n"
    "v_fma_f32 v48, v48, v92, -v94\n"
    "v_fma_f32 v49, v49, v93, -v95\n"
    "v_fma_f32 v50, v50, v92, -v94\n"
    "v_fma_f32 v51, v51, v93, -v95\n"
    "v_cvt_pk_bf16_f32 v48, v48, v49\n"
    "v_cvt_pk_bf16_f32 v50, v50, v51\n"
    "s_add_u32 m0, s65, 29696\n"
    "s_add_u32 s72, s85, 5120\n"
    "buffer_load_dwordx4 v90, s[20:23], s72 offen offset:0 nt lds\n"
    "buffer_load_dwordx4 v[120:123], v162, s[28:31], s66 offen offset:3328\n"
    "buffer_store_dword v48, v244, s[16:19], s54 offen offset:1280 sc1\n"
    "buffer_store_dword v50, v244, s[16:19], s54 offen offset:1408 sc1\n"
    "v_fma_f32 v52, v52, v92, -v94\n"
    "v_fma_f32 v53, v53, v93, -v95\n"
    "v_fma_f32 v54, v54, v92, -v94\n"
    "v_fma_f32 v55, v55, v93, -v95\n"
    "v_cvt_pk_bf16_f32 v52, v52, v53\n"
    "v_cvt_pk_bf16_f32 v54, v54, v55\n"
    "buffer_store_dword v52, v244, s[16:19], s55 offen offset:0 sc1\n"
    "buffer_store_dword v54, v244, s[16:19], s55 offen offset:128 sc1\n"
    "v_fma_f32 v56, v56, v92, -v94\n"
    "v_fma_f32 v57, v57, v93, -v95\n"
    "v_fma_f32 v58, v58, v92, -v94\n"
    "v_fma_f32 v59, v59, v93, -v95\n"
    "v_cvt_pk_bf16_f32 v56, v56, v57\n"
    "v_cvt_pk_bf16_f32 v58, v58, v59\n"
    "buffer_store_dword v56, v244, s[16:19], s55 offen offset:1024 sc1\n"
    "buffer_store_dword v58, v244, s[16:19], s55 offen offset:1152 sc1\n"
    "v_fma_f32 v60, v60, v92, -v94\n"
    "v_fma_f32 v61, v61, v93, -v95\n"
    "v_fma_f32 v62, v62, v92, -v94\n"
    "v_fma_f32 v63, v63, v93, -v95\n"
    "v_cvt_pk_bf16_f32 v60, v60, v61\n"
    "v_cvt_pk_bf16_f32 v62, v62, v63\n"
    "buffer_store_dword v60, v244, s[16:19], s55 offen offset:256 sc1\n"
    "buffer_store_dword v62, v244, s[16:19], s55 offen offset:384 sc1\n"
    "v_fma_f32 v64, v64, v92, -v94\n"
    "v_fma_f32 v65, v65, v93, -v95\n"
    "v_fma_f32 v66, v66, v92, -v94\n"
    "v_fma_f32 v67, v67, v93, -v95\n"
    "v_cvt_pk_bf16_f32 v64, v64, v65\n"
    "v_cvt_pk_bf16_f32 v66, v66, v67\n"
    "buffer_store_dword v64, v244, s[16:19], s55 offen offset:1280 sc1\n"
    "buffer_store_dword v66, v244, s[16:19], s55 offen offset:1408 sc1\n"
    "v_add_u32 v243, 512, v243\n"
    "s_add_u32 m0, s65, 73728\n"
    "s_add_u32 s72, s85, 6144\n"
    "buffer_load_dwordx4 v88, s[20:23], s72 offen offset:0 nt lds\n"
    "s_waitcnt vmcnt(56)\n"
    "v_lshlrev_b32 v204, 16, v136\n"
    "v_and_b32 v205, 0xffff0000, v136\n"
    "v_lshlrev_b32 v206, 16, v137\n"
    "v_and_b32 v207, 0xffff0000, v137\n"
    "v_lshlrev_b32 v208, 16, v138\n"
    "v_and_b32 v209, 0xffff0000, v138\n"
    "v_lshlrev_b32 v210, 16, v139\n"
    "v_and_b32 v211, 0xffff0000, v139\n"
    "v_log_f32 v68, v204\n"
    "v_log_f32 v69, v205\n"
    "v_log_f32 v70, v206\n"
    "v_log_f32 v71, v207\n"
    "v_log_f32 v72, v208\n"
    "v_log_f32 v73, v209\n"
    "v_log_f32 v74, v210\n"
    "v_log_f32 v75, v211\n"
    "v_rcp_f32 v204, v204\n"
    "v_rcp_f32 v205, v205\n"
    "v_rcp_f32 v206, v206\n"
    "s_add_u32 m0, s65, 74752\n"
    "s_add_u32 s72, s85, 7168\n"
    "buffer_load_dwordx4 v89, s[20:23], s72 offen offset:0 nt lds\n"
    "v_rcp_f32 v207, v207\n"
    "v_rcp_f32 v208, v208\n"
    "v_rcp_f32 v209, v209\n"
    "v_rcp_f32 v210, v210\n"
    "v_rcp_f32 v211, v211\n"
    "s_mov_b32 s73, 0x41a00000\n"
    "v_mov_b32 v228, 0x7fc00000\n"
    "v_cmp_le_f32 vcc, |v68|, s73\n"
    "s_nop 1\n"
    "v_cndmask_b32 v68, v228, v68, vcc\n"
    "v_cmp_le_f32 vcc, |v69|, s73\n"
    "s_nop 1\n"
    "v_cndmask_b32 v69, v228, v69, vcc\n"
    "v_cmp_le_f32 vcc, |v70|, s73\n"
    "s_nop 1\n"
    "v_cndmask_b32 v70, v228, v70, vcc\n"
    "v_cmp_le_f32 vcc, |v71|, s73\n"
    "s_nop 1\n"
    "v_cndmask_b32 v71, v228, v71, vcc\n"
    "v_cmp_le_f32 vcc, |v72|, s73\n"
    "s_nop 1\n"
    "v_cndmask_b32 v72, v228, v72, vcc\n"
    "v_cmp_le_f32 vcc, |v73|, s73\n"
    "s_nop 1\n"
    "v_cndmask_b32 v73, v228, v73, vcc\n"
    "v_cmp_le_f32 vcc, |v74|, s73\n"
    "s_nop 1\n"
    "v_cndmask_b32 v74, v228, v74, vcc\n"
    "s_add_u32 m0, s65, 75776\n"
    "s_add_u32 s72, s85, 8192\n"
    "buffer_load_dwordx4 v90, s[20:23], s72 offen offset:0 nt lds\n"
    "v_cmp_le_f32 vcc, |v75|, s73\n"
    "s_nop 1\n"
    "v_cndmask_b32 v75, v228, v75, vcc\n"
    "v_cvt_pk_bf16_f32 v220, v204, v205\n"
    "v_cvt_pk_bf16_f32 v221, v206, v207\n"
    "v_cvt_pk_bf16_f32 v222, v208, v209\n"
    "v_cvt_pk_bf16_f32 v223, v210, v211\n"
    "v_lshlrev_b32 v212, 16, v220\n"
    "v_and_b32 v213, 0xffff0000, v220\n"
    "v_lshlrev_b32 v214, 16, v221\n"
    "v_and_b32 v215, 0xffff0000, v221\n"
    "v_lshlrev_b32 v216, 16, v222\n"
    "v_and_b32 v217, 0xffff0000, v222\n"
    "v_lshlrev_b32 v218, 16, v223\n"
    "v_and_b32 v219, 0xffff0000, v223\n"
    "v_sub_f32 v204, v204, v212\n"
    "v_sub_f32 v205, v205, v213\n"
    "v_sub_f32 v206, v206, v214\n"
    "v_sub_f32 v207, v207, v215\n"
    "v_sub_f32 v208, v208, v216\n"
    "v_sub_f32 v209, v209, v217\n"
    "s_add_u32 m0, s65, 76800\n"
    "s_add_u32 s72, s85, 9216\n"
    "buffer_load_dwordx4 v88, s[20:23], s72 offen offset:0 nt lds\n"
    "v_sub_f32 v210, v210, v218\n"
    "v_sub_f32 v211, v211, v219\n"
    "v_cvt_pk_bf16_f32 v224, v204, v205\n"
    "v_cvt_pk_bf16_f32 v225, v206, v207\n"
    "v_cvt_pk_bf16_f32 v226, v208, v209\n"
    "v_cvt_pk_bf16_f32 v227, v210, v211\n"
    "v_mov_b32 v204, 0\n"
    "v_mov_b32 v205, 0\n"
    "v_mov_b32 v206, 0\n"
    "v_mov_b32 v207, 0\n"
    "v_swap_b32 v221, v222\n"
    "v_swap_b32 v225, v226\n"
    "v_swap_b32 v141, v142\n"
    "ds_write2_b64 v144, v[220:221], v[222:223] offset0:32 offset1:40\n"
    "ds_write2_b64 v144, v[224:225], v[226:227] offset0:48 offset1:56\n"
    "ds_write2_b64 v144, v[140:141], v[142:143] offset0:64 offset1:72\n"
    "ds_write2_b64 v144, v[204:205], v[206:207] offset0:80 offset1:88\n"
    "s_waitcnt vmcnt(31)\n"
    "v_fma_f32 v4, v4, v180, -v184\n"
    "v_fma_f32 v5, v5, v181, -v185\n"
    "v_fma_f32 v6, v6, v182, -v186\n"
    "s_add_u32 m0, s65, 77824\n"
    "s_add_u32 s72, s85, 10240\n"
    "buffer_load_dwordx4 v89, s[20:23], s72 offen offset:0 nt lds\n"
    "v_fma_f32 v7, v7, v183, -v187\n"
    "v_fma_f32 v8, v8, v180, -v184\n"
    "v_fma_f32 v9, v9, v181, -v185\n"
    "v_fma_f32 v10, v10, v182, -v186\n"
    "v_fma_f32 v11, v11, v183, -v187\n"
    "v_cvt_pk_bf16_f32 v4, v4, v5\n"
    "v_cvt_pk_bf16_f32 v5, v6, v7\n"
    "v_cvt_pk_bf16_f32 v6, v8, v9\n"
    "v_cvt_pk_bf16_f32 v7, v10, v11\n"
    "ds_write_b64 v163, v[4:5] offset:0\n"
    "ds_write_b64 v163, v[6:7] offset:1024\n"
    "v_fma_f32 v12, v12, v188, -v192\n"
    "v_fma_f32 v13, v13, v189, -v193\n"
    "v_fma_f32 v14, v14, v190, -v194\n"
    "v_fma_f32 v15, v15, v191, -v195\n"
    "v_fma_f32 v16, v16, v188, -v192\n"
    "v_fma_f32 v17, v17, v189, -v193\n"
    "v_fma_f32 v18, v18, v190, -v194\n"
    "v_fma_f32 v19, v19, v191, -v195\n"
    "v_cvt_pk_bf16_f32 v12, v12, v13\n"
    "s_add_u32 m0, s65, 78848\n"
    "s_add_u32 s72, s85, 11264\n"
    "buffer_load_dwordx4 v90, s[20:23], s72 offen offset:0 nt lds\n"
    "v_cvt_pk_bf16_f32 v13, v14, v15\n"
    "v_cvt_pk_bf16_f32 v14, v16, v17\n"
    "v_cvt_pk_bf16_f32 v15, v18, v19\n"
    "ds_write_b64 v163, v[12:13] offset:2048\n"
    "ds_write_b64 v163, v[14:15] offset:3072\n"
    "v_fma_f32 v20, v20, v196, -v200\n"
    "v_fma_f32 v21, v21, v197, -v201\n"
    "v_fma_f32 v22, v22, v198, -v202\n"
    "v_fma_f32 v23, v23, v199, -v203\n"
    "v_fma_f32 v24, v24, v196, -v200\n"
    "v_fma_f32 v25, v25, v197, -v201\n"
    "v_fma_f32 v26, v26, v198, -v202\n"
    "v_fma_f32 v27, v27, v199, -v203\n"
    "v_cvt_pk_bf16_f32 v20, v20, v21\n"
    "v_cvt_pk_bf16_f32 v21, v22, v23\n"
    "v_cvt_pk_bf16_f32 v22, v24, v25\n"
    "v_cvt_pk_bf16_f32 v23, v26, v27\n"
    "ds_write_b64 v163, v[20:21] offset:4096\n"
    "ds_write_b64 v163, v[22:23] offset:5120\n"
    "s_waitcnt vmcnt(16)\n"
    "v_fma_f32 v100, v100, v180, -v184\n"
    "s_add_u32 m0, s65, 49152\n"
    "s_add_u32 s72, s85, 12288\n"
    "buffer_load_dwordx4 v88, s[20:23], s72 offen offset:0 nt lds\n"
    "v_fma_f32 v101, v101, v181, -v185\n"
    "v_fma_f32 v102, v102, v182, -v186\n"
    "v_fma_f32 v103, v103, v183, -v187\n"
    "v_fma_f32 v104, v104, v180, -v184\n"
    "v_fma_f32 v105, v105, v181, -v185\n"
    "v_fma_f32 v106, v106, v182, -v186\n"
    "v_fma_f32 v107, v107, v183, -v187\n"
    "v_cvt_pk_bf16_f32 v100, v100, v101\n"
    "v_cvt_pk_bf16_f32 v101, v102, v103\n"
    "v_cvt_pk_bf16_f32 v102, v104, v105\n"
    "v_cvt_pk_bf16_f32 v103, v106, v107\n"
    "ds_write_b64 v163, v[100:101] offset:12288\n"
    "ds_write_b64 v163, v[102:103] offset:13312\n"
    "v_fma_f32 v108, v108, v188, -v192\n"
    "v_fma_f32 v109, v109, v189, -v193\n"
    "v_fma_f32 v110, v110, v190, -v194\n"
    "v_fma_f32 v111, v111, v191, -v195\n"
    "v_fma_f32 v112, v112, v188, -v192\n"
    "v_fma_f32 v113, v113, v189, -v193\n"
    "v_fma_f32 v114, v114, v190, -v194\n"
    "s_add_u32 m0, s65, 50176\n"
    "s_add_u32 s72, s85, 13312\n"
    "buffer_load_dwordx4 v89, s[20:23], s72 offen offset:0 nt lds\n"
    "v_fma_f32 v115, v115, v191, -v195\n"
    "v_cvt_pk_bf16_f32 v108, v108, v109\n"
    "v_cvt_pk_bf16_f32 v109, v110, v111\n"
    "v_cvt_pk_bf16_f32 v110, v112, v113\n"
    "v_cvt_pk_bf16_f32 v111, v114, v115\n"
    "ds_write_b64 v163, v[108:109] offset:14336\n"
    "ds_write_b64 v163, v[110:111] offset:15360\n"
    "v_fma_f32 v116, v116, v196, -v200\n"
    "v_fma_f32 v117, v117, v197, -v201\n"
    "v_fma_f32 v118, v118, v198, -v202\n"
    "v_fma_f32 v119, v119, v199, -v203\n"
    "v_fma_f32 v120, v120, v196, -v200\n"
    "v_fma_f32 v121, v121, v197, -v201\n"
    "v_fma_f32 v122, v122, v198, -v202\n"
    "v_fma_f32 v123, v123, v199, -v203\n"
    "v_cvt_pk_bf16_f32 v116, v116, v117\n"
    "v_cvt_pk_bf16_f32 v117, v118, v119\n"
    "v_cvt_pk_bf16_f32 v118, v120, v121\n"
    "v_cvt_pk_bf16_f32 v119, v122, v123\n"
    "ds_write_b64 v163, v[116:117] offset:16384\n"
    "ds_write_b64 v163, v[118:119] offset:17408\n"
    "s_waitcnt vmcnt(19)\n"
    "ds_read_b128 v[36:39], v154 offset:0\n"
    "ds_read_b128 v[40:43], v155 offset:0\n"
    "ds_read_b128 v[44:47], v154 offset:128\n"
    "ds_read_b128 v[48:51], v155 offset:128\n"
    "ds_read_b128 v[52:55], v154 offset:256\n"
    "ds_read_b128 v[56:59], v155 offset:256\n"
    "s_waitcnt lgkmcnt(0)\n"
    "s_barrier\n"
    "ds_read_b128 a[112:115], v252 offset:0\n"
    "ds_read_b128 a[116:119], v252 offset:2048\n"
    "ds_read_b128 a[120:123], v252 offset:4096\n"
    "ds_read_b128 a[124:127], v252 offset:6144\n"
    "ds_read_b128 a[128:131], v252 offset:8192\n"
    "ds_read_b128 a[132:135], v252 offset:10240\n"
    "ds_read_b128 a[136:139], v252 offset:1024\n"
    "ds_read_b128 a[140:143], v252 offset:3072\n"
    "ds_read_b128 a[144:147], v252 offset:5120\n"
    "ds_read_b128 a[148:151], v252 offset:7168\n"
    "ds_read_b128 a[152:155], v252 offset:9216\n"
    "ds_read_b128 a[156:159], v252 offset:11264\n"
    "ds_read_b128 a[0:3], v252 offset:12288\n"
    "ds_read_b128 a[4:7], v252 offset:14336\n"
    "ds_read_b128 a[8:11], v252 offset:16384\n"
    "ds_read_b128 a[12:15], v252 offset:18432\n"
    "ds_read_b128 a[16:19], v252 offset:20480\n"
    "ds_read_b128 a[20:23], v252 offset:22528\n"
    "ds_read_b128 a[24:27], v252 offset:13312\n"
    "ds_read_b128 a[28:31], v252 offset:15360\n"
    "ds_read_b128 a[32:35], v252 offset:17408\n"
    "ds_read_b128 a[36:39], v252 offset:19456\n"
    "ds_read_b128 a[40:43], v252 offset:21504\n"
    "ds_read_b128 a[44:47], v252 offset:23552\n"
    "s_add_u32 m0, s65, 51200\n"
    "s_add_u32 s72, s85, 14336\n"
    "buffer_load_dwordx4 v90, s[20:23], s72 offen offset:0 nt lds\n"
    "s_add_u32 m0, s65, 52224\n"
    "s_add_u32 s72, s85, 15360\n"
    "buffer_load_dwordx4 v88, s[20:23], s72 offen offset:0 nt lds\n"
    "v_pk_maximum3_f16 v98, v36, v37, v98\n"
    "v_pk_maximum3_f16 v98, v38, v39, v98\n"
    "v_pk_add_u16 v36, s89, v36 clamp\n"
    "v_pk_add_u16 v37, s89, v37 clamp\n"
    "v_pk_add_u16 v38, s89, v38 clamp\n"
    "v_pk_add_u16 v39, s89, v39 clamp\n"
    "v_pk_maximum3_f16 v98, v40, v41, v98\n"
    "v_pk_maximum3_f16 v98, v42, v43, v98\n"
    "v_pk_add_u16 v40, s89, v40 clamp\n"
    "v_pk_add_u16 v41, s89, v41 clamp\n"
    "v_pk_add_u16 v42, s89, v42 clamp\n"
    "v_pk_add_u16 v43, s89, v43 clamp\n"
    "v_pk_maximum3_f16 v98, v44, v45, v98\n"
    "v_pk_maximum3_f16 v98, v46, v47, v98\n"
    "v_pk_add_u16 v44, s89, v44 clamp\n"
    "v_pk_add_u16 v45, s89, v45 clamp\n"
    "v_pk_add_u16 v46, s89, v46 clamp\n"
    "v_pk_add_u16 v47, s89, v47 clamp\n"
    "v_pk_maximum3_f16 v98, v48, v49, v98\n"
    "v_pk_maximum3_f16 v98, v50, v51, v98\n"
    "v_pk_add_u16 v48, s89, v48 clamp\n"
    "v_pk_add_u16 v49, s89, v49 clamp\n"
    "v_pk_add_u16 v50, s89, v50 clamp\n"
    "v_pk_add_u16 v51, s89, v51 clamp\n"
    "v_pk_maximum3_f16 v98, v52, v53, v98\n"
    "v_pk_maximum3_f16 v98, v54, v55, v98\n"
    "v_pk_add_u16 v52, s89, v52 clamp\n"
    "v_pk_add_u16 v53, s89, v53 clamp\n"
    "v_pk_add_u16 v54, s89, v54 clamp\n"
    "v_pk_add_u16 v55, s89, v55 clamp\n"
    "v_pk_maximum3_f16 v98, v56, v57, v98\n"
    "v_pk_maximum3_f16 v98, v58, v59, v98\n"
    "v_pk_add_u16 v56, s89, v56 clamp\n"
    "v_pk_add_u16 v57, s89, v57 clamp\n"
    "v_pk_add_u16 v58, s89, v58 clamp\n"
    "v_pk_add_u16 v59, s89, v59 clamp\n"
    "v_mov_b32 v108, 0\n"
    "v_mov_b32 v109, 0\n"
    "v_mov_b32 v110, 0\n"
    "v_mov_b32 v111, 0\n"
    "v_mov_b32 v112, 0\n"
    "v_mov_b32 v113, 0\n"
    "v_mov_b32 v114, 0\n"
    "v_mov_b32 v115, 0\n"
    "v_mov_b32 v116, 0\n"
    "v_mov_b32 v117, 0\n"
    "v_mov_b32 v118, 0\n"
    "v_mov_b32 v119, 0\n"
    "v_mov_b32 v120, 0\n"
    "v_mov_b32 v121, 0\n"
    "v_mov_b32 v122, 0\n"
    "v_mov_b32 v123, 0\n"
    "s_mul_i32 s73, s79, 768\n"
    "v_add_u32 v78, s73, v250\n"
    "v_mov_b32 v79, 155936\n"
    "s_waitcnt lgkmcnt(15)\n"
    "v_mfma_f32_16x16x32_bf16 v[4:7], a[112:115], v[36:39], 0\n"
    "s_waitcnt lgkmcnt(15)\n"
    "v_mfma_f32_16x16x32_bf16 v[4:7], a[116:119], v[40:43], v[4:7]\n"
    "s_waitcnt lgkmcnt(15)\n"
    "v_mfma_f32_16x16x32_bf16 v[4:7], a[120:123], v[44:47], v[4:7]\n"
    "s_waitcnt lgkmcnt(15)\n"
    "v_mfma_f32_16x16x32_bf16 v[4:7], a[124:127], v[48:51], v[4:7]\n"
    "s_waitcnt lgkmcnt(15)\n"
    "v_mfma_f32_16x16x32_bf16 v[4:7], a[128:131], v[52:55], v[4:7]\n"
    "s_waitcnt lgkmcnt(15)\n"
    "v_mfma_f32_16x16x32_bf16 v[4:7], a[132:135], v[56:59], v[4:7]\n"
    "s_waitcnt lgkmcnt(15)\n"
    "v_mfma_f32_16x16x32_bf16 v[20:23], a[136:139], v[36:39], 0\n"
    "v_accvgpr_write_b32 a192, v36\n"
    "v_accvgpr_write_b32 a193, v37\n"
    "v_accvgpr_write_b32 a194, v38\n"
    "v_accvgpr_write_b32 a195, v39\n"
    "s_waitcnt lgkmcnt(15)\n"
    "v_mfma_f32_16x16x32_bf16 v[20:23], a[140:143], v[40:43], v[20:23]\n"
    "v_accvgpr_write_b32 a196, v40\n"
    "v_accvgpr_write_b32 a197, v41\n"
    "v_accvgpr_write_b32 a198, v42\n"
    "v_accvgpr_write_b32 a199, v43\n"
    "s_waitcnt lgkmcnt(15)\n"
    "v_mfma_f32_16x16x32_bf16 v[20:23], a[144:147], v[44:47], v[20:23]\n"
    "v_accvgpr_write_b32 a200, v44\n"
    "v_accvgpr_write_b32 a201, v45\n"
    "v_accvgpr_write_b32 a202, v46\n"
    "v_accvgpr_write_b32 a203, v47\n"
    "s_waitcnt lgkmcnt(14)\n"
    "v_mfma_f32_16x16x32_bf16 v[20:23], a[148:151], v[48:51], v[20:23]\n"
    "v_accvgpr_write_b32 a204, v48\n"
    "v_accvgpr_write_b32 a205, v49\n"
    "v_accvgpr_write_b32 a206, v50\n"
    "v_accvgpr_write_b32 a207, v51\n"
    "s_waitcnt lgkmcnt(13)\n"
    "v_mfma_f32_16x16x32_bf16 v[20:23], a[152:155], v[52:55], v[20:23]\n"
    "v_accvgpr_write_b32 a208, v52\n"
    "v_accvgpr_write_b32 a209, v53\n"
    "v_accvgpr_write_b32 a210, v54\n"
    "v_accvgpr_write_b32 a211, v55\n"
    "s_waitcnt lgkmcnt(12)\n"
    "v_mfma_f32_16x16x32_bf16 v[20:23], a[156:159], v[56:59], v[20:23]\n"
    "v_accvgpr_write_b32 a212, v56\n"
    "v_accvgpr_write_b32 a213, v57\n"
    "v_accvgpr_write_b32 a214, v58\n"
    "v_accvgpr_write_b32 a215, v59\n"
    "s_add_u32 m0, s65, 53248\n"
    "s_add_u32 s72, s85, 16384\n"
    "buffer_load_dwordx4 v89, s[20:23], s72 offen offset:0 nt lds\n"
    "buffer_load_dwordx2 v[236:237], v87, s[36:39], 0 offen offset:0 sc1\n"
    "ds_read_b128 v[16:19], v78 offset:0\n"
    "ds_read_b128 v[32:35], v78 offset:16\n"
    "ds_read_b128 v[48:51], v78 offset:128\n"
    "ds_read_b128 v[64:67], v78 offset:144\n"
    "ds_read_b128 v[100:103], v78 offset:768\n"
    "ds_read_b128 v[104:107], v78 offset:784\n"
    "s_add_u32 m0, s65, 54272\n"
    "s_add_u32 s72, s85, 17408\n"
    "buffer_load_dwordx4 v90, s[20:23], s72 offen offset:0 nt lds\n"
    "s_add_u32 m0, s65, 98304\n"
    "s_add_u32 s72, s85, 18432\n"
    "buffer_load_dwordx4 v88, s[20:23], s72 offen offset:0 nt lds\n"
    "s_waitcnt lgkmcnt(0)\n"
    "s_barrier\n"
    "ds_read_b128 v[80:83], v79 offset:0\n"
    "s_waitcnt vmcnt(14)\n"
    "buffer_store_dwordx2 v[238:239], v240, s[36:39], s80 offen offset:0 sc1\n"
    "s_lshl_b32 s73, s79, 8\n"
    "s_add_u32 s80, s80, s73\n"
    "s_mul_i32 s73, s79, 98304\n"
    "s_add_u32 s71, s71, s73\n"
    "s_add_u32 m0, s65, 99328\n"
    "s_add_u32 s72, s85, 19456\n"
    "buffer_load_dwordx4 v89, s[20:23], s72 offen offset:0 nt lds\n"
    "s_waitcnt lgkmcnt(0)\n"
    "v_max3_f32 v76, v80, v81, v82\n"
    "v_max_f32 v76, v76, v83\n"
    "v_add_f32 v76, 0xc2b00000, v76\n"
    "s_add_u32 m0, s65, 100352\n"
    "s_add_u32 s72, s85, 20480\n"
    "buffer_load_dwordx4 v90, s[20:23], s72 offen offset:0 nt lds\n"
    "s_add_u32 m0, s65, 101376\n"
    "s_add_u32 s72, s85, 21504\n"
    "buffer_load_dwordx4 v88, s[20:23], s72 offen offset:0 nt lds\n"
    "s_add_u32 m0, s65, 102400\n"
    "s_add_u32 s72, s85, 22528\n"
    "buffer_load_dwordx4 v89, s[20:23], s72 offen offset:0 nt lds\n"
    "s_add_u32 m0, s65, 103424\n"
    "s_add_u32 s72, s85, 23552\n"
    "buffer_load_dwordx4 v90, s[20:23], s72 offen offset:0 nt lds\n"
    "v_sub_f32 v48, v48, v76\n"
    "v_sub_f32 v51, v51, v76\n"
    "v_sub_f32 v49, v49, v76\n"
    "v_sub_f32 v50, v50, v76\n"
    "v_sub_f32 v66, v66, v76\n"
    "v_sub_f32 v65, v65, v76\n"
    "v_mfma_f32_16x16x32_bf16 v[36:39], a[0:3], a[192:195], v[48:51]\n"
    "s_waitcnt vmcnt(14)\n"
    "ds_read_b128 v[200:203], v97 offset:24832\n"
    "v_sub_f32 v67, v67, v76\n"
    "v_mfma_f32_16x16x32_bf16 v[36:39], a[4:7], a[196:199], v[36:39]\n"
    "v_sub_f32 v33, v33, v76\n"
    "v_sub_f32 v32, v32, v76\n"
    "v_mfma_f32_16x16x32_bf16 v[36:39], a[8:11], a[200:203], v[36:39]\n"
    "ds_read_b128 v[184:187], v97 offset:24576\n"
    "ds_read_b128 v[196:199], v96 offset:24832\n"
    "ds_read_b128 v[192:195], v97 offset:24704\n"
    "v_mfma_f32_16x16x32_bf16 v[36:39], a[12:15], a[204:207], v[36:39]\n"
    "v_sub_f32 v64, v64, v76\n"
    "v_sub_f32 v35, v35, v76\n"
    "v_sub_f32 v19, v19, v76\n"
    "v_mfma_f32_16x16x32_bf16 v[52:55], a[24:27], a[192:195], v[64:67]\n"
    "v_add_f32 v21, v21, v33\n"
    "v_sub_f32 v16, v16, v76\n"
    "v_mfma_f32_16x16x32_bf16 v[36:39], a[16:19], a[208:211], v[36:39]\n"
    "v_sub_f32 v18, v18, v76\n"
    "v_add_f32 v20, v20, v32\n"
    "v_sub_f32 v17, v17, v76\n"
    "v_mfma_f32_16x16x32_bf16 v[52:55], a[28:31], a[196:199], v[52:55]\n"
    "ds_read_b128 v[188:191], v96 offset:24704\n"
    "ds_read_b128 v[180:183], v96 offset:24576\n"
    "v_add_f32 v6, v6, v18\n"
    "v_sub_f32 v34, v34, v76\n"
    "v_mfma_f32_16x16x32_bf16 v[36:39], a[20:23], a[212:215], v[36:39]\n"
    "s_waitcnt lgkmcnt(5)\n"
    "v_pk_maximum3_f16 v98, v200, v201, v98\n"
    "v_exp_f32 v20, v20\n"
    "v_pk_maximum3_f16 v98, v202, v203, v98\n"
    "v_pk_add_u16 v203, s89, v203 clamp\n"
    "v_pk_add_u16 v200, s89, v200 clamp\n"
    "v_mfma_f32_16x16x32_bf16 v[52:55], a[32:35], a[200:203], v[52:55]\n"
    "s_waitcnt lgkmcnt(3)\n"
    "v_pk_maximum3_f16 v98, v196, v197, v98\n"
    "v_pk_maximum3_f16 v98, v198, v199, v98\n"
    "v_mfma_f32_16x16x32_bf16 v[52:55], a[36:39], a[204:207], v[52:55]\n"
    "v_exp_f32 v21, v21\n"
    "v_pk_add_u16 v202, s89, v202 clamp\n"
    "v_pk_add_u16 v201, s89, v201 clamp\n"
    "v_mfma_f32_16x16x32_bf16 v[52:55], a[40:43], a[208:211], v[52:55]\n"
    "v_add_f32 v22, v22, v34\n"
    "v_add_f32 v7, v7, v19\n"
    "v_mfma_f32_16x16x32_bf16 v[24:27], a[156:159], v[200:203], v[32:35]\n"
    "s_waitcnt lgkmcnt(2)\n"
    "v_pk_maximum3_f16 v98, v192, v193, v98\n"
    "v_pk_maximum3_f16 v98, v194, v195, v98\n"
    "v_pk_add_u16 v193, s89, v193 clamp\n"
    "v_pk_add_u16 v198, s89, v198 clamp\n"
    "v_add_f32 v5, v5, v17\n"
    "v_mfma_f32_16x16x32_bf16 v[52:55], a[44:47], a[212:215], v[52:55]\n"
    "s_waitcnt lgkmcnt(1)\n"
    "v_pk_add_u16 v194, s89, v194 clamp\n"
    "v_sub_f32 v69, v69, v76\n"
    "v_sub_f32 v73, v73, v76\n"
    "v_pk_maximum3_f16 v98, v188, v189, v98\n"
    "v_pk_maximum3_f16 v98, v190, v191, v98\n"
    "v_pk_add_u16 v196, s89, v196 clamp\n"
    "v_pk_maximum3_f16 v98, v184, v185, v98\n"
    "v_pk_add_u16 v197, s89, v197 clamp\n"
    "v_pk_maximum3_f16 v98, v186, v187, v98\n"
    "v_mfma_f32_16x16x32_bf16 v[8:11], a[132:135], v[200:203], v[16:19]\n"
    "v_sub_f32 v75, v75, v76\n"
    "v_pk_add_u16 v199, s89, v199 clamp\n"
    "v_sub_f32 v101, v101, v76\n"
    "v_exp_f32 v54, v54\n"
    "v_mfma_f32_16x16x32_bf16 v[24:27], a[152:155], v[196:199], v[24:27]\n"
    "s_waitcnt lgkmcnt(0)\n"
    "v_pk_add_u16 v188, s89, v188 clamp\n"
    "v_pk_add_u16 v184, s89, v184 clamp\n"
    "v_sub_f32 v70, v70, v76\n"
    "v_pk_maximum3_f16 v98, v180, v181, v98\n"
    "v_pk_add_u16 v186, s89, v186 clamp\n"
    "v_mfma_f32_16x16x32_bf16 v[56:59], a[44:47], v[200:203], v[64:67]\n"
    "v_pk_add_u16 v189, s89, v189 clamp\n"
    "v_pk_add_u16 v195, s89, v195 clamp\n"
    "v_pk_add_u16 v192, s89, v192 clamp\n"
    "v_pk_add_u16 v185, s89, v185 clamp\n"
    "v_pk_maximum3_f16 v98, v182, v183, v98\n"
    "v_exp_f32 v55, v55\n"
    "v_sub_f32 v68, v68, v76\n"
    "v_mfma_f32_16x16x32_bf16 v[56:59], a[40:43], v[196:199], v[56:59]\n"
    "v_exp_f32 v52, v52\n"
    "v_pk_add_u16 v190, s89, v190 clamp\n"
    "v_mfma_f32_16x16x32_bf16 v[8:11], a[128:131], v[196:199], v[8:11]\n"
    "v_accvgpr_write_b32 a231, v195\n"
    "v_sub_f32 v102, v102, v76\n"
    "v_sub_f32 v71, v71, v76\n"
    "v_sub_f32 v107, v107, v76\n"
    "v_mfma_f32_16x16x32_bf16 v[40:43], a[20:23], v[200:203], v[48:51]\n"
    "v_sub_f32 v74, v74, v76\n"
    "v_add_f32 v23, v23, v35\n"
    "v_mfma_f32_16x16x32_bf16 v[40:43], a[16:19], v[196:199], v[40:43]\n"
    "v_pk_add_u16 v183, s89, v183 clamp\n"
    "v_accvgpr_write_b32 a228, v192\n"
    "v_mfma_f32_16x16x32_bf16 v[56:59], a[36:39], v[192:195], v[56:59]\n"
    "v_add_f32 v4, v4, v16\n"
    "v_exp_f32 v6, v6\n"
    "v_mfma_f32_16x16x32_bf16 v[24:27], a[148:151], v[192:195], v[24:27]\n"
    "v_sub_f32 v72, v72, v76\n"
    "v_pk_add_u16 v187, s89, v187 clamp\n"
    "v_pk_add_u16 v191, s89, v191 clamp\n"
    "v_mfma_f32_16x16x32_bf16 v[8:11], a[124:127], v[192:195], v[8:11]\n"
    "v_exp_f32 v53, v53\n"
    "v_accvgpr_write_b32 a226, v190\n"
    "v_mfma_f32_16x16x32_bf16 v[8:11], a[120:123], v[188:191], v[8:11]\n"
    "v_accvgpr_write_b32 a221, v185\n"
    "v_exp_f32 v39, v39\n"
    "v_exp_f32 v22, v22\n"
    "v_pk_add_u16 v181, s89, v181 clamp\n"
    "v_mfma_f32_16x16x32_bf16 v[56:59], a[32:35], v[188:191], v[56:59]\n"
    "v_exp_f32 v36, v36\n"
    "v_pk_add_u16 v182, s89, v182 clamp\n"
    "v_exp_f32 v23, v23\n"
    "v_mfma_f32_16x16x32_bf16 v[24:27], a[144:147], v[188:191], v[24:27]\n"
    "v_accvgpr_write_b32 a237, v201\n"
    "v_pk_add_u16 v180, s89, v180 clamp\n"
    "v_mfma_f32_16x16x32_bf16 v[40:43], a[12:15], v[192:195], v[40:43]\n"
    "v_accvgpr_write_b32 a239, v203\n"
    "v_accvgpr_write_b32 a223, v187\n"
    "v_exp_f32 v7, v7\n"
    "v_accvgpr_write_b32 a233, v197\n"
    "v_mfma_f32_16x16x32_bf16 v[8:11], a[116:119], v[184:187], v[8:11]\n"
    "v_accvgpr_write_b32 a232, v196\n"
    "v_accvgpr_write_b32 a235, v199\n"
    "v_mfma_f32_16x16x32_bf16 v[40:43], a[8:11], v[188:191], v[40:43]\n"
    "ds_write_b128 v86, v[68:71] offset:0\n"
    "v_mfma_f32_16x16x32_bf16 v[40:43], a[4:7], v[184:187], v[40:43]\n"
    "v_sub_f32 v106, v106, v76\n"
    "v_accvgpr_write_b32 a234, v198\n"
    "v_mfma_f32_16x16x32_bf16 v[8:11], a[112:115], v[180:183], v[8:11]\n"
    "v_cvt_pk_bf16_f32 v70, v20, v21\n"
    "v_exp_f32 v37, v37\n"
    "v_cvt_pk_bf16_f32 v69, v6, v7\n"
    "v_accvgpr_write_b32 a218, v182\n"
    "v_mfma_f32_16x16x32_bf16 v[40:43], a[0:3], v[180:183], v[40:43]\n"
    "ds_write_b128 v86, v[72:75] offset:16\n"
    "v_accvgpr_write_b32 a230, v194\n"
    "v_mfma_f32_16x16x32_bf16 v[24:27], a[140:143], v[184:187], v[24:27]\n"
    "v_exp_f32 v10, v10\n"
    "v_mfma_f32_16x16x32_bf16 v[56:59], a[28:31], v[184:187], v[56:59]\n"
    "v_cvt_pk_bf16_f32 v72, v36, v37\n"
    "v_sub_f32 v103, v103, v76\n"
    "v_mfma_f32_16x16x32_bf16 v[56:59], a[24:27], v[180:183], v[56:59]\n"
    "v_sub_f32 v104, v104, v76\n"
    "v_accvgpr_write_b32 a217, v181\n"
    "v_exp_f32 v4, v4\n"
    "v_sub_f32 v100, v100, v76\n"
    "v_accvgpr_write_b32 a227, v191\n"
    "v_exp_f32 v11, v11\n"
    "v_exp_f32 v5, v5\n"
    "v_exp_f32 v38, v38\n"
    "v_exp_f32 v41, v41\n"
    "v_exp_f32 v9, v9\n"
    "v_exp_f32 v59, v59\n"
    "v_accvgpr_write_b32 a220, v184\n"
    "v_accvgpr_write_b32 a238, v202\n"
    "v_accvgpr_write_b32 a236, v200\n"
    "v_exp_f32 v43, v43\n"
    "v_cvt_pk_bf16_f32 v77, v10, v11\n"
    "v_exp_f32 v8, v8\n"
    "v_cvt_pk_bf16_f32 v75, v54, v55\n"
    "v_mfma_f32_16x16x32_bf16 v[24:27], a[136:139], v[180:183], v[24:27]\n"
    "v_cvt_pk_bf16_f32 v71, v22, v23\n"
    "v_sub_f32 v105, v105, v76\n"
    "v_accvgpr_write_b32 a216, v180\n"
    "v_exp_f32 v58, v58\n"
    "v_cvt_pk_bf16_f32 v73, v38, v39\n"
    "v_exp_f32 v40, v40\n"
    "v_accvgpr_write_b32 a222, v186\n"
    "v_accvgpr_write_b32 a225, v189\n"
    "v_exp_f32 v24, v24\n"
    "v_exp_f32 v57, v57\n"
    "v_cvt_pk_bf16_f32 v68, v4, v5\n"
    "v_cvt_pk_bf16_f32 v80, v40, v41\n"
    "v_exp_f32 v27, v27\n"
    "v_exp_f32 v25, v25\n"
    "v_cvt_pk_bf16_f32 v76, v8, v9\n"
    "v_accvgpr_write_b32 a224, v188\n"
    "v_accvgpr_write_b32 a219, v183\n"
    "v_exp_f32 v26, v26\n"
    "v_exp_f32 v42, v42\n"
    "v_cvt_pk_bf16_f32 v78, v24, v25\n"
    "v_exp_f32 v56, v56\n"
    "v_cvt_pk_bf16_f32 v74, v52, v53\n"
    "v_accvgpr_write_b32 a229, v193\n"
    "v_cvt_pk_bf16_f32 v79, v26, v27\n"
    "v_cvt_pk_bf16_f32 v82, v56, v57\n"
    "v_cvt_pk_bf16_f32 v83, v58, v59\n"
    "v_cvt_pk_bf16_f32 v81, v42, v43\n"
    "s_waitcnt vmcnt(8)\n"
    "v_cmp_eq_u64 s[54:55], v[236:237], v[238:239]\n"
    "s_nop 0\n"
    "s_cmp_eq_u64 s[54:55], -1\n"
    "s_cbranch_scc0 .LpollAK\n"
    ".LpollAKr:\n"
    "s_add_u32 m0, s65, 24576\n"
    "s_add_u32 s72, s65, 49152\n"
    "buffer_load_dwordx4 v1, s[16:19], s72 offen offset:0 sc1 lds\n"
    "s_add_u32 m0, s65, 25600\n"
    "s_add_u32 s72, s65, 50176\n"
    "buffer_load_dwordx4 v1, s[16:19], s72 offen offset:0 sc1 lds\n"
    "s_add_u32 m0, s65, 26624\n"
    "s_add_u32 s72, s65, 51200\n"
    "buffer_load_dwordx4 v1, s[16:19], s72 offen offset:0 sc1 lds\n"
    "s_add_u32 m0, s65, 27648\n"
    "s_add_u32 s72, s65, 52224\n"
    "buffer_load_dwordx4 v1, s[16:19], s72 offen offset:0 sc1 lds\n"
    "s_add_u32 m0, s65, 28672\n"
    "s_add_u32 s72, s65, 53248\n"
    "buffer_load_dwordx4 v1, s[16:19], s72 offen offset:0 sc1 lds\n"
    "s_add_u32 m0, s65, 29696\n"
    "s_add_u32 s72, s65, 54272\n"
    "buffer_load_dwordx4 v1, s[16:19], s72 offen offset:0 sc1 lds\n"
    "s_waitcnt vmcnt(13)\n"
    "ds_read_b128 v[156:159], v96 offset:0\n"
    "ds_read_b128 v[172:175], v96 offset:256\n"
    "ds_read_b128 v[168:171], v97 offset:128\n"
    "ds_read_b128 v[176:179], v97 offset:256\n"
    "ds_read_b128 v[160:163], v97 offset:0\n"
    "ds_read_b128 v[164:167], v96 offset:128\n"
    "s_waitcnt lgkmcnt(5)\n"
    "v_pk_maximum3_f16 v98, v156, v157, v98\n"
    "v_pk_maximum3_f16 v98, v158, v159, v98\n"
    "v_pk_add_u16 v157, s89, v157 clamp\n"
    "v_pk_add_u16 v158, s89, v158 clamp\n"
    "v_pk_add_u16 v156, s89, v156 clamp\n"
    "v_pk_add_u16 v159, s89, v159 clamp\n"
    "s_waitcnt lgkmcnt(1)\n"
    "v_pk_maximum3_f16 v98, v160, v161, v98\n"
    "s_add_u32 m0, s65, 73728\n"
    "s_add_u32 s72, s65, 73728\n"
    "buffer_load_dwordx4 v1, s[16:19], s72 offen offset:0 sc1 lds\n"
    "v_mfma_f32_16x16x32_bf16 v[28:31], a[136:139], v[156:159], v[32:35]\n"
    "s_waitcnt lgkmcnt(0)\n"
    "v_pk_maximum3_f16 v98, v162, v163, v98\n"
    "v_pk_maximum3_f16 v98, v164, v165, v98\n"
    "v_mfma_f32_16x16x32_bf16 v[12:15], a[112:115], v[156:159], v[16:19]\n"
    "v_pk_add_u16 v161, s89, v161 clamp\n"
    "v_pk_maximum3_f16 v98, v166, v167, v98\n"
    "v_pk_add_u16 v160, s89, v160 clamp\n"
    "v_pk_add_u16 v162, s89, v162 clamp\n"
    "v_mfma_f32_16x16x32_bf16 v[60:63], a[24:27], v[156:159], v[64:67]\n"
    "v_pk_maximum3_f16 v98, v168, v169, v98\n"
    "v_pk_add_u16 v163, s89, v163 clamp\n"
    "v_mfma_f32_16x16x32_bf16 v[44:47], a[0:3], v[156:159], v[48:51]\n"
    "v_pk_maximum3_f16 v98, v170, v171, v98\n"
    "v_pk_add_u16 v164, s89, v164 clamp\n"
    "v_mfma_f32_16x16x32_bf16 v[12:15], a[116:119], v[160:163], v[12:15]\n"
    "v_pk_add_u16 v165, s89, v165 clamp\n"
    "s_add_u32 m0, s65, 74752\n"
    "s_add_u32 s72, s65, 74752\n"
    "buffer_load_dwordx4 v1, s[16:19], s72 offen offset:0 sc1 lds\n"
    "v_pk_add_u16 v169, s89, v169 clamp\n"
    "v_pk_add_u16 v168, s89, v168 clamp\n"
    "v_mfma_f32_16x16x32_bf16 v[60:63], a[28:31], v[160:163], v[60:63]\n"
    "v_pk_maximum3_f16 v98, v172, v173, v98\n"
    "v_pk_add_u16 v170, s89, v170 clamp\n"
    "v_mfma_f32_16x16x32_bf16 v[28:31], a[140:143], v[160:163], v[28:31]\n"
    "v_pk_maximum3_f16 v98, v174, v175, v98\n"
    "v_pk_add_u16 v166, s89, v166 clamp\n"
    "v_pk_add_u16 v167, s89, v167 clamp\n"
    "v_mfma_f32_16x16x32_bf16 v[44:47], a[4:7], v[160:163], v[44:47]\n"
    "v_pk_maximum3_f16 v98, v176, v177, v98\n"
    "v_accvgpr_write_b32 a251, v167\n"
    "v_mfma_f32_16x16x32_bf16 v[44:47], a[8:11], v[164:167], v[44:47]\n"
    "v_pk_maximum3_f16 v98, v178, v179, v98\n"
    "v_pk_add_u16 v176, s89, v176 clamp\n"
    "v_mfma_f32_16x16x32_bf16 v[60:63], a[32:35], v[164:167], v[60:63]\n"
    "v_pk_add_u16 v171, s89, v171 clamp\n"
    "s_add_u32 m0, s65, 75776\n"
    "s_add_u32 s72, s65, 75776\n"
    "buffer_load_dwordx4 v1, s[16:19], s72 offen offset:0 sc1 lds\n"
    "v_pk_add_u16 v174, s89, v174 clamp\n"
    "v_mfma_f32_16x16x32_bf16 v[28:31], a[144:147], v[164:167], v[28:31]\n"
    "v_pk_add_u16 v173, s89, v173 clamp\n"
    "v_pk_add_u16 v172, s89, v172 clamp\n"
    "v_mfma_f32_16x16x32_bf16 v[12:15], a[120:123], v[164:167], v[12:15]\n"
    "v_accvgpr_write_b32 a254, v170\n"
    "v_pk_add_u16 v178, s89, v178 clamp\n"
    "v_mfma_f32_16x16x32_bf16 v[28:31], a[148:151], v[168:171], v[28:31]\n"
    "v_accvgpr_write_b32 a243, v159\n"
    "v_accvgpr_write_b32 a162, v174\n"
    "v_mfma_f32_16x16x32_bf16 v[12:15], a[124:127], v[168:171], v[12:15]\n"
    "v_pk_add_u16 v179, s89, v179 clamp\n"
    "v_pk_add_u16 v175, s89, v175 clamp\n"
    "v_mfma_f32_16x16x32_bf16 v[44:47], a[12:15], v[168:171], v[44:47]\n"
    "v_accvgpr_write_b32 a166, v178\n"
    "v_accvgpr_write_b32 a161, v173\n"
    "v_accvgpr_write_b32 a245, v161\n"
    "s_add_u32 m0, s65, 76800\n"
    "s_add_u32 s72, s65, 76800\n"
    "buffer_load_dwordx4 v1, s[16:19], s72 offen offset:0 sc1 lds\n"
    "v_mfma_f32_16x16x32_bf16 v[28:31], a[152:155], v[172:175], v[28:31]\n"
    "v_accvgpr_write_b32 a163, v175\n"
    "v_pk_add_u16 v177, s89, v177 clamp\n"
    "v_mfma_f32_16x16x32_bf16 v[44:47], a[16:19], v[172:175], v[44:47]\n"
    "v_accvgpr_write_b32 a160, v172\n"
    "v_accvgpr_write_b32 a252, v168\n"
    "v_mfma_f32_16x16x32_bf16 v[28:31], a[156:159], v[176:179], v[28:31]\n"
    "v_accvgpr_write_b32 a249, v165\n"
    "v_accvgpr_write_b32 a255, v171\n"
    "v_mfma_f32_16x16x32_bf16 v[12:15], a[128:131], v[172:175], v[12:15]\n"
    "v_accvgpr_write_b32 a247, v163\n"
    "v_accvgpr_write_b32 a164, v176\n"
    "v_mfma_f32_16x16x32_bf16 v[60:63], a[36:39], v[168:171], v[60:63]\n"
    "v_accvgpr_write_b32 a242, v158\n"
    "v_accvgpr_write_b32 a250, v166\n"
    "v_mfma_f32_16x16x32_bf16 v[44:47], a[20:23], v[176:179], v[44:47]\n"
    "v_accvgpr_write_b32 a241, v157\n"
    "s_add_u32 m0, s65, 77824\n"
    "s_add_u32 s72, s65, 77824\n"
    "buffer_load_dwordx4 v1, s[16:19], s72 offen offset:0 sc1 lds\n"
    "v_accvgpr_write_b32 a248, v164\n"
    "v_mfma_f32_16x16x32_bf16 v[12:15], a[132:135], v[176:179], v[12:15]\n"
    "v_accvgpr_write_b32 a246, v162\n"
    "v_exp_f32 v29, v29\n"
    "v_mfma_f32_16x16x32_bf16 v[60:63], a[40:43], v[172:175], v[60:63]\n"
    "v_exp_f32 v28, v28\n"
    "v_mfma_f32_16x16x32_bf16 v[60:63], a[44:47], v[176:179], v[60:63]\n"
    "v_exp_f32 v31, v31\n"
    "v_exp_f32 v30, v30\n"
    "v_exp_f32 v45, v45\n"
    "v_cvt_pk_bf16_f32 v86, v28, v29\n"
    "v_exp_f32 v47, v47\n"
    "v_accvgpr_write_b32 a240, v156\n"
    "v_exp_f32 v12, v12\n"
    "v_exp_f32 v14, v14\n"
    "v_accvgpr_write_b32 a167, v179\n"
    "v_exp_f32 v15, v15\n"
    "s_add_u32 m0, s65, 78848\n"
    "s_add_u32 s72, s65, 78848\n"
    "buffer_load_dwordx4 v1, s[16:19], s72 offen offset:0 sc1 lds\n"
    "v_accvgpr_write_b32 a253, v169\n"
    "v_exp_f32 v63, v63\n"
    "v_exp_f32 v13, v13\n"
    "v_exp_f32 v44, v44\n"
    "v_cvt_pk_bf16_f32 v84, v12, v13\n"
    "v_cvt_pk_bf16_f32 v87, v30, v31\n"
    "v_exp_f32 v46, v46\n"
    "v_exp_f32 v61, v61\n"
    "v_accvgpr_write_b32 a165, v177\n"
    "v_exp_f32 v60, v60\n"
    "v_exp_f32 v62, v62\n"
    "v_cvt_pk_bf16_f32 v90, v60, v61\n"
    "v_accvgpr_write_b32 a244, v160\n"
    "v_cvt_pk_bf16_f32 v85, v14, v15\n"
    "v_cvt_pk_bf16_f32 v89, v46, v47\n"
    "v_cvt_pk_bf16_f32 v88, v44, v45\n"
    "v_cvt_pk_bf16_f32 v91, v62, v63\n"
    "s_bitcmp1_b32 s83, 1\n"
    "s_cbranch_scc1 .Lf3b\n"
    "s_mov_b32 s66, 1\n"
    "s_add_u32 s68, s68, 24576\n"
    "s_add_u32 s81, s81, 128\n"
    "s_add_u32 s80, s80, 256\n"
    "s_add_u32 s71, s71, 98304\n"
    "s_add_u32 s75, s71, s63\n"
    "s_add_u32 s76, s75, 6144\n"
    "s_add_u32 s77, s75, 24576\n"
    "s_add_u32 s78, s75, 30720\n"
    "s_branch .LhKP\n"
    "s_branch .Lf3e\n"
    ".Lf3b:\n"
    "s_add_u32 s75, s71, s63\n"
    "s_add_u32 s76, s75, 6144\n"
    "s_add_u32 s77, s75, 24576\n"
    "s_add_u32 s78, s75, 30720\n"
    "buffer_load_dwordx2 v[204:205], v242, s[28:31], s75 offen offset:0 nt\n"
    "buffer_load_dwordx2 v[206:207], v242, s[28:31], s75 offen offset:768 nt\n"
    "buffer_load_dwordx2 v[208:209], v242, s[28:31], s75 offen offset:3072 nt\n"
    "buffer_load_dwordx2 v[210:211], v242, s[28:31], s75 offen offset:3840 nt\n"
    "buffer_load_dwordx2 v[212:213], v242, s[28:31], s76 offen offset:0 nt\n"
    "buffer_load_dwordx2 v[214:215], v242, s[28:31], s76 offen offset:768 nt\n"
    "buffer_load_dwordx2 v[216:217], v242, s[28:31], s76 offen offset:3072 nt\n"
    "buffer_load_dwordx2 v[218:219], v242, s[28:31], s76 offen offset:3840 nt\n"
    "buffer_load_dwordx2 v[220:221], v242, s[28:31], s77 offen offset:0 nt\n"
    "buffer_load_dwordx2 v[222:223], v242, s[28:31], s77 offen offset:768 nt\n"
    "buffer_load_dwordx2 v[224:225], v242, s[28:31], s77 offen offset:3072 nt\n"
    "buffer_load_dwordx2 v[226:227], v242, s[28:31], s77 offen offset:3840 nt\n"
    "buffer_load_dwordx2 v[228:229], v242, s[28:31], s78 offen offset:0 nt\n"
    "buffer_load_dwordx2 v[230:231], v242, s[28:31], s78 offen offset:768 nt\n"
    "buffer_load_dwordx2 v[232:233], v242, s[28:31], s78 offen offset:3072 nt\n"
    "buffer_load_dwordx2 v[234:235], v242, s[28:31], s78 offen offset:3840 nt\n"
    "s_mov_b32 s66, 1\n"
    "s_add_u32 s68, s68, 24576\n"
    "s_add_u32 s81, s81, 128\n"
    "s_branch .LhKN\n"
    ".Lf3e:\n"
    "s_nop 4\n"
    "s_branch .Lf5e\n"
    ".Lf5b:\n"
    "s_lshl_b32 s73, s79, 8\n"
    "s_mul_i32 s74, s59, 6144\n"
    "s_add_u32 s73, s73, s74\n"
    "v_add_u32 v163, s73, v244\n"
    "s_lshr_b32 s73, s53, 2\n"
    "s_mul_i32 s73, s73, 12288\n"
    "s_and_b32 s74, s53, 3\n"
    "s_lshl_b32 s74, s74, 8\n"
    "s_add_u32 s73, s73, s74\n"
    "s_cmp_eq_u32 s59, 0\n"
    "s_cselect_b32 s73, 0x7f000000, s73\n"
    "s_add_u32 s60, s73, 24576\n"
    "s_add_u32 s61, s73, 28672\n"
    "s_add_u32 s62, s73, 32768\n"
    "v_mov_b32 v244, v243\n"
    "v_mov_b32 v243, 0\n"
    "buffer_load_dwordx4 v[40:43], v242, s[40:43], s75 offen offset:768 nt\n"
    "v_add_u32 v159, 0, v156\n"
    "v_mul_u32_u24 v159, 2731, v159\n"
    "v_lshrrev_b32 v159, 16, v159\n"
    "v_xor_b32 v159, v159, v156\n"
    "v_and_b32 v159, 7, v159\n"
    "v_and_b32 v88, 0x380, v1\n"
    "v_lshl_add_u32 v88, v159, 4, v88\n"
    "v_add_u32 v159, 64, v156\n"
    "v_mul_u32_u24 v159, 2731, v159\n"
    "v_lshrrev_b32 v159, 16, v159\n"
    "v_xor_b32 v159, v159, v156\n"
    "v_and_b32 v159, 7, v159\n"
    "v_and_b32 v89, 0x380, v1\n"
    "v_lshl_add_u32 v89, v159, 4, v89\n"
    "v_add_u32 v159, 128, v156\n"
    "v_mul_u32_u24 v159, 2731, v159\n"
    "v_lshrrev_b32 v159, 16, v159\n"
    "v_xor_b32 v159, v159, v156\n"
    "v_and_b32 v159, 7, v159\n"
    "v_and_b32 v90, 0x380, v1\n"
    "v_lshl_add_u32 v90, v159, 4, v90\n"
    "v_and_b32 v2, 7, v156\n"
    "v_lshrrev_b32 v144, 7, v130\n"
    "v_mul_u32_u24 v144, 768, v144\n"
    "v_lshl_add_u32 v144, v2, 3, v144\n"
    "buffer_load_dwordx4 v[44:47], v242, s[40:43], s75 offen offset:256 nt\n"
    "v_add_u32 v144, 131072, v144\n"
    "v_mad_u32_u24 v86, v2, 24, v144\n"
    "v_and_b32 v96, 15, v156\n"
    "v_lshrrev_b32 v159, 4, v156\n"
    "v_xor_b32 v159, v159, v2\n"
    "v_mul_u32_u24 v96, 384, v96\n"
    "v_lshl_add_u32 v96, v159, 4, v96\n"
    "s_add_u32 s73, s65, 49152\n"
    "v_add_u32 v96, s73, v96\n"
    "v_xor_b32 v97, 64, v96\n"
    "v_subrev_u32 v154, 24576, v96\n"
    "v_subrev_u32 v155, 24576, v97\n"
    "s_add_u32 s67, s65, 98304\n"
    "s_add_u32 s68, s65, 24576\n"
    "s_add_u32 s69, s65, 73728\n"
    "s_mov_b32 s81, 256\n"
    "s_mul_i32 s74, s79, 24576\n"
    "s_add_u32 s70, s74, s65\n"
    "s_add_u32 s70, s70, 73728\n"
    "s_sub_u32 s64, s65, s74\n"
    "s_add_u32 s64, s64, 98304\n"
    "v_and_b32 v249, 0x310, v1\n"
    "v_and_b32 v159, 12, v156\n"
    "v_lshl_add_u32 v249, v159, 3, v249\n"
    "v_and_b32 v159, 2, v156\n"
    "buffer_load_dwordx4 v[48:51], v242, s[40:43], s75 offen offset:1024 nt\n"
    "v_lshl_add_u32 v249, v159, 6, v249\n"
    "s_add_u32 s73, s74, 73728\n"
    "v_add_u32 v248, s73, v249\n"
    "s_sub_u32 s73, 98304, s74\n"
    "v_add_u32 v249, s73, v249\n"
    "s_or_b32 s73, s53, s84\n"
    "s_cmp_eq_u32 s73, 0\n"
    "s_cselect_b32 s82, 0, 0x7fff0000\n"
    "v_and_b32 v252, 12, v156\n"
    "v_bfe_u32 v159, v156, 1, 1\n"
    "v_or_b32 v252, v252, v159\n"
    "v_and_b32 v159, 1, v156\n"
    "v_lshl_or_b32 v252, v159, 1, v252\n"
    "v_lshlrev_b32 v252, 6, v252\n"
    "v_lshlrev_b32 v159, 2, v156\n"
    "v_and_b32 v159, 32, v159\n"
    "v_xor_b32 v159, v159, v156\n"
    "v_and_or_b32 v252, v159, 48, v252\n"
    "v_mov_b32 v2, v252\n"
    "v_add_u32 v3, 24576, v252\n"
    "v_lshrrev_b32 v91, 3, v156\n"
    "v_lshl_add_u32 v87, v91, 6, 64\n"
    "v_add_u32 v87, 64, v87\n"
    "v_and_b32 v241, 1, v156\n"
    "v_lshlrev_b32 v241, 3, v241\n"
    "buffer_load_dwordx4 v[52:55], v242, s[40:43], s75 offen offset:512 nt\n"
    "v_lshl_add_u32 v241, v91, 4, v241\n"
    "v_add_u32 v241, 1152, v241\n"
    "v_lshlrev_b32 v91, 24, v91\n"
    "v_lshl_add_u32 v91, v156, 3, v91\n"
    "s_lshl_b32 s73, s53, 4\n"
    "s_lshl_b32 s74, s59, 3\n"
    "s_add_u32 s73, s73, s74\n"
    "s_add_u32 s73, s73, 1152\n"
    "v_mov_b32 v240, 0x7fff0000\n"
    "v_writelane_b32 v240, s73, 0\n"
    "v_and_b32 v159, 48, v156\n"
    "s_mul_i32 s73, s79, 768\n"
    "s_sub_u32 s73, 131072, s73\n"
    "v_lshlrev_b32 v250, 1, v159\n"
    "v_add_u32 v250, s73, v250\n"
    "v_and_b32 v251, 3, v156\n"
    "v_lshl_add_u32 v251, v251, 7, v159\n"
    "s_add_u32 s73, s73, 256\n"
    "v_add_u32 v251, s73, v251\n"
    "v_and_b32 v159, 64, v1\n"
    "v_add_u32 v251, v159, v251\n"
    "v_bfe_i32 v159, v156, 2, 1\n"
    "v_and_b32 v159, 0xaaaa, v159\n"
    "v_or_b32 v253, 0x4444, v159\n"
    "s_mov_b32 s58, 0\n"
    "s_mov_b32 s59, 8\n"
    "s_waitcnt vmcnt(14)\n"
    "v_pk_min_i16 v4, v4, v10\n"
    "v_pk_min_i16 v5, v5, v11\n"
    "v_pk_min_i16 v6, v6, v12\n"
    "v_pk_min_i16 v7, v7, v13\n"
    "v_pk_min_i16 v8, v8, v14\n"
    "v_pk_min_i16 v9, v9, v15\n"
    "v_pk_min_i16 v16, v16, v18\n"
    "v_pk_min_i16 v17, v17, v19\n"
    "v_pk_min_i16 v4, v4, v7\n"
    "v_pk_min_i16 v5, v5, v8\n"
    "v_pk_min_i16 v6, v6, v9\n"
    "v_pk_min_i16 v16, v16, v17\n"
    "v_pk_min_i16 v4, v4, v5\n"
    "v_lshlrev_b32 v17, 16, v16\n"
    "v_pk_min_i16 v4, v4, v6\n"
    "v_min_i32 v16, v16, v17\n"
    "v_lshlrev_b32 v5, 16, v4\n"
    "v_mov_b32 v7, 155920\n"
    "v_min_i32 v4, v4, v5\n"
    "v_lshl_add_u32 v6, s84, 2, v7\n"
    "v_min_i32_dpp v16, v16, v16 row_shr:1\n"
    "v_min_i32_dpp v4, v4, v4 row_shr:1\n"
    "s_nop 0\n"
    "v_min_i32_dpp v16, v16, v16 row_shr:2\n"
    "v_min_i32_dpp v4, v4, v4 row_shr:2\n"
    "s_nop 0\n"
    "v_min_i32_dpp v16, v16, v16 row_shr:4\n"
    "v_min_i32_dpp v4, v4, v4 row_shr:4\n"
    "s_nop 0\n"
    "v_min_i32_dpp v16, v16, v16 row_shr:8\n"
    "v_min_i32_dpp v4, v4, v4 row_shr:8\n"
    "s_nop 0\n"
    "v_min_i32_dpp v16, v16, v16 row_bcast:15 row_mask:0xa\n"
    "v_min_i32_dpp v4, v4, v4 row_bcast:15 row_mask:0xa\n"
    "s_nop 0\n"
    "v_min_i32_dpp v16, v16, v16 row_bcast:31 row_mask:0xc\n"
    "v_min_i32_dpp v4, v4, v4 row_bcast:31 row_mask:0xc\n"
    "v_readlane_b32 s35, v16, 63\n"
    "v_readlane_b32 s73, v4, 63\n"
    "s_max_i32 s35, s35, 0\n"
    "s_and_b32 s35, s35, 0xffff0000\n"
    "v_mov_b32 v4, s73\n"
    "ds_write_b32 v6, v4\n"
    "v_mov_b32 v14, -32\n"
    "v_mov_b32 v12, -126\n"
    "v_mov_b32 v13, 0x3dd53b95\n"
    "v_mov_b32 v98, 0\n"
    "s_waitcnt lgkmcnt(0)\n"
    "s_barrier\n"
    "ds_read_b128 v[8:11], v7 offset:0\n"
    "buffer_load_dwordx4 v[56:59], v242, s[40:43], s75 offen offset:1280 nt\n"
    "buffer_load_dwordx4 v[136:139], v130, s[92:95], 0 offen offset:0\n"
    "buffer_load_dwordx4 v[140:143], v130, s[96:99], 0 offen offset:0\n"
    "s_nop 0\n"
    "s_nop 0\n"
    "s_nop 0\n"
    "s_nop 0\n"
    "s_nop 0\n"
    "s_nop 0\n"
    "s_nop 0\n"
    "s_nop 0\n"
    "s_nop 0\n"
    "s_nop 0\n"
    "s_nop 0\n"
    "s_nop 0\n"
    "s_nop 0\n"
    "s_nop 0\n"
    "s_nop 0\n"
    "s_nop 0\n"
    "s_nop 0\n"
    "s_nop 0\n"
    "s_nop 0\n"
    "s_nop 0\n"
    "s_nop 0\n"
    "s_nop 0\n"
    "s_nop 0\n"
    "s_nop 0\n"
    "s_nop 0\n"
    "s_nop 0\n"
    "s_nop 0\n"
    "s_nop 0\n"
    "s_nop 0\n"
    "s_nop 0\n"
    "s_nop 0\n"
    "s_nop 0\n"
    "s_nop 0\n"
    "s_nop 0\n"
    "s_nop 0\n"
    "s_nop 0\n"
    "s_nop 0\n"
    "s_nop 0\n"
    "s_nop 0\n"
    "s_nop 0\n"
    "s_nop 0\n"
    "s_nop 0\n"
    "s_nop 0\n"
    "s_nop 0\n"
    "s_nop 0\n"
    "s_nop 0\n"
    "s_nop 0\n"
    "s_nop 0\n"
    "s_nop 0\n"
    "s_nop 0\n"
    "s_nop 0\n"
    "s_nop 0\n"
    "s_nop 0\n"
    "s_nop 0\n"
    "s_nop 0\n"
    "s_nop 0\n"
    "s_nop 0\n"
    "s_nop 0\n"
    "s_nop 0\n"
    "s_nop 0\n"
    "s_waitcnt lgkmcnt(0)\n"
    "v_min3_i32 v4, v8, v9, v10\n"
    "v_min_i32 v4, v4, v11\n"
    "v_max_i32 v4, 0, v4\n"
    "v_and_b32 v4, 0xffff0000, v4\n"
    "v_mul_f32 v4, s35, v4\n"
    "v_mul_f32 v4, 0x3ed53b95, v4\n"
    "v_lshrrev_b32 v4, 23, v4\n"
    "v_sub_u32 v4, 2, v4\n"
    "v_med3_i32 v4, v4, v14, v12\n"
    "v_ldexp_f32 v13, v13, v4\n"
    "v_readfirstlane_b32 s89, v4\n"
    "v_readfirstlane_b32 s88, v13\n"
    "s_mul_i32 s89, s89, 0xff7fff80\n"
    "s_mov_b32 s90, 149\n"
    "s_waitcnt vmcnt(11)\n"
    "s_add_u32 s54, s71, s60\n"
    "s_add_u32 s55, s71, s61\n"
    "s_add_u32 s56, s71, s62\n"
    "v_cvt_pk_u16_u32 v204, v204, v208\n"
    "v_cvt_pk_u16_u32 v205, v205, v209\n"
    "v_cvt_pk_u16_u32 v206, v206, v210\n"
    "v_cvt_pk_u16_u32 v207, v207, v211\n"
    "v_or3_b32 v243, v204, v205, v243\n"
    "v_or3_b32 v243, v206, v207, v243\n"
    "buffer_store_dword v204, v244, s[16:19], s54 offen offset:0 sc1\n"
    "buffer_store_dword v205, v244, s[16:19], s54 offen offset:1024 sc1\n"
    "buffer_load_dwordx4 v[4:7], v162, s[28:31], s57 offen offset:0\n"
    "buffer_load_dwordx4 v[8:11], v162, s[28:31], s57 offen offset:3072\n"
    "buffer_store_dword v206, v244, s[16:19], s54 offen offset:2048 sc1\n"
    "buffer_store_dword v207, v244, s[16:19], s54 offen offset:3072 sc1\n"
    "v_cvt_pk_u16_u32 v212, v212, v216\n"
    "v_cvt_pk_u16_u32 v213, v213, v217\n"
    "v_cvt_pk_u16_u32 v214, v214, v218\n"
    "v_cvt_pk_u16_u32 v215, v215, v219\n"
    "v_or3_b32 v243, v212, v213, v243\n"
    "v_or3_b32 v243, v214, v215, v243\n"
    "buffer_store_dword v212, v244, s[16:19], s55 offen offset:0 sc1\n"
    "buffer_store_dword v213, v244, s[16:19], s55 offen offset:1024 sc1\n"
    "buffer_store_dword v214, v244, s[16:19], s55 offen offset:2048 sc1\n"
    "buffer_load_dwordx4 v[12:15], v162, s[28:31], s57 offen offset:128\n"
    "buffer_store_dword v215, v244, s[16:19], s55 offen offset:3072 sc1\n"
    "v_cvt_pk_u16_u32 v220, v220, v224\n"
    "v_cvt_pk_u16_u32 v221, v221, v225\n"
    "v_cvt_pk_u16_u32 v222, v222, v226\n"
    "v_cvt_pk_u16_u32 v223, v223, v227\n"
    "v_or3_b32 v243, v220, v221, v243\n"
    "v_or3_b32 v243, v222, v223, v243\n"
    "buffer_store_dword v220, v244, s[16:19], s56 offen offset:0 sc1\n"
    "buffer_store_dword v221, v244, s[16:19], s56 offen offset:1024 sc1\n"
    "buffer_store_dword v222, v244, s[16:19], s56 offen offset:2048 sc1\n"
    "buffer_store_dword v223, v244, s[16:19], s56 offen offset:3072 sc1\n"
    "s_add_u32 s71, s71, 98304\n"
    "s_add_u32 m0, s65, 24576\n"
    "s_add_u32 s72, s85, 0\n"
    "buffer_load_dwordx4 v88, s[20:23], s72 offen offset:0 nt lds\n"
    "buffer_load_dwordx4 v[16:19], v162, s[28:31], s57 offen offset:3200\n"
    "buffer_load_dwordx4 v[20:23], v162, s[28:31], s57 offen offset:256\n"
    "s_waitcnt vmcnt(25)\n"
    "v_add_co_u32 v238, vcc, 0x7f4a7c15, v238\n"
    "v_mov_b32 v159, 0x9e3779b9\n"
    "s_nop 0\n"
    "v_addc_co_u32 v239, vcc, v159, v239, vcc\n"
    "v_mov_b32 v246, 1\n"
    "s_waitcnt vmcnt(26)\n"
    "v_lshlrev_b32 v180, 16, v124\n"
    "v_and_b32 v181, 0xffff0000, v124\n"
    "v_lshlrev_b32 v182, 16, v125\n"
    "v_and_b32 v183, 0xffff0000, v125\n"
    "v_lshlrev_b32 v184, 16, v126\n"
    "v_and_b32 v185, 0xffff0000, v126\n"
    "v_lshlrev_b32 v186, 16, v127\n"
    "v_and_b32 v187, 0xffff0000, v127\n"
    "v_mul_f32 v180, s88, v180\n"
    "v_mul_f32 v181, s88, v181\n"
    "v_mul_f32 v182, s88, v182\n"
    "v_mul_f32 v183, s88, v183\n"
    "v_fmaak_f32 v184, v184, v180, 0x80730000\n"
    "v_fmaak_f32 v185, v185, v181, 0x80730000\n"
    "v_fmaak_f32 v186, v186, v182, 0x80730000\n"
    "v_fmaak_f32 v187, v187, v183, 0x80730000\n"
    "s_add_u32 m0, s65, 25600\n"
    "s_add_u32 s72, s85, 1024\n"
    "buffer_load_dwordx4 v89, s[20:23], s72 offen offset:0 nt lds\n"
    "buffer_load_dwordx4 v[24:27], v162, s[28:31], s57 offen offset:3328\n"
    "v_ldexp_f32 v180, v180, s90\n"
    "v_ldexp_f32 v181, v181, s90\n"
    "v_ldexp_f32 v182, v182, s90\n"
    "v_ldexp_f32 v183, v183, s90\n"
    "v_fma_f32 v180, v180, 0, v180\n"
    "v_fma_f32 v181, v181, 0, v181\n"
    "v_fma_f32 v182, v182, 0, v182\n"
    "v_fma_f32 v183, v183, 0, v183\n"
    "ds_write_b128 v131, v[180:183] offset:0\n"
    "ds_write_b128 v131, v[184:187] offset:1024\n"
    "ds_read_b128 v[180:183], v132 offset:0\n"
    "ds_read_b128 v[184:187], v132 offset:1024\n"
    "ds_read_b128 v[188:191], v132 offset:128\n"
    "ds_read_b128 v[192:195], v132 offset:1152\n"
    "ds_read_b128 v[196:199], v132 offset:256\n"
    "ds_read_b128 v[200:203], v132 offset:1280\n"
    "s_nop 0\n"
    "s_nop 0\n"
    "s_nop 0\n"
    "s_nop 0\n"
    "s_nop 0\n"
    "s_nop 0\n"
    "s_nop 0\n"
    "s_add_u32 m0, s65, 26624\n"
    "s_add_u32 s72, s85, 2048\n"
    "buffer_load_dwordx4 v90, s[20:23], s72 offen offset:0 nt lds\n"
    "buffer_load_dwordx4 v[100:103], v162, s[28:31], s66 offen offset:0\n"
    "buffer_load_dwordx4 v[104:107], v162, s[28:31], s66 offen offset:3072\n"
    "s_nop 0\n"
    "s_nop 0\n"
    "s_nop 0\n"
    "s_nop 0\n"
    "s_nop 0\n"
    "s_nop 0\n"
    "s_nop 0\n"
    "s_nop 0\n"
    "s_nop 0\n"
    "s_nop 0\n"
    "s_nop 0\n"
    "s_nop 0\n"
    "s_nop 0\n"
    "s_nop 0\n"
    "s_nop 0\n"
    "s_nop 0\n"
    "s_nop 0\n"
    "s_nop 0\n"
    "s_nop 0\n"
    "s_nop 0\n"
    "s_nop 0\n"
    "s_nop 0\n"
    "s_add_u32 m0, s65, 27648\n"
    "s_add_u32 s72, s85, 3072\n"
    "buffer_load_dwordx4 v88, s[20:23], s72 offen offset:0 nt lds\n"
    "buffer_load_dwordx4 v[108:111], v162, s[28:31], s66 offen offset:128\n"
    "s_nop 0\n"
    "s_nop 0\n"
    "s_nop 0\n"
    "s_nop 0\n"
    "s_nop 0\n"
    "s_nop 0\n"
    "s_nop 0\n"
    "s_nop 0\n"
    "s_nop 0\n"
    "s_nop 0\n"
    "s_nop 0\n"
    "s_nop 0\n"
    "s_nop 0\n"
    "s_nop 0\n"
    "s_nop 0\n"
    "s_nop 0\n"
    "s_nop 0\n"
    "s_nop 0\n"
    "s_nop 0\n"
    "s_nop 0\n"
    "s_nop 0\n"
    "s_nop 0\n"
    "s_nop 0\n"
    "s_waitcnt lgkmcnt(0)\n"
    "s_waitcnt vmcnt(10)\n"
    "ds_add_rtn_u32 v245, v247, v246\n"
    "s_lshl_b32 s73, s53, 6\n"
    "s_add_u32 s73, s73, 128\n"
    "s_waitcnt lgkmcnt(0)\n"
    "v_readfirstlane_b32 s74, v245\n"
    "s_nop 0\n"
    "s_cmp_eq_u32 s74, 3\n"
    "s_cselect_b32 s73, s73, 0x7fff0000\n"
    "buffer_store_dwordx2 v[238:239], v91, s[36:39], s73 offen offset:0 sc1\n"
    "s_waitcnt vmcnt(28)\n"
    "s_add_u32 m0, s65, 28672\n"
    "s_add_u32 s72, s85, 4096\n"
    "buffer_load_dwordx4 v89, s[20:23], s72 offen offset:0 nt lds\n"
    "buffer_load_dwordx4 v[112:115], v162, s[28:31], s66 offen offset:3200\n"
    "buffer_load_dwordx4 v[116:119], v162, s[28:31], s66 offen offset:256\n"
    "s_add_u32 s54, s71, s60\n"
    "s_add_u32 s55, s71, s61\n"
    "s_add_u32 s56, s71, s62\n"
    "v_cvt_pk_u16_u32 v36, v36, v40\n"
    "v_cvt_pk_u16_u32 v37, v37, v41\n"
    "v_cvt_pk_u16_u32 v38, v38, v42\n"
    "v_cvt_pk_u16_u32 v39, v39, v43\n"
    "v_or3_b32 v243, v36, v37, v243\n"
    "v_or3_b32 v243, v38, v39, v243\n"
    "buffer_store_dword v36, v244, s[16:19], s54 offen offset:0 sc1\n"
    "buffer_store_dword v37, v244, s[16:19], s54 offen offset:1024 sc1\n"
    "buffer_store_dword v38, v244, s[16:19], s54 offen offset:2048 sc1\n"
    "buffer_store_dword v39, v244, s[16:19], s54 offen offset:3072 sc1\n"
    "v_cvt_pk_u16_u32 v44, v44, v48\n"
    "v_cvt_pk_u16_u32 v45, v45, v49\n"
    "v_cvt_pk_u16_u32 v46, v46, v50\n"
    "s_add_u32 m0, s65, 29696\n"
    "s_add_u32 s72, s85, 5120\n"
    "buffer_load_dwordx4 v90, s[20:23], s72 offen offset:0 nt lds\n"
    "buffer_load_dwordx4 v[120:123], v162, s[28:31], s66 offen offset:3328\n"
    "v_cvt_pk_u16_u32 v47, v47, v51\n"
    "v_or3_b32 v243, v44, v45, v243\n"
    "v_or3_b32 v243, v46, v47, v243\n"
    "buffer_store_dword v44, v244, s[16:19], s55 offen offset:0 sc1\n"
    "buffer_store_dword v45, v244, s[16:19], s55 offen offset:1024 sc1\n"
    "buffer_store_dword v46, v244, s[16:19], s55 offen offset:2048 sc1\n"
    "buffer_store_dword v47, v244, s[16:19], s55 offen offset:3072 sc1\n"
    "v_cvt_pk_u16_u32 v52, v52, v56\n"
    "v_cvt_pk_u16_u32 v53, v53, v57\n"
    "v_cvt_pk_u16_u32 v54, v54, v58\n"
    "v_cvt_pk_u16_u32 v55, v55, v59\n"
    "v_or3_b32 v243, v52, v53, v243\n"
    "v_or3_b32 v243, v54, v55, v243\n"
    "buffer_store_dword v52, v244, s[16:19], s56 offen offset:0 sc1\n"
    "buffer_store_dword v53, v244, s[16:19], s56 offen offset:1024 sc1\n"
    "buffer_store_dword v54, v244, s[16:19], s56 offen offset:2048 sc1\n"
    "buffer_store_dword v55, v244, s[16:19], s56 offen offset:3072 sc1\n"
    "s_add_u32 m0, s65, 73728\n"
    "s_add_u32 s72, s85, 6144\n"
    "buffer_load_dwordx4 v88, s[20:23], s72 offen offset:0 nt lds\n"
    "s_waitcnt vmcnt(44)\n"
    "v_lshlrev_b32 v204, 16, v136\n"
    "v_and_b32 v205, 0xffff0000, v136\n"
    "v_lshlrev_b32 v206, 16, v137\n"
    "v_and_b32 v207, 0xffff0000, v137\n"
    "v_lshlrev_b32 v208, 16, v138\n"
    "v_and_b32 v209, 0xffff0000, v138\n"
    "v_lshlrev_b32 v210, 16, v139\n"
    "v_and_b32 v211, 0xffff0000, v139\n"
    "v_log_f32 v68, v204\n"
    "v_log_f32 v69, v205\n"
    "v_log_f32 v70, v206\n"
    "v_log_f32 v71, v207\n"
    "v_log_f32 v72, v208\n"
    "v_log_f32 v73, v209\n"
    "v_log_f32 v74, v210\n"
    "v_log_f32 v75, v211\n"
    "v_rcp_f32 v204, v204\n"
    "v_rcp_f32 v205, v205\n"
    "v_rcp_f32 v206, v206\n"
    "s_add_u32 m0, s65, 74752\n"
    "s_add_u32 s72, s85, 7168\n"
    "buffer_load_dwordx4 v89, s[20:23], s72 offen offset:0 nt lds\n"
    "v_rcp_f32 v207, v207\n"
    "v_rcp_f32 v208, v208\n"
    "v_rcp_f32 v209, v209\n"
    "v_rcp_f32 v210, v210\n"
    "v_rcp_f32 v211, v211\n"
    "s_mov_b32 s73, 0x41a00000\n"
    "v_mov_b32 v228, 0x7fc00000\n"
    "v_cmp_le_f32 vcc, |v68|, s73\n"
    "s_nop 1\n"
    "v_cndmask_b32 v68, v228, v68, vcc\n"
    "v_cmp_le_f32 vcc, |v69|, s73\n"
    "s_nop 1\n"
    "v_cndmask_b32 v69, v228, v69, vcc\n"
    "v_cmp_le_f32 vcc, |v70|, s73\n"
    "s_nop 1\n"
    "v_cndmask_b32 v70, v228, v70, vcc\n"
    "v_cmp_le_f32 vcc, |v71|, s73\n"
    "s_nop 1\n"
    "v_cndmask_b32 v71, v228, v71, vcc\n"
    "v_cmp_le_f32 vcc, |v72|, s73\n"
    "s_nop 1\n"
    "v_cndmask_b32 v72, v228, v72, vcc\n"
    "v_cmp_le_f32 vcc, |v73|, s73\n"
    "s_nop 1\n"
    "v_cndmask_b32 v73, v228, v73, vcc\n"
    "v_cmp_le_f32 vcc, |v74|, s73\n"
    "s_nop 1\n"
    "v_cndmask_b32 v74, v228, v74, vcc\n"
    "s_add_u32 m0, s65, 75776\n"
    "s_add_u32 s72, s85, 8192\n"
    "buffer_load_dwordx4 v90, s[20:23], s72 offen offset:0 nt lds\n"
    "v_cmp_le_f32 vcc, |v75|, s73\n"
    "s_nop 1\n"
    "v_cndmask_b32 v75, v228, v75, vcc\n"
    "ds_write_b128 v86, v[68:71] offset:0\n"
    "ds_write_b128 v86, v[72:75] offset:16\n"
    "v_cvt_pk_bf16_f32 v220, v204, v205\n"
    "v_cvt_pk_bf16_f32 v221, v206, v207\n"
    "v_cvt_pk_bf16_f32 v222, v208, v209\n"
    "v_cvt_pk_bf16_f32 v223, v210, v211\n"
    "v_lshlrev_b32 v212, 16, v220\n"
    "v_and_b32 v213, 0xffff0000, v220\n"
    "v_lshlrev_b32 v214, 16, v221\n"
    "v_and_b32 v215, 0xffff0000, v221\n"
    "v_lshlrev_b32 v216, 16, v222\n"
    "v_and_b32 v217, 0xffff0000, v222\n"
    "v_lshlrev_b32 v218, 16, v223\n"
    "v_and_b32 v219, 0xffff0000, v223\n"
    "v_sub_f32 v204, v204, v212\n"
    "v_sub_f32 v205, v205, v213\n"
    "v_sub_f32 v206, v206, v214\n"
    "v_sub_f32 v207, v207, v215\n"
    "v_sub_f32 v208, v208, v216\n"
    "s_add_u32 m0, s65, 76800\n"
    "s_add_u32 s72, s85, 9216\n"
    "buffer_load_dwordx4 v88, s[20:23], s72 offen offset:0 nt lds\n"
    "v_sub_f32 v209, v209, v217\n"
    "v_sub_f32 v210, v210, v218\n"
    "v_sub_f32 v211, v211, v219\n"
    "v_cvt_pk_bf16_f32 v224, v204, v205\n"
    "v_cvt_pk_bf16_f32 v225, v206, v207\n"
    "v_cvt_pk_bf16_f32 v226, v208, v209\n"
    "v_cvt_pk_bf16_f32 v227, v210, v211\n"
    "v_mov_b32 v204, 0\n"
    "v_mov_b32 v205, 0\n"
    "v_mov_b32 v206, 0\n"
    "v_mov_b32 v207, 0\n"
    "v_swap_b32 v221, v222\n"
    "v_swap_b32 v225, v226\n"
    "v_swap_b32 v141, v142\n"
    "ds_write2_b64 v144, v[220:221], v[222:223] offset0:32 offset1:40\n"
    "ds_write2_b64 v144, v[224:225], v[226:227] offset0:48 offset1:56\n"
    "ds_write2_b64 v144, v[140:141], v[142:143] offset0:64 offset1:72\n"
    "ds_write2_b64 v144, v[204:205], v[206:207] offset0:80 offset1:88\n"
    "s_waitcnt vmcnt(27)\n"
    "v_fma_f32 v4, v4, v180, -v184\n"
    "v_fma_f32 v5, v5, v181, -v185\n"
    "s_add_u32 m0, s65, 77824\n"
    "s_add_u32 s72, s85, 10240\n"
    "buffer_load_dwordx4 v89, s[20:23], s72 offen offset:0 nt lds\n"
    "v_fma_f32 v6, v6, v182, -v186\n"
    "v_fma_f32 v7, v7, v183, -v187\n"
    "v_fma_f32 v8, v8, v180, -v184\n"
    "v_fma_f32 v9, v9, v181, -v185\n"
    "v_fma_f32 v10, v10, v182, -v186\n"
    "v_fma_f32 v11, v11, v183, -v187\n"
    "v_cvt_pk_bf16_f32 v4, v4, v5\n"
    "v_cvt_pk_bf16_f32 v5, v6, v7\n"
    "v_cvt_pk_bf16_f32 v6, v8, v9\n"
    "v_cvt_pk_bf16_f32 v7, v10, v11\n"
    "ds_write_b64 v163, v[4:5] offset:0\n"
    "ds_write_b64 v163, v[6:7] offset:1024\n"
    "v_fma_f32 v12, v12, v188, -v192\n"
    "v_fma_f32 v13, v13, v189, -v193\n"
    "v_fma_f32 v14, v14, v190, -v194\n"
    "v_fma_f32 v15, v15, v191, -v195\n"
    "v_fma_f32 v16, v16, v188, -v192\n"
    "v_fma_f32 v17, v17, v189, -v193\n"
    "v_fma_f32 v18, v18, v190, -v194\n"
    "v_fma_f32 v19, v19, v191, -v195\n"
    "s_add_u32 m0, s65, 78848\n"
    "s_add_u32 s72, s85, 11264\n"
    "buffer_load_dwordx4 v90, s[20:23], s72 offen offset:0 nt lds\n"
    "v_cvt_pk_bf16_f32 v12, v12, v13\n"
    "v_cvt_pk_bf16_f32 v13, v14, v15\n"
    "v_cvt_pk_bf16_f32 v14, v16, v17\n"
    "v_cvt_pk_bf16_f32 v15, v18, v19\n"
    "ds_write_b64 v163, v[12:13] offset:2048\n"
    "ds_write_b64 v163, v[14:15] offset:3072\n"
    "v_fma_f32 v20, v20, v196, -v200\n"
    "v_fma_f32 v21, v21, v197, -v201\n"
    "v_fma_f32 v22, v22, v198, -v202\n"
    "v_fma_f32 v23, v23, v199, -v203\n"
    "v_fma_f32 v24, v24, v196, -v200\n"
    "v_fma_f32 v25, v25, v197, -v201\n"
    "v_fma_f32 v26, v26, v198, -v202\n"
    "v_fma_f32 v27, v27, v199, -v203\n"
    "v_cvt_pk_bf16_f32 v20, v20, v21\n"
    "v_cvt_pk_bf16_f32 v21, v22, v23\n"
    "v_cvt_pk_bf16_f32 v22, v24, v25\n"
    "v_cvt_pk_bf16_f32 v23, v26, v27\n"
    "ds_write_b64 v163, v[20:21] offset:4096\n"
    "ds_write_b64 v163, v[22:23] offset:5120\n"
    "s_waitcnt vmcnt(14)\n"
    "s_add_u32 m0, s65, 49152\n"
    "s_add_u32 s72, s85, 12288\n"
    "buffer_load_dwordx4 v88, s[20:23], s72 offen offset:0 nt lds\n"
    "v_fma_f32 v100, v100, v180, -v184\n"
    "v_fma_f32 v101, v101, v181, -v185\n"
    "v_fma_f32 v102, v102, v182, -v186\n"
    "v_fma_f32 v103, v103, v183, -v187\n"
    "v_fma_f32 v104, v104, v180, -v184\n"
    "v_fma_f32 v105, v105, v181, -v185\n"
    "v_fma_f32 v106, v106, v182, -v186\n"
    "v_fma_f32 v107, v107, v183, -v187\n"
    "v_cvt_pk_bf16_f32 v100, v100, v101\n"
    "v_cvt_pk_bf16_f32 v101, v102, v103\n"
    "v_cvt_pk_bf16_f32 v102, v104, v105\n"
    "v_cvt_pk_bf16_f32 v103, v106, v107\n"
    "ds_write_b64 v163, v[100:101] offset:12288\n"
    "ds_write_b64 v163, v[102:103] offset:13312\n"
    "v_fma_f32 v108, v108, v188, -v192\n"
    "v_fma_f32 v109, v109, v189, -v193\n"
    "v_fma_f32 v110, v110, v190, -v194\n"
    "v_fma_f32 v111, v111, v191, -v195\n"
    "v_fma_f32 v112, v112, v188, -v192\n"
    "v_fma_f32 v113, v113, v189, -v193\n"
    "v_fma_f32 v114, v114, v190, -v194\n"
    "s_add_u32 m0, s65, 50176\n"
    "s_add_u32 s72, s85, 13312\n"
    "buffer_load_dwordx4 v89, s[20:23], s72 offen offset:0 nt lds\n"
    "v_fma_f32 v115, v115, v191, -v195\n"
    "v_cvt_pk_bf16_f32 v108, v108, v109\n"
    "v_cvt_pk_bf16_f32 v109, v110, v111\n"
    "v_cvt_pk_bf16_f32 v110, v112, v113\n"
    "v_cvt_pk_bf16_f32 v111, v114, v115\n"
    "ds_write_b64 v163, v[108:109] offset:14336\n"
    "ds_write_b64 v163, v[110:111] offset:15360\n"
    "v_fma_f32 v116, v116, v196, -v200\n"
    "v_fma_f32 v117, v117, v197, -v201\n"
    "v_fma_f32 v118, v118, v198, -v202\n"
    "v_fma_f32 v119, v119, v199, -v203\n"
    "v_fma_f32 v120, v120, v196, -v200\n"
    "v_fma_f32 v121, v121, v197, -v201\n"
    "v_fma_f32 v122, v122, v198, -v202\n"
    "v_fma_f32 v123, v123, v199, -v203\n"
    "v_cvt_pk_bf16_f32 v116, v116, v117\n"
    "v_cvt_pk_bf16_f32 v117, v118, v119\n"
    "v_cvt_pk_bf16_f32 v118, v120, v121\n"
    "v_cvt_pk_bf16_f32 v119, v122, v123\n"
    "ds_write_b64 v163, v[116:117] offset:16384\n"
    "ds_write_b64 v163, v[118:119] offset:17408\n"
    "s_waitcnt vmcnt(17)\n"
    "ds_read_b128 v[36:39], v154 offset:0\n"
    "ds_read_b128 v[40:43], v155 offset:0\n"
    "ds_read_b128 v[44:47], v154 offset:128\n"
    "ds_read_b128 v[48:51], v155 offset:128\n"
    "ds_read_b128 v[52:55], v154 offset:256\n"
    "ds_read_b128 v[56:59], v155 offset:256\n"
    "s_mov_b64 exec, 0\n"
    "s_bitcmp1_b32 s83, 1\n"
    "s_cbranch_scc1 .LwV\n"
    "s_cmp_eq_u32 0, 0\n"
    "s_branch .LwK\n"
    ".Lw2:\n"
    "s_bitcmp1_b32 s83, 1\n"
    "s_cbranch_scc1 .Lwp1\n"
    "s_branch .Lwp0\n"
    ".Lw3:\n"
    "s_mov_b64 exec, -1\n"
    "s_waitcnt lgkmcnt(5)\n"
    "v_pk_maximum3_f16 v98, v36, v37, v98\n"
    "v_pk_maximum3_f16 v98, v38, v39, v98\n"
    "v_pk_add_u16 v36, s89, v36 clamp\n"
    "v_pk_add_u16 v37, s89, v37 clamp\n"
    "v_pk_add_u16 v38, s89, v38 clamp\n"
    "v_pk_add_u16 v39, s89, v39 clamp\n"
    "s_waitcnt lgkmcnt(4)\n"
    "v_pk_maximum3_f16 v98, v40, v41, v98\n"
    "v_pk_maximum3_f16 v98, v42, v43, v98\n"
    "v_pk_add_u16 v40, s89, v40 clamp\n"
    "v_pk_add_u16 v41, s89, v41 clamp\n"
    "v_pk_add_u16 v42, s89, v42 clamp\n"
    "v_pk_add_u16 v43, s89, v43 clamp\n"
    "s_waitcnt lgkmcnt(3)\n"
    "v_pk_maximum3_f16 v98, v44, v45, v98\n"
    "v_pk_maximum3_f16 v98, v46, v47, v98\n"
    "v_pk_add_u16 v44, s89, v44 clamp\n"
    "v_pk_add_u16 v45, s89, v45 clamp\n"
    "v_pk_add_u16 v46, s89, v46 clamp\n"
    "v_pk_add_u16 v47, s89, v47 clamp\n"
    "s_waitcnt lgkmcnt(2)\n"
    "v_pk_maximum3_f16 v98, v48, v49, v98\n"
    "v_pk_maximum3_f16 v98, v50, v51, v98\n"
    "v_pk_add_u16 v48, s89, v48 clamp\n"
    "v_pk_add_u16 v49, s89, v49 clamp\n"
    "v_pk_add_u16 v50, s89, v50 clamp\n"
    "v_pk_add_u16 v51, s89, v51 clamp\n"
    "s_waitcnt lgkmcnt(1)\n"
    "v_pk_maximum3_f16 v98, v52, v53, v98\n"
    "v_pk_maximum3_f16 v98, v54, v55, v98\n"
    "v_pk_add_u16 v52, s89, v52 clamp\n"
    "v_pk_add_u16 v53, s89, v53 clamp\n"
    "v_pk_add_u16 v54, s89, v54 clamp\n"
    "v_pk_add_u16 v55, s89, v55 clamp\n"
    "s_waitcnt lgkmcnt(0)\n"
    "v_pk_maximum3_f16 v98, v56, v57, v98\n"
    "v_pk_maximum3_f16 v98, v58, v59, v98\n"
    "v_pk_add_u16 v56, s89, v56 clamp\n"
    "v_pk_add_u16 v57, s89, v57 clamp\n"
    "v_pk_add_u16 v58, s89, v58 clamp\n"
    "v_pk_add_u16 v59, s89, v59 clamp\n"
    "v_mov_b32 v108, 0\n"
    "v_mov_b32 v109, 0\n"
    "v_mov_b32 v110, 0\n"
    "v_mov_b32 v111, 0\n"
    "v_mov_b32 v112, 0\n"
    "v_mov_b32 v113, 0\n"
    "v_mov_b32 v114, 0\n"
    "v_mov_b32 v115, 0\n"
    "v_mov_b32 v116, 0\n"
    "v_mov_b32 v117, 0\n"
    "v_mov_b32 v118, 0\n"
    "v_mov_b32 v119, 0\n"
    "v_mov_b32 v120, 0\n"
    "v_mov_b32 v121, 0\n"
    "v_mov_b32 v122, 0\n"
    "v_mov_b32 v123, 0\n"
    "s_mul_i32 s73, s79, 768\n"
    "v_add_u32 v78, s73, v250\n"
    "v_mov_b32 v79, 155936\n"
    "s_lshl2_add_u32 s74, s84, 155936\n"
    "v_mov_b32 v77, 155952\n"
    "v_writelane_b32 v77, s74, 63\n"
    "s_waitcnt lgkmcnt(0)\n"
    "s_barrier\n"
    "ds_read_b128 a[112:115], v252 offset:0\n"
    "ds_read_b128 a[116:119], v252 offset:2048\n"
    "ds_read_b128 a[120:123], v252 offset:4096\n"
    "ds_read_b128 a[124:127], v252 offset:6144\n"
    "ds_read_b128 a[128:131], v252 offset:8192\n"
    "ds_read_b128 a[132:135], v252 offset:10240\n"
    "ds_read_b128 a[136:139], v252 offset:1024\n"
    "ds_read_b128 a[140:143], v252 offset:3072\n"
    "ds_read_b128 a[144:147], v252 offset:5120\n"
    "ds_read_b128 a[148:151], v252 offset:7168\n"
    "ds_read_b128 a[152:155], v252 offset:9216\n"
    "ds_read_b128 a[156:159], v252 offset:11264\n"
    "s_add_u32 m0, s65, 51200\n"
    "s_add_u32 s72, s85, 14336\n"
    "buffer_load_dwordx4 v90, s[20:23], s72 offen offset:0 nt lds\n"
    "s_add_u32 m0, s65, 52224\n"
    "s_add_u32 s72, s85, 15360\n"
    "buffer_load_dwordx4 v88, s[20:23], s72 offen offset:0 nt lds\n"
    "s_waitcnt lgkmcnt(11)\n"
    "v_mfma_f32_16x16x32_bf16 v[4:7], a[112:115], v[36:39], 0\n"
    "s_waitcnt lgkmcnt(10)\n"
    "v_mfma_f32_16x16x32_bf16 v[4:7], a[116:119], v[40:43], v[4:7]\n"
    "s_waitcnt lgkmcnt(9)\n"
    "v_mfma_f32_16x16x32_bf16 v[4:7], a[120:123], v[44:47], v[4:7]\n"
    "s_waitcnt lgkmcnt(8)\n"
    "v_mfma_f32_16x16x32_bf16 v[4:7], a[124:127], v[48:51], v[4:7]\n"
    "s_waitcnt lgkmcnt(7)\n"
    "v_mfma_f32_16x16x32_bf16 v[4:7], a[128:131], v[52:55], v[4:7]\n"
    "s_waitcnt lgkmcnt(6)\n"
    "v_mfma_f32_16x16x32_bf16 v[4:7], a[132:135], v[56:59], v[4:7]\n"
    "s_waitcnt lgkmcnt(5)\n"
    "v_mfma_f32_16x16x32_bf16 v[20:23], a[136:139], v[36:39], 0\n"
    "v_accvgpr_write_b32 a192, v36\n"
    "v_accvgpr_write_b32 a193, v37\n"
    "v_accvgpr_write_b32 a194, v38\n"
    "v_accvgpr_write_b32 a195, v39\n"
    "s_waitcnt lgkmcnt(4)\n"
    "v_mfma_f32_16x16x32_bf16 v[20:23], a[140:143], v[40:43], v[20:23]\n"
    "v_accvgpr_write_b32 a196, v40\n"
    "v_accvgpr_write_b32 a197, v41\n"
    "v_accvgpr_write_b32 a198, v42\n"
    "v_accvgpr_write_b32 a199, v43\n"
    "s_waitcnt lgkmcnt(3)\n"
    "v_mfma_f32_16x16x32_bf16 v[20:23], a[144:147], v[44:47], v[20:23]\n"
    "v_accvgpr_write_b32 a200, v44\n"
    "v_accvgpr_write_b32 a201, v45\n"
    "v_accvgpr_write_b32 a202, v46\n"
    "v_accvgpr_write_b32 a203, v47\n"
    "s_waitcnt lgkmcnt(2)\n"
    "v_mfma_f32_16x16x32_bf16 v[20:23], a[148:151], v[48:51], v[20:23]\n"
    "v_accvgpr_write_b32 a204, v48\n"
    "v_accvgpr_write_b32 a205, v49\n"
    "v_accvgpr_write_b32 a206, v50\n"
    "v_accvgpr_write_b32 a207, v51\n"
    "s_waitcnt lgkmcnt(1)\n"
    "v_mfma_f32_16x16x32_bf16 v[20:23], a[152:155], v[52:55], v[20:23]\n"
    "v_accvgpr_write_b32 a208, v52\n"
    "v_accvgpr_write_b32 a209, v53\n"
    "v_accvgpr_write_b32 a210, v54\n"
    "v_accvgpr_write_b32 a211, v55\n"
    "s_waitcnt lgkmcnt(0)\n"
    "v_mfma_f32_16x16x32_bf16 v[20:23], a[156:159], v[56:59], v[20:23]\n"
    "v_accvgpr_write_b32 a212, v56\n"
    "v_accvgpr_write_b32 a213, v57\n"
    "v_accvgpr_write_b32 a214, v58\n"
    "v_accvgpr_write_b32 a215, v59\n"
    "s_add_u32 m0, s65, 53248\n"
    "s_add_u32 s72, s85, 16384\n"
    "buffer_load_dwordx4 v89, s[20:23], s72 offen offset:0 nt lds\n"
    "buffer_load_dwordx2 v[236:237], v87, s[36:39], 0 offen offset:0 sc1\n"
    "ds_read_b128 v[16:19], v78 offset:0\n"
    "ds_read_b128 v[32:35], v78 offset:16\n"
    "ds_read_b128 v[48:51], v78 offset:128\n"
    "ds_read_b128 v[64:67], v78 offset:144\n"
    "ds_read_b128 v[100:103], v78 offset:768\n"
    "ds_read_b128 v[104:107], v78 offset:784\n"
    "v_max3_f32 v76, v4, v5, v6\n"
    "ds_read_b128 a[0:3], v252 offset:12288\n"
    "v_max3_f32 v76, v76, v7, v20\n"
    "ds_read_b128 a[4:7], v252 offset:14336\n"
    "v_max3_f32 v76, v76, v21, v22\n"
    "ds_read_b128 a[8:11], v252 offset:16384\n"
    "v_max_f32 v76, v76, v23\n"
    "ds_read_b128 a[12:15], v252 offset:18432\n"
    "s_nop 0\n"
    "v_max_f32_dpp v76, v76, v76 row_shr:1\n"
    "ds_read_b128 a[16:19], v252 offset:20480\n"
    "ds_read_b128 a[20:23], v252 offset:22528\n"
    "v_max_f32_dpp v76, v76, v76 row_shr:2\n"
    "ds_read_b128 a[24:27], v252 offset:13312\n"
    "s_nop 0\n"
    "v_max_f32_dpp v76, v76, v76 row_shr:4\n"
    "ds_read_b128 a[28:31], v252 offset:15360\n"
    "s_nop 0\n"
    "v_max_f32_dpp v76, v76, v76 row_shr:8\n"
    "ds_read_b128 a[32:35], v252 offset:17408\n"
    "s_nop 0\n"
    "v_max_f32_dpp v76, v76, v76 row_bcast:15 row_mask:0xa\n"
    "ds_read_b128 a[36:39], v252 offset:19456\n"
    "s_nop 0\n"
    "v_max_f32_dpp v76, v76, v76 row_bcast:31 row_mask:0xc\n"
    "ds_read_b128 a[40:43], v252 offset:21504\n"
    "ds_read_b128 a[44:47], v252 offset:23552\n"
    "ds_write_b32 v77, v76\n"
    "s_add_u32 m0, s65, 54272\n"
    "s_add_u32 s72, s85, 17408\n"
    "buffer_load_dwordx4 v90, s[20:23], s72 offen offset:0 nt lds\n"
    "s_add_u32 m0, s65, 98304\n"
    "s_add_u32 s72, s85, 18432\n"
    "buffer_load_dwordx4 v88, s[20:23], s72 offen offset:0 nt lds\n"
    "s_waitcnt lgkmcnt(0)\n"
    "s_barrier\n"
    "ds_read_b128 v[80:83], v79 offset:0\n"
    "s_waitcnt vmcnt(14)\n"
    "buffer_store_dwordx2 v[238:239], v240, s[36:39], s80 offen offset:0 sc1\n"
    "s_lshl_b32 s73, s79, 8\n"
    "s_add_u32 s80, s80, s73\n"
    "s_mul_i32 s73, s79, 98304\n"
    "s_add_u32 s71, s71, s73\n"
    "s_add_u32 m0, s65, 99328\n"
    "s_add_u32 s72, s85, 19456\n"
    "buffer_load_dwordx4 v89, s[20:23], s72 offen offset:0 nt lds\n"
    "s_waitcnt lgkmcnt(0)\n"
    "v_max3_f32 v76, v80, v81, v82\n"
    "v_max_f32 v76, v76, v83\n"
    "v_add_f32 v76, 0xc2b00000, v76\n"
    "s_add_u32 m0, s65, 100352\n"
    "s_add_u32 s72, s85, 20480\n"
    "buffer_load_dwordx4 v90, s[20:23], s72 offen offset:0 nt lds\n"
    "s_add_u32 m0, s65, 101376\n"
    "s_add_u32 s72, s85, 21504\n"
    "buffer_load_dwordx4 v88, s[20:23], s72 offen offset:0 nt lds\n"
    "s_add_u32 m0, s65, 102400\n"
    "s_add_u32 s72, s85, 22528\n"
    "buffer_load_dwordx4 v89, s[20:23], s72 offen offset:0 nt lds\n"
    "s_add_u32 m0, s65, 103424\n"
    "s_add_u32 s72, s85, 23552\n"
    "buffer_load_dwordx4 v90, s[20:23], s72 offen offset:0 nt lds\n"
    "v_sub_f32 v48, v48, v76\n"
    "v_sub_f32 v51, v51, v76\n"
    "v_sub_f32 v49, v49, v76\n"
    "v_sub_f32 v50, v50, v76\n"
    "v_sub_f32 v66, v66, v76\n"
    "v_sub_f32 v65, v65, v76\n"
    "v_mfma_f32_16x16x32_bf16 v[36:39], a[0:3], a[192:195], v[48:51]\n"
    "s_waitcnt vmcnt(14)\n"
    "ds_read_b128 v[200:203], v97 offset:24832\n"
    "v_sub_f32 v67, v67, v76\n"
    "v_mfma_f32_16x16x32_bf16 v[36:39], a[4:7], a[196:199], v[36:39]\n"
    "v_sub_f32 v33, v33, v76\n"
    "v_sub_f32 v32, v32, v76\n"
    "v_mfma_f32_16x16x32_bf16 v[36:39], a[8:11], a[200:203], v[36:39]\n"
    "ds_read_b128 v[184:187], v97 offset:24576\n"
    "ds_read_b128 v[196:199], v96 offset:24832\n"
    "ds_read_b128 v[192:195], v97 offset:24704\n"
    "v_mfma_f32_16x16x32_bf16 v[36:39], a[12:15], a[204:207], v[36:39]\n"
    "v_sub_f32 v64, v64, v76\n"
    "v_sub_f32 v35, v35, v76\n"
    "v_sub_f32 v19, v19, v76\n"
    "v_mfma_f32_16x16x32_bf16 v[52:55], a[24:27], a[192:195], v[64:67]\n"
    "v_add_f32 v21, v21, v33\n"
    "v_sub_f32 v16, v16, v76\n"
    "v_mfma_f32_16x16x32_bf16 v[36:39], a[16:19], a[208:211], v[36:39]\n"
    "v_sub_f32 v18, v18, v76\n"
    "v_add_f32 v20, v20, v32\n"
    "v_sub_f32 v17, v17, v76\n"
    "v_mfma_f32_16x16x32_bf16 v[52:55], a[28:31], a[196:199], v[52:55]\n"
    "ds_read_b128 v[188:191], v96 offset:24704\n"
    "ds_read_b128 v[180:183], v96 offset:24576\n"
    "v_add_f32 v6, v6, v18\n"
    "v_sub_f32 v34, v34, v76\n"
    "v_mfma_f32_16x16x32_bf16 v[36:39], a[20:23], a[212:215], v[36:39]\n"
    "s_waitcnt lgkmcnt(5)\n"
    "v_pk_maximum3_f16 v98, v200, v201, v98\n"
    "v_exp_f32 v20, v20\n"
    "v_pk_maximum3_f16 v98, v202, v203, v98\n"
    "v_pk_add_u16 v203, s89, v203 clamp\n"
    "v_pk_add_u16 v200, s89, v200 clamp\n"
    "v_mfma_f32_16x16x32_bf16 v[52:55], a[32:35], a[200:203], v[52:55]\n"
    "s_waitcnt lgkmcnt(3)\n"
    "v_pk_maximum3_f16 v98, v196, v197, v98\n"
    "v_pk_maximum3_f16 v98, v198, v199, v98\n"
    "v_mfma_f32_16x16x32_bf16 v[52:55], a[36:39], a[204:207], v[52:55]\n"
    "v_exp_f32 v21, v21\n"
    "v_pk_add_u16 v202, s89, v202 clamp\n"
    "v_pk_add_u16 v201, s89, v201 clamp\n"
    "v_mfma_f32_16x16x32_bf16 v[52:55], a[40:43], a[208:211], v[52:55]\n"
    "v_add_f32 v22, v22, v34\n"
    "v_add_f32 v7, v7, v19\n"
    "v_mfma_f32_16x16x32_bf16 v[24:27], a[156:159], v[200:203], v[32:35]\n"
    "s_waitcnt lgkmcnt(2)\n"
    "v_pk_maximum3_f16 v98, v192, v193, v98\n"
    "v_pk_maximum3_f16 v98, v194, v195, v98\n"
    "v_pk_add_u16 v193, s89, v193 clamp\n"
    "v_pk_add_u16 v198, s89, v198 clamp\n"
    "v_add_f32 v5, v5, v17\n"
    "v_mfma_f32_16x16x32_bf16 v[52:55], a[44:47], a[212:215], v[52:55]\n"
    "s_waitcnt lgkmcnt(1)\n"
    "v_pk_add_u16 v194, s89, v194 clamp\n"
    "v_sub_f32 v69, v69, v76\n"
    "v_sub_f32 v73, v73, v76\n"
    "v_pk_maximum3_f16 v98, v188, v189, v98\n"
    "v_pk_maximum3_f16 v98, v190, v191, v98\n"
    "v_pk_add_u16 v196, s89, v196 clamp\n"
    "v_pk_maximum3_f16 v98, v184, v185, v98\n"
    "v_pk_add_u16 v197, s89, v197 clamp\n"
    "v_pk_maximum3_f16 v98, v186, v187, v98\n"
    "v_mfma_f32_16x16x32_bf16 v[8:11], a[132:135], v[200:203], v[16:19]\n"
    "v_sub_f32 v75, v75, v76\n"
    "v_pk_add_u16 v199, s89, v199 clamp\n"
    "v_sub_f32 v101, v101, v76\n"
    "v_exp_f32 v54, v54\n"
    "v_mfma_f32_16x16x32_bf16 v[24:27], a[152:155], v[196:199], v[24:27]\n"
    "s_waitcnt lgkmcnt(0)\n"
    "v_pk_add_u16 v188, s89, v188 clamp\n"
    "v_pk_add_u16 v184, s89, v184 clamp\n"
    "v_sub_f32 v70, v70, v76\n"
    "v_pk_maximum3_f16 v98, v180, v181, v98\n"
    "v_pk_add_u16 v186, s89, v186 clamp\n"
    "v_mfma_f32_16x16x32_bf16 v[56:59], a[44:47], v[200:203], v[64:67]\n"
    "v_pk_add_u16 v189, s89, v189 clamp\n"
    "v_pk_add_u16 v195, s89, v195 clamp\n"
    "v_pk_add_u16 v192, s89, v192 clamp\n"
    "v_pk_add_u16 v185, s89, v185 clamp\n"
    "v_pk_maximum3_f16 v98, v182, v183, v98\n"
    "v_exp_f32 v55, v55\n"
    "v_sub_f32 v68, v68, v76\n"
    "v_mfma_f32_16x16x32_bf16 v[56:59], a[40:43], v[196:199], v[56:59]\n"
    "v_exp_f32 v52, v52\n"
    "v_pk_add_u16 v190, s89, v190 clamp\n"
    "v_mfma_f32_16x16x32_bf16 v[8:11], a[128:131], v[196:199], v[8:11]\n"
    "v_accvgpr_write_b32 a231, v195\n"
    "v_sub_f32 v102, v102, v76\n"
    "v_sub_f32 v71, v71, v76\n"
    "v_sub_f32 v107, v107, v76\n"
    "v_mfma_f32_16x16x32_bf16 v[40:43], a[20:23], v[200:203], v[48:51]\n"
    "v_sub_f32 v74, v74, v76\n"
    "v_add_f32 v23, v23, v35\n"
    "v_mfma_f32_16x16x32_bf16 v[40:43], a[16:19], v[196:199], v[40:43]\n"
    "v_pk_add_u16 v183, s89, v183 clamp\n"
    "v_accvgpr_write_b32 a228, v192\n"
    "v_mfma_f32_16x16x32_bf16 v[56:59], a[36:39], v[192:195], v[56:59]\n"
    "v_add_f32 v4, v4, v16\n"
    "v_exp_f32 v6, v6\n"
    "v_mfma_f32_16x16x32_bf16 v[24:27], a[148:151], v[192:195], v[24:27]\n"
    "v_sub_f32 v72, v72, v76\n"
    "v_pk_add_u16 v187, s89, v187 clamp\n"
    "v_pk_add_u16 v191, s89, v191 clamp\n"
    "v_mfma_f32_16x16x32_bf16 v[8:11], a[124:127], v[192:195], v[8:11]\n"
    "v_exp_f32 v53, v53\n"
    "v_accvgpr_write_b32 a226, v190\n"
    "v_mfma_f32_16x16x32_bf16 v[8:11], a[120:123], v[188:191], v[8:11]\n"
    "v_accvgpr_write_b32 a221, v185\n"
    "v_exp_f32 v39, v39\n"
    "v_exp_f32 v22, v22\n"
    "v_pk_add_u16 v181, s89, v181 clamp\n"
    "v_mfma_f32_16x16x32_bf16 v[56:59], a[32:35], v[188:191], v[56:59]\n"
    "v_exp_f32 v36, v36\n"
    "v_pk_add_u16 v182, s89, v182 clamp\n"
    "v_exp_f32 v23, v23\n"
    "v_mfma_f32_16x16x32_bf16 v[24:27], a[144:147], v[188:191], v[24:27]\n"
    "v_accvgpr_write_b32 a237, v201\n"
    "v_pk_add_u16 v180, s89, v180 clamp\n"
    "v_mfma_f32_16x16x32_bf16 v[40:43], a[12:15], v[192:195], v[40:43]\n"
    "v_accvgpr_write_b32 a239, v203\n"
    "v_accvgpr_write_b32 a223, v187\n"
    "v_exp_f32 v7, v7\n"
    "v_accvgpr_write_b32 a233, v197\n"
    "v_mfma_f32_16x16x32_bf16 v[8:11], a[116:119], v[184:187], v[8:11]\n"
    "v_accvgpr_write_b32 a232, v196\n"
    "v_accvgpr_write_b32 a235, v199\n"
    "v_mfma_f32_16x16x32_bf16 v[40:43], a[8:11], v[188:191], v[40:43]\n"
    "ds_write_b128 v86, v[68:71] offset:0\n"
    "v_mfma_f32_16x16x32_bf16 v[40:43], a[4:7], v[184:187], v[40:43]\n"
    "v_sub_f32 v106, v106, v76\n"
    "v_accvgpr_write_b32 a234, v198\n"
    "v_mfma_f32_16x16x32_bf16 v[8:11], a[112:115], v[180:183], v[8:11]\n"
    "v_cvt_pk_bf16_f32 v70, v20, v21\n"
    "v_exp_f32 v37, v37\n"
    "v_cvt_pk_bf16_f32 v69, v6, v7\n"
    "v_accvgpr_write_b32 a218, v182\n"
    "v_mfma_f32_16x16x32_bf16 v[40:43], a[0:3], v[180:183], v[40:43]\n"
    "ds_write_b128 v86, v[72:75] offset:16\n"
    "v_accvgpr_write_b32 a230, v194\n"
    "v_mfma_f32_16x16x32_bf16 v[24:27], a[140:143], v[184:187], v[24:27]\n"
    "v_exp_f32 v10, v10\n"
    "v_mfma_f32_16x16x32_bf16 v[56:59], a[28:31], v[184:187], v[56:59]\n"
    "v_cvt_pk_bf16_f32 v72, v36, v37\n"
    "v_sub_f32 v103, v103, v76\n"
    "v_mfma_f32_16x16x32_bf16 v[56:59], a[24:27], v[180:183], v[56:59]\n"
    "v_sub_f32 v104, v104, v76\n"
    "v_accvgpr_write_b32 a217, v181\n"
    "v_exp_f32 v4, v4\n"
    "v_sub_f32 v100, v100, v76\n"
    "v_accvgpr_write_b32 a227, v191\n"
    "v_exp_f32 v11, v11\n"
    "v_exp_f32 v5, v5\n"
    "v_exp_f32 v38, v38\n"
    "v_exp_f32 v41, v41\n"
    "v_exp_f32 v9, v9\n"
    "v_exp_f32 v59, v59\n"
    "v_accvgpr_write_b32 a220, v184\n"
    "v_accvgpr_write_b32 a238, v202\n"
    "v_accvgpr_write_b32 a236, v200\n"
    "v_exp_f32 v43, v43\n"
    "v_cvt_pk_bf16_f32 v77, v10, v11\n"
    "v_exp_f32 v8, v8\n"
    "v_cvt_pk_bf16_f32 v75, v54, v55\n"
    "v_mfma_f32_16x16x32_bf16 v[24:27], a[136:139], v[180:183], v[24:27]\n"
    "v_cvt_pk_bf16_f32 v71, v22, v23\n"
    "v_sub_f32 v105, v105, v76\n"
    "v_accvgpr_write_b32 a216, v180\n"
    "v_exp_f32 v58, v58\n"
    "v_cvt_pk_bf16_f32 v73, v38, v39\n"
    "v_exp_f32 v40, v40\n"
    "v_accvgpr_write_b32 a222, v186\n"
    "v_accvgpr_write_b32 a225, v189\n"
    "v_exp_f32 v24, v24\n"
    "v_exp_f32 v57, v57\n"
    "v_cvt_pk_bf16_f32 v68, v4, v5\n"
    "v_cvt_pk_bf16_f32 v80, v40, v41\n"
    "v_exp_f32 v27, v27\n"
    "v_exp_f32 v25, v25\n"
    "v_cvt_pk_bf16_f32 v76, v8, v9\n"
    "v_accvgpr_write_b32 a224, v188\n"
    "v_accvgpr_write_b32 a219, v183\n"
    "v_exp_f32 v26, v26\n"
    "v_exp_f32 v42, v42\n"
    "v_cvt_pk_bf16_f32 v78, v24, v25\n"
    "v_exp_f32 v56, v56\n"
    "v_cvt_pk_bf16_f32 v74, v52, v53\n"
    "v_accvgpr_write_b32 a229, v193\n"
    "v_cvt_pk_bf16_f32 v79, v26, v27\n"
    "v_cvt_pk_bf16_f32 v82, v56, v57\n"
    "v_cvt_pk_bf16_f32 v83, v58, v59\n"
    "v_cvt_pk_bf16_f32 v81, v42, v43\n"
    "s_waitcnt vmcnt(8)\n"
    "v_cmp_eq_u64 s[54:55], v[236:237], v[238:239]\n"
    "s_nop 0\n"
    "s_cmp_eq_u64 s[54:55], -1\n"
    "s_cbranch_scc0 .LpollAV\n"
    ".LpollAVr:\n"
    "s_add_u32 m0, s65, 24576\n"
    "s_add_u32 s72, s65, 49152\n"
    "buffer_load_dwordx4 v1, s[16:19], s72 offen offset:0 sc1 lds\n"
    "s_add_u32 m0, s65, 25600\n"
    "s_add_u32 s72, s65, 50176\n"
    "buffer_load_dwordx4 v1, s[16:19], s72 offen offset:0 sc1 lds\n"
    "s_add_u32 m0, s65, 26624\n"
    "s_add_u32 s72, s65, 51200\n"
    "buffer_load_dwordx4 v1, s[16:19], s72 offen offset:0 sc1 lds\n"
    "s_add_u32 m0, s65, 27648\n"
    "s_add_u32 s72, s65, 52224\n"
    "buffer_load_dwordx4 v1, s[16:19], s72 offen offset:0 sc1 lds\n"
    "s_add_u32 m0, s65, 28672\n"
    "s_add_u32 s72, s65, 53248\n"
    "buffer_load_dwordx4 v1, s[16:19], s72 offen offset:0 sc1 lds\n"
    "s_add_u32 m0, s65, 29696\n"
    "s_add_u32 s72, s65, 54272\n"
    "buffer_load_dwordx4 v1, s[16:19], s72 offen offset:0 sc1 lds\n"
    "s_waitcnt vmcnt(13)\n"
    "ds_read_b128 v[156:159], v96 offset:0\n"
    "ds_read_b128 v[172:175], v96 offset:256\n"
    "ds_read_b128 v[168:171], v97 offset:128\n"
    "ds_read_b128 v[176:179], v97 offset:256\n"
    "ds_read_b128 v[160:163], v97 offset:0\n"
    "ds_read_b128 v[164:167], v96 offset:128\n"
    "s_waitcnt lgkmcnt(5)\n"
    "v_pk_maximum3_f16 v98, v156, v157, v98\n"
    "v_pk_maximum3_f16 v98, v158, v159, v98\n"
    "v_pk_add_u16 v157, s89, v157 clamp\n"
    "v_pk_add_u16 v158, s89, v158 clamp\n"
    "v_pk_add_u16 v156, s89, v156 clamp\n"
    "v_pk_add_u16 v159, s89, v159 clamp\n"
    "s_waitcnt lgkmcnt(1)\n"
    "v_pk_maximum3_f16 v98, v160, v161, v98\n"
    "s_add_u32 m0, s65, 73728\n"
    "s_add_u32 s72, s65, 73728\n"
    "buffer_load_dwordx4 v1, s[16:19], s72 offen offset:0 sc1 lds\n"
    "v_mfma_f32_16x16x32_bf16 v[28:31], a[136:139], v[156:159], v[32:35]\n"
    "s_waitcnt lgkmcnt(0)\n"
    "v_pk_maximum3_f16 v98, v162, v163, v98\n"
    "v_pk_maximum3_f16 v98, v164, v165, v98\n"
    "v_mfma_f32_16x16x32_bf16 v[12:15], a[112:115], v[156:159], v[16:19]\n"
    "v_pk_add_u16 v161, s89, v161 clamp\n"
    "v_pk_maximum3_f16 v98, v166, v167, v98\n"
    "v_pk_add_u16 v160, s89, v160 clamp\n"
    "v_pk_add_u16 v162, s89, v162 clamp\n"
    "v_mfma_f32_16x16x32_bf16 v[60:63], a[24:27], v[156:159], v[64:67]\n"
    "v_pk_maximum3_f16 v98, v168, v169, v98\n"
    "v_pk_add_u16 v163, s89, v163 clamp\n"
    "v_mfma_f32_16x16x32_bf16 v[44:47], a[0:3], v[156:159], v[48:51]\n"
    "v_pk_maximum3_f16 v98, v170, v171, v98\n"
    "v_pk_add_u16 v164, s89, v164 clamp\n"
    "v_mfma_f32_16x16x32_bf16 v[12:15], a[116:119], v[160:163], v[12:15]\n"
    "v_pk_add_u16 v165, s89, v165 clamp\n"
    "s_add_u32 m0, s65, 74752\n"
    "s_add_u32 s72, s65, 74752\n"
    "buffer_load_dwordx4 v1, s[16:19], s72 offen offset:0 sc1 lds\n"
    "v_pk_add_u16 v169, s89, v169 clamp\n"
    "v_pk_add_u16 v168, s89, v168 clamp\n"
    "v_mfma_f32_16x16x32_bf16 v[60:63], a[28:31], v[160:163], v[60:63]\n"
    "v_pk_maximum3_f16 v98, v172, v173, v98\n"
    "v_pk_add_u16 v170, s89, v170 clamp\n"
    "v_mfma_f32_16x16x32_bf16 v[28:31], a[140:143], v[160:163], v[28:31]\n"
    "v_pk_maximum3_f16 v98, v174, v175, v98\n"
    "v_pk_add_u16 v166, s89, v166 clamp\n"
    "v_pk_add_u16 v167, s89, v167 clamp\n"
    "v_mfma_f32_16x16x32_bf16 v[44:47], a[4:7], v[160:163], v[44:47]\n"
    "v_pk_maximum3_f16 v98, v176, v177, v98\n"
    "v_accvgpr_write_b32 a251, v167\n"
    "v_mfma_f32_16x16x32_bf16 v[44:47], a[8:11], v[164:167], v[44:47]\n"
    "v_pk_maximum3_f16 v98, v178, v179, v98\n"
    "v_pk_add_u16 v176, s89, v176 clamp\n"
    "v_mfma_f32_16x16x32_bf16 v[60:63], a[32:35], v[164:167], v[60:63]\n"
    "v_pk_add_u16 v171, s89, v171 clamp\n"
    "s_add_u32 m0, s65, 75776\n"
    "s_add_u32 s72, s65, 75776\n"
    "buffer_load_dwordx4 v1, s[16:19], s72 offen offset:0 sc1 lds\n"
    "v_pk_add_u16 v174, s89, v174 clamp\n"
    "v_mfma_f32_16x16x32_bf16 v[28:31], a[144:147], v[164:167], v[28:31]\n"
    "v_pk_add_u16 v173, s89, v173 clamp\n"
    "v_pk_add_u16 v172, s89, v172 clamp\n"
    "v_mfma_f32_16x16x32_bf16 v[12:15], a[120:123], v[164:167], v[12:15]\n"
    "v_accvgpr_write_b32 a254, v170\n"
    "v_pk_add_u16 v178, s89, v178 clamp\n"
    "v_mfma_f32_16x16x32_bf16 v[28:31], a[148:151], v[168:171], v[28:31]\n"
    "v_accvgpr_write_b32 a243, v159\n"
    "v_accvgpr_write_b32 a162, v174\n"
    "v_mfma_f32_16x16x32_bf16 v[12:15], a[124:127], v[168:171], v[12:15]\n"
    "v_pk_add_u16 v179, s89, v179 clamp\n"
    "v_pk_add_u16 v175, s89, v175 clamp\n"
    "v_mfma_f32_16x16x32_bf16 v[44:47], a[12:15], v[168:171], v[44:47]\n"
    "v_accvgpr_write_b32 a166, v178\n"
    "v_accvgpr_write_b32 a161, v173\n"
    "v_accvgpr_write_b32 a245, v161\n"
    "s_add_u32 m0, s65, 76800\n"
    "s_add_u32 s72, s65, 76800\n"
    "buffer_load_dwordx4 v1, s[16:19], s72 offen offset:0 sc1 lds\n"
    "v_mfma_f32_16x16x32_bf16 v[28:31], a[152:155], v[172:175], v[28:31]\n"
    "v_accvgpr_write_b32 a163, v175\n"
    "v_pk_add_u16 v177, s89, v177 clamp\n"
    "v_mfma_f32_16x16x32_bf16 v[44:47], a[16:19], v[172:175], v[44:47]\n"
    "v_accvgpr_write_b32 a160, v172\n"
    "v_accvgpr_write_b32 a252, v168\n"
    "v_mfma_f32_16x16x32_bf16 v[28:31], a[156:159], v[176:179], v[28:31]\n"
    "v_accvgpr_write_b32 a249, v165\n"
    "v_accvgpr_write_b32 a255, v171\n"
    "v_mfma_f32_16x16x32_bf16 v[12:15], a[128:131], v[172:175], v[12:15]\n"
    "v_accvgpr_write_b32 a247, v163\n"
    "v_accvgpr_write_b32 a164, v176\n"
    "v_mfma_f32_16x16x32_bf16 v[60:63], a[36:39], v[168:171], v[60:63]\n"
    "v_accvgpr_write_b32 a242, v158\n"
    "v_accvgpr_write_b32 a250, v166\n"
    "v_mfma_f32_16x16x32_bf16 v[44:47], a[20:23], v[176:179], v[44:47]\n"
    "v_accvgpr_write_b32 a241, v157\n"
    "s_add_u32 m0, s65, 77824\n"
    "s_add_u32 s72, s65, 77824\n"
    "buffer_load_dwordx4 v1, s[16:19], s72 offen offset:0 sc1 lds\n"
    "v_accvgpr_write_b32 a248, v164\n"
    "v_mfma_f32_16x16x32_bf16 v[12:15], a[132:135], v[176:179], v[12:15]\n"
    "v_accvgpr_write_b32 a246, v162\n"
    "v_exp_f32 v29, v29\n"
    "v_mfma_f32_16x16x32_bf16 v[60:63], a[40:43], v[172:175], v[60:63]\n"
    "v_exp_f32 v28, v28\n"
    "v_mfma_f32_16x16x32_bf16 v[60:63], a[44:47], v[176:179], v[60:63]\n"
    "v_exp_f32 v31, v31\n"
    "v_exp_f32 v30, v30\n"
    "v_exp_f32 v45, v45\n"
    "v_cvt_pk_bf16_f32 v86, v28, v29\n"
    "v_exp_f32 v47, v47\n"
    "v_accvgpr_write_b32 a240, v156\n"
    "v_exp_f32 v12, v12\n"
    "v_exp_f32 v14, v14\n"
    "v_accvgpr_write_b32 a167, v179\n"
    "v_exp_f32 v15, v15\n"
    "s_add_u32 m0, s65, 78848\n"
    "s_add_u32 s72, s65, 78848\n"
    "buffer_load_dwordx4 v1, s[16:19], s72 offen offset:0 sc1 lds\n"
    "v_accvgpr_write_b32 a253, v169\n"
    "v_exp_f32 v63, v63\n"
    "v_exp_f32 v13, v13\n"
    "v_exp_f32 v44, v44\n"
    "v_cvt_pk_bf16_f32 v84, v12, v13\n"
    "v_cvt_pk_bf16_f32 v87, v30, v31\n"
    "v_exp_f32 v46, v46\n"
    "v_exp_f32 v61, v61\n"
    "v_accvgpr_write_b32 a165, v177\n"
    "v_exp_f32 v60, v60\n"
    "v_exp_f32 v62, v62\n"
    "v_cvt_pk_bf16_f32 v90, v60, v61\n"
    "v_accvgpr_write_b32 a244, v160\n"
    "v_cvt_pk_bf16_f32 v85, v14, v15\n"
    "v_cvt_pk_bf16_f32 v89, v46, v47\n"
    "v_cvt_pk_bf16_f32 v88, v44, v45\n"
    "v_cvt_pk_bf16_f32 v91, v62, v63\n"
    "s_bitcmp1_b32 s83, 1\n"
    "s_cbranch_scc1 .Lf4b\n"
    "s_lshl_b32 s73, s53, 1\n"
    "s_add_u32 s73, s73, s79\n"
    "s_mul_i32 s74, s73, 6144\n"
    "s_add_u32 s74, s74, 1425408\n"
    "s_lshl_b32 s73, s73, 8\n"
    "s_add_u32 s73, s73, 1557504\n"
    "s_mov_b64 s[20:21], s[16:17]\n"
    "s_mov_b32 s44, 98304\n"
    "s_mov_b32 s22, 1572864\n"
    "s_bitcmp1_b32 s84, 0\n"
    "s_cselect_b32 s52, s71, s73\n"
    "s_cselect_b32 s71, s71, s74\n"
    "s_cselect_b32 s63, s63, 0\n"
    "s_cselect_b32 s45, s44, 12288\n"
    "s_cselect_b32 s44, s44, 24576\n"
    "s_cselect_b32 s42, 1523712, s42\n"
    "s_cselect_b32 s22, s22, s18\n"
    "s_cselect_b32 s60, s60, 24576\n"
    "s_cselect_b32 s61, s61, 28672\n"
    "s_cselect_b32 s62, s62, 32768\n"
    "s_mov_b32 s66, 1\n"
    "s_add_u32 s68, s68, 24576\n"
    "s_add_u32 s81, s81, 128\n"
    "s_add_u32 s80, s80, 256\n"
    "s_add_u32 s71, s71, s44\n"
    "s_add_u32 s52, s52, s45\n"
    "s_add_u32 s75, s71, s63\n"
    "s_branch .LhVP\n"
    "s_branch .Lf4e\n"
    ".Lf4b:\n"
    "s_add_u32 s75, s71, s63\n"
    "buffer_load_dwordx4 v[204:207], v242, s[40:43], s75 offen offset:0 nt\n"
    "buffer_load_dwordx4 v[208:211], v242, s[40:43], s75 offen offset:768 nt\n"
    "buffer_load_dwordx4 v[212:215], v242, s[40:43], s75 offen offset:256 nt\n"
    "buffer_load_dwordx4 v[216:219], v242, s[40:43], s75 offen offset:1024 nt\n"
    "buffer_load_dwordx4 v[220:223], v242, s[40:43], s75 offen offset:512 nt\n"
    "buffer_load_dwordx4 v[224:227], v242, s[40:43], s75 offen offset:1280 nt\n"
    "s_lshl_b32 s73, s53, 1\n"
    "s_add_u32 s73, s73, s79\n"
    "s_mul_i32 s74, s73, 6144\n"
    "s_add_u32 s74, s74, 1425408\n"
    "s_lshl_b32 s73, s73, 8\n"
    "s_add_u32 s73, s73, 1557504\n"
    "s_mov_b64 s[20:21], s[16:17]\n"
    "s_mov_b32 s44, 98304\n"
    "s_mov_b32 s22, 1572864\n"
    "s_bitcmp1_b32 s84, 0\n"
    "s_cselect_b32 s52, s71, s73\n"
    "s_cselect_b32 s71, s71, s74\n"
    "s_cselect_b32 s63, s63, 0\n"
    "s_cselect_b32 s45, s44, 12288\n"
    "s_cselect_b32 s44, s44, 24576\n"
    "s_cselect_b32 s42, 1523712, s42\n"
    "s_cselect_b32 s22, s22, s18\n"
    "s_cselect_b32 s60, s60, 24576\n"
    "s_cselect_b32 s61, s61, 28672\n"
    "s_cselect_b32 s62, s62, 32768\n"
    "s_mov_b32 s66, 1\n"
    "s_add_u32 s68, s68, 24576\n"
    "s_add_u32 s81, s81, 128\n"
    "s_branch .LhVN\n"
    ".Lf4e:\n"
    "s_nop 4\n"
    ".Lf5e:\n"
    "s_nop 4\n"
    ".fill ((0x20000 - (. - _Z8attn_fwdPKtPKiS2_S0_S0_S0_S0_PcPt)) & 4095) / 4, 4, 0xBF800000\n"
    ".LhKP:\n"
    "s_waitcnt vmcnt(12)\n"
    "ds_read_b128 v[52:55], v251 offset:0\n"
    "ds_read_b128 v[192:195], v97 offset:49280\n"
    "ds_read_b128 v[196:199], v96 offset:49408\n"
    "ds_read_b128 v[200:203], v97 offset:49408\n"
    "s_add_u32 s83, s67, 4096\n"
    "ds_read_b128 v[188:191], v96 offset:49280\n"
    "ds_read_b128 v[180:183], v96 offset:49152\n"
    "s_nop 0\n"
    "ds_read_b128 v[184:187], v97 offset:49152\n"
    "s_waitcnt lgkmcnt(6)\n"
    "v_smfmac_f32_16x16x64_bf16 v[108:111], v[52:55], v[68:75], v253\n"
    "s_waitcnt lgkmcnt(3)\n"
    "v_pk_maximum3_f16 v98, v200, v201, v98\n"
    "v_pk_maximum3_f16 v98, v202, v203, v98\n"
    "v_pk_maximum3_f16 v98, v196, v197, v98\n"
    "v_pk_add_u16 v203, s89, v203 clamp\n"
    "v_pk_maximum3_f16 v98, v198, v199, v98\n"
    "v_pk_add_u16 v200, s89, v200 clamp\n"
    "v_pk_add_u16 v202, s89, v202 clamp\n"
    "v_pk_add_u16 v201, s89, v201 clamp\n"
    "v_pk_maximum3_f16 v98, v192, v193, v98\n"
    "v_pk_maximum3_f16 v98, v194, v195, v98\n"
    "v_mfma_f32_16x16x32_bf16 v[16:19], a[132:135], v[200:203], v[16:19]\n"
    "s_add_u32 m0, s68, .Lend-.Lend2+(0)\n"
    "v_pk_add_u16 v199, s89, v199 clamp\n"
    "v_pk_add_u16 v197, s89, v197 clamp\n"
    "v_mfma_f32_16x16x32_bf16 v[64:67], a[44:47], v[200:203], v[64:67]\n"
    "buffer_load_dwordx4 v1, s[16:19], s67 offen offset:0 sc1 lds\n"
    "v_pk_add_u16 v198, s89, v198 clamp\n"
    "v_mfma_f32_16x16x32_bf16 v[32:35], a[156:159], v[200:203], v[32:35]\n"
    "v_pk_add_u16 v196, s89, v196 clamp\n"
    "v_accvgpr_write_b32 a191, v203\n"
    "v_mfma_f32_16x16x32_bf16 v[48:51], a[20:23], v[200:203], v[48:51]\n"
    "s_waitcnt lgkmcnt(2)\n"
    "v_pk_maximum3_f16 v98, v188, v189, v98\n"
    "v_pk_add_u16 v195, s89, v195 clamp\n"
    "v_mfma_f32_16x16x32_bf16 v[64:67], a[40:43], v[196:199], v[64:67]\n"
    "v_pk_add_u16 v192, s89, v192 clamp\n"
    "v_pk_add_u16 v193, s89, v193 clamp\n"
    "v_mfma_f32_16x16x32_bf16 v[32:35], a[152:155], v[196:199], v[32:35]\n"
    "v_pk_maximum3_f16 v98, v190, v191, v98\n"
    "v_pk_add_u16 v194, s89, v194 clamp\n"
    "v_smfmac_f32_16x16x64_bf16 v[116:119], v[52:55], v[84:91], v253\n"
    "v_pk_add_u16 v191, s89, v191 clamp\n"
    "v_mfma_f32_16x16x32_bf16 v[48:51], a[16:19], v[196:199], v[48:51]\n"
    "buffer_load_dwordx4 v1, s[16:19], s67 offen offset:1024 sc1 lds\n"
    "v_mfma_f32_16x16x32_bf16 v[16:19], a[128:131], v[196:199], v[16:19]\n"
    "s_waitcnt lgkmcnt(0)\n"
    "v_pk_maximum3_f16 v98, v184, v185, v98\n"
    "v_pk_add_u16 v189, s89, v189 clamp\n"
    "v_mfma_f32_16x16x32_bf16 v[48:51], a[12:15], v[192:195], v[48:51]\n"
    "v_pk_add_u16 v190, s89, v190 clamp\n"
    "v_pk_add_u16 v184, s89, v184 clamp\n"
    "v_accvgpr_write_b32 a188, v200\n"
    "v_mfma_f32_16x16x32_bf16 v[64:67], a[36:39], v[192:195], v[64:67]\n"
    "v_pk_maximum3_f16 v98, v186, v187, v98\n"
    "v_pk_add_u16 v188, s89, v188 clamp\n"
    "v_mfma_f32_16x16x32_bf16 v[32:35], a[148:151], v[192:195], v[32:35]\n"
    "v_pk_add_u16 v187, s89, v187 clamp\n"
    "v_pk_maximum3_f16 v98, v180, v181, v98\n"
    "v_mfma_f32_16x16x32_bf16 v[32:35], a[144:147], v[188:191], v[32:35]\n"
    "v_pk_maximum3_f16 v98, v182, v183, v98\n"
    "v_pk_add_u16 v185, s89, v185 clamp\n"
    "v_mfma_f32_16x16x32_bf16 v[16:19], a[124:127], v[192:195], v[16:19]\n"
    "buffer_load_dwordx4 v1, s[16:19], s67 offen offset:2048 sc1 lds\n"
    "v_mfma_f32_16x16x32_bf16 v[16:19], a[120:123], v[188:191], v[16:19]\n"
    "s_waitcnt vmcnt(9)\n"
    "v_pk_add_u16 v186, s89, v186 clamp\n"
    "v_pk_add_u16 v181, s89, v181 clamp\n"
    "v_mfma_f32_16x16x32_bf16 v[64:67], a[32:35], v[188:191], v[64:67]\n"
    "s_barrier\n"
    "ds_read_b128 a[48:51], v252 offset:24576\n"
    "v_mfma_f32_16x16x32_bf16 v[48:51], a[8:11], v[188:191], v[48:51]\n"
    "ds_read_b128 a[72:75], v252 offset:25600\n"
    "ds_read_b128 a[92:95], v252 offset:35840\n"
    "v_mfma_f32_16x16x32_bf16 v[32:35], a[140:143], v[184:187], v[32:35]\n"
    "v_pk_add_u16 v183, s89, v183 clamp\n"
    "v_pk_add_u16 v180, s89, v180 clamp\n"
    "v_pk_add_u16 v182, s89, v182 clamp\n"
    "v_mfma_f32_16x16x32_bf16 v[16:19], a[116:119], v[184:187], v[16:19]\n"
    "ds_read_b128 a[84:87], v252 offset:31744\n"
    "ds_read_b128 a[52:55], v252 offset:26624\n"
    "v_mfma_f32_16x16x32_bf16 v[32:35], a[136:139], v[180:183], v[32:35]\n"
    "buffer_load_dwordx4 v1, s[16:19], s67 offen offset:3072 sc1 lds\n"
    "v_mfma_f32_16x16x32_bf16 v[48:51], a[4:7], v[184:187], v[48:51]\n"
    "ds_read_b128 a[88:91], v252 offset:33792\n"
    "ds_read_b128 a[56:59], v252 offset:28672\n"
    "v_mfma_f32_16x16x32_bf16 v[64:67], a[28:31], v[184:187], v[64:67]\n"
    "v_accvgpr_write_b32 a170, v182\n"
    "v_accvgpr_write_b32 a177, v189\n"
    "v_mfma_f32_16x16x32_bf16 v[16:19], a[112:115], v[180:183], v[16:19]\n"
    "s_add_u32 m0, s68, 4096\n"
    "ds_read_b128 a[76:79], v252 offset:27648\n"
    "ds_read_b128 a[68:71], v252 offset:34816\n"
    "ds_read_b128 a[60:63], v252 offset:30720\n"
    "v_mfma_f32_16x16x32_bf16 v[48:51], a[0:3], v[180:183], v[48:51]\n"
    "s_waitcnt lgkmcnt(9)\n"
    "ds_read_b128 a[80:83], v252 offset:29696\n"
    "v_accvgpr_write_b32 a179, v191\n"
    "v_mfma_f32_16x16x32_bf16 v[4:7], a[48:51], a[192:195], v[100:103]\n"
    "s_waitcnt lgkmcnt(8)\n"
    "v_pk_add_u16 v98, s89, v98\n"
    "v_accvgpr_write_b32 a174, v186\n"
    "v_mfma_f32_16x16x32_bf16 v[24:27], a[92:95], a[236:239], v[104:107]\n"
    "buffer_load_dwordx4 v1, s[16:19], s83 offen offset:0 sc1 lds\n"
    "v_mfma_f32_16x16x32_bf16 v[12:15], a[48:51], a[240:243], v[100:103]\n"
    "v_accvgpr_write_b32 a186, v198\n"
    "v_accvgpr_write_b32 a180, v192\n"
    "v_mfma_f32_16x16x32_bf16 v[20:23], a[72:75], a[192:195], v[104:107]\n"
    "v_accvgpr_write_b32 a181, v193\n"
    "v_accvgpr_write_b32 a172, v184\n"
    "v_mfma_f32_16x16x32_bf16 v[28:31], a[72:75], a[240:243], v[104:107]\n"
    "s_waitcnt lgkmcnt(6)\n"
    "v_exp_f32 v33, v33\n"
    "v_mfma_f32_16x16x32_bf16 v[4:7], a[52:55], a[196:199], v[4:7]\n"
    "ds_read_b128 a[64:67], v252 offset:32768\n"
    "v_accvgpr_write_b32 a184, v196\n"
    "v_mfma_f32_16x16x32_bf16 v[12:15], a[52:55], a[244:247], v[12:15]\n"
    "s_waitcnt lgkmcnt(6)\n"
    "v_accvgpr_write_b32 a168, v180\n"
    "v_accvgpr_write_b32 a182, v194\n"
    "v_mfma_f32_16x16x32_bf16 v[24:27], a[88:91], a[232:235], v[24:27]\n"
    "s_waitcnt lgkmcnt(3)\n"
    "v_accvgpr_write_b32 a183, v195\n"
    "v_accvgpr_write_b32 a190, v202\n"
    "v_mfma_f32_16x16x32_bf16 v[8:11], a[68:71], a[236:239], v[100:103]\n"
    "buffer_load_dwordx4 v1, s[16:19], s83 offen offset:1024 sc1 lds\n"
    "v_mfma_f32_16x16x32_bf16 v[4:7], a[56:59], a[200:203], v[4:7]\n"
    "v_accvgpr_write_b32 a173, v185\n"
    "v_accvgpr_write_b32 a171, v183\n"
    "v_mfma_f32_16x16x32_bf16 v[12:15], a[56:59], a[248:251], v[12:15]\n"
    "s_add_u32 m0, s64, 0\n"
    "v_accvgpr_write_b32 a176, v188\n"
    "v_accvgpr_write_b32 a189, v201\n"
    "v_accvgpr_write_b32 a169, v181\n"
    "v_mfma_f32_16x16x32_bf16 v[64:67], a[24:27], v[180:183], v[64:67]\n"
    "v_accvgpr_write_b32 a175, v187\n"
    "v_and_b32 v98, 0x80008000, v98\n"
    "v_mfma_f32_16x16x32_bf16 v[20:23], a[76:79], a[196:199], v[20:23]\n"
    "s_waitcnt lgkmcnt(0)\n"
    "v_cmp_ne_u32 s[34:35], 0, v98\n"
    "v_accvgpr_write_b32 a187, v199\n"
    "v_mfma_f32_16x16x32_bf16 v[8:11], a[64:67], a[232:235], v[8:11]\n"
    "s_add_u32 s83, s67, 24576\n"
    "v_accvgpr_write_b32 a185, v197\n"
    "v_accvgpr_write_b32 a178, v190\n"
    "v_mfma_f32_16x16x32_bf16 v[24:27], a[84:87], a[228:231], v[24:27]\n"
    "buffer_load_dwordx4 v1, s[16:19], s83 offen offset:0 sc1 lds\n"
    "v_mfma_f32_16x16x32_bf16 v[12:15], a[60:63], a[252:255], v[12:15]\n"
    "v_exp_f32 v32, v32\n"
    "v_mfma_f32_16x16x32_bf16 v[4:7], a[60:63], a[204:207], v[4:7]\n"
    "v_exp_f32 v18, v18\n"
    "v_mfma_f32_16x16x32_bf16 v[12:15], a[64:67], a[160:163], v[12:15]\n"
    "v_exp_f32 v48, v48\n"
    "v_mfma_f32_16x16x32_bf16 v[4:7], a[64:67], a[208:211], v[4:7]\n"
    "v_exp_f32 v19, v19\n"
    "v_mfma_f32_16x16x32_bf16 v[4:7], a[68:71], a[212:215], v[4:7]\n"
    "v_cvt_pk_bf16_f32 v93, v18, v19\n"
    "v_exp_f32 v17, v17\n"
    "v_mfma_f32_16x16x32_bf16 v[28:31], a[76:79], a[244:247], v[28:31]\n"
    "buffer_load_dwordx4 v1, s[16:19], s83 offen offset:1024 sc1 lds\n"
    "v_mfma_f32_16x16x32_bf16 v[24:27], a[80:83], a[224:227], v[24:27]\n"
    "v_exp_f32 v35, v35\n"
    "v_mfma_f32_16x16x32_bf16 v[20:23], a[80:83], a[200:203], v[20:23]\n"
    "v_exp_f32 v16, v16\n"
    "v_mfma_f32_16x16x32_bf16 v[8:11], a[60:63], a[228:231], v[8:11]\n"
    "v_exp_f32 v34, v34\n"
    "v_mfma_f32_16x16x32_bf16 v[20:23], a[84:87], a[204:207], v[20:23]\n"
    "v_cvt_pk_bf16_f32 v95, v34, v35\n"
    "v_mfma_f32_16x16x32_bf16 v[8:11], a[56:59], a[224:227], v[8:11]\n"
    "v_exp_f32 v66, v66\n"
    "v_mfma_f32_16x16x32_bf16 v[24:27], a[76:79], a[220:223], v[24:27]\n"
    "v_cvt_pk_bf16_f32 v94, v32, v33\n"
    "v_mfma_f32_16x16x32_bf16 v[8:11], a[52:55], a[220:223], v[8:11]\n"
    "buffer_load_dwordx4 v1, s[16:19], s83 offen offset:2048 sc1 lds\n"
    "v_mfma_f32_16x16x32_bf16 v[32:35], a[92:95], a[188:191], v[104:107]\n"
    "v_exp_f32 v49, v49\n"
    "v_mfma_f32_16x16x32_bf16 v[12:15], a[68:71], a[164:167], v[12:15]\n"
    "v_exp_f32 v67, v67\n"
    "v_mfma_f32_16x16x32_bf16 v[32:35], a[88:91], a[184:187], v[32:35]\n"
    "s_waitcnt vmcnt(9)\n"
    "s_barrier\n"
    "ds_read_b128 a[116:119], v248 offset:12288\n"
    "v_mfma_f32_16x16x32_bf16 v[8:11], a[48:51], a[216:219], v[8:11]\n"
    "ds_read_b128 a[144:147], v248 offset:4096\n"
    "v_cvt_pk_bf16_f32 v99, v66, v67\n"
    "v_mfma_f32_16x16x32_bf16 v[32:35], a[84:87], a[180:183], v[32:35]\n"
    "ds_read_b128 a[124:127], v248 offset:13312\n"
    "v_cvt_pk_bf16_f32 v92, v16, v17\n"
    "v_mfma_f32_16x16x32_bf16 v[16:19], a[68:71], a[188:191], v[100:103]\n"
    "buffer_load_dwordx4 v1, s[16:19], s83 offen offset:3072 sc1 lds\n"
    "v_mfma_f32_16x16x32_bf16 v[20:23], a[88:91], a[208:211], v[20:23]\n"
    "s_add_u32 s83, s67, 28672\n"
    "ds_read_b128 a[136:139], v248 offset:3072\n"
    "ds_read_b128 a[140:143], v248 offset:15360\n"
    "v_mfma_f32_16x16x32_bf16 v[32:35], a[80:83], a[176:179], v[32:35]\n"
    "ds_read_b128 a[120:123], v248 offset:1024\n"
    "ds_read_b128 a[148:151], v248 offset:16384\n"
    "v_mfma_f32_16x16x32_bf16 v[28:31], a[80:83], a[248:251], v[28:31]\n"
    "v_exp_f32 v64, v64\n"
    "v_mfma_f32_16x16x32_bf16 v[28:31], a[84:87], a[252:255], v[28:31]\n"
    "s_add_u32 m0, s64, 4096\n"
    "v_exp_f32 v50, v50\n"
    "v_mfma_f32_16x16x32_bf16 v[16:19], a[64:67], a[184:187], v[16:19]\n"
    "v_cvt_pk_bf16_f32 v96, v48, v49\n"
    "v_mfma_f32_16x16x32_bf16 v[16:19], a[60:63], a[180:183], v[16:19]\n"
    "ds_read_b128 a[152:155], v248 offset:5120\n"
    "ds_read_b128 a[132:135], v248 offset:14336\n"
    "v_mfma_f32_16x16x32_bf16 v[32:35], a[76:79], a[172:175], v[32:35]\n"
    "s_waitcnt lgkmcnt(7)\n"
    "buffer_load_dwordx4 v1, s[16:19], s83 offen offset:0 sc1 lds\n"
    "v_mfma_f32_16x16x32_bf16 a[32:35], a[144:147], v[92:95], 0\n"
    "ds_read_b128 a[128:131], v248 offset:2048\n"
    "v_smfmac_f32_16x16x64_bf16 v[112:115], v[52:55], v[76:83], v253\n"
    "v_mfma_f32_16x16x32_bf16 v[24:27], a[72:75], a[216:219], v[24:27]\n"
    "s_waitcnt lgkmcnt(5)\n"
    "v_exp_f32 v65, v65\n"
    "v_mfma_f32_16x16x32_bf16 v[152:155], a[140:143], v[80:83], 0\n"
    "s_waitcnt lgkmcnt(4)\n"
    "v_mfma_f32_16x16x32_bf16 v[144:147], a[120:123], v[76:79], 0\n"
    "v_exp_f32 v51, v51\n"
    "v_mfma_f32_16x16x32_bf16 v[16:19], a[56:59], a[176:179], v[16:19]\n"
    "s_waitcnt lgkmcnt(3)\n"
    "buffer_load_dwordx4 v1, s[16:19], s83 offen offset:1024 sc1 lds\n"
    "v_mfma_f32_16x16x32_bf16 v[188:191], a[148:151], v[72:75], 0\n"
    "s_waitcnt lgkmcnt(1)\n"
    "v_cvt_pk_bf16_f32 v97, v50, v51\n"
    "v_cvt_pk_bf16_f32 v98, v64, v65\n"
    "v_mfma_f32_16x16x32_bf16 v[148:151], a[132:135], v[80:83], 0\n"
    "s_waitcnt lgkmcnt(0)\n"
    "ds_read_b128 a[112:115], v248 offset:0\n"
    "v_mfma_f32_16x16x32_bf16 v[148:151], a[128:131], v[76:79], v[148:151]\n"
    "v_exp_f32 v4, v4\n"
    "v_mfma_f32_16x16x32_bf16 v[180:183], a[132:135], v[96:99], 0\n"
    "v_mfma_f32_16x16x32_bf16 v[176:179], a[120:123], v[92:95], 0\n"
    "v_exp_f32 v14, v14\n"
    "v_mfma_f32_16x16x32_bf16 v[28:31], a[88:91], a[160:163], v[28:31]\n"
    "v_exp_f32 v10, v10\n"
    "v_mfma_f32_16x16x32_bf16 v[164:167], a[128:131], v[84:87], 0\n"
    "buffer_load_dwordx2 v[204:205], v242, s[28:31], s75 offen offset:0 nt\n"
    "v_mfma_f32_16x16x32_bf16 v[176:179], a[124:127], v[96:99], v[176:179]\n"
    "v_mfma_f32_16x16x32_bf16 v[20:23], a[92:95], a[212:215], v[20:23]\n"
    "buffer_load_dwordx2 v[206:207], v242, s[28:31], s75 offen offset:768 nt\n"
    "v_mfma_f32_16x16x32_bf16 v[16:19], a[52:55], a[172:175], v[16:19]\n"
    "v_mfma_f32_16x16x32_bf16 v[132:135], a[128:131], v[68:71], 0\n"
    "buffer_load_dwordx2 v[208:209], v242, s[28:31], s75 offen offset:3072 nt\n"
    "v_mfma_f32_16x16x32_bf16 v[136:139], a[136:139], v[68:71], 0\n"
    "v_mfma_f32_16x16x32_bf16 v[16:19], a[48:51], a[168:171], v[16:19]\n"
    "buffer_load_dwordx2 v[210:211], v242, s[28:31], s75 offen offset:3840 nt\n"
    "v_mfma_f32_16x16x32_bf16 v[180:183], a[128:131], v[92:95], v[180:183]\n"
    "v_mfma_f32_16x16x32_bf16 v[184:187], a[140:143], v[96:99], 0\n"
    "s_waitcnt lgkmcnt(0)\n"
    "buffer_load_dwordx2 v[212:213], v242, s[28:31], s76 offen offset:0 nt\n"
    "v_mfma_f32_16x16x32_bf16 v[156:159], a[112:115], v[84:87], 0\n"
    "v_mfma_f32_16x16x32_bf16 a[16:19], a[148:151], v[88:91], 0\n"
    "buffer_load_dwordx2 v[214:215], v242, s[28:31], s76 offen offset:768 nt\n"
    "v_mfma_f32_16x16x32_bf16 v[168:171], a[136:139], v[84:87], 0\n"
    "v_exp_f32 v22, v22\n"
    "v_mfma_f32_16x16x32_bf16 v[152:155], a[136:139], v[76:79], v[152:155]\n"
    "buffer_load_dwordx2 v[216:217], v242, s[28:31], s76 offen offset:3072 nt\n"
    "v_mfma_f32_16x16x32_bf16 v[160:163], a[124:127], v[88:91], 0\n"
    "v_mfma_f32_16x16x32_bf16 v[140:143], a[116:119], v[80:83], 0\n"
    "buffer_load_dwordx2 v[218:219], v242, s[28:31], s76 offen offset:3840 nt\n"
    "v_mfma_f32_16x16x32_bf16 v[132:135], a[132:135], v[72:75], v[132:135]\n"
    "ds_read_b128 v[100:103], v250 offset:896\n"
    "ds_read_b128 a[128:131], v248 offset:8192\n"
    "v_mfma_f32_16x16x32_bf16 a[16:19], a[144:147], v[84:87], a[16:19]\n"
    "buffer_load_dwordx2 v[220:221], v242, s[28:31], s77 offen offset:0 nt\n"
    "v_mfma_f32_16x16x32_bf16 v[140:143], a[112:115], v[76:79], v[140:143]\n"
    "v_mfma_f32_16x16x32_bf16 v[128:131], a[124:127], v[72:75], 0\n"
    "buffer_load_dwordx2 v[222:223], v242, s[28:31], s77 offen offset:768 nt\n"
    "v_mfma_f32_16x16x32_bf16 a[0:3], a[144:147], v[76:79], 0\n"
    "v_exp_f32 v8, v8\n"
    "v_mfma_f32_16x16x32_bf16 a[20:23], a[152:155], v[84:87], 0\n"
    "buffer_load_dwordx2 v[224:225], v242, s[28:31], s77 offen offset:3072 nt\n"
    "v_mfma_f32_16x16x32_bf16 v[188:191], a[144:147], v[68:71], v[188:191]\n"
    "v_mfma_f32_16x16x32_bf16 v[144:147], a[124:127], v[80:83], v[144:147]\n"
    "buffer_load_dwordx2 v[226:227], v242, s[28:31], s77 offen offset:3840 nt\n"
    "v_mfma_f32_16x16x32_bf16 a[0:3], a[148:151], v[80:83], a[0:3]\n"
    "v_exp_f32 v15, v15\n"
    "v_mfma_f32_16x16x32_bf16 v[172:175], a[116:119], v[96:99], 0\n"
    "s_waitcnt lgkmcnt(0)\n"
    "buffer_load_dwordx2 v[228:229], v242, s[28:31], s78 offen offset:0 nt\n"
    "v_mfma_f32_16x16x32_bf16 a[48:51], a[128:131], v[68:71], 0\n"
    "ds_read_b128 a[156:159], v248 offset:17408\n"
    "v_mfma_f32_16x16x32_bf16 v[164:167], a[132:135], v[88:91], v[164:167]\n"
    "buffer_load_dwordx2 v[230:231], v242, s[28:31], s78 offen offset:768 nt\n"
    "v_mfma_f32_16x16x32_bf16 v[124:127], a[112:115], v[68:71], 0\n"
    "v_mfma_f32_16x16x32_bf16 v[172:175], a[112:115], v[92:95], v[172:175]\n"
    "buffer_load_dwordx2 v[232:233], v242, s[28:31], s78 offen offset:3072 nt\n"
    "v_mfma_f32_16x16x32_bf16 a[32:35], a[148:151], v[96:99], a[32:35]\n"
    "v_mfma_f32_16x16x32_bf16 v[128:131], a[120:123], v[68:71], v[128:131]\n"
    "buffer_load_dwordx2 v[234:235], v242, s[28:31], s78 offen offset:3840 nt\n"
    "v_mfma_f32_16x16x32_bf16 v[192:195], a[152:155], v[68:71], 0\n"
    "ds_read_b128 a[132:135], v248 offset:20480\n"
    "ds_read_b128 a[112:115], v248 offset:6144\n"
    "v_mfma_f32_16x16x32_bf16 v[160:163], a[120:123], v[84:87], v[160:163]\n"
    "s_waitcnt lgkmcnt(2)\n"
    "v_mfma_f32_16x16x32_bf16 a[36:39], a[156:159], v[96:99], 0\n"
    "ds_read_b128 a[144:147], v248 offset:10240\n"
    "ds_read_b128 a[124:127], v248 offset:19456\n"
    "v_mfma_f32_16x16x32_bf16 v[32:35], a[72:75], a[168:171], v[32:35]\n"
    "v_exp_f32 v7, v7\n"
    "v_mfma_f32_16x16x32_bf16 v[192:195], a[156:159], v[72:75], v[192:195]\n"
    "v_exp_f32 v21, v21\n"
    "v_mfma_f32_16x16x32_bf16 v[184:187], a[136:139], v[92:95], v[184:187]\n"
    "ds_read_b128 a[120:123], v248 offset:7168\n"
    "v_mfma_f32_16x16x32_bf16 v[28:31], a[92:95], a[164:167], v[28:31]\n"
    "v_mfma_f32_16x16x32_bf16 v[168:171], a[140:143], v[88:91], v[168:171]\n"
    "ds_read_b128 a[148:151], v248 offset:22528\n"
    "ds_read_b128 a[136:139], v248 offset:9216\n"
    "v_mfma_f32_16x16x32_bf16 a[4:7], a[156:159], v[80:83], 0\n"
    "v_exp_f32 v33, v33\n"
    "v_mfma_f32_16x16x32_bf16 a[36:39], a[152:155], v[92:95], a[36:39]\n"
    "v_mfma_f32_16x16x32_bf16 v[124:127], a[116:119], v[72:75], v[124:127]\n"
    "v_exp_f32 v16, v16\n"
    "v_mfma_f32_16x16x32_bf16 a[4:7], a[152:155], v[76:79], a[4:7]\n"
    "v_exp_f32 v5, v5\n"
    "v_mfma_f32_16x16x32_bf16 v[136:139], a[140:143], v[72:75], v[136:139]\n"
    "ds_read_b128 a[140:143], v248 offset:21504\n"
    "ds_read_b128 a[152:155], v248 offset:11264\n"
    "v_mfma_f32_16x16x32_bf16 v[156:159], a[116:119], v[88:91], v[156:159]\n"
    "v_smfmac_f32_16x16x64_bf16 v[120:123], v[52:55], v[92:99], v253\n"
    "s_waitcnt lgkmcnt(8)\n"
    "v_mfma_f32_16x16x32_bf16 a[64:67], a[132:135], v[80:83], 0\n"
    "s_waitcnt lgkmcnt(6)\n"
    "v_mfma_f32_16x16x32_bf16 a[104:107], a[144:147], v[92:95], 0\n"
    "s_waitcnt lgkmcnt(2)\n"
    "v_exp_f32 v20, v20\n"
    "v_mfma_f32_16x16x32_bf16 a[52:55], a[136:139], v[68:71], 0\n"
    "ds_read_b128 a[116:119], v248 offset:18432\n"
    "v_mfma_f32_16x16x32_bf16 a[20:23], a[156:159], v[88:91], a[20:23]\n"
    "ds_read_b128 a[156:159], v248 offset:23552\n"
    "v_mfma_f32_16x16x32_bf16 v[196:199], a[112:115], v[68:71], 0\n"
    "buffer_load_dwordx2 v[236:237], v241, s[36:39], s81 offen offset:0 sc1\n"
    "v_mfma_f32_16x16x32_bf16 v[200:203], a[124:127], v[72:75], 0\n"
    "s_waitcnt lgkmcnt(2)\n"
    "v_exp_f32 v27, v27\n"
    "v_mfma_f32_16x16x32_bf16 a[60:63], a[152:155], v[68:71], 0\n"
    "v_mfma_f32_16x16x32_bf16 a[96:99], a[132:135], v[96:99], 0\n"
    "v_exp_f32 v17, v17\n"
    "v_mfma_f32_16x16x32_bf16 a[72:75], a[144:147], v[76:79], 0\n"
    "v_exp_f32 v24, v24\n"
    "v_mfma_f32_16x16x32_bf16 a[24:27], a[112:115], v[84:87], 0\n"
    "v_exp_f32 v35, v35\n"
    "v_mfma_f32_16x16x32_bf16 a[64:67], a[128:131], v[76:79], a[64:67]\n"
    "v_mfma_f32_16x16x32_bf16 a[80:83], a[128:131], v[84:87], 0\n"
    "s_add_u32 s67, s67, 49152\n"
    "v_exp_f32 v32, v32\n"
    "v_mfma_f32_16x16x32_bf16 a[12:15], a[120:123], v[76:79], 0\n"
    "v_mfma_f32_16x16x32_bf16 a[104:107], a[148:151], v[96:99], a[104:107]\n"
    "v_mov_b32 v2, v3\n"
    "v_mfma_f32_16x16x32_bf16 a[28:31], a[124:127], v[88:91], 0\n"
    "v_mfma_f32_16x16x32_bf16 a[28:31], a[120:123], v[84:87], a[28:31]\n"
    "v_mfma_f32_16x16x32_bf16 a[92:95], a[152:155], v[84:87], 0\n"
    "v_mfma_f32_16x16x32_bf16 v[200:203], a[120:123], v[68:71], v[200:203]\n"
    "s_waitcnt lgkmcnt(0)\n"
    "v_exp_f32 v12, v12\n"
    "v_mfma_f32_16x16x32_bf16 a[108:111], a[156:159], v[96:99], 0\n"
    "v_exp_f32 v18, v18\n"
    "v_mfma_f32_16x16x32_bf16 a[80:83], a[132:135], v[88:91], a[80:83]\n"
    "v_mfma_f32_16x16x32_bf16 a[44:47], a[120:123], v[92:95], 0\n"
    "v_exp_f32 v29, v29\n"
    "v_mfma_f32_16x16x32_bf16 a[12:15], a[124:127], v[80:83], a[12:15]\n"
    "ds_read_b128 a[120:123], v3 offset:16384\n"
    "v_mfma_f32_16x16x32_bf16 a[44:47], a[124:127], v[96:99], a[44:47]\n"
    "v_mfma_f32_16x16x32_bf16 a[56:59], a[148:151], v[72:75], 0\n"
    "v_exp_f32 v28, v28\n"
    "v_mfma_f32_16x16x32_bf16 a[96:99], a[128:131], v[92:95], a[96:99]\n"
    "v_exp_f32 v19, v19\n"
    "v_mfma_f32_16x16x32_bf16 a[40:43], a[116:119], v[96:99], 0\n"
    "v_exp_f32 v31, v31\n"
    "v_mfma_f32_16x16x32_bf16 a[60:63], a[156:159], v[72:75], a[60:63]\n"
    "v_exp_f32 v26, v26\n"
    "v_mfma_f32_16x16x32_bf16 a[72:75], a[148:151], v[80:83], a[72:75]\n"
    "v_mfma_f32_16x16x32_bf16 a[8:11], a[116:119], v[80:83], 0\n"
    "v_exp_f32 v6, v6\n"
    "v_mfma_f32_16x16x32_bf16 a[68:71], a[140:143], v[80:83], 0\n"
    "v_exp_f32 v25, v25\n"
    "v_mfma_f32_16x16x32_bf16 a[48:51], a[132:135], v[72:75], a[48:51]\n"
    "ds_read_b128 a[124:127], v3 offset:18432\n"
    "v_mfma_f32_16x16x32_bf16 a[40:43], a[112:115], v[92:95], a[40:43]\n"
    "ds_read_b128 a[132:135], v3 offset:22528\n"
    "v_mfma_f32_16x16x32_bf16 a[100:103], a[140:143], v[96:99], 0\n"
    "v_mfma_f32_16x16x32_bf16 v[196:199], a[116:119], v[72:75], v[196:199]\n"
    "v_exp_f32 v30, v30\n"
    "v_mfma_f32_16x16x32_bf16 a[108:111], a[152:155], v[92:95], a[108:111]\n"
    "v_mfma_f32_16x16x32_bf16 a[88:91], a[148:151], v[88:91], 0\n"
    "s_waitcnt vmcnt(17)\n"
    "v_exp_f32 v11, v11\n"
    "v_mfma_f32_16x16x32_bf16 a[8:11], a[112:115], v[76:79], a[8:11]\n"
    "ds_read_b128 a[128:131], v3 offset:20480\n"
    "ds_read_b128 a[112:115], v3 offset:12288\n"
    "v_mfma_f32_16x16x32_bf16 a[76:79], a[156:159], v[80:83], 0\n"
    "ds_add_u32 v247, v246\n"
    "v_mfma_f32_16x16x32_bf16 a[24:27], a[116:119], v[88:91], a[24:27]\n"
    "ds_read_b128 a[116:119], v3 offset:14336\n"
    "v_mfma_f32_16x16x32_bf16 a[56:59], a[144:147], v[68:71], a[56:59]\n"
    "v_exp_f32 v9, v9\n"
    "v_mfma_f32_16x16x32_bf16 a[84:87], a[136:139], v[84:87], 0\n"
    "v_exp_f32 v23, v23\n"
    "v_mfma_f32_16x16x32_bf16 a[88:91], a[144:147], v[84:87], a[88:91]\n"
    "v_add_u32 v3, 49152, v252\n"
    "v_exp_f32 v34, v34\n"
    "v_mfma_f32_16x16x32_bf16 a[84:87], a[140:143], v[88:91], a[84:87]\n"
    "v_mfma_f32_16x16x32_bf16 a[100:103], a[136:139], v[92:95], a[100:103]\n"
    "v_exp_f32 v13, v13\n"
    "v_mfma_f32_16x16x32_bf16 a[76:79], a[152:155], v[76:79], a[76:79]\n"
    "v_mfma_f32_16x16x32_bf16 a[68:71], a[136:139], v[76:79], a[68:71]\n"
    "s_waitcnt lgkmcnt(0)\n"
    "v_mfma_f32_16x16x32_bf16 a[92:95], a[156:159], v[88:91], a[92:95]\n"
    "v_mfma_f32_16x16x32_bf16 a[52:55], a[140:143], v[72:75], a[52:55]\n"
    "s_branch .LNK\n"
    ".fill ((0x20000 - (. - _Z8attn_fwdPKtPKiS2_S0_S0_S0_S0_PcPt)) & 4095) / 4, 4, 0xBF800000\n"
    ".LhKN:\n"
    "s_waitcnt vmcnt(28)\n"
    "ds_read_b128 v[52:55], v251 offset:768\n"
    "ds_read_b128 v[180:183], v96 offset:49152\n"
    "ds_read_b128 v[200:203], v97 offset:49408\n"
    "ds_read_b128 v[184:187], v97 offset:49152\n"
    "s_add_u32 m0, s68, 0\n"
    "ds_read_b128 v[196:199], v96 offset:49408\n"
    "ds_read_b128 v[188:191], v96 offset:49280\n"
    "ds_read_b128 v[192:195], v97 offset:49280\n"
    "s_waitcnt lgkmcnt(6)\n"
    "v_smfmac_f32_16x16x64_bf16 v[108:111], v[52:55], v[68:75], v253\n"
    "s_waitcnt lgkmcnt(4)\n"
    "v_pk_maximum3_f16 v98, v200, v201, v98\n"
    "v_pk_maximum3_f16 v98, v202, v203, v98\n"
    "v_pk_add_u16 v200, s89, v200 clamp\n"
    "v_pk_add_u16 v203, s89, v203 clamp\n"
    "v_smfmac_f32_16x16x64_bf16 v[116:119], v[52:55], v[84:91], v253\n"
    "s_waitcnt lgkmcnt(2)\n"
    "s_nop 0\n"
    "v_pk_add_u16 v202, s89, v202 clamp\n"
    "v_pk_add_u16 v201, s89, v201 clamp\n"
    "v_pk_maximum3_f16 v98, v196, v197, v98\n"
    "v_pk_maximum3_f16 v98, v198, v199, v98\n"
    "v_mfma_f32_16x16x32_bf16 v[64:67], a[44:47], v[200:203], v[64:67]\n"
    "v_pk_add_u16 v198, s89, v198 clamp\n"
    "v_pk_add_u16 v196, s89, v196 clamp\n"
    "v_mfma_f32_16x16x32_bf16 v[48:51], a[20:23], v[200:203], v[48:51]\n"
    "v_pk_add_u16 v199, s89, v199 clamp\n"
    "v_pk_add_u16 v197, s89, v197 clamp\n"
    "v_mfma_f32_16x16x32_bf16 v[32:35], a[156:159], v[200:203], v[32:35]\n"
    "s_waitcnt lgkmcnt(0)\n"
    "v_pk_maximum3_f16 v98, v192, v193, v98\n"
    "v_pk_maximum3_f16 v98, v194, v195, v98\n"
    "v_mfma_f32_16x16x32_bf16 v[64:67], a[40:43], v[196:199], v[64:67]\n"
    "v_pk_maximum3_f16 v98, v188, v189, v98\n"
    "v_pk_add_u16 v193, s89, v193 clamp\n"
    "v_mfma_f32_16x16x32_bf16 v[48:51], a[16:19], v[196:199], v[48:51]\n"
    "buffer_load_dwordx4 v1, s[16:19], s67 offen offset:0 sc1 lds\n"
    "v_mfma_f32_16x16x32_bf16 v[16:19], a[132:135], v[200:203], v[16:19]\n"
    "v_pk_add_u16 v194, s89, v194 clamp\n"
    "v_pk_maximum3_f16 v98, v190, v191, v98\n"
    "v_pk_add_u16 v192, s89, v192 clamp\n"
    "v_smfmac_f32_16x16x64_bf16 v[112:115], v[52:55], v[76:83], v253\n"
    "v_pk_add_u16 v191, s89, v191 clamp\n"
    "v_mfma_f32_16x16x32_bf16 v[16:19], a[128:131], v[196:199], v[16:19]\n"
    "v_pk_add_u16 v195, s89, v195 clamp\n"
    "v_pk_maximum3_f16 v98, v184, v185, v98\n"
    "v_mfma_f32_16x16x32_bf16 v[32:35], a[152:155], v[196:199], v[32:35]\n"
    "v_pk_add_u16 v185, s89, v185 clamp\n"
    "v_pk_add_u16 v188, s89, v188 clamp\n"
    "v_mfma_f32_16x16x32_bf16 v[64:67], a[36:39], v[192:195], v[64:67]\n"
    "v_pk_add_u16 v189, s89, v189 clamp\n"
    "v_pk_maximum3_f16 v98, v186, v187, v98\n"
    "v_mfma_f32_16x16x32_bf16 v[32:35], a[148:151], v[192:195], v[32:35]\n"
    "buffer_load_dwordx4 v1, s[16:19], s67 offen offset:1024 sc1 lds\n"
    "v_mfma_f32_16x16x32_bf16 v[16:19], a[124:127], v[192:195], v[16:19]\n"
    "v_pk_add_u16 v190, s89, v190 clamp\n"
    "v_pk_add_u16 v184, s89, v184 clamp\n"
    "v_pk_maximum3_f16 v98, v180, v181, v98\n"
    "v_mfma_f32_16x16x32_bf16 v[32:35], a[144:147], v[188:191], v[32:35]\n"
    "v_pk_add_u16 v187, s89, v187 clamp\n"
    "v_pk_add_u16 v186, s89, v186 clamp\n"
    "v_mfma_f32_16x16x32_bf16 v[48:51], a[12:15], v[192:195], v[48:51]\n"
    "s_waitcnt vmcnt(24)\n"
    "v_pk_maximum3_f16 v98, v182, v183, v98\n"
    "v_pk_add_u16 v182, s89, v182 clamp\n"
    "v_mfma_f32_16x16x32_bf16 v[48:51], a[8:11], v[188:191], v[48:51]\n"
    "s_barrier\n"
    "v_pk_add_u16 v180, s89, v180 clamp\n"
    "v_mfma_f32_16x16x32_bf16 v[64:67], a[32:35], v[188:191], v[64:67]\n"
    "ds_read_b128 a[72:75], v252 offset:25600\n"
    "v_pk_add_u16 v183, s89, v183 clamp\n"
    "v_mfma_f32_16x16x32_bf16 v[32:35], a[140:143], v[184:187], v[32:35]\n"
    "ds_read_b128 a[48:51], v252 offset:24576\n"
    "v_pk_add_u16 v181, s89, v181 clamp\n"
    "v_mfma_f32_16x16x32_bf16 v[16:19], a[120:123], v[188:191], v[16:19]\n"
    "buffer_load_dwordx4 v1, s[16:19], s67 offen offset:2048 sc1 lds\n"
    "v_mfma_f32_16x16x32_bf16 v[32:35], a[136:139], v[180:183], v[32:35]\n"
    "v_accvgpr_write_b32 a187, v199\n"
    "v_accvgpr_write_b32 a186, v198\n"
    "v_mfma_f32_16x16x32_bf16 v[64:67], a[28:31], v[184:187], v[64:67]\n"
    "ds_read_b128 a[92:95], v252 offset:35840\n"
    "ds_read_b128 a[64:67], v252 offset:32768\n"
    "v_mfma_f32_16x16x32_bf16 v[48:51], a[4:7], v[184:187], v[48:51]\n"
    "s_add_u32 s83, s67, 4096\n"
    "ds_read_b128 a[68:71], v252 offset:34816\n"
    "ds_read_b128 a[88:91], v252 offset:33792\n"
    "v_mfma_f32_16x16x32_bf16 v[16:19], a[116:119], v[184:187], v[16:19]\n"
    "v_accvgpr_write_b32 a178, v190\n"
    "v_accvgpr_write_b32 a173, v185\n"
    "v_mfma_f32_16x16x32_bf16 v[64:67], a[24:27], v[180:183], v[64:67]\n"
    "ds_read_b128 a[52:55], v252 offset:26624\n"
    "ds_read_b128 a[56:59], v252 offset:28672\n"
    "v_mfma_f32_16x16x32_bf16 v[48:51], a[0:3], v[180:183], v[48:51]\n"
    "ds_read_b128 a[84:87], v252 offset:31744\n"
    "v_accvgpr_write_b32 a177, v189\n"
    "v_mfma_f32_16x16x32_bf16 v[16:19], a[112:115], v[180:183], v[16:19]\n"
    "s_waitcnt lgkmcnt(8)\n"
    "buffer_load_dwordx4 v1, s[16:19], s67 offen offset:3072 sc1 lds\n"
    "v_mfma_f32_16x16x32_bf16 v[28:31], a[72:75], a[240:243], v[104:107]\n"
    "s_waitcnt lgkmcnt(7)\n"
    "ds_read_b128 a[60:63], v252 offset:30720\n"
    "v_accvgpr_write_b32 a170, v182\n"
    "v_mfma_f32_16x16x32_bf16 v[4:7], a[48:51], a[192:195], v[100:103]\n"
    "ds_read_b128 a[76:79], v252 offset:27648\n"
    "ds_read_b128 a[80:83], v252 offset:29696\n"
    "v_mfma_f32_16x16x32_bf16 v[12:15], a[48:51], a[240:243], v[100:103]\n"
    "s_waitcnt lgkmcnt(9)\n"
    "v_exp_f32 v16, v16\n"
    "v_mfma_f32_16x16x32_bf16 v[24:27], a[92:95], a[236:239], v[104:107]\n"
    "s_waitcnt lgkmcnt(6)\n"
    "v_accvgpr_write_b32 a183, v195\n"
    "v_accvgpr_write_b32 a190, v202\n"
    "v_mfma_f32_16x16x32_bf16 v[24:27], a[88:91], a[232:235], v[24:27]\n"
    "v_exp_f32 v17, v17\n"
    "v_mfma_f32_16x16x32_bf16 v[20:23], a[72:75], a[192:195], v[104:107]\n"
    "s_waitcnt lgkmcnt(5)\n"
    "v_accvgpr_write_b32 a172, v184\n"
    "v_accvgpr_write_b32 a168, v180\n"
    "v_mfma_f32_16x16x32_bf16 v[4:7], a[52:55], a[196:199], v[4:7]\n"
    "s_add_u32 m0, s68, 4096\n"
    "v_exp_f32 v34, v34\n"
    "v_mfma_f32_16x16x32_bf16 v[12:15], a[52:55], a[244:247], v[12:15]\n"
    "buffer_load_dwordx4 v1, s[16:19], s83 offen offset:0 sc1 lds\n"
    "v_mfma_f32_16x16x32_bf16 v[8:11], a[68:71], a[236:239], v[100:103]\n"
    "s_waitcnt lgkmcnt(4)\n"
    "v_accvgpr_write_b32 a182, v194\n"
    "v_accvgpr_write_b32 a189, v201\n"
    "v_mfma_f32_16x16x32_bf16 v[4:7], a[56:59], a[200:203], v[4:7]\n"
    "s_waitcnt lgkmcnt(2)\n"
    "v_accvgpr_write_b32 a175, v187\n"
    "v_accvgpr_write_b32 a180, v192\n"
    "v_mfma_f32_16x16x32_bf16 v[4:7], a[60:63], a[204:207], v[4:7]\n"
    "s_waitcnt lgkmcnt(1)\n"
    "v_accvgpr_write_b32 a176, v188\n"
    "v_pk_add_u16 v98, s89, v98\n"
    "v_mfma_f32_16x16x32_bf16 v[28:31], a[76:79], a[244:247], v[28:31]\n"
    "v_accvgpr_write_b32 a188, v200\n"
    "v_accvgpr_write_b32 a191, v203\n"
    "v_mfma_f32_16x16x32_bf16 v[20:23], a[76:79], a[196:199], v[20:23]\n"
    "s_waitcnt lgkmcnt(0)\n"
    "v_accvgpr_write_b32 a184, v196\n"
    "v_mfma_f32_16x16x32_bf16 v[20:23], a[80:83], a[200:203], v[20:23]\n"
    "v_accvgpr_write_b32 a181, v193\n"
    "v_mfma_f32_16x16x32_bf16 v[12:15], a[56:59], a[248:251], v[12:15]\n"
    "buffer_load_dwordx4 v1, s[16:19], s83 offen offset:1024 sc1 lds\n"
    "v_mfma_f32_16x16x32_bf16 v[20:23], a[84:87], a[204:207], v[20:23]\n"
    "v_accvgpr_write_b32 a171, v183\n"
    "v_accvgpr_write_b32 a185, v197\n"
    "v_mfma_f32_16x16x32_bf16 v[4:7], a[64:67], a[208:211], v[4:7]\n"
    "s_add_u32 m0, s70, 0\n"
    "v_accvgpr_write_b32 a179, v191\n"
    "v_accvgpr_write_b32 a169, v181\n"
    "v_mfma_f32_16x16x32_bf16 v[4:7], a[68:71], a[212:215], v[4:7]\n"
    "s_add_u32 s83, s67, 24576\n"
    "v_mfma_f32_16x16x32_bf16 v[28:31], a[80:83], a[248:251], v[28:31]\n"
    "v_exp_f32 v19, v19\n"
    "v_mfma_f32_16x16x32_bf16 v[8:11], a[64:67], a[232:235], v[8:11]\n"
    "v_accvgpr_write_b32 a174, v186\n"
    "v_mfma_f32_16x16x32_bf16 v[24:27], a[84:87], a[228:231], v[24:27]\n"
    "v_exp_f32 v33, v33\n"
    "v_mfma_f32_16x16x32_bf16 v[20:23], a[88:91], a[208:211], v[20:23]\n"
    "buffer_load_dwordx4 v1, s[16:19], s83 offen offset:0 sc1 lds\n"
    "v_mfma_f32_16x16x32_bf16 v[28:31], a[84:87], a[252:255], v[28:31]\n"
    "v_exp_f32 v32, v32\n"
    "v_mfma_f32_16x16x32_bf16 v[28:31], a[88:91], a[160:163], v[28:31]\n"
    "v_exp_f32 v18, v18\n"
    "v_mfma_f32_16x16x32_bf16 v[24:27], a[80:83], a[224:227], v[24:27]\n"
    "v_exp_f32 v49, v49\n"
    "v_mfma_f32_16x16x32_bf16 v[8:11], a[60:63], a[228:231], v[8:11]\n"
    "v_exp_f32 v35, v35\n"
    "v_mfma_f32_16x16x32_bf16 v[24:27], a[76:79], a[220:223], v[24:27]\n"
    "v_cvt_pk_bf16_f32 v92, v16, v17\n"
    "v_cvt_pk_bf16_f32 v93, v18, v19\n"
    "v_mfma_f32_16x16x32_bf16 v[12:15], a[60:63], a[252:255], v[12:15]\n"
    "v_and_b32 v98, 0x80008000, v98\n"
    "v_cvt_pk_bf16_f32 v94, v32, v33\n"
    "v_mfma_f32_16x16x32_bf16 v[24:27], a[72:75], a[216:219], v[24:27]\n"
    "buffer_load_dwordx4 v1, s[16:19], s83 offen offset:1024 sc1 lds\n"
    "v_mfma_f32_16x16x32_bf16 v[16:19], a[68:71], a[188:191], v[100:103]\n"
    "v_cvt_pk_bf16_f32 v95, v34, v35\n"
    "v_mfma_f32_16x16x32_bf16 v[16:19], a[64:67], a[184:187], v[16:19]\n"
    "s_waitcnt vmcnt(24)\n"
    "s_barrier\n"
    "v_mfma_f32_16x16x32_bf16 v[8:11], a[56:59], a[224:227], v[8:11]\n"
    "ds_read_b128 a[124:127], v249 offset:13312\n"
    "ds_read_b128 a[120:123], v249 offset:1024\n"
    "v_mfma_f32_16x16x32_bf16 v[32:35], a[92:95], a[188:191], v[104:107]\n"
    "ds_read_b128 a[116:119], v249 offset:12288\n"
    "ds_read_b128 a[112:115], v249 offset:0\n"
    "v_mfma_f32_16x16x32_bf16 v[32:35], a[88:91], a[184:187], v[32:35]\n"
    "ds_read_b128 a[132:135], v249 offset:14336\n"
    "ds_read_b128 a[144:147], v249 offset:4096\n"
    "v_mfma_f32_16x16x32_bf16 v[8:11], a[52:55], a[220:223], v[8:11]\n"
    "s_add_u32 s76, s71, s60\n"
    "v_exp_f32 v51, v51\n"
    "v_mfma_f32_16x16x32_bf16 v[12:15], a[64:67], a[160:163], v[12:15]\n"
    "buffer_load_dwordx4 v1, s[16:19], s83 offen offset:2048 sc1 lds\n"
    "v_mfma_f32_16x16x32_bf16 v[28:31], a[92:95], a[164:167], v[28:31]\n"
    "v_exp_f32 v66, v66\n"
    "v_mfma_f32_16x16x32_bf16 v[16:19], a[60:63], a[180:183], v[16:19]\n"
    "v_exp_f32 v50, v50\n"
    "v_mfma_f32_16x16x32_bf16 v[12:15], a[68:71], a[164:167], v[12:15]\n"
    "v_exp_f32 v67, v67\n"
    "v_mfma_f32_16x16x32_bf16 v[8:11], a[48:51], a[216:219], v[8:11]\n"
    "s_add_u32 s77, s71, s61\n"
    "ds_read_b128 a[156:159], v249 offset:17408\n"
    "s_waitcnt lgkmcnt(6)\n"
    "v_mfma_f32_16x16x32_bf16 v[128:131], a[124:127], v[72:75], 0\n"
    "ds_read_b128 a[136:139], v249 offset:3072\n"
    "v_mfma_f32_16x16x32_bf16 v[160:163], a[124:127], v[88:91], 0\n"
    "s_waitcnt lgkmcnt(4)\n"
    "ds_read_b128 a[148:151], v249 offset:16384\n"
    "ds_read_b128 a[128:131], v249 offset:2048\n"
    "v_mfma_f32_16x16x32_bf16 v[156:159], a[112:115], v[84:87], 0\n"
    "buffer_load_dwordx4 v1, s[16:19], s83 offen offset:3072 sc1 lds\n"
    "v_mfma_f32_16x16x32_bf16 v[16:19], a[56:59], a[176:179], v[16:19]\n"
    "s_add_u32 s83, s67, 28672\n"
    "ds_read_b128 a[152:155], v249 offset:5120\n"
    "v_mfma_f32_16x16x32_bf16 v[16:19], a[52:55], a[172:175], v[16:19]\n"
    "s_add_u32 m0, s70, 4096\n"
    "v_exp_f32 v7, v7\n"
    "v_mfma_f32_16x16x32_bf16 v[20:23], a[92:95], a[212:215], v[20:23]\n"
    "v_mfma_f32_16x16x32_bf16 v[156:159], a[116:119], v[88:91], v[156:159]\n"
    "v_exp_f32 v48, v48\n"
    "v_mfma_f32_16x16x32_bf16 v[32:35], a[84:87], a[180:183], v[32:35]\n"
    "s_waitcnt lgkmcnt(6)\n"
    "v_exp_f32 v65, v65\n"
    "v_mfma_f32_16x16x32_bf16 v[148:151], a[132:135], v[80:83], 0\n"
    "ds_read_b128 v[36:39], v243 offset:0\n"
    "v_cmp_ne_u32 s[34:35], 0, v98\n"
    "v_mfma_f32_16x16x32_bf16 v[124:127], a[112:115], v[68:71], 0\n"
    "buffer_load_dwordx4 v1, s[16:19], s83 offen offset:0 sc1 lds\n"
    "v_mfma_f32_16x16x32_bf16 v[176:179], a[120:123], v[92:95], 0\n"
    "s_waitcnt lgkmcnt(3)\n"
    "v_exp_f32 v10, v10\n"
    "v_mfma_f32_16x16x32_bf16 a[16:19], a[148:151], v[88:91], 0\n"
    "v_exp_f32 v64, v64\n"
    "v_mfma_f32_16x16x32_bf16 v[136:139], a[136:139], v[68:71], 0\n"
    "v_exp_f32 v4, v4\n"
    "v_mfma_f32_16x16x32_bf16 v[32:35], a[80:83], a[176:179], v[32:35]\n"
    "ds_read_b128 a[140:143], v249 offset:15360\n"
    "v_add_u32 v251, 1536, v251\n"
    "v_mfma_f32_16x16x32_bf16 v[140:143], a[116:119], v[80:83], 0\n"
    "v_cvt_pk_bf16_f32 v97, v50, v51\n"
    "v_mfma_f32_16x16x32_bf16 v[16:19], a[48:51], a[168:171], v[16:19]\n"
    "s_waitcnt lgkmcnt(3)\n"
    "v_cvt_pk_bf16_f32 v98, v64, v65\n"
    "v_cvt_pk_bf16_f32 v99, v66, v67\n"
    "v_mfma_f32_16x16x32_bf16 v[164:167], a[128:131], v[84:87], 0\n"
    "buffer_load_dwordx4 v1, s[16:19], s83 offen offset:1024 sc1 lds\n"
    "v_mfma_f32_16x16x32_bf16 a[4:7], a[156:159], v[80:83], 0\n"
    "v_exp_f32 v27, v27\n"
    "v_mfma_f32_16x16x32_bf16 v[128:131], a[120:123], v[68:71], v[128:131]\n"
    "s_waitcnt lgkmcnt(2)\n"
    "v_mfma_f32_16x16x32_bf16 v[192:195], a[152:155], v[68:71], 0\n"
    "v_exp_f32 v26, v26\n"
    "v_mfma_f32_16x16x32_bf16 a[32:35], a[144:147], v[92:95], 0\n"
    "ds_read_b128 v[100:103], v250 offset:1664\n"
    "s_waitcnt lgkmcnt(1)\n"
    "v_mfma_f32_16x16x32_bf16 v[152:155], a[140:143], v[80:83], 0\n"
    "v_exp_f32 v22, v22\n"
    "v_mfma_f32_16x16x32_bf16 v[32:35], a[76:79], a[172:175], v[32:35]\n"
    "v_cvt_pk_bf16_f32 v96, v48, v49\n"
    "v_mfma_f32_16x16x32_bf16 v[144:147], a[120:123], v[76:79], 0\n"
    "s_waitcnt vmcnt(12)\n"
    "v_fma_f32 v216, v216, v36, -v38\n"
    "v_fma_f32 v229, v229, v37, -v39\n"
    "v_mfma_f32_16x16x32_bf16 v[160:163], a[120:123], v[84:87], v[160:163]\n"
    "v_fma_f32 v206, v206, v36, -v38\n"
    "v_fma_f32 v213, v213, v37, -v39\n"
    "v_mfma_f32_16x16x32_bf16 v[172:175], a[116:119], v[96:99], 0\n"
    "v_fma_f32 v214, v214, v36, -v38\n"
    "v_mfma_f32_16x16x32_bf16 v[124:127], a[116:119], v[72:75], v[124:127]\n"
    "v_fma_f32 v207, v207, v37, -v39\n"
    "v_fma_f32 v205, v205, v37, -v39\n"
    "v_mfma_f32_16x16x32_bf16 v[176:179], a[124:127], v[96:99], v[176:179]\n"
    "ds_read_b128 a[116:119], v249 offset:18432\n"
    "v_fma_f32 v209, v209, v37, -v39\n"
    "v_mfma_f32_16x16x32_bf16 v[144:147], a[124:127], v[80:83], v[144:147]\n"
    "v_fma_f32 v215, v215, v37, -v39\n"
    "v_fma_f32 v217, v217, v37, -v39\n"
    "v_mfma_f32_16x16x32_bf16 a[4:7], a[152:155], v[76:79], a[4:7]\n"
    "v_exp_f32 v13, v13\n"
    "v_mfma_f32_16x16x32_bf16 v[140:143], a[112:115], v[76:79], v[140:143]\n"
    "v_fma_f32 v212, v212, v36, -v38\n"
    "v_mfma_f32_16x16x32_bf16 v[184:187], a[140:143], v[96:99], 0\n"
    "v_cvt_pk_bf16_f32 v216, v216, v217\n"
    "v_fma_f32 v223, v223, v37, -v39\n"
    "v_mfma_f32_16x16x32_bf16 v[164:167], a[132:135], v[88:91], v[164:167]\n"
    "v_fma_f32 v222, v222, v36, -v38\n"
    "v_fma_f32 v231, v231, v37, -v39\n"
    "v_mfma_f32_16x16x32_bf16 v[188:191], a[148:151], v[72:75], 0\n"
    "v_exp_f32 v21, v21\n"
    "v_mfma_f32_16x16x32_bf16 v[172:175], a[112:115], v[92:95], v[172:175]\n"
    "v_fma_f32 v235, v235, v37, -v39\n"
    "v_fma_f32 v211, v211, v37, -v39\n"
    "v_mfma_f32_16x16x32_bf16 v[132:135], a[128:131], v[68:71], 0\n"
    "ds_read_b128 a[112:115], v249 offset:6144\n"
    "v_mfma_f32_16x16x32_bf16 a[32:35], a[148:151], v[96:99], a[32:35]\n"
    "v_exp_f32 v5, v5\n"
    "v_mfma_f32_16x16x32_bf16 v[180:183], a[132:135], v[96:99], 0\n"
    "ds_read_b128 a[120:123], v249 offset:7168\n"
    "v_cvt_pk_bf16_f32 v206, v206, v207\n"
    "v_mfma_f32_16x16x32_bf16 v[132:135], a[132:135], v[72:75], v[132:135]\n"
    "ds_read_b128 a[124:127], v249 offset:19456\n"
    "v_fma_f32 v221, v221, v37, -v39\n"
    "v_mfma_f32_16x16x32_bf16 v[180:183], a[128:131], v[92:95], v[180:183]\n"
    "ds_read_b128 a[132:135], v249 offset:20480\n"
    "v_fma_f32 v233, v233, v37, -v39\n"
    "v_mfma_f32_16x16x32_bf16 a[0:3], a[144:147], v[76:79], 0\n"
    "v_exp_f32 v12, v12\n"
    "v_mfma_f32_16x16x32_bf16 a[0:3], a[148:151], v[80:83], a[0:3]\n"
    "s_waitcnt lgkmcnt(4)\n"
    "v_fma_f32 v204, v204, v36, -v38\n"
    "v_fma_f32 v218, v218, v36, -v38\n"
    "v_mfma_f32_16x16x32_bf16 a[8:11], a[116:119], v[80:83], 0\n"
    "v_cvt_pk_bf16_f32 v212, v212, v213\n"
    "v_fma_f32 v219, v219, v37, -v39\n"
    "v_mfma_f32_16x16x32_bf16 a[20:23], a[152:155], v[84:87], 0\n"
    "v_cvt_pk_bf16_f32 v204, v204, v205\n"
    "v_fma_f32 v220, v220, v36, -v38\n"
    "v_mfma_f32_16x16x32_bf16 a[36:39], a[156:159], v[96:99], 0\n"
    "v_mfma_f32_16x16x32_bf16 a[16:19], a[144:147], v[84:87], a[16:19]\n"
    "s_waitcnt lgkmcnt(2)\n"
    "buffer_store_dword v204, v244, s[16:19], s76 offen offset:0 sc1\n"
    "v_exp_f32 v16, v16\n"
    "v_mfma_f32_16x16x32_bf16 a[12:15], a[120:123], v[76:79], 0\n"
    "v_mfma_f32_16x16x32_bf16 v[32:35], a[72:75], a[168:171], v[32:35]\n"
    "v_fma_f32 v234, v234, v36, -v38\n"
    "v_fma_f32 v208, v208, v36, -v38\n"
    "v_mfma_f32_16x16x32_bf16 a[36:39], a[152:155], v[92:95], a[36:39]\n"
    "buffer_store_dword v206, v244, s[16:19], s76 offen offset:128 sc1\n"
    "v_fma_f32 v225, v225, v37, -v39\n"
    "v_fma_f32 v224, v224, v36, -v38\n"
    "v_mfma_f32_16x16x32_bf16 v[168:171], a[136:139], v[84:87], 0\n"
    "s_waitcnt lgkmcnt(1)\n"
    "v_cvt_pk_bf16_f32 v208, v208, v209\n"
    "v_fma_f32 v230, v230, v36, -v38\n"
    "v_mfma_f32_16x16x32_bf16 v[200:203], a[124:127], v[72:75], 0\n"
    "v_fma_f32 v226, v226, v36, -v38\n"
    "v_fma_f32 v210, v210, v36, -v38\n"
    "v_mfma_f32_16x16x32_bf16 a[44:47], a[120:123], v[92:95], 0\n"
    "buffer_store_dword v208, v244, s[16:19], s76 offen offset:1024 sc1\n"
    "v_smfmac_f32_16x16x64_bf16 v[120:123], v[52:55], v[92:99], v253\n"
    "v_cvt_pk_bf16_f32 v210, v210, v211\n"
    "v_mfma_f32_16x16x32_bf16 v[188:191], a[144:147], v[68:71], v[188:191]\n"
    "buffer_store_dword v210, v244, s[16:19], s76 offen offset:1152 sc1\n"
    "v_mfma_f32_16x16x32_bf16 v[148:151], a[128:131], v[76:79], v[148:151]\n"
    "v_cvt_pk_bf16_f32 v214, v214, v215\n"
    "v_fma_f32 v227, v227, v37, -v39\n"
    "v_mfma_f32_16x16x32_bf16 a[8:11], a[112:115], v[76:79], a[8:11]\n"
    "ds_read_b128 a[144:147], v249 offset:10240\n"
    "ds_read_b128 a[128:131], v249 offset:8192\n"
    "v_mfma_f32_16x16x32_bf16 a[28:31], a[124:127], v[88:91], 0\n"
    "v_exp_f32 v24, v24\n"
    "v_mfma_f32_16x16x32_bf16 a[24:27], a[112:115], v[84:87], 0\n"
    "v_mov_b32 v2, v3\n"
    "v_cvt_pk_bf16_f32 v222, v222, v223\n"
    "v_mfma_f32_16x16x32_bf16 a[40:43], a[116:119], v[96:99], 0\n"
    "ds_read_b128 a[148:151], v249 offset:22528\n"
    "v_fma_f32 v232, v232, v36, -v38\n"
    "v_mfma_f32_16x16x32_bf16 v[200:203], a[120:123], v[68:71], v[200:203]\n"
    "buffer_store_dword v212, v244, s[16:19], s76 offen offset:256 sc1\n"
    "v_cvt_pk_bf16_f32 v224, v224, v225\n"
    "v_mfma_f32_16x16x32_bf16 v[152:155], a[136:139], v[76:79], v[152:155]\n"
    "s_waitcnt lgkmcnt(3)\n"
    "v_exp_f32 v14, v14\n"
    "v_mfma_f32_16x16x32_bf16 a[96:99], a[132:135], v[96:99], 0\n"
    "v_exp_f32 v31, v31\n"
    "v_mfma_f32_16x16x32_bf16 v[196:199], a[112:115], v[68:71], 0\n"
    "s_waitcnt lgkmcnt(2)\n"
    "buffer_store_dword v214, v244, s[16:19], s76 offen offset:384 sc1\n"
    "v_exp_f32 v6, v6\n"
    "v_mfma_f32_16x16x32_bf16 a[104:107], a[144:147], v[92:95], 0\n"
    "v_exp_f32 v18, v18\n"
    "v_mfma_f32_16x16x32_bf16 a[28:31], a[120:123], v[84:87], a[28:31]\n"
    "v_exp_f32 v33, v33\n"
    "v_mfma_f32_16x16x32_bf16 a[64:67], a[132:135], v[80:83], 0\n"
    "v_cvt_pk_bf16_f32 v230, v230, v231\n"
    "v_mfma_f32_16x16x32_bf16 v[168:171], a[140:143], v[88:91], v[168:171]\n"
    "s_waitcnt lgkmcnt(1)\n"
    "buffer_load_dwordx2 v[236:237], v241, s[36:39], s81 offen offset:0 sc1\n"
    "v_mfma_f32_16x16x32_bf16 a[48:51], a[128:131], v[68:71], 0\n"
    "v_exp_f32 v19, v19\n"
    "v_mfma_f32_16x16x32_bf16 v[136:139], a[140:143], v[72:75], v[136:139]\n"
    "s_waitcnt lgkmcnt(0)\n"
    "buffer_store_dword v216, v244, s[16:19], s76 offen offset:1280 sc1\n"
    "v_mfma_f32_16x16x32_bf16 a[104:107], a[148:151], v[96:99], a[104:107]\n"
    "v_exp_f32 v29, v29\n"
    "v_mfma_f32_16x16x32_bf16 a[88:91], a[148:151], v[88:91], 0\n"
    "v_fma_f32 v228, v228, v36, -v38\n"
    "v_cvt_pk_bf16_f32 v226, v226, v227\n"
    "v_mfma_f32_16x16x32_bf16 v[196:199], a[116:119], v[72:75], v[196:199]\n"
    "v_cvt_pk_bf16_f32 v218, v218, v219\n"
    "v_cvt_pk_bf16_f32 v220, v220, v221\n"
    "v_mfma_f32_16x16x32_bf16 v[184:187], a[136:139], v[92:95], v[184:187]\n"
    "buffer_store_dword v218, v244, s[16:19], s76 offen offset:1408 sc1\n"
    "ds_read_b128 a[140:143], v249 offset:21504\n"
    "v_mfma_f32_16x16x32_bf16 a[24:27], a[116:119], v[88:91], a[24:27]\n"
    "v_exp_f32 v32, v32\n"
    "v_mfma_f32_16x16x32_bf16 v[192:195], a[156:159], v[72:75], v[192:195]\n"
    "ds_read_b128 a[152:155], v249 offset:11264\n"
    "v_cvt_pk_bf16_f32 v232, v232, v233\n"
    "v_mfma_f32_16x16x32_bf16 a[20:23], a[156:159], v[88:91], a[20:23]\n"
    "v_exp_f32 v9, v9\n"
    "v_mfma_f32_16x16x32_bf16 a[80:83], a[128:131], v[84:87], 0\n"
    "v_exp_f32 v11, v11\n"
    "v_mfma_f32_16x16x32_bf16 a[12:15], a[124:127], v[80:83], a[12:15]\n"
    "buffer_store_dword v220, v244, s[16:19], s77 offen offset:0 sc1\n"
    "ds_read_b128 a[156:159], v249 offset:23552\n"
    "v_mfma_f32_16x16x32_bf16 a[72:75], a[144:147], v[76:79], 0\n"
    "v_exp_f32 v28, v28\n"
    "v_mfma_f32_16x16x32_bf16 a[64:67], a[128:131], v[76:79], a[64:67]\n"
    "buffer_store_dword v222, v244, s[16:19], s77 offen offset:128 sc1\n"
    "v_mfma_f32_16x16x32_bf16 a[80:83], a[132:135], v[88:91], a[80:83]\n"
    "v_mfma_f32_16x16x32_bf16 a[88:91], a[144:147], v[84:87], a[88:91]\n"
    "v_exp_f32_e64 v30, v30\n"
    "v_mfma_f32_16x16x32_bf16 a[56:59], a[148:151], v[72:75], 0\n"
    "v_add_u32 v243, 512, v243\n"
    "v_cvt_pk_bf16_f32 v228, v228, v229\n"
    "v_mfma_f32_16x16x32_bf16 a[56:59], a[144:147], v[68:71], a[56:59]\n"
    "s_add_u32 s67, s67, 49152\n"
    "buffer_store_dword v224, v244, s[16:19], s77 offen offset:1024 sc1\n"
    "v_mfma_f32_16x16x32_bf16 a[48:51], a[132:135], v[72:75], a[48:51]\n"
    "v_exp_f32 v20, v20\n"
    "v_mfma_f32_16x16x32_bf16 a[44:47], a[124:127], v[96:99], a[44:47]\n"
    "s_waitcnt lgkmcnt(0)\n"
    "buffer_store_dword v226, v244, s[16:19], s77 offen offset:1152 sc1\n"
    "ds_read_b128 a[136:139], v249 offset:9216\n"
    "v_mfma_f32_16x16x32_bf16 a[108:111], a[156:159], v[96:99], 0\n"
    "v_mfma_f32_16x16x32_bf16 a[96:99], a[128:131], v[92:95], a[96:99]\n"
    "v_exp_f32 v15, v15\n"
    "v_mfma_f32_16x16x32_bf16 a[100:103], a[140:143], v[96:99], 0\n"
    "buffer_store_dword v228, v244, s[16:19], s77 offen offset:256 sc1\n"
    "v_exp_f32 v35, v35\n"
    "v_mfma_f32_16x16x32_bf16 a[68:71], a[140:143], v[80:83], 0\n"
    "ds_read_b128 a[120:123], v3 offset:16384\n"
    "ds_read_b128 a[116:119], v3 offset:14336\n"
    "v_mfma_f32_16x16x32_bf16 a[40:43], a[112:115], v[92:95], a[40:43]\n"
    "v_exp_f32 v8, v8\n"
    "v_mfma_f32_16x16x32_bf16 a[76:79], a[156:159], v[80:83], 0\n"
    "buffer_store_dword v230, v244, s[16:19], s77 offen offset:384 sc1\n"
    "v_mfma_f32_16x16x32_bf16 a[76:79], a[152:155], v[76:79], a[76:79]\n"
    "v_exp_f32 v34, v34\n"
    "v_mfma_f32_16x16x32_bf16 a[60:63], a[152:155], v[68:71], 0\n"
    "s_waitcnt lgkmcnt(2)\n"
    "ds_read_b128 a[124:127], v3 offset:18432\n"
    "ds_read_b128 a[128:131], v3 offset:20480\n"
    "v_mfma_f32_16x16x32_bf16 a[92:95], a[152:155], v[84:87], 0\n"
    "s_waitcnt vmcnt(15)\n"
    "ds_add_u32 v247, v246\n"
    "v_mfma_f32_16x16x32_bf16 a[108:111], a[152:155], v[92:95], a[108:111]\n"
    "ds_read_b128 a[132:135], v3 offset:22528\n"
    "v_add_u32 v250, 1536, v250\n"
    "v_mfma_f32_16x16x32_bf16 a[52:55], a[136:139], v[68:71], 0\n"
    "ds_read_b128 a[112:115], v3 offset:12288\n"
    "v_mfma_f32_16x16x32_bf16 a[84:87], a[136:139], v[84:87], 0\n"
    "v_add_u32 v3, 49152, v252\n"
    "v_mfma_f32_16x16x32_bf16 a[72:75], a[148:151], v[80:83], a[72:75]\n"
    "buffer_store_dword v232, v244, s[16:19], s77 offen offset:1280 sc1\n"
    "v_cvt_pk_bf16_f32 v234, v234, v235\n"
    "v_mfma_f32_16x16x32_bf16 a[100:103], a[136:139], v[92:95], a[100:103]\n"
    "v_exp_f32 v17, v17\n"
    "v_mfma_f32_16x16x32_bf16 a[84:87], a[140:143], v[88:91], a[84:87]\n"
    "v_exp_f32 v23, v23\n"
    "v_mfma_f32_16x16x32_bf16 a[60:63], a[156:159], v[72:75], a[60:63]\n"
    "buffer_store_dword v234, v244, s[16:19], s77 offen offset:1408 sc1\n"
    "v_mfma_f32_16x16x32_bf16 a[52:55], a[140:143], v[72:75], a[52:55]\n"
    "v_exp_f32 v25, v25\n"
    "v_mfma_f32_16x16x32_bf16 a[68:71], a[136:139], v[76:79], a[68:71]\n"
    "s_waitcnt lgkmcnt(0)\n"
    "v_mfma_f32_16x16x32_bf16 a[92:95], a[156:159], v[88:91], a[92:95]\n"
    "s_branch .LPK\n"
    ".fill ((0x20000 - (. - _Z8attn_fwdPKtPKiS2_S0_S0_S0_S0_PcPt)) & 4095) / 4, 4, 0xBF800000\n"
    ".LhVP:\n"
    "s_waitcnt vmcnt(12)\n"
    "ds_read_b128 v[52:55], v251 offset:0\n"
    "s_add_u32 s83, s67, 4096\n"
    "ds_read_b128 v[200:203], v97 offset:49408\n"
    "ds_read_b128 v[196:199], v96 offset:49408\n"
    "v_mov_b32 v2, v3\n"
    "ds_read_b128 v[192:195], v97 offset:49280\n"
    "ds_read_b128 v[188:191], v96 offset:49280\n"
    "ds_read_b128 v[184:187], v97 offset:49152\n"
    "s_add_u32 m0, s68, .Lend-.Lend2+(0)\n"
    "ds_read_b128 v[180:183], v96 offset:49152\n"
    "s_waitcnt lgkmcnt(6)\n"
    "v_smfmac_f32_16x16x64_bf16 v[108:111], v[52:55], v[68:75], v253\n"
    "s_waitcnt lgkmcnt(4)\n"
    "v_pk_maximum3_f16 v98, v200, v201, v98\n"
    "v_pk_maximum3_f16 v98, v202, v203, v98\n"
    "v_pk_add_u16 v203, s89, v203 clamp\n"
    "v_pk_add_u16 v201, s89, v201 clamp\n"
    "v_pk_add_u16 v200, s89, v200 clamp\n"
    "v_pk_add_u16 v202, s89, v202 clamp\n"
    "v_pk_maximum3_f16 v98, v196, v197, v98\n"
    "v_smfmac_f32_16x16x64_bf16 v[116:119], v[52:55], v[84:91], v253\n"
    "v_pk_maximum3_f16 v98, v198, v199, v98\n"
    "v_mfma_f32_16x16x32_bf16 v[64:67], a[44:47], v[200:203], v[64:67]\n"
    "s_waitcnt lgkmcnt(3)\n"
    "v_pk_add_u16 v197, s89, v197 clamp\n"
    "v_pk_maximum3_f16 v98, v192, v193, v98\n"
    "v_mfma_f32_16x16x32_bf16 v[48:51], a[20:23], v[200:203], v[48:51]\n"
    "buffer_load_dwordx4 v1, s[16:19], s67 offen offset:0 sc1 lds\n"
    "v_mfma_f32_16x16x32_bf16 v[16:19], a[132:135], v[200:203], v[16:19]\n"
    "v_pk_add_u16 v199, s89, v199 clamp\n"
    "v_pk_add_u16 v198, s89, v198 clamp\n"
    "v_smfmac_f32_16x16x64_bf16 v[112:115], v[52:55], v[76:83], v253\n"
    "v_pk_add_u16 v196, s89, v196 clamp\n"
    "v_mfma_f32_16x16x32_bf16 v[32:35], a[156:159], v[200:203], v[32:35]\n"
    "s_waitcnt lgkmcnt(2)\n"
    "v_pk_add_u16 v193, s89, v193 clamp\n"
    "v_pk_maximum3_f16 v98, v194, v195, v98\n"
    "v_pk_maximum3_f16 v98, v188, v189, v98\n"
    "v_mfma_f32_16x16x32_bf16 v[32:35], a[152:155], v[196:199], v[32:35]\n"
    "v_pk_maximum3_f16 v98, v190, v191, v98\n"
    "v_pk_add_u16 v192, s89, v192 clamp\n"
    "v_mfma_f32_16x16x32_bf16 v[16:19], a[128:131], v[196:199], v[16:19]\n"
    "v_pk_add_u16 v194, s89, v194 clamp\n"
    "v_pk_add_u16 v195, s89, v195 clamp\n"
    "v_mfma_f32_16x16x32_bf16 v[48:51], a[16:19], v[196:199], v[48:51]\n"
    "buffer_load_dwordx4 v1, s[16:19], s67 offen offset:1024 sc1 lds\n"
    "v_mfma_f32_16x16x32_bf16 v[48:51], a[12:15], v[192:195], v[48:51]\n"
    "s_waitcnt lgkmcnt(1)\n"
    "v_pk_maximum3_f16 v98, v184, v185, v98\n"
    "v_pk_add_u16 v188, s89, v188 clamp\n"
    "v_pk_maximum3_f16 v98, v186, v187, v98\n"
    "v_mfma_f32_16x16x32_bf16 v[32:35], a[148:151], v[192:195], v[32:35]\n"
    "v_pk_add_u16 v190, s89, v190 clamp\n"
    "v_accvgpr_write_b32 a184, v196\n"
    "v_mfma_f32_16x16x32_bf16 v[16:19], a[124:127], v[192:195], v[16:19]\n"
    "v_pk_add_u16 v189, s89, v189 clamp\n"
    "v_pk_add_u16 v191, s89, v191 clamp\n"
    "v_mfma_f32_16x16x32_bf16 v[64:67], a[40:43], v[196:199], v[64:67]\n"
    "v_pk_add_u16 v186, s89, v186 clamp\n"
    "v_pk_add_u16 v187, s89, v187 clamp\n"
    "v_mfma_f32_16x16x32_bf16 v[32:35], a[144:147], v[188:191], v[32:35]\n"
    "s_waitcnt lgkmcnt(0)\n"
    "v_pk_add_u16 v185, s89, v185 clamp\n"
    "v_pk_maximum3_f16 v98, v180, v181, v98\n"
    "v_mfma_f32_16x16x32_bf16 v[48:51], a[8:11], v[188:191], v[48:51]\n"
    "s_waitcnt vmcnt(8)\n"
    "buffer_load_dwordx4 v1, s[16:19], s67 offen offset:2048 sc1 lds\n"
    "v_mfma_f32_16x16x32_bf16 v[64:67], a[36:39], v[192:195], v[64:67]\n"
    "s_barrier\n"
    "v_pk_add_u16 v184, s89, v184 clamp\n"
    "v_mfma_f32_16x16x32_bf16 v[16:19], a[120:123], v[188:191], v[16:19]\n"
    "ds_read_b128 a[48:51], v252 offset:24576\n"
    "ds_read_b128 a[68:71], v252 offset:34816\n"
    "v_mfma_f32_16x16x32_bf16 v[32:35], a[140:143], v[184:187], v[32:35]\n"
    "ds_read_b128 a[76:79], v252 offset:27648\n"
    "ds_read_b128 a[72:75], v252 offset:25600\n"
    "v_mfma_f32_16x16x32_bf16 v[48:51], a[4:7], v[184:187], v[48:51]\n"
    "ds_read_b128 a[92:95], v252 offset:35840\n"
    "v_pk_maximum3_f16 v98, v182, v183, v98\n"
    "v_mfma_f32_16x16x32_bf16 v[64:67], a[32:35], v[188:191], v[64:67]\n"
    "v_pk_add_u16 v182, s89, v182 clamp\n"
    "v_pk_add_u16 v181, s89, v181 clamp\n"
    "v_mfma_f32_16x16x32_bf16 v[64:67], a[28:31], v[184:187], v[64:67]\n"
    "v_pk_add_u16 v183, s89, v183 clamp\n"
    "v_pk_add_u16 v180, s89, v180 clamp\n"
    "v_mfma_f32_16x16x32_bf16 v[16:19], a[116:119], v[184:187], v[16:19]\n"
    "buffer_load_dwordx4 v1, s[16:19], s67 offen offset:3072 sc1 lds\n"
    "v_mfma_f32_16x16x32_bf16 v[32:35], a[136:139], v[180:183], v[32:35]\n"
    "ds_read_b128 a[64:67], v252 offset:32768\n"
    "v_accvgpr_write_b32 a176, v188\n"
    "v_mfma_f32_16x16x32_bf16 v[16:19], a[112:115], v[180:183], v[16:19]\n"
    "v_accvgpr_write_b32 a175, v187\n"
    "s_waitcnt lgkmcnt(5)\n"
    "v_mfma_f32_16x16x32_bf16 v[12:15], a[48:51], a[240:243], v[100:103]\n"
    "s_waitcnt lgkmcnt(4)\n"
    "ds_read_b128 a[56:59], v252 offset:28672\n"
    "v_accvgpr_write_b32 a177, v189\n"
    "v_mfma_f32_16x16x32_bf16 v[48:51], a[0:3], v[180:183], v[48:51]\n"
    "s_add_u32 m0, s68, 4096\n"
    "ds_read_b128 a[80:83], v252 offset:29696\n"
    "ds_read_b128 a[88:91], v252 offset:33792\n"
    "v_mfma_f32_16x16x32_bf16 v[8:11], a[68:71], a[236:239], v[100:103]\n"
    "v_accvgpr_write_b32 a188, v200\n"
    "v_accvgpr_write_b32 a190, v202\n"
    "v_mfma_f32_16x16x32_bf16 v[4:7], a[48:51], a[192:195], v[100:103]\n"
    "s_waitcnt lgkmcnt(5)\n"
    "ds_read_b128 a[60:63], v252 offset:30720\n"
    "ds_read_b128 a[52:55], v252 offset:26624\n"
    "v_mfma_f32_16x16x32_bf16 v[20:23], a[72:75], a[192:195], v[104:107]\n"
    "buffer_load_dwordx4 v1, s[16:19], s83 offen offset:0 sc1 lds\n"
    "v_mfma_f32_16x16x32_bf16 v[28:31], a[72:75], a[240:243], v[104:107]\n"
    "s_waitcnt lgkmcnt(6)\n"
    "v_accvgpr_write_b32 a171, v183\n"
    "v_accvgpr_write_b32 a186, v198\n"
    "v_mfma_f32_16x16x32_bf16 v[24:27], a[92:95], a[236:239], v[104:107]\n"
    "v_exp_f32 v18, v18\n"
    "v_mfma_f32_16x16x32_bf16 v[64:67], a[24:27], v[180:183], v[64:67]\n"
    "v_accvgpr_write_b32 a174, v186\n"
    "v_accvgpr_write_b32 a178, v190\n"
    "v_mfma_f32_16x16x32_bf16 v[20:23], a[76:79], a[196:199], v[20:23]\n"
    "v_accvgpr_write_b32 a191, v203\n"
    "v_accvgpr_write_b32 a179, v191\n"
    "v_mfma_f32_16x16x32_bf16 v[28:31], a[76:79], a[244:247], v[28:31]\n"
    "s_waitcnt lgkmcnt(5)\n"
    "v_accvgpr_write_b32 a180, v192\n"
    "v_mfma_f32_16x16x32_bf16 v[8:11], a[64:67], a[232:235], v[8:11]\n"
    "s_waitcnt lgkmcnt(3)\n"
    "ds_read_b128 a[84:87], v252 offset:31744\n"
    "v_accvgpr_write_b32 a187, v199\n"
    "v_mfma_f32_16x16x32_bf16 v[28:31], a[80:83], a[248:251], v[28:31]\n"
    "v_accvgpr_write_b32 a169, v181\n"
    "v_accvgpr_write_b32 a173, v185\n"
    "v_mfma_f32_16x16x32_bf16 v[20:23], a[80:83], a[200:203], v[20:23]\n"
    "s_waitcnt lgkmcnt(1)\n"
    "buffer_load_dwordx4 v1, s[16:19], s83 offen offset:1024 sc1 lds\n"
    "v_mfma_f32_16x16x32_bf16 v[4:7], a[52:55], a[196:199], v[4:7]\n"
    "v_exp_f32 v16, v16\n"
    "v_mfma_f32_16x16x32_bf16 v[24:27], a[88:91], a[232:235], v[24:27]\n"
    "v_accvgpr_write_b32 a168, v180\n"
    "v_accvgpr_write_b32 a182, v194\n"
    "v_mfma_f32_16x16x32_bf16 v[8:11], a[60:63], a[228:231], v[8:11]\n"
    "v_accvgpr_write_b32 a181, v193\n"
    "v_accvgpr_write_b32 a189, v201\n"
    "v_mfma_f32_16x16x32_bf16 v[8:11], a[56:59], a[224:227], v[8:11]\n"
    "s_add_u32 m0, s64, 0\n"
    "v_accvgpr_write_b32 a170, v182\n"
    "v_accvgpr_write_b32 a172, v184\n"
    "v_mfma_f32_16x16x32_bf16 v[4:7], a[56:59], a[200:203], v[4:7]\n"
    "v_exp_f32 v32, v32\n"
    "v_mfma_f32_16x16x32_bf16 v[4:7], a[60:63], a[204:207], v[4:7]\n"
    "s_waitcnt lgkmcnt(0)\n"
    "v_accvgpr_write_b32 a183, v195\n"
    "v_accvgpr_write_b32 a185, v197\n"
    "v_mfma_f32_16x16x32_bf16 v[28:31], a[84:87], a[252:255], v[28:31]\n"
    "s_add_u32 s83, s67, 24576\n"
    "buffer_load_dwordx4 v1, s[16:19], s83 offen offset:0 sc1 lds\n"
    "v_mfma_f32_16x16x32_bf16 v[12:15], a[52:55], a[244:247], v[12:15]\n"
    "v_exp_f32 v17, v17\n"
    "v_mfma_f32_16x16x32_bf16 v[12:15], a[56:59], a[248:251], v[12:15]\n"
    "v_exp_f32 v19, v19\n"
    "v_mfma_f32_16x16x32_bf16 v[8:11], a[52:55], a[220:223], v[8:11]\n"
    "v_exp_f32 v35, v35\n"
    "v_mfma_f32_16x16x32_bf16 v[20:23], a[84:87], a[204:207], v[20:23]\n"
    "v_exp_f32 v51, v51\n"
    "v_mfma_f32_16x16x32_bf16 v[12:15], a[60:63], a[252:255], v[12:15]\n"
    "v_exp_f32 v65, v65\n"
    "v_mfma_f32_16x16x32_bf16 v[4:7], a[64:67], a[208:211], v[4:7]\n"
    "v_mfma_f32_16x16x32_bf16 v[12:15], a[64:67], a[160:163], v[12:15]\n"
    "buffer_load_dwordx4 v1, s[16:19], s83 offen offset:1024 sc1 lds\n"
    "v_mfma_f32_16x16x32_bf16 v[4:7], a[68:71], a[212:215], v[4:7]\n"
    "v_cvt_pk_bf16_f32 v93, v18, v19\n"
    "v_mfma_f32_16x16x32_bf16 v[24:27], a[84:87], a[228:231], v[24:27]\n"
    "v_exp_f32 v34, v34\n"
    "v_mfma_f32_16x16x32_bf16 v[8:11], a[48:51], a[216:219], v[8:11]\n"
    "v_exp_f32 v33, v33\n"
    "v_mfma_f32_16x16x32_bf16 v[12:15], a[68:71], a[164:167], v[12:15]\n"
    "v_cvt_pk_bf16_f32 v92, v16, v17\n"
    "v_mfma_f32_16x16x32_bf16 v[28:31], a[88:91], a[160:163], v[28:31]\n"
    "s_waitcnt vmcnt(8)\n"
    "v_exp_f32 v48, v48\n"
    "v_mfma_f32_16x16x32_bf16 v[16:19], a[68:71], a[188:191], v[100:103]\n"
    "s_barrier\n"
    "ds_read_b128 a[120:123], v248 offset:1024\n"
    "v_mfma_f32_16x16x32_bf16 v[24:27], a[80:83], a[224:227], v[24:27]\n"
    "buffer_load_dwordx4 v1, s[16:19], s83 offen offset:2048 sc1 lds\n"
    "v_mfma_f32_16x16x32_bf16 v[16:19], a[64:67], a[184:187], v[16:19]\n"
    "ds_read_b128 a[124:127], v248 offset:13312\n"
    "v_cvt_pk_bf16_f32 v94, v32, v33\n"
    "v_mfma_f32_16x16x32_bf16 v[16:19], a[60:63], a[180:183], v[16:19]\n"
    "ds_read_b128 a[136:139], v248 offset:3072\n"
    "ds_read_b128 a[128:131], v248 offset:2048\n"
    "v_mfma_f32_16x16x32_bf16 v[24:27], a[76:79], a[220:223], v[24:27]\n"
    "v_exp_f32 v50, v50\n"
    "v_mfma_f32_16x16x32_bf16 v[20:23], a[88:91], a[208:211], v[20:23]\n"
    "v_cvt_pk_bf16_f32 v97, v50, v51\n"
    "v_cvt_pk_bf16_f32 v95, v34, v35\n"
    "v_mfma_f32_16x16x32_bf16 v[32:35], a[92:95], a[188:191], v[104:107]\n"
    "v_exp_f32 v67, v67\n"
    "v_mfma_f32_16x16x32_bf16 v[16:19], a[56:59], a[176:179], v[16:19]\n"
    "v_mfma_f32_16x16x32_bf16 v[32:35], a[88:91], a[184:187], v[32:35]\n"
    "buffer_load_dwordx4 v1, s[16:19], s83 offen offset:3072 sc1 lds\n"
    "v_mfma_f32_16x16x32_bf16 v[20:23], a[92:95], a[212:215], v[20:23]\n"
    "v_pk_add_u16 v98, s89, v98\n"
    "s_waitcnt lgkmcnt(2)\n"
    "v_mfma_f32_16x16x32_bf16 v[160:163], a[124:127], v[88:91], 0\n"
    "ds_read_b128 a[132:135], v248 offset:14336\n"
    "s_waitcnt lgkmcnt(2)\n"
    "v_mfma_f32_16x16x32_bf16 v[136:139], a[136:139], v[68:71], 0\n"
    "ds_read_b128 a[144:147], v248 offset:4096\n"
    "v_mfma_f32_16x16x32_bf16 v[128:131], a[124:127], v[72:75], 0\n"
    "s_add_u32 s83, s67, 28672\n"
    "ds_read_b128 a[148:151], v248 offset:16384\n"
    "ds_read_b128 a[112:115], v248 offset:0\n"
    "v_mfma_f32_16x16x32_bf16 v[144:147], a[120:123], v[76:79], 0\n"
    "v_exp_f32 v64, v64\n"
    "v_mfma_f32_16x16x32_bf16 v[32:35], a[84:87], a[180:183], v[32:35]\n"
    "s_add_u32 m0, s64, 4096\n"
    "ds_read_b128 a[152:155], v248 offset:5120\n"
    "v_mfma_f32_16x16x32_bf16 v[16:19], a[52:55], a[172:175], v[16:19]\n"
    "buffer_load_dwordx4 v1, s[16:19], s83 offen offset:0 sc1 lds\n"
    "v_mfma_f32_16x16x32_bf16 v[128:131], a[120:123], v[68:71], v[128:131]\n"
    "v_exp_f32 v66, v66\n"
    "v_mfma_f32_16x16x32_bf16 v[176:179], a[120:123], v[92:95], 0\n"
    "s_waitcnt lgkmcnt(5)\n"
    "ds_read_b128 a[116:119], v248 offset:12288\n"
    "v_cvt_pk_bf16_f32 v99, v66, v67\n"
    "v_mfma_f32_16x16x32_bf16 v[132:135], a[128:131], v[68:71], 0\n"
    "v_exp_f32 v49, v49\n"
    "v_mfma_f32_16x16x32_bf16 v[28:31], a[92:95], a[164:167], v[28:31]\n"
    "s_waitcnt lgkmcnt(5)\n"
    "v_cvt_pk_bf16_f32 v96, v48, v49\n"
    "v_mfma_f32_16x16x32_bf16 v[148:151], a[132:135], v[80:83], 0\n"
    "v_and_b32 v98, 0x80008000, v98\n"
    "v_mfma_f32_16x16x32_bf16 v[164:167], a[128:131], v[84:87], 0\n"
    "v_exp_f32 v4, v4\n"
    "v_mfma_f32_16x16x32_bf16 v[24:27], a[72:75], a[216:219], v[24:27]\n"
    "buffer_load_dwordx4 v1, s[16:19], s83 offen offset:1024 sc1 lds\n"
    "v_mfma_f32_16x16x32_bf16 v[32:35], a[80:83], a[176:179], v[32:35]\n"
    "v_exp_f32 v12, v12\n"
    "v_mfma_f32_16x16x32_bf16 v[144:147], a[124:127], v[80:83], v[144:147]\n"
    "v_exp_f32 v31, v31\n"
    "v_mfma_f32_16x16x32_bf16 v[160:163], a[120:123], v[84:87], v[160:163]\n"
    "s_waitcnt lgkmcnt(2)\n"
    "v_mfma_f32_16x16x32_bf16 v[124:127], a[112:115], v[68:71], 0\n"
    "v_exp_f32 v27, v27\n"
    "v_mfma_f32_16x16x32_bf16 v[188:191], a[148:151], v[72:75], 0\n"
    "ds_read_b128 a[140:143], v248 offset:15360\n"
    "v_mfma_f32_16x16x32_bf16 v[16:19], a[48:51], a[168:171], v[16:19]\n"
    "s_waitcnt lgkmcnt(2)\n"
    "ds_read_b128 a[120:123], v248 offset:7168\n"
    "v_cmp_ne_u32 s[34:35], 0, v98\n"
    "v_mfma_f32_16x16x32_bf16 a[20:23], a[152:155], v[84:87], 0\n"
    "s_waitcnt lgkmcnt(2)\n"
    "v_exp_f32 v29, v29\n"
    "v_mfma_f32_16x16x32_bf16 v[140:143], a[116:119], v[80:83], 0\n"
    "buffer_load_dwordx4 v[204:207], v242, s[40:43], s75 offen offset:0 nt\n"
    "v_mfma_f32_16x16x32_bf16 v[188:191], a[144:147], v[68:71], v[188:191]\n"
    "v_cvt_pk_bf16_f32 v98, v64, v65\n"
    "v_mfma_f32_16x16x32_bf16 v[124:127], a[116:119], v[72:75], v[124:127]\n"
    "ds_read_b128 a[156:159], v248 offset:17408\n"
    "v_mfma_f32_16x16x32_bf16 v[132:135], a[132:135], v[72:75], v[132:135]\n"
    "v_exp_f32 v5, v5\n"
    "v_mfma_f32_16x16x32_bf16 v[192:195], a[152:155], v[68:71], 0\n"
    "buffer_load_dwordx4 v[208:211], v242, s[40:43], s75 offen offset:768 nt\n"
    "v_mfma_f32_16x16x32_bf16 v[176:179], a[124:127], v[96:99], v[176:179]\n"
    "ds_read_b128 a[124:127], v248 offset:19456\n"
    "v_mfma_f32_16x16x32_bf16 v[140:143], a[112:115], v[76:79], v[140:143]\n"
    "v_exp_f32 v26, v26\n"
    "v_mfma_f32_16x16x32_bf16 v[172:175], a[116:119], v[96:99], 0\n"
    "s_waitcnt lgkmcnt(2)\n"
    "buffer_load_dwordx4 v[212:215], v242, s[40:43], s75 offen offset:256 nt\n"
    "v_mfma_f32_16x16x32_bf16 a[12:15], a[120:123], v[76:79], 0\n"
    "v_exp_f32 v20, v20\n"
    "v_mfma_f32_16x16x32_bf16 a[0:3], a[144:147], v[76:79], 0\n"
    "v_mfma_f32_16x16x32_bf16 v[152:155], a[140:143], v[80:83], 0\n"
    "v_mfma_f32_16x16x32_bf16 v[172:175], a[112:115], v[92:95], v[172:175]\n"
    "v_exp_f32 v16, v16\n"
    "v_mfma_f32_16x16x32_bf16 a[16:19], a[148:151], v[88:91], 0\n"
    "v_exp_f32 v17, v17\n"
    "v_mfma_f32_16x16x32_bf16 v[164:167], a[132:135], v[88:91], v[164:167]\n"
    "v_exp_f32 v24, v24\n"
    "v_mfma_f32_16x16x32_bf16 v[152:155], a[136:139], v[76:79], v[152:155]\n"
    "buffer_load_dwordx4 v[216:219], v242, s[40:43], s75 offen offset:1024 nt\n"
    "v_mfma_f32_16x16x32_bf16 v[180:183], a[132:135], v[96:99], 0\n"
    "v_exp_f32 v19, v19\n"
    "v_mfma_f32_16x16x32_bf16 v[156:159], a[112:115], v[84:87], 0\n"
    "v_exp_f32 v14, v14\n"
    "v_mfma_f32_16x16x32_bf16 a[44:47], a[120:123], v[92:95], 0\n"
    "v_exp_f32 v9, v9\n"
    "v_mfma_f32_16x16x32_bf16 a[32:35], a[144:147], v[92:95], 0\n"
    "s_waitcnt lgkmcnt(1)\n"
    "buffer_load_dwordx4 v[220:223], v242, s[40:43], s75 offen offset:512 nt\n"
    "v_mfma_f32_16x16x32_bf16 a[36:39], a[156:159], v[96:99], 0\n"
    "v_mfma_f32_16x16x32_bf16 a[0:3], a[148:151], v[80:83], a[0:3]\n"
    "buffer_load_dwordx4 v[224:227], v242, s[40:43], s75 offen offset:1280 nt\n"
    "v_mfma_f32_16x16x32_bf16 a[32:35], a[148:151], v[96:99], a[32:35]\n"
    "ds_read_b128 a[132:135], v248 offset:20480\n"
    "v_mfma_f32_16x16x32_bf16 v[148:151], a[128:131], v[76:79], v[148:151]\n"
    "v_exp_f32 v25, v25\n"
    "v_mfma_f32_16x16x32_bf16 a[4:7], a[156:159], v[80:83], 0\n"
    "ds_read_b128 a[112:115], v248 offset:6144\n"
    "v_mfma_f32_16x16x32_bf16 v[180:183], a[128:131], v[92:95], v[180:183]\n"
    "ds_read_b128 a[148:151], v248 offset:22528\n"
    "v_mfma_f32_16x16x32_bf16 v[184:187], a[140:143], v[96:99], 0\n"
    "v_mfma_f32_16x16x32_bf16 v[156:159], a[116:119], v[88:91], v[156:159]\n"
    "v_exp_f32 v10, v10\n"
    "v_mfma_f32_16x16x32_bf16 v[136:139], a[140:143], v[72:75], v[136:139]\n"
    "s_waitcnt lgkmcnt(3)\n"
    "v_mfma_f32_16x16x32_bf16 v[200:203], a[124:127], v[72:75], 0\n"
    "ds_read_b128 a[116:119], v248 offset:18432\n"
    "ds_read_b128 a[128:131], v248 offset:8192\n"
    "v_mfma_f32_16x16x32_bf16 a[20:23], a[156:159], v[88:91], a[20:23]\n"
    "v_exp_f32 v7, v7\n"
    "v_mfma_f32_16x16x32_bf16 a[4:7], a[152:155], v[76:79], a[4:7]\n"
    "v_mfma_f32_16x16x32_bf16 v[192:195], a[156:159], v[72:75], v[192:195]\n"
    "v_exp_f32 v21, v21\n"
    "v_mfma_f32_16x16x32_bf16 a[36:39], a[152:155], v[92:95], a[36:39]\n"
    "ds_read_b128 a[156:159], v248 offset:23552\n"
    "v_mfma_f32_16x16x32_bf16 v[184:187], a[136:139], v[92:95], v[184:187]\n"
    "s_waitcnt lgkmcnt(5)\n"
    "v_mfma_f32_16x16x32_bf16 a[64:67], a[132:135], v[80:83], 0\n"
    "v_mfma_f32_16x16x32_bf16 v[168:171], a[136:139], v[84:87], 0\n"
    "s_waitcnt lgkmcnt(3)\n"
    "v_mfma_f32_16x16x32_bf16 a[88:91], a[148:151], v[88:91], 0\n"
    "v_mfma_f32_16x16x32_bf16 a[16:19], a[144:147], v[84:87], a[16:19]\n"
    "s_waitcnt lgkmcnt(2)\n"
    "ds_read_b128 a[152:155], v248 offset:11264\n"
    "v_mfma_f32_16x16x32_bf16 a[8:11], a[116:119], v[80:83], 0\n"
    "v_mfma_f32_16x16x32_bf16 v[168:171], a[140:143], v[88:91], v[168:171]\n"
    "v_exp_f32 v15, v15\n"
    "v_mfma_f32_16x16x32_bf16 a[28:31], a[124:127], v[88:91], 0\n"
    "s_waitcnt lgkmcnt(2)\n"
    "v_exp_f32 v11, v11\n"
    "v_mfma_f32_16x16x32_bf16 a[48:51], a[128:131], v[68:71], 0\n"
    "v_mfma_f32_16x16x32_bf16 a[56:59], a[148:151], v[72:75], 0\n"
    "v_mfma_f32_16x16x32_bf16 a[28:31], a[120:123], v[84:87], a[28:31]\n"
    "v_mfma_f32_16x16x32_bf16 a[8:11], a[112:115], v[76:79], a[8:11]\n"
    "buffer_load_dwordx2 v[236:237], v241, s[36:39], s81 offen offset:0 sc1\n"
    "v_mfma_f32_16x16x32_bf16 v[200:203], a[120:123], v[68:71], v[200:203]\n"
    "v_exp_f32 v28, v28\n"
    "v_mfma_f32_16x16x32_bf16 v[196:199], a[112:115], v[68:71], 0\n"
    "s_waitcnt lgkmcnt(1)\n"
    "v_mfma_f32_16x16x32_bf16 a[108:111], a[156:159], v[96:99], 0\n"
    "v_mfma_f32_16x16x32_bf16 v[32:35], a[76:79], a[172:175], v[32:35]\n"
    "ds_read_b128 a[136:139], v248 offset:9216\n"
    "ds_read_b128 a[140:143], v248 offset:21504\n"
    "v_mfma_f32_16x16x32_bf16 a[80:83], a[128:131], v[84:87], 0\n"
    "v_mfma_f32_16x16x32_bf16 a[24:27], a[112:115], v[84:87], 0\n"
    "v_mfma_f32_16x16x32_bf16 a[12:15], a[124:127], v[80:83], a[12:15]\n"
    "v_exp_f32 v23, v23\n"
    "v_mfma_f32_16x16x32_bf16 v[32:35], a[72:75], a[168:171], v[32:35]\n"
    "ds_read_b128 a[120:123], v3 offset:16384\n"
    "ds_read_b128 a[144:147], v248 offset:10240\n"
    "v_mfma_f32_16x16x32_bf16 a[40:43], a[116:119], v[96:99], 0\n"
    "v_exp_f32 v22, v22\n"
    "v_mfma_f32_16x16x32_bf16 a[80:83], a[132:135], v[88:91], a[80:83]\n"
    "v_mfma_f32_16x16x32_bf16 a[76:79], a[156:159], v[80:83], 0\n"
    "v_mfma_f32_16x16x32_bf16 a[40:43], a[112:115], v[92:95], a[40:43]\n"
    "s_waitcnt lgkmcnt(3)\n"
    "v_exp_f32 v32, v32\n"
    "v_mfma_f32_16x16x32_bf16 a[52:55], a[136:139], v[68:71], 0\n"
    "v_exp_f32 v33, v33\n"
    "v_mfma_f32_16x16x32_bf16 a[48:51], a[132:135], v[72:75], a[48:51]\n"
    "v_mfma_f32_16x16x32_bf16 a[92:95], a[152:155], v[84:87], 0\n"
    "s_waitcnt lgkmcnt(2)\n"
    "v_mfma_f32_16x16x32_bf16 a[68:71], a[140:143], v[80:83], 0\n"
    "v_mfma_f32_16x16x32_bf16 a[100:103], a[140:143], v[96:99], 0\n"
    "s_waitcnt lgkmcnt(0)\n"
    "ds_read_b128 v[100:103], v250 offset:896\n"
    "v_mfma_f32_16x16x32_bf16 a[88:91], a[144:147], v[84:87], a[88:91]\n"
    "v_mfma_f32_16x16x32_bf16 a[24:27], a[116:119], v[88:91], a[24:27]\n"
    "v_mfma_f32_16x16x32_bf16 a[84:87], a[136:139], v[84:87], 0\n"
    "ds_read_b128 a[112:115], v3 offset:12288\n"
    "v_mfma_f32_16x16x32_bf16 a[96:99], a[132:135], v[96:99], 0\n"
    "v_exp_f32 v34, v34\n"
    "v_mfma_f32_16x16x32_bf16 v[196:199], a[116:119], v[72:75], v[196:199]\n"
    "v_exp_f32 v6, v6\n"
    "v_mfma_f32_16x16x32_bf16 a[44:47], a[124:127], v[96:99], a[44:47]\n"
    "v_mfma_f32_16x16x32_bf16 a[108:111], a[152:155], v[92:95], a[108:111]\n"
    "v_exp_f32 v8, v8\n"
    "v_mfma_f32_16x16x32_bf16 a[76:79], a[152:155], v[76:79], a[76:79]\n"
    "v_mfma_f32_16x16x32_bf16 a[104:107], a[144:147], v[92:95], 0\n"
    "s_add_u32 s67, s67, 49152\n"
    "v_mfma_f32_16x16x32_bf16 a[56:59], a[144:147], v[68:71], a[56:59]\n"
    "v_exp_f32 v30, v30\n"
    "v_mfma_f32_16x16x32_bf16 a[96:99], a[128:131], v[92:95], a[96:99]\n"
    "ds_read_b128 a[124:127], v3 offset:18432\n"
    "v_mfma_f32_16x16x32_bf16 a[72:75], a[144:147], v[76:79], 0\n"
    "ds_read_b128 a[132:135], v3 offset:22528\n"
    "s_waitcnt vmcnt(7)\n"
    "v_mfma_f32_16x16x32_bf16 a[104:107], a[148:151], v[96:99], a[104:107]\n"
    "ds_add_u32 v247, v246\n"
    "v_mfma_f32_16x16x32_bf16 a[64:67], a[128:131], v[76:79], a[64:67]\n"
    "ds_read_b128 a[116:119], v3 offset:14336\n"
    "ds_read_b128 a[128:131], v3 offset:20480\n"
    "v_smfmac_f32_16x16x64_bf16 v[120:123], v[52:55], v[92:99], v253\n"
    "v_add_u32 v3, 49152, v252\n"
    "v_mfma_f32_16x16x32_bf16 a[60:63], a[152:155], v[68:71], 0\n"
    "v_mfma_f32_16x16x32_bf16 a[52:55], a[140:143], v[72:75], a[52:55]\n"
    "v_mfma_f32_16x16x32_bf16 a[84:87], a[140:143], v[88:91], a[84:87]\n"
    "v_exp_f32 v13, v13\n"
    "v_mfma_f32_16x16x32_bf16 a[60:63], a[156:159], v[72:75], a[60:63]\n"
    "v_exp_f32 v35, v35\n"
    "v_mfma_f32_16x16x32_bf16 a[92:95], a[156:159], v[88:91], a[92:95]\n"
    "v_exp_f32 v18, v18\n"
    "v_mfma_f32_16x16x32_bf16 a[72:75], a[148:151], v[80:83], a[72:75]\n"
    "v_mfma_f32_16x16x32_bf16 a[100:103], a[136:139], v[92:95], a[100:103]\n"
    "s_waitcnt lgkmcnt(0)\n"
    "v_mfma_f32_16x16x32_bf16 a[68:71], a[136:139], v[76:79], a[68:71]\n"
    "s_branch .LNV\n"
    ".fill ((0x20000 - (. - _Z8attn_fwdPKtPKiS2_S0_S0_S0_S0_PcPt)) & 4095) / 4, 4, 0xBF800000\n"
    ".LhVN:\n"
    "s_waitcnt vmcnt(18)\n"
    "ds_read_b128 v[52:55], v251 offset:768\n"
    "ds_read_b128 v[184:187], v97 offset:49152\n"
    "ds_read_b128 v[196:199], v96 offset:49408\n"
    "ds_read_b128 v[200:203], v97 offset:49408\n"
    "ds_read_b128 v[188:191], v96 offset:49280\n"
    "ds_read_b128 v[180:183], v96 offset:49152\n"
    "v_mov_b32 v2, v3\n"
    "ds_read_b128 v[192:195], v97 offset:49280\n"
    "s_add_u32 m0, s68, .Lend-.Lend2+(0)\n"
    "s_waitcnt lgkmcnt(6)\n"
    "v_smfmac_f32_16x16x64_bf16 v[108:111], v[52:55], v[68:75], v253\n"
    "s_waitcnt lgkmcnt(3)\n"
    "v_pk_maximum3_f16 v98, v200, v201, v98\n"
    "v_pk_maximum3_f16 v98, v202, v203, v98\n"
    "v_pk_add_u16 v200, s89, v200 clamp\n"
    "v_smfmac_f32_16x16x64_bf16 v[116:119], v[52:55], v[84:91], v253\n"
    "s_add_u32 s83, s67, 4096\n"
    "v_pk_add_u16 v201, s89, v201 clamp\n"
    "v_pk_add_u16 v202, s89, v202 clamp\n"
    "v_pk_add_u16 v203, s89, v203 clamp\n"
    "v_pk_maximum3_f16 v98, v196, v197, v98\n"
    "v_pk_maximum3_f16 v98, v198, v199, v98\n"
    "v_mfma_f32_16x16x32_bf16 v[48:51], a[20:23], v[200:203], v[48:51]\n"
    "v_pk_add_u16 v197, s89, v197 clamp\n"
    "v_pk_add_u16 v198, s89, v198 clamp\n"
    "v_mfma_f32_16x16x32_bf16 v[64:67], a[44:47], v[200:203], v[64:67]\n"
    "buffer_load_dwordx4 v1, s[16:19], s67 offen offset:0 sc1 lds\n"
    "v_mfma_f32_16x16x32_bf16 v[16:19], a[132:135], v[200:203], v[16:19]\n"
    "v_pk_add_u16 v199, s89, v199 clamp\n"
    "v_pk_add_u16 v196, s89, v196 clamp\n"
    "v_mfma_f32_16x16x32_bf16 v[32:35], a[156:159], v[200:203], v[32:35]\n"
    "s_waitcnt lgkmcnt(0)\n"
    "v_pk_maximum3_f16 v98, v192, v193, v98\n"
    "v_pk_maximum3_f16 v98, v194, v195, v98\n"
    "v_pk_add_u16 v194, s89, v194 clamp\n"
    "v_mfma_f32_16x16x32_bf16 v[32:35], a[152:155], v[196:199], v[32:35]\n"
    "v_pk_add_u16 v192, s89, v192 clamp\n"
    "v_pk_add_u16 v193, s89, v193 clamp\n"
    "v_mfma_f32_16x16x32_bf16 v[64:67], a[40:43], v[196:199], v[64:67]\n"
    "v_pk_maximum3_f16 v98, v188, v189, v98\n"
    "v_pk_add_u16 v195, s89, v195 clamp\n"
    "v_mfma_f32_16x16x32_bf16 v[16:19], a[128:131], v[196:199], v[16:19]\n"
    "v_pk_add_u16 v188, s89, v188 clamp\n"
    "v_pk_maximum3_f16 v98, v190, v191, v98\n"
    "v_mfma_f32_16x16x32_bf16 v[32:35], a[148:151], v[192:195], v[32:35]\n"
    "buffer_load_dwordx4 v1, s[16:19], s67 offen offset:1024 sc1 lds\n"
    "v_mfma_f32_16x16x32_bf16 v[64:67], a[36:39], v[192:195], v[64:67]\n"
    "v_pk_maximum3_f16 v98, v184, v185, v98\n"
    "v_pk_add_u16 v189, s89, v189 clamp\n"
    "v_mfma_f32_16x16x32_bf16 v[48:51], a[16:19], v[196:199], v[48:51]\n"
    "v_pk_add_u16 v185, s89, v185 clamp\n"
    "v_pk_add_u16 v191, s89, v191 clamp\n"
    "v_pk_add_u16 v190, s89, v190 clamp\n"
    "v_mfma_f32_16x16x32_bf16 v[48:51], a[12:15], v[192:195], v[48:51]\n"
    "v_pk_maximum3_f16 v98, v186, v187, v98\n"
    "v_pk_add_u16 v186, s89, v186 clamp\n"
    "v_mfma_f32_16x16x32_bf16 v[16:19], a[124:127], v[192:195], v[16:19]\n"
    "v_pk_maximum3_f16 v98, v180, v181, v98\n"
    "v_pk_add_u16 v187, s89, v187 clamp\n"
    "v_mfma_f32_16x16x32_bf16 v[16:19], a[120:123], v[188:191], v[16:19]\n"
    "v_pk_add_u16 v180, s89, v180 clamp\n"
    "v_pk_add_u16 v184, s89, v184 clamp\n"
    "v_mfma_f32_16x16x32_bf16 v[64:67], a[32:35], v[188:191], v[64:67]\n"
    "v_accvgpr_write_b32 a177, v189\n"
    "v_accvgpr_write_b32 a190, v202\n"
    "v_mfma_f32_16x16x32_bf16 v[32:35], a[144:147], v[188:191], v[32:35]\n"
    "s_waitcnt vmcnt(14)\n"
    "buffer_load_dwordx4 v1, s[16:19], s67 offen offset:2048 sc1 lds\n"
    "v_smfmac_f32_16x16x64_bf16 v[112:115], v[52:55], v[76:83], v253\n"
    "s_barrier\n"
    "ds_read_b128 a[48:51], v252 offset:24576\n"
    "v_mfma_f32_16x16x32_bf16 v[48:51], a[8:11], v[188:191], v[48:51]\n"
    "ds_read_b128 a[68:71], v252 offset:34816\n"
    "ds_read_b128 a[72:75], v252 offset:25600\n"
    "v_mfma_f32_16x16x32_bf16 v[32:35], a[140:143], v[184:187], v[32:35]\n"
    "v_pk_add_u16 v181, s89, v181 clamp\n"
    "v_accvgpr_write_b32 a188, v200\n"
    "v_mfma_f32_16x16x32_bf16 v[48:51], a[4:7], v[184:187], v[48:51]\n"
    "v_pk_maximum3_f16 v98, v182, v183, v98\n"
    "v_accvgpr_write_b32 a176, v188\n"
    "v_mfma_f32_16x16x32_bf16 v[64:67], a[28:31], v[184:187], v[64:67]\n"
    "v_pk_add_u16 v183, s89, v183 clamp\n"
    "v_pk_add_u16 v182, s89, v182 clamp\n"
    "v_mfma_f32_16x16x32_bf16 v[16:19], a[116:119], v[184:187], v[16:19]\n"
    "ds_read_b128 a[52:55], v252 offset:26624\n"
    "ds_read_b128 a[76:79], v252 offset:27648\n"
    "v_mfma_f32_16x16x32_bf16 v[48:51], a[0:3], v[180:183], v[48:51]\n"
    "buffer_load_dwordx4 v1, s[16:19], s67 offen offset:3072 sc1 lds\n"
    "v_mfma_f32_16x16x32_bf16 v[32:35], a[136:139], v[180:183], v[32:35]\n"
    "ds_read_b128 a[92:95], v252 offset:35840\n"
    "ds_read_b128 a[88:91], v252 offset:33792\n"
    "v_mfma_f32_16x16x32_bf16 v[16:19], a[112:115], v[180:183], v[16:19]\n"
    "s_waitcnt lgkmcnt(6)\n"
    "v_accvgpr_write_b32 a180, v192\n"
    "v_accvgpr_write_b32 a181, v193\n"
    "v_mfma_f32_16x16x32_bf16 v[4:7], a[48:51], a[192:195], v[100:103]\n"
    "s_waitcnt lgkmcnt(4)\n"
    "ds_read_b128 a[60:63], v252 offset:30720\n"
    "ds_read_b128 a[56:59], v252 offset:28672\n"
    "v_mfma_f32_16x16x32_bf16 v[20:23], a[72:75], a[192:195], v[104:107]\n"
    "ds_read_b128 a[64:67], v252 offset:32768\n"
    "v_accvgpr_write_b32 a191, v203\n"
    "v_mfma_f32_16x16x32_bf16 v[28:31], a[72:75], a[240:243], v[104:107]\n"
    "s_add_u32 m0, s68, 4096\n"
    "v_accvgpr_write_b32 a175, v187\n"
    "v_accvgpr_write_b32 a182, v194\n"
    "v_mfma_f32_16x16x32_bf16 v[12:15], a[48:51], a[240:243], v[100:103]\n"
    "v_accvgpr_write_b32 a178, v190\n"
    "v_accvgpr_write_b32 a171, v183\n"
    "v_mfma_f32_16x16x32_bf16 v[8:11], a[68:71], a[236:239], v[100:103]\n"
    "buffer_load_dwordx4 v1, s[16:19], s83 offen offset:0 sc1 lds\n"
    "v_mfma_f32_16x16x32_bf16 v[64:67], a[24:27], v[180:183], v[64:67]\n"
    "s_waitcnt lgkmcnt(6)\n"
    "ds_read_b128 a[80:83], v252 offset:29696\n"
    "v_accvgpr_write_b32 a172, v184\n"
    "v_mfma_f32_16x16x32_bf16 v[4:7], a[52:55], a[196:199], v[4:7]\n"
    "s_waitcnt lgkmcnt(5)\n"
    "v_exp_f32 v16, v16\n"
    "v_mfma_f32_16x16x32_bf16 v[24:27], a[92:95], a[236:239], v[104:107]\n"
    "s_waitcnt lgkmcnt(4)\n"
    "v_accvgpr_write_b32 a174, v186\n"
    "v_accvgpr_write_b32 a169, v181\n"
    "v_mfma_f32_16x16x32_bf16 v[24:27], a[88:91], a[232:235], v[24:27]\n"
    "s_waitcnt lgkmcnt(2)\n"
    "ds_read_b128 a[84:87], v252 offset:31744\n"
    "v_mfma_f32_16x16x32_bf16 v[4:7], a[56:59], a[200:203], v[4:7]\n"
    "s_waitcnt lgkmcnt(2)\n"
    "v_accvgpr_write_b32 a189, v201\n"
    "v_accvgpr_write_b32 a179, v191\n"
    "v_mfma_f32_16x16x32_bf16 v[8:11], a[64:67], a[232:235], v[8:11]\n"
    "v_exp_f32 v17, v17\n"
    "v_mfma_f32_16x16x32_bf16 v[28:31], a[76:79], a[244:247], v[28:31]\n"
    "buffer_load_dwordx4 v1, s[16:19], s83 offen offset:1024 sc1 lds\n"
    "v_mfma_f32_16x16x32_bf16 v[20:23], a[76:79], a[196:199], v[20:23]\n"
    "v_accvgpr_write_b32 a184, v196\n"
    "v_accvgpr_write_b32 a170, v182\n"
    "v_mfma_f32_16x16x32_bf16 v[12:15], a[52:55], a[244:247], v[12:15]\n"
    "v_accvgpr_write_b32 a185, v197\n"
    "v_accvgpr_write_b32 a186, v198\n"
    "v_mfma_f32_16x16x32_bf16 v[12:15], a[56:59], a[248:251], v[12:15]\n"
    "v_cvt_pk_bf16_f32 v92, v16, v17\n"
    "v_mfma_f32_16x16x32_bf16 v[12:15], a[60:63], a[252:255], v[12:15]\n"
    "s_add_u32 m0, s70, 0\n"
    "s_add_u32 s83, s67, 24576\n"
    "v_accvgpr_write_b32 a168, v180\n"
    "v_mfma_f32_16x16x32_bf16 v[12:15], a[64:67], a[160:163], v[12:15]\n"
    "s_waitcnt lgkmcnt(1)\n"
    "v_accvgpr_write_b32 a183, v195\n"
    "v_pk_add_u16 v98, s89, v98\n"
    "v_mfma_f32_16x16x32_bf16 v[28:31], a[80:83], a[248:251], v[28:31]\n"
    "s_waitcnt lgkmcnt(0)\n"
    "v_accvgpr_write_b32 a173, v185\n"
    "v_accvgpr_write_b32 a187, v199\n"
    "v_mfma_f32_16x16x32_bf16 v[28:31], a[84:87], a[252:255], v[28:31]\n"
    "buffer_load_dwordx4 v1, s[16:19], s83 offen offset:0 sc1 lds\n"
    "v_mfma_f32_16x16x32_bf16 v[4:7], a[60:63], a[204:207], v[4:7]\n"
    "v_exp_f32 v18, v18\n"
    "v_mfma_f32_16x16x32_bf16 v[8:11], a[60:63], a[228:231], v[8:11]\n"
    "v_exp_f32 v19, v19\n"
    "v_mfma_f32_16x16x32_bf16 v[8:11], a[56:59], a[224:227], v[8:11]\n"
    "v_exp_f32 v32, v32\n"
    "v_mfma_f32_16x16x32_bf16 v[8:11], a[52:55], a[220:223], v[8:11]\n"
    "v_exp_f32 v35, v35\n"
    "v_mfma_f32_16x16x32_bf16 v[28:31], a[88:91], a[160:163], v[28:31]\n"
    "v_cvt_pk_bf16_f32 v93, v18, v19\n"
    "v_mfma_f32_16x16x32_bf16 v[24:27], a[84:87], a[228:231], v[24:27]\n"
    "v_exp_f32 v34, v34\n"
    "v_mfma_f32_16x16x32_bf16 v[20:23], a[80:83], a[200:203], v[20:23]\n"
    "v_exp_f32 v67, v67\n"
    "v_mfma_f32_16x16x32_bf16 v[16:19], a[68:71], a[188:191], v[100:103]\n"
    "buffer_load_dwordx4 v1, s[16:19], s83 offen offset:1024 sc1 lds\n"
    "v_mfma_f32_16x16x32_bf16 v[4:7], a[64:67], a[208:211], v[4:7]\n"
    "v_exp_f32 v33, v33\n"
    "v_mfma_f32_16x16x32_bf16 v[20:23], a[84:87], a[204:207], v[20:23]\n"
    "v_mfma_f32_16x16x32_bf16 v[20:23], a[88:91], a[208:211], v[20:23]\n"
    "v_mfma_f32_16x16x32_bf16 v[24:27], a[80:83], a[224:227], v[24:27]\n"
    "v_cvt_pk_bf16_f32 v95, v34, v35\n"
    "v_cvt_pk_bf16_f32 v94, v32, v33\n"
    "v_mfma_f32_16x16x32_bf16 v[32:35], a[92:95], a[188:191], v[104:107]\n"
    "s_waitcnt vmcnt(14)\n"
    "v_exp_f32 v66, v66\n"
    "v_mfma_f32_16x16x32_bf16 v[16:19], a[64:67], a[184:187], v[16:19]\n"
    "s_barrier\n"
    "ds_read_b128 a[112:115], v249 offset:0\n"
    "v_mfma_f32_16x16x32_bf16 v[32:35], a[88:91], a[184:187], v[32:35]\n"
    "v_exp_f32 v51, v51\n"
    "v_mfma_f32_16x16x32_bf16 v[32:35], a[84:87], a[180:183], v[32:35]\n"
    "buffer_load_dwordx4 v1, s[16:19], s83 offen offset:2048 sc1 lds\n"
    "v_mfma_f32_16x16x32_bf16 v[16:19], a[60:63], a[180:183], v[16:19]\n"
    "ds_read_b128 a[144:147], v249 offset:4096\n"
    "ds_read_b128 a[140:143], v249 offset:15360\n"
    "v_mfma_f32_16x16x32_bf16 v[16:19], a[56:59], a[176:179], v[16:19]\n"
    "ds_read_b128 a[152:155], v249 offset:5120\n"
    "ds_read_b128 a[132:135], v249 offset:14336\n"
    "v_mfma_f32_16x16x32_bf16 v[4:7], a[68:71], a[212:215], v[4:7]\n"
    "ds_read_b128 a[120:123], v249 offset:1024\n"
    "ds_read_b128 a[128:131], v249 offset:2048\n"
    "v_mfma_f32_16x16x32_bf16 v[8:11], a[48:51], a[216:219], v[8:11]\n"
    "ds_read_b128 a[124:127], v249 offset:13312\n"
    "ds_read_b128 a[136:139], v249 offset:3072\n"
    "v_mfma_f32_16x16x32_bf16 v[24:27], a[76:79], a[220:223], v[24:27]\n"
    "v_exp_f32 v65, v65\n"
    "v_mfma_f32_16x16x32_bf16 v[12:15], a[68:71], a[164:167], v[12:15]\n"
    "ds_read_b128 a[116:119], v249 offset:12288\n"
    "v_mfma_f32_16x16x32_bf16 v[32:35], a[80:83], a[176:179], v[32:35]\n"
    "buffer_load_dwordx4 v1, s[16:19], s83 offen offset:3072 sc1 lds\n"
    "v_mfma_f32_16x16x32_bf16 v[20:23], a[92:95], a[212:215], v[20:23]\n"
    "v_mfma_f32_16x16x32_bf16 v[16:19], a[52:55], a[172:175], v[16:19]\n"
    "s_waitcnt lgkmcnt(7)\n"
    "v_exp_f32 v50, v50\n"
    "v_mfma_f32_16x16x32_bf16 v[152:155], a[140:143], v[80:83], 0\n"
    "v_and_b32 v98, 0x80008000, v98\n"
    "v_mfma_f32_16x16x32_bf16 v[24:27], a[72:75], a[216:219], v[24:27]\n"
    "s_add_u32 m0, s70, 4096\n"
    "s_add_u32 s83, s67, 28672\n"
    "s_waitcnt lgkmcnt(2)\n"
    "v_mfma_f32_16x16x32_bf16 v[128:131], a[124:127], v[72:75], 0\n"
    "ds_read_b128 a[148:151], v249 offset:16384\n"
    "v_mfma_f32_16x16x32_bf16 v[148:151], a[132:135], v[80:83], 0\n"
    "s_add_u32 s76, s52, s60\n"
    "v_mfma_f32_16x16x32_bf16 v[28:31], a[92:95], a[164:167], v[28:31]\n"
    "buffer_load_dwordx4 v1, s[16:19], s83 offen offset:0 sc1 lds\n"
    "v_mfma_f32_16x16x32_bf16 v[160:163], a[124:127], v[88:91], 0\n"
    "s_waitcnt lgkmcnt(2)\n"
    "v_exp_f32 v48, v48\n"
    "v_mfma_f32_16x16x32_bf16 v[136:139], a[136:139], v[68:71], 0\n"
    "v_exp_f32 v49, v49\n"
    "v_mfma_f32_16x16x32_bf16 a[32:35], a[144:147], v[92:95], 0\n"
    "v_exp_f32 v27, v27\n"
    "v_mfma_f32_16x16x32_bf16 v[136:139], a[140:143], v[72:75], v[136:139]\n"
    "v_cmp_ne_u32 s[34:35], 0, v98\n"
    "v_mfma_f32_16x16x32_bf16 v[156:159], a[112:115], v[84:87], 0\n"
    "v_mfma_f32_16x16x32_bf16 v[132:135], a[128:131], v[68:71], 0\n"
    "v_cvt_pk_bf16_f32 v96, v48, v49\n"
    "v_mfma_f32_16x16x32_bf16 v[144:147], a[120:123], v[76:79], 0\n"
    "buffer_load_dwordx4 v1, s[16:19], s83 offen offset:1024 sc1 lds\n"
    "v_mfma_f32_16x16x32_bf16 v[16:19], a[48:51], a[168:171], v[16:19]\n"
    "s_waitcnt lgkmcnt(0)\n"
    "v_exp_f32 v20, v20\n"
    "v_mfma_f32_16x16x32_bf16 a[16:19], a[148:151], v[88:91], 0\n"
    "v_exp_f32 v64, v64\n"
    "v_mfma_f32_16x16x32_bf16 v[176:179], a[120:123], v[92:95], 0\n"
    "v_mfma_f32_16x16x32_bf16 v[124:127], a[112:115], v[68:71], 0\n"
    "v_exp_f32 v4, v4\n"
    "v_mfma_f32_16x16x32_bf16 v[156:159], a[116:119], v[88:91], v[156:159]\n"
    "v_exp_f32 v7, v7\n"
    "v_mfma_f32_16x16x32_bf16 v[188:191], a[148:151], v[72:75], 0\n"
    "v_exp_f32 v10, v10\n"
    "v_mfma_f32_16x16x32_bf16 v[32:35], a[76:79], a[172:175], v[32:35]\n"
    "v_mfma_f32_16x16x32_bf16 v[192:195], a[152:155], v[68:71], 0\n"
    "s_add_u32 s78, s52, s62\n"
    "v_exp_f32 v11, v11\n"
    "v_mfma_f32_16x16x32_bf16 v[124:127], a[116:119], v[72:75], v[124:127]\n"
    "s_waitcnt vmcnt(12)\n"
    "v_cvt_pk_bf16_f32 v97, v50, v51\n"
    "v_cvt_pk_bf16_f32 v99, v66, v67\n"
    "v_mfma_f32_16x16x32_bf16 v[164:167], a[128:131], v[84:87], 0\n"
    "v_exp_f32 v19, v19\n"
    "v_mfma_f32_16x16x32_bf16 v[160:163], a[120:123], v[84:87], v[160:163]\n"
    "v_cvt_pk_u16_u32 v204, v204, v208\n"
    "v_cvt_pk_bf16_f32 v98, v64, v65\n"
    "v_mfma_f32_16x16x32_bf16 a[20:23], a[152:155], v[84:87], 0\n"
    "v_cvt_pk_u16_u32 v214, v214, v218\n"
    "v_mfma_f32_16x16x32_bf16 v[148:151], a[128:131], v[76:79], v[148:151]\n"
    "v_cvt_pk_u16_u32 v207, v207, v211\n"
    "v_cvt_pk_u16_u32 v205, v205, v209\n"
    "v_mfma_f32_16x16x32_bf16 v[128:131], a[120:123], v[68:71], v[128:131]\n"
    "ds_read_b128 a[156:159], v249 offset:17408\n"
    "v_mfma_f32_16x16x32_bf16 v[172:175], a[116:119], v[96:99], 0\n"
    "s_add_u32 s77, s52, s61\n"
    "v_exp_f32 v25, v25\n"
    "v_mfma_f32_16x16x32_bf16 v[132:135], a[132:135], v[72:75], v[132:135]\n"
    "v_mfma_f32_16x16x32_bf16 v[144:147], a[124:127], v[80:83], v[144:147]\n"
    "v_mfma_f32_16x16x32_bf16 v[180:183], a[132:135], v[96:99], 0\n"
    "v_exp_f32 v17, v17\n"
    "v_mfma_f32_16x16x32_bf16 v[168:171], a[136:139], v[84:87], 0\n"
    "v_exp_f32 v9, v9\n"
    "v_mfma_f32_16x16x32_bf16 v[164:167], a[132:135], v[88:91], v[164:167]\n"
    "v_mfma_f32_16x16x32_bf16 v[152:155], a[136:139], v[76:79], v[152:155]\n"
    "v_exp_f32 v18, v18\n"
    "v_mfma_f32_16x16x32_bf16 v[176:179], a[124:127], v[96:99], v[176:179]\n"
    "ds_read_b128 a[132:135], v249 offset:20480\n"
    "v_mfma_f32_16x16x32_bf16 v[140:143], a[116:119], v[80:83], 0\n"
    "v_exp_f32 v31, v31\n"
    "v_mfma_f32_16x16x32_bf16 v[188:191], a[144:147], v[68:71], v[188:191]\n"
    "v_mfma_f32_16x16x32_bf16 v[168:171], a[140:143], v[88:91], v[168:171]\n"
    "v_exp_f32 v12, v12\n"
    "v_mfma_f32_16x16x32_bf16 v[180:183], a[128:131], v[92:95], v[180:183]\n"
    "v_cvt_pk_u16_u32 v212, v212, v216\n"
    "v_cvt_pk_u16_u32 v223, v223, v227\n"
    "v_mfma_f32_16x16x32_bf16 v[140:143], a[112:115], v[76:79], v[140:143]\n"
    "s_waitcnt lgkmcnt(1)\n"
    "v_exp_f32 v21, v21\n"
    "v_mfma_f32_16x16x32_bf16 a[36:39], a[156:159], v[96:99], 0\n"
    "ds_read_b128 a[128:131], v249 offset:8192\n"
    "v_cvt_pk_u16_u32 v222, v222, v226\n"
    "v_mfma_f32_16x16x32_bf16 v[172:175], a[112:115], v[92:95], v[172:175]\n"
    "v_or3_b32 v243, v204, v205, v243\n"
    "s_waitcnt lgkmcnt(1)\n"
    "v_mfma_f32_16x16x32_bf16 a[96:99], a[132:135], v[96:99], 0\n"
    "v_mfma_f32_16x16x32_bf16 a[20:23], a[156:159], v[88:91], a[20:23]\n"
    "ds_read_b128 a[116:119], v249 offset:18432\n"
    "v_mfma_f32_16x16x32_bf16 a[16:19], a[144:147], v[84:87], a[16:19]\n"
    "v_exp_f32 v15, v15\n"
    "v_mfma_f32_16x16x32_bf16 a[0:3], a[144:147], v[76:79], 0\n"
    "ds_read_b128 a[120:123], v249 offset:7168\n"
    "v_mfma_f32_16x16x32_bf16 v[184:187], a[140:143], v[96:99], 0\n"
    "v_exp_f32 v26, v26\n"
    "v_mfma_f32_16x16x32_bf16 a[64:67], a[132:135], v[80:83], 0\n"
    "v_cvt_pk_u16_u32 v206, v206, v210\n"
    "v_mfma_f32_16x16x32_bf16 a[36:39], a[152:155], v[92:95], a[36:39]\n"
    "ds_read_b128 a[124:127], v249 offset:19456\n"
    "ds_read_b128 a[140:143], v249 offset:21504\n"
    "v_mfma_f32_16x16x32_bf16 a[0:3], a[148:151], v[80:83], a[0:3]\n"
    "v_cvt_pk_u16_u32 v220, v220, v224\n"
    "v_cvt_pk_u16_u32 v221, v221, v225\n"
    "v_mfma_f32_16x16x32_bf16 v[184:187], a[136:139], v[92:95], v[184:187]\n"
    "s_waitcnt lgkmcnt(4)\n"
    "v_or3_b32 v243, v206, v207, v243\n"
    "v_cvt_pk_u16_u32 v213, v213, v217\n"
    "v_mfma_f32_16x16x32_bf16 a[80:83], a[128:131], v[84:87], 0\n"
    "s_add_u32 s67, s67, 49152\n"
    "ds_read_b128 a[136:139], v249 offset:9216\n"
    "ds_read_b128 a[144:147], v249 offset:10240\n"
    "v_mfma_f32_16x16x32_bf16 a[64:67], a[128:131], v[76:79], a[64:67]\n"
    "v_exp_f32 v23, v23\n"
    "v_mfma_f32_16x16x32_bf16 a[32:35], a[148:151], v[96:99], a[32:35]\n"
    "v_mfma_f32_16x16x32_bf16 a[4:7], a[156:159], v[80:83], 0\n"
    "v_mfma_f32_16x16x32_bf16 a[4:7], a[152:155], v[76:79], a[4:7]\n"
    "v_or3_b32 v243, v212, v213, v243\n"
    "v_cvt_pk_u16_u32 v215, v215, v219\n"
    "v_mfma_f32_16x16x32_bf16 v[192:195], a[156:159], v[72:75], v[192:195]\n"
    "v_exp_f32 v28, v28\n"
    "v_mfma_f32_16x16x32_bf16 a[96:99], a[128:131], v[92:95], a[96:99]\n"
    "s_waitcnt lgkmcnt(3)\n"
    "ds_read_b128 a[112:115], v249 offset:6144\n"
    "ds_read_b128 a[148:151], v249 offset:22528\n"
    "v_mfma_f32_16x16x32_bf16 v[200:203], a[124:127], v[72:75], 0\n"
    "v_exp_f32 v16, v16\n"
    "v_mfma_f32_16x16x32_bf16 a[12:15], a[120:123], v[76:79], 0\n"
    "buffer_load_dwordx2 v[236:237], v241, s[36:39], s81 offen offset:0 sc1\n"
    "v_mfma_f32_16x16x32_bf16 v[200:203], a[120:123], v[68:71], v[200:203]\n"
    "s_waitcnt lgkmcnt(4)\n"
    "v_add_u32 v251, 1536, v251\n"
    "v_mfma_f32_16x16x32_bf16 a[100:103], a[140:143], v[96:99], 0\n"
    "v_exp_f32 v29, v29\n"
    "v_mfma_f32_16x16x32_bf16 a[28:31], a[124:127], v[88:91], 0\n"
    "buffer_store_dword v204, v244, s[20:23], s76 offen offset:0 sc1\n"
    "v_exp_f32 v30, v30\n"
    "v_mfma_f32_16x16x32_bf16 v[32:35], a[72:75], a[168:171], v[32:35]\n"
    "v_mfma_f32_16x16x32_bf16 a[40:43], a[116:119], v[96:99], 0\n"
    "v_exp_f32 v13, v13\n"
    "v_mfma_f32_16x16x32_bf16 a[68:71], a[140:143], v[80:83], 0\n"
    "s_waitcnt lgkmcnt(3)\n"
    "v_mfma_f32_16x16x32_bf16 a[52:55], a[136:139], v[68:71], 0\n"
    "buffer_store_dword v205, v244, s[20:23], s76 offen offset:1024 sc1\n"
    "v_exp_f32 v22, v22\n"
    "v_mfma_f32_16x16x32_bf16 a[84:87], a[136:139], v[84:87], 0\n"
    "v_exp_f32 v24, v24\n"
    "v_mfma_f32_16x16x32_bf16 a[12:15], a[124:127], v[80:83], a[12:15]\n"
    "ds_read_b128 a[156:159], v249 offset:23552\n"
    "v_mfma_f32_16x16x32_bf16 a[44:47], a[120:123], v[92:95], 0\n"
    "v_exp_f32 v35, v35\n"
    "v_mfma_f32_16x16x32_bf16 a[80:83], a[132:135], v[88:91], a[80:83]\n"
    "buffer_store_dword v206, v244, s[20:23], s76 offen offset:2048 sc1\n"
    "ds_read_b128 v[100:103], v250 offset:1664\n"
    "v_mfma_f32_16x16x32_bf16 a[84:87], a[140:143], v[88:91], a[84:87]\n"
    "v_mfma_f32_16x16x32_bf16 a[8:11], a[116:119], v[80:83], 0\n"
    "s_waitcnt lgkmcnt(2)\n"
    "buffer_store_dword v207, v244, s[20:23], s76 offen offset:3072 sc1\n"
    "v_or3_b32 v243, v214, v215, v243\n"
    "v_or3_b32 v243, v220, v221, v243\n"
    "v_mfma_f32_16x16x32_bf16 a[88:91], a[148:151], v[88:91], 0\n"
    "v_mfma_f32_16x16x32_bf16 a[44:47], a[124:127], v[96:99], a[44:47]\n"
    "buffer_store_dword v212, v244, s[20:23], s77 offen offset:0 sc1\n"
    "v_mfma_f32_16x16x32_bf16 a[28:31], a[120:123], v[84:87], a[28:31]\n"
    "v_mfma_f32_16x16x32_bf16 a[24:27], a[112:115], v[84:87], 0\n"
    "ds_read_b128 a[152:155], v249 offset:11264\n"
    "v_mfma_f32_16x16x32_bf16 a[40:43], a[112:115], v[92:95], a[40:43]\n"
    "buffer_store_dword v213, v244, s[20:23], s77 offen offset:1024 sc1\n"
    "v_exp_f32 v6, v6\n"
    "v_mfma_f32_16x16x32_bf16 a[48:51], a[128:131], v[68:71], 0\n"
    "s_waitcnt lgkmcnt(2)\n"
    "v_exp_f32 v8, v8\n"
    "v_mfma_f32_16x16x32_bf16 a[76:79], a[156:159], v[80:83], 0\n"
    "buffer_store_dword v214, v244, s[20:23], s77 offen offset:2048 sc1\n"
    "v_mfma_f32_16x16x32_bf16 a[104:107], a[144:147], v[92:95], 0\n"
    "v_exp_f32 v34, v34\n"
    "v_mfma_f32_16x16x32_bf16 a[52:55], a[140:143], v[72:75], a[52:55]\n"
    "buffer_store_dword v215, v244, s[20:23], s77 offen offset:3072 sc1\n"
    "v_exp_f32 v14, v14\n"
    "v_mfma_f32_16x16x32_bf16 a[48:51], a[132:135], v[72:75], a[48:51]\n"
    "v_mfma_f32_16x16x32_bf16 a[24:27], a[116:119], v[88:91], a[24:27]\n"
    "buffer_store_dword v220, v244, s[20:23], s78 offen offset:0 sc1\n"
    "ds_read_b128 a[120:123], v3 offset:16384\n"
    "v_mfma_f32_16x16x32_bf16 v[196:199], a[112:115], v[68:71], 0\n"
    "v_mfma_f32_16x16x32_bf16 v[196:199], a[116:119], v[72:75], v[196:199]\n"
    "v_mfma_f32_16x16x32_bf16 a[8:11], a[112:115], v[76:79], a[8:11]\n"
    "s_waitcnt lgkmcnt(1)\n"
    "ds_read_b128 a[132:135], v3 offset:22528\n"
    "v_mfma_f32_16x16x32_bf16 a[76:79], a[152:155], v[76:79], a[76:79]\n"
    "buffer_store_dword v221, v244, s[20:23], s78 offen offset:1024 sc1\n"
    "v_mfma_f32_16x16x32_bf16 a[108:111], a[156:159], v[96:99], 0\n"
    "ds_read_b128 a[124:127], v3 offset:18432\n"
    "ds_read_b128 a[128:131], v3 offset:20480\n"
    "v_mfma_f32_16x16x32_bf16 a[92:95], a[152:155], v[84:87], 0\n"
    "v_or3_b32 v243, v222, v223, v243\n"
    "v_mfma_f32_16x16x32_bf16 a[92:95], a[156:159], v[88:91], a[92:95]\n"
    "ds_read_b128 a[112:115], v3 offset:12288\n"
    "ds_read_b128 a[116:119], v3 offset:14336\n"
    "v_mfma_f32_16x16x32_bf16 a[72:75], a[144:147], v[76:79], 0\n"
    "s_waitcnt vmcnt(11)\n"
    "ds_add_u32 v247, v246\n"
    "v_mfma_f32_16x16x32_bf16 a[56:59], a[148:151], v[72:75], 0\n"
    "v_exp_f32 v32, v32\n"
    "v_mfma_f32_16x16x32_bf16 a[60:63], a[152:155], v[68:71], 0\n"
    "v_and_b32 v208, 0xff00ff00, v243\n"
    "v_add_u32 v250, 1536, v250\n"
    "v_mfma_f32_16x16x32_bf16 a[68:71], a[136:139], v[76:79], a[68:71]\n"
    "v_exp_f32 v33, v33\n"
    "v_mfma_f32_16x16x32_bf16 a[88:91], a[144:147], v[84:87], a[88:91]\n"
    "buffer_store_dword v222, v244, s[20:23], s78 offen offset:2048 sc1\n"
    "v_exp_f32 v5, v5\n"
    "v_mfma_f32_16x16x32_bf16 a[72:75], a[148:151], v[80:83], a[72:75]\n"
    "v_cmp_ne_u32 vcc, 0, v208\n"
    "v_mfma_f32_16x16x32_bf16 a[104:107], a[148:151], v[96:99], a[104:107]\n"
    "v_mfma_f32_16x16x32_bf16 a[108:111], a[152:155], v[92:95], a[108:111]\n"
    "buffer_store_dword v223, v244, s[20:23], s78 offen offset:3072 sc1\n"
    "v_add_u32 v3, 49152, v252\n"
    "v_mfma_f32_16x16x32_bf16 a[56:59], a[144:147], v[68:71], a[56:59]\n"
    "s_waitcnt lgkmcnt(1)\n"
    "v_cndmask_b32 v208, 2.0, v241, vcc\n"
    "v_mfma_f32_16x16x32_bf16 a[60:63], a[156:159], v[72:75], a[60:63]\n"
    "v_mfma_f32_16x16x32_bf16 a[100:103], a[136:139], v[92:95], a[100:103]\n"
    "buffer_store_dwordx2 v[238:239], v208, s[36:39], 0 offen offset:0 sc1\n"
    "v_smfmac_f32_16x16x64_bf16 v[120:123], v[52:55], v[92:99], v253\n"
    "s_branch .LPV\n"
    ".fill ((0x20000 - (.LwKe - .LwK) - (. - _Z8attn_fwdPKtPKiS2_S0_S0_S0_S0_PcPt)) & 4095) / 4, 4, 0xBF800000\n"
    ".LwK:\n"
    "s_branch .LwKe\n"
    ".LPK:\n"
    "v_mfma_f32_16x16x32_bf16 v[36:39], a[112:115], a[192:195], v[100:103]\n"
    "s_add_u32 s71, s71, 98304\n"
    "ds_read_b128 a[136:139], v2 offset:13312\n"
    "v_cvt_pk_bf16_f32 v68, v4, v5\n"
    "v_mfma_f32_16x16x32_bf16 v[36:39], a[116:119], a[196:199], v[36:39]\n"
    "s_add_u32 s75, s71, s63\n"
    "ds_read_b128 a[140:143], v2 offset:15360\n"
    "v_cvt_pk_bf16_f32 v69, v6, v7\n"
    "v_mfma_f32_16x16x32_bf16 v[36:39], a[120:123], a[200:203], v[36:39]\n"
    "s_add_u32 s76, s75, 6144\n"
    "ds_read_b128 a[144:147], v2 offset:17408\n"
    "v_cvt_pk_bf16_f32 v76, v8, v9\n"
    "s_nop 0\n"
    "v_mfma_f32_16x16x32_bf16 v[36:39], a[124:127], a[204:207], v[36:39]\n"
    "s_add_u32 s77, s75, 24576\n"
    "ds_read_b128 a[148:151], v2 offset:19456\n"
    "v_cvt_pk_bf16_f32 v77, v10, v11\n"
    "v_mfma_f32_16x16x32_bf16 v[36:39], a[128:131], a[208:211], v[36:39]\n"
    "s_add_u32 s78, s75, 30720\n"
    "ds_read_b128 a[152:155], v2 offset:21504\n"
    "v_cvt_pk_bf16_f32 v84, v12, v13\n"
    "v_mfma_f32_16x16x32_bf16 v[36:39], a[132:135], a[212:215], v[36:39]\n"
    "ds_read_b128 a[156:159], v2 offset:23552\n"
    "v_cvt_pk_bf16_f32 v85, v14, v15\n"
    "v_mfma_f32_16x16x32_bf16 v[40:43], a[132:135], a[236:239], v[100:103]\n"
    "ds_read_b128 v[104:107], v250 offset:144\n"
    "v_cvt_pk_bf16_f32 v92, v16, v17\n"
    "v_mfma_f32_16x16x32_bf16 v[40:43], a[128:131], a[232:235], v[40:43]\n"
    "s_add_u32 s68, s68, 24576\n"
    "v_cvt_pk_bf16_f32 v93, v18, v19\n"
    "v_cvt_pk_bf16_f32 v70, v20, v21\n"
    "v_mfma_f32_16x16x32_bf16 v[40:43], a[124:127], a[228:231], v[40:43]\n"
    "s_cmp_ge_u32 s68, s69\n"
    "s_cselect_b32 s68, s65, s68\n"
    "v_cvt_pk_bf16_f32 v71, v22, v23\n"
    "v_mfma_f32_16x16x32_bf16 v[40:43], a[120:123], a[224:227], v[40:43]\n"
    "v_cvt_pk_bf16_f32 v78, v24, v25\n"
    "v_cvt_pk_bf16_f32 v79, v26, v27\n"
    "v_mfma_f32_16x16x32_bf16 v[40:43], a[116:119], a[220:223], v[40:43]\n"
    "v_cvt_pk_bf16_f32 v86, v28, v29\n"
    "v_cvt_pk_bf16_f32 v87, v30, v31\n"
    "v_mfma_f32_16x16x32_bf16 v[40:43], a[112:115], a[216:219], v[40:43]\n"
    "v_cvt_pk_bf16_f32 v94, v32, v33\n"
    "v_cvt_pk_bf16_f32 v95, v34, v35\n"
    "v_mfma_f32_16x16x32_bf16 v[44:47], a[112:115], a[240:243], v[100:103]\n"
    "v_exp_f32 v36, v36\n"
    "v_mfma_f32_16x16x32_bf16 v[44:47], a[116:119], a[244:247], v[44:47]\n"
    "v_exp_f32 v37, v37\n"
    "v_mfma_f32_16x16x32_bf16 v[44:47], a[120:123], a[248:251], v[44:47]\n"
    "v_exp_f32 v38, v38\n"
    "v_mfma_f32_16x16x32_bf16 v[44:47], a[124:127], a[252:255], v[44:47]\n"
    "v_exp_f32 v39, v39\n"
    "v_mfma_f32_16x16x32_bf16 v[44:47], a[128:131], a[160:163], v[44:47]\n"
    "v_cvt_pk_bf16_f32 v72, v36, v37\n"
    "v_cvt_pk_bf16_f32 v73, v38, v39\n"
    "v_mfma_f32_16x16x32_bf16 v[44:47], a[132:135], a[164:167], v[44:47]\n"
    "v_exp_f32 v40, v40\n"
    "v_mfma_f32_16x16x32_bf16 v[48:51], a[132:135], a[188:191], v[100:103]\n"
    "ds_read_b32 v245, v247\n"
    "v_mfma_f32_16x16x32_bf16 v[48:51], a[128:131], a[184:187], v[48:51]\n"
    "s_add_u32 m0, s68, 0\n"
    "v_exp_f32 v41, v41\n"
    "v_mfma_f32_16x16x32_bf16 v[48:51], a[124:127], a[180:183], v[48:51]\n"
    "v_exp_f32 v42, v42\n"
    "v_mfma_f32_16x16x32_bf16 v[48:51], a[120:123], a[176:179], v[48:51]\n"
    "v_exp_f32 v43, v43\n"
    "v_mfma_f32_16x16x32_bf16 v[48:51], a[116:119], a[172:175], v[48:51]\n"
    "s_waitcnt vmcnt(10)\n"
    "v_cmp_eq_u64 s[54:55], v[236:237], v[238:239]\n"
    "v_cvt_pk_bf16_f32 v80, v40, v41\n"
    "v_mfma_f32_16x16x32_bf16 v[48:51], a[112:115], a[168:171], v[48:51]\n"
    "s_waitcnt lgkmcnt(1)\n"
    "v_cvt_pk_bf16_f32 v81, v42, v43\n"
    "v_mfma_f32_16x16x32_bf16 v[52:55], a[136:139], a[192:195], v[104:107]\n"
    "s_waitcnt lgkmcnt(0)\n"
    "v_cmp_le_u32 s[56:57], s59, v245\n"
    "v_mfma_f32_16x16x32_bf16 v[52:55], a[140:143], a[196:199], v[52:55]\n"
    "v_exp_f32 v44, v44\n"
    "v_mfma_f32_16x16x32_bf16 v[52:55], a[144:147], a[200:203], v[52:55]\n"
    "s_and_b64 s[54:55], s[54:55], s[56:57]\n"
    "s_cmp_eq_u64 s[54:55], -1\n"
    "v_mfma_f32_16x16x32_bf16 v[52:55], a[148:151], a[204:207], v[52:55]\n"
    "s_cbranch_scc0 .Lpoll1\n"
    ".Lpoll1r:\n"
    "v_mfma_f32_16x16x32_bf16 v[52:55], a[152:155], a[208:211], v[52:55]\n"
    "buffer_load_dwordx4 v1, s[16:19], s67 offen offset:0 sc1 lds\n"
    "v_mfma_f32_16x16x32_bf16 v[52:55], a[156:159], a[212:215], v[52:55]\n"
    "ds_read_b128 a[112:115], v3 offset:0\n"
    "v_mfma_f32_16x16x32_bf16 v[56:59], a[156:159], a[236:239], v[104:107]\n"
    "ds_read_b128 a[116:119], v3 offset:2048\n"
    "v_mfma_f32_16x16x32_bf16 v[56:59], a[152:155], a[232:235], v[56:59]\n"
    "ds_read_b128 a[120:123], v3 offset:4096\n"
    "v_mfma_f32_16x16x32_bf16 v[56:59], a[148:151], a[228:231], v[56:59]\n"
    "buffer_load_dwordx4 v1, s[16:19], s67 offen offset:1024 sc1 lds\n"
    "v_mfma_f32_16x16x32_bf16 v[56:59], a[144:147], a[224:227], v[56:59]\n"
    "ds_read_b128 a[124:127], v3 offset:6144\n"
    "v_mfma_f32_16x16x32_bf16 v[56:59], a[140:143], a[220:223], v[56:59]\n"
    "ds_read_b128 a[128:131], v3 offset:8192\n"
    "v_mfma_f32_16x16x32_bf16 v[56:59], a[136:139], a[216:219], v[56:59]\n"
    "ds_read_b128 a[132:135], v3 offset:10240\n"
    "v_mfma_f32_16x16x32_bf16 v[60:63], a[136:139], a[240:243], v[104:107]\n"
    "buffer_load_dwordx4 v1, s[16:19], s67 offen offset:2048 sc1 lds\n"
    "v_mfma_f32_16x16x32_bf16 v[60:63], a[140:143], a[244:247], v[60:63]\n"
    "ds_read_b128 v[100:103], v250 offset:768\n"
    "v_mfma_f32_16x16x32_bf16 v[60:63], a[144:147], a[248:251], v[60:63]\n"
    "v_exp_f32 v52, v52\n"
    "v_mfma_f32_16x16x32_bf16 v[60:63], a[148:151], a[252:255], v[60:63]\n"
    "v_exp_f32 v53, v53\n"
    "v_mfma_f32_16x16x32_bf16 v[60:63], a[152:155], a[160:163], v[60:63]\n"
    "buffer_load_dwordx4 v1, s[16:19], s67 offen offset:3072 sc1 lds\n"
    "v_mfma_f32_16x16x32_bf16 v[60:63], a[156:159], a[164:167], v[60:63]\n"
    "s_add_u32 m0, s68, 4096\n"
    "v_exp_f32 v54, v54\n"
    "v_mfma_f32_16x16x32_bf16 v[64:67], a[156:159], a[188:191], v[104:107]\n"
    "s_add_u32 s83, s67, 4096\n"
    "v_exp_f32 v55, v55\n"
    "v_mfma_f32_16x16x32_bf16 v[64:67], a[152:155], a[184:187], v[64:67]\n"
    "v_cvt_pk_bf16_f32 v74, v52, v53\n"
    "v_cvt_pk_bf16_f32 v75, v54, v55\n"
    "v_mfma_f32_16x16x32_bf16 v[64:67], a[148:151], a[180:183], v[64:67]\n"
    "buffer_load_dwordx4 v1, s[16:19], s83 offen offset:0 sc1 lds\n"
    "v_mfma_f32_16x16x32_bf16 v[64:67], a[144:147], a[176:179], v[64:67]\n"
    "v_exp_f32 v56, v56\n"
    "v_mfma_f32_16x16x32_bf16 v[64:67], a[140:143], a[172:175], v[64:67]\n"
    "v_exp_f32 v57, v57\n"
    "v_mfma_f32_16x16x32_bf16 v[64:67], a[136:139], a[168:171], v[64:67]\n"
    "s_waitcnt lgkmcnt(0)\n"
    "v_exp_f32 v58, v58\n"
    "v_mfma_f32_16x16x32_bf16 v[4:7], a[112:115], a[192:195], v[100:103]\n"
    "buffer_load_dwordx4 v1, s[16:19], s83 offen offset:1024 sc1 lds\n"
    "v_mfma_f32_16x16x32_bf16 v[4:7], a[116:119], a[196:199], v[4:7]\n"
    "s_add_u32 m0, s64, 0\n"
    "ds_read_b128 a[136:139], v3 offset:1024\n"
    "v_cvt_pk_bf16_f32 v82, v56, v57\n"
    "v_mfma_f32_16x16x32_bf16 v[4:7], a[120:123], a[200:203], v[4:7]\n"
    "s_add_u32 s83, s67, 24576\n"
    "ds_read_b128 a[140:143], v3 offset:3072\n"
    "v_mfma_f32_16x16x32_bf16 v[4:7], a[124:127], a[204:207], v[4:7]\n"
    "ds_read_b128 a[144:147], v3 offset:5120\n"
    "v_mfma_f32_16x16x32_bf16 v[4:7], a[128:131], a[208:211], v[4:7]\n"
    "buffer_load_dwordx4 v1, s[16:19], s83 offen offset:0 sc1 lds\n"
    "v_mfma_f32_16x16x32_bf16 v[4:7], a[132:135], a[212:215], v[4:7]\n"
    "ds_read_b128 a[148:151], v3 offset:7168\n"
    "v_mfma_f32_16x16x32_bf16 v[8:11], a[132:135], a[236:239], v[100:103]\n"
    "ds_read_b128 a[152:155], v3 offset:9216\n"
    "v_mfma_f32_16x16x32_bf16 v[8:11], a[128:131], a[232:235], v[8:11]\n"
    "ds_read_b128 a[156:159], v3 offset:11264\n"
    "v_mfma_f32_16x16x32_bf16 v[8:11], a[124:127], a[228:231], v[8:11]\n"
    "buffer_load_dwordx4 v1, s[16:19], s83 offen offset:1024 sc1 lds\n"
    "v_mfma_f32_16x16x32_bf16 v[8:11], a[120:123], a[224:227], v[8:11]\n"
    "ds_read_b128 v[104:107], v250 offset:784\n"
    "v_mfma_f32_16x16x32_bf16 v[8:11], a[116:119], a[220:223], v[8:11]\n"
    "v_exp_f32 v59, v59\n"
    "v_mfma_f32_16x16x32_bf16 v[8:11], a[112:115], a[216:219], v[8:11]\n"
    "v_cvt_pk_bf16_f32 v83, v58, v59\n"
    "v_mfma_f32_16x16x32_bf16 v[12:15], a[112:115], a[240:243], v[100:103]\n"
    "buffer_load_dwordx4 v1, s[16:19], s83 offen offset:2048 sc1 lds\n"
    "v_mfma_f32_16x16x32_bf16 v[12:15], a[116:119], a[244:247], v[12:15]\n"
    "v_exp_f32 v45, v45\n"
    "v_mfma_f32_16x16x32_bf16 v[12:15], a[120:123], a[248:251], v[12:15]\n"
    "v_exp_f32 v46, v46\n"
    "v_mfma_f32_16x16x32_bf16 v[12:15], a[124:127], a[252:255], v[12:15]\n"
    "v_exp_f32 v47, v47\n"
    "v_mfma_f32_16x16x32_bf16 v[12:15], a[128:131], a[160:163], v[12:15]\n"
    "buffer_load_dwordx4 v1, s[16:19], s83 offen offset:3072 sc1 lds\n"
    "v_mfma_f32_16x16x32_bf16 v[12:15], a[132:135], a[164:167], v[12:15]\n"
    "s_add_u32 m0, s64, 4096\n"
    "v_exp_f32 v60, v60\n"
    "v_mfma_f32_16x16x32_bf16 v[16:19], a[132:135], a[188:191], v[100:103]\n"
    "s_add_u32 s83, s67, 28672\n"
    "v_exp_f32 v61, v61\n"
    "v_mfma_f32_16x16x32_bf16 v[16:19], a[128:131], a[184:187], v[16:19]\n"
    "v_exp_f32 v62, v62\n"
    "v_mfma_f32_16x16x32_bf16 v[16:19], a[124:127], a[180:183], v[16:19]\n"
    "buffer_load_dwordx4 v1, s[16:19], s83 offen offset:0 sc1 lds\n"
    "v_mfma_f32_16x16x32_bf16 v[16:19], a[120:123], a[176:179], v[16:19]\n"
    "v_exp_f32 v63, v63\n"
    "v_mfma_f32_16x16x32_bf16 v[16:19], a[116:119], a[172:175], v[16:19]\n"
    "v_cvt_pk_bf16_f32 v88, v44, v45\n"
    "v_cvt_pk_bf16_f32 v89, v46, v47\n"
    "v_mfma_f32_16x16x32_bf16 v[16:19], a[112:115], a[168:171], v[16:19]\n"
    "s_waitcnt lgkmcnt(0)\n"
    "v_cvt_pk_bf16_f32 v90, v60, v61\n"
    "v_cvt_pk_bf16_f32 v91, v62, v63\n"
    "v_mfma_f32_16x16x32_bf16 v[20:23], a[136:139], a[192:195], v[104:107]\n"
    "buffer_load_dwordx4 v1, s[16:19], s83 offen offset:1024 sc1 lds\n"
    "v_mfma_f32_16x16x32_bf16 v[20:23], a[140:143], a[196:199], v[20:23]\n"
    "s_add_u32 s67, s67, 49152\n"
    "ds_read_b128 a[112:115], v248 offset:0\n"
    "v_mfma_f32_16x16x32_bf16 v[20:23], a[144:147], a[200:203], v[20:23]\n"
    "s_add_u32 s59, s59, 4\n"
    "ds_read_b128 a[116:119], v248 offset:12288\n"
    "v_mfma_f32_16x16x32_bf16 v[20:23], a[148:151], a[204:207], v[20:23]\n"
    "s_waitcnt vmcnt(12)\n"
    "ds_read_b128 a[120:123], v248 offset:1024\n"
    "v_mfma_f32_16x16x32_bf16 v[20:23], a[152:155], a[208:211], v[20:23]\n"
    "buffer_store_dwordx2 v[238:239], v240, s[36:39], s80 offen offset:0 sc1\n"
    "v_mfma_f32_16x16x32_bf16 v[20:23], a[156:159], a[212:215], v[20:23]\n"
    "buffer_load_dwordx2 v[204:205], v242, s[28:31], s75 offen offset:0 nt\n"
    "v_mfma_f32_16x16x32_bf16 v[24:27], a[156:159], a[236:239], v[104:107]\n"
    "s_add_u32 s80, s80, 256\n"
    "ds_read_b128 a[124:127], v248 offset:13312\n"
    "v_mfma_f32_16x16x32_bf16 v[24:27], a[152:155], a[232:235], v[24:27]\n"
    "buffer_load_dwordx2 v[206:207], v242, s[28:31], s75 offen offset:768 nt\n"
    "v_mfma_f32_16x16x32_bf16 v[24:27], a[148:151], a[228:231], v[24:27]\n"
    "ds_read_b128 a[128:131], v248 offset:2048\n"
    "v_mfma_f32_16x16x32_bf16 v[24:27], a[144:147], a[224:227], v[24:27]\n"
    "buffer_load_dwordx2 v[208:209], v242, s[28:31], s75 offen offset:3072 nt\n"
    "v_mfma_f32_16x16x32_bf16 v[24:27], a[140:143], a[220:223], v[24:27]\n"
    "ds_read_b128 a[132:135], v248 offset:14336\n"
    "v_mfma_f32_16x16x32_bf16 v[24:27], a[136:139], a[216:219], v[24:27]\n"
    "buffer_load_dwordx2 v[210:211], v242, s[28:31], s75 offen offset:3840 nt\n"
    "v_mfma_f32_16x16x32_bf16 v[28:31], a[136:139], a[240:243], v[104:107]\n"
    "v_exp_f32 v48, v48\n"
    "v_mfma_f32_16x16x32_bf16 v[28:31], a[140:143], a[244:247], v[28:31]\n"
    "buffer_load_dwordx2 v[212:213], v242, s[28:31], s76 offen offset:0 nt\n"
    "v_mfma_f32_16x16x32_bf16 v[28:31], a[144:147], a[248:251], v[28:31]\n"
    "v_exp_f32 v49, v49\n"
    "v_mfma_f32_16x16x32_bf16 v[28:31], a[148:151], a[252:255], v[28:31]\n"
    "buffer_load_dwordx2 v[214:215], v242, s[28:31], s76 offen offset:768 nt\n"
    "v_mfma_f32_16x16x32_bf16 v[28:31], a[152:155], a[160:163], v[28:31]\n"
    "v_exp_f32 v50, v50\n"
    "v_mfma_f32_16x16x32_bf16 v[28:31], a[156:159], a[164:167], v[28:31]\n"
    "buffer_load_dwordx2 v[216:217], v242, s[28:31], s76 offen offset:3072 nt\n"
    "v_mfma_f32_16x16x32_bf16 v[32:35], a[156:159], a[188:191], v[104:107]\n"
    "v_exp_f32 v51, v51\n"
    "v_mfma_f32_16x16x32_bf16 v[32:35], a[152:155], a[184:187], v[32:35]\n"
    "buffer_load_dwordx2 v[218:219], v242, s[28:31], s76 offen offset:3840 nt\n"
    "v_mfma_f32_16x16x32_bf16 v[32:35], a[148:151], a[180:183], v[32:35]\n"
    "v_exp_f32 v64, v64\n"
    "v_mfma_f32_16x16x32_bf16 v[32:35], a[144:147], a[176:179], v[32:35]\n"
    "buffer_load_dwordx2 v[220:221], v242, s[28:31], s77 offen offset:0 nt\n"
    "v_mfma_f32_16x16x32_bf16 v[32:35], a[140:143], a[172:175], v[32:35]\n"
    "v_exp_f32 v65, v65\n"
    "v_mfma_f32_16x16x32_bf16 v[32:35], a[136:139], a[168:171], v[32:35]\n"
    "s_waitcnt lgkmcnt(0)\n"
    "v_exp_f32 v66, v66\n"
    "v_mfma_f32_16x16x32_bf16 v[124:127], a[112:115], v[68:71], v[124:127]\n"
    "buffer_load_dwordx2 v[222:223], v242, s[28:31], s77 offen offset:768 nt\n"
    "v_mfma_f32_16x16x32_bf16 v[124:127], a[116:119], v[72:75], v[124:127]\n"
    "ds_read_b128 a[136:139], v248 offset:3072\n"
    "v_cvt_pk_bf16_f32 v96, v48, v49\n"
    "v_mfma_f32_16x16x32_bf16 v[128:131], a[124:127], v[72:75], v[128:131]\n"
    "buffer_load_dwordx2 v[224:225], v242, s[28:31], s77 offen offset:3072 nt\n"
    "v_mfma_f32_16x16x32_bf16 v[128:131], a[120:123], v[68:71], v[128:131]\n"
    "ds_read_b128 a[140:143], v248 offset:15360\n"
    "v_cvt_pk_bf16_f32 v97, v50, v51\n"
    "v_mfma_f32_16x16x32_bf16 v[132:135], a[128:131], v[68:71], v[132:135]\n"
    "buffer_load_dwordx2 v[226:227], v242, s[28:31], s77 offen offset:3840 nt\n"
    "v_mfma_f32_16x16x32_bf16 v[132:135], a[132:135], v[72:75], v[132:135]\n"
    "ds_read_b128 a[144:147], v248 offset:4096\n"
    "v_cvt_pk_bf16_f32 v98, v64, v65\n"
    "v_mfma_f32_16x16x32_bf16 v[148:151], a[132:135], v[80:83], v[148:151]\n"
    "buffer_load_dwordx2 v[228:229], v242, s[28:31], s78 offen offset:0 nt\n"
    "v_mfma_f32_16x16x32_bf16 v[148:151], a[128:131], v[76:79], v[148:151]\n"
    "ds_read_b128 a[148:151], v248 offset:16384\n"
    "v_mfma_f32_16x16x32_bf16 v[144:147], a[120:123], v[76:79], v[144:147]\n"
    "buffer_load_dwordx2 v[230:231], v242, s[28:31], s78 offen offset:768 nt\n"
    "v_mfma_f32_16x16x32_bf16 v[144:147], a[124:127], v[80:83], v[144:147]\n"
    "ds_read_b128 a[152:155], v248 offset:5120\n"
    "v_mfma_f32_16x16x32_bf16 v[140:143], a[116:119], v[80:83], v[140:143]\n"
    "buffer_load_dwordx2 v[232:233], v242, s[28:31], s78 offen offset:3072 nt\n"
    "v_mfma_f32_16x16x32_bf16 v[140:143], a[112:115], v[76:79], v[140:143]\n"
    "ds_read_b128 a[156:159], v248 offset:17408\n"
    "v_mfma_f32_16x16x32_bf16 v[156:159], a[112:115], v[84:87], v[156:159]\n"
    "buffer_load_dwordx2 v[234:235], v242, s[28:31], s78 offen offset:3840 nt\n"
    "v_mfma_f32_16x16x32_bf16 v[156:159], a[116:119], v[88:91], v[156:159]\n"
    "ds_read_b128 v[104:107], v251 offset:0\n"
    "v_mfma_f32_16x16x32_bf16 v[160:163], a[124:127], v[88:91], v[160:163]\n"
    "v_exp_f32 v67, v67\n"
    "v_mfma_f32_16x16x32_bf16 v[160:163], a[120:123], v[84:87], v[160:163]\n"
    "v_cvt_pk_bf16_f32 v99, v66, v67\n"
    "v_mfma_f32_16x16x32_bf16 v[164:167], a[128:131], v[84:87], v[164:167]\n"
    "v_exp_f32 v4, v4\n"
    "v_mfma_f32_16x16x32_bf16 v[164:167], a[132:135], v[88:91], v[164:167]\n"
    "v_exp_f32 v5, v5\n"
    "v_mfma_f32_16x16x32_bf16 v[180:183], a[132:135], v[96:99], v[180:183]\n"
    "v_exp_f32 v6, v6\n"
    "v_mfma_f32_16x16x32_bf16 v[180:183], a[128:131], v[92:95], v[180:183]\n"
    "v_exp_f32 v7, v7\n"
    "v_mfma_f32_16x16x32_bf16 v[176:179], a[120:123], v[92:95], v[176:179]\n"
    "v_exp_f32 v8, v8\n"
    "v_mfma_f32_16x16x32_bf16 v[176:179], a[124:127], v[96:99], v[176:179]\n"
    "v_exp_f32 v9, v9\n"
    "v_mfma_f32_16x16x32_bf16 v[172:175], a[116:119], v[96:99], v[172:175]\n"
    "v_exp_f32 v10, v10\n"
    "v_mfma_f32_16x16x32_bf16 v[172:175], a[112:115], v[92:95], v[172:175]\n"
    "s_waitcnt lgkmcnt(0)\n"
    "v_exp_f32 v11, v11\n"
    "v_mfma_f32_16x16x32_bf16 v[136:139], a[136:139], v[68:71], v[136:139]\n"
    "ds_read_b128 a[112:115], v248 offset:6144\n"
    "v_mfma_f32_16x16x32_bf16 v[136:139], a[140:143], v[72:75], v[136:139]\n"
    "ds_read_b128 a[116:119], v248 offset:18432\n"
    "v_mfma_f32_16x16x32_bf16 v[188:191], a[148:151], v[72:75], v[188:191]\n"
    "ds_read_b128 a[120:123], v248 offset:7168\n"
    "v_mfma_f32_16x16x32_bf16 v[188:191], a[144:147], v[68:71], v[188:191]\n"
    "ds_read_b128 a[124:127], v248 offset:19456\n"
    "v_mfma_f32_16x16x32_bf16 v[192:195], a[152:155], v[68:71], v[192:195]\n"
    "ds_read_b128 a[128:131], v248 offset:8192\n"
    "v_mfma_f32_16x16x32_bf16 v[192:195], a[156:159], v[72:75], v[192:195]\n"
    "ds_read_b128 a[132:135], v248 offset:20480\n"
    "v_smfmac_f32_16x16x64_bf16 v[108:111], v[104:107], v[68:75], v253\n"
    "s_add_u32 s81, s81, 128\n"
    "s_cmp_gt_u32 s81, 4096\n"
    "s_cselect_b32 s81, 0, s81\n"
    "v_smfmac_f32_16x16x64_bf16 v[112:115], v[104:107], v[76:83], v253\n"
    "v_mfma_f32_16x16x32_bf16 a[4:7], a[156:159], v[80:83], a[4:7]\n"
    "v_exp_f32 v12, v12\n"
    "v_mfma_f32_16x16x32_bf16 a[4:7], a[152:155], v[76:79], a[4:7]\n"
    "v_exp_f32 v13, v13\n"
    "v_mfma_f32_16x16x32_bf16 a[0:3], a[144:147], v[76:79], a[0:3]\n"
    "v_exp_f32 v14, v14\n"
    "v_mfma_f32_16x16x32_bf16 a[0:3], a[148:151], v[80:83], a[0:3]\n"
    "v_exp_f32 v15, v15\n"
    "v_mfma_f32_16x16x32_bf16 v[152:155], a[140:143], v[80:83], v[152:155]\n"
    "v_exp_f32 v16, v16\n"
    "v_mfma_f32_16x16x32_bf16 v[152:155], a[136:139], v[76:79], v[152:155]\n"
    "v_mfma_f32_16x16x32_bf16 v[168:171], a[136:139], v[84:87], v[168:171]\n"
    "v_exp_f32 v17, v17\n"
    "v_mfma_f32_16x16x32_bf16 v[168:171], a[140:143], v[88:91], v[168:171]\n"
    "v_mfma_f32_16x16x32_bf16 a[16:19], a[148:151], v[88:91], a[16:19]\n"
    "v_mfma_f32_16x16x32_bf16 a[16:19], a[144:147], v[84:87], a[16:19]\n"
    "v_exp_f32 v18, v18\n"
    "v_mfma_f32_16x16x32_bf16 a[20:23], a[152:155], v[84:87], a[20:23]\n"
    "v_mfma_f32_16x16x32_bf16 a[20:23], a[156:159], v[88:91], a[20:23]\n"
    "v_smfmac_f32_16x16x64_bf16 v[116:119], v[104:107], v[84:91], v253\n"
    "v_smfmac_f32_16x16x64_bf16 v[120:123], v[104:107], v[92:99], v253\n"
    "v_mfma_f32_16x16x32_bf16 a[36:39], a[156:159], v[96:99], a[36:39]\n"
    "v_exp_f32 v19, v19\n"
    "v_mfma_f32_16x16x32_bf16 a[36:39], a[152:155], v[92:95], a[36:39]\n"
    "v_exp_f32 v20, v20\n"
    "v_mfma_f32_16x16x32_bf16 a[32:35], a[144:147], v[92:95], a[32:35]\n"
    "v_mfma_f32_16x16x32_bf16 a[32:35], a[148:151], v[96:99], a[32:35]\n"
    "v_exp_f32 v21, v21\n"
    "v_mfma_f32_16x16x32_bf16 v[184:187], a[140:143], v[96:99], v[184:187]\n"
    "v_mfma_f32_16x16x32_bf16 v[184:187], a[136:139], v[92:95], v[184:187]\n"
    "s_waitcnt lgkmcnt(0)\n"
    "v_mfma_f32_16x16x32_bf16 v[196:199], a[112:115], v[68:71], v[196:199]\n"
    "ds_read_b128 a[136:139], v248 offset:9216\n"
    "v_mfma_f32_16x16x32_bf16 v[196:199], a[116:119], v[72:75], v[196:199]\n"
    "ds_read_b128 a[140:143], v248 offset:21504\n"
    "v_mfma_f32_16x16x32_bf16 v[200:203], a[124:127], v[72:75], v[200:203]\n"
    "ds_read_b128 a[144:147], v248 offset:10240\n"
    "v_mfma_f32_16x16x32_bf16 v[200:203], a[120:123], v[68:71], v[200:203]\n"
    "ds_read_b128 a[148:151], v248 offset:22528\n"
    "v_mfma_f32_16x16x32_bf16 a[48:51], a[128:131], v[68:71], a[48:51]\n"
    "buffer_load_dwordx2 v[236:237], v241, s[36:39], s81 offen offset:0 sc1\n"
    "v_mfma_f32_16x16x32_bf16 a[48:51], a[132:135], v[72:75], a[48:51]\n"
    "ds_read_b128 a[152:155], v248 offset:11264\n"
    "v_mfma_f32_16x16x32_bf16 a[64:67], a[132:135], v[80:83], a[64:67]\n"
    "ds_read_b128 a[156:159], v248 offset:23552\n"
    "v_mfma_f32_16x16x32_bf16 a[64:67], a[128:131], v[76:79], a[64:67]\n"
    "v_exp_f32 v22, v22\n"
    "v_mfma_f32_16x16x32_bf16 a[12:15], a[120:123], v[76:79], a[12:15]\n"
    "v_exp_f32 v23, v23\n"
    "v_mfma_f32_16x16x32_bf16 a[12:15], a[124:127], v[80:83], a[12:15]\n"
    "v_exp_f32 v24, v24\n"
    "v_mfma_f32_16x16x32_bf16 a[8:11], a[116:119], v[80:83], a[8:11]\n"
    "v_exp_f32 v25, v25\n"
    "v_mfma_f32_16x16x32_bf16 a[8:11], a[112:115], v[76:79], a[8:11]\n"
    "v_exp_f32 v26, v26\n"
    "v_mfma_f32_16x16x32_bf16 a[24:27], a[112:115], v[84:87], a[24:27]\n"
    "v_mfma_f32_16x16x32_bf16 a[24:27], a[116:119], v[88:91], a[24:27]\n"
    "v_exp_f32 v27, v27\n"
    "v_mfma_f32_16x16x32_bf16 a[28:31], a[124:127], v[88:91], a[28:31]\n"
    "v_mfma_f32_16x16x32_bf16 a[28:31], a[120:123], v[84:87], a[28:31]\n"
    "v_mfma_f32_16x16x32_bf16 a[80:83], a[128:131], v[84:87], a[80:83]\n"
    "v_exp_f32 v28, v28\n"
    "v_mfma_f32_16x16x32_bf16 a[80:83], a[132:135], v[88:91], a[80:83]\n"
    "v_mfma_f32_16x16x32_bf16 a[96:99], a[132:135], v[96:99], a[96:99]\n"
    "v_mfma_f32_16x16x32_bf16 a[96:99], a[128:131], v[92:95], a[96:99]\n"
    "v_exp_f32 v29, v29\n"
    "v_mfma_f32_16x16x32_bf16 a[44:47], a[120:123], v[92:95], a[44:47]\n"
    "v_mfma_f32_16x16x32_bf16 a[44:47], a[124:127], v[96:99], a[44:47]\n"
    "v_exp_f32 v30, v30\n"
    "v_mfma_f32_16x16x32_bf16 a[40:43], a[116:119], v[96:99], a[40:43]\n"
    "v_mfma_f32_16x16x32_bf16 a[40:43], a[112:115], v[92:95], a[40:43]\n"
    "s_waitcnt lgkmcnt(0)\n"
    "v_mfma_f32_16x16x32_bf16 a[52:55], a[136:139], v[68:71], a[52:55]\n"
    "ds_read_b128 a[112:115], v3 offset:12288\n"
    "v_mfma_f32_16x16x32_bf16 a[52:55], a[140:143], v[72:75], a[52:55]\n"
    "ds_read_b128 a[116:119], v3 offset:14336\n"
    "v_mfma_f32_16x16x32_bf16 a[56:59], a[148:151], v[72:75], a[56:59]\n"
    "ds_read_b128 a[120:123], v3 offset:16384\n"
    "v_mfma_f32_16x16x32_bf16 a[56:59], a[144:147], v[68:71], a[56:59]\n"
    "ds_read_b128 a[124:127], v3 offset:18432\n"
    "v_mfma_f32_16x16x32_bf16 a[60:63], a[152:155], v[68:71], a[60:63]\n"
    "ds_read_b128 a[128:131], v3 offset:20480\n"
    "v_mfma_f32_16x16x32_bf16 a[60:63], a[156:159], v[72:75], a[60:63]\n"
    "ds_read_b128 a[132:135], v3 offset:22528\n"
    "v_mfma_f32_16x16x32_bf16 a[76:79], a[156:159], v[80:83], a[76:79]\n"
    "ds_read_b128 v[100:103], v250 offset:896\n"
    "v_mfma_f32_16x16x32_bf16 a[76:79], a[152:155], v[76:79], a[76:79]\n"
    "v_exp_f32 v31, v31\n"
    "v_mfma_f32_16x16x32_bf16 a[72:75], a[144:147], v[76:79], a[72:75]\n"
    "v_exp_f32 v32, v32\n"
    "v_mfma_f32_16x16x32_bf16 a[72:75], a[148:151], v[80:83], a[72:75]\n"
    "v_exp_f32 v33, v33\n"
    "v_mfma_f32_16x16x32_bf16 a[68:71], a[140:143], v[80:83], a[68:71]\n"
    "v_exp_f32 v34, v34\n"
    "v_mfma_f32_16x16x32_bf16 a[68:71], a[136:139], v[76:79], a[68:71]\n"
    "v_exp_f32 v35, v35\n"
    "v_mfma_f32_16x16x32_bf16 a[84:87], a[136:139], v[84:87], a[84:87]\n"
    "v_mfma_f32_16x16x32_bf16 a[84:87], a[140:143], v[88:91], a[84:87]\n"
    "v_mfma_f32_16x16x32_bf16 a[88:91], a[148:151], v[88:91], a[88:91]\n"
    "s_waitcnt vmcnt(18)\n"
    "ds_add_u32 v247, v246\n"
    "v_mfma_f32_16x16x32_bf16 a[88:91], a[144:147], v[84:87], a[88:91]\n"
    "v_mfma_f32_16x16x32_bf16 a[92:95], a[152:155], v[84:87], a[92:95]\n"
    "v_mfma_f32_16x16x32_bf16 a[92:95], a[156:159], v[88:91], a[92:95]\n"
    "v_mfma_f32_16x16x32_bf16 a[108:111], a[156:159], v[96:99], a[108:111]\n"
    "v_mfma_f32_16x16x32_bf16 a[108:111], a[152:155], v[92:95], a[108:111]\n"
    "v_mfma_f32_16x16x32_bf16 a[104:107], a[144:147], v[92:95], a[104:107]\n"
    "s_sub_u32 s74, s68, s65\n"
    "v_mfma_f32_16x16x32_bf16 a[104:107], a[148:151], v[96:99], a[104:107]\n"
    "s_add_u32 s66, s66, 1\n"
    "v_mov_b32 v2, v3\n"
    "v_add_u32_e64 v3, s74, v252\n"
    "v_mfma_f32_16x16x32_bf16 a[100:103], a[140:143], v[96:99], a[100:103]\n"
    "s_waitcnt lgkmcnt(1)\n"
    "s_cmp_eq_u32 s66, .Lend-.Lend2+(29)\n"
    "v_mfma_f32_16x16x32_bf16 a[100:103], a[136:139], v[92:95], a[100:103]\n"
    ".LwKe:\n"
    "s_cbranch_scc1 .LtailP\n"
    ".LNK:\n"
    "v_mfma_f32_16x16x32_bf16 v[36:39], a[112:115], a[192:195], v[100:103]\n"
    "ds_read_b128 a[136:139], v2 offset:13312\n"
    "v_cvt_pk_bf16_f32 v68, v4, v5\n"
    "v_mfma_f32_16x16x32_bf16 v[36:39], a[116:119], a[196:199], v[36:39]\n"
    "ds_read_b128 a[140:143], v2 offset:15360\n"
    "v_mfma_f32_16x16x32_bf16 v[36:39], a[120:123], a[200:203], v[36:39]\n"
    "ds_read_b128 a[144:147], v2 offset:17408\n"
    "v_mfma_f32_16x16x32_bf16 v[36:39], a[124:127], a[204:207], v[36:39]\n"
    "ds_read_b128 a[148:151], v2 offset:19456\n"
    "v_cvt_pk_bf16_f32 v69, v6, v7\n"
    "v_mfma_f32_16x16x32_bf16 v[36:39], a[128:131], a[208:211], v[36:39]\n"
    "ds_read_b128 a[152:155], v2 offset:21504\n"
    "v_mfma_f32_16x16x32_bf16 v[36:39], a[132:135], a[212:215], v[36:39]\n"
    "ds_read_b128 a[156:159], v2 offset:23552\n"
    "v_mfma_f32_16x16x32_bf16 v[40:43], a[132:135], a[236:239], v[100:103]\n"
    "ds_read_b128 v[104:107], v250 offset:912\n"
    "v_mfma_f32_16x16x32_bf16 v[40:43], a[128:131], a[232:235], v[40:43]\n"
    "s_add_u32 s68, s68, 24576\n"
    "v_cvt_pk_bf16_f32 v76, v8, v9\n"
    "v_mfma_f32_16x16x32_bf16 v[40:43], a[124:127], a[228:231], v[40:43]\n"
    "s_cmp_ge_u32 s68, s69\n"
    "s_cselect_b32 s68, s65, s68\n"
    "v_mfma_f32_16x16x32_bf16 v[40:43], a[120:123], a[224:227], v[40:43]\n"
    "v_exp_f32 v36, v36\n"
    "v_mfma_f32_16x16x32_bf16 v[40:43], a[116:119], a[220:223], v[40:43]\n"
    "v_exp_f32 v37, v37\n"
    "v_mfma_f32_16x16x32_bf16 v[40:43], a[112:115], a[216:219], v[40:43]\n"
    "v_cvt_pk_bf16_f32 v77, v10, v11\n"
    "v_cvt_pk_bf16_f32 v72, v36, v37\n"
    "v_mfma_f32_16x16x32_bf16 v[44:47], a[112:115], a[240:243], v[100:103]\n"
    "v_mfma_f32_16x16x32_bf16 v[44:47], a[116:119], a[244:247], v[44:47]\n"
    "v_exp_f32 v38, v38\n"
    "v_mfma_f32_16x16x32_bf16 v[44:47], a[120:123], a[248:251], v[44:47]\n"
    "v_mfma_f32_16x16x32_bf16 v[44:47], a[124:127], a[252:255], v[44:47]\n"
    "v_cvt_pk_bf16_f32 v84, v12, v13\n"
    "v_mfma_f32_16x16x32_bf16 v[44:47], a[128:131], a[160:163], v[44:47]\n"
    "v_exp_f32 v39, v39\n"
    "v_mfma_f32_16x16x32_bf16 v[44:47], a[132:135], a[164:167], v[44:47]\n"
    "v_cvt_pk_bf16_f32 v73, v38, v39\n"
    "v_mfma_f32_16x16x32_bf16 v[48:51], a[132:135], a[188:191], v[100:103]\n"
    "ds_read_b32 v245, v247\n"
    "v_cvt_pk_bf16_f32 v85, v14, v15\n"
    "v_mfma_f32_16x16x32_bf16 v[48:51], a[128:131], a[184:187], v[48:51]\n"
    "s_add_u32 m0, s68, 0\n"
    "v_exp_f32 v40, v40\n"
    "v_mfma_f32_16x16x32_bf16 v[48:51], a[124:127], a[180:183], v[48:51]\n"
    "v_mfma_f32_16x16x32_bf16 v[48:51], a[120:123], a[176:179], v[48:51]\n"
    "v_mfma_f32_16x16x32_bf16 v[48:51], a[116:119], a[172:175], v[48:51]\n"
    "s_waitcnt vmcnt(0)\n"
    "v_cmp_eq_u64 s[54:55], v[236:237], v[238:239]\n"
    "v_cvt_pk_bf16_f32 v92, v16, v17\n"
    "v_mfma_f32_16x16x32_bf16 v[48:51], a[112:115], a[168:171], v[48:51]\n"
    "s_waitcnt lgkmcnt(1)\n"
    "v_exp_f32 v41, v41\n"
    "v_mfma_f32_16x16x32_bf16 v[52:55], a[136:139], a[192:195], v[104:107]\n"
    "s_waitcnt lgkmcnt(0)\n"
    "v_cmp_le_u32 s[56:57], s59, v245\n"
    "v_cvt_pk_bf16_f32 v80, v40, v41\n"
    "v_mfma_f32_16x16x32_bf16 v[52:55], a[140:143], a[196:199], v[52:55]\n"
    "s_waitcnt vmcnt(1)\n"
    "v_exp_f32 v42, v42\n"
    "v_mfma_f32_16x16x32_bf16 v[52:55], a[144:147], a[200:203], v[52:55]\n"
    "s_and_b64 s[54:55], s[54:55], s[56:57]\n"
    "s_cmp_eq_u64 s[54:55], -1\n"
    "v_mfma_f32_16x16x32_bf16 v[52:55], a[148:151], a[204:207], v[52:55]\n"
    "s_cbranch_scc0 .Lpoll2\n"
    ".Lpoll2r:\n"
    "v_mfma_f32_16x16x32_bf16 v[52:55], a[152:155], a[208:211], v[52:55]\n"
    "buffer_load_dwordx4 v1, s[16:19], s67 offen offset:0 sc1 lds\n"
    "v_mfma_f32_16x16x32_bf16 v[52:55], a[156:159], a[212:215], v[52:55]\n"
    "s_add_u32 s76, s71, s60\n"
    "ds_read_b128 a[112:115], v3 offset:0\n"
    "v_cvt_pk_bf16_f32 v93, v18, v19\n"
    "v_mfma_f32_16x16x32_bf16 v[56:59], a[156:159], a[236:239], v[104:107]\n"
    "s_add_u32 s77, s71, s61\n"
    "ds_read_b128 a[116:119], v3 offset:2048\n"
    "v_cvt_pk_bf16_f32 v70, v20, v21\n"
    "v_mfma_f32_16x16x32_bf16 v[56:59], a[152:155], a[232:235], v[56:59]\n"
    "ds_read_b128 a[120:123], v3 offset:4096\n"
    "v_mfma_f32_16x16x32_bf16 v[56:59], a[148:151], a[228:231], v[56:59]\n"
    "buffer_load_dwordx4 v1, s[16:19], s67 offen offset:1024 sc1 lds\n"
    "v_mfma_f32_16x16x32_bf16 v[56:59], a[144:147], a[224:227], v[56:59]\n"
    "ds_read_b128 a[124:127], v3 offset:6144\n"
    "v_cvt_pk_bf16_f32 v71, v22, v23\n"
    "v_mfma_f32_16x16x32_bf16 v[56:59], a[140:143], a[220:223], v[56:59]\n"
    "ds_read_b128 a[128:131], v3 offset:8192\n"
    "v_mfma_f32_16x16x32_bf16 v[56:59], a[136:139], a[216:219], v[56:59]\n"
    "ds_read_b128 a[132:135], v3 offset:10240\n"
    "v_mfma_f32_16x16x32_bf16 v[60:63], a[136:139], a[240:243], v[104:107]\n"
    "buffer_load_dwordx4 v1, s[16:19], s67 offen offset:2048 sc1 lds\n"
    "v_mfma_f32_16x16x32_bf16 v[60:63], a[140:143], a[244:247], v[60:63]\n"
    "ds_read_b128 v[100:103], v250 offset:1536\n"
    "v_cvt_pk_bf16_f32 v78, v24, v25\n"
    "v_mfma_f32_16x16x32_bf16 v[60:63], a[144:147], a[248:251], v[60:63]\n"
    "v_exp_f32 v43, v43\n"
    "v_mfma_f32_16x16x32_bf16 v[60:63], a[148:151], a[252:255], v[60:63]\n"
    "ds_read_b128 v[36:39], v243 offset:0\n"
    "v_cvt_pk_bf16_f32 v81, v42, v43\n"
    "v_mfma_f32_16x16x32_bf16 v[60:63], a[152:155], a[160:163], v[60:63]\n"
    "buffer_load_dwordx4 v1, s[16:19], s67 offen offset:3072 sc1 lds\n"
    "v_mfma_f32_16x16x32_bf16 v[60:63], a[156:159], a[164:167], v[60:63]\n"
    "s_add_u32 m0, s68, 4096\n"
    "v_cvt_pk_bf16_f32 v79, v26, v27\n"
    "v_add_u32 v243, 512, v243\n"
    "v_mfma_f32_16x16x32_bf16 v[64:67], a[156:159], a[188:191], v[104:107]\n"
    "s_add_u32 s83, s67, 4096\n"
    "v_exp_f32 v44, v44\n"
    "v_mfma_f32_16x16x32_bf16 v[64:67], a[152:155], a[184:187], v[64:67]\n"
    "v_exp_f32 v45, v45\n"
    "v_mfma_f32_16x16x32_bf16 v[64:67], a[148:151], a[180:183], v[64:67]\n"
    "buffer_load_dwordx4 v1, s[16:19], s83 offen offset:0 sc1 lds\n"
    "v_mfma_f32_16x16x32_bf16 v[64:67], a[144:147], a[176:179], v[64:67]\n"
    "v_cvt_pk_bf16_f32 v86, v28, v29\n"
    "v_cvt_pk_bf16_f32 v88, v44, v45\n"
    "v_mfma_f32_16x16x32_bf16 v[64:67], a[140:143], a[172:175], v[64:67]\n"
    "v_exp_f32 v46, v46\n"
    "v_mfma_f32_16x16x32_bf16 v[64:67], a[136:139], a[168:171], v[64:67]\n"
    "s_waitcnt lgkmcnt(1)\n"
    "v_exp_f32 v47, v47\n"
    "v_mfma_f32_16x16x32_bf16 v[4:7], a[112:115], a[192:195], v[100:103]\n"
    "buffer_load_dwordx4 v1, s[16:19], s83 offen offset:1024 sc1 lds\n"
    "v_mfma_f32_16x16x32_bf16 v[4:7], a[116:119], a[196:199], v[4:7]\n"
    "s_add_u32 m0, s70, .Lend-.Lend2+(0)\n"
    "ds_read_b128 a[136:139], v3 offset:1024\n"
    "v_cvt_pk_bf16_f32 v87, v30, v31\n"
    "v_mfma_f32_16x16x32_bf16 v[4:7], a[120:123], a[200:203], v[4:7]\n"
    "s_add_u32 s83, s67, 24576\n"
    "ds_read_b128 a[140:143], v3 offset:3072\n"
    "v_cvt_pk_bf16_f32 v89, v46, v47\n"
    "v_mfma_f32_16x16x32_bf16 v[4:7], a[124:127], a[204:207], v[4:7]\n"
    "s_waitcnt lgkmcnt(2)\n"
    "ds_read_b128 a[144:147], v3 offset:5120\n"
    "v_fma_f32 v204, v204, v36, -v38\n"
    "v_mfma_f32_16x16x32_bf16 v[4:7], a[128:131], a[208:211], v[4:7]\n"
    "buffer_load_dwordx4 v1, s[16:19], s83 offen offset:0 sc1 lds\n"
    "v_mfma_f32_16x16x32_bf16 v[4:7], a[132:135], a[212:215], v[4:7]\n"
    "ds_read_b128 a[148:151], v3 offset:7168\n"
    "v_cvt_pk_bf16_f32 v94, v32, v33\n"
    "v_mfma_f32_16x16x32_bf16 v[8:11], a[132:135], a[236:239], v[100:103]\n"
    "ds_read_b128 a[152:155], v3 offset:9216\n"
    "v_fma_f32 v205, v205, v37, -v39\n"
    "v_mfma_f32_16x16x32_bf16 v[8:11], a[128:131], a[232:235], v[8:11]\n"
    "ds_read_b128 a[156:159], v3 offset:11264\n"
    "v_fma_f32 v206, v206, v36, -v38\n"
    "v_mfma_f32_16x16x32_bf16 v[8:11], a[124:127], a[228:231], v[8:11]\n"
    "buffer_load_dwordx4 v1, s[16:19], s83 offen offset:1024 sc1 lds\n"
    "v_mfma_f32_16x16x32_bf16 v[8:11], a[120:123], a[224:227], v[8:11]\n"
    "ds_read_b128 v[104:107], v250 offset:1552\n"
    "v_cvt_pk_bf16_f32 v95, v34, v35\n"
    "v_mfma_f32_16x16x32_bf16 v[8:11], a[116:119], a[220:223], v[8:11]\n"
    "v_exp_f32 v52, v52\n"
    "v_mfma_f32_16x16x32_bf16 v[8:11], a[112:115], a[216:219], v[8:11]\n"
    "v_exp_f32 v53, v53\n"
    "v_mfma_f32_16x16x32_bf16 v[12:15], a[112:115], a[240:243], v[100:103]\n"
    "buffer_load_dwordx4 v1, s[16:19], s83 offen offset:2048 sc1 lds\n"
    "v_mfma_f32_16x16x32_bf16 v[12:15], a[116:119], a[244:247], v[12:15]\n"
    "v_exp_f32 v54, v54\n"
    "v_mfma_f32_16x16x32_bf16 v[12:15], a[120:123], a[248:251], v[12:15]\n"
    "v_exp_f32 v55, v55\n"
    "v_mfma_f32_16x16x32_bf16 v[12:15], a[124:127], a[252:255], v[12:15]\n"
    "v_cvt_pk_bf16_f32 v74, v52, v53\n"
    "v_cvt_pk_bf16_f32 v75, v54, v55\n"
    "v_mfma_f32_16x16x32_bf16 v[12:15], a[128:131], a[160:163], v[12:15]\n"
    "buffer_load_dwordx4 v1, s[16:19], s83 offen offset:3072 sc1 lds\n"
    "v_mfma_f32_16x16x32_bf16 v[12:15], a[132:135], a[164:167], v[12:15]\n"
    "s_add_u32 m0, s70, 4096\n"
    "v_exp_f32 v56, v56\n"
    "v_mfma_f32_16x16x32_bf16 v[16:19], a[132:135], a[188:191], v[100:103]\n"
    "s_add_u32 s83, s67, 28672\n"
    "v_exp_f32 v48, v48\n"
    "v_mfma_f32_16x16x32_bf16 v[16:19], a[128:131], a[184:187], v[16:19]\n"
    "v_exp_f32 v49, v49\n"
    "v_mfma_f32_16x16x32_bf16 v[16:19], a[124:127], a[180:183], v[16:19]\n"
    "buffer_load_dwordx4 v1, s[16:19], s83 offen offset:0 sc1 lds\n"
    "v_mfma_f32_16x16x32_bf16 v[16:19], a[120:123], a[176:179], v[16:19]\n"
    "v_exp_f32 v57, v57\n"
    "v_mfma_f32_16x16x32_bf16 v[16:19], a[116:119], a[172:175], v[16:19]\n"
    "v_cvt_pk_bf16_f32 v82, v56, v57\n"
    "v_cvt_pk_bf16_f32 v96, v48, v49\n"
    "v_mfma_f32_16x16x32_bf16 v[16:19], a[112:115], a[168:171], v[16:19]\n"
    "s_waitcnt lgkmcnt(0)\n"
    "v_exp_f32 v58, v58\n"
    "v_mfma_f32_16x16x32_bf16 v[20:23], a[136:139], a[192:195], v[104:107]\n"
    "buffer_load_dwordx4 v1, s[16:19], s83 offen offset:1024 sc1 lds\n"
    "v_mfma_f32_16x16x32_bf16 v[20:23], a[140:143], a[196:199], v[20:23]\n"
    "s_add_u32 s67, s67, 49152\n"
    "ds_read_b128 a[112:115], v249 offset:0\n"
    "v_fma_f32 v207, v207, v37, -v39\n"
    "v_mfma_f32_16x16x32_bf16 v[20:23], a[144:147], a[200:203], v[20:23]\n"
    "s_add_u32 s59, s59, 4\n"
    "ds_read_b128 a[116:119], v249 offset:12288\n"
    "v_cvt_pk_bf16_f32 v204, v204, v205\n"
    "v_mfma_f32_16x16x32_bf16 v[20:23], a[148:151], a[204:207], v[20:23]\n"
    "ds_read_b128 a[120:123], v249 offset:1024\n"
    "v_cvt_pk_bf16_f32 v206, v206, v207\n"
    "v_mfma_f32_16x16x32_bf16 v[20:23], a[152:155], a[208:211], v[20:23]\n"
    "ds_read_b128 a[124:127], v249 offset:13312\n"
    "v_fma_f32 v208, v208, v36, -v38\n"
    "v_mfma_f32_16x16x32_bf16 v[20:23], a[156:159], a[212:215], v[20:23]\n"
    "ds_read_b128 a[128:131], v249 offset:2048\n"
    "v_fma_f32 v209, v209, v37, -v39\n"
    "v_mfma_f32_16x16x32_bf16 v[24:27], a[156:159], a[236:239], v[104:107]\n"
    "ds_read_b128 a[132:135], v249 offset:14336\n"
    "v_fma_f32 v210, v210, v36, -v38\n"
    "v_mfma_f32_16x16x32_bf16 v[24:27], a[152:155], a[232:235], v[24:27]\n"
    "v_exp_f32 v59, v59\n"
    "v_mfma_f32_16x16x32_bf16 v[24:27], a[148:151], a[228:231], v[24:27]\n"
    "v_cvt_pk_bf16_f32 v83, v58, v59\n"
    "v_fma_f32 v211, v211, v37, -v39\n"
    "v_mfma_f32_16x16x32_bf16 v[24:27], a[144:147], a[224:227], v[24:27]\n"
    "v_exp_f32 v60, v60\n"
    "v_mfma_f32_16x16x32_bf16 v[24:27], a[140:143], a[220:223], v[24:27]\n"
    "v_exp_f32 v61, v61\n"
    "v_mfma_f32_16x16x32_bf16 v[24:27], a[136:139], a[216:219], v[24:27]\n"
    "v_exp_f32 v62, v62\n"
    "v_mfma_f32_16x16x32_bf16 v[28:31], a[136:139], a[240:243], v[104:107]\n"
    "v_cvt_pk_bf16_f32 v90, v60, v61\n"
    "v_cvt_pk_bf16_f32 v208, v208, v209\n"
    "v_mfma_f32_16x16x32_bf16 v[28:31], a[140:143], a[244:247], v[28:31]\n"
    "v_exp_f32 v63, v63\n"
    "v_mfma_f32_16x16x32_bf16 v[28:31], a[144:147], a[248:251], v[28:31]\n"
    "v_cvt_pk_bf16_f32 v91, v62, v63\n"
    "v_cvt_pk_bf16_f32 v210, v210, v211\n"
    "v_mfma_f32_16x16x32_bf16 v[28:31], a[148:151], a[252:255], v[28:31]\n"
    "v_exp_f32 v50, v50\n"
    "v_mfma_f32_16x16x32_bf16 v[28:31], a[152:155], a[160:163], v[28:31]\n"
    "v_exp_f32 v51, v51\n"
    "v_mfma_f32_16x16x32_bf16 v[28:31], a[156:159], a[164:167], v[28:31]\n"
    "v_exp_f32 v64, v64\n"
    "v_mfma_f32_16x16x32_bf16 v[32:35], a[156:159], a[188:191], v[104:107]\n"
    "v_cvt_pk_bf16_f32 v97, v50, v51\n"
    "v_fma_f32 v212, v212, v36, -v38\n"
    "v_mfma_f32_16x16x32_bf16 v[32:35], a[152:155], a[184:187], v[32:35]\n"
    "v_exp_f32 v65, v65\n"
    "v_mfma_f32_16x16x32_bf16 v[32:35], a[148:151], a[180:183], v[32:35]\n"
    "v_cvt_pk_bf16_f32 v98, v64, v65\n"
    "v_fma_f32 v213, v213, v37, -v39\n"
    "v_mfma_f32_16x16x32_bf16 v[32:35], a[144:147], a[176:179], v[32:35]\n"
    "buffer_store_dword v204, v244, s[16:19], s76 offen offset:0 sc1\n"
    "v_mfma_f32_16x16x32_bf16 v[32:35], a[140:143], a[172:175], v[32:35]\n"
    "v_exp_f32 v66, v66\n"
    "v_mfma_f32_16x16x32_bf16 v[32:35], a[136:139], a[168:171], v[32:35]\n"
    "s_waitcnt lgkmcnt(0)\n"
    "v_fma_f32 v214, v214, v36, -v38\n"
    "v_fma_f32 v215, v215, v37, -v39\n"
    "v_mfma_f32_16x16x32_bf16 v[124:127], a[112:115], v[68:71], v[124:127]\n"
    "ds_read_b128 a[136:139], v249 offset:3072\n"
    "v_cvt_pk_bf16_f32 v212, v212, v213\n"
    "v_mfma_f32_16x16x32_bf16 v[124:127], a[116:119], v[72:75], v[124:127]\n"
    "ds_read_b128 a[140:143], v249 offset:15360\n"
    "v_cvt_pk_bf16_f32 v214, v214, v215\n"
    "v_mfma_f32_16x16x32_bf16 v[128:131], a[124:127], v[72:75], v[128:131]\n"
    "ds_read_b128 a[144:147], v249 offset:4096\n"
    "v_fma_f32 v216, v216, v36, -v38\n"
    "v_mfma_f32_16x16x32_bf16 v[128:131], a[120:123], v[68:71], v[128:131]\n"
    "ds_read_b128 a[148:151], v249 offset:16384\n"
    "v_fma_f32 v217, v217, v37, -v39\n"
    "v_mfma_f32_16x16x32_bf16 v[132:135], a[128:131], v[68:71], v[132:135]\n"
    "ds_read_b128 a[152:155], v249 offset:5120\n"
    "v_fma_f32 v218, v218, v36, -v38\n"
    "v_mfma_f32_16x16x32_bf16 v[132:135], a[132:135], v[72:75], v[132:135]\n"
    "ds_read_b128 a[156:159], v249 offset:17408\n"
    "v_fma_f32 v219, v219, v37, -v39\n"
    "v_mfma_f32_16x16x32_bf16 v[148:151], a[132:135], v[80:83], v[148:151]\n"
    "ds_read_b128 v[104:107], v251 offset:768\n"
    "v_cvt_pk_bf16_f32 v216, v216, v217\n"
    "v_mfma_f32_16x16x32_bf16 v[148:151], a[128:131], v[76:79], v[148:151]\n"
    "v_exp_f32 v67, v67\n"
    "v_mfma_f32_16x16x32_bf16 v[144:147], a[120:123], v[76:79], v[144:147]\n"
    "v_cvt_pk_bf16_f32 v99, v66, v67\n"
    "v_cvt_pk_bf16_f32 v218, v218, v219\n"
    "v_mfma_f32_16x16x32_bf16 v[144:147], a[124:127], v[80:83], v[144:147]\n"
    "buffer_store_dword v206, v244, s[16:19], s76 offen offset:128 sc1\n"
    "v_mfma_f32_16x16x32_bf16 v[140:143], a[116:119], v[80:83], v[140:143]\n"
    "v_exp_f32 v4, v4\n"
    "v_mfma_f32_16x16x32_bf16 v[140:143], a[112:115], v[76:79], v[140:143]\n"
    "buffer_store_dword v208, v244, s[16:19], s76 offen offset:1024 sc1\n"
    "v_mfma_f32_16x16x32_bf16 v[156:159], a[112:115], v[84:87], v[156:159]\n"
    "v_fma_f32 v220, v220, v36, -v38\n"
    "v_fma_f32 v221, v221, v37, -v39\n"
    "v_mfma_f32_16x16x32_bf16 v[156:159], a[116:119], v[88:91], v[156:159]\n"
    "buffer_store_dword v210, v244, s[16:19], s76 offen offset:1152 sc1\n"
    "v_mfma_f32_16x16x32_bf16 v[160:163], a[124:127], v[88:91], v[160:163]\n"
    "v_exp_f32 v5, v5\n"
    "v_mfma_f32_16x16x32_bf16 v[160:163], a[120:123], v[84:87], v[160:163]\n"
    "buffer_store_dword v212, v244, s[16:19], s76 offen offset:256 sc1\n"
    "v_mfma_f32_16x16x32_bf16 v[164:167], a[128:131], v[84:87], v[164:167]\n"
    "v_exp_f32 v6, v6\n"
    "v_mfma_f32_16x16x32_bf16 v[164:167], a[132:135], v[88:91], v[164:167]\n"
    "buffer_store_dword v214, v244, s[16:19], s76 offen offset:384 sc1\n"
    "v_mfma_f32_16x16x32_bf16 v[180:183], a[132:135], v[96:99], v[180:183]\n"
    "v_fma_f32 v222, v222, v36, -v38\n"
    "v_fma_f32 v223, v223, v37, -v39\n"
    "v_mfma_f32_16x16x32_bf16 v[180:183], a[128:131], v[92:95], v[180:183]\n"
    "v_exp_f32 v7, v7\n"
    "v_mfma_f32_16x16x32_bf16 v[176:179], a[120:123], v[92:95], v[176:179]\n"
    "v_exp_f32 v8, v8\n"
    "v_mfma_f32_16x16x32_bf16 v[176:179], a[124:127], v[96:99], v[176:179]\n"
    "v_cvt_pk_bf16_f32 v220, v220, v221\n"
    "v_cvt_pk_bf16_f32 v222, v222, v223\n"
    "v_mfma_f32_16x16x32_bf16 v[172:175], a[116:119], v[96:99], v[172:175]\n"
    "v_exp_f32 v9, v9\n"
    "v_mfma_f32_16x16x32_bf16 v[172:175], a[112:115], v[92:95], v[172:175]\n"
    "s_waitcnt lgkmcnt(0)\n"
    "v_exp_f32 v10, v10\n"
    "v_mfma_f32_16x16x32_bf16 v[136:139], a[136:139], v[68:71], v[136:139]\n"
    "ds_read_b128 a[112:115], v249 offset:6144\n"
    "v_fma_f32 v224, v224, v36, -v38\n"
    "v_mfma_f32_16x16x32_bf16 v[136:139], a[140:143], v[72:75], v[136:139]\n"
    "ds_read_b128 a[116:119], v249 offset:18432\n"
    "v_fma_f32 v225, v225, v37, -v39\n"
    "v_mfma_f32_16x16x32_bf16 v[188:191], a[148:151], v[72:75], v[188:191]\n"
    "ds_read_b128 a[120:123], v249 offset:7168\n"
    "v_fma_f32 v226, v226, v36, -v38\n"
    "v_mfma_f32_16x16x32_bf16 v[188:191], a[144:147], v[68:71], v[188:191]\n"
    "ds_read_b128 a[124:127], v249 offset:19456\n"
    "v_fma_f32 v227, v227, v37, -v39\n"
    "v_mfma_f32_16x16x32_bf16 v[192:195], a[152:155], v[68:71], v[192:195]\n"
    "ds_read_b128 a[128:131], v249 offset:8192\n"
    "v_cvt_pk_bf16_f32 v224, v224, v225\n"
    "v_mfma_f32_16x16x32_bf16 v[192:195], a[156:159], v[72:75], v[192:195]\n"
    "ds_read_b128 a[132:135], v249 offset:20480\n"
    "v_smfmac_f32_16x16x64_bf16 v[108:111], v[104:107], v[68:75], v253\n"
    "s_add_u32 s81, s81, 128\n"
    "s_cmp_gt_u32 s81, 4096\n"
    "s_cselect_b32 s81, 0, s81\n"
    "v_smfmac_f32_16x16x64_bf16 v[112:115], v[104:107], v[76:83], v253\n"
    "v_mfma_f32_16x16x32_bf16 a[4:7], a[156:159], v[80:83], a[4:7]\n"
    "v_exp_f32 v11, v11\n"
    "v_mfma_f32_16x16x32_bf16 a[4:7], a[152:155], v[76:79], a[4:7]\n"
    "v_exp_f32 v12, v12\n"
    "v_mfma_f32_16x16x32_bf16 a[0:3], a[144:147], v[76:79], a[0:3]\n"
    "v_exp_f32 v13, v13\n"
    "v_mfma_f32_16x16x32_bf16 a[0:3], a[148:151], v[80:83], a[0:3]\n"
    "v_exp_f32 v14, v14\n"
    "v_mfma_f32_16x16x32_bf16 v[152:155], a[140:143], v[80:83], v[152:155]\n"
    "v_exp_f32 v15, v15\n"
    "v_mfma_f32_16x16x32_bf16 v[152:155], a[136:139], v[76:79], v[152:155]\n"
    "v_cvt_pk_bf16_f32 v226, v226, v227\n"
    "v_fma_f32 v228, v228, v36, -v38\n"
    "v_mfma_f32_16x16x32_bf16 v[168:171], a[136:139], v[84:87], v[168:171]\n"
    "v_exp_f32 v16, v16\n"
    "v_mfma_f32_16x16x32_bf16 v[168:171], a[140:143], v[88:91], v[168:171]\n"
    "v_exp_f32 v17, v17\n"
    "v_mfma_f32_16x16x32_bf16 a[16:19], a[148:151], v[88:91], a[16:19]\n"
    "v_fma_f32 v229, v229, v37, -v39\n"
    "v_fma_f32 v230, v230, v36, -v38\n"
    "v_mfma_f32_16x16x32_bf16 a[16:19], a[144:147], v[84:87], a[16:19]\n"
    "v_exp_f32 v18, v18\n"
    "v_mfma_f32_16x16x32_bf16 a[20:23], a[152:155], v[84:87], a[20:23]\n"
    "v_fma_f32 v231, v231, v37, -v39\n"
    "v_mfma_f32_16x16x32_bf16 a[20:23], a[156:159], v[88:91], a[20:23]\n"
    "v_smfmac_f32_16x16x64_bf16 v[116:119], v[104:107], v[84:91], v253\n"
    "v_smfmac_f32_16x16x64_bf16 v[120:123], v[104:107], v[92:99], v253\n"
    "v_mfma_f32_16x16x32_bf16 a[36:39], a[156:159], v[96:99], a[36:39]\n"
    "v_exp_f32 v19, v19\n"
    "v_mfma_f32_16x16x32_bf16 a[36:39], a[152:155], v[92:95], a[36:39]\n"
    "v_exp_f32 v20, v20\n"
    "v_mfma_f32_16x16x32_bf16 a[32:35], a[144:147], v[92:95], a[32:35]\n"
    "v_cvt_pk_bf16_f32 v228, v228, v229\n"
    "v_cvt_pk_bf16_f32 v230, v230, v231\n"
    "v_mfma_f32_16x16x32_bf16 a[32:35], a[148:151], v[96:99], a[32:35]\n"
    "v_exp_f32 v21, v21\n"
    "v_mfma_f32_16x16x32_bf16 v[184:187], a[140:143], v[96:99], v[184:187]\n"
    "v_fma_f32 v232, v232, v36, -v38\n"
    "v_mfma_f32_16x16x32_bf16 v[184:187], a[136:139], v[92:95], v[184:187]\n"
    "s_waitcnt lgkmcnt(0)\n"
    "v_mfma_f32_16x16x32_bf16 v[196:199], a[112:115], v[68:71], v[196:199]\n"
    "ds_read_b128 a[136:139], v249 offset:9216\n"
    "v_fma_f32 v233, v233, v37, -v39\n"
    "v_mfma_f32_16x16x32_bf16 v[196:199], a[116:119], v[72:75], v[196:199]\n"
    "ds_read_b128 a[140:143], v249 offset:21504\n"
    "v_mfma_f32_16x16x32_bf16 v[200:203], a[124:127], v[72:75], v[200:203]\n"
    "ds_read_b128 a[144:147], v249 offset:10240\n"
    "v_mfma_f32_16x16x32_bf16 v[200:203], a[120:123], v[68:71], v[200:203]\n"
    "ds_read_b128 a[148:151], v249 offset:22528\n"
    "v_fma_f32 v234, v234, v36, -v38\n"
    "v_mfma_f32_16x16x32_bf16 a[48:51], a[128:131], v[68:71], a[48:51]\n"
    "buffer_load_dwordx2 v[236:237], v241, s[36:39], s81 offen offset:0 sc1\n"
    "v_mfma_f32_16x16x32_bf16 a[48:51], a[132:135], v[72:75], a[48:51]\n"
    "ds_read_b128 a[152:155], v249 offset:11264\n"
    "v_fma_f32 v235, v235, v37, -v39\n"
    "v_mfma_f32_16x16x32_bf16 a[64:67], a[132:135], v[80:83], a[64:67]\n"
    "ds_read_b128 a[156:159], v249 offset:23552\n"
    "v_mfma_f32_16x16x32_bf16 a[64:67], a[128:131], v[76:79], a[64:67]\n"
    "buffer_store_dword v216, v244, s[16:19], s76 offen offset:1280 sc1\n"
    "v_mfma_f32_16x16x32_bf16 a[12:15], a[120:123], v[76:79], a[12:15]\n"
    "v_exp_f32 v22, v22\n"
    "v_mfma_f32_16x16x32_bf16 a[12:15], a[124:127], v[80:83], a[12:15]\n"
    "buffer_store_dword v218, v244, s[16:19], s76 offen offset:1408 sc1\n"
    "v_mfma_f32_16x16x32_bf16 a[8:11], a[116:119], v[80:83], a[8:11]\n"
    "v_exp_f32 v23, v23\n"
    "v_mfma_f32_16x16x32_bf16 a[8:11], a[112:115], v[76:79], a[8:11]\n"
    "buffer_store_dword v220, v244, s[16:19], s77 offen offset:0 sc1\n"
    "v_mfma_f32_16x16x32_bf16 a[24:27], a[112:115], v[84:87], a[24:27]\n"
    "v_exp_f32 v24, v24\n"
    "v_mfma_f32_16x16x32_bf16 a[24:27], a[116:119], v[88:91], a[24:27]\n"
    "buffer_store_dword v222, v244, s[16:19], s77 offen offset:128 sc1\n"
    "v_mfma_f32_16x16x32_bf16 a[28:31], a[124:127], v[88:91], a[28:31]\n"
    "v_exp_f32 v25, v25\n"
    "v_mfma_f32_16x16x32_bf16 a[28:31], a[120:123], v[84:87], a[28:31]\n"
    "buffer_store_dword v224, v244, s[16:19], s77 offen offset:1024 sc1\n"
    "v_mfma_f32_16x16x32_bf16 a[80:83], a[128:131], v[84:87], a[80:83]\n"
    "v_exp_f32 v26, v26\n"
    "v_mfma_f32_16x16x32_bf16 a[80:83], a[132:135], v[88:91], a[80:83]\n"
    "buffer_store_dword v226, v244, s[16:19], s77 offen offset:1152 sc1\n"
    "v_mfma_f32_16x16x32_bf16 a[96:99], a[132:135], v[96:99], a[96:99]\n"
    "v_cvt_pk_bf16_f32 v232, v232, v233\n"
    "v_cvt_pk_bf16_f32 v234, v234, v235\n"
    "v_mfma_f32_16x16x32_bf16 a[96:99], a[128:131], v[92:95], a[96:99]\n"
    "buffer_store_dword v228, v244, s[16:19], s77 offen offset:256 sc1\n"
    "v_mfma_f32_16x16x32_bf16 a[44:47], a[120:123], v[92:95], a[44:47]\n"
    "v_exp_f32 v27, v27\n"
    "v_mfma_f32_16x16x32_bf16 a[44:47], a[124:127], v[96:99], a[44:47]\n"
    "buffer_store_dword v230, v244, s[16:19], s77 offen offset:384 sc1\n"
    "v_mfma_f32_16x16x32_bf16 a[40:43], a[116:119], v[96:99], a[40:43]\n"
    "v_exp_f32 v28, v28\n"
    "v_mfma_f32_16x16x32_bf16 a[40:43], a[112:115], v[92:95], a[40:43]\n"
    "s_waitcnt lgkmcnt(0)\n"
    "v_exp_f32 v29, v29\n"
    "v_mfma_f32_16x16x32_bf16 a[52:55], a[136:139], v[68:71], a[52:55]\n"
    "ds_read_b128 a[112:115], v3 offset:12288\n"
    "v_mfma_f32_16x16x32_bf16 a[52:55], a[140:143], v[72:75], a[52:55]\n"
    "ds_read_b128 a[116:119], v3 offset:14336\n"
    "v_mfma_f32_16x16x32_bf16 a[56:59], a[148:151], v[72:75], a[56:59]\n"
    "ds_read_b128 a[120:123], v3 offset:16384\n"
    "v_mfma_f32_16x16x32_bf16 a[56:59], a[144:147], v[68:71], a[56:59]\n"
    "ds_read_b128 a[124:127], v3 offset:18432\n"
    "v_mfma_f32_16x16x32_bf16 a[60:63], a[152:155], v[68:71], a[60:63]\n"
    "ds_read_b128 a[128:131], v3 offset:20480\n"
    "v_mfma_f32_16x16x32_bf16 a[60:63], a[156:159], v[72:75], a[60:63]\n"
    "ds_read_b128 a[132:135], v3 offset:22528\n"
    "v_mfma_f32_16x16x32_bf16 a[76:79], a[156:159], v[80:83], a[76:79]\n"
    "ds_read_b128 v[100:103], v250 offset:1664\n"
    "v_mfma_f32_16x16x32_bf16 a[76:79], a[152:155], v[76:79], a[76:79]\n"
    "buffer_store_dword v232, v244, s[16:19], s77 offen offset:1280 sc1\n"
    "v_mfma_f32_16x16x32_bf16 a[72:75], a[144:147], v[76:79], a[72:75]\n"
    "v_exp_f32 v30, v30\n"
    "v_mfma_f32_16x16x32_bf16 a[72:75], a[148:151], v[80:83], a[72:75]\n"
    "buffer_store_dword v234, v244, s[16:19], s77 offen offset:1408 sc1\n"
    "v_mfma_f32_16x16x32_bf16 a[68:71], a[140:143], v[80:83], a[68:71]\n"
    "v_exp_f32 v31, v31\n"
    "v_mfma_f32_16x16x32_bf16 a[68:71], a[136:139], v[76:79], a[68:71]\n"
    "v_exp_f32 v32, v32\n"
    "v_mfma_f32_16x16x32_bf16 a[84:87], a[136:139], v[84:87], a[84:87]\n"
    "v_exp_f32 v33, v33\n"
    "v_mfma_f32_16x16x32_bf16 a[84:87], a[140:143], v[88:91], a[84:87]\n"
    "v_exp_f32 v34, v34\n"
    "v_mfma_f32_16x16x32_bf16 a[88:91], a[148:151], v[88:91], a[88:91]\n"
    "s_waitcnt vmcnt(17)\n"
    "ds_add_u32 v247, v246\n"
    "v_mfma_f32_16x16x32_bf16 a[88:91], a[144:147], v[84:87], a[88:91]\n"
    "v_exp_f32 v35, v35\n"
    "v_mfma_f32_16x16x32_bf16 a[92:95], a[152:155], v[84:87], a[92:95]\n"
    "v_mfma_f32_16x16x32_bf16 a[92:95], a[156:159], v[88:91], a[92:95]\n"
    "v_mfma_f32_16x16x32_bf16 a[108:111], a[156:159], v[96:99], a[108:111]\n"
    "v_mfma_f32_16x16x32_bf16 a[108:111], a[152:155], v[92:95], a[108:111]\n"
    "v_mfma_f32_16x16x32_bf16 a[104:107], a[144:147], v[92:95], a[104:107]\n"
    "s_sub_u32 s74, s68, s65\n"
    "v_add_u32 v250, 1536, v250\n"
    "v_add_u32 v251, 1536, v251\n"
    "v_mfma_f32_16x16x32_bf16 a[104:107], a[148:151], v[96:99], a[104:107]\n"
    "s_add_u32 s66, s66, 1\n"
    "v_mov_b32 v2, v3\n"
    "v_add_u32 v3, s74, v252\n"
    "v_mfma_f32_16x16x32_bf16 a[100:103], a[140:143], v[96:99], a[100:103]\n"
    "s_waitcnt lgkmcnt(1)\n"
    "s_cmp_lt_u32 s66, 29\n"
    "v_mfma_f32_16x16x32_bf16 a[100:103], a[136:139], v[92:95], a[100:103]\n"
    "s_cbranch_scc1 .LPK\n"
    "s_branch .Ltail\n"
    "s_nop 0\n"
    ".LpollAK:\n"
    "s_sleep 1\n"
    "buffer_load_dwordx2 v[236:237], v87, s[36:39], 0 offen offset:0 sc1\n"
    "s_waitcnt vmcnt(0)\n"
    "v_cmp_eq_u64 s[54:55], v[236:237], v[238:239]\n"
    "s_add_u32 s58, s58, 1\n"
    "s_cmp_eq_u64 s[54:55], -1\n"
    "s_cbranch_scc1 .LpollAKr\n"
    "s_cmp_lt_u32 s58, 0x80000\n"
    "s_cbranch_scc1 .LpollAK\n"
    "s_branch .LpollAKr\n"
    "s_nop 0\n"
    "s_nop 0\n"
    "s_nop 0\n"
    ".LpollAV:\n"
    "s_sleep 1\n"
    "buffer_load_dwordx2 v[236:237], v87, s[36:39], 0 offen offset:0 sc1\n"
    "s_waitcnt vmcnt(0)\n"
    "v_cmp_eq_u64 s[54:55], v[236:237], v[238:239]\n"
    "s_add_u32 s58, s58, 1\n"
    "s_cmp_eq_u64 s[54:55], -1\n"
    "s_cbranch_scc1 .LpollAVr\n"
    "s_cmp_lt_u32 s58, 0x80000\n"
    "s_cbranch_scc1 .LpollAV\n"
    "s_branch .LpollAVr\n"
    "s_nop 0\n"
    "s_nop 0\n"
    "s_nop 0\n"
    "s_nop 0\n"
    "s_nop 0\n"
    "s_nop 0\n"
    "s_nop 0\n"
    ".Lpoll1:\n"
    "s_sleep 1\n"
    "v_cmp_eq_u64 s[54:55], v[236:237], v[238:239]\n"
    "s_nop 1\n"
    "s_cmp_eq_u64 s[54:55], -1\n"
    "s_cbranch_scc1 .Lpoll1l\n"
    "buffer_load_dwordx2 v[236:237], v241, s[36:39], s81 offen offset:0 sc1\n"
    "s_waitcnt vmcnt(0)\n"
    "v_cmp_eq_u64 s[54:55], v[236:237], v[238:239]\n"
    ".Lpoll1l:\n"
    "ds_read_b32 v245, v247\n"
    "s_waitcnt lgkmcnt(0)\n"
    "v_cmp_le_u32 s[56:57], s59, v245\n"
    "s_add_u32 s58, s58, 1\n"
    "s_nop 1\n"
    "s_and_b64 s[54:55], s[54:55], s[56:57]\n"
    "s_cmp_eq_u64 s[54:55], -1\n"
    "s_cbranch_scc1 .Lpoll1r\n"
    "s_cmp_lt_u32 s58, 0x80000\n"
    "s_cbranch_scc1 .Lpoll1\n"
    "s_branch .Lpoll1r\n"
    "s_nop 0\n"
    "s_nop 0\n"
    "s_nop 0\n"
    "s_nop 0\n"
    "s_nop 0\n"
    "s_nop 0\n"
    "s_nop 0\n"
    ".Lpoll2:\n"
    "s_sleep 1\n"
    "v_cmp_eq_u64 s[54:55], v[236:237], v[238:239]\n"
    "s_nop 1\n"
    "s_cmp_eq_u64 s[54:55], -1\n"
    "s_cbranch_scc1 .Lpoll2l\n"
    "buffer_load_dwordx2 v[236:237], v241, s[36:39], s81 offen offset:0 sc1\n"
    "s_waitcnt vmcnt(0)\n"
    "v_cmp_eq_u64 s[54:55], v[236:237], v[238:239]\n"
    ".Lpoll2l:\n"
    "ds_read_b32 v245, v247\n"
    "s_waitcnt lgkmcnt(0)\n"
    "v_cmp_le_u32 s[56:57], s59, v245\n"
    "s_add_u32 s58, s58, 1\n"
    "s_nop 1\n"
    "s_and_b64 s[54:55], s[54:55], s[56:57]\n"
    "s_cmp_eq_u64 s[54:55], -1\n"
    "s_cbranch_scc1 .Lpoll2r\n"
    "s_cmp_lt_u32 s58, 0x80000\n"
    "s_cbranch_scc1 .Lpoll2\n"
    "s_branch .Lpoll2r\n"
    "s_nop 0\n"
    "s_nop 0\n"
    "s_nop 0\n"
    "s_nop 0\n"
    "s_nop 0\n"
    "s_nop 0\n"
    "s_nop 0\n"
    ".Lpoll3:\n"
    "s_sleep 1\n"
    "v_cmp_eq_u64 s[54:55], v[236:237], v[238:239]\n"
    "s_nop 1\n"
    "s_cmp_eq_u64 s[54:55], -1\n"
    "s_cbranch_scc1 .Lpoll3l\n"
    "buffer_load_dwordx2 v[236:237], v241, s[36:39], s81 offen offset:0 sc1\n"
    "s_waitcnt vmcnt(0)\n"
    "v_cmp_eq_u64 s[54:55], v[236:237], v[238:239]\n"
    ".Lpoll3l:\n"
    "ds_read_b32 v245, v247\n"
    "s_waitcnt lgkmcnt(0)\n"
    "v_cmp_le_u32 s[56:57], s59, v245\n"
    "s_add_u32 s58, s58, 1\n"
    "s_nop 1\n"
    "s_and_b64 s[54:55], s[54:55], s[56:57]\n"
    "s_cmp_eq_u64 s[54:55], -1\n"
    "s_cbranch_scc1 .Lpoll3r\n"
    "s_cmp_lt_u32 s58, 0x80000\n"
    "s_cbranch_scc1 .Lpoll3\n"
    "s_branch .Lpoll3r\n"
    "s_nop 0\n"
    "s_nop 0\n"
    "s_nop 0\n"
    "s_nop 0\n"
    "s_nop 0\n"
    "s_nop 0\n"
    "s_nop 0\n"
    ".Lpoll4:\n"
    "s_sleep 1\n"
    "v_cmp_eq_u64 s[54:55], v[236:237], v[238:239]\n"
    "s_nop 1\n"
    "s_cmp_eq_u64 s[54:55], -1\n"
    "s_cbranch_scc1 .Lpoll4l\n"
    "buffer_load_dwordx2 v[236:237], v241, s[36:39], s81 offen offset:0 sc1\n"
    "s_waitcnt vmcnt(0)\n"
    "v_cmp_eq_u64 s[54:55], v[236:237], v[238:239]\n"
    ".Lpoll4l:\n"
    "ds_read_b32 v245, v247\n"
    "s_waitcnt lgkmcnt(0)\n"
    "v_cmp_le_u32 s[56:57], s59, v245\n"
    "s_add_u32 s58, s58, 1\n"
    "s_nop 1\n"
    "s_and_b64 s[54:55], s[54:55], s[56:57]\n"
    "s_cmp_eq_u64 s[54:55], -1\n"
    "s_cbranch_scc1 .Lpoll4r\n"
    "s_cmp_lt_u32 s58, 0x80000\n"
    "s_cbranch_scc1 .Lpoll4\n"
    "s_branch .Lpoll4r\n"
    "s_nop 0\n"
    "s_nop 0\n"
    "s_nop 0\n"
    "s_nop 0\n"
    "s_nop 0\n"
    "s_nop 0\n"
    "s_nop 0\n"
    ".Lpt0:\n"
    "s_sleep 1\n"
    "v_cmp_eq_u64 s[54:55], v[236:237], v[238:239]\n"
    "s_nop 1\n"
    "s_cmp_eq_u64 s[54:55], -1\n"
    "s_cbranch_scc1 .Lpt0l\n"
    "buffer_load_dwordx2 v[236:237], v241, s[36:39], s81 offen offset:0 sc1\n"
    "s_waitcnt vmcnt(0)\n"
    "v_cmp_eq_u64 s[54:55], v[236:237], v[238:239]\n"
    ".Lpt0l:\n"
    "ds_read_b32 v245, v247\n"
    "s_waitcnt lgkmcnt(0)\n"
    "v_cmp_le_u32 s[56:57], s59, v245\n"
    "s_add_u32 s58, s58, 1\n"
    "s_nop 1\n"
    "s_and_b64 s[54:55], s[54:55], s[56:57]\n"
    "s_cmp_eq_u64 s[54:55], -1\n"
    "s_cbranch_scc1 .Lpt0r\n"
    "s_cmp_lt_u32 s58, 0x80000\n"
    "s_cbranch_scc1 .Lpt0\n"
    "s_branch .Lpt0r\n"
    "s_nop 0\n"
    "s_nop 0\n"
    "s_nop 0\n"
    ".Lpt1:\n"
    "s_sleep 1\n"
    "ds_read_b32 v245, v247\n"
    "s_waitcnt lgkmcnt(0)\n"
    "v_cmp_le_u32 s[56:57], s59, v245\n"
    "s_add_u32 s58, s58, 1\n"
    "s_nop 1\n"
    "s_cmp_eq_u64 s[56:57], -1\n"
    "s_cbranch_scc1 .Lpt1r\n"
    "s_cmp_lt_u32 s58, 0x80000\n"
    "s_cbranch_scc1 .Lpt1\n"
    "s_branch .Lpt1r\n"
    "s_nop 0\n"
    "s_nop 0\n"
    "s_nop 0\n"
    ".Lpt4:\n"
    "s_sleep 1\n"
    "ds_read_b32 v245, v247\n"
    "s_waitcnt lgkmcnt(0)\n"
    "v_cmp_le_u32 s[56:57], s59, v245\n"
    "s_add_u32 s58, s58, 1\n"
    "s_nop 1\n"
    "s_cmp_eq_u64 s[56:57], -1\n"
    "s_cbranch_scc1 .Lpt4r\n"
    "s_cmp_lt_u32 s58, 0x80000\n"
    "s_cbranch_scc1 .Lpt4\n"
    "s_branch .Lpt4r\n"
    "s_nop 0\n"
    ".Lpt3:\n"
    "s_sleep 1\n"
    "ds_read_b32 v245, v247\n"
    "s_waitcnt lgkmcnt(0)\n"
    "v_cmp_le_u32 s[56:57], s59, v245\n"
    "s_add_u32 s58, s58, 1\n"
    "s_nop 1\n"
    "s_cmp_eq_u64 s[56:57], -1\n"
    "s_cbranch_scc1 .Lpt3r\n"
    "s_cmp_lt_u32 s58, 0x80000\n"
    "s_cbranch_scc1 .Lpt3\n"
    "s_branch .Lpt3r\n"
    ".fill ((0x20000 - (.LwVe - .LwV) - (. - _Z8attn_fwdPKtPKiS2_S0_S0_S0_S0_PcPt)) & 4095) / 4, 4, 0xBF800000\n"
    ".LwV:\n"
    "s_branch .LwVe\n"
    ".LPV:\n"
    "v_mfma_f32_16x16x32_bf16 v[36:39], a[112:115], a[192:195], v[100:103]\n"
    "s_add_u32 s71, s71, s44\n"
    "ds_read_b128 a[136:139], v2 offset:13312\n"
    "v_cvt_pk_bf16_f32 v68, v4, v5\n"
    "v_mfma_f32_16x16x32_bf16 v[36:39], a[116:119], a[196:199], v[36:39]\n"
    "s_add_u32 s75, s71, s63\n"
    "ds_read_b128 a[140:143], v2 offset:15360\n"
    "v_cvt_pk_bf16_f32 v69, v6, v7\n"
    "v_mfma_f32_16x16x32_bf16 v[36:39], a[120:123], a[200:203], v[36:39]\n"
    "ds_read_b128 a[144:147], v2 offset:17408\n"
    "v_cvt_pk_bf16_f32 v76, v8, v9\n"
    "v_mfma_f32_16x16x32_bf16 v[36:39], a[124:127], a[204:207], v[36:39]\n"
    "ds_read_b128 a[148:151], v2 offset:19456\n"
    "v_cvt_pk_bf16_f32 v77, v10, v11\n"
    "v_mfma_f32_16x16x32_bf16 v[36:39], a[128:131], a[208:211], v[36:39]\n"
    "ds_read_b128 a[152:155], v2 offset:21504\n"
    "v_cvt_pk_bf16_f32 v84, v12, v13\n"
    "v_mfma_f32_16x16x32_bf16 v[36:39], a[132:135], a[212:215], v[36:39]\n"
    "ds_read_b128 a[156:159], v2 offset:23552\n"
    "v_cvt_pk_bf16_f32 v85, v14, v15\n"
    "v_mfma_f32_16x16x32_bf16 v[40:43], a[132:135], a[236:239], v[100:103]\n"
    "ds_read_b128 v[104:107], v250 offset:144\n"
    "v_cvt_pk_bf16_f32 v92, v16, v17\n"
    "v_mfma_f32_16x16x32_bf16 v[40:43], a[128:131], a[232:235], v[40:43]\n"
    "s_add_u32 s68, s68, 24576\n"
    "v_cvt_pk_bf16_f32 v93, v18, v19\n"
    "v_cvt_pk_bf16_f32 v70, v20, v21\n"
    "v_mfma_f32_16x16x32_bf16 v[40:43], a[124:127], a[228:231], v[40:43]\n"
    "s_cmp_ge_u32 s68, s69\n"
    "s_cselect_b32 s68, s65, s68\n"
    "v_cvt_pk_bf16_f32 v71, v22, v23\n"
    "v_mfma_f32_16x16x32_bf16 v[40:43], a[120:123], a[224:227], v[40:43]\n"
    "v_cvt_pk_bf16_f32 v78, v24, v25\n"
    "v_cvt_pk_bf16_f32 v79, v26, v27\n"
    "v_mfma_f32_16x16x32_bf16 v[40:43], a[116:119], a[220:223], v[40:43]\n"
    "v_cvt_pk_bf16_f32 v86, v28, v29\n"
    "v_cvt_pk_bf16_f32 v87, v30, v31\n"
    "v_mfma_f32_16x16x32_bf16 v[40:43], a[112:115], a[216:219], v[40:43]\n"
    "v_cvt_pk_bf16_f32 v94, v32, v33\n"
    "v_cvt_pk_bf16_f32 v95, v34, v35\n"
    "v_mfma_f32_16x16x32_bf16 v[44:47], a[112:115], a[240:243], v[100:103]\n"
    "v_exp_f32 v36, v36\n"
    "v_mfma_f32_16x16x32_bf16 v[44:47], a[116:119], a[244:247], v[44:47]\n"
    "v_exp_f32 v37, v37\n"
    "v_mfma_f32_16x16x32_bf16 v[44:47], a[120:123], a[248:251], v[44:47]\n"
    "v_exp_f32 v38, v38\n"
    "v_mfma_f32_16x16x32_bf16 v[44:47], a[124:127], a[252:255], v[44:47]\n"
    "v_exp_f32 v39, v39\n"
    "v_mfma_f32_16x16x32_bf16 v[44:47], a[128:131], a[160:163], v[44:47]\n"
    "v_cvt_pk_bf16_f32 v72, v36, v37\n"
    "v_cvt_pk_bf16_f32 v73, v38, v39\n"
    "v_mfma_f32_16x16x32_bf16 v[44:47], a[132:135], a[164:167], v[44:47]\n"
    "v_exp_f32 v40, v40\n"
    "v_mfma_f32_16x16x32_bf16 v[48:51], a[132:135], a[188:191], v[100:103]\n"
    "ds_read_b32 v245, v247\n"
    "v_mfma_f32_16x16x32_bf16 v[48:51], a[128:131], a[184:187], v[48:51]\n"
    "s_add_u32 m0, s68, 0\n"
    "v_exp_f32 v41, v41\n"
    "v_mfma_f32_16x16x32_bf16 v[48:51], a[124:127], a[180:183], v[48:51]\n"
    "v_exp_f32 v42, v42\n"
    "v_mfma_f32_16x16x32_bf16 v[48:51], a[120:123], a[176:179], v[48:51]\n"
    "v_exp_f32 v43, v43\n"
    "v_mfma_f32_16x16x32_bf16 v[48:51], a[116:119], a[172:175], v[48:51]\n"
    "s_waitcnt vmcnt(13)\n"
    "v_cmp_eq_u64 s[54:55], v[236:237], v[238:239]\n"
    "v_cvt_pk_bf16_f32 v80, v40, v41\n"
    "v_mfma_f32_16x16x32_bf16 v[48:51], a[112:115], a[168:171], v[48:51]\n"
    "s_waitcnt lgkmcnt(1)\n"
    "v_cvt_pk_bf16_f32 v81, v42, v43\n"
    "v_mfma_f32_16x16x32_bf16 v[52:55], a[136:139], a[192:195], v[104:107]\n"
    "s_waitcnt lgkmcnt(0)\n"
    "v_cmp_le_u32 s[56:57], s59, v245\n"
    "v_mfma_f32_16x16x32_bf16 v[52:55], a[140:143], a[196:199], v[52:55]\n"
    "v_exp_f32 v44, v44\n"
    "v_mfma_f32_16x16x32_bf16 v[52:55], a[144:147], a[200:203], v[52:55]\n"
    "s_and_b64 s[54:55], s[54:55], s[56:57]\n"
    "s_cmp_eq_u64 s[54:55], -1\n"
    "v_mfma_f32_16x16x32_bf16 v[52:55], a[148:151], a[204:207], v[52:55]\n"
    "s_cbranch_scc0 .Lpoll3\n"
    ".Lpoll3r:\n"
    "v_mfma_f32_16x16x32_bf16 v[52:55], a[152:155], a[208:211], v[52:55]\n"
    "buffer_load_dwordx4 v1, s[16:19], s67 offen offset:0 sc1 lds\n"
    "v_mfma_f32_16x16x32_bf16 v[52:55], a[156:159], a[212:215], v[52:55]\n"
    "ds_read_b128 a[112:115], v3 offset:0\n"
    "v_mfma_f32_16x16x32_bf16 v[56:59], a[156:159], a[236:239], v[104:107]\n"
    "ds_read_b128 a[116:119], v3 offset:2048\n"
    "v_mfma_f32_16x16x32_bf16 v[56:59], a[152:155], a[232:235], v[56:59]\n"
    "ds_read_b128 a[120:123], v3 offset:4096\n"
    "v_mfma_f32_16x16x32_bf16 v[56:59], a[148:151], a[228:231], v[56:59]\n"
    "buffer_load_dwordx4 v1, s[16:19], s67 offen offset:1024 sc1 lds\n"
    "v_mfma_f32_16x16x32_bf16 v[56:59], a[144:147], a[224:227], v[56:59]\n"
    "ds_read_b128 a[124:127], v3 offset:6144\n"
    "v_mfma_f32_16x16x32_bf16 v[56:59], a[140:143], a[220:223], v[56:59]\n"
    "ds_read_b128 a[128:131], v3 offset:8192\n"
    "v_mfma_f32_16x16x32_bf16 v[56:59], a[136:139], a[216:219], v[56:59]\n"
    "ds_read_b128 a[132:135], v3 offset:10240\n"
    "v_mfma_f32_16x16x32_bf16 v[60:63], a[136:139], a[240:243], v[104:107]\n"
    "buffer_load_dwordx4 v1, s[16:19], s67 offen offset:2048 sc1 lds\n"
    "v_mfma_f32_16x16x32_bf16 v[60:63], a[140:143], a[244:247], v[60:63]\n"
    "ds_read_b128 v[100:103], v250 offset:768\n"
    "v_mfma_f32_16x16x32_bf16 v[60:63], a[144:147], a[248:251], v[60:63]\n"
    "v_exp_f32 v52, v52\n"
    "v_mfma_f32_16x16x32_bf16 v[60:63], a[148:151], a[252:255], v[60:63]\n"
    "v_exp_f32 v53, v53\n"
    "v_mfma_f32_16x16x32_bf16 v[60:63], a[152:155], a[160:163], v[60:63]\n"
    "buffer_load_dwordx4 v1, s[16:19], s67 offen offset:3072 sc1 lds\n"
    "v_mfma_f32_16x16x32_bf16 v[60:63], a[156:159], a[164:167], v[60:63]\n"
    "s_add_u32 m0, s68, 4096\n"
    "v_exp_f32 v54, v54\n"
    "v_mfma_f32_16x16x32_bf16 v[64:67], a[156:159], a[188:191], v[104:107]\n"
    "s_add_u32 s83, s67, 4096\n"
    "v_exp_f32 v55, v55\n"
    "v_mfma_f32_16x16x32_bf16 v[64:67], a[152:155], a[184:187], v[64:67]\n"
    "v_cvt_pk_bf16_f32 v74, v52, v53\n"
    "v_cvt_pk_bf16_f32 v75, v54, v55\n"
    "v_mfma_f32_16x16x32_bf16 v[64:67], a[148:151], a[180:183], v[64:67]\n"
    "buffer_load_dwordx4 v1, s[16:19], s83 offen offset:0 sc1 lds\n"
    "v_mfma_f32_16x16x32_bf16 v[64:67], a[144:147], a[176:179], v[64:67]\n"
    "v_exp_f32 v56, v56\n"
    "v_mfma_f32_16x16x32_bf16 v[64:67], a[140:143], a[172:175], v[64:67]\n"
    "v_exp_f32 v57, v57\n"
    "v_mfma_f32_16x16x32_bf16 v[64:67], a[136:139], a[168:171], v[64:67]\n"
    "s_waitcnt lgkmcnt(0)\n"
    "v_exp_f32 v58, v58\n"
    "v_mfma_f32_16x16x32_bf16 v[4:7], a[112:115], a[192:195], v[100:103]\n"
    "buffer_load_dwordx4 v1, s[16:19], s83 offen offset:1024 sc1 lds\n"
    "v_mfma_f32_16x16x32_bf16 v[4:7], a[116:119], a[196:199], v[4:7]\n"
    "s_add_u32 m0, s64, 0\n"
    "ds_read_b128 a[136:139], v3 offset:1024\n"
    "v_cvt_pk_bf16_f32 v82, v56, v57\n"
    "v_mfma_f32_16x16x32_bf16 v[4:7], a[120:123], a[200:203], v[4:7]\n"
    "s_add_u32 s83, s67, 24576\n"
    "ds_read_b128 a[140:143], v3 offset:3072\n"
    "v_mfma_f32_16x16x32_bf16 v[4:7], a[124:127], a[204:207], v[4:7]\n"
    "ds_read_b128 a[144:147], v3 offset:5120\n"
    "v_mfma_f32_16x16x32_bf16 v[4:7], a[128:131], a[208:211], v[4:7]\n"
    "buffer_load_dwordx4 v1, s[16:19], s83 offen offset:0 sc1 lds\n"
    "v_mfma_f32_16x16x32_bf16 v[4:7], a[132:135], a[212:215], v[4:7]\n"
    "ds_read_b128 a[148:151], v3 offset:7168\n"
    "v_mfma_f32_16x16x32_bf16 v[8:11], a[132:135], a[236:239], v[100:103]\n"
    "ds_read_b128 a[152:155], v3 offset:9216\n"
    "v_mfma_f32_16x16x32_bf16 v[8:11], a[128:131], a[232:235], v[8:11]\n"
    "ds_read_b128 a[156:159], v3 offset:11264\n"
    "v_mfma_f32_16x16x32_bf16 v[8:11], a[124:127], a[228:231], v[8:11]\n"
    "buffer_load_dwordx4 v1, s[16:19], s83 offen offset:1024 sc1 lds\n"
    "v_mfma_f32_16x16x32_bf16 v[8:11], a[120:123], a[224:227], v[8:11]\n"
    "ds_read_b128 v[104:107], v250 offset:784\n"
    "v_mfma_f32_16x16x32_bf16 v[8:11], a[116:119], a[220:223], v[8:11]\n"
    "v_exp_f32 v59, v59\n"
    "v_mfma_f32_16x16x32_bf16 v[8:11], a[112:115], a[216:219], v[8:11]\n"
    "v_cvt_pk_bf16_f32 v83, v58, v59\n"
    "v_mfma_f32_16x16x32_bf16 v[12:15], a[112:115], a[240:243], v[100:103]\n"
    "buffer_load_dwordx4 v1, s[16:19], s83 offen offset:2048 sc1 lds\n"
    "v_mfma_f32_16x16x32_bf16 v[12:15], a[116:119], a[244:247], v[12:15]\n"
    "v_exp_f32 v45, v45\n"
    "v_mfma_f32_16x16x32_bf16 v[12:15], a[120:123], a[248:251], v[12:15]\n"
    "v_exp_f32 v46, v46\n"
    "v_mfma_f32_16x16x32_bf16 v[12:15], a[124:127], a[252:255], v[12:15]\n"
    "v_exp_f32 v47, v47\n"
    "v_mfma_f32_16x16x32_bf16 v[12:15], a[128:131], a[160:163], v[12:15]\n"
    "buffer_load_dwordx4 v1, s[16:19], s83 offen offset:3072 sc1 lds\n"
    "v_mfma_f32_16x16x32_bf16 v[12:15], a[132:135], a[164:167], v[12:15]\n"
    "s_add_u32 m0, s64, 4096\n"
    "v_exp_f32 v60, v60\n"
    "v_mfma_f32_16x16x32_bf16 v[16:19], a[132:135], a[188:191], v[100:103]\n"
    "s_add_u32 s83, s67, 28672\n"
    "v_exp_f32 v61, v61\n"
    "v_mfma_f32_16x16x32_bf16 v[16:19], a[128:131], a[184:187], v[16:19]\n"
    "v_exp_f32 v62, v62\n"
    "v_mfma_f32_16x16x32_bf16 v[16:19], a[124:127], a[180:183], v[16:19]\n"
    "buffer_load_dwordx4 v1, s[16:19], s83 offen offset:0 sc1 lds\n"
    "v_mfma_f32_16x16x32_bf16 v[16:19], a[120:123], a[176:179], v[16:19]\n"
    "v_exp_f32 v63, v63\n"
    "v_mfma_f32_16x16x32_bf16 v[16:19], a[116:119], a[172:175], v[16:19]\n"
    "v_cvt_pk_bf16_f32 v88, v44, v45\n"
    "v_cvt_pk_bf16_f32 v89, v46, v47\n"
    "v_mfma_f32_16x16x32_bf16 v[16:19], a[112:115], a[168:171], v[16:19]\n"
    "s_waitcnt lgkmcnt(0)\n"
    "v_cvt_pk_bf16_f32 v90, v60, v61\n"
    "v_cvt_pk_bf16_f32 v91, v62, v63\n"
    "v_mfma_f32_16x16x32_bf16 v[20:23], a[136:139], a[192:195], v[104:107]\n"
    "buffer_load_dwordx4 v1, s[16:19], s83 offen offset:1024 sc1 lds\n"
    "v_mfma_f32_16x16x32_bf16 v[20:23], a[140:143], a[196:199], v[20:23]\n"
    "s_add_u32 s67, s67, 49152\n"
    "ds_read_b128 a[112:115], v248 offset:0\n"
    "v_mfma_f32_16x16x32_bf16 v[20:23], a[144:147], a[200:203], v[20:23]\n"
    "s_add_u32 s59, s59, 4\n"
    "ds_read_b128 a[116:119], v248 offset:12288\n"
    "v_mfma_f32_16x16x32_bf16 v[20:23], a[148:151], a[204:207], v[20:23]\n"
    "s_waitcnt vmcnt(12)\n"
    "ds_read_b128 a[120:123], v248 offset:1024\n"
    "v_mfma_f32_16x16x32_bf16 v[20:23], a[152:155], a[208:211], v[20:23]\n"
    "buffer_store_dwordx2 v[238:239], v240, s[36:39], s80 offen offset:0 sc1\n"
    "v_mfma_f32_16x16x32_bf16 v[20:23], a[156:159], a[212:215], v[20:23]\n"
    "buffer_load_dwordx4 v[204:207], v242, s[40:43], s75 offen offset:0 nt\n"
    "v_mfma_f32_16x16x32_bf16 v[24:27], a[156:159], a[236:239], v[104:107]\n"
    "s_add_u32 s80, s80, 256\n"
    "ds_read_b128 a[124:127], v248 offset:13312\n"
    "v_mfma_f32_16x16x32_bf16 v[24:27], a[152:155], a[232:235], v[24:27]\n"
    "buffer_load_dwordx4 v[208:211], v242, s[40:43], s75 offen offset:768 nt\n"
    "v_mfma_f32_16x16x32_bf16 v[24:27], a[148:151], a[228:231], v[24:27]\n"
    "ds_read_b128 a[128:131], v248 offset:2048\n"
    "v_mfma_f32_16x16x32_bf16 v[24:27], a[144:147], a[224:227], v[24:27]\n"
    "buffer_load_dwordx4 v[212:215], v242, s[40:43], s75 offen offset:256 nt\n"
    "v_mfma_f32_16x16x32_bf16 v[24:27], a[140:143], a[220:223], v[24:27]\n"
    "ds_read_b128 a[132:135], v248 offset:14336\n"
    "v_mfma_f32_16x16x32_bf16 v[24:27], a[136:139], a[216:219], v[24:27]\n"
    "buffer_load_dwordx4 v[216:219], v242, s[40:43], s75 offen offset:1024 nt\n"
    "v_mfma_f32_16x16x32_bf16 v[28:31], a[136:139], a[240:243], v[104:107]\n"
    "v_exp_f32 v48, v48\n"
    "v_mfma_f32_16x16x32_bf16 v[28:31], a[140:143], a[244:247], v[28:31]\n"
    "buffer_load_dwordx4 v[220:223], v242, s[40:43], s75 offen offset:512 nt\n"
    "v_mfma_f32_16x16x32_bf16 v[28:31], a[144:147], a[248:251], v[28:31]\n"
    "v_exp_f32 v49, v49\n"
    "v_mfma_f32_16x16x32_bf16 v[28:31], a[148:151], a[252:255], v[28:31]\n"
    "buffer_load_dwordx4 v[224:227], v242, s[40:43], s75 offen offset:1280 nt\n"
    "v_mfma_f32_16x16x32_bf16 v[28:31], a[152:155], a[160:163], v[28:31]\n"
    "v_exp_f32 v50, v50\n"
    "v_mfma_f32_16x16x32_bf16 v[28:31], a[156:159], a[164:167], v[28:31]\n"
    "v_exp_f32 v51, v51\n"
    "v_mfma_f32_16x16x32_bf16 v[32:35], a[156:159], a[188:191], v[104:107]\n"
    "v_exp_f32 v64, v64\n"
    "v_mfma_f32_16x16x32_bf16 v[32:35], a[152:155], a[184:187], v[32:35]\n"
    "v_exp_f32 v65, v65\n"
    "v_mfma_f32_16x16x32_bf16 v[32:35], a[148:151], a[180:183], v[32:35]\n"
    "v_exp_f32 v66, v66\n"
    "v_mfma_f32_16x16x32_bf16 v[32:35], a[144:147], a[176:179], v[32:35]\n"
    "v_exp_f32 v67, v67\n"
    "v_mfma_f32_16x16x32_bf16 v[32:35], a[140:143], a[172:175], v[32:35]\n"
    "v_cvt_pk_bf16_f32 v96, v48, v49\n"
    "v_cvt_pk_bf16_f32 v97, v50, v51\n"
    "v_mfma_f32_16x16x32_bf16 v[32:35], a[136:139], a[168:171], v[32:35]\n"
    "s_waitcnt lgkmcnt(0)\n"
    "v_cvt_pk_bf16_f32 v98, v64, v65\n"
    "v_cvt_pk_bf16_f32 v99, v66, v67\n"
    "v_mfma_f32_16x16x32_bf16 v[124:127], a[112:115], v[68:71], v[124:127]\n"
    "ds_read_b128 a[136:139], v248 offset:3072\n"
    "v_mfma_f32_16x16x32_bf16 v[124:127], a[116:119], v[72:75], v[124:127]\n"
    "ds_read_b128 a[140:143], v248 offset:15360\n"
    "v_mfma_f32_16x16x32_bf16 v[128:131], a[124:127], v[72:75], v[128:131]\n"
    "ds_read_b128 a[144:147], v248 offset:4096\n"
    "v_mfma_f32_16x16x32_bf16 v[128:131], a[120:123], v[68:71], v[128:131]\n"
    "ds_read_b128 a[148:151], v248 offset:16384\n"
    "v_mfma_f32_16x16x32_bf16 v[132:135], a[128:131], v[68:71], v[132:135]\n"
    "ds_read_b128 a[152:155], v248 offset:5120\n"
    "v_mfma_f32_16x16x32_bf16 v[132:135], a[132:135], v[72:75], v[132:135]\n"
    "ds_read_b128 a[156:159], v248 offset:17408\n"
    "v_mfma_f32_16x16x32_bf16 v[148:151], a[132:135], v[80:83], v[148:151]\n"
    "ds_read_b128 v[104:107], v251 offset:0\n"
    "v_mfma_f32_16x16x32_bf16 v[148:151], a[128:131], v[76:79], v[148:151]\n"
    "v_exp_f32 v4, v4\n"
    "v_mfma_f32_16x16x32_bf16 v[144:147], a[120:123], v[76:79], v[144:147]\n"
    "v_exp_f32 v5, v5\n"
    "v_mfma_f32_16x16x32_bf16 v[144:147], a[124:127], v[80:83], v[144:147]\n"
    "v_exp_f32 v6, v6\n"
    "v_mfma_f32_16x16x32_bf16 v[140:143], a[116:119], v[80:83], v[140:143]\n"
    "v_mfma_f32_16x16x32_bf16 v[140:143], a[112:115], v[76:79], v[140:143]\n"
    "v_mfma_f32_16x16x32_bf16 v[156:159], a[112:115], v[84:87], v[156:159]\n"
    "v_exp_f32 v7, v7\n"
    "v_mfma_f32_16x16x32_bf16 v[156:159], a[116:119], v[88:91], v[156:159]\n"
    "v_mfma_f32_16x16x32_bf16 v[160:163], a[124:127], v[88:91], v[160:163]\n"
    "v_exp_f32 v8, v8\n"
    "v_mfma_f32_16x16x32_bf16 v[160:163], a[120:123], v[84:87], v[160:163]\n"
    "v_mfma_f32_16x16x32_bf16 v[164:167], a[128:131], v[84:87], v[164:167]\n"
    "v_mfma_f32_16x16x32_bf16 v[164:167], a[132:135], v[88:91], v[164:167]\n"
    "v_exp_f32 v9, v9\n"
    "v_mfma_f32_16x16x32_bf16 v[180:183], a[132:135], v[96:99], v[180:183]\n"
    "v_mfma_f32_16x16x32_bf16 v[180:183], a[128:131], v[92:95], v[180:183]\n"
    "v_mfma_f32_16x16x32_bf16 v[176:179], a[120:123], v[92:95], v[176:179]\n"
    "v_exp_f32 v10, v10\n"
    "v_mfma_f32_16x16x32_bf16 v[176:179], a[124:127], v[96:99], v[176:179]\n"
    "v_mfma_f32_16x16x32_bf16 v[172:175], a[116:119], v[96:99], v[172:175]\n"
    "v_exp_f32 v11, v11\n"
    "v_mfma_f32_16x16x32_bf16 v[172:175], a[112:115], v[92:95], v[172:175]\n"
    "s_waitcnt lgkmcnt(0)\n"
    "v_mfma_f32_16x16x32_bf16 v[136:139], a[136:139], v[68:71], v[136:139]\n"
    "ds_read_b128 a[112:115], v248 offset:6144\n"
    "v_mfma_f32_16x16x32_bf16 v[136:139], a[140:143], v[72:75], v[136:139]\n"
    "ds_read_b128 a[116:119], v248 offset:18432\n"
    "v_mfma_f32_16x16x32_bf16 v[188:191], a[148:151], v[72:75], v[188:191]\n"
    "ds_read_b128 a[120:123], v248 offset:7168\n"
    "v_mfma_f32_16x16x32_bf16 v[188:191], a[144:147], v[68:71], v[188:191]\n"
    "ds_read_b128 a[124:127], v248 offset:19456\n"
    "v_mfma_f32_16x16x32_bf16 v[192:195], a[152:155], v[68:71], v[192:195]\n"
    "ds_read_b128 a[128:131], v248 offset:8192\n"
    "v_mfma_f32_16x16x32_bf16 v[192:195], a[156:159], v[72:75], v[192:195]\n"
    "ds_read_b128 a[132:135], v248 offset:20480\n"
    "v_smfmac_f32_16x16x64_bf16 v[108:111], v[104:107], v[68:75], v253\n"
    "s_add_u32 s81, s81, 128\n"
    "s_cmp_gt_u32 s81, 4096\n"
    "s_cselect_b32 s81, 0, s81\n"
    "v_smfmac_f32_16x16x64_bf16 v[112:115], v[104:107], v[76:83], v253\n"
    "v_mfma_f32_16x16x32_bf16 a[4:7], a[156:159], v[80:83], a[4:7]\n"
    "v_exp_f32 v12, v12\n"
    "v_mfma_f32_16x16x32_bf16 a[4:7], a[152:155], v[76:79], a[4:7]\n"
    "v_exp_f32 v13, v13\n"
    "v_mfma_f32_16x16x32_bf16 a[0:3], a[144:147], v[76:79], a[0:3]\n"
    "v_exp_f32 v14, v14\n"
    "v_mfma_f32_16x16x32_bf16 a[0:3], a[148:151], v[80:83], a[0:3]\n"
    "v_exp_f32 v15, v15\n"
    "v_mfma_f32_16x16x32_bf16 v[152:155], a[140:143], v[80:83], v[152:155]\n"
    "v_exp_f32 v16, v16\n"
    "v_mfma_f32_16x16x32_bf16 v[152:155], a[136:139], v[76:79], v[152:155]\n"
    "v_mfma_f32_16x16x32_bf16 v[168:171], a[136:139], v[84:87], v[168:171]\n"
    "v_exp_f32 v17, v17\n"
    "v_mfma_f32_16x16x32_bf16 v[168:171], a[140:143], v[88:91], v[168:171]\n"
    "v_mfma_f32_16x16x32_bf16 a[16:19], a[148:151], v[88:91], a[16:19]\n"
    "v_mfma_f32_16x16x32_bf16 a[16:19], a[144:147], v[84:87], a[16:19]\n"
    "v_exp_f32 v18, v18\n"
    "v_mfma_f32_16x16x32_bf16 a[20:23], a[152:155], v[84:87], a[20:23]\n"
    "v_mfma_f32_16x16x32_bf16 a[20:23], a[156:159], v[88:91], a[20:23]\n"
    "v_smfmac_f32_16x16x64_bf16 v[116:119], v[104:107], v[84:91], v253\n"
    "s_add_u32 s52, s52, s45\n"
    "v_smfmac_f32_16x16x64_bf16 v[120:123], v[104:107], v[92:99], v253\n"
    "v_mfma_f32_16x16x32_bf16 a[36:39], a[156:159], v[96:99], a[36:39]\n"
    "v_exp_f32 v19, v19\n"
    "v_mfma_f32_16x16x32_bf16 a[36:39], a[152:155], v[92:95], a[36:39]\n"
    "v_exp_f32 v20, v20\n"
    "v_mfma_f32_16x16x32_bf16 a[32:35], a[144:147], v[92:95], a[32:35]\n"
    "v_mfma_f32_16x16x32_bf16 a[32:35], a[148:151], v[96:99], a[32:35]\n"
    "v_exp_f32 v21, v21\n"
    "v_mfma_f32_16x16x32_bf16 v[184:187], a[140:143], v[96:99], v[184:187]\n"
    "v_mfma_f32_16x16x32_bf16 v[184:187], a[136:139], v[92:95], v[184:187]\n"
    "s_waitcnt lgkmcnt(0)\n"
    "v_mfma_f32_16x16x32_bf16 v[196:199], a[112:115], v[68:71], v[196:199]\n"
    "ds_read_b128 a[136:139], v248 offset:9216\n"
    "v_mfma_f32_16x16x32_bf16 v[196:199], a[116:119], v[72:75], v[196:199]\n"
    "ds_read_b128 a[140:143], v248 offset:21504\n"
    "v_mfma_f32_16x16x32_bf16 v[200:203], a[124:127], v[72:75], v[200:203]\n"
    "ds_read_b128 a[144:147], v248 offset:10240\n"
    "v_mfma_f32_16x16x32_bf16 v[200:203], a[120:123], v[68:71], v[200:203]\n"
    "ds_read_b128 a[148:151], v248 offset:22528\n"
    "v_mfma_f32_16x16x32_bf16 a[48:51], a[128:131], v[68:71], a[48:51]\n"
    "buffer_load_dwordx2 v[236:237], v241, s[36:39], s81 offen offset:0 sc1\n"
    "v_mfma_f32_16x16x32_bf16 a[48:51], a[132:135], v[72:75], a[48:51]\n"
    "ds_read_b128 a[152:155], v248 offset:11264\n"
    "v_mfma_f32_16x16x32_bf16 a[64:67], a[132:135], v[80:83], a[64:67]\n"
    "ds_read_b128 a[156:159], v248 offset:23552\n"
    "v_mfma_f32_16x16x32_bf16 a[64:67], a[128:131], v[76:79], a[64:67]\n"
    "v_exp_f32 v22, v22\n"
    "v_mfma_f32_16x16x32_bf16 a[12:15], a[120:123], v[76:79], a[12:15]\n"
    "v_exp_f32 v23, v23\n"
    "v_mfma_f32_16x16x32_bf16 a[12:15], a[124:127], v[80:83], a[12:15]\n"
    "v_exp_f32 v24, v24\n"
    "v_mfma_f32_16x16x32_bf16 a[8:11], a[116:119], v[80:83], a[8:11]\n"
    "v_exp_f32 v25, v25\n"
    "v_mfma_f32_16x16x32_bf16 a[8:11], a[112:115], v[76:79], a[8:11]\n"
    "v_exp_f32 v26, v26\n"
    "v_mfma_f32_16x16x32_bf16 a[24:27], a[112:115], v[84:87], a[24:27]\n"
    "v_mfma_f32_16x16x32_bf16 a[24:27], a[116:119], v[88:91], a[24:27]\n"
    "v_exp_f32 v27, v27\n"
    "v_mfma_f32_16x16x32_bf16 a[28:31], a[124:127], v[88:91], a[28:31]\n"
    "v_mfma_f32_16x16x32_bf16 a[28:31], a[120:123], v[84:87], a[28:31]\n"
    "v_mfma_f32_16x16x32_bf16 a[80:83], a[128:131], v[84:87], a[80:83]\n"
    "v_exp_f32 v28, v28\n"
    "v_mfma_f32_16x16x32_bf16 a[80:83], a[132:135], v[88:91], a[80:83]\n"
    "v_mfma_f32_16x16x32_bf16 a[96:99], a[132:135], v[96:99], a[96:99]\n"
    "v_mfma_f32_16x16x32_bf16 a[96:99], a[128:131], v[92:95], a[96:99]\n"
    "v_exp_f32 v29, v29\n"
    "v_mfma_f32_16x16x32_bf16 a[44:47], a[120:123], v[92:95], a[44:47]\n"
    "v_mfma_f32_16x16x32_bf16 a[44:47], a[124:127], v[96:99], a[44:47]\n"
    "v_exp_f32 v30, v30\n"
    "v_mfma_f32_16x16x32_bf16 a[40:43], a[116:119], v[96:99], a[40:43]\n"
    "v_mfma_f32_16x16x32_bf16 a[40:43], a[112:115], v[92:95], a[40:43]\n"
    "s_waitcnt lgkmcnt(0)\n"
    "v_mfma_f32_16x16x32_bf16 a[52:55], a[136:139], v[68:71], a[52:55]\n"
    "ds_read_b128 a[112:115], v3 offset:12288\n"
    "v_mfma_f32_16x16x32_bf16 a[52:55], a[140:143], v[72:75], a[52:55]\n"
    "ds_read_b128 a[116:119], v3 offset:14336\n"
    "v_mfma_f32_16x16x32_bf16 a[56:59], a[148:151], v[72:75], a[56:59]\n"
    "ds_read_b128 a[120:123], v3 offset:16384\n"
    "v_mfma_f32_16x16x32_bf16 a[56:59], a[144:147], v[68:71], a[56:59]\n"
    "ds_read_b128 a[124:127], v3 offset:18432\n"
    "v_mfma_f32_16x16x32_bf16 a[60:63], a[152:155], v[68:71], a[60:63]\n"
    "ds_read_b128 a[128:131], v3 offset:20480\n"
    "v_mfma_f32_16x16x32_bf16 a[60:63], a[156:159], v[72:75], a[60:63]\n"
    "ds_read_b128 a[132:135], v3 offset:22528\n"
    "v_mfma_f32_16x16x32_bf16 a[76:79], a[156:159], v[80:83], a[76:79]\n"
    "ds_read_b128 v[100:103], v250 offset:896\n"
    "v_mfma_f32_16x16x32_bf16 a[76:79], a[152:155], v[76:79], a[76:79]\n"
    "v_exp_f32 v31, v31\n"
    "v_mfma_f32_16x16x32_bf16 a[72:75], a[144:147], v[76:79], a[72:75]\n"
    "v_exp_f32 v32, v32\n"
    "v_mfma_f32_16x16x32_bf16 a[72:75], a[148:151], v[80:83], a[72:75]\n"
    "v_exp_f32 v33, v33\n"
    "v_mfma_f32_16x16x32_bf16 a[68:71], a[140:143], v[80:83], a[68:71]\n"
    "v_exp_f32 v34, v34\n"
    "v_mfma_f32_16x16x32_bf16 a[68:71], a[136:139], v[76:79], a[68:71]\n"
    "v_exp_f32_e64 v35, v35\n"
    "v_mfma_f32_16x16x32_bf16 a[84:87], a[136:139], v[84:87], a[84:87]\n"
    "v_mfma_f32_16x16x32_bf16 a[84:87], a[140:143], v[88:91], a[84:87]\n"
    "v_mfma_f32_16x16x32_bf16 a[88:91], a[148:151], v[88:91], a[88:91]\n"
    "s_waitcnt vmcnt(8)\n"
    "ds_add_u32 v247, v246\n"
    "v_mfma_f32_16x16x32_bf16 a[88:91], a[144:147], v[84:87], a[88:91]\n"
    "v_mfma_f32_16x16x32_bf16 a[92:95], a[152:155], v[84:87], a[92:95]\n"
    "v_mfma_f32_16x16x32_bf16 a[92:95], a[156:159], v[88:91], a[92:95]\n"
    "v_mfma_f32_16x16x32_bf16 a[108:111], a[156:159], v[96:99], a[108:111]\n"
    "v_mfma_f32_16x16x32_bf16 a[108:111], a[152:155], v[92:95], a[108:111]\n"
    "v_mfma_f32_16x16x32_bf16 a[104:107], a[144:147], v[92:95], a[104:107]\n"
    "s_sub_u32 s74, s68, s65\n"
    "v_mfma_f32_16x16x32_bf16 a[104:107], a[148:151], v[96:99], a[104:107]\n"
    "s_add_u32 s66, s66, .Lend-.Lend2+(1)\n"
    "v_mov_b32_e64 v2, v3\n"
    "v_add_u32_e64 v3, s74, v252\n"
    "v_mfma_f32_16x16x32_bf16 a[100:103], a[140:143], v[96:99], a[100:103]\n"
    "s_waitcnt lgkmcnt(1)\n"
    "s_cmp_eq_u32 s66, .Lend-.Lend2+(29)\n"
    "v_mfma_f32_16x16x32_bf16 a[100:103], a[136:139], v[92:95], a[100:103]\n"
    ".LwVe:\n"
    "s_cbranch_scc1 .LtailP\n"
    ".LNV:\n"
    "v_mfma_f32_16x16x32_bf16 v[36:39], a[112:115], a[192:195], v[100:103]\n"
    "ds_read_b128 a[136:139], v2 offset:13312\n"
    "v_cvt_pk_bf16_f32 v68, v4, v5\n"
    "v_mfma_f32_16x16x32_bf16 v[36:39], a[116:119], a[196:199], v[36:39]\n"
    "ds_read_b128 a[140:143], v2 offset:15360\n"
    "v_mfma_f32_16x16x32_bf16 v[36:39], a[120:123], a[200:203], v[36:39]\n"
    "ds_read_b128 a[144:147], v2 offset:17408\n"
    "v_mfma_f32_16x16x32_bf16 v[36:39], a[124:127], a[204:207], v[36:39]\n"
    "ds_read_b128 a[148:151], v2 offset:19456\n"
    "v_cvt_pk_bf16_f32 v69, v6, v7\n"
    "v_mfma_f32_16x16x32_bf16 v[36:39], a[128:131], a[208:211], v[36:39]\n"
    "ds_read_b128 a[152:155], v2 offset:21504\n"
    "v_mfma_f32_16x16x32_bf16 v[36:39], a[132:135], a[212:215], v[36:39]\n"
    "ds_read_b128 a[156:159], v2 offset:23552\n"
    "v_mfma_f32_16x16x32_bf16 v[40:43], a[132:135], a[236:239], v[100:103]\n"
    "ds_read_b128 v[104:107], v250 offset:912\n"
    "v_mfma_f32_16x16x32_bf16 v[40:43], a[128:131], a[232:235], v[40:43]\n"
    "s_add_u32 s68, s68, 24576\n"
    "v_cvt_pk_bf16_f32 v76, v8, v9\n"
    "v_mfma_f32_16x16x32_bf16 v[40:43], a[124:127], a[228:231], v[40:43]\n"
    "s_cmp_ge_u32 s68, s69\n"
    "s_cselect_b32 s68, s65, s68\n"
    "v_mfma_f32_16x16x32_bf16 v[40:43], a[120:123], a[224:227], v[40:43]\n"
    "v_exp_f32 v36, v36\n"
    "v_mfma_f32_16x16x32_bf16 v[40:43], a[116:119], a[220:223], v[40:43]\n"
    "v_exp_f32 v37, v37\n"
    "v_mfma_f32_16x16x32_bf16 v[40:43], a[112:115], a[216:219], v[40:43]\n"
    "v_cvt_pk_bf16_f32 v77, v10, v11\n"
    "v_cvt_pk_bf16_f32 v72, v36, v37\n"
    "v_mfma_f32_16x16x32_bf16 v[44:47], a[112:115], a[240:243], v[100:103]\n"
    "v_mfma_f32_16x16x32_bf16 v[44:47], a[116:119], a[244:247], v[44:47]\n"
    "v_exp_f32 v38, v38\n"
    "v_mfma_f32_16x16x32_bf16 v[44:47], a[120:123], a[248:251], v[44:47]\n"
    "v_mfma_f32_16x16x32_bf16 v[44:47], a[124:127], a[252:255], v[44:47]\n"
    "v_cvt_pk_bf16_f32 v84, v12, v13\n"
    "v_mfma_f32_16x16x32_bf16 v[44:47], a[128:131], a[160:163], v[44:47]\n"
    "v_exp_f32 v39, v39\n"
    "v_mfma_f32_16x16x32_bf16 v[44:47], a[132:135], a[164:167], v[44:47]\n"
    "v_cvt_pk_bf16_f32 v73, v38, v39\n"
    "v_mfma_f32_16x16x32_bf16 v[48:51], a[132:135], a[188:191], v[100:103]\n"
    "ds_read_b32 v245, v247\n"
    "v_cvt_pk_bf16_f32 v85, v14, v15\n"
    "v_mfma_f32_16x16x32_bf16 v[48:51], a[128:131], a[184:187], v[48:51]\n"
    "s_add_u32 m0, s68, 0\n"
    "v_exp_f32 v40, v40\n"
    "v_mfma_f32_16x16x32_bf16 v[48:51], a[124:127], a[180:183], v[48:51]\n"
    "v_mfma_f32_16x16x32_bf16 v[48:51], a[120:123], a[176:179], v[48:51]\n"
    "v_mfma_f32_16x16x32_bf16 v[48:51], a[116:119], a[172:175], v[48:51]\n"
    "s_waitcnt vmcnt(0)\n"
    "v_cmp_eq_u64 s[54:55], v[236:237], v[238:239]\n"
    "v_cvt_pk_bf16_f32 v92, v16, v17\n"
    "v_mfma_f32_16x16x32_bf16 v[48:51], a[112:115], a[168:171], v[48:51]\n"
    "s_waitcnt lgkmcnt(1)\n"
    "v_exp_f32 v41, v41\n"
    "v_mfma_f32_16x16x32_bf16 v[52:55], a[136:139], a[192:195], v[104:107]\n"
    "s_waitcnt lgkmcnt(0)\n"
    "v_cmp_le_u32 s[56:57], s59, v245\n"
    "v_cvt_pk_bf16_f32 v80, v40, v41\n"
    "v_mfma_f32_16x16x32_bf16 v[52:55], a[140:143], a[196:199], v[52:55]\n"
    "s_waitcnt vmcnt(1)\n"
    "v_exp_f32 v42, v42\n"
    "v_mfma_f32_16x16x32_bf16 v[52:55], a[144:147], a[200:203], v[52:55]\n"
    "s_and_b64 s[54:55], s[54:55], s[56:57]\n"
    "s_cmp_eq_u64 s[54:55], -1\n"
    "v_mfma_f32_16x16x32_bf16 v[52:55], a[148:151], a[204:207], v[52:55]\n"
    "s_cbranch_scc0 .Lpoll4\n"
    ".Lpoll4r:\n"
    "v_mfma_f32_16x16x32_bf16 v[52:55], a[152:155], a[208:211], v[52:55]\n"
    "buffer_load_dwordx4 v1, s[16:19], s67 offen offset:0 sc1 lds\n"
    "v_mfma_f32_16x16x32_bf16 v[52:55], a[156:159], a[212:215], v[52:55]\n"
    "s_add_u32 s76, s52, s60\n"
    "ds_read_b128 a[112:115], v3 offset:0\n"
    "v_cvt_pk_bf16_f32 v93, v18, v19\n"
    "v_mfma_f32_16x16x32_bf16 v[56:59], a[156:159], a[236:239], v[104:107]\n"
    "s_add_u32 s77, s52, s61\n"
    "ds_read_b128 a[116:119], v3 offset:2048\n"
    "v_cvt_pk_bf16_f32 v70, v20, v21\n"
    "v_mfma_f32_16x16x32_bf16 v[56:59], a[152:155], a[232:235], v[56:59]\n"
    "ds_read_b128 a[120:123], v3 offset:4096\n"
    "v_mfma_f32_16x16x32_bf16 v[56:59], a[148:151], a[228:231], v[56:59]\n"
    "buffer_load_dwordx4 v1, s[16:19], s67 offen offset:1024 sc1 lds\n"
    "v_mfma_f32_16x16x32_bf16 v[56:59], a[144:147], a[224:227], v[56:59]\n"
    "ds_read_b128 a[124:127], v3 offset:6144\n"
    "v_cvt_pk_bf16_f32 v71, v22, v23\n"
    "v_mfma_f32_16x16x32_bf16 v[56:59], a[140:143], a[220:223], v[56:59]\n"
    "s_add_u32 s78, s52, s62\n"
    "ds_read_b128 a[128:131], v3 offset:8192\n"
    "v_mfma_f32_16x16x32_bf16 v[56:59], a[136:139], a[216:219], v[56:59]\n"
    "ds_read_b128 a[132:135], v3 offset:10240\n"
    "v_mfma_f32_16x16x32_bf16 v[60:63], a[136:139], a[240:243], v[104:107]\n"
    "buffer_load_dwordx4 v1, s[16:19], s67 offen offset:2048 sc1 lds\n"
    "v_mfma_f32_16x16x32_bf16 v[60:63], a[140:143], a[244:247], v[60:63]\n"
    "ds_read_b128 v[100:103], v250 offset:1536\n"
    "v_cvt_pk_bf16_f32 v78, v24, v25\n"
    "v_mfma_f32_16x16x32_bf16 v[60:63], a[144:147], a[248:251], v[60:63]\n"
    "v_exp_f32 v43, v43\n"
    "v_mfma_f32_16x16x32_bf16 v[60:63], a[148:151], a[252:255], v[60:63]\n"
    "v_cvt_pk_bf16_f32 v81, v42, v43\n"
    "v_cvt_pk_u16_u32 v204, v204, v208\n"
    "v_mfma_f32_16x16x32_bf16 v[60:63], a[152:155], a[160:163], v[60:63]\n"
    "buffer_load_dwordx4 v1, s[16:19], s67 offen offset:3072 sc1 lds\n"
    "v_mfma_f32_16x16x32_bf16 v[60:63], a[156:159], a[164:167], v[60:63]\n"
    "s_add_u32 m0, s68, 4096\n"
    "v_cvt_pk_bf16_f32 v79, v26, v27\n"
    "v_mfma_f32_16x16x32_bf16 v[64:67], a[156:159], a[188:191], v[104:107]\n"
    "s_add_u32 s83, s67, 4096\n"
    "v_exp_f32 v44, v44\n"
    "v_mfma_f32_16x16x32_bf16 v[64:67], a[152:155], a[184:187], v[64:67]\n"
    "v_exp_f32 v45, v45\n"
    "v_mfma_f32_16x16x32_bf16 v[64:67], a[148:151], a[180:183], v[64:67]\n"
    "buffer_load_dwordx4 v1, s[16:19], s83 offen offset:0 sc1 lds\n"
    "v_mfma_f32_16x16x32_bf16 v[64:67], a[144:147], a[176:179], v[64:67]\n"
    "v_cvt_pk_bf16_f32 v86, v28, v29\n"
    "v_cvt_pk_bf16_f32 v88, v44, v45\n"
    "v_mfma_f32_16x16x32_bf16 v[64:67], a[140:143], a[172:175], v[64:67]\n"
    "v_exp_f32 v46, v46\n"
    "v_mfma_f32_16x16x32_bf16 v[64:67], a[136:139], a[168:171], v[64:67]\n"
    "s_waitcnt lgkmcnt(0)\n"
    "v_exp_f32 v47, v47\n"
    "v_mfma_f32_16x16x32_bf16 v[4:7], a[112:115], a[192:195], v[100:103]\n"
    "buffer_load_dwordx4 v1, s[16:19], s83 offen offset:1024 sc1 lds\n"
    "v_mfma_f32_16x16x32_bf16 v[4:7], a[116:119], a[196:199], v[4:7]\n"
    "s_add_u32 m0, s70, 0\n"
    "ds_read_b128 a[136:139], v3 offset:1024\n"
    "v_cvt_pk_bf16_f32 v87, v30, v31\n"
    "v_mfma_f32_16x16x32_bf16 v[4:7], a[120:123], a[200:203], v[4:7]\n"
    "s_add_u32 s83, s67, 24576\n"
    "ds_read_b128 a[140:143], v3 offset:3072\n"
    "v_cvt_pk_bf16_f32 v89, v46, v47\n"
    "v_mfma_f32_16x16x32_bf16 v[4:7], a[124:127], a[204:207], v[4:7]\n"
    "ds_read_b128 a[144:147], v3 offset:5120\n"
    "v_cvt_pk_u16_u32 v205, v205, v209\n"
    "v_mfma_f32_16x16x32_bf16 v[4:7], a[128:131], a[208:211], v[4:7]\n"
    "buffer_load_dwordx4 v1, s[16:19], s83 offen offset:0 sc1 lds\n"
    "v_mfma_f32_16x16x32_bf16 v[4:7], a[132:135], a[212:215], v[4:7]\n"
    "ds_read_b128 a[148:151], v3 offset:7168\n"
    "v_cvt_pk_bf16_f32 v94, v32, v33\n"
    "v_mfma_f32_16x16x32_bf16 v[8:11], a[132:135], a[236:239], v[100:103]\n"
    "ds_read_b128 a[152:155], v3 offset:9216\n"
    "v_cvt_pk_u16_u32 v206, v206, v210\n"
    "v_mfma_f32_16x16x32_bf16 v[8:11], a[128:131], a[232:235], v[8:11]\n"
    "ds_read_b128 a[156:159], v3 offset:11264\n"
    "v_cvt_pk_u16_u32 v207, v207, v211\n"
    "v_mfma_f32_16x16x32_bf16 v[8:11], a[124:127], a[228:231], v[8:11]\n"
    "buffer_load_dwordx4 v1, s[16:19], s83 offen offset:1024 sc1 lds\n"
    "v_mfma_f32_16x16x32_bf16 v[8:11], a[120:123], a[224:227], v[8:11]\n"
    "ds_read_b128 v[104:107], v250 offset:1552\n"
    "v_cvt_pk_bf16_f32 v95, v34, v35\n"
    "v_mfma_f32_16x16x32_bf16 v[8:11], a[116:119], a[220:223], v[8:11]\n"
    "v_exp_f32 v52, v52\n"
    "v_mfma_f32_16x16x32_bf16 v[8:11], a[112:115], a[216:219], v[8:11]\n"
    "v_exp_f32 v53, v53\n"
    "v_mfma_f32_16x16x32_bf16 v[12:15], a[112:115], a[240:243], v[100:103]\n"
    "buffer_load_dwordx4 v1, s[16:19], s83 offen offset:2048 sc1 lds\n"
    "v_mfma_f32_16x16x32_bf16 v[12:15], a[116:119], a[244:247], v[12:15]\n"
    "v_exp_f32 v54, v54\n"
    "v_mfma_f32_16x16x32_bf16 v[12:15], a[120:123], a[248:251], v[12:15]\n"
    "v_exp_f32 v55, v55\n"
    "v_mfma_f32_16x16x32_bf16 v[12:15], a[124:127], a[252:255], v[12:15]\n"
    "v_cvt_pk_bf16_f32 v74, v52, v53\n"
    "v_cvt_pk_bf16_f32 v75, v54, v55\n"
    "v_mfma_f32_16x16x32_bf16 v[12:15], a[128:131], a[160:163], v[12:15]\n"
    "buffer_load_dwordx4 v1, s[16:19], s83 offen offset:3072 sc1 lds\n"
    "v_mfma_f32_16x16x32_bf16 v[12:15], a[132:135], a[164:167], v[12:15]\n"
    "s_add_u32 m0, s70, 4096\n"
    "v_exp_f32 v56, v56\n"
    "v_mfma_f32_16x16x32_bf16 v[16:19], a[132:135], a[188:191], v[100:103]\n"
    "s_add_u32 s83, s67, 28672\n"
    "v_exp_f32 v48, v48\n"
    "v_mfma_f32_16x16x32_bf16 v[16:19], a[128:131], a[184:187], v[16:19]\n"
    "v_exp_f32 v49, v49\n"
    "v_mfma_f32_16x16x32_bf16 v[16:19], a[124:127], a[180:183], v[16:19]\n"
    "buffer_load_dwordx4 v1, s[16:19], s83 offen offset:0 sc1 lds\n"
    "v_mfma_f32_16x16x32_bf16 v[16:19], a[120:123], a[176:179], v[16:19]\n"
    "v_exp_f32 v57, v57\n"
    "v_mfma_f32_16x16x32_bf16 v[16:19], a[116:119], a[172:175], v[16:19]\n"
    "v_cvt_pk_bf16_f32 v82, v56, v57\n"
    "v_cvt_pk_bf16_f32 v96, v48, v49\n"
    "v_mfma_f32_16x16x32_bf16 v[16:19], a[112:115], a[168:171], v[16:19]\n"
    "s_waitcnt lgkmcnt(0)\n"
    "v_exp_f32 v58, v58\n"
    "v_mfma_f32_16x16x32_bf16 v[20:23], a[136:139], a[192:195], v[104:107]\n"
    "buffer_load_dwordx4 v1, s[16:19], s83 offen offset:1024 sc1 lds\n"
    "v_mfma_f32_16x16x32_bf16 v[20:23], a[140:143], a[196:199], v[20:23]\n"
    "s_add_u32 s67, s67, 49152\n"
    "ds_read_b128 a[112:115], v249 offset:0\n"
    "v_or3_b32 v243, v204, v205, v243\n"
    "v_mfma_f32_16x16x32_bf16 v[20:23], a[144:147], a[200:203], v[20:23]\n"
    "s_add_u32 s59, s59, 4\n"
    "ds_read_b128 a[116:119], v249 offset:12288\n"
    "v_or3_b32 v243, v206, v207, v243\n"
    "v_mfma_f32_16x16x32_bf16 v[20:23], a[148:151], a[204:207], v[20:23]\n"
    "ds_read_b128 a[120:123], v249 offset:1024\n"
    "v_cvt_pk_u16_u32 v212, v212, v216\n"
    "v_mfma_f32_16x16x32_bf16 v[20:23], a[152:155], a[208:211], v[20:23]\n"
    "ds_read_b128 a[124:127], v249 offset:13312\n"
    "v_cvt_pk_u16_u32 v213, v213, v217\n"
    "v_mfma_f32_16x16x32_bf16 v[20:23], a[156:159], a[212:215], v[20:23]\n"
    "ds_read_b128 a[128:131], v249 offset:2048\n"
    "v_mfma_f32_16x16x32_bf16 v[24:27], a[156:159], a[236:239], v[104:107]\n"
    "ds_read_b128 a[132:135], v249 offset:14336\n"
    "v_mfma_f32_16x16x32_bf16 v[24:27], a[152:155], a[232:235], v[24:27]\n"
    "v_exp_f32 v59, v59\n"
    "v_mfma_f32_16x16x32_bf16 v[24:27], a[148:151], a[228:231], v[24:27]\n"
    "v_cvt_pk_bf16_f32 v83, v58, v59\n"
    "v_cvt_pk_u16_u32 v214, v214, v218\n"
    "v_mfma_f32_16x16x32_bf16 v[24:27], a[144:147], a[224:227], v[24:27]\n"
    "v_exp_f32 v60, v60\n"
    "v_mfma_f32_16x16x32_bf16 v[24:27], a[140:143], a[220:223], v[24:27]\n"
    "v_exp_f32 v61, v61\n"
    "v_mfma_f32_16x16x32_bf16 v[24:27], a[136:139], a[216:219], v[24:27]\n"
    "v_exp_f32 v62, v62\n"
    "v_mfma_f32_16x16x32_bf16 v[28:31], a[136:139], a[240:243], v[104:107]\n"
    "v_cvt_pk_bf16_f32 v90, v60, v61\n"
    "v_mfma_f32_16x16x32_bf16 v[28:31], a[140:143], a[244:247], v[28:31]\n"
    "v_exp_f32 v63, v63\n"
    "v_mfma_f32_16x16x32_bf16 v[28:31], a[144:147], a[248:251], v[28:31]\n"
    "v_cvt_pk_bf16_f32 v91, v62, v63\n"
    "v_cvt_pk_u16_u32 v215, v215, v219\n"
    "v_mfma_f32_16x16x32_bf16 v[28:31], a[148:151], a[252:255], v[28:31]\n"
    "v_exp_f32 v50, v50\n"
    "v_mfma_f32_16x16x32_bf16 v[28:31], a[152:155], a[160:163], v[28:31]\n"
    "v_exp_f32 v51, v51\n"
    "v_mfma_f32_16x16x32_bf16 v[28:31], a[156:159], a[164:167], v[28:31]\n"
    "v_exp_f32 v64, v64\n"
    "v_mfma_f32_16x16x32_bf16 v[32:35], a[156:159], a[188:191], v[104:107]\n"
    "v_cvt_pk_bf16_f32 v97, v50, v51\n"
    "v_mfma_f32_16x16x32_bf16 v[32:35], a[152:155], a[184:187], v[32:35]\n"
    "v_exp_f32 v65, v65\n"
    "v_mfma_f32_16x16x32_bf16 v[32:35], a[148:151], a[180:183], v[32:35]\n"
    "v_cvt_pk_bf16_f32 v98, v64, v65\n"
    "v_or3_b32 v243, v212, v213, v243\n"
    "v_mfma_f32_16x16x32_bf16 v[32:35], a[144:147], a[176:179], v[32:35]\n"
    "v_mfma_f32_16x16x32_bf16 v[32:35], a[140:143], a[172:175], v[32:35]\n"
    "v_exp_f32 v66, v66\n"
    "v_mfma_f32_16x16x32_bf16 v[32:35], a[136:139], a[168:171], v[32:35]\n"
    "s_waitcnt lgkmcnt(0)\n"
    "v_mfma_f32_16x16x32_bf16 v[124:127], a[112:115], v[68:71], v[124:127]\n"
    "ds_read_b128 a[136:139], v249 offset:3072\n"
    "v_or3_b32 v243, v214, v215, v243\n"
    "v_mfma_f32_16x16x32_bf16 v[124:127], a[116:119], v[72:75], v[124:127]\n"
    "ds_read_b128 a[140:143], v249 offset:15360\n"
    "v_mfma_f32_16x16x32_bf16 v[128:131], a[124:127], v[72:75], v[128:131]\n"
    "ds_read_b128 a[144:147], v249 offset:4096\n"
    "v_mfma_f32_16x16x32_bf16 v[128:131], a[120:123], v[68:71], v[128:131]\n"
    "ds_read_b128 a[148:151], v249 offset:16384\n"
    "v_mfma_f32_16x16x32_bf16 v[132:135], a[128:131], v[68:71], v[132:135]\n"
    "ds_read_b128 a[152:155], v249 offset:5120\n"
    "v_mfma_f32_16x16x32_bf16 v[132:135], a[132:135], v[72:75], v[132:135]\n"
    "ds_read_b128 a[156:159], v249 offset:17408\n"
    "v_cvt_pk_u16_u32 v220, v220, v224\n"
    "v_mfma_f32_16x16x32_bf16 v[148:151], a[132:135], v[80:83], v[148:151]\n"
    "ds_read_b128 v[104:107], v251 offset:768\n"
    "v_mfma_f32_16x16x32_bf16 v[148:151], a[128:131], v[76:79], v[148:151]\n"
    "v_exp_f32 v67, v67\n"
    "v_mfma_f32_16x16x32_bf16 v[144:147], a[120:123], v[76:79], v[144:147]\n"
    "v_cvt_pk_bf16_f32 v99, v66, v67\n"
    "v_mfma_f32_16x16x32_bf16 v[144:147], a[124:127], v[80:83], v[144:147]\n"
    "v_exp_f32 v4, v4\n"
    "v_mfma_f32_16x16x32_bf16 v[140:143], a[116:119], v[80:83], v[140:143]\n"
    "v_exp_f32 v5, v5\n"
    "v_mfma_f32_16x16x32_bf16 v[140:143], a[112:115], v[76:79], v[140:143]\n"
    "v_exp_f32 v6, v6\n"
    "v_mfma_f32_16x16x32_bf16 v[156:159], a[112:115], v[84:87], v[156:159]\n"
    "v_exp_f32 v7, v7\n"
    "v_mfma_f32_16x16x32_bf16 v[156:159], a[116:119], v[88:91], v[156:159]\n"
    "v_cvt_pk_u16_u32 v221, v221, v225\n"
    "v_mfma_f32_16x16x32_bf16 v[160:163], a[124:127], v[88:91], v[160:163]\n"
    "v_exp_f32 v8, v8\n"
    "v_mfma_f32_16x16x32_bf16 v[160:163], a[120:123], v[84:87], v[160:163]\n"
    "v_cvt_pk_u16_u32 v222, v222, v226\n"
    "v_mfma_f32_16x16x32_bf16 v[164:167], a[128:131], v[84:87], v[164:167]\n"
    "v_mfma_f32_16x16x32_bf16 v[164:167], a[132:135], v[88:91], v[164:167]\n"
    "v_exp_f32 v9, v9\n"
    "v_mfma_f32_16x16x32_bf16 v[180:183], a[132:135], v[96:99], v[180:183]\n"
    "v_mfma_f32_16x16x32_bf16 v[180:183], a[128:131], v[92:95], v[180:183]\n"
    "v_mfma_f32_16x16x32_bf16 v[176:179], a[120:123], v[92:95], v[176:179]\n"
    "v_exp_f32 v10, v10\n"
    "v_mfma_f32_16x16x32_bf16 v[176:179], a[124:127], v[96:99], v[176:179]\n"
    "v_cvt_pk_u16_u32 v223, v223, v227\n"
    "v_mfma_f32_16x16x32_bf16 v[172:175], a[116:119], v[96:99], v[172:175]\n"
    "v_exp_f32 v11, v11\n"
    "v_mfma_f32_16x16x32_bf16 v[172:175], a[112:115], v[92:95], v[172:175]\n"
    "s_waitcnt lgkmcnt(0)\n"
    "v_mfma_f32_16x16x32_bf16 v[136:139], a[136:139], v[68:71], v[136:139]\n"
    "ds_read_b128 a[112:115], v249 offset:6144\n"
    "v_mfma_f32_16x16x32_bf16 v[136:139], a[140:143], v[72:75], v[136:139]\n"
    "ds_read_b128 a[116:119], v249 offset:18432\n"
    "v_mfma_f32_16x16x32_bf16 v[188:191], a[148:151], v[72:75], v[188:191]\n"
    "ds_read_b128 a[120:123], v249 offset:7168\n"
    "v_or3_b32 v243, v220, v221, v243\n"
    "v_mfma_f32_16x16x32_bf16 v[188:191], a[144:147], v[68:71], v[188:191]\n"
    "ds_read_b128 a[124:127], v249 offset:19456\n"
    "v_mfma_f32_16x16x32_bf16 v[192:195], a[152:155], v[68:71], v[192:195]\n"
    "ds_read_b128 a[128:131], v249 offset:8192\n"
    "v_mfma_f32_16x16x32_bf16 v[192:195], a[156:159], v[72:75], v[192:195]\n"
    "ds_read_b128 a[132:135], v249 offset:20480\n"
    "v_smfmac_f32_16x16x64_bf16 v[108:111], v[104:107], v[68:75], v253\n"
    "s_add_u32 s81, s81, 128\n"
    "s_cmp_gt_u32 s81, 4096\n"
    "s_cselect_b32 s81, 0, s81\n"
    "v_smfmac_f32_16x16x64_bf16 v[112:115], v[104:107], v[76:83], v253\n"
    "v_mfma_f32_16x16x32_bf16 a[4:7], a[156:159], v[80:83], a[4:7]\n"
    "v_exp_f32 v12, v12\n"
    "v_mfma_f32_16x16x32_bf16 a[4:7], a[152:155], v[76:79], a[4:7]\n"
    "v_exp_f32 v13, v13\n"
    "v_mfma_f32_16x16x32_bf16 a[0:3], a[144:147], v[76:79], a[0:3]\n"
    "v_exp_f32 v14, v14\n"
    "v_mfma_f32_16x16x32_bf16 a[0:3], a[148:151], v[80:83], a[0:3]\n"
    "v_exp_f32 v15, v15\n"
    "v_mfma_f32_16x16x32_bf16 v[152:155], a[140:143], v[80:83], v[152:155]\n"
    "v_or3_b32 v243, v222, v223, v243\n"
    "v_and_b32 v208, 0xff00ff00, v243\n"
    "v_mfma_f32_16x16x32_bf16 v[152:155], a[136:139], v[76:79], v[152:155]\n"
    "v_exp_f32 v16, v16\n"
    "v_mfma_f32_16x16x32_bf16 v[168:171], a[136:139], v[84:87], v[168:171]\n"
    "v_exp_f32 v17, v17\n"
    "v_mfma_f32_16x16x32_bf16 v[168:171], a[140:143], v[88:91], v[168:171]\n"
    "v_mfma_f32_16x16x32_bf16 a[16:19], a[148:151], v[88:91], a[16:19]\n"
    "v_mfma_f32_16x16x32_bf16 a[16:19], a[144:147], v[84:87], a[16:19]\n"
    "v_exp_f32 v18, v18\n"
    "v_mfma_f32_16x16x32_bf16 a[20:23], a[152:155], v[84:87], a[20:23]\n"
    "v_cmp_ne_u32 vcc, 0, v208\n"
    "v_mfma_f32_16x16x32_bf16 a[20:23], a[156:159], v[88:91], a[20:23]\n"
    "v_smfmac_f32_16x16x64_bf16 v[116:119], v[104:107], v[84:91], v253\n"
    "v_smfmac_f32_16x16x64_bf16 v[120:123], v[104:107], v[92:99], v253\n"
    "v_mfma_f32_16x16x32_bf16 a[36:39], a[156:159], v[96:99], a[36:39]\n"
    "v_exp_f32 v19, v19\n"
    "v_mfma_f32_16x16x32_bf16 a[36:39], a[152:155], v[92:95], a[36:39]\n"
    "v_exp_f32 v20, v20\n"
    "v_mfma_f32_16x16x32_bf16 a[32:35], a[144:147], v[92:95], a[32:35]\n"
    "v_cndmask_b32 v208, 2.0, v241, vcc\n"
    "v_mfma_f32_16x16x32_bf16 a[32:35], a[148:151], v[96:99], a[32:35]\n"
    "v_exp_f32 v21, v21\n"
    "v_mfma_f32_16x16x32_bf16 v[184:187], a[140:143], v[96:99], v[184:187]\n"
    "v_mfma_f32_16x16x32_bf16 v[184:187], a[136:139], v[92:95], v[184:187]\n"
    "s_waitcnt lgkmcnt(0)\n"
    "v_mfma_f32_16x16x32_bf16 v[196:199], a[112:115], v[68:71], v[196:199]\n"
    "ds_read_b128 a[136:139], v249 offset:9216\n"
    "v_mfma_f32_16x16x32_bf16 v[196:199], a[116:119], v[72:75], v[196:199]\n"
    "ds_read_b128 a[140:143], v249 offset:21504\n"
    "v_mfma_f32_16x16x32_bf16 v[200:203], a[124:127], v[72:75], v[200:203]\n"
    "ds_read_b128 a[144:147], v249 offset:10240\n"
    "v_mfma_f32_16x16x32_bf16 v[200:203], a[120:123], v[68:71], v[200:203]\n"
    "ds_read_b128 a[148:151], v249 offset:22528\n"
    "v_mfma_f32_16x16x32_bf16 a[48:51], a[128:131], v[68:71], a[48:51]\n"
    "buffer_load_dwordx2 v[236:237], v241, s[36:39], s81 offen offset:0 sc1\n"
    "v_mfma_f32_16x16x32_bf16 a[48:51], a[132:135], v[72:75], a[48:51]\n"
    "ds_read_b128 a[152:155], v249 offset:11264\n"
    "v_mfma_f32_16x16x32_bf16 a[64:67], a[132:135], v[80:83], a[64:67]\n"
    "ds_read_b128 a[156:159], v249 offset:23552\n"
    "v_mfma_f32_16x16x32_bf16 a[64:67], a[128:131], v[76:79], a[64:67]\n"
    "buffer_store_dword v204, v244, s[20:23], s76 offen offset:0 sc1\n"
    "v_mfma_f32_16x16x32_bf16 a[12:15], a[120:123], v[76:79], a[12:15]\n"
    "v_exp_f32 v22, v22\n"
    "v_mfma_f32_16x16x32_bf16 a[12:15], a[124:127], v[80:83], a[12:15]\n"
    "buffer_store_dword v205, v244, s[20:23], s76 offen offset:1024 sc1\n"
    "v_mfma_f32_16x16x32_bf16 a[8:11], a[116:119], v[80:83], a[8:11]\n"
    "v_exp_f32 v23, v23\n"
    "v_mfma_f32_16x16x32_bf16 a[8:11], a[112:115], v[76:79], a[8:11]\n"
    "buffer_store_dword v206, v244, s[20:23], s76 offen offset:2048 sc1\n"
    "v_mfma_f32_16x16x32_bf16 a[24:27], a[112:115], v[84:87], a[24:27]\n"
    "v_exp_f32 v24, v24\n"
    "v_mfma_f32_16x16x32_bf16 a[24:27], a[116:119], v[88:91], a[24:27]\n"
    "buffer_store_dword v207, v244, s[20:23], s76 offen offset:3072 sc1\n"
    "v_mfma_f32_16x16x32_bf16 a[28:31], a[124:127], v[88:91], a[28:31]\n"
    "v_exp_f32 v25, v25\n"
    "v_mfma_f32_16x16x32_bf16 a[28:31], a[120:123], v[84:87], a[28:31]\n"
    "buffer_store_dword v212, v244, s[20:23], s77 offen offset:0 sc1\n"
    "v_mfma_f32_16x16x32_bf16 a[80:83], a[128:131], v[84:87], a[80:83]\n"
    "v_exp_f32 v26, v26\n"
    "v_mfma_f32_16x16x32_bf16 a[80:83], a[132:135], v[88:91], a[80:83]\n"
    "buffer_store_dword v213, v244, s[20:23], s77 offen offset:1024 sc1\n"
    "v_mfma_f32_16x16x32_bf16 a[96:99], a[132:135], v[96:99], a[96:99]\n"
    "v_exp_f32 v27, v27\n"
    "v_mfma_f32_16x16x32_bf16 a[96:99], a[128:131], v[92:95], a[96:99]\n"
    "buffer_store_dword v214, v244, s[20:23], s77 offen offset:2048 sc1\n"
    "v_mfma_f32_16x16x32_bf16 a[44:47], a[120:123], v[92:95], a[44:47]\n"
    "v_exp_f32 v28, v28\n"
    "v_mfma_f32_16x16x32_bf16 a[44:47], a[124:127], v[96:99], a[44:47]\n"
    "buffer_store_dword v215, v244, s[20:23], s77 offen offset:3072 sc1\n"
    "v_mfma_f32_16x16x32_bf16 a[40:43], a[116:119], v[96:99], a[40:43]\n"
    "v_exp_f32 v29, v29\n"
    "v_mfma_f32_16x16x32_bf16 a[40:43], a[112:115], v[92:95], a[40:43]\n"
    "s_waitcnt lgkmcnt(0)\n"
    "v_exp_f32 v30, v30\n"
    "v_mfma_f32_16x16x32_bf16 a[52:55], a[136:139], v[68:71], a[52:55]\n"
    "ds_read_b128 a[112:115], v3 offset:12288\n"
    "v_mfma_f32_16x16x32_bf16 a[52:55], a[140:143], v[72:75], a[52:55]\n"
    "ds_read_b128 a[116:119], v3 offset:14336\n"
    "v_mfma_f32_16x16x32_bf16 a[56:59], a[148:151], v[72:75], a[56:59]\n"
    "ds_read_b128 a[120:123], v3 offset:16384\n"
    "v_mfma_f32_16x16x32_bf16 a[56:59], a[144:147], v[68:71], a[56:59]\n"
    "ds_read_b128 a[124:127], v3 offset:18432\n"
    "v_mfma_f32_16x16x32_bf16 a[60:63], a[152:155], v[68:71], a[60:63]\n"
    "ds_read_b128 a[128:131], v3 offset:20480\n"
    "v_mfma_f32_16x16x32_bf16 a[60:63], a[156:159], v[72:75], a[60:63]\n"
    "ds_read_b128 a[132:135], v3 offset:22528\n"
    "v_mfma_f32_16x16x32_bf16 a[76:79], a[156:159], v[80:83], a[76:79]\n"
    "ds_read_b128 v[100:103], v250 offset:1664\n"
    "v_mfma_f32_16x16x32_bf16 a[76:79], a[152:155], v[76:79], a[76:79]\n"
    "buffer_store_dword v220, v244, s[20:23], s78 offen offset:0 sc1\n"
    "v_mfma_f32_16x16x32_bf16 a[72:75], a[144:147], v[76:79], a[72:75]\n"
    "v_exp_f32 v31, v31\n"
    "v_mfma_f32_16x16x32_bf16 a[72:75], a[148:151], v[80:83], a[72:75]\n"
    "buffer_store_dword v221, v244, s[20:23], s78 offen offset:1024 sc1\n"
    "v_mfma_f32_16x16x32_bf16 a[68:71], a[140:143], v[80:83], a[68:71]\n"
    "v_exp_f32 v32, v32\n"
    "v_mfma_f32_16x16x32_bf16 a[68:71], a[136:139], v[76:79], a[68:71]\n"
    "buffer_store_dword v222, v244, s[20:23], s78 offen offset:2048 sc1\n"
    "v_mfma_f32_16x16x32_bf16 a[84:87], a[136:139], v[84:87], a[84:87]\n"
    "v_exp_f32 v33, v33\n"
    "v_mfma_f32_16x16x32_bf16 a[84:87], a[140:143], v[88:91], a[84:87]\n"
    "buffer_store_dword v223, v244, s[20:23], s78 offen offset:3072 sc1\n"
    "v_mfma_f32_16x16x32_bf16 a[88:91], a[148:151], v[88:91], a[88:91]\n"
    "s_waitcnt vmcnt(13)\n"
    "ds_add_u32 v247, v246\n"
    "v_mfma_f32_16x16x32_bf16 a[88:91], a[144:147], v[84:87], a[88:91]\n"
    "buffer_store_dwordx2 v[238:239], v208, s[36:39], 0 offen offset:0 sc1\n"
    "v_mfma_f32_16x16x32_bf16 a[92:95], a[152:155], v[84:87], a[92:95]\n"
    "v_exp_f32 v34, v34\n"
    "v_mfma_f32_16x16x32_bf16 a[92:95], a[156:159], v[88:91], a[92:95]\n"
    "v_exp_f32 v35, v35\n"
    "v_mfma_f32_16x16x32_bf16 a[108:111], a[156:159], v[96:99], a[108:111]\n"
    "v_mfma_f32_16x16x32_bf16 a[108:111], a[152:155], v[92:95], a[108:111]\n"
    "v_mfma_f32_16x16x32_bf16 a[104:107], a[144:147], v[92:95], a[104:107]\n"
    "s_sub_u32 s74, s68, s65\n"
    "v_add_u32 v250, 1536, v250\n"
    "v_add_u32 v251, 1536, v251\n"
    "v_mfma_f32_16x16x32_bf16 a[104:107], a[148:151], v[96:99], a[104:107]\n"
    "s_add_u32 s66, s66, 1\n"
    "v_mov_b32 v2, v3\n"
    "v_add_u32 v3, s74, v252\n"
    "v_mfma_f32_16x16x32_bf16 a[100:103], a[140:143], v[96:99], a[100:103]\n"
    "s_waitcnt lgkmcnt(1)\n"
    "s_cmp_lt_u32 s66, 29\n"
    "v_mfma_f32_16x16x32_bf16 a[100:103], a[136:139], v[92:95], a[100:103]\n"
    "s_cbranch_scc1 .LPV\n"
    "s_branch .Ltail\n"
    "s_nop 0\n"
    "s_nop 0\n"
    "s_nop 0\n"
    "s_nop 0\n"
    "s_nop 0\n"
    "s_nop 0\n"
    ".LtailP:\n"
    "s_cbranch_execz .Lw2\n"
    "v_swap_b32 v248, v249\n"
    "s_mov_b32 s64, s70\n"
    "v_add_u32 v250, 768, v250\n"
    "v_add_u32 v251, 768, v251\n"
    ".Ltail:\n"
    "v_mfma_f32_16x16x32_bf16 v[36:39], a[112:115], a[192:195], v[100:103]\n"
    "v_cvt_pk_bf16_f32 v93, v18, v19\n"
    "v_cvt_pk_bf16_f32 v84, v12, v13\n"
    "v_mfma_f32_16x16x32_bf16 v[36:39], a[116:119], a[196:199], v[36:39]\n"
    "ds_read_b128 a[136:139], v2 offset:13312\n"
    "ds_read_b128 a[140:143], v2 offset:15360\n"
    "v_mfma_f32_16x16x32_bf16 v[36:39], a[120:123], a[200:203], v[36:39]\n"
    "s_mul_i32 s75, s84, 24576\n"
    "v_mfma_f32_16x16x32_bf16 v[36:39], a[124:127], a[204:207], v[36:39]\n"
    "v_cvt_pk_bf16_f32 v77, v10, v11\n"
    "v_subrev_u32 v249, 24576, v249\n"
    "v_mfma_f32_16x16x32_bf16 v[36:39], a[128:131], a[208:211], v[36:39]\n"
    "ds_read_b128 a[152:155], v2 offset:21504\n"
    "v_mfma_f32_16x16x32_bf16 v[40:43], a[132:135], a[236:239], v[100:103]\n"
    "v_cvt_pk_bf16_f32 v69, v6, v7\n"
    "v_lshlrev_b32 v240, 16, v1\n"
    "v_mfma_f32_16x16x32_bf16 v[36:39], a[132:135], a[212:215], v[36:39]\n"
    "ds_read_b128 a[156:159], v2 offset:23552\n"
    "ds_read_b128 v[104:107], v250 offset:144\n"
    "v_mfma_f32_16x16x32_bf16 v[48:51], a[132:135], a[188:191], v[100:103]\n"
    "s_add_u32 s76, s75, 6144\n"
    "ds_read_b128 a[144:147], v2 offset:17408\n"
    "v_and_b32 v3, 0xf0, v1\n"
    "v_mfma_f32_16x16x32_bf16 v[48:51], a[128:131], a[184:187], v[48:51]\n"
    "ds_read_b128 a[148:151], v2 offset:19456\n"
    "s_add_u32 s77, s76, 6144\n"
    "v_mfma_f32_16x16x32_bf16 v[40:43], a[128:131], a[232:235], v[40:43]\n"
    "v_exp_f32 v37, v37\n"
    "v_mfma_f32_16x16x32_bf16 v[48:51], a[124:127], a[180:183], v[48:51]\n"
    "v_exp_f32 v38, v38\n"
    "v_mfma_f32_16x16x32_bf16 v[48:51], a[120:123], a[176:179], v[48:51]\n"
    "s_add_u32 s78, s77, 6144\n"
    "v_cvt_pk_bf16_f32 v76, v8, v9\n"
    "v_cvt_pk_bf16_f32 v68, v4, v5\n"
    "v_mfma_f32_16x16x32_bf16 v[40:43], a[124:127], a[228:231], v[40:43]\n"
    "v_cvt_pk_bf16_f32 v85, v14, v15\n"
    "v_cvt_pk_bf16_f32 v71, v22, v23\n"
    "v_mfma_f32_16x16x32_bf16 v[48:51], a[116:119], a[172:175], v[48:51]\n"
    "v_and_b32 v2, 0x70, v1\n"
    "v_cvt_pk_bf16_f32 v94, v32, v33\n"
    "v_mfma_f32_16x16x32_bf16 v[44:47], a[112:115], a[240:243], v[100:103]\n"
    "v_cvt_pk_bf16_f32 v95, v34, v35\n"
    "v_cvt_pk_bf16_f32 v86, v28, v29\n"
    "v_mfma_f32_16x16x32_bf16 v[48:51], a[112:115], a[168:171], v[48:51]\n"
    "v_exp_f32 v39, v39\n"
    "v_mfma_f32_16x16x32_bf16 v[44:47], a[116:119], a[244:247], v[44:47]\n"
    "s_waitcnt lgkmcnt(2)\n"
    "v_cvt_pk_bf16_f32 v70, v20, v21\n"
    "v_cvt_pk_bf16_f32 v92, v16, v17\n"
    "v_mfma_f32_16x16x32_bf16 v[60:63], a[136:139], a[240:243], v[104:107]\n"
    "v_exp_f32 v36, v36\n"
    "v_mfma_f32_16x16x32_bf16 v[56:59], a[156:159], a[236:239], v[104:107]\n"
    "s_add_u32 s72, s65, 122880\n"
    "ds_read_b32 v245, v247\n"
    "v_mfma_f32_16x16x32_bf16 v[40:43], a[120:123], a[224:227], v[40:43]\n"
    "v_exp_f32 v51, v51\n"
    "v_mfma_f32_16x16x32_bf16 v[60:63], a[140:143], a[244:247], v[60:63]\n"
    "v_exp_f32 v48, v48\n"
    "v_mfma_f32_16x16x32_bf16 v[44:47], a[120:123], a[248:251], v[44:47]\n"
    "v_exp_f32 v50, v50\n"
    "v_mfma_f32_16x16x32_bf16 v[40:43], a[116:119], a[220:223], v[40:43]\n"
    "s_waitcnt vmcnt(0)\n"
    "v_exp_f32 v49, v49\n"
    "v_mfma_f32_16x16x32_bf16 v[44:47], a[124:127], a[252:255], v[44:47]\n"
    "ds_read_b128 v[232:235], v251 offset:0\n"
    "ds_read_b128 v[100:103], v250 offset:768\n"
    "v_mfma_f32_16x16x32_bf16 v[52:55], a[136:139], a[192:195], v[104:107]\n"
    "v_cvt_pk_bf16_f32 v79, v26, v27\n"
    "v_lshrrev_b32 v224, 4, v1\n"
    "v_mfma_f32_16x16x32_bf16 v[64:67], a[156:159], a[188:191], v[104:107]\n"
    "v_lshl_add_u32 v3, v3, 3, s72\n"
    "v_cmp_eq_u64 s[54:55], v[236:237], v[238:239]\n"
    "v_mfma_f32_16x16x32_bf16 v[52:55], a[140:143], a[196:199], v[52:55]\n"
    "s_waitcnt lgkmcnt(2)\n"
    "v_cvt_pk_bf16_f32 v78, v24, v25\n"
    "v_cmp_le_u32 s[56:57], s59, v245\n"
    "v_mfma_f32_16x16x32_bf16 v[40:43], a[112:115], a[216:219], v[40:43]\n"
    "s_and_b64 s[54:55], s[54:55], s[56:57]\n"
    "v_and_b32 v224, 48, v224\n"
    "s_cmp_eq_u64 s[54:55], -1\n"
    "v_mfma_f32_16x16x32_bf16 v[60:63], a[144:147], a[248:251], v[60:63]\n"
    "s_cbranch_scc0 .Lpt0\n"
    ".Lpt0r:\n"
    "v_mfma_f32_16x16x32_bf16 v[44:47], a[128:131], a[160:163], v[44:47]\n"
    "s_add_u32 s81, s81, 128\n"
    "ds_read_b128 a[116:119], v252 offset:2048\n"
    "s_add_u32 m0, s65, 24576\n"
    "v_mfma_f32_16x16x32_bf16 v[52:55], a[144:147], a[200:203], v[52:55]\n"
    "s_add_u32 s83, s67, .Lend-.Lend2+(0)\n"
    "buffer_load_dwordx4 v1, s[16:19], s83 offen offset:0 sc1 lds\n"
    "v_mfma_f32_16x16x32_bf16 v[60:63], a[148:151], a[252:255], v[60:63]\n"
    "ds_read_b128 a[120:123], v252 offset:4096\n"
    "ds_read_b128 a[128:131], v252 offset:8192\n"
    "v_mfma_f32_16x16x32_bf16 v[56:59], a[152:155], a[232:235], v[56:59]\n"
    "buffer_load_dwordx4 v1, s[16:19], s83 offen offset:1024 sc1 lds\n"
    "v_mfma_f32_16x16x32_bf16 v[60:63], a[152:155], a[160:163], v[60:63]\n"
    "ds_read_b128 v[104:107], v250 offset:784\n"
    "ds_read_b128 a[112:115], v252 offset:0\n"
    "v_mfma_f32_16x16x32_bf16 v[44:47], a[132:135], a[164:167], v[44:47]\n"
    "buffer_load_dwordx4 v1, s[16:19], s83 offen offset:2048 sc1 lds\n"
    "v_mfma_f32_16x16x32_bf16 v[52:55], a[148:151], a[204:207], v[52:55]\n"
    "ds_read_b128 a[132:135], v252 offset:10240\n"
    "v_mfma_f32_16x16x32_bf16 v[52:55], a[152:155], a[208:211], v[52:55]\n"
    "buffer_load_dwordx4 v1, s[16:19], s83 offen offset:3072 sc1 lds\n"
    "v_mfma_f32_16x16x32_bf16 v[60:63], a[156:159], a[164:167], v[60:63]\n"
    "s_add_u32 m0, s65, 28672\n"
    "v_exp_f32_e64 v40, v40\n"
    "v_mfma_f32_16x16x32_bf16 v[56:59], a[148:151], a[228:231], v[56:59]\n"
    "s_add_u32 s83, s67, 4096\n"
    "buffer_load_dwordx4 v1, s[16:19], s83 offen offset:0 sc1 lds\n"
    "v_mfma_f32_16x16x32_bf16 v[56:59], a[144:147], a[224:227], v[56:59]\n"
    "ds_read_b128 a[124:127], v252 offset:6144\n"
    "v_cvt_pk_bf16_f32 v72, v36, v37\n"
    "v_mfma_f32_16x16x32_bf16 v[56:59], a[140:143], a[220:223], v[56:59]\n"
    "buffer_load_dwordx4 v1, s[16:19], s83 offen offset:1024 sc1 lds\n"
    "v_mfma_f32_16x16x32_bf16 v[52:55], a[156:159], a[212:215], v[52:55]\n"
    "v_exp_f32 v45, v45\n"
    "v_mfma_f32_16x16x32_bf16 v[56:59], a[136:139], a[216:219], v[56:59]\n"
    "v_exp_f32 v62, v62\n"
    "v_mfma_f32_16x16x32_bf16 v[64:67], a[152:155], a[184:187], v[64:67]\n"
    "s_waitcnt lgkmcnt(2)\n"
    "v_xor_b32 v224, v224, v2\n"
    "v_cvt_pk_bf16_f32 v87, v30, v31\n"
    "v_mfma_f32_16x16x32_bf16 v[12:15], a[112:115], a[240:243], v[100:103]\n"
    "s_waitcnt lgkmcnt(1)\n"
    "ds_read_b128 a[156:159], v252 offset:11264\n"
    "ds_read_b128 a[152:155], v252 offset:9216\n"
    "v_mfma_f32_16x16x32_bf16 v[16:19], a[132:135], a[188:191], v[100:103]\n"
    ".Lwp0:\n"
    "s_cbranch_execz .Lw3\n"
    "s_add_u32 m0, s65, 73728\n"
    "v_exp_f32 v56, v56\n"
    "v_mfma_f32_16x16x32_bf16 v[64:67], a[148:151], a[180:183], v[64:67]\n"
    "v_exp_f32 v58, v58\n"
    "v_mfma_f32_16x16x32_bf16 v[4:7], a[112:115], a[192:195], v[100:103]\n"
    "s_add_u32 s83, s67, 24576\n"
    "v_exp_f32 v61, v61\n"
    "v_mfma_f32_16x16x32_bf16 v[16:19], a[128:131], a[184:187], v[16:19]\n"
    "v_exp_f32 v53, v53\n"
    "v_mfma_f32_16x16x32_bf16 v[64:67], a[144:147], a[176:179], v[64:67]\n"
    "v_exp_f32 v54, v54\n"
    "v_mfma_f32_16x16x32_bf16 v[64:67], a[140:143], a[172:175], v[64:67]\n"
    "v_exp_f32 v52, v52\n"
    "v_mfma_f32_16x16x32_bf16 v[8:11], a[132:135], a[236:239], v[100:103]\n"
    "s_waitcnt lgkmcnt(2)\n"
    "v_exp_f32 v63, v63\n"
    "v_mfma_f32_16x16x32_bf16 v[16:19], a[124:127], a[180:183], v[16:19]\n"
    "v_exp_f32 v41, v41\n"
    "v_mfma_f32_16x16x32_bf16 v[64:67], a[136:139], a[168:171], v[64:67]\n"
    "v_exp_f32 v43, v43\n"
    "v_mfma_f32_16x16x32_bf16 v[8:11], a[128:131], a[232:235], v[8:11]\n"
    "ds_read_b128 a[144:147], v252 offset:5120\n"
    "ds_read_b128 a[148:151], v252 offset:7168\n"
    "v_mfma_f32_16x16x32_bf16 v[12:15], a[116:119], a[244:247], v[12:15]\n"
    "v_exp_f32 v44, v44\n"
    "v_mfma_f32_16x16x32_bf16 v[4:7], a[116:119], a[196:199], v[4:7]\n"
    "v_exp_f32 v55, v55\n"
    "v_mfma_f32_16x16x32_bf16 v[16:19], a[120:123], a[176:179], v[16:19]\n"
    "ds_read_b128 a[136:139], v252 offset:1024\n"
    "v_cvt_pk_bf16_f32 v80, v40, v41\n"
    "v_mfma_f32_16x16x32_bf16 v[12:15], a[120:123], a[248:251], v[12:15]\n"
    "buffer_load_dwordx4 v1, s[16:19], s83 offen offset:0 sc1 lds\n"
    "v_mfma_f32_16x16x32_bf16 v[4:7], a[120:123], a[200:203], v[4:7]\n"
    "v_exp_f32 v42, v42\n"
    "v_mfma_f32_16x16x32_bf16 v[16:19], a[116:119], a[172:175], v[16:19]\n"
    "v_exp_f32 v66, v66\n"
    "v_mfma_f32_16x16x32_bf16 v[4:7], a[124:127], a[204:207], v[4:7]\n"
    "v_exp_f32 v59, v59\n"
    "v_mfma_f32_16x16x32_bf16 v[4:7], a[128:131], a[208:211], v[4:7]\n"
    "buffer_load_dwordx4 v1, s[16:19], s83 offen offset:1024 sc1 lds\n"
    "v_mfma_f32_16x16x32_bf16 v[4:7], a[132:135], a[212:215], v[4:7]\n"
    "v_exp_f32 v57, v57\n"
    "v_mfma_f32_16x16x32_bf16 v[12:15], a[124:127], a[252:255], v[12:15]\n"
    "ds_read_b128 v[100:103], v250 offset:896\n"
    "ds_read_b128 a[140:143], v252 offset:3072\n"
    "v_mfma_f32_16x16x32_bf16 v[12:15], a[128:131], a[160:163], v[12:15]\n"
    "v_exp_f32 v65, v65\n"
    "v_mfma_f32_16x16x32_bf16 v[8:11], a[124:127], a[228:231], v[8:11]\n"
    "v_exp_f32 v47, v47\n"
    "v_mfma_f32_16x16x32_bf16 v[12:15], a[132:135], a[164:167], v[12:15]\n"
    "s_waitcnt lgkmcnt(6)\n"
    "buffer_load_dwordx4 v1, s[16:19], s83 offen offset:2048 sc1 lds\n"
    "v_mfma_f32_16x16x32_bf16 v[32:35], a[156:159], a[188:191], v[104:107]\n"
    "v_exp_f32 v60, v60\n"
    "v_mfma_f32_16x16x32_bf16 v[24:27], a[156:159], a[236:239], v[104:107]\n"
    "s_waitcnt lgkmcnt(2)\n"
    "v_cvt_pk_bf16_f32 v81, v42, v43\n"
    "v_mfma_f32_16x16x32_bf16 v[28:31], a[136:139], a[240:243], v[104:107]\n"
    "v_cvt_pk_bf16_f32 v96, v48, v49\n"
    "v_cvt_pk_bf16_f32 v90, v60, v61\n"
    "v_mfma_f32_16x16x32_bf16 v[8:11], a[120:123], a[224:227], v[8:11]\n"
    "v_exp_f32 v7, v7\n"
    "v_mfma_f32_16x16x32_bf16 v[24:27], a[152:155], a[232:235], v[24:27]\n"
    "v_exp_f32 v67, v67\n"
    "v_mfma_f32_16x16x32_bf16 v[8:11], a[116:119], a[220:223], v[8:11]\n"
    "ds_read_b128 a[124:127], v252 offset:18432\n"
    "v_mfma_f32_16x16x32_bf16 v[16:19], a[112:115], a[168:171], v[16:19]\n"
    "buffer_load_dwordx4 v1, s[16:19], s83 offen offset:3072 sc1 lds\n"
    "v_mfma_f32_16x16x32_bf16 v[32:35], a[152:155], a[184:187], v[32:35]\n"
    "s_waitcnt lgkmcnt(1)\n"
    "ds_read_b128 a[132:135], v252 offset:22528\n"
    "v_cvt_pk_bf16_f32 v75, v54, v55\n"
    "v_mfma_f32_16x16x32_bf16 v[24:27], a[148:151], a[228:231], v[24:27]\n"
    "s_add_u32 m0, s65, 77824\n"
    "v_cvt_pk_bf16_f32 v74, v52, v53\n"
    "v_cvt_pk_bf16_f32 v97, v50, v51\n"
    "v_mfma_f32_16x16x32_bf16 v[20:23], a[136:139], a[192:195], v[104:107]\n"
    "s_add_u32 s83, s67, 28672\n"
    "v_exp_f32 v6, v6\n"
    "v_mfma_f32_16x16x32_bf16 v[20:23], a[140:143], a[196:199], v[20:23]\n"
    "v_cvt_pk_bf16_f32 v99, v66, v67\n"
    "v_exp_f32 v14, v14\n"
    "v_mfma_f32_16x16x32_bf16 v[24:27], a[144:147], a[224:227], v[24:27]\n"
    "ds_read_b128 a[128:131], v252 offset:20480\n"
    "ds_read_b128 v[104:107], v250 offset:912\n"
    "v_mfma_f32_16x16x32_bf16 v[8:11], a[112:115], a[216:219], v[8:11]\n"
    "v_exp_f32 v46, v46\n"
    "v_mfma_f32_16x16x32_bf16 v[28:31], a[140:143], a[244:247], v[28:31]\n"
    "v_exp_f32 v13, v13\n"
    "v_mfma_f32_16x16x32_bf16 v[32:35], a[148:151], a[180:183], v[32:35]\n"
    "s_waitcnt lgkmcnt(2)\n"
    "v_exp_f32 v64, v64\n"
    "v_mfma_f32_16x16x32_bf16 v[40:43], a[132:135], a[236:239], v[100:103]\n"
    "ds_read_b128 a[120:123], v252 offset:16384\n"
    "v_cvt_pk_bf16_f32 v91, v62, v63\n"
    "v_mfma_f32_16x16x32_bf16 v[32:35], a[144:147], a[176:179], v[32:35]\n"
    "ds_read_b128 a[112:115], v252 offset:12288\n"
    "v_cvt_pk_bf16_f32 v73, v38, v39\n"
    "v_mfma_f32_16x16x32_bf16 v[28:31], a[144:147], a[248:251], v[28:31]\n"
    "v_exp_f32 v11, v11\n"
    "v_mfma_f32_16x16x32_bf16 v[32:35], a[140:143], a[172:175], v[32:35]\n"
    "v_cvt_pk_bf16_f32 v88, v44, v45\n"
    "v_cvt_pk_bf16_f32 v89, v46, v47\n"
    "v_mfma_f32_16x16x32_bf16 v[32:35], a[136:139], a[168:171], v[32:35]\n"
    "buffer_load_dwordx4 v1, s[16:19], s83 offen offset:0 sc1 lds\n"
    "v_mfma_f32_16x16x32_bf16 v[24:27], a[140:143], a[220:223], v[24:27]\n"
    "v_exp_f32 v9, v9\n"
    "v_mfma_f32_16x16x32_bf16 v[20:23], a[144:147], a[200:203], v[20:23]\n"
    "buffer_load_dwordx4 v1, s[16:19], s83 offen offset:1024 sc1 lds\n"
    "v_mfma_f32_16x16x32_bf16 v[20:23], a[148:151], a[204:207], v[20:23]\n"
    "v_exp_f32 v15, v15\n"
    "v_mfma_f32_16x16x32_bf16 v[20:23], a[152:155], a[208:211], v[20:23]\n"
    "v_exp_f32 v34, v34\n"
    "v_mfma_f32_16x16x32_bf16 v[20:23], a[156:159], a[212:215], v[20:23]\n"
    "v_exp_f32 v10, v10\n"
    "v_mfma_f32_16x16x32_bf16 v[28:31], a[148:151], a[252:255], v[28:31]\n"
    "s_add_u32 s59, s59, 4\n"
    "ds_read_b128 a[140:143], v252 offset:15360\n"
    "ds_read_b128 a[116:119], v252 offset:14336\n"
    "v_mfma_f32_16x16x32_bf16 v[28:31], a[152:155], a[160:163], v[28:31]\n"
    "s_waitcnt lgkmcnt(5)\n"
    "v_exp_f32 v4, v4\n"
    "v_mfma_f32_16x16x32_bf16 v[40:43], a[128:131], a[232:235], v[40:43]\n"
    "v_exp_f32 v12, v12\n"
    "v_mfma_f32_16x16x32_bf16 v[24:27], a[136:139], a[216:219], v[24:27]\n"
    "ds_read_b128 a[136:139], v252 offset:13312\n"
    "v_add_u32 v3, v3, v224\n"
    "v_mfma_f32_16x16x32_bf16 v[48:51], a[132:135], a[188:191], v[100:103]\n"
    "v_exp_f32 v23, v23\n"
    "v_smfmac_f32_16x16x64_bf16 v[116:119], v[232:235], v[84:91], v253\n"
    "ds_read_b128 a[152:155], v252 offset:21504\n"
    "v_mfma_f32_16x16x32_bf16 v[28:31], a[156:159], a[164:167], v[28:31]\n"
    "s_waitcnt lgkmcnt(5)\n"
    "v_exp_f32 v5, v5\n"
    "v_mfma_f32_16x16x32_bf16 v[40:43], a[124:127], a[228:231], v[40:43]\n"
    "s_mov_b32 s74, 0x47000000\n"
    "v_exp_f32 v20, v20\n"
    "v_mfma_f32_16x16x32_bf16 v[40:43], a[120:123], a[224:227], v[40:43]\n"
    "s_waitcnt lgkmcnt(4)\n"
    "ds_read_b128 a[156:159], v252 offset:23552\n"
    "v_cvt_pk_bf16_f32 v204, v4, v5\n"
    "v_mfma_f32_16x16x32_bf16 v[36:39], a[112:115], a[192:195], v[100:103]\n"
    "s_waitcnt lgkmcnt(3)\n"
    "v_exp_f32 v32, v32\n"
    "v_mfma_f32_16x16x32_bf16 v[36:39], a[116:119], a[196:199], v[36:39]\n"
    "v_cvt_pk_bf16_f32 v220, v12, v13\n"
    "v_cvt_pk_bf16_f32 v82, v56, v57\n"
    "v_mfma_f32_16x16x32_bf16 v[40:43], a[116:119], a[220:223], v[40:43]\n"
    "v_exp_f32 v16, v16\n"
    "v_mfma_f32_16x16x32_bf16 v[40:43], a[112:115], a[216:219], v[40:43]\n"
    "s_waitcnt lgkmcnt(2)\n"
    "v_exp_f32 v28, v28\n"
    "v_mfma_f32_16x16x32_bf16 v[52:55], a[136:139], a[192:195], v[104:107]\n"
    "v_exp_f32 v33, v33\n"
    "v_mfma_f32_16x16x32_bf16 v[48:51], a[128:131], a[184:187], v[48:51]\n"
    "v_exp_f32 v8, v8\n"
    "v_mfma_f32_16x16x32_bf16 v[44:47], a[112:115], a[240:243], v[100:103]\n"
    "v_exp_f32 v17, v17\n"
    "v_mfma_f32_16x16x32_bf16 v[60:63], a[136:139], a[240:243], v[104:107]\n"
    "v_cvt_pk_bf16_f32 v98, v64, v65\n"
    "v_cvt_pk_bf16_f32 v83, v58, v59\n"
    "v_mfma_f32_16x16x32_bf16 v[48:51], a[124:127], a[180:183], v[48:51]\n"
    "v_exp_f32 v35, v35\n"
    "v_mfma_f32_16x16x32_bf16 v[44:47], a[116:119], a[244:247], v[44:47]\n"
    "v_exp_f32 v41, v41\n"
    "v_mfma_f32_16x16x32_bf16 v[44:47], a[120:123], a[248:251], v[44:47]\n"
    "v_exp_f32 v29, v29\n"
    "v_mfma_f32_16x16x32_bf16 v[60:63], a[140:143], a[244:247], v[60:63]\n"
    "v_exp_f32 v31, v31\n"
    "v_mfma_f32_16x16x32_bf16 v[36:39], a[120:123], a[200:203], v[36:39]\n"
    "v_lshrrev_b32 v224, 3, v1\n"
    "v_cvt_pk_bf16_f32 v205, v6, v7\n"
    "v_mfma_f32_16x16x32_bf16 v[48:51], a[120:123], a[176:179], v[48:51]\n"
    "s_waitcnt vmcnt(6)\n"
    "ds_add_u32 v247, v246\n"
    "v_mfma_f32_16x16x32_bf16 v[48:51], a[116:119], a[172:175], v[48:51]\n"
    "s_waitcnt lgkmcnt(1)\n"
    "ds_read_b128 a[144:147], v252 offset:17408\n"
    "ds_read_b128 a[148:151], v252 offset:19456\n"
    "v_mfma_f32_16x16x32_bf16 v[56:59], a[156:159], a[236:239], v[104:107]\n"
    "v_exp_f32 v25, v25\n"
    "v_cvt_pk_bf16_f32 v228, v16, v17\n"
    "v_mfma_f32_16x16x32_bf16 v[52:55], a[140:143], a[196:199], v[52:55]\n"
    "v_exp_f32 v18, v18\n"
    "v_mfma_f32_16x16x32_bf16 v[44:47], a[124:127], a[252:255], v[44:47]\n"
    "v_exp_f32 v19, v19\n"
    "v_mfma_f32_16x16x32_bf16 v[64:67], a[156:159], a[188:191], v[104:107]\n"
    "v_exp_f32 v43, v43\n"
    "v_mfma_f32_16x16x32_bf16 v[48:51], a[112:115], a[168:171], v[48:51]\n"
    "v_exp_f32 v27, v27\n"
    "v_mfma_f32_16x16x32_bf16 v[36:39], a[124:127], a[204:207], v[36:39]\n"
    "v_exp_f32 v30, v30\n"
    "v_mfma_f32_16x16x32_bf16 v[64:67], a[152:155], a[184:187], v[64:67]\n"
    "v_exp_f32 v22, v22\n"
    "v_mfma_f32_16x16x32_bf16 v[36:39], a[128:131], a[208:211], v[36:39]\n"
    "v_exp_f32 v42, v42\n"
    "v_mfma_f32_16x16x32_bf16 v[44:47], a[128:131], a[160:163], v[44:47]\n"
    "s_waitcnt lgkmcnt(1)\n"
    "v_exp_f32 v26, v26\n"
    "v_mfma_f32_16x16x32_bf16 v[60:63], a[144:147], a[248:251], v[60:63]\n"
    "s_waitcnt lgkmcnt(0)\n"
    "v_exp_f32 v40, v40\n"
    "v_mfma_f32_16x16x32_bf16 v[64:67], a[148:151], a[180:183], v[64:67]\n"
    "ds_read_b128 v[100:103], v250 offset:1536\n"
    "ds_read_b32 v245, v247\n"
    "v_mfma_f32_16x16x32_bf16 v[56:59], a[152:155], a[232:235], v[56:59]\n"
    "v_cvt_pk_bf16_f32 v216, v40, v41\n"
    "v_cvt_pk_bf16_f32 v231, v34, v35\n"
    "v_mfma_f32_16x16x32_bf16 v[64:67], a[144:147], a[176:179], v[64:67]\n"
    "v_exp_f32 v21, v21\n"
    "v_smfmac_f32_16x16x64_bf16 v[120:123], v[232:235], v[92:99], v253\n"
    "v_cvt_pk_bf16_f32 v221, v14, v15\n"
    "v_mfma_f32_16x16x32_bf16 v[44:47], a[132:135], a[164:167], v[44:47]\n"
    "v_cvt_pk_bf16_f32 v207, v22, v23\n"
    "v_cvt_pk_bf16_f32 v229, v18, v19\n"
    "v_mfma_f32_16x16x32_bf16 v[64:67], a[140:143], a[172:175], v[64:67]\n"
    "v_cvt_pk_bf16_f32 v206, v20, v21\n"
    "v_cvt_pk_bf16_f32 v212, v8, v9\n"
    "v_mfma_f32_16x16x32_bf16 v[52:55], a[144:147], a[200:203], v[52:55]\n"
    "v_exp_f32 v50, v50\n"
    "v_mfma_f32_16x16x32_bf16 v[60:63], a[148:151], a[252:255], v[60:63]\n"
    "v_exp_f32 v24, v24\n"
    "v_mfma_f32_16x16x32_bf16 v[36:39], a[132:135], a[212:215], v[36:39]\n"
    "s_waitcnt lgkmcnt(0)\n"
    "v_cmp_le_u32 s[56:57], s59, v245\n"
    "v_cvt_pk_bf16_f32 v213, v10, v11\n"
    "v_smfmac_f32_16x16x64_bf16 v[108:111], v[232:235], v[68:75], v253\n"
    "s_cmp_eq_u64 s[56:57], -1\n"
    "v_cvt_pk_bf16_f32 v214, v24, v25\n"
    "v_smfmac_f32_16x16x64_bf16 v[112:115], v[232:235], v[76:83], v253\n"
    "s_cbranch_scc0 .Lpt1\n"
    ".Lpt1r:\n"
    "v_mfma_f32_16x16x32_bf16 v[52:55], a[148:151], a[204:207], v[52:55]\n"
    "s_add_u32 m0, s65, 49152\n"
    "ds_read_b128 a[128:131], v252 offset:32768\n"
    "ds_read_b128 a[124:127], v252 offset:30720\n"
    "v_mfma_f32_16x16x32_bf16 v[60:63], a[152:155], a[160:163], v[60:63]\n"
    "ds_read_b128 a[112:115], v252 offset:24576\n"
    "v_cvt_pk_bf16_f32 v222, v28, v29\n"
    "v_mfma_f32_16x16x32_bf16 v[60:63], a[156:159], a[164:167], v[60:63]\n"
    "s_add_u32 s83, s67, 73728\n"
    "ds_read_b128 a[132:135], v252 offset:34816\n"
    "v_cvt_pk_bf16_f32 v215, v26, v27\n"
    "v_mfma_f32_16x16x32_bf16 v[56:59], a[148:151], a[228:231], v[56:59]\n"
    "buffer_load_dwordx4 v1, s[16:19], s83 offen offset:0 sc1 lds\n"
    "v_mfma_f32_16x16x32_bf16 v[56:59], a[144:147], a[224:227], v[56:59]\n"
    "ds_read_b128 a[116:119], v252 offset:26624\n"
    "v_cvt_pk_bf16_f32 v223, v30, v31\n"
    "v_mfma_f32_16x16x32_bf16 v[56:59], a[140:143], a[220:223], v[56:59]\n"
    "v_exp_f32 v38, v38\n"
    "v_exp_f32 v47, v47\n"
    "v_mfma_f32_16x16x32_bf16 v[52:55], a[152:155], a[208:211], v[52:55]\n"
    "ds_read_b128 a[120:123], v252 offset:28672\n"
    "ds_read_b128 v[104:107], v250 offset:1552\n"
    "v_mfma_f32_16x16x32_bf16 v[64:67], a[136:139], a[168:171], v[64:67]\n"
    "buffer_load_dwordx4 v1, s[16:19], s83 offen offset:1024 sc1 lds\n"
    "v_mfma_f32_16x16x32_bf16 v[56:59], a[136:139], a[216:219], v[56:59]\n"
    "v_exp_f32 v37, v37\n"
    "v_mfma_f32_16x16x32_bf16 v[52:55], a[156:159], a[212:215], v[52:55]\n"
    "s_waitcnt lgkmcnt(4)\n"
    "buffer_load_dwordx4 v1, s[16:19], s83 offen offset:2048 sc1 lds\n"
    "v_mfma_f32_16x16x32_bf16 v[4:7], a[112:115], a[192:195], v[100:103]\n"
    "ds_read_b128 a[140:143], v252 offset:27648\n"
    "v_cvt_pk_bf16_f32 v230, v32, v33\n"
    "v_mfma_f32_16x16x32_bf16 v[20:23], a[112:115], a[240:243], v[100:103]\n"
    "s_waitcnt lgkmcnt(3)\n"
    "buffer_load_dwordx4 v1, s[16:19], s83 offen offset:3072 sc1 lds\n"
    "v_mfma_f32_16x16x32_bf16 v[20:23], a[116:119], a[244:247], v[20:23]\n"
    "s_add_u32 m0, s65, 53248\n"
    "v_exp_f32 v58, v58\n"
    "v_mfma_f32_16x16x32_bf16 v[28:31], a[132:135], a[188:191], v[100:103]\n"
    "s_add_u32 s83, s67, 77824\n"
    "v_exp_f32 v44, v44\n"
    "v_mfma_f32_16x16x32_bf16 v[28:31], a[128:131], a[184:187], v[28:31]\n"
    "ds_read_b128 a[148:151], v252 offset:31744\n"
    "ds_read_b128 a[152:155], v252 offset:33792\n"
    "v_mfma_f32_16x16x32_bf16 v[12:15], a[132:135], a[236:239], v[100:103]\n"
    "ds_read_b128 a[156:159], v252 offset:35840\n"
    "v_and_b32 v224, 0x70, v224\n"
    "v_mfma_f32_16x16x32_bf16 v[12:15], a[128:131], a[232:235], v[12:15]\n"
    "buffer_load_dwordx4 v1, s[16:19], s83 offen offset:0 sc1 lds\n"
    "v_mfma_f32_16x16x32_bf16 v[28:31], a[124:127], a[180:183], v[28:31]\n"
    "v_exp_f32 v55, v55\n"
    "v_mfma_f32_16x16x32_bf16 v[4:7], a[116:119], a[196:199], v[4:7]\n"
    "s_waitcnt lgkmcnt(5)\n"
    "buffer_load_dwordx4 v1, s[16:19], s83 offen offset:1024 sc1 lds\n"
    "v_mfma_f32_16x16x32_bf16 v[28:31], a[120:123], a[176:179], v[28:31]\n"
    "v_exp_f32 v65, v65\n"
    "v_mfma_f32_16x16x32_bf16 v[28:31], a[116:119], a[172:175], v[28:31]\n"
    "v_exp_f32 v61, v61\n"
    "v_mfma_f32_16x16x32_bf16 v[12:15], a[124:127], a[228:231], v[12:15]\n"
    "v_exp_f32 v53, v53\n"
    "v_mfma_f32_16x16x32_bf16 v[4:7], a[120:123], a[200:203], v[4:7]\n"
    "buffer_load_dwordx2 v[236:237], v241, s[36:39], 0 offen offset:0 sc1\n"
    "v_mfma_f32_16x16x32_bf16 v[20:23], a[120:123], a[248:251], v[20:23]\n"
    "s_waitcnt lgkmcnt(0)\n"
    "v_exp_f32 v67, v67\n"
    "v_mfma_f32_16x16x32_bf16 v[16:19], a[156:159], a[236:239], v[104:107]\n"
    "ds_read_b128 a[136:139], v252 offset:25600\n"
    "ds_read_b128 v[100:103], v250 offset:1664\n"
    "v_mfma_f32_16x16x32_bf16 v[4:7], a[124:127], a[204:207], v[4:7]\n"
    "v_exp_f32 v36, v36\n"
    "v_mfma_f32_16x16x32_bf16 v[28:31], a[112:115], a[168:171], v[28:31]\n"
    "s_waitcnt vmcnt(7)\n"
    "buffer_store_dwordx2 v[238:239], v240, s[36:39], s82 offen offset:0 sc1\n"
    "v_exp_f32 v46, v46\n"
    "v_mfma_f32_16x16x32_bf16 v[32:35], a[156:159], a[188:191], v[104:107]\n"
    "v_exp_f32 v60, v60\n"
    "v_mfma_f32_16x16x32_bf16 v[12:15], a[120:123], a[224:227], v[12:15]\n"
    "v_exp_f32 v64, v64\n"
    "v_mfma_f32_16x16x32_bf16 v[4:7], a[128:131], a[208:211], v[4:7]\n"
    "v_exp_f32 v66, v66\n"
    "v_mfma_f32_16x16x32_bf16 v[12:15], a[116:119], a[220:223], v[12:15]\n"
    "v_exp_f32 v49, v49\n"
    "v_mfma_f32_16x16x32_bf16 v[20:23], a[124:127], a[252:255], v[20:23]\n"
    "ds_read_b128 a[116:119], v252 offset:38912\n"
    "v_cvt_pk_bf16_f32 v235, v66, v67\n"
    "v_mfma_f32_16x16x32_bf16 v[32:35], a[152:155], a[184:187], v[32:35]\n"
    "ds_read_b128 a[120:123], v252 offset:40960\n"
    "v_cvt_pk_bf16_f32 v208, v36, v37\n"
    "v_mfma_f32_16x16x32_bf16 v[12:15], a[112:115], a[216:219], v[12:15]\n"
    "s_mov_b32 s73, 0x7f800000\n"
    "ds_read_b128 a[144:147], v252 offset:29696\n"
    "ds_read_b128 a[112:115], v252 offset:36864\n"
    "v_mfma_f32_16x16x32_bf16 v[4:7], a[132:135], a[212:215], v[4:7]\n"
    "s_waitcnt lgkmcnt(5)\n"
    "ds_add_u32 v247, v246\n"
    "v_mfma_f32_16x16x32_bf16 v[24:27], a[136:139], a[240:243], v[104:107]\n"
    "v_exp_f32 v39, v39\n"
    "v_mfma_f32_16x16x32_bf16 v[16:19], a[152:155], a[232:235], v[16:19]\n"
    "v_cvt_pk_bf16_f32 v226, v60, v61\n"
    "v_cvt_pk_bf16_f32 v234, v64, v65\n"
    "v_mfma_f32_16x16x32_bf16 v[8:11], a[136:139], a[192:195], v[104:107]\n"
    "v_exp_f32 v48, v48\n"
    "v_mfma_f32_16x16x32_bf16 v[24:27], a[140:143], a[244:247], v[24:27]\n"
    "v_exp_f32 v56, v56\n"
    "v_mfma_f32_16x16x32_bf16 v[20:23], a[128:131], a[160:163], v[20:23]\n"
    "ds_read_b128 v[240:243], v251 offset:768\n"
    "v_cvt_pk_bf16_f32 v209, v38, v39\n"
    "v_mfma_f32_16x16x32_bf16 v[16:19], a[148:151], a[228:231], v[16:19]\n"
    "v_exp_f32 v62, v62\n"
    "v_mfma_f32_16x16x32_bf16 v[20:23], a[132:135], a[164:167], v[20:23]\n"
    "v_exp_f32 v63, v63\n"
    "v_mfma_f32_16x16x32_bf16 v[8:11], a[140:143], a[196:199], v[8:11]\n"
    "s_waitcnt lgkmcnt(3)\n"
    "ds_read_b128 a[128:131], v252 offset:45056\n"
    "ds_read_b128 a[132:135], v252 offset:47104\n"
    "v_mfma_f32_16x16x32_bf16 v[24:27], a[144:147], a[248:251], v[24:27]\n"
    "v_exp_f32 v12, v12\n"
    "v_mfma_f32_16x16x32_bf16 v[8:11], a[144:147], a[200:203], v[8:11]\n"
    "v_exp_f32 v13, v13\n"
    "v_mfma_f32_16x16x32_bf16 v[24:27], a[148:151], a[252:255], v[24:27]\n"
    "v_exp_f32 v4, v4\n"
    "v_mfma_f32_16x16x32_bf16 v[24:27], a[152:155], a[160:163], v[24:27]\n"
    "v_exp_f32 v59, v59\n"
    "v_mfma_f32_16x16x32_bf16 v[16:19], a[144:147], a[224:227], v[16:19]\n"
    "s_mov_b32 s79, 133\n"
    "v_exp_f32 v5, v5\n"
    "v_mfma_f32_16x16x32_bf16 v[16:19], a[140:143], a[220:223], v[16:19]\n"
    "v_exp_f32 v54, v54\n"
    "v_mfma_f32_16x16x32_bf16 v[8:11], a[148:151], a[204:207], v[8:11]\n"
    "v_exp_f32 v45, v45\n"
    "v_mfma_f32_16x16x32_bf16 v[16:19], a[136:139], a[216:219], v[16:19]\n"
    "ds_read_b128 a[124:127], v252 offset:43008\n"
    "ds_read_b32 v245, v247\n"
    "v_mfma_f32_16x16x32_bf16 v[32:35], a[148:151], a[180:183], v[32:35]\n"
    "v_exp_f32 v6, v6\n"
    "v_mfma_f32_16x16x32_bf16 v[8:11], a[152:155], a[208:211], v[8:11]\n"
    "ds_read_b128 a[152:155], v252 offset:46080\n"
    "v_cvt_pk_bf16_f32 v217, v42, v43\n"
    "v_mfma_f32_16x16x32_bf16 v[8:11], a[156:159], a[212:215], v[8:11]\n"
    "v_exp_f32 v19, v19\n"
    "v_mfma_f32_16x16x32_bf16 v[24:27], a[156:159], a[164:167], v[24:27]\n"
    "v_exp_f32 v51, v51\n"
    "v_mfma_f32_16x16x32_bf16 v[32:35], a[144:147], a[176:179], v[32:35]\n"
    "s_waitcnt lgkmcnt(7)\n"
    "v_cvt_pk_bf16_f32 v219, v58, v59\n"
    "v_cvt_pk_bf16_f32 v233, v50, v51\n"
    "v_mfma_f32_16x16x32_bf16 v[36:39], a[112:115], a[192:195], v[100:103]\n"
    "s_waitcnt lgkmcnt(3)\n"
    "v_exp_f32 v21, v21\n"
    "v_mfma_f32_16x16x32_bf16 v[40:43], a[132:135], a[236:239], v[100:103]\n"
    "ds_read_b128 a[156:159], v252 offset:48128\n"
    "v_cvt_pk_bf16_f32 v227, v62, v63\n"
    "v_mfma_f32_16x16x32_bf16 v[36:39], a[116:119], a[196:199], v[36:39]\n"
    "s_add_u32 s59, s59, 4\n"
    "v_exp_f32 v28, v28\n"
    "v_mfma_f32_16x16x32_bf16 v[32:35], a[140:143], a[172:175], v[32:35]\n"
    "v_exp_f32 v52, v52\n"
    "v_mfma_f32_16x16x32_bf16 v[32:35], a[136:139], a[168:171], v[32:35]\n"
    "v_cvt_pk_bf16_f32 v232, v48, v49\n"
    "v_mad_u32_u24 v244, v224, 24, v2\n"
    "v_mfma_f32_16x16x32_bf16 v[48:51], a[132:135], a[188:191], v[100:103]\n"
    "ds_read_b128 a[136:139], v252 offset:37888\n"
    "ds_read_b128 v[104:107], v250 offset:1680\n"
    "v_mfma_f32_16x16x32_bf16 v[36:39], a[120:123], a[200:203], v[36:39]\n"
    "s_waitcnt lgkmcnt(5)\n"
    "v_exp_f32 v57, v57\n"
    "v_mfma_f32_16x16x32_bf16 v[36:39], a[124:127], a[204:207], v[36:39]\n"
    "v_cvt_pk_bf16_f32 v225, v46, v47\n"
    "v_cvt_pk_bf16_f32 v210, v52, v53\n"
    "v_mfma_f32_16x16x32_bf16 v[36:39], a[128:131], a[208:211], v[36:39]\n"
    "v_cvt_pk_bf16_f32 v224, v44, v45\n"
    "v_cvt_pk_bf16_f32 v211, v54, v55\n"
    "v_mfma_f32_16x16x32_bf16 v[40:43], a[128:131], a[232:235], v[40:43]\n"
    "ds_read_b128 a[144:147], v252 offset:41984\n"
    "ds_read_b128 a[140:143], v252 offset:39936\n"
    "v_mfma_f32_16x16x32_bf16 v[44:47], a[112:115], a[240:243], v[100:103]\n"
    "v_exp_f32 v20, v20\n"
    "v_mfma_f32_16x16x32_bf16 v[40:43], a[124:127], a[228:231], v[40:43]\n"
    "v_cvt_pk_bf16_f32 v12, v12, v13\n"
    "v_cvt_pk_bf16_f32 v218, v56, v57\n"
    "v_mfma_f32_16x16x32_bf16 v[36:39], a[132:135], a[212:215], v[36:39]\n"
    "v_exp_f32 v32, v32\n"
    "v_mfma_f32_16x16x32_bf16 v[40:43], a[120:123], a[224:227], v[40:43]\n"
    "v_exp_f32 v30, v30\n"
    "v_smfmac_f32_16x16x64_bf16 v[120:123], v[240:243], v[228:235], v253\n"
    "ds_read_b128 a[148:151], v252 offset:44032\n"
    "v_mfma_f32_16x16x32_bf16 v[44:47], a[116:119], a[244:247], v[44:47]\n"
    "s_waitcnt lgkmcnt(3)\n"
    "v_exp_f32 v26, v26\n"
    "v_mfma_f32_16x16x32_bf16 v[52:55], a[136:139], a[192:195], v[104:107]\n"
    "v_exp_f32 v15, v15\n"
    "v_mfma_f32_16x16x32_bf16 v[56:59], a[156:159], a[236:239], v[104:107]\n"
    "s_waitcnt lgkmcnt(1)\n"
    "v_cvt_pk_bf16_f32 v4, v4, v5\n"
    "v_cmp_le_u32 s[56:57], s59, v245\n"
    "v_smfmac_f32_16x16x64_bf16 v[108:111], v[240:243], v[204:211], v253\n"
    "s_cmp_eq_u64 s[56:57], -1\n"
    "v_lshrrev_b32 v252, 3, v1\n"
    "v_mfma_f32_16x16x32_bf16 v[52:55], a[140:143], a[196:199], v[52:55]\n"
    "v_exp_f32 v7, v7\n"
    "v_mfma_f32_16x16x32_bf16 v[60:63], a[136:139], a[240:243], v[104:107]\n"
    "v_exp_f32 v17, v17\n"
    "v_mfma_f32_16x16x32_bf16 v[60:63], a[140:143], a[244:247], v[60:63]\n"
    "v_exp_f32 v14, v14\n"
    "v_mfma_f32_16x16x32_bf16 v[56:59], a[152:155], a[232:235], v[56:59]\n"
    "v_exp_f32 v39, v39\n"
    "v_mfma_f32_16x16x32_bf16 v[64:67], a[156:159], a[188:191], v[104:107]\n"
    "v_exp_f32 v11, v11\n"
    "v_mfma_f32_16x16x32_bf16 v[60:63], a[144:147], a[248:251], v[60:63]\n"
    "v_exp_f32 v22, v22\n"
    "v_mfma_f32_16x16x32_bf16 v[52:55], a[144:147], a[200:203], v[52:55]\n"
    "s_waitcnt lgkmcnt(0)\n"
    "v_exp_f32 v27, v27\n"
    "v_mfma_f32_16x16x32_bf16 v[52:55], a[148:151], a[204:207], v[52:55]\n"
    "v_exp_f32 v8, v8\n"
    "v_mfma_f32_16x16x32_bf16 v[52:55], a[152:155], a[208:211], v[52:55]\n"
    "v_exp_f32 v9, v9\n"
    "v_mfma_f32_16x16x32_bf16 v[56:59], a[148:151], a[228:231], v[56:59]\n"
    "ds_read_b128 a[204:207], v248 offset:13312\n"
    "v_cvt_pk_bf16_f32 v5, v6, v7\n"
    "v_mfma_f32_16x16x32_bf16 v[64:67], a[152:155], a[184:187], v[64:67]\n"
    "v_exp_f32 v16, v16\n"
    "v_mfma_f32_16x16x32_bf16 v[56:59], a[144:147], a[224:227], v[56:59]\n"
    "ds_read_b128 a[208:211], v248 offset:2048\n"
    "ds_read_b128 a[196:199], v248 offset:12288\n"
    "v_mfma_f32_16x16x32_bf16 v[40:43], a[116:119], a[220:223], v[40:43]\n"
    "v_exp_f32_e64 v38, v38\n"
    "v_mfma_f32_16x16x32_bf16 v[52:55], a[156:159], a[212:215], v[52:55]\n"
    "v_exp_f32_e64 v37, v37\n"
    "v_smfmac_f32_16x16x64_bf16 v[116:119], v[240:243], v[220:227], v253\n"
    "v_cvt_pk_bf16_f32 v6, v8, v9\n"
    "v_smfmac_f32_16x16x64_bf16 v[112:115], v[240:243], v[212:219], v253\n"
    "s_cbranch_scc0 .Lpt4\n"
    ".Lpt4r:\n"
    "v_mfma_f32_16x16x32_bf16 v[44:47], a[120:123], a[248:251], v[44:47]\n"
    "v_exp_f32 v10, v10\n"
    "v_mfma_f32_16x16x32_bf16 v[40:43], a[112:115], a[216:219], v[40:43]\n"
    "v_exp_f32 v25, v25\n"
    "v_mfma_f32_16x16x32_bf16 v[48:51], a[128:131], a[184:187], v[48:51]\n"
    "v_exp_f32 v54, v54\n"
    "v_mfma_f32_16x16x32_bf16 v[56:59], a[140:143], a[220:223], v[56:59]\n"
    "s_waitcnt lgkmcnt(1)\n"
    "ds_read_b128 a[212:215], v248 offset:14336\n"
    "v_cvt_pk_bf16_f32 v20, v20, v21\n"
    "v_mfma_f32_16x16x32_bf16 v[148:151], a[208:211], v[76:79], v[148:151]\n"
    "s_waitcnt vmcnt(0)\n"
    "ds_add_u32 v247, v246\n"
    "v_mfma_f32_16x16x32_bf16 v[64:67], a[148:151], a[180:183], v[64:67]\n"
    "ds_read_b128 a[192:195], v248 offset:0\n"
    "ds_read_b128 a[200:203], v248 offset:1024\n"
    "v_mfma_f32_16x16x32_bf16 v[48:51], a[124:127], a[180:183], v[48:51]\n"
    "v_exp_f32 v52, v52\n"
    "v_mfma_f32_16x16x32_bf16 v[128:131], a[204:207], v[72:75], v[128:131]\n"
    "v_exp_f32 v41, v41\n"
    "v_mfma_f32_16x16x32_bf16 v[144:147], a[204:207], v[80:83], v[144:147]\n"
    "v_exp_f32 v36, v36\n"
    "v_mfma_f32_16x16x32_bf16 v[44:47], a[124:127], a[252:255], v[44:47]\n"
    "v_exp_f32 v24, v24\n"
    "v_mfma_f32_16x16x32_bf16 v[60:63], a[148:151], a[252:255], v[60:63]\n"
    "v_exp_f32 v23, v23\n"
    "v_mfma_f32_16x16x32_bf16 v[44:47], a[128:131], a[160:163], v[44:47]\n"
    "v_exp_f32 v55, v55\n"
    "v_mfma_f32_16x16x32_bf16 v[48:51], a[120:123], a[176:179], v[48:51]\n"
    "v_exp_f32 v29, v29\n"
    "v_mfma_f32_16x16x32_bf16 v[64:67], a[144:147], a[176:179], v[64:67]\n"
    "ds_read_b128 a[236:239], v249 offset:37888\n"
    "v_cvt_pk_bf16_f32 v9, v38, v39\n"
    "v_mfma_f32_16x16x32_bf16 v[56:59], a[136:139], a[216:219], v[56:59]\n"
    "ds_read_b128 a[220:223], v248 offset:15360\n"
    "ds_read_b128 a[244:247], v249 offset:38912\n"
    "v_mfma_f32_16x16x32_bf16 v[48:51], a[116:119], a[172:175], v[48:51]\n"
    "s_waitcnt lgkmcnt(6)\n"
    "ds_read_b128 a[232:235], v249 offset:25600\n"
    "ds_read_b128 a[240:243], v249 offset:26624\n"
    "v_mfma_f32_16x16x32_bf16 v[64:67], a[140:143], a[172:175], v[64:67]\n"
    "s_add_u32 s59, s59, 4\n"
    "v_cvt_pk_bf16_f32 v7, v10, v11\n"
    "v_cvt_pk_bf16_f32 v13, v14, v15\n"
    "v_mfma_f32_16x16x32_bf16 v[148:151], a[212:215], v[80:83], v[148:151]\n"
    "v_exp_f32 v53, v53\n"
    "v_mfma_f32_16x16x32_bf16 v[44:47], a[132:135], a[164:167], v[44:47]\n"
    "ds_read_b32 v245, v247\n"
    "v_cvt_pk_bf16_f32 v11, v54, v55\n"
    "v_mfma_f32_16x16x32_bf16 v[60:63], a[152:155], a[160:163], v[60:63]\n"
    "ds_read_b128 v[240:243], v251 offset:1536\n"
    "ds_read_b128 a[216:219], v248 offset:3072\n"
    "v_mfma_f32_16x16x32_bf16 v[64:67], a[136:139], a[168:171], v[64:67]\n"
    "s_waitcnt lgkmcnt(8)\n"
    "v_cvt_pk_bf16_f32 v8, v36, v37\n"
    "v_cvt_pk_bf16_f32 v10, v52, v53\n"
    "v_mfma_f32_16x16x32_bf16 v[128:131], a[200:203], v[68:71], v[128:131]\n"
    "v_exp_f32 v43, v43\n"
    "v_mfma_f32_16x16x32_bf16 v[60:63], a[156:159], a[164:167], v[60:63]\n"
    "v_exp_f32 v45, v45\n"
    "v_mfma_f32_16x16x32_bf16 v[140:143], a[192:195], v[76:79], v[140:143]\n"
    "ds_read_b128 a[252:255], v249 offset:39936\n"
    "ds_read_b128 a[224:227], v249 offset:24576\n"
    "v_mfma_f32_16x16x32_bf16 v[124:127], a[192:195], v[68:71], v[124:127]\n"
    "s_waitcnt lgkmcnt(9)\n"
    "v_cvt_pk_bf16_f32 v14, v16, v17\n"
    "v_cvt_pk_bf16_f32 v21, v22, v23\n"
    "v_mfma_f32_16x16x32_bf16 v[128:131], a[236:239], v[208:211], v[128:131]\n"
    "v_exp_f32 v58, v58\n"
    "v_mfma_f32_16x16x32_bf16 v[140:143], a[196:199], v[80:83], v[140:143]\n"
    "s_waitcnt lgkmcnt(4)\n"
    "ds_read_b128 a[228:231], v249 offset:36864\n"
    "v_cmp_le_u32 s[56:57], s59, v245\n"
    "v_mfma_f32_16x16x32_bf16 v[124:127], a[196:199], v[72:75], v[124:127]\n"
    "v_exp_f32 v62, v62\n"
    "v_mfma_f32_16x16x32_bf16 v[148:151], a[240:243], v[212:215], v[148:151]\n"
    "s_cmp_eq_u64 s[56:57], -1\n"
    "v_exp_f32 v40, v40\n"
    "v_mfma_f32_16x16x32_bf16 v[132:135], a[208:211], v[68:71], v[132:135]\n"
    "s_waitcnt lgkmcnt(4)\n"
    "v_exp_f32 v42, v42\n"
    "v_smfmac_f32_16x16x64_bf16 v[108:111], v[240:243], v[4:11], v253\n"
    "s_cbranch_scc0 .Lpt3\n"
    ".Lpt3r:\n"
    "v_mfma_f32_16x16x32_bf16 v[48:51], a[112:115], a[168:171], v[48:51]\n"
    "s_waitcnt lgkmcnt(1)\n"
    "ds_read_b128 a[176:179], v249 offset:2048\n"
    "ds_read_b128 a[168:171], v249 offset:1024\n"
    "v_mfma_f32_16x16x32_bf16 v[124:127], a[224:227], v[204:207], v[124:127]\n"
    "v_exp_f32 v18, v18\n"
    "v_mfma_f32_16x16x32_bf16 v[136:139], a[220:223], v[72:75], v[136:139]\n"
    "v_cvt_pk_bf16_f32 v16, v40, v41\n"
    "v_cvt_pk_bf16_f32 v15, v18, v19\n"
    "v_mfma_f32_16x16x32_bf16 v[152:155], a[220:223], v[80:83], v[152:155]\n"
    "s_waitcnt lgkmcnt(2)\n"
    "v_exp_f32 v61, v61\n"
    "v_mfma_f32_16x16x32_bf16 v[124:127], a[228:231], v[208:211], v[124:127]\n"
    "v_exp_f32 v63, v63\n"
    "v_mov_b32 v111, v110\n"
    "v_mfma_f32_16x16x32_bf16 v[152:155], a[216:219], v[76:79], v[152:155]\n"
    "v_add_f32 v108, v108, v109\n"
    "v_cvt_pk_bf16_f32 v22, v24, v25\n"
    "v_mfma_f32_16x16x32_bf16 v[132:135], a[212:215], v[72:75], v[132:135]\n"
    "ds_read_b128 a[188:191], v249 offset:15360\n"
    "ds_read_b128 a[248:251], v249 offset:27648\n"
    "v_mfma_f32_16x16x32_bf16 v[136:139], a[216:219], v[68:71], v[136:139]\n"
    "ds_read_b128 a[172:175], v249 offset:13312\n"
    "ds_read_b128 a[180:183], v249 offset:14336\n"
    "v_mfma_f32_16x16x32_bf16 v[144:147], a[200:203], v[76:79], v[144:147]\n"
    "v_exp_f32 v35, v35\n"
    "v_mfma_f32_16x16x32_bf16 v[132:135], a[240:243], v[204:207], v[132:135]\n"
    "ds_read_b128 a[160:163], v249 offset:0\n"
    "v_mov_b32 v109, v108\n"
    "v_mfma_f32_16x16x32_bf16 v[136:139], a[252:255], v[208:211], v[136:139]\n"
    "ds_read_b128 a[164:167], v249 offset:12288\n"
    "ds_read_b128 a[184:187], v249 offset:3072\n"
    "v_mfma_f32_16x16x32_bf16 v[132:135], a[244:247], v[208:211], v[132:135]\n"
    "s_waitcnt lgkmcnt(8)\n"
    "v_exp_f32 v46, v46\n"
    "v_mfma_f32_16x16x32_bf16 v[132:135], a[176:179], v[4:7], v[132:135]\n"
    "v_exp_f32 v59, v59\n"
    "v_mfma_f32_16x16x32_bf16 v[144:147], a[236:239], v[216:219], v[144:147]\n"
    "v_exp_f32 v67, v67\n"
    "v_mfma_f32_16x16x32_bf16 v[144:147], a[232:235], v[212:215], v[144:147]\n"
    "v_permlane16_swap_b32 v108, v109\n"
    "v_mfma_f32_16x16x32_bf16 v[128:131], a[232:235], v[204:207], v[128:131]\n"
    "s_waitcnt lgkmcnt(3)\n"
    "v_exp_f32 v44, v44\n"
    "v_mfma_f32_16x16x32_bf16 v[132:135], a[180:183], v[8:11], v[132:135]\n"
    "v_cvt_pk_bf16_f32 v23, v26, v27\n"
    "v_cvt_pk_bf16_f32 v27, v62, v63\n"
    "v_mfma_f32_16x16x32_bf16 v[136:139], a[248:251], v[204:207], v[136:139]\n"
    "s_waitcnt lgkmcnt(2)\n"
    "v_exp_f32 v57, v57\n"
    "v_mfma_f32_16x16x32_bf16 v[124:127], a[160:163], v[4:7], v[124:127]\n"
    "v_exp_f32 v56, v56\n"
    "v_mfma_f32_16x16x32_bf16 v[140:143], a[224:227], v[212:215], v[140:143]\n"
    "s_waitcnt lgkmcnt(1)\n"
    "v_and_b32 v252, 0x70, v252\n"
    "v_add_f32 v108, v108, v109\n"
    "v_mfma_f32_16x16x32_bf16 v[124:127], a[164:167], v[8:11], v[124:127]\n"
    "v_permlane16_swap_b32 v110, v111\n"
    "v_mfma_f32_16x16x32_bf16 v[128:131], a[172:175], v[8:11], v[128:131]\n"
    "v_add_f32 v110, v110, v111\n"
    "v_cvt_pk_bf16_f32 v18, v56, v57\n"
    "v_mfma_f32_16x16x32_bf16 v[128:131], a[168:171], v[4:7], v[128:131]\n"
    ".Lwp1:\n"
    "s_cbranch_execz .Lwp2\n"
    "v_rcp_f32 v109, v108\n"
    "v_mfma_f32_16x16x32_bf16 v[136:139], a[188:191], v[8:11], v[136:139]\n"
    "s_waitcnt lgkmcnt(0)\n"
    "v_exp_f32 v60, v60\n"
    "v_mfma_f32_16x16x32_bf16 v[136:139], a[184:187], v[4:7], v[136:139]\n"
    "v_mul_f32 v111, -v110, v109\n"
    "v_cvt_pk_bf16_f32 v19, v58, v59\n"
    "v_cvt_pk_bf16_f32 v17, v42, v43\n"
    "v_xor_b32 v252, v252, v1\n"
    "v_ldexp_f32 v109, v109, s79\n"
    "v_pk_fma_f32 v[134:135], v[134:135], v[108:109], v[110:111] op_sel:[0,1,1] op_sel_hi:[1,1,1]\n"
    "v_pk_fma_f32 v[124:125], v[124:125], v[108:109], v[110:111] op_sel:[0,1,1] op_sel_hi:[1,1,1]\n"
    "v_pk_fma_f32 v[126:127], v[126:127], v[108:109], v[110:111] op_sel:[0,1,1] op_sel_hi:[1,1,1]\n"
    "v_pk_fma_f32 v[128:129], v[128:129], v[108:109], v[110:111] op_sel:[0,1,1] op_sel_hi:[1,1,1]\n"
    "v_pk_fma_f32 v[132:133], v[132:133], v[108:109], v[110:111] op_sel:[0,1,1] op_sel_hi:[1,1,1]\n"
    "v_pk_fma_f32 v[130:131], v[130:131], v[108:109], v[110:111] op_sel:[0,1,1] op_sel_hi:[1,1,1]\n"
    "v_pk_fma_f32 v[138:139], v[138:139], v[108:109], v[110:111] op_sel:[0,1,1] op_sel_hi:[1,1,1]\n"
    "v_pk_fma_f32 v[136:137], v[136:137], v[108:109], v[110:111] op_sel:[0,1,1] op_sel_hi:[1,1,1]\n"
    "v_cvt_pk_bf16_f32 v124, v124, v128\n"
    "v_cvt_pk_bf16_f32 v128, v126, v130\n"
    "v_mfma_f32_16x16x32_bf16 v[152:155], a[252:255], v[216:219], v[152:155]\n"
    "v_cvt_pk_bf16_f32 v130, v127, v131\n"
    "v_cvt_pk_bf16_f32 v126, v125, v129\n"
    "v_cvt_pk_bf16_f32 v127, v133, v137\n"
    "v_mfma_f32_16x16x32_bf16 v[148:151], a[244:247], v[216:219], v[148:151]\n"
    "v_add_u32 v252, s72, v252\n"
    "v_cvt_pk_bf16_f32 v125, v132, v136\n"
    "v_mfma_f32_16x16x32_bf16 v[140:143], a[228:231], v[216:219], v[140:143]\n"
    "ds_write_b128 v3, v[124:127] offset:0\n"
    "v_smfmac_f32_16x16x64_bf16 v[112:115], v[240:243], v[12:19], v253\n"
    "v_cvt_pk_bf16_f32 v129, v134, v138\n"
    "v_cvt_pk_bf16_f32 v26, v60, v61\n"
    "v_mfma_f32_16x16x32_bf16 v[148:151], a[176:179], v[12:15], v[148:151]\n"
    "v_exp_f32 v51, v51\n"
    "v_mfma_f32_16x16x32_bf16 v[140:143], a[160:163], v[12:15], v[140:143]\n"
    "v_cvt_pk_bf16_f32 v131, v135, v139\n"
    "v_xor_b32 v2, 64, v3\n"
    "v_mfma_f32_16x16x32_bf16 v[156:159], a[192:195], v[84:87], v[156:159]\n"
    "ds_write_b128 v2, v[128:131] offset:0\n"
    "v_mfma_f32_16x16x32_bf16 v[156:159], a[196:199], v[88:91], v[156:159]\n"
    "v_exp_f32 v47, v47\n"
    "v_mfma_f32_16x16x32_bf16 v[152:155], a[248:251], v[212:215], v[152:155]\n"
    "v_cvt_pk_bf16_f32 v24, v44, v45\n"
    "v_cvt_pk_bf16_f32 v25, v46, v47\n"
    "v_mfma_f32_16x16x32_bf16 v[156:159], a[224:227], v[220:223], v[156:159]\n"
    "ds_read_b128 v[132:135], v252 offset:0\n"
    "v_add_f32 v112, v112, v113\n"
    "v_mfma_f32_16x16x32_bf16 v[140:143], a[164:167], v[16:19], v[140:143]\n"
    "v_exp_f32 v34, v34\n"
    "v_mfma_f32_16x16x32_bf16 v[144:147], a[172:175], v[16:19], v[144:147]\n"
    "v_mov_b32 v113, v112\n"
    "v_mov_b32 v115, v114\n"
    "v_mfma_f32_16x16x32_bf16 v[144:147], a[168:171], v[12:15], v[144:147]\n"
    "ds_read_b128 v[136:139], v252 offset:1024\n"
    "v_permlane16_swap_b32 v112, v113\n"
    "v_mfma_f32_16x16x32_bf16 v[164:167], a[208:211], v[84:87], v[164:167]\n"
    "v_permlane16_swap_b32 v114, v115\n"
    "v_mfma_f32_16x16x32_bf16 v[152:155], a[188:191], v[16:19], v[152:155]\n"
    "v_exp_f32_e64 v48, v48\n"
    "v_mfma_f32_16x16x32_bf16 v[148:151], a[180:183], v[16:19], v[148:151]\n"
    "v_add_f32 v112, v112, v113\n"
    "v_cvt_pk_bf16_f32 v28, v28, v29\n"
    "v_mfma_f32_16x16x32_bf16 v[152:155], a[184:187], v[12:15], v[152:155]\n"
    "v_rcp_f32 v113, v112\n"
    "v_smfmac_f32_16x16x64_bf16 v[116:119], v[240:243], v[20:27], v253\n"
    "s_waitcnt lgkmcnt(1)\n"
    "buffer_store_dwordx4 v[132:135], v244, s[24:27], s75 offen offset:0 sc1\n"
    "v_add_f32 v114, v114, v115\n"
    "v_mul_f32 v115, -v114, v113\n"
    "v_ldexp_f32 v113, v113, s79\n"
    "v_pk_fma_f32 v[142:143], v[142:143], v[112:113], v[114:115] op_sel:[0,1,1] op_sel_hi:[1,1,1]\n"
    "v_pk_fma_f32 v[144:145], v[144:145], v[112:113], v[114:115] op_sel:[0,1,1] op_sel_hi:[1,1,1]\n"
    "v_pk_fma_f32 v[140:141], v[140:141], v[112:113], v[114:115] op_sel:[0,1,1] op_sel_hi:[1,1,1]\n"
    "v_pk_fma_f32 v[146:147], v[146:147], v[112:113], v[114:115] op_sel:[0,1,1] op_sel_hi:[1,1,1]\n"
    "v_pk_fma_f32 v[150:151], v[150:151], v[112:113], v[114:115] op_sel:[0,1,1] op_sel_hi:[1,1,1]\n"
    "v_add_f32_e64 v116, v116, v117\n"
    "v_pk_fma_f32 v[152:153], v[152:153], v[112:113], v[114:115] op_sel:[0,1,1] op_sel_hi:[1,1,1]\n"
    "v_pk_fma_f32 v[148:149], v[148:149], v[112:113], v[114:115] op_sel:[0,1,1] op_sel_hi:[1,1,1]\n"
    "v_pk_fma_f32 v[154:155], v[154:155], v[112:113], v[114:115] op_sel:[0,1,1] op_sel_hi:[1,1,1]\n"
    "v_cvt_pk_bf16_f32 v140, v140, v144\n"
    "v_cvt_pk_bf16_f32 v144, v142, v146\n"
    "v_mfma_f32_16x16x32_bf16 v[160:163], a[204:207], v[88:91], v[160:163]\n"
    "v_cvt_pk_bf16_f32 v146, v143, v147\n"
    "v_cvt_pk_bf16_f32 v142, v141, v145\n"
    "v_mfma_f32_16x16x32_bf16 v[156:159], a[228:231], v[224:227], v[156:159]\n"
    "v_cvt_pk_bf16_f32 v141, v148, v152\n"
    "v_cvt_pk_bf16_f32 v143, v149, v153\n"
    "v_mfma_f32_16x16x32_bf16 v[156:159], a[160:163], v[20:23], v[156:159]\n"
    "s_waitcnt lgkmcnt(0)\n"
    "buffer_store_dwordx4 v[136:139], v244, s[24:27], s75 offen offset:3072 sc1\n"
    "ds_write_b128 v3, v[140:143] offset:2048\n"
    "v_mfma_f32_16x16x32_bf16 v[172:175], a[192:195], v[92:95], v[172:175]\n"
    "v_mov_b32 v119, v118\n"
    "v_cvt_pk_bf16_f32 v145, v150, v154\n"
    "v_mfma_f32_16x16x32_bf16 v[156:159], a[164:167], v[24:27], v[156:159]\n"
    "v_cvt_pk_bf16_f32 v147, v151, v155\n"
    "v_mov_b32 v117, v116\n"
    "v_mfma_f32_16x16x32_bf16 v[180:183], a[208:211], v[92:95], v[180:183]\n"
    "ds_write_b128 v2, v[144:147] offset:2048\n"
    "v_mfma_f32_16x16x32_bf16 v[160:163], a[200:203], v[84:87], v[160:163]\n"
    "v_permlane16_swap_b32 v118, v119\n"
    "v_mfma_f32_16x16x32_bf16 v[168:171], a[220:223], v[88:91], v[168:171]\n"
    "v_exp_f32 v33, v33\n"
    "v_mfma_f32_16x16x32_bf16 v[172:175], a[196:199], v[96:99], v[172:175]\n"
    "ds_read_b128 v[148:151], v252 offset:2048\n"
    "ds_read_b128 v[152:155], v252 offset:3072\n"
    "v_mfma_f32_16x16x32_bf16 v[180:183], a[212:215], v[96:99], v[180:183]\n"
    "v_exp_f32 v49, v49\n"
    "v_mfma_f32_16x16x32_bf16 v[160:163], a[236:239], v[224:227], v[160:163]\n"
    "v_permlane16_swap_b32 v116, v117\n"
    "v_mfma_f32_16x16x32_bf16 v[168:171], a[216:219], v[84:87], v[168:171]\n"
    "v_exp_f32 v31, v31\n"
    "v_mfma_f32_16x16x32_bf16 v[176:179], a[204:207], v[96:99], v[176:179]\n"
    "v_exp_f32 v66, v66\n"
    "v_mfma_f32_16x16x32_bf16 v[164:167], a[212:215], v[88:91], v[164:167]\n"
    "ds_read_b128 a[204:207], v248 offset:17408\n"
    "v_add_f32 v118, v118, v119\n"
    "v_mfma_f32_16x16x32_bf16 v[160:163], a[232:235], v[220:223], v[160:163]\n"
    "v_cvt_pk_bf16_f32 v29, v30, v31\n"
    "v_add_f32 v116, v116, v117\n"
    "v_mfma_f32_16x16x32_bf16 v[168:171], a[252:255], v[224:227], v[168:171]\n"
    "v_rcp_f32 v117, v116\n"
    "v_mfma_f32_16x16x32_bf16 v[160:163], a[172:175], v[24:27], v[160:163]\n"
    "s_waitcnt lgkmcnt(1)\n"
    "buffer_store_dwordx4 v[152:155], v244, s[24:27], s76 offen offset:3072 sc1\n"
    "v_exp_f32 v50, v50\n"
    "v_exp_f32 v64, v64\n"
    "v_mfma_f32_16x16x32_bf16 v[176:179], a[200:203], v[92:95], v[176:179]\n"
    "ds_read_b128 a[196:199], v248 offset:16384\n"
    "v_cvt_pk_bf16_f32 v31, v34, v35\n"
    "v_mfma_f32_16x16x32_bf16 v[160:163], a[168:171], v[20:23], v[160:163]\n"
    "v_mul_f32 v119, -v118, v117\n"
    "v_cvt_pk_bf16_f32 v30, v32, v33\n"
    "v_mfma_f32_16x16x32_bf16 v[164:167], a[240:243], v[220:223], v[164:167]\n"
    "buffer_store_dwordx4 v[148:151], v244, s[24:27], s76 offen offset:0 sc1\n"
    "v_cvt_pk_bf16_f32 v35, v66, v67\n"
    "v_ldexp_f32 v117, v117, s79\n"
    "v_exp_f32 v65, v65\n"
    "v_mfma_f32_16x16x32_bf16 v[184:187], a[220:223], v[96:99], v[184:187]\n"
    "v_fma_f32 v156, v156, v117, v119\n"
    "v_fma_f32 v157, v157, v117, v119\n"
    "v_mfma_f32_16x16x32_bf16 v[168:171], a[248:251], v[220:223], v[168:171]\n"
    "v_fma_f32 v158, v158, v117, v119\n"
    "v_fma_f32 v159, v159, v117, v119\n"
    "v_mfma_f32_16x16x32_bf16 v[164:167], a[244:247], v[224:227], v[164:167]\n"
    "v_fma_f32 v160, v160, v117, v119\n"
    "v_fma_f32 v161, v161, v117, v119\n"
    "v_mfma_f32_16x16x32_bf16 v[168:171], a[188:191], v[24:27], v[168:171]\n"
    "v_fma_f32 v162, v162, v117, v119\n"
    "v_fma_f32 v163, v163, v117, v119\n"
    "v_mfma_f32_16x16x32_bf16 v[172:175], a[224:227], v[228:231], v[172:175]\n"
    "ds_read_b128 a[224:227], v249 offset:28672\n"
    "v_cvt_pk_bf16_f32 v34, v64, v65\n"
    "v_mfma_f32_16x16x32_bf16 v[168:171], a[184:187], v[20:23], v[168:171]\n"
    "v_cvt_pk_bf16_f32 v156, v156, v160\n"
    "v_cvt_pk_bf16_f32 v160, v158, v162\n"
    "v_mfma_f32_16x16x32_bf16 v[164:167], a[176:179], v[20:23], v[164:167]\n"
    "v_cvt_pk_bf16_f32 v162, v159, v163\n"
    "v_cvt_pk_bf16_f32 v32, v48, v49\n"
    "v_mfma_f32_16x16x32_bf16 v[164:167], a[180:183], v[24:27], v[164:167]\n"
    "ds_read_b128 a[192:195], v248 offset:4096\n"
    "v_cvt_pk_bf16_f32 v158, v157, v161\n"
    "v_mfma_f32_16x16x32_bf16 v[180:183], a[240:243], v[228:231], v[180:183]\n"
    "v_cvt_pk_bf16_f32 v33, v50, v51\n"
    "v_fma_f32 v168, v168, v117, v119\n"
    "v_fma_f32 v169, v169, v117, v119\n"
    "v_mfma_f32_16x16x32_bf16 v[184:187], a[216:219], v[92:95], v[184:187]\n"
    "v_fma_f32 v170, v170, v117, v119\n"
    "v_fma_f32 v171, v171, v117, v119\n"
    "v_smfmac_f32_16x16x64_bf16 v[120:123], v[240:243], v[28:35], v253\n"
    "ds_read_b128 a[220:223], v248 offset:19456\n"
    "v_mfma_f32_16x16x32_bf16 v[184:187], a[252:255], v[232:235], v[184:187]\n"
    "v_fma_f32 v164, v164, v117, v119\n"
    "v_fma_f32 v165, v165, v117, v119\n"
    "v_mfma_f32_16x16x32_bf16 v[172:175], a[228:231], v[232:235], v[172:175]\n"
    "v_cvt_pk_bf16_f32 v157, v164, v168\n"
    "v_cvt_pk_bf16_f32 v159, v165, v169\n"
    "v_mfma_f32_16x16x32_bf16 v[176:179], a[236:239], v[232:235], v[176:179]\n"
    "ds_write_b128 v3, v[156:159] offset:4096\n"
    "v_mfma_f32_16x16x32_bf16 v[180:183], a[244:247], v[232:235], v[180:183]\n"
    "s_waitcnt lgkmcnt(2)\n"
    "v_fma_f32 v166, v166, v117, v119\n"
    "v_fma_f32 v167, v167, v117, v119\n"
    "v_mfma_f32_16x16x32_bf16 v[188:191], a[192:195], v[68:71], v[188:191]\n"
    "v_cvt_pk_bf16_f32 v163, v167, v171\n"
    "v_cvt_pk_bf16_f32 v161, v166, v170\n"
    "v_mfma_f32_16x16x32_bf16 v[184:187], a[248:251], v[228:231], v[184:187]\n"
    "ds_write_b128 v2, v[160:163] offset:4096\n"
    "v_mfma_f32_16x16x32_bf16 v[192:195], a[204:207], v[72:75], v[192:195]\n"
    "v_add_f32_e64 v120, v120, v121\n"
    "v_mov_b32_e64 v121, v120\n"
    "v_mfma_f32_16x16x32_bf16 v[188:191], a[196:199], v[72:75], v[188:191]\n"
    "s_nop 0\n"
    "v_permlane16_swap_b32 v120, v121\n"
    "v_mfma_f32_16x16x32_bf16 v[176:179], a[232:235], v[228:231], v[176:179]\n"
    "s_waitcnt lgkmcnt(2)\n"
    "ds_read_b128 v[164:167], v252 offset:4096\n"
    "ds_read_b128 a[228:231], v249 offset:40960\n"
    "v_mfma_f32_16x16x32_bf16 v[200:203], a[220:223], v[72:75], v[200:203]\n"
    "ds_read_b128 a[212:215], v248 offset:18432\n"
    "ds_read_b128 v[168:171], v252 offset:5120\n"
    "v_mfma_f32_16x16x32_bf16 v[180:183], a[176:179], v[28:31], v[180:183]\n"
    "ds_read_b128 a[132:135], v249 offset:18432\n"
    "v_mov_b32 v123, v122\n"
    "v_mfma_f32_16x16x32_bf16 v[176:179], a[172:175], v[32:35], v[176:179]\n"
    "ds_read_b128 a[208:211], v248 offset:6144\n"
    "ds_read_b128 a[216:219], v248 offset:7168\n"
    "v_mfma_f32_16x16x32_bf16 v[188:191], a[224:227], v[204:207], v[188:191]\n"
    "ds_read_b128 a[248:251], v249 offset:31744\n"
    "ds_read_b128 a[236:239], v249 offset:41984\n"
    "v_mfma_f32_16x16x32_bf16 v[184:187], a[188:191], v[32:35], v[184:187]\n"
    "v_permlane16_swap_b32_e64 v122, v123\n"
    "v_mfma_f32_16x16x32_bf16 v[172:175], a[160:163], v[28:31], v[172:175]\n"
    "ds_read_b128 a[140:143], v249 offset:19456\n"
    "ds_read_b128 a[200:203], v248 offset:5120\n"
    "v_mfma_f32_16x16x32_bf16 v[180:183], a[180:183], v[32:35], v[180:183]\n"
    "ds_read_b128 a[136:139], v249 offset:7168\n"
    "ds_read_b128 a[124:127], v249 offset:17408\n"
    "v_mfma_f32_16x16x32_bf16 v[184:187], a[184:187], v[28:31], v[184:187]\n"
    "s_waitcnt lgkmcnt(12)\n"
    "buffer_store_dwordx4 v[164:167], v244, s[24:27], s77 offen offset:0 sc1\n"
    "ds_read_b128 a[240:243], v249 offset:30720\n"
    "ds_read_b128 a[252:255], v249 offset:44032\n"
    "v_add_f32 v122, v122, v123\n"
    "v_add_f32 v120, v120, v121\n"
    "v_mfma_f32_16x16x32_bf16 v[176:179], a[168:171], v[28:31], v[176:179]\n"
    "ds_read_b128 a[112:115], v249 offset:4096\n"
    "ds_read_b128 a[128:131], v249 offset:6144\n"
    "v_mfma_f32_16x16x32_bf16 v[172:175], a[164:167], v[32:35], v[172:175]\n"
    "s_waitcnt lgkmcnt(11)\n"
    "v_rcp_f32 v121, v120\n"
    "v_mfma_f32_16x16x32_bf16 v[196:199], a[208:211], v[68:71], v[196:199]\n"
    "ds_read_b128 a[244:247], v249 offset:43008\n"
    "v_mul_f32 v123, -v122, v121\n"
    "v_mfma_f32_16x16x32_bf16 v[188:191], a[228:231], v[208:211], v[188:191]\n"
    "buffer_store_dwordx4 v[168:171], v244, s[24:27], s77 offen offset:3072 sc1\n"
    "ds_read_b128 a[232:235], v249 offset:29696\n"
    "ds_read_b128 a[116:119], v249 offset:16384\n"
    "ds_read_b128 a[120:123], v249 offset:5120\n"
    "v_ldexp_f32 v121, v121, s79\n"
    "v_mfma_f32_16x16x32_bf16 v[196:199], a[212:215], v[72:75], v[196:199]\n"
    "s_waitcnt lgkmcnt(10)\n"
    "v_fma_f32 v174, v174, v121, v123\n"
    "v_fma_f32 v175, v175, v121, v123\n"
    "v_mfma_f32_16x16x32_bf16 v[192:195], a[200:203], v[68:71], v[192:195]\n"
    "v_fma_f32 v176, v176, v121, v123\n"
    "v_fma_f32 v177, v177, v121, v123\n"
    "v_mfma_f32_16x16x32_bf16 v[200:203], a[216:219], v[68:71], v[200:203]\n"
    "s_waitcnt lgkmcnt(7)\n"
    "v_fma_f32 v186, v186, v121, v123\n"
    "v_fma_f32 v187, v187, v121, v123\n"
    "v_mfma_f32_16x16x32_bf16 v[196:199], a[240:243], v[204:207], v[196:199]\n"
    "s_waitcnt lgkmcnt(6)\n"
    "v_fma_f32 v182, v182, v121, v123\n"
    "v_fma_f32 v183, v183, v121, v123\n"
    "v_mfma_f32_16x16x32_bf16 v[200:203], a[252:255], v[208:211], v[200:203]\n"
    "v_fma_f32 v178, v178, v121, v123\n"
    "v_fma_f32 v179, v179, v121, v123\n"
    "v_mfma_f32_16x16x32_bf16 v[200:203], a[248:251], v[204:207], v[200:203]\n"
    "s_waitcnt lgkmcnt(3)\n"
    "v_fma_f32 v172, v172, v121, v123\n"
    "v_fma_f32 v173, v173, v121, v123\n"
    "v_mfma_f32_16x16x32_bf16 v[196:199], a[244:247], v[208:211], v[196:199]\n"
    "v_cvt_pk_bf16_f32 v172, v172, v176\n"
    "v_cvt_pk_bf16_f32 v176, v174, v178\n"
    "v_mfma_f32_16x16x32_bf16 v[192:195], a[236:239], v[208:211], v[192:195]\n"
    "s_waitcnt lgkmcnt(2)\n"
    "v_cvt_pk_bf16_f32 v174, v173, v177\n"
    "v_cvt_pk_bf16_f32 v178, v175, v179\n"
    "v_mfma_f32_16x16x32_bf16 v[192:195], a[232:235], v[204:207], v[192:195]\n"
    "v_cvt_pk_bf16_f32 v177, v182, v186\n"
    "v_cvt_pk_bf16_f32 v179, v183, v187\n"
    "v_mfma_f32_16x16x32_bf16 v[196:199], a[128:131], v[4:7], v[196:199]\n"
    "v_fma_f32 v180, v180, v121, v123\n"
    "v_fma_f32 v181, v181, v121, v123\n"
    "v_mfma_f32_16x16x32_bf16 v[188:191], a[112:115], v[4:7], v[188:191]\n"
    "s_waitcnt lgkmcnt(1)\n"
    "ds_write_b128 v2, v[176:179] offset:0\n"
    "v_mfma_f32_16x16x32_bf16 v[188:191], a[116:119], v[8:11], v[188:191]\n"
    "v_fma_f32 v184, v184, v121, v123\n"
    "v_fma_f32 v185, v185, v121, v123\n"
    "v_mfma_f32_16x16x32_bf16 v[192:195], a[124:127], v[8:11], v[192:195]\n"
    "v_cvt_pk_bf16_f32 v175, v181, v185\n"
    "v_cvt_pk_bf16_f32 v173, v180, v184\n"
    "v_mfma_f32_16x16x32_bf16 v[196:199], a[132:135], v[8:11], v[196:199]\n"
    "ds_write_b128 v3, v[172:175] offset:0\n"
    "v_mfma_f32_16x16x32_bf16 a[4:7], a[204:207], v[80:83], a[4:7]\n"
    "v_fma_f32 v188, v188, v109, v111\n"
    "v_fma_f32 v189, v189, v109, v111\n"
    "v_mfma_f32_16x16x32_bf16 a[0:3], a[192:195], v[76:79], a[0:3]\n"
    "s_waitcnt lgkmcnt(2)\n"
    "v_fma_f32 v190, v190, v109, v111\n"
    "v_fma_f32 v191, v191, v109, v111\n"
    "v_mfma_f32_16x16x32_bf16 v[192:195], a[120:123], v[4:7], v[192:195]\n"
    "ds_read_b128 v[180:183], v252 offset:0\n"
    "ds_read_b128 v[184:187], v252 offset:1024\n"
    "v_mfma_f32_16x16x32_bf16 v[200:203], a[140:143], v[8:11], v[200:203]\n"
    "v_fma_f32 v198, v198, v109, v111\n"
    "v_fma_f32 v199, v199, v109, v111\n"
    "v_mfma_f32_16x16x32_bf16 a[0:3], a[196:199], v[80:83], a[0:3]\n"
    "v_fma_f32 v196, v196, v109, v111\n"
    "v_fma_f32 v197, v197, v109, v111\n"
    "v_mfma_f32_16x16x32_bf16 a[12:15], a[220:223], v[80:83], a[12:15]\n"
    "v_fma_f32 v192, v192, v109, v111\n"
    "v_fma_f32 v193, v193, v109, v111\n"
    "v_mfma_f32_16x16x32_bf16 a[4:7], a[200:203], v[76:79], a[4:7]\n"
    "ds_read_b128 a[164:167], v249 offset:20480\n"
    "ds_read_b128 a[180:183], v249 offset:22528\n"
    "v_mfma_f32_16x16x32_bf16 v[200:203], a[136:139], v[4:7], v[200:203]\n"
    "v_fma_f32 v194, v194, v109, v111\n"
    "v_fma_f32 v195, v195, v109, v111\n"
    "v_mfma_f32_16x16x32_bf16 a[0:3], a[224:227], v[212:215], a[0:3]\n"
    "ds_read_b128 a[172:175], v249 offset:21504\n"
    "v_mfma_f32_16x16x32_bf16 a[4:7], a[236:239], v[216:219], a[4:7]\n"
    "v_cvt_pk_bf16_f32 v188, v188, v192\n"
    "v_cvt_pk_bf16_f32 v192, v190, v194\n"
    "v_mfma_f32_16x16x32_bf16 a[0:3], a[228:231], v[216:219], a[0:3]\n"
    "s_waitcnt lgkmcnt(4)\n"
    "buffer_store_dwordx4 v[180:183], v244, s[24:27], s78 offen offset:0 sc1\n"
    "v_cvt_pk_bf16_f32 v190, v189, v193\n"
    "v_cvt_pk_bf16_f32 v194, v191, v195\n"
    "v_fma_f32 v200, v200, v109, v111\n"
    "v_fma_f32 v201, v201, v109, v111\n"
    "v_mfma_f32_16x16x32_bf16 a[0:3], a[112:115], v[12:15], a[0:3]\n"
    "v_cvt_pk_bf16_f32 v189, v196, v200\n"
    "v_cvt_pk_bf16_f32 v191, v197, v201\n"
    "v_mfma_f32_16x16x32_bf16 a[0:3], a[116:119], v[16:19], a[0:3]\n"
    "ds_read_b128 a[176:179], v249 offset:10240\n"
    "v_mfma_f32_16x16x32_bf16 a[4:7], a[232:235], v[212:215], a[4:7]\n"
    "s_waitcnt lgkmcnt(4)\n"
    "buffer_store_dwordx4 v[184:187], v244, s[24:27], s78 offen offset:3072 sc1\n"
    "ds_write_b128 v3, v[188:191] offset:2048\n"
    "v_mfma_f32_16x16x32_bf16 a[8:11], a[208:211], v[76:79], a[8:11]\n"
    "v_fma_f32 v202, v202, v109, v111\n"
    "v_fma_f32 v203, v203, v109, v111\n"
    "v_mfma_f32_16x16x32_bf16 a[12:15], a[216:219], v[76:79], a[12:15]\n"
    "v_cvt_pk_bf16_f32 v195, v199, v203\n"
    "v_cvt_pk_bf16_f32 v193, v198, v202\n"
    "v_mfma_f32_16x16x32_bf16 a[12:15], a[252:255], v[216:219], a[12:15]\n"
    "ds_write_b128 v2, v[192:195] offset:2048\n"
    "v_mfma_f32_16x16x32_bf16 a[4:7], a[124:127], v[16:19], a[4:7]\n"
    "v_accvgpr_read_b32 v40, a1\n"
    "v_mfma_f32_16x16x32_bf16 a[20:23], a[204:207], v[88:91], a[20:23]\n"
    "v_accvgpr_read_b32 v56, a3\n"
    "v_mov_b32_e64 v250, 0\n"
    "v_mfma_f32_16x16x32_bf16 a[4:7], a[120:123], v[12:15], a[4:7]\n"
    "ds_read_b128 v[200:203], v252 offset:3072\n"
    "ds_read_b128 v[196:199], v252 offset:2048\n"
    "v_mfma_f32_16x16x32_bf16 a[8:11], a[212:215], v[80:83], a[8:11]\n"
    "ds_read_b128 a[188:191], v249 offset:23552\n"
    "v_mfma_f32_16x16x32_bf16 a[8:11], a[240:243], v[212:215], a[8:11]\n"
    "v_accvgpr_read_b32 v36, a0\n"
    "v_accvgpr_read_b32 v52, a2\n"
    "v_mfma_f32_16x16x32_bf16 a[8:11], a[244:247], v[216:219], a[8:11]\n"
    "v_accvgpr_read_b32 v41, a5\n"
    "v_accvgpr_read_b32 v57, a7\n"
    "v_mfma_f32_16x16x32_bf16 a[8:11], a[128:131], v[12:15], a[8:11]\n"
    "v_accvgpr_read_b32 v37, a4\n"
    "v_accvgpr_read_b32 v53, a6\n"
    "v_mfma_f32_16x16x32_bf16 a[8:11], a[132:135], v[16:19], a[8:11]\n"
    "v_fma_f32 v40, v40, v113, v115\n"
    "v_fma_f32 v41, v41, v113, v115\n"
    "v_mfma_f32_16x16x32_bf16 a[12:15], a[248:251], v[212:215], a[12:15]\n"
    "ds_read_b128 a[160:163], v249 offset:8192\n"
    "v_mfma_f32_16x16x32_bf16 a[12:15], a[140:143], v[16:19], a[12:15]\n"
    "v_fma_f32 v52, v52, v113, v115\n"
    "v_fma_f32 v53, v53, v113, v115\n"
    "v_mfma_f32_16x16x32_bf16 a[12:15], a[136:139], v[12:15], a[12:15]\n"
    "s_waitcnt lgkmcnt(3)\n"
    "buffer_store_dwordx4 v[200:203], v244, s[24:27], s75 offen offset:3200 sc1\n"
    "v_cvt_pk_bf16_f32 v52, v52, v53\n"
    "v_fma_f32 v36, v36, v113, v115\n"
    "v_fma_f32 v37, v37, v113, v115\n"
    "v_accvgpr_read_b32 v38, a8\n"
    "v_mfma_f32_16x16x32_bf16 a[16:19], a[192:195], v[84:87], a[16:19]\n"
    "ds_read_b128 a[168:171], v249 offset:9216\n"
    "v_accvgpr_read_b32 v42, a9\n"
    "v_mfma_f32_16x16x32_bf16 a[16:19], a[196:199], v[88:91], a[16:19]\n"
    "v_accvgpr_read_b32 v54, a10\n"
    "v_accvgpr_read_b32 v39, a12\n"
    "v_mfma_f32_16x16x32_bf16 a[20:23], a[200:203], v[84:87], a[20:23]\n"
    "v_accvgpr_read_b32 v55, a14\n"
    "v_accvgpr_read_b32 v58, a11\n"
    "v_mfma_f32_16x16x32_bf16 a[16:19], a[224:227], v[220:223], a[16:19]\n"
    "v_accvgpr_read_b32 v59, a15\n"
    "v_cvt_pk_bf16_f32 v36, v36, v37\n"
    "v_mfma_f32_16x16x32_bf16 a[20:23], a[236:239], v[224:227], a[20:23]\n"
    "v_fma_f32 v38, v38, v113, v115\n"
    "v_fma_f32 v39, v39, v113, v115\n"
    "v_mfma_f32_16x16x32_bf16 a[24:27], a[208:211], v[84:87], a[24:27]\n"
    "v_fma_f32 v54, v54, v113, v115\n"
    "v_fma_f32 v55, v55, v113, v115\n"
    "v_mfma_f32_16x16x32_bf16 a[20:23], a[232:235], v[220:223], a[20:23]\n"
    "v_accvgpr_read_b32 v43, a13\n"
    "v_cvt_pk_bf16_f32 v53, v54, v55\n"
    "v_mfma_f32_16x16x32_bf16 a[20:23], a[124:127], v[24:27], a[20:23]\n"
    "v_fma_f32 v42, v42, v113, v115\n"
    "v_fma_f32 v43, v43, v113, v115\n"
    "v_mfma_f32_16x16x32_bf16 a[16:19], a[228:231], v[224:227], a[16:19]\n"
    "ds_read_b128 a[184:187], v249 offset:11264\n"
    "v_cvt_pk_bf16_f32 v37, v38, v39\n"
    "v_mfma_f32_16x16x32_bf16 a[20:23], a[120:123], v[20:23], a[20:23]\n"
    "v_cvt_pk_bf16_f32 v39, v42, v43\n"
    "v_cvt_pk_bf16_f32 v38, v40, v41\n"
    "v_mfma_f32_16x16x32_bf16 a[28:31], a[220:223], v[88:91], a[28:31]\n"
    "s_waitcnt lgkmcnt(4)\n"
    "buffer_store_dwordx4 v[196:199], v244, s[24:27], s75 offen offset:128 sc1\n"
    "ds_write_b128 v3, v[36:39] offset:4096\n"
    "v_mfma_f32_16x16x32_bf16 a[16:19], a[112:115], v[20:23], a[16:19]\n"
    "v_fma_f32 v58, v58, v113, v115\n"
    "v_fma_f32 v59, v59, v113, v115\n"
    "v_mfma_f32_16x16x32_bf16 a[16:19], a[116:119], v[24:27], a[16:19]\n"
    "v_fma_f32 v56, v56, v113, v115\n"
    "v_fma_f32 v57, v57, v113, v115\n"
    "v_cvt_pk_bf16_f32 v55, v58, v59\n"
    "v_mfma_f32_16x16x32_bf16 a[28:31], a[216:219], v[84:87], a[28:31]\n"
    "v_accvgpr_read_b32 v49, a21\n"
    "v_cvt_pk_bf16_f32 v54, v56, v57\n"
    "v_mfma_f32_16x16x32_bf16 a[24:27], a[212:215], v[88:91], a[24:27]\n"
    "ds_write_b128 v2, v[52:55] offset:4096\n"
    "v_mfma_f32_16x16x32_bf16 a[32:35], a[192:195], v[92:95], a[32:35]\n"
    "v_accvgpr_read_b32 v64, a19\n"
    "v_accvgpr_read_b32 v45, a20\n"
    "v_mfma_f32_16x16x32_bf16 a[24:27], a[240:243], v[220:223], a[24:27]\n"
    "v_accvgpr_read_b32 v44, a16\n"
    "v_accvgpr_read_b32 v65, a23\n"
    "v_mfma_f32_16x16x32_bf16 a[24:27], a[244:247], v[224:227], a[24:27]\n"
    "ds_read_b128 v[56:59], v252 offset:5120\n"
    "v_accvgpr_read_b32 v48, a17\n"
    "v_mfma_f32_16x16x32_bf16 a[28:31], a[252:255], v[224:227], a[28:31]\n"
    "v_fma_f32 v64, v64, v117, v119\n"
    "v_fma_f32 v65, v65, v117, v119\n"
    "v_mfma_f32_16x16x32_bf16 a[24:27], a[128:131], v[20:23], a[24:27]\n"
    "ds_read_b128 v[40:43], v252 offset:4096\n"
    "v_accvgpr_read_b32 v60, a18\n"
    "v_mfma_f32_16x16x32_bf16 a[24:27], a[132:135], v[24:27], a[24:27]\n"
    "v_fma_f32 v44, v44, v117, v119\n"
    "v_fma_f32 v45, v45, v117, v119\n"
    "v_mfma_f32_16x16x32_bf16 a[28:31], a[248:251], v[220:223], a[28:31]\n"
    "ds_read_b128 a[192:195], v248 offset:8192\n"
    "v_accvgpr_read_b32 v61, a22\n"
    "v_mfma_f32_16x16x32_bf16 a[28:31], a[140:143], v[24:27], a[28:31]\n"
    "v_fma_f32 v48, v48, v117, v119\n"
    "v_fma_f32 v49, v49, v117, v119\n"
    "v_mfma_f32_16x16x32_bf16 a[28:31], a[136:139], v[20:23], a[28:31]\n"
    "v_fma_f32 v60, v60, v117, v119\n"
    "v_fma_f32 v61, v61, v117, v119\n"
    "v_mfma_f32_16x16x32_bf16 a[40:43], a[208:211], v[92:95], a[40:43]\n"
    "v_accvgpr_read_b32 v62, a26\n"
    "v_accvgpr_read_b32 v66, a27\n"
    "v_mfma_f32_16x16x32_bf16 a[36:39], a[204:207], v[96:99], a[36:39]\n"
    "s_waitcnt lgkmcnt(2)\n"
    "buffer_store_dwordx4 v[56:59], v244, s[24:27], s76 offen offset:3200 sc1\n"
    "v_accvgpr_read_b32 v46, a24\n"
    "v_accvgpr_read_b32 v63, a30\n"
    "v_accvgpr_read_b32 v47, a28\n"
    "v_accvgpr_read_b32 v51, a29\n"
    "v_mfma_f32_16x16x32_bf16 a[40:43], a[212:215], v[96:99], a[40:43]\n"
    "v_fma_f32 v62, v62, v117, v119\n"
    "v_fma_f32 v63, v63, v117, v119\n"
    "v_mfma_f32_16x16x32_bf16 a[44:47], a[220:223], v[96:99], a[44:47]\n"
    "v_accvgpr_read_b32 v50, a25\n"
    "v_cvt_pk_bf16_f32 v44, v44, v45\n"
    "v_cvt_pk_bf16_f32 v60, v60, v61\n"
    "v_mfma_f32_16x16x32_bf16 a[36:39], a[200:203], v[92:95], a[36:39]\n"
    "v_fma_f32 v50, v50, v117, v119\n"
    "v_fma_f32 v51, v51, v117, v119\n"
    "v_mfma_f32_16x16x32_bf16 a[44:47], a[216:219], v[92:95], a[44:47]\n"
    "v_fma_f32 v46, v46, v117, v119\n"
    "v_fma_f32 v47, v47, v117, v119\n"
    "v_mfma_f32_16x16x32_bf16 a[36:39], a[236:239], v[232:235], a[36:39]\n"
    "v_cvt_pk_bf16_f32 v61, v62, v63\n"
    "v_cvt_pk_bf16_f32 v45, v46, v47\n"
    "v_mfma_f32_16x16x32_bf16 a[40:43], a[240:243], v[228:231], a[40:43]\n"
    "v_accvgpr_read_b32 v67, a31\n"
    "v_cvt_pk_bf16_f32 v47, v50, v51\n"
    "v_mfma_f32_16x16x32_bf16 a[36:39], a[232:235], v[228:231], a[36:39]\n"
    "ds_read_b128 a[208:211], v248 offset:10240\n"
    "v_cvt_pk_bf16_f32 v46, v48, v49\n"
    "v_mfma_f32_16x16x32_bf16 a[36:39], a[124:127], v[32:35], a[36:39]\n"
    "s_waitcnt lgkmcnt(2)\n"
    "buffer_store_dwordx4 v[40:43], v244, s[24:27], s76 offen offset:128 sc1\n"
    "ds_write_b128 v3, v[44:47] offset:0\n"
    "v_mfma_f32_16x16x32_bf16 a[36:39], a[120:123], v[28:31], a[36:39]\n"
    "v_fma_f32 v66, v66, v117, v119\n"
    "v_fma_f32 v67, v67, v117, v119\n"
    "v_mfma_f32_16x16x32_bf16 a[40:43], a[244:247], v[232:235], a[40:43]\n"
    "v_cvt_pk_bf16_f32 v62, v64, v65\n"
    "v_cvt_pk_bf16_f32 v63, v66, v67\n"
    "v_mfma_f32_16x16x32_bf16 a[44:47], a[252:255], v[232:235], a[44:47]\n"
    "ds_write_b128 v2, v[60:63] offset:0\n"
    "v_mfma_f32_16x16x32_bf16 a[32:35], a[196:199], v[96:99], a[32:35]\n"
    "v_accvgpr_read_b32 v125, a36\n"
    "v_fma_f32 v250, |v110|, 4.0, v250\n"
    "v_mfma_f32_16x16x32_bf16 a[44:47], a[248:251], v[228:231], a[44:47]\n"
    "v_fma_f32 v250, v108, 4.0, v250\n"
    "v_accvgpr_read_b32 v137, a39\n"
    "v_mfma_f32_16x16x32_bf16 a[32:35], a[224:227], v[228:231], a[32:35]\n"
    "ds_read_b128 a[236:239], v249 offset:46080\n"
    "ds_read_b128 v[48:51], v252 offset:0\n"
    "ds_read_b128 a[212:215], v248 offset:22528\n"
    "v_mfma_f32_16x16x32_bf16 a[32:35], a[228:231], v[232:235], a[32:35]\n"
    "ds_read_b128 a[224:227], v249 offset:32768\n"
    "v_accvgpr_read_b32 v133, a38\n"
    "v_mfma_f32_16x16x32_bf16 a[40:43], a[128:131], v[28:31], a[40:43]\n"
    "ds_read_b128 a[204:207], v248 offset:21504\n"
    "v_mfma_f32_16x16x32_bf16 a[44:47], a[140:143], v[32:35], a[44:47]\n"
    "ds_read_b128 v[64:67], v252 offset:1024\n"
    "ds_read_b128 a[240:243], v249 offset:34816\n"
    "v_mfma_f32_16x16x32_bf16 a[32:35], a[112:115], v[28:31], a[32:35]\n"
    "ds_read_b128 a[216:219], v248 offset:11264\n"
    "ds_read_b128 a[196:199], v248 offset:20480\n"
    "v_mfma_f32_16x16x32_bf16 a[32:35], a[116:119], v[32:35], a[32:35]\n"
    "s_waitcnt lgkmcnt(12)\n"
    "ds_read_b128 a[244:247], v249 offset:47104\n"
    "ds_read_b128 a[200:203], v248 offset:9216\n"
    "v_mfma_f32_16x16x32_bf16 a[48:51], a[192:195], v[68:71], a[48:51]\n"
    "ds_read_b128 a[232:235], v249 offset:33792\n"
    "ds_read_b128 a[252:255], v249 offset:48128\n"
    "v_mfma_f32_16x16x32_bf16 a[40:43], a[132:135], v[32:35], a[40:43]\n"
    "s_nop 0\n"
    "v_accvgpr_read_b32 v124, a32\n"
    "v_mfma_f32_16x16x32_bf16 a[44:47], a[136:139], v[28:31], a[44:47]\n"
    "s_waitcnt lgkmcnt(11)\n"
    "buffer_store_dwordx4 v[48:51], v244, s[24:27], s77 offen offset:128 sc1\n"
    "v_accvgpr_read_b32 v128, a33\n"
    "v_accvgpr_read_b32 v132, a34\n"
    "v_fma_f32 v132, v132, v121, v123\n"
    "v_fma_f32 v133, v133, v121, v123\n"
    "v_accvgpr_read_b32 v134, a42\n"
    "v_mfma_f32_16x16x32_bf16 a[56:59], a[208:211], v[68:71], a[56:59]\n"
    "s_waitcnt lgkmcnt(8)\n"
    "v_accvgpr_read_b32 v129, a37\n"
    "v_accvgpr_read_b32 v131, a45\n"
    "v_mfma_f32_16x16x32_bf16 a[52:55], a[204:207], v[72:75], a[52:55]\n"
    "v_accvgpr_read_b32 v130, a41\n"
    "v_accvgpr_read_b32 v138, a43\n"
    "v_mfma_f32_16x16x32_bf16 a[56:59], a[212:215], v[72:75], a[56:59]\n"
    "s_waitcnt lgkmcnt(4)\n"
    "v_accvgpr_read_b32 v135, a46\n"
    "v_fma_f32 v124, v124, v121, v123\n"
    "v_fma_f32 v125, v125, v121, v123\n"
    "v_mfma_f32_16x16x32_bf16 a[48:51], a[196:199], v[72:75], a[48:51]\n"
    "s_waitcnt lgkmcnt(2)\n"
    "ds_read_b128 a[228:231], v249 offset:45056\n"
    "ds_read_b128 a[248:251], v249 offset:35840\n"
    "v_mfma_f32_16x16x32_bf16 a[52:55], a[200:203], v[68:71], a[52:55]\n"
    ".Lwp2:\n"
    "s_cbranch_execz .Lw3\n"
    "v_fma_f32 v130, v130, v121, v123\n"
    "v_fma_f32 v131, v131, v121, v123\n"
    "v_mfma_f32_16x16x32_bf16 a[56:59], a[240:243], v[204:207], a[56:59]\n"
    "v_accvgpr_read_b32 v126, a40\n"
    "v_fma_f32 v134, v134, v121, v123\n"
    "v_fma_f32 v135, v135, v121, v123\n"
    "v_mfma_f32_16x16x32_bf16 a[48:51], a[224:227], v[204:207], a[48:51]\n"
    "ds_read_b128 a[220:223], v248 offset:23552\n"
    "v_accvgpr_read_b32 v139, a47\n"
    "v_mfma_f32_16x16x32_bf16 a[56:59], a[244:247], v[208:211], a[56:59]\n"
    "buffer_store_dwordx4 v[64:67], v244, s[24:27], s77 offen offset:3200 sc1\n"
    "v_accvgpr_read_b32 v136, a35\n"
    "v_accvgpr_read_b32 v127, a44\n"
    "v_fma_f32 v138, v138, v121, v123\n"
    "v_fma_f32 v139, v139, v121, v123\n"
    "v_mfma_f32_16x16x32_bf16 a[56:59], a[176:179], v[4:7], a[56:59]\n"
    "v_fma_f32 v126, v126, v121, v123\n"
    "v_fma_f32 v127, v127, v121, v123\n"
    "v_mfma_f32_16x16x32_bf16 a[56:59], a[180:183], v[8:11], a[56:59]\n"
    "v_cvt_pk_bf16_f32 v124, v124, v125\n"
    "v_cvt_pk_bf16_f32 v125, v126, v127\n"
    "v_mfma_f32_16x16x32_bf16 a[52:55], a[236:239], v[208:211], a[52:55]\n"
    "s_waitcnt lgkmcnt(2)\n"
    "v_fma_f32 v128, v128, v121, v123\n"
    "v_fma_f32 v129, v129, v121, v123\n"
    "v_mfma_f32_16x16x32_bf16 a[48:51], a[228:231], v[208:211], a[48:51]\n"
    "v_cvt_pk_bf16_f32 v132, v132, v133\n"
    "v_cvt_pk_bf16_f32 v126, v128, v129\n"
    "v_mfma_f32_16x16x32_bf16 a[52:55], a[232:235], v[204:207], a[52:55]\n"
    "v_cvt_pk_bf16_f32 v133, v134, v135\n"
    "v_cvt_pk_bf16_f32 v127, v130, v131\n"
    "v_mfma_f32_16x16x32_bf16 a[52:55], a[172:175], v[8:11], a[52:55]\n"
    "s_waitcnt lgkmcnt(0)\n"
    "ds_write_b128 v3, v[124:127] offset:2048\n"
    "v_mfma_f32_16x16x32_bf16 a[60:63], a[220:223], v[72:75], a[60:63]\n"
    "v_fma_f32 v136, v136, v121, v123\n"
    "v_fma_f32 v137, v137, v121, v123\n"
    "v_mfma_f32_16x16x32_bf16 a[52:55], a[168:171], v[4:7], a[52:55]\n"
    "v_cvt_pk_bf16_f32 v135, v138, v139\n"
    "v_cvt_pk_bf16_f32 v134, v136, v137\n"
    "v_mfma_f32_16x16x32_bf16 a[60:63], a[216:219], v[68:71], a[60:63]\n"
    "ds_write_b128 v2, v[132:135] offset:2048\n"
    "v_mfma_f32_16x16x32_bf16 a[48:51], a[160:163], v[4:7], a[48:51]\n"
    "v_accvgpr_read_b32 v146, a57\n"
    "v_mfma_f32_16x16x32_bf16 a[48:51], a[164:167], v[8:11], a[48:51]\n"
    "ds_read_b128 v[128:131], v252 offset:2048\n"
    "v_mfma_f32_16x16x32_bf16 a[60:63], a[252:255], v[208:211], a[60:63]\n"
    "v_accvgpr_read_b32 v141, a52\n"
    "v_accvgpr_read_b32 v153, a55\n"
    "v_mfma_f32_16x16x32_bf16 a[64:67], a[192:195], v[76:79], a[64:67]\n"
    "v_accvgpr_read_b32 v149, a54\n"
    "v_accvgpr_read_b32 v150, a58\n"
    "v_mfma_f32_16x16x32_bf16 a[60:63], a[248:251], v[204:207], a[60:63]\n"
    "ds_read_b128 v[136:139], v252 offset:3072\n"
    "v_accvgpr_read_b32 v142, a56\n"
    "v_mfma_f32_16x16x32_bf16 a[60:63], a[188:191], v[8:11], a[60:63]\n"
    "v_accvgpr_read_b32 v145, a53\n"
    "v_accvgpr_read_b32 v148, a50\n"
    "v_mfma_f32_16x16x32_bf16 a[60:63], a[184:187], v[4:7], a[60:63]\n"
    "v_accvgpr_read_b32 v154, a59\n"
    "v_accvgpr_read_b32 v140, a48\n"
    "v_mfma_f32_16x16x32_bf16 a[64:67], a[196:199], v[80:83], a[64:67]\n"
    "v_fma_f32 v140, v140, v109, v111\n"
    "v_fma_f32 v141, v141, v109, v111\n"
    "v_mfma_f32_16x16x32_bf16 a[68:71], a[204:207], v[80:83], a[68:71]\n"
    "s_nop 1\n"
    "v_accvgpr_read_b32 v143, a60\n"
    "v_mfma_f32_16x16x32_bf16 a[68:71], a[200:203], v[76:79], a[68:71]\n"
    "s_waitcnt lgkmcnt(1)\n"
    "buffer_store_dwordx4 v[128:131], v244, s[24:27], s78 offen offset:128 sc1\n"
    "v_fma_f32 v148, v148, v109, v111\n"
    "v_fma_f32 v149, v149, v109, v111\n"
    "v_accvgpr_read_b32 v144, a49\n"
    "v_cvt_pk_bf16_f32 v148, v148, v149\n"
    "v_mfma_f32_16x16x32_bf16 a[76:79], a[220:223], v[80:83], a[76:79]\n"
    "v_cvt_pk_bf16_f32 v140, v140, v141\n"
    "v_accvgpr_read_b32 v152, a51\n"
    "v_mfma_f32_16x16x32_bf16 a[76:79], a[216:219], v[76:79], a[76:79]\n"
    "v_fma_f32 v142, v142, v109, v111\n"
    "v_fma_f32 v143, v143, v109, v111\n"
    "v_mfma_f32_16x16x32_bf16 a[64:67], a[224:227], v[212:215], a[64:67]\n"
    "v_fma_f32 v144, v144, v109, v111\n"
    "v_fma_f32 v145, v145, v109, v111\n"
    "v_accvgpr_read_b32 v151, a62\n"
    "v_mfma_f32_16x16x32_bf16 a[68:71], a[236:239], v[216:219], a[68:71]\n"
    "v_accvgpr_read_b32 v155, a63\n"
    "s_nop 0\n"
    "v_mfma_f32_16x16x32_bf16 a[68:71], a[232:235], v[212:215], a[68:71]\n"
    "s_waitcnt lgkmcnt(0)\n"
    "buffer_store_dwordx4 v[136:139], v244, s[24:27], s78 offen offset:3200 sc1\n"
    "v_fma_f32 v150, v150, v109, v111\n"
    "v_fma_f32 v151, v151, v109, v111\n"
    "v_cvt_pk_bf16_f32 v141, v142, v143\n"
    "v_accvgpr_read_b32 v147, a61\n"
    "v_mfma_f32_16x16x32_bf16 a[72:75], a[208:211], v[76:79], a[72:75]\n"
    "v_fma_f32 v146, v146, v109, v111\n"
    "v_fma_f32 v147, v147, v109, v111\n"
    "v_mfma_f32_16x16x32_bf16 a[64:67], a[228:231], v[216:219], a[64:67]\n"
    "v_mfma_f32_16x16x32_bf16 a[68:71], a[172:175], v[16:19], a[68:71]\n"
    "v_fma_f32 v154, v154, v109, v111\n"
    "v_fma_f32 v155, v155, v109, v111\n"
    "v_mfma_f32_16x16x32_bf16 a[64:67], a[160:163], v[12:15], a[64:67]\n"
    "v_cvt_pk_bf16_f32 v149, v150, v151\n"
    "v_cvt_pk_bf16_f32 v142, v144, v145\n"
    "v_cvt_pk_bf16_f32 v143, v146, v147\n"
    "v_mfma_f32_16x16x32_bf16 a[68:71], a[168:171], v[12:15], a[68:71]\n"
    "ds_write_b128 v3, v[140:143] offset:4096\n"
    "v_mfma_f32_16x16x32_bf16 a[72:75], a[212:215], v[80:83], a[72:75]\n"
    "v_fma_f32 v152, v152, v109, v111\n"
    "v_fma_f32 v153, v153, v109, v111\n"
    "v_mfma_f32_16x16x32_bf16 a[72:75], a[240:243], v[212:215], a[72:75]\n"
    "v_cvt_pk_bf16_f32 v150, v152, v153\n"
    "v_cvt_pk_bf16_f32 v151, v154, v155\n"
    "v_mfma_f32_16x16x32_bf16 a[92:95], a[220:223], v[88:91], a[92:95]\n"
    "ds_write_b128 v2, v[148:151] offset:4096\n"
    "v_mfma_f32_16x16x32_bf16 a[92:95], a[216:219], v[84:87], a[92:95]\n"
    "v_accvgpr_read_b32 v161, a69\n"
    "v_fma_f32 v250, v109, s74, v250\n"
    "v_mfma_f32_16x16x32_bf16 a[64:67], a[164:167], v[16:19], a[64:67]\n"
    "v_fma_f32 v250, |v114|, 4.0, v250\n"
    "v_mfma_f32_16x16x32_bf16 a[72:75], a[244:247], v[216:219], a[72:75]\n"
    "ds_read_b128 v[144:147], v252 offset:4096\n"
    "v_mfma_f32_16x16x32_bf16 a[72:75], a[176:179], v[12:15], a[72:75]\n"
    "ds_read_b128 v[152:155], v252 offset:5120\n"
    "v_accvgpr_read_b32 v165, a70\n"
    "v_mfma_f32_16x16x32_bf16 a[72:75], a[180:183], v[16:19], a[72:75]\n"
    "s_nop 0\n"
    "v_accvgpr_read_b32 v156, a64\n"
    "v_accvgpr_read_b32 v160, a65\n"
    "v_mfma_f32_16x16x32_bf16 a[76:79], a[252:255], v[216:219], a[76:79]\n"
    "v_fma_f32 v160, v160, v113, v115\n"
    "v_fma_f32 v161, v161, v113, v115\n"
    "v_mfma_f32_16x16x32_bf16 a[84:87], a[204:207], v[88:91], a[84:87]\n"
    "s_nop 0\n"
    "v_accvgpr_read_b32 v162, a73\n"
    "v_accvgpr_read_b32 v169, a71\n"
    "v_mfma_f32_16x16x32_bf16 a[80:83], a[192:195], v[84:87], a[80:83]\n"
    "v_fma_f32 v250, v112, 4.0, v250\n"
    "s_nop 0\n"
    "v_mfma_f32_16x16x32_bf16 a[76:79], a[248:251], v[212:215], a[76:79]\n"
    "v_accvgpr_read_b32 v166, a74\n"
    "v_accvgpr_read_b32 v157, a68\n"
    "v_mfma_f32_16x16x32_bf16 a[76:79], a[188:191], v[16:19], a[76:79]\n"
    "v_accvgpr_read_b32 v164, a66\n"
    "v_accvgpr_read_b32 v168, a67\n"
    "v_mfma_f32_16x16x32_bf16 a[76:79], a[184:187], v[12:15], a[76:79]\n"
    "s_waitcnt lgkmcnt(1)\n"
    "buffer_store_dwordx4 v[144:147], v244, s[24:27], s75 offen offset:256 sc1\n"
    "v_fma_f32 v164, v164, v113, v115\n"
    "v_fma_f32 v165, v165, v113, v115\n"
    "v_accvgpr_read_b32 v158, a72\n"
    "v_fma_f32 v156, v156, v113, v115\n"
    "v_fma_f32 v157, v157, v113, v115\n"
    "v_mfma_f32_16x16x32_bf16 a[80:83], a[196:199], v[88:91], a[80:83]\n"
    "v_accvgpr_read_b32 v167, a78\n"
    "v_cvt_pk_bf16_f32 v156, v156, v157\n"
    "v_fma_f32 v250, v113, s74, v250\n"
    "v_mfma_f32_16x16x32_bf16 a[88:91], a[208:211], v[84:87], a[88:91]\n"
    "s_nop 0\n"
    "v_accvgpr_read_b32 v159, a76\n"
    "v_fma_f32 v166, v166, v113, v115\n"
    "v_fma_f32 v167, v167, v113, v115\n"
    "v_mfma_f32_16x16x32_bf16 a[84:87], a[200:203], v[84:87], a[84:87]\n"
    "s_waitcnt lgkmcnt(0)\n"
    "buffer_store_dwordx4 v[152:155], v244, s[24:27], s75 offen offset:3328 sc1\n"
    "v_fma_f32 v158, v158, v113, v115\n"
    "v_fma_f32 v159, v159, v113, v115\n"
    "v_accvgpr_read_b32 v170, a75\n"
    "v_accvgpr_read_b32 v171, a79\n"
    "v_mfma_f32_16x16x32_bf16 a[84:87], a[236:239], v[224:227], a[84:87]\n"
    "v_cvt_pk_bf16_f32 v157, v158, v159\n"
    "v_cvt_pk_bf16_f32 v158, v160, v161\n"
    "v_mfma_f32_16x16x32_bf16 a[84:87], a[232:235], v[220:223], a[84:87]\n"
    "v_cvt_pk_bf16_f32 v164, v164, v165\n"
    "v_accvgpr_read_b32 v163, a77\n"
    "v_mfma_f32_16x16x32_bf16 a[80:83], a[224:227], v[220:223], a[80:83]\n"
    "v_fma_f32 v162, v162, v113, v115\n"
    "v_fma_f32 v163, v163, v113, v115\n"
    "v_mfma_f32_16x16x32_bf16 a[88:91], a[212:215], v[88:91], a[88:91]\n"
    "v_cvt_pk_bf16_f32 v159, v162, v163\n"
    "v_cvt_pk_bf16_f32 v165, v166, v167\n"
    "v_mfma_f32_16x16x32_bf16 a[84:87], a[172:175], v[24:27], a[84:87]\n"
    "ds_write_b128 v3, v[156:159] offset:0\n"
    "v_mfma_f32_16x16x32_bf16 a[84:87], a[168:171], v[20:23], a[84:87]\n"
    "v_fma_f32 v168, v168, v113, v115\n"
    "v_fma_f32 v169, v169, v113, v115\n"
    "v_mfma_f32_16x16x32_bf16 a[92:95], a[252:255], v[224:227], a[92:95]\n"
    "v_fma_f32 v170, v170, v113, v115\n"
    "v_fma_f32 v171, v171, v113, v115\n"
    "v_fma_f32 v250, |v118|, 4.0, v250\n"
    "v_cvt_pk_bf16_f32 v167, v170, v171\n"
    "v_cvt_pk_bf16_f32 v166, v168, v169\n"
    "v_mfma_f32_16x16x32_bf16 a[80:83], a[228:231], v[224:227], a[80:83]\n"
    "ds_write_b128 v2, v[164:167] offset:0\n"
    "v_mfma_f32_16x16x32_bf16 a[80:83], a[160:163], v[20:23], a[80:83]\n"
    "v_mfma_f32_16x16x32_bf16 a[92:95], a[248:251], v[220:223], a[92:95]\n"
    "ds_read_b128 v[160:163], v252 offset:0\n"
    "v_mfma_f32_16x16x32_bf16 a[80:83], a[164:167], v[24:27], a[80:83]\n"
    "ds_read_b128 v[168:171], v252 offset:1024\n"
    "v_accvgpr_read_b32 v173, a84\n"
    "v_mfma_f32_16x16x32_bf16 a[92:95], a[188:191], v[24:27], a[92:95]\n"
    "v_accvgpr_read_b32 v177, a85\n"
    "v_mfma_f32_16x16x32_bf16 a[88:91], a[240:243], v[220:223], a[88:91]\n"
    "v_mfma_f32_16x16x32_bf16 a[88:91], a[244:247], v[224:227], a[88:91]\n"
    "s_nop 1\n"
    "v_accvgpr_read_b32 v176, a81\n"
    "v_mfma_f32_16x16x32_bf16 a[92:95], a[184:187], v[20:23], a[92:95]\n"
    "v_accvgpr_read_b32 v172, a80\n"
    "v_accvgpr_read_b32 v184, a83\n"
    "v_mfma_f32_16x16x32_bf16 a[88:91], a[176:179], v[20:23], a[88:91]\n"
    "v_fma_f32 v172, v172, v117, v119\n"
    "v_fma_f32 v173, v173, v117, v119\n"
    "v_mfma_f32_16x16x32_bf16 a[88:91], a[180:183], v[24:27], a[88:91]\n"
    "v_fma_f32 v176, v176, v117, v119\n"
    "v_fma_f32 v177, v177, v117, v119\n"
    "v_mfma_f32_16x16x32_bf16 a[96:99], a[192:195], v[92:95], a[96:99]\n"
    "s_waitcnt lgkmcnt(1)\n"
    "buffer_store_dwordx4 v[160:163], v244, s[24:27], s76 offen offset:256 sc1\n"
    "v_accvgpr_read_b32 v175, a92\n"
    "v_accvgpr_read_b32 v180, a82\n"
    "v_accvgpr_read_b32 v181, a86\n"
    "v_cvt_pk_bf16_f32 v172, v172, v173\n"
    "v_mfma_f32_16x16x32_bf16 a[108:111], a[220:223], v[96:99], a[108:111]\n"
    "v_accvgpr_read_b32 v179, a93\n"
    "v_accvgpr_read_b32 v174, a88\n"
    "v_mfma_f32_16x16x32_bf16 a[108:111], a[216:219], v[92:95], a[108:111]\n"
    "v_fma_f32 v250, v116, 4.0, v250\n"
    "v_accvgpr_read_b32 v183, a94\n"
    "v_mfma_f32_16x16x32_bf16 a[96:99], a[196:199], v[96:99], a[96:99]\n"
    "v_accvgpr_read_b32 v187, a95\n"
    "v_accvgpr_read_b32 v178, a89\n"
    "v_mfma_f32_16x16x32_bf16 a[100:103], a[204:207], v[96:99], a[100:103]\n"
    "v_mfma_f32_16x16x32_bf16 a[104:107], a[208:211], v[92:95], a[104:107]\n"
    "s_nop 0\n"
    "v_fma_f32 v174, v174, v117, v119\n"
    "v_fma_f32 v175, v175, v117, v119\n"
    "v_mfma_f32_16x16x32_bf16 a[108:111], a[252:255], v[232:235], a[108:111]\n"
    "s_waitcnt lgkmcnt(0)\n"
    "buffer_store_dwordx4 v[168:171], v244, s[24:27], s76 offen offset:3328 sc1\n"
    "v_cvt_pk_bf16_f32 v173, v174, v175\n"
    "v_accvgpr_read_b32 v182, a90\n"
    "v_accvgpr_read_b32 v185, a87\n"
    "v_cvt_pk_bf16_f32 v174, v176, v177\n"
    "v_pk_fma_f32 v[178:179], v[178:179], v[116:117], v[118:119] op_sel:[0,1,1] op_sel_hi:[1,1,1]\n"
    "v_accvgpr_read_b32 v186, a91\n"
    "v_cvt_pk_bf16_f32 v175, v178, v179\n"
    "v_fma_f32 v186, v186, v117, v119\n"
    "v_fma_f32 v187, v187, v117, v119\n"
    "v_pk_fma_f32 v[180:181], v[180:181], v[116:117], v[118:119] op_sel:[0,1,1] op_sel_hi:[1,1,1]\n"
    "v_cmp_eq_u64 s[56:57], v[236:237], v[238:239]\n"
    "v_fma_f32 v182, v182, v117, v119\n"
    "v_fma_f32 v183, v183, v117, v119\n"
    "v_mfma_f32_16x16x32_bf16 a[108:111], a[248:251], v[228:231], a[108:111]\n"
    "ds_write_b128 v3, v[172:175] offset:2048\n"
    "v_cvt_pk_bf16_f32 v180, v180, v181\n"
    "v_mfma_f32_16x16x32_bf16 a[96:99], a[224:227], v[228:231], a[96:99]\n"
    "v_fma_f32 v184, v184, v117, v119\n"
    "v_fma_f32 v185, v185, v117, v119\n"
    "v_cvt_pk_bf16_f32 v181, v182, v183\n"
    "v_mfma_f32_16x16x32_bf16 a[104:107], a[212:215], v[96:99], a[104:107]\n"
    "v_cvt_pk_bf16_f32 v182, v184, v185\n"
    "v_cvt_pk_bf16_f32 v183, v186, v187\n"
    "v_mfma_f32_16x16x32_bf16 a[96:99], a[228:231], v[232:235], a[96:99]\n"
    "ds_write_b128 v2, v[180:183] offset:2048\n"
    "v_mfma_f32_16x16x32_bf16 a[96:99], a[160:163], v[28:31], a[96:99]\n"
    "v_mfma_f32_16x16x32_bf16 a[104:107], a[240:243], v[228:231], a[104:107]\n"
    "ds_read_b128 v[176:179], v252 offset:2048\n"
    "v_mfma_f32_16x16x32_bf16 a[104:107], a[244:247], v[232:235], a[104:107]\n"
    "v_mfma_f32_16x16x32_bf16 a[100:103], a[200:203], v[92:95], a[100:103]\n"
    "v_mfma_f32_16x16x32_bf16 a[104:107], a[176:179], v[28:31], a[104:107]\n"
    "v_mfma_f32_16x16x32_bf16 a[104:107], a[180:183], v[32:35], a[104:107]\n"
    "v_mfma_f32_16x16x32_bf16 a[108:111], a[188:191], v[32:35], a[108:111]\n"
    "s_or_b64 s[56:57], s[56:57], s[34:35]\n"
    "ds_read_b128 v[184:187], v252 offset:3072\n"
    "v_mfma_f32_16x16x32_bf16 a[100:103], a[236:239], v[232:235], a[100:103]\n"
    "v_mfma_f32_16x16x32_bf16 a[96:99], a[164:167], v[32:35], a[96:99]\n"
    "v_mfma_f32_16x16x32_bf16 a[100:103], a[232:235], v[228:231], a[100:103]\n"
    "s_waitcnt lgkmcnt(1)\n"
    "buffer_store_dwordx4 v[176:179], v244, s[24:27], s77 offen offset:256 sc1\n"
    "v_accvgpr_read_b32 v198, a106\n"
    "v_fma_f32 v250, v117, s74, v250\n"
    "v_fma_f32 v250, |v122|, 4.0, v250\n"
    "v_accvgpr_read_b32 v194, a105\n"
    "s_nop 0\n"
    "v_accvgpr_read_b32 v196, a98\n"
    "v_mfma_f32_16x16x32_bf16 a[108:111], a[184:187], v[28:31], a[108:111]\n"
    "v_accvgpr_read_b32 v202, a107\n"
    "v_mfma_f32_16x16x32_bf16 a[100:103], a[172:175], v[32:35], a[100:103]\n"
    "s_nop 0\n"
    "v_mfma_f32_16x16x32_bf16 a[100:103], a[168:171], v[28:31], a[100:103]\n"
    "s_waitcnt lgkmcnt(0)\n"
    "buffer_store_dwordx4 v[184:187], v244, s[24:27], s77 offen offset:3328 sc1\n"
    "v_accvgpr_read_b32 v190, a104\n"
    "s_nop 1\n"
    "v_accvgpr_read_b32 v191, a108\n"
    "v_accvgpr_read_b32 v199, a110\n"
    "v_fma_f32 v250, v120, 4.0, v250\n"
    "v_accvgpr_read_b32 v197, a102\n"
    "v_accvgpr_read_b32 v200, a99\n"
    "v_accvgpr_read_b32 v195, a109\n"
    "v_accvgpr_read_b32 v188, a96\n"
    "v_pk_fma_f32 v[190:191], v[190:191], v[120:121], v[122:123] op_sel:[0,1,1] op_sel_hi:[1,1,1]\n"
    "v_accvgpr_read_b32 v201, a103\n"
    "v_accvgpr_read_b32 v203, a111\n"
    "v_accvgpr_read_b32 v189, a100\n"
    "v_pk_fma_f32 v[194:195], v[194:195], v[120:121], v[122:123] op_sel:[0,1,1] op_sel_hi:[1,1,1]\n"
    "v_fma_f32 v198, v198, v121, v123\n"
    "v_fma_f32 v199, v199, v121, v123\n"
    "v_fma_f32 v188, v188, v121, v123\n"
    "v_fma_f32 v189, v189, v121, v123\n"
    "v_accvgpr_read_b32 v192, a97\n"
    "v_cvt_pk_bf16_f32 v188, v188, v189\n"
    "v_accvgpr_read_b32 v193, a101\n"
    "v_cvt_pk_bf16_f32 v189, v190, v191\n"
    "v_cvt_pk_bf16_f32 v191, v194, v195\n"
    "v_fma_f32 v192, v192, v121, v123\n"
    "v_fma_f32 v193, v193, v121, v123\n"
    "v_cvt_pk_bf16_f32 v190, v192, v193\n"
    "ds_write_b128 v3, v[188:191] offset:4096\n"
    "v_pk_fma_f32 v[196:197], v[196:197], v[120:121], v[122:123] op_sel:[0,1,1] op_sel_hi:[1,1,1]\n"
    "v_cvt_pk_bf16_f32 v196, v196, v197\n"
    "v_cvt_pk_bf16_f32 v197, v198, v199\n"
    "v_pk_fma_f32 v[200:201], v[200:201], v[120:121], v[122:123] op_sel:[0,1,1] op_sel_hi:[1,1,1]\n"
    "v_fma_f32 v250, v121, s74, v250\n"
    "v_pk_fma_f32 v[202:203], v[202:203], v[120:121], v[122:123] op_sel:[0,1,1] op_sel_hi:[1,1,1]\n"
    "v_cvt_pk_bf16_f32 v199, v202, v203\n"
    "v_cvt_pk_bf16_f32 v198, v200, v201\n"
    "ds_write_b128 v2, v[196:199] offset:4096\n"
    "v_cmp_nlt_f32 s[54:55], |v250|, s73\n"
    "ds_read_b128 v[200:203], v252 offset:5120\n"
    "ds_read_b128 v[192:195], v252 offset:4096\n"
    "s_waitcnt lgkmcnt(1)\n"
    "buffer_store_dwordx4 v[200:203], v244, s[24:27], s78 offen offset:3328 sc1\n"
    "s_or_b64 s[54:55], s[54:55], s[56:57]\n"
    "s_waitcnt lgkmcnt(0)\n"
    "buffer_store_dwordx4 v[192:195], v244, s[24:27], s78 offen offset:256 sc1\n"
    "s_or_b32 %0, s54, s55\n"
    "s_cmp_eq_u32 %0, 0\n"
    "s_cbranch_scc1 .Lfin\n"
    "s_waitcnt vmcnt(0)\n"
    ".Lfin:\n"
    ".Lend:\n"
    ".Lend2:\n"
    : "=&s"(bad), "+s"(ka) : "s"(b), "v"(tid)
    : "memory", "vcc", "s4", "s5", "s6", "s7", "s8", "s9", "s10", "s11", "s12", "s13", "s14", "s15", "s16", "s17", "s18", "s19", "s20", "s21", "s22", "s23", "s24", "s25", "s26", "s27", "s28", "s29", "s30", "s31", "s34", "s35", "s36", "s37", "s38", "s39", "s40", "s41", "s42", "s43", "s44", "s45", "s46", "s47", "s48", "s49", "s50", "s51", "s52", "s53", "s54", "s55", "s56", "s57", "s58", "s59", "s60", "s61", "s62", "s63", "s64", "s65", "s66", "s67", "s68", "s69", "s70", "s71", "s72", "s73", "s74", "s75", "s76", "s77", "s78", "s79", "s80", "s81", "s82", "s83", "s84", "s85", "s86", "s87", "s88", "s89", "s90", "s91", "s92", "s93", "s94", "s95", "s96", "s97", "s98", "s99", "v1", "v2", "v3", "v4", "v5", "v6", "v7", "v8", "v9", "v10", "v11", "v12", "v13", "v14", "v15", "v16", "v17", "v18", "v19", "v20", "v21", "v22", "v23", "v24", "v25", "v26", "v27", "v28", "v29", "v30", "v31", "v32", "v33", "v34", "v35", "v36", "v37", "v38", "v39", "v40", "v41", "v42", "v43", "v44", "v45", "v46", "v47", "v48", "v49", "v50", "v51", "v52", "v53", "v54", "v55", "v56", "v57", "v58", "v59", "v60", "v61", "v62", "v63", "v64", "v65", "v66", "v67", "v68", "v69", "v70", "v71", "v72", "v73", "v74", "v75", "v76", "v77", "v78", "v79", "v80", "v81", "v82", "v83", "v84", "v85", "v86", "v87", "v88", "v89", "v90", "v91", "v92", "v93", "v94", "v95", "v96", "v97", "v98", "v99", "v100", "v101", "v102", "v103", "v104", "v105", "v106", "v107", "v108", "v109", "v110", "v111", "v112", "v113", "v114", "v115", "v116", "v117", "v118", "v119", "v120", "v121", "v122", "v123", "v124", "v125", "v126", "v127", "v128", "v129", "v130", "v131", "v132", "v133", "v134", "v135", "v136", "v137", "v138", "v139", "v140", "v141", "v142", "v143", "v144", "v145", "v146", "v147", "v148", "v149", "v150", "v151", "v152", "v153", "v154", "v155", "v156", "v157", "v158", "v159", "v160", "v161", "v162", "v163", "v164", "v165", "v166", "v167", "v168", "v169", "v170", "v171", "v172", "v173", "v174", "v175", "v176", "v177", "v178", "v179", "v180", "v181", "v182", "v183", "v184", "v185", "v186", "v187", "v188", "v189", "v190", "v191", "v192", "v193", "v194", "v195", "v196", "v197", "v198", "v199", "v200", "v201", "v202", "v203", "v204", "v205", "v206", "v207", "v208", "v209", "v210", "v211", "v212", "v213", "v214", "v215", "v216", "v217", "v218", "v219", "v220", "v221", "v222", "v223", "v224", "v225", "v226", "v227", "v228", "v229", "v230", "v231", "v232", "v233", "v234", "v235", "v236", "v237", "v238", "v239", "v240", "v241", "v242", "v243", "v244", "v245", "v246", "v247", "v248", "v249", "v250", "v251", "v252", "v253", "v254", "v255", "a0", "a1", "a2", "a3", "a4", "a5", "a6", "a7", "a8", "a9", "a10", "a11", "a12", "a13", "a14", "a15", "a16", "a17", "a18", "a19", "a20", "a21", "a22", "a23", "a24", "a25", "a26", "a27", "a28", "a29", "a30", "a31", "a32", "a33", "a34", "a35", "a36", "a37", "a38", "a39", "a40", "a41", "a42", "a43", "a44", "a45", "a46", "a47", "a48", "a49", "a50", "a51", "a52", "a53", "a54", "a55", "a56", "a57", "a58", "a59", "a60", "a61", "a62", "a63", "a64", "a65", "a66", "a67", "a68", "a69", "a70", "a71", "a72", "a73", "a74", "a75", "a76", "a77", "a78", "a79", "a80", "a81", "a82", "a83", "a84", "a85", "a86", "a87", "a88", "a89", "a90", "a91", "a92", "a93", "a94", "a95", "a96", "a97", "a98", "a99", "a100", "a101", "a102", "a103", "a104", "a105", "a106", "a107", "a108", "a109", "a110", "a111", "a112", "a113", "a114", "a115", "a116", "a117", "a118", "a119", "a120", "a121", "a122", "a123", "a124", "a125", "a126", "a127", "a128", "a129", "a130", "a131", "a132", "a133", "a134", "a135", "a136", "a137", "a138", "a139", "a140", "a141", "a142", "a143", "a144", "a145", "a146", "a147", "a148", "a149", "a150", "a151", "a152", "a153", "a154", "a155", "a156", "a157", "a158", "a159", "a160", "a161", "a162", "a163", "a164", "a165", "a166", "a167", "a168", "a169", "a170", "a171", "a172", "a173", "a174", "a175", "a176", "a177", "a178", "a179", "a180", "a181", "a182", "a183", "a184", "a185", "a186", "a187", "a188", "a189", "a190", "a191", "a192", "a193", "a194", "a195", "a196", "a197", "a198", "a199", "a200", "a201", "a202", "a203", "a204", "a205", "a206", "a207", "a208", "a209", "a210", "a211", "a212", "a213", "a214", "a215", "a216", "a217", "a218", "a219", "a220", "a221", "a222", "a223", "a224", "a225", "a226", "a227", "a228", "a229", "a230", "a231", "a232", "a233", "a234", "a235", "a236", "a237", "a238", "a239", "a240", "a241", "a242", "a243", "a244", "a245", "a246", "a247", "a248", "a249", "a250", "a251", "a252", "a253", "a254", "a255");
  if (bad) {
    slow_rows((const Args*)ka, (b & 7) * 4 + ((b >> 3) & 3), (b >> 5) * 256 + (tid & 192), tid & 63);
  }
}

extern "C" void kernel_launch(void* const* d_in, const int* in_sizes, int n_in,
                              void* d_out, int out_size, void* d_ws, size_t ws_size,
                              hipStream_t stream) {
  (void)hipFuncSetAttribute((const void*)attn_fwd, hipFuncAttributeMaxDynamicSharedMemorySize, SHM);
  attn_fwd<<<256, 256, SHM, stream>>>((const unsigned short*)d_in[0], (const int*)d_in[1], (const int*)d_in[2], (const unsigned short*)d_in[3], (const unsigned short*)d_in[4],
                                      (const unsigned short*)d_in[5], (const unsigned short*)d_in[6], (char*)d_ws, (unsigned short*)d_out);
}
```

```cpp
#include <hip/hip_runtime.h>

constexpr int D = 192, NH = 32, S = 2048;
constexpr int SHM = 155968;

struct Args { const unsigned short* q; const int* qk; const int* qv; const unsigned short* ksc; const unsigned short* kzr;
              const unsigned short* vsc; const unsigned short* vzr; char* ws; unsigned short* o; };

__device__ __forceinline__ float bf2f(unsigned short x) { return __uint_as_float((unsigned)x << 16); }
__device__ __forceinline__ float rbf(float x) { return (float)(__bf16)x; }

__device__ __forceinline__ void slow_rows(const Args* a, unsigned h, unsigned row0, unsigned lane) {
  const unsigned short* q = a->q + ((size_t)h * S + row0) * D;
  unsigned short* o = a->o + ((size_t)h * S + row0) * D;
  const int* qk = a->qk + (size_t)h * S * D; const int* qv = a->qv + (size_t)h * S * D;
  const unsigned short* ks = a->ksc + h * 32 * D; const unsigned short* kz = a->kzr + h * 32 * D;
  const unsigned short* vs = a->vsc + h * S; const unsigned short* vz = a->vzr + h * S;
#pragma unroll 1
  for (int n = 0; n < 64; ++n) {
    const int r = (n >> 4) * 512 + (n & 15);
    float m = -INFINITY, l = 0.f, a0 = 0.f, a1 = 0.f, a2 = 0.f;
#pragma unroll 1
    for (int t = 0; t < 32; ++t) {
      const int* kr = qk + (size_t)(t * 64 + lane) * D;
      float s = 0.f;
#pragma unroll 4
      for (int d = 0; d < D; ++d) s = fmaf(bf2f(q[r * D + d]), rbf(rbf(rbf((float)kr[d]) - bf2f(kz[t * D + d])) * bf2f(ks[t * D + d])), s);
      s *= 0.07216878364870322f;
      float mx = s;
      for (int x = 32; x; x >>= 1) mx = fmaxf(mx, __shfl_xor(mx, x));
      const float mn = fmaxf(m, mx), c = __expf(m - mn), p = __expf(s - mn);
      float ps = p;
      for (int x = 32; x; x >>= 1) ps += __shfl_xor(ps, x);
      l = l * c + ps; a0 *= c; a1 *= c; a2 *= c; m = mn;
#pragma unroll 2
      for (int k = 0; k < 64; ++k) {
        const float pk = __shfl(p, k), z = bf2f(vz[t * 64 + k]), sc = bf2f(vs[t * 64 + k]);
        const int* vr = qv + (size_t)(t * 64 + k) * D + lane;
        a0 = fmaf(pk, rbf(rbf(rbf((float)vr[0]) - z) * sc), a0); a1 = fmaf(pk, rbf(rbf(rbf((float)vr[64]) - z) * sc), a1); a2 = fmaf(pk, rbf(rbf(rbf((float)vr[128]) - z) * sc), a2);
      }
    }
    const float i = 1.f / l;
    o[r * D + lane] = __builtin_bit_cast(unsigned short, (__bf16)(a0 * i)); o[r * D + 64 + lane] = __builtin_bit_cast(unsigned short, (__bf16)(a1 * i));
    o[r * D + 128 + lane] = __builtin_bit_cast(unsigned short, (__bf16)(a2 * i));
  }
}

__global__ __launch_bounds__(256, 1) __attribute__((aligned(4096))) void attn_fwd(const unsigned short* q, const int* qk, const int* qv, const unsigned short* ksc, const unsigned short* kzr,
                                                   const unsigned short* vsc, const unsigned short* vzr, char* ws, unsigned short* o) {
  unsigned long long ka = (unsigned long long)__builtin_amdgcn_kernarg_segment_ptr();
  unsigned bad, b = blockIdx.x;
  const unsigned tid = threadIdx.x;
  asm volatile(
    "s_load_dwordx16 s[4:19], %1, 0\n"
    "s_load_dwordx2 s[24:25], %1, 64\n"
    "s_getpc_b64 s[54:55]\n"
    ".Lpc:\n"
    "s_and_b32 s72, %2, 24\n"
    "s_lshl_b32 s72, s72, 4\n"
    "v_lshl_add_u32 v246, %3, 9, s72\n"
    "v_min_u32 v246, .Lend-.Lpc-4, v246\n"
    "global_load_dword v246, v246, s[54:55]\n"
    "s_getreg_b32 s72, hwreg(HW_REG_XCC_ID, 0, 1)\n"
    "s_cmp_eq_u32 s72, 1\n"
    "s_cbranch_scc1 .Ldly\n"
    "s_sleep 38\n"
    ".Ldly:\n"
    "v_readfirstlane_b32 s84, %3\n"
    "v_and_b32 v156, 63, %3\n"
    "s_and_b32 s52, %2, 7\n"
    "s_bfe_u32 s72, %2, 0x20003\n"
    "s_lshr_b32 s53, %2, 5\n"
    "s_lshl_b32 s52, s52, 2\n"
    "s_lshr_b32 s84, s84, 6\n"
    "s_or_b32 s52, s52, s72\n"
    "v_lshlrev_b32 v1, 4, v156\n"
    "s_mov_b32 s30, 1572864\n"
    "s_mov_b32 s31, 0x20000\n"
    "s_mov_b32 s42, 1572864\n"
    "s_mov_b32 s43, 0x20000\n"
    "s_mov_b32 s46, 12288\n"
    "s_mov_b32 s47, 0x20000\n"
    "s_mov_b32 s90, 12288\n"
    "s_mov_b32 s91, 0x20000\n"
    "s_lshr_b32 s79, s84, 1\n"
    "s_and_b32 s59, s84, 1\n"
    "s_cmp_ge_u32 s53, 6\n"
    "s_cselect_b32 s83, 1, 0\n"
    "s_or_b32 s83, s83, s84\n"
    "s_sub_u32 s58, 2, s79\n"
    "s_mul_i32 s71, s58, 49152\n"
    "s_mul_i32 s72, s52, 1572864\n"
    "s_mul_i32 s73, s52, 12288\n"
    "s_mul_i32 s82, s52, 3\n"
    "s_and_b32 s82, s82, 31\n"
    "s_mul_i32 s82, s82, 23\n"
    "s_lshr_b32 s82, s82, 5\n"
    "s_add_u32 s82, s82, 1\n"
    "s_mul_i32 s67, s82, 49152\n"
    "s_add_u32 s30, s30, s67\n"
    "s_add_u32 s42, s42, s67\n"
    "v_lshrrev_b32 v157, 2, v156\n"
    "v_and_b32 v158, 3, v156\n"
    "s_mul_i32 s35, s84, 3072\n"
    "s_bitcmp1_b32 s83, 0\n"
    "s_cbranch_scc1 .Lf2b\n"
    "v_and_b32 v159, 32, v156\n"
    "v_mul_u32_u24 v242, 376, v159\n"
    "v_and_b32 v159, 16, v156\n"
    "v_mul_u32_u24 v159, 88, v159\n"
    "v_lshl_add_u32 v242, v156, 3, v242\n"
    "v_add_u32 v242, v159, v242\n"
    "s_lshl_b32 s63, s53, 7\n"
    "s_add_u32 s63, s63, s67\n"
    "s_and_b32 s81, s58, 1\n"
    "s_add_u32 s81, s81, s82\n"
    "v_lshl_add_u32 v160, v157, 1, s81\n"
    "v_and_b32 v160, 31, v160\n"
    "v_mul_u32_u24 v160, 384, v160\n"
    "v_lshl_add_u32 v160, v158, 4, v160\n"
    "s_lshl_b32 s81, s53, 6\n"
    "s_add_u32 s75, s71, s63\n"
    "s_add_u32 s76, s75, 6144\n"
    "s_add_u32 s77, s75, 24576\n"
    "s_add_u32 s78, s75, 30720\n"
    "s_waitcnt lgkmcnt(0)\n"
    "s_add_u32 s44, s10, s73\n"
    "s_addc_u32 s45, s11, 0\n"
    "s_add_u32 s88, s12, s73\n"
    "s_addc_u32 s89, s13, 0\n"
    "buffer_load_dwordx4 v[4:7], v1, s[44:47], s35 offen offset:0\n"
    "buffer_load_dwordx4 v[8:11], v1, s[44:47], s35 offen offset:1024\n"
    "buffer_load_dwordx4 v[12:15], v1, s[44:47], s35 offen offset:2048\n"
    "buffer_load_dwordx4 v[16:19], v1, s[88:91], 0 offen offset:0\n"
    "buffer_load_dwordx4 v[164:167], v160, s[44:47], s81 offen offset:0\n"
    "buffer_load_dwordx4 v[168:171], v160, s[88:91], s81 offen offset:0\n"
    "s_add_u32 s28, s6, s72\n"
    "s_addc_u32 s29, s7, 0\n"
    "buffer_load_dwordx2 v[204:205], v242, s[28:31], s75 offen offset:0 nt\n"
    "buffer_load_dwordx2 v[206:207], v242, s[28:31], s75 offen offset:768 nt\n"
    "buffer_load_dwordx2 v[208:209], v242, s[28:31], s75 offen offset:3072 nt\n"
    "buffer_load_dwordx2 v[210:211], v242, s[28:31], s75 offen offset:3840 nt\n"
    "buffer_load_dwordx2 v[212:213], v242, s[28:31], s76 offen offset:0 nt\n"
    "buffer_load_dwordx2 v[214:215], v242, s[28:31], s76 offen offset:768 nt\n"
    "buffer_load_dwordx2 v[216:217], v242, s[28:31], s76 offen offset:3072 nt\n"
    "buffer_load_dwordx2 v[218:219], v242, s[28:31], s76 offen offset:3840 nt\n"
    "buffer_load_dwordx2 v[220:221], v242, s[28:31], s77 offen offset:0 nt\n"
    "buffer_load_dwordx2 v[222:223], v242, s[28:31], s77 offen offset:768 nt\n"
    "buffer_load_dwordx2 v[224:225], v242, s[28:31], s77 offen offset:3072 nt\n"
    "buffer_load_dwordx2 v[226:227], v242, s[28:31], s77 offen offset:3840 nt\n"
    "buffer_load_dwordx2 v[228:229], v242, s[28:31], s78 offen offset:0 nt\n"
    "buffer_load_dwordx2 v[230:231], v242, s[28:31], s78 offen offset:768 nt\n"
    "buffer_load_dwordx2 v[232:233], v242, s[28:31], s78 offen offset:3072 nt\n"
    "buffer_load_dwordx2 v[234:235], v242, s[28:31], s78 offen offset:3840 nt\n"
    "s_add_u32 s75, s71, s63\n"
    "s_add_u32 s75, s75, 98304\n"
    "s_add_u32 s76, s75, 6144\n"
    "s_add_u32 s77, s75, 24576\n"
    "s_add_u32 s78, s75, 30720\n"
    "buffer_load_dwordx2 v[36:37], v242, s[28:31], s75 offen offset:0 nt\n"
    "buffer_load_dwordx2 v[38:39], v242, s[28:31], s75 offen offset:768 nt\n"
    "v_lshrrev_b32 v162, 1, v156\n"
    "v_and_b32 v162, 16, v162\n"
    "v_bfe_u32 v159, v156, 4, 1\n"
    "v_or_b32 v162, v162, v159\n"
    "v_and_or_b32 v162, v157, 2, v162\n"
    "v_mul_u32_u24 v162, 768, v162\n"
    "v_and_b32 v163, 0x70, v1\n"
    "v_add_u32 v162, v162, v163\n"
    "s_branch .Lf2e\n"
    ".Lf2b:\n"
    "v_and_b32 v159, 48, v156\n"
    "v_lshlrev_b32 v242, 2, v159\n"
    "v_lshl_add_u32 v243, v158, 4, v242\n"
    "v_and_b32 v159, 12, v156\n"
    "v_mul_u32_u24 v242, 384, v159\n"
    "v_add_u32 v242, v243, v242\n"
    "v_add_u32 v243, v243, v159\n"
    "s_mul_i32 s63, s53, 6144\n"
    "s_sub_u32 s63, s63, 49152\n"
    "s_add_u32 s63, s63, s67\n"
    "s_cmp_eq_u32 s59, 0\n"
    "s_cselect_b32 s63, 0x7f000000, s63\n"
    "s_add_u32 s75, s71, s63\n"
    "s_waitcnt lgkmcnt(0)\n"
    "s_add_u32 s44, s10, s73\n"
    "s_addc_u32 s45, s11, 0\n"
    "s_add_u32 s88, s12, s73\n"
    "s_addc_u32 s89, s13, 0\n"
    "buffer_load_dwordx4 v[4:7], v1, s[44:47], s35 offen offset:0\n"
    "buffer_load_dwordx4 v[8:11], v1, s[44:47], s35 offen offset:1024\n"
    "buffer_load_dwordx4 v[12:15], v1, s[44:47], s35 offen offset:2048\n"
    "buffer_load_dwordx4 v[16:19], v1, s[88:91], 0 offen offset:0\n"
    "s_add_u32 s40, s8, s72\n"
    "s_addc_u32 s41, s9, 0\n"
    "buffer_load_dwordx4 v[204:207], v242, s[40:43], s75 offen offset:0 nt\n"
    "buffer_load_dwordx4 v[208:211], v242, s[40:43], s75 offen offset:768 nt\n"
    "buffer_load_dwordx4 v[212:215], v242, s[40:43], s75 offen offset:256 nt\n"
    "buffer_load_dwordx4 v[216:219], v242, s[40:43], s75 offen offset:1024 nt\n"
    "buffer_load_dwordx4 v[220:223], v242, s[40:43], s75 offen offset:512 nt\n"
    "buffer_load_dwordx4 v[224:227], v242, s[40:43], s75 offen offset:1280 nt\n"
    "s_add_u32 s75, s71, s63\n"
    "s_add_u32 s75, s75, 98304\n"
    "buffer_load_dwordx4 v[36:39], v242, s[40:43], s75 offen offset:0 nt\n"
    "s_add_u32 s28, s6, s72\n"
    "s_addc_u32 s29, s7, 0\n"
    "v_lshrrev_b32 v162, 1, v156\n"
    "v_and_b32 v162, 16, v162\n"
    "v_bfe_u32 v159, v156, 4, 1\n"
    "v_or_b32 v162, v162, v159\n"
    "v_and_or_b32 v162, v157, 2, v162\n"
    "v_mul_u32_u24 v162, 768, v162\n"
    "v_and_b32 v163, 0x70, v1\n"
    "v_add_u32 v162, v162, v163\n"
    ".Lf2e:\n"
    "s_nop 4\n"
    "v_lshlrev_b32 v247, 2, v156\n"
    "v_add_u32 v247, 155648, v247\n"
    "v_mov_b32 v159, 0\n"
    "ds_write_b32 v247, v159\n"
    "v_mov_b32 v159, 0xff800000\n"
    "s_lshl2_add_u32 s74, s84, 155936\n"
    "v_mov_b32 v160, s74\n"
    "ds_write_b32 v160, v159\n"
    "s_mov_b32 s38, 5376\n"
    "s_mov_b32 s39, 0x20000\n"
    "s_mov_b32 s94, 4096\n"
    "s_mov_b32 s95, 0x20000\n"
    "s_mov_b32 s98, 4096\n"
    "s_mov_b32 s99, 0x20000\n"
    "s_mov_b32 s22, 743424\n"
    "s_mov_b32 s23, 0x20000\n"
    "s_mov_b32 s26, 743424\n"
    "s_mov_b32 s27, 0x20000\n"
    "s_lshl_b32 s80, s58, 7\n"
    "s_add_u32 s80, s80, 256\n"
    "s_mul_i32 s74, s52, 65536\n"
    "s_mul_i32 s69, s59, 384\n"
    "s_mul_i32 s57, s79, 6144\n"
    "s_add_u32 s57, s57, s69\n"
    "s_add_u32 s57, s57, s67\n"
    "s_mul_i32 s67, s82, 384\n"
    "s_add_u32 s66, s57, 24576\n"
    "s_add_u32 s36, s18, s74\n"
    "s_addc_u32 s37, s19, 0\n"
    "s_lshl_b32 s74, s52, 12\n"
    "s_add_u32 s92, s14, s74\n"
    "s_addc_u32 s93, s15, 0\n"
    "s_add_u32 s96, s16, s74\n"
    "s_addc_u32 s97, s17, 0\n"
    "v_lshlrev_b32 v128, 3, v156\n"
    "v_mov_b32 v129, 0\n"
    "v_lshlrev_b32 v130, 4, %3\n"
    "v_xor_b32 v130, 1024, v130\n"
    "buffer_load_dwordx2 v[124:125], v128, s[44:47], s67 offen offset:0\n"
    "buffer_load_dwordx2 v[126:127], v128, s[88:91], s67 offen offset:0\n"
    "buffer_load_dwordx2 v[238:239], v129, s[36:39], 0 offen offset:0 sc1\n"
    "s_mul_i32 s74, s52, 786432\n"
    "s_mul_i32 s73, s53, 6144\n"
    "s_add_u32 s74, s74, s73\n"
    "s_add_u32 s20, s4, s74\n"
    "s_addc_u32 s21, s5, 0\n"
    "s_add_u32 s24, s24, s74\n"
    "s_addc_u32 s25, s25, 0\n"
    "s_mul_i32 s72, s52, 1626112\n"
    "s_add_u32 s72, s72, 2097152\n"
    "s_add_u32 s16, s18, s72\n"
    "s_addc_u32 s17, s19, 0\n"
    "s_mov_b32 s18, 1622016\n"
    "s_mov_b32 s19, 0x20000\n"
    "s_mul_i32 s65, s84, 6144\n"
    "s_mul_i32 s85, s84, 49152\n"
    "s_waitcnt lgkmcnt(0)\n"
    "s_barrier\n"
    "s_add_u32 s73, s65, 49152\n"
    "v_add_u32 v131, s73, v1\n"
    "s_add_u32 s73, s73, s69\n"
    "v_add_u32 v132, s73, v163\n"
    "v_and_b32 v159, 32, v156\n"
    "v_lshlrev_b32 v244, 3, v156\n"
    "v_xor_b32 v244, v244, v159\n"
    "v_lshl_add_u32 v244, v159, 3, v244\n"
    "s_bitcmp1_b32 s83, 0\n"
    "s_cbranch_scc1 .Lf5b\n"
    "s_lshl_b32 s60, s53, 11\n"
    "s_add_u32 s61, s60, 12288\n"
    "s_lshl_b32 s73, s58, 8\n"
    "s_add_u32 s73, s73, 122880\n"
    "v_and_b32 v243, 0xf0, v1\n"
    "v_add_u32 v243, s73, v243\n"
    "s_and_b32 s73, s73, 0x1fd00\n"
    "v_lshlrev_b32 v160, 9, v157\n"
    "v_lshl_add_u32 v160, v158, 6, v160\n"
    "v_add_u32 v160, s73, v160\n"
    "s_lshl_b32 s73, s79, 8\n"
    "s_mul_i32 s74, s59, 6144\n"
    "s_add_u32 s73, s73, s74\n"
    "v_add_u32 v163, s73, v244\n"
    "v_and_b32 v159, 32, v156\n"
    "v_lshlrev_b32 v244, 2, v156\n"
    "v_xor_b32 v244, v244, v159\n"
    "v_mad_u32_u24 v244, v159, 12, v244\n"
    "s_sub_u32 s6, s28, 1572864\n"
    "s_subb_u32 s7, s29, 0\n"
    "buffer_load_dwordx2 v[40:41], v242, s[28:31], s75 offen offset:3072 nt\n"
    "buffer_load_dwordx2 v[42:43], v242, s[28:31], s75 offen offset:3840 nt\n"
    "v_add_u32 v159, 0, v156\n"
    "v_mul_u32_u24 v159, 2731, v159\n"
    "v_lshrrev_b32 v159, 16, v159\n"
    "v_xor_b32 v159, v159, v156\n"
    "v_and_b32 v159, 7, v159\n"
    "v_and_b32 v88, 0x380, v1\n"
    "v_lshl_add_u32 v88, v159, 4, v88\n"
    "v_add_u32 v159, 64, v156\n"
    "v_mul_u32_u24 v159, 2731, v159\n"
    "v_lshrrev_b32 v159, 16, v159\n"
    "v_xor_b32 v159, v159, v156\n"
    "v_and_b32 v159, 7, v159\n"
    "v_and_b32 v89, 0x380, v1\n"
    "v_lshl_add_u32 v89, v159, 4, v89\n"
    "v_add_u32 v159, 128, v156\n"
    "v_mul_u32_u24 v159, 2731, v159\n"
    "v_lshrrev_b32 v159, 16, v159\n"
    "v_xor_b32 v159, v159, v156\n"
    "v_and_b32 v159, 7, v159\n"
    "v_and_b32 v90, 0x380, v1\n"
    "v_lshl_add_u32 v90, v159, 4, v90\n"
    "v_and_b32 v2, 7, v156\n"
    "v_lshrrev_b32 v144, 7, v130\n"
    "v_mul_u32_u24 v144, 768, v144\n"
    "v_lshl_add_u32 v144, v2, 3, v144\n"
    "buffer_load_dwordx2 v[44:45], v242, s[28:31], s76 offen offset:0 nt\n"
    "buffer_load_dwordx2 v[46:47], v242, s[28:31], s76 offen offset:768 nt\n"
    "v_add_u32 v144, 131072, v144\n"
    "v_mad_u32_u24 v86, v2, 24, v144\n"
    "v_lshl_add_u32 v130, s82, 7, v130\n"
    "v_and_b32 v130, 4095, v130\n"
    "v_and_b32 v96, 15, v156\n"
    "v_lshrrev_b32 v159, 4, v156\n"
    "v_xor_b32 v159, v159, v2\n"
    "v_mul_u32_u24 v96, 384, v96\n"
    "v_lshl_add_u32 v96, v159, 4, v96\n"
    "s_add_u32 s73, s65, 49152\n"
    "v_add_u32 v96, s73, v96\n"
    "v_xor_b32 v97, 64, v96\n"
    "v_subrev_u32 v154, 24576, v96\n"
    "v_subrev_u32 v155, 24576, v97\n"
    "s_add_u32 s67, s65, 98304\n"
    "s_add_u32 s68, s65, 24576\n"
    "s_add_u32 s69, s65, 73728\n"
    "s_mov_b32 s81, 256\n"
    "s_mul_i32 s74, s79, 24576\n"
    "s_add_u32 s70, s74, s65\n"
    "s_add_u32 s70, s70, 73728\n"
    "s_sub_u32 s64, s65, s74\n"
    "s_add_u32 s64, s64, 98304\n"
    "v_and_b32 v249, 0x310, v1\n"
    "v_and_b32 v159, 12, v156\n"
    "v_lshl_add_u32 v249, v159, 3, v249\n"
    "buffer_load_dwordx2 v[48:49], v242, s[28:31], s76 offen offset:3072 nt\n"
    "buffer_load_dwordx2 v[50:51], v242, s[28:31], s76 offen offset:3840 nt\n"
    "v_and_b32 v159, 2, v156\n"
    "v_lshl_add_u32 v249, v159, 6, v249\n"
    "s_add_u32 s73, s74, 73728\n"
    "v_add_u32 v248, s73, v249\n"
    "s_sub_u32 s73, 98304, s74\n"
    "v_add_u32 v249, s73, v249\n"
    "s_or_b32 s73, s53, s84\n"
    "s_cmp_eq_u32 s73, 0\n"
    "s_cselect_b32 s82, 0, 0x7fff0000\n"
    "v_and_b32 v252, 12, v156\n"
    "v_bfe_u32 v159, v156, 1, 1\n"
    "v_or_b32 v252, v252, v159\n"
    "v_and_b32 v159, 1, v156\n"
    "v_lshl_or_b32 v252, v159, 1, v252\n"
    "v_lshlrev_b32 v252, 6, v252\n"
    "v_lshlrev_b32 v159, 2, v156\n"
    "v_and_b32 v159, 32, v159\n"
    "v_xor_b32 v159, v159, v156\n"
    "v_and_or_b32 v252, v159, 48, v252\n"
    "v_mov_b32 v2, v252\n"
    "v_add_u32 v3, 24576, v252\n"
    "v_lshrrev_b32 v91, 3, v156\n"
    "v_lshl_add_u32 v87, v91, 6, 64\n"
    "v_add_u32 v87, 64, v87\n"
    "v_and_b32 v241, 1, v156\n"
    "v_lshlrev_b32 v241, 3, v241\n"
    "buffer_load_dwordx2 v[52:53], v242, s[28:31], s77 offen offset:0 nt\n"
    "buffer_load_dwordx2 v[54:55], v242, s[28:31], s77 offen offset:768 nt\n"
    "v_lshl_add_u32 v241, v91, 4, v241\n"
    "v_add_u32 v241, 1152, v241\n"
    "v_lshlrev_b32 v91, 24, v91\n"
    "v_lshl_add_u32 v91, v156, 3, v91\n"
    "s_lshl_b32 s73, s53, 4\n"
    "s_lshl_b32 s74, s59, 3\n"
    "s_add_u32 s73, s73, s74\n"
    "s_add_u32 s73, s73, 1152\n"
    "v_mov_b32 v240, 0x7fff0000\n"
    "v_writelane_b32 v240, s73, 0\n"
    "v_and_b32 v159, 48, v156\n"
    "s_mul_i32 s73, s79, 768\n"
    "s_sub_u32 s73, 131072, s73\n"
    "v_lshlrev_b32 v250, 1, v159\n"
    "v_add_u32 v250, s73, v250\n"
    "v_and_b32 v251, 3, v156\n"
    "v_lshl_add_u32 v251, v251, 7, v159\n"
    "s_add_u32 s73, s73, 256\n"
    "v_add_u32 v251, s73, v251\n"
    "v_and_b32 v159, 64, v1\n"
    "v_add_u32 v251, v159, v251\n"
    "v_bfe_i32 v159, v156, 2, 1\n"
    "v_and_b32 v159, 0xaaaa, v159\n"
    "v_or_b32 v253, 0x4444, v159\n"
    "s_mov_b32 s58, 0\n"
    "s_mov_b32 s59, 8\n"
    "s_waitcnt vmcnt(31)\n"
    "v_pk_min_i16 v4, v4, v10\n"
    "v_pk_min_i16 v5, v5, v11\n"
    "v_pk_min_i16 v6, v6, v12\n"
    "v_pk_min_i16 v7, v7, v13\n"
    "v_pk_min_i16 v8, v8, v14\n"
    "v_pk_min_i16 v9, v9, v15\n"
    "v_pk_min_i16 v16, v16, v18\n"
    "v_pk_min_i16 v17, v17, v19\n"
    "v_pk_min_i16 v4, v4, v7\n"
    "v_pk_min_i16 v5, v5, v8\n"
    "v_pk_min_i16 v6, v6, v9\n"
    "v_pk_min_i16 v16, v16, v17\n"
    "v_pk_min_i16 v4, v4, v5\n"
    "v_lshlrev_b32 v17, 16, v16\n"
    "v_pk_min_i16 v4, v4, v6\n"
    "v_min_i32 v16, v16, v17\n"
    "v_lshlrev_b32 v5, 16, v4\n"
    "v_mov_b32 v7, 155920\n"
    "v_min_i32 v4, v4, v5\n"
    "v_lshl_add_u32 v6, s84, 2, v7\n"
    "v_min_i32_dpp v16, v16, v16 row_shr:1\n"
    "v_min_i32_dpp v4, v4, v4 row_shr:1\n"
    "s_nop 0\n"
    "v_min_i32_dpp v16, v16, v16 row_shr:2\n"
    "v_min_i32_dpp v4, v4, v4 row_shr:2\n"
    "s_nop 0\n"
    "v_min_i32_dpp v16, v16, v16 row_shr:4\n"
    "v_min_i32_dpp v4, v4, v4 row_shr:4\n"
    "s_nop 0\n"
    "v_min_i32_dpp v16, v16, v16 row_shr:8\n"
    "v_min_i32_dpp v4, v4, v4 row_shr:8\n"
    "s_nop 0\n"
    "v_min_i32_dpp v16, v16, v16 row_bcast:15 row_mask:0xa\n"
    "v_min_i32_dpp v4, v4, v4 row_bcast:15 row_mask:0xa\n"
    "s_nop 0\n"
    "v_min_i32_dpp v16, v16, v16 row_bcast:31 row_mask:0xc\n"
    "v_min_i32_dpp v4, v4, v4 row_bcast:31 row_mask:0xc\n"
    "v_readlane_b32 s35, v16, 63\n"
    "v_readlane_b32 s73, v4, 63\n"
    "s_max_i32 s35, s35, 0\n"
    "s_and_b32 s35, s35, 0xffff0000\n"
    "v_mov_b32 v4, s73\n"
    "ds_write_b32 v6, v4\n"
    "v_mov_b32 v14, -32\n"
    "v_mov_b32 v12, -126\n"
    "v_mov_b32 v13, 0x3dd53b95\n"
    "v_mov_b32 v98, 0\n"
    "s_waitcnt vmcnt(29)\n"
    "v_lshlrev_b32 v180, 16, v164\n"
    "v_and_b32 v181, 0xffff0000, v164\n"
    "v_lshlrev_b32 v182, 16, v168\n"
    "v_and_b32 v183, 0xffff0000, v168\n"
    "v_lshlrev_b32 v184, 16, v165\n"
    "v_and_b32 v185, 0xffff0000, v165\n"
    "v_lshlrev_b32 v186, 16, v169\n"
    "v_and_b32 v187, 0xffff0000, v169\n"
    "v_lshlrev_b32 v188, 16, v166\n"
    "v_and_b32 v189, 0xffff0000, v166\n"
    "v_lshlrev_b32 v190, 16, v170\n"
    "v_and_b32 v191, 0xffff0000, v170\n"
    "v_lshlrev_b32 v192, 16, v167\n"
    "v_and_b32 v193, 0xffff0000, v167\n"
    "v_lshlrev_b32 v194, 16, v171\n"
    "v_and_b32 v195, 0xffff0000, v171\n"
    "s_waitcnt lgkmcnt(0)\n"
    "s_barrier\n"
    "ds_read_b128 v[8:11], v7 offset:0\n"
    "buffer_load_dwordx2 v[56:57], v242, s[28:31], s77 offen offset:3072 nt\n"
    "buffer_load_dwordx2 v[58:59], v242, s[28:31], s77 offen offset:3840 nt\n"
    "buffer_load_dwordx4 v[136:139], v130, s[92:95], 0 offen offset:0\n"
    "buffer_load_dwordx4 v[140:143], v130, s[96:99], 0 offen offset:0\n"
    "s_nop 0\n"
    "s_nop 0\n"
    "s_nop 0\n"
    "s_nop 0\n"
    "s_nop 0\n"
    "s_nop 0\n"
    "s_nop 0\n"
    "s_nop 0\n"
    "s_nop 0\n"
    "s_nop 0\n"
    "s_nop 0\n"
    "s_nop 0\n"
    "s_nop 0\n"
    "s_nop 0\n"
    "s_nop 0\n"
    "s_nop 0\n"
    "s_nop 0\n"
    "s_nop 0\n"
    "s_nop 0\n"
    "s_nop 0\n"
    "s_nop 0\n"
    "s_nop 0\n"
    "s_nop 0\n"
    "s_nop 0\n"
    "s_nop 0\n"
    "s_nop 0\n"
    "s_nop 0\n"
    "s_nop 0\n"
    "s_nop 0\n"
    "s_nop 0\n"
    "s_nop 0\n"
    "s_nop 0\n"
    "s_nop 0\n"
    "s_nop 0\n"
    "s_nop 0\n"
    "s_nop 0\n"
    "s_nop 0\n"
    "s_nop 0\n"
    "s_nop 0\n"
    "s_nop 0\n"
    "s_nop 0\n"
    "s_nop 0\n"
    "s_nop 0\n"
    "s_nop 0\n"
    "s_nop 0\n"
    "s_nop 0\n"
    "s_nop 0\n"
    "s_nop 0\n"
    "s_nop 0\n"
    "s_nop 0\n"
    "s_nop 0\n"
    "s_nop 0\n"
    "s_nop 0\n"
    "s_nop 0\n"
    "s_nop 0\n"
    "s_nop 0\n"
    "s_nop 0\n"
    "s_nop 0\n"
    "s_nop 0\n"
    "s_nop 0\n"
    "s_waitcnt lgkmcnt(0)\n"
    "v_min3_i32 v4, v8, v9, v10\n"
    "v_min_i32 v4, v4, v11\n"
    "v_max_i32 v4, 0, v4\n"
    "v_and_b32 v4, 0xffff0000, v4\n"
    "v_mul_f32 v4, s35, v4\n"
    "v_mul_f32 v4, 0x3ed53b95, v4\n"
    "v_lshrrev_b32 v4, 23, v4\n"
    "v_sub_u32 v4, 2, v4\n"
    "v_med3_i32 v4, v4, v14, v12\n"
    "v_ldexp_f32 v13, v13, v4\n"
    "v_readfirstlane_b32 s89, v4\n"
    "v_readfirstlane_b32 s88, v13\n"
    "s_mul_i32 s89, s89, 0xff7fff80\n"
    "s_mov_b32 s90, 149\n"
    "v_mul_f32 v180, s88, v180\n"
    "v_mul_f32 v181, s88, v181\n"
    "v_mul_f32 v184, s88, v184\n"
    "v_mul_f32 v185, s88, v185\n"
    "v_mul_f32 v188, s88, v188\n"
    "v_mul_f32 v189, s88, v189\n"
    "v_mul_f32 v192, s88, v192\n"
    "v_mul_f32 v193, s88, v193\n"
    "v_fmaak_f32 v182, v182, v180, 0x80730000\n"
    "v_fmaak_f32 v183, v183, v181, 0x80730000\n"
    "v_fmaak_f32 v186, v186, v184, 0x80730000\n"
    "v_fmaak_f32 v187, v187, v185, 0x80730000\n"
    "v_fmaak_f32 v190, v190, v188, 0x80730000\n"
    "v_fmaak_f32 v191, v191, v189, 0x80730000\n"
    "v_fmaak_f32 v194, v194, v192, 0x80730000\n"
    "v_fmaak_f32 v195, v195, v193, 0x80730000\n"
    "v_ldexp_f32 v180, v180, s90\n"
    "v_ldexp_f32 v181, v181, s90\n"
    "v_ldexp_f32 v184, v184, s90\n"
    "v_ldexp_f32 v185, v185, s90\n"
    "v_ldexp_f32 v188, v188, s90\n"
    "v_ldexp_f32 v189, v189, s90\n"
    "v_ldexp_f32 v192, v192, s90\n"
    "v_ldexp_f32 v193, v193, s90\n"
    "v_fma_f32 v180, v180, 0, v180\n"
    "v_fma_f32 v181, v181, 0, v181\n"
    "v_fma_f32 v184, v184, 0, v184\n"
    "v_fma_f32 v185, v185, 0, v185\n"
    "v_fma_f32 v188, v188, 0, v188\n"
    "v_fma_f32 v189, v189, 0, v189\n"
    "v_fma_f32 v192, v192, 0, v192\n"
    "v_fma_f32 v193, v193, 0, v193\n"
    "ds_write_b128 v160, v[180:183] offset:0\n"
    "ds_write_b128 v160, v[184:187] offset:16\n"
    "ds_write_b128 v160, v[188:191] offset:32\n"
    "ds_write_b128 v160, v[192:195] offset:48\n"
    "s_waitcnt vmcnt(17)\n"
    "s_add_u32 s54, s71, s60\n"
    "s_add_u32 s55, s71, s61\n"
    "ds_read_b128 v[92:95], v243 offset:0\n"
    "s_waitcnt lgkmcnt(0)\n"
    "v_fma_f32 v204, v204, v92, -v94\n"
    "v_fma_f32 v205, v205, v93, -v95\n"
    "v_fma_f32 v206, v206, v92, -v94\n"
    "v_fma_f32 v207, v207, v93, -v95\n"
    "v_cvt_pk_bf16_f32 v204, v204, v205\n"
    "v_cvt_pk_bf16_f32 v206, v206, v207\n"
    "buffer_store_dword v204, v244, s[16:19], s54 offen offset:0 sc1\n"
    "buffer_store_dword v206, v244, s[16:19], s54 offen offset:128 sc1\n"
    "v_fma_f32 v208, v208, v92, -v94\n"
    "v_fma_f32 v209, v209, v93, -v95\n"
    "v_fma_f32 v210, v210, v92, -v94\n"
    "v_fma_f32 v211, v211, v93, -v95\n"
    "v_cvt_pk_bf16_f32 v208, v208, v209\n"
    "v_cvt_pk_bf16_f32 v210, v210, v211\n"
    "buffer_store_dword v208, v244, s[16:19], s54 offen offset:1024 sc1\n"
    "buffer_store_dword v210, v244, s[16:19], s54 offen offset:1152 sc1\n"
    "v_fma_f32 v212, v212, v92, -v94\n"
    "v_fma_f32 v213, v213, v93, -v95\n"
    "buffer_load_dwordx2 v[60:61], v242, s[28:31], s78 offen offset:0 nt\n"
    "buffer_load_dwordx2 v[62:63], v242, s[28:31], s78 offen offset:768 nt\n"
    "buffer_load_dwordx4 v[4:7], v162, s[28:31], s57 offen offset:0\n"
    "buffer_load_dwordx4 v[8:11], v162, s[28:31], s57 offen offset:3072\n"
    "v_fma_f32 v214, v214, v92, -v94\n"
    "v_fma_f32 v215, v215, v93, -v95\n"
    "v_cvt_pk_bf16_f32 v212, v212, v213\n"
    "v_cvt_pk_bf16_f32 v214, v214, v215\n"
    "buffer_store_dword v212, v244, s[16:19], s54 offen offset:256 sc1\n"
    "buffer_store_dword v214, v244, s[16:19], s54 offen offset:384 sc1\n"
    "v_fma_f32 v216, v216, v92, -v94\n"
    "v_fma_f32 v217, v217, v93, -v95\n"
    "v_fma_f32 v218, v218, v92, -v94\n"
    "v_fma_f32 v219, v219, v93, -v95\n"
    "v_cvt_pk_bf16_f32 v216, v216, v217\n"
    "v_cvt_pk_bf16_f32 v218, v218, v219\n"
    "buffer_store_dword v216, v244, s[16:19], s54 offen offset:1280 sc1\n"
    "buffer_store_dword v218, v244, s[16:19], s54 offen offset:1408 sc1\n"
    "v_fma_f32 v220, v220, v92, -v94\n"
    "v_fma_f32 v221, v221, v93, -v95\n"
    "v_fma_f32 v222, v222, v92, -v94\n"
    "v_fma_f32 v223, v223, v93, -v95\n"
    "v_cvt_pk_bf16_f32 v220, v220, v221\n"
    "v_cvt_pk_bf16_f32 v222, v222, v223\n"
    "buffer_store_dword v220, v244, s[16:19], s55 offen offset:0 sc1\n"
    "buffer_store_dword v222, v244, s[16:19], s55 offen offset:128 sc1\n"
    "v_fma_f32 v224, v224, v92, -v94\n"
    "buffer_load_dwordx2 v[64:65], v242, s[28:31], s78 offen offset:3072 nt\n"
    "buffer_load_dwordx2 v[66:67], v242, s[28:31], s78 offen offset:3840 nt\n"
    "buffer_load_dwordx4 v[12:15], v162, s[28:31], s57 offen offset:128\n"
    "v_fma_f32 v225, v225, v93, -v95\n"
    "v_fma_f32 v226, v226, v92, -v94\n"
    "v_fma_f32 v227, v227, v93, -v95\n"
    "v_cvt_pk_bf16_f32 v224, v224, v225\n"
    "v_cvt_pk_bf16_f32 v226, v226, v227\n"
    "buffer_store_dword v224, v244, s[16:19], s55 offen offset:1024 sc1\n"
    "buffer_store_dword v226, v244, s[16:19], s55 offen offset:1152 sc1\n"
    "v_fma_f32 v228, v228, v92, -v94\n"
    "v_fma_f32 v229, v229, v93, -v95\n"
    "v_fma_f32 v230, v230, v92, -v94\n"
    "v_fma_f32 v231, v231, v93, -v95\n"
    "v_cvt_pk_bf16_f32 v228, v228, v229\n"
    "v_cvt_pk_bf16_f32 v230, v230, v231\n"
    "buffer_store_dword v228, v244, s[16:19], s55 offen offset:256 sc1\n"
    "buffer_store_dword v230, v244, s[16:19], s55 offen offset:384 sc1\n"
    "v_fma_f32 v232, v232, v92, -v94\n"
    "v_fma_f32 v233, v233, v93, -v95\n"
    "v_fma_f32 v234, v234, v92, -v94\n"
    "v_fma_f32 v235, v235, v93, -v95\n"
    "v_cvt_pk_bf16_f32 v232, v232, v233\n"
    "v_cvt_pk_bf16_f32 v234, v234, v235\n"
    "buffer_store_dword v232, v244, s[16:19], s55 offen offset:1280 sc1\n"
    "buffer_store_dword v234, v244, s[16:19], s55 offen offset:1408 sc1\n"
    "v_add_u32 v243, 512, v243\n"
    "s_add_u32 s71, s71, 98304\n"
    "s_add_u32 m0, s65, 24576\n"
    "s_add_u32 s72, s85, 0\n"
    "buffer_load_dwordx4 v88, s[20:23], s72 offen offset:0 nt lds\n"
    "buffer_load_dwordx4 v[16:19], v162, s[28:31], s57 offen offset:3200\n"
    "buffer_load_dwordx4 v[20:23], v162, s[28:31], s57 offen offset:256\n"
    "s_waitcnt vmcnt(38)\n"
    "v_add_co_u32 v238, vcc, 0x7f4a7c15, v238\n"
    "v_mov_b32 v159, 0x9e3779b9\n"
    "s_nop 0\n"
    "v_addc_co_u32 v239, vcc, v159, v239, vcc\n"
    "v_mov_b32 v246, 1\n"
    "s_waitcnt vmcnt(39)\n"
    "v_lshlrev_b32 v180, 16, v124\n"
    "v_and_b32 v181, 0xffff0000, v124\n"
    "v_lshlrev_b32 v182, 16, v125\n"
    "v_and_b32 v183, 0xffff0000, v125\n"
    "v_lshlrev_b32 v184, 16, v126\n"
    "v_and_b32 v185, 0xffff0000, v126\n"
    "v_lshlrev_b32 v186, 16, v127\n"
    "v_and_b32 v187, 0xffff0000, v127\n"
    "v_mul_f32 v180, s88, v180\n"
    "v_mul_f32 v181, s88, v181\n"
    "v_mul_f32 v182, s88, v182\n"
    "v_mul_f32 v183, s88, v183\n"
    "v_fmaak_f32 v184, v184, v180, 0x80730000\n"
    "v_fmaak_f32 v185, v185, v181, 0x80730000\n"
    "v_fmaak_f32 v186, v186, v182, 0x80730000\n"
    "v_fmaak_f32 v187, v187, v183, 0x80730000\n"
    "s_add_u32 m0, s65, 25600\n"
    "s_add_u32 s72, s85, 1024\n"
    "buffer_load_dwordx4 v89, s[20:23], s72 offen offset:0 nt lds\n"
    "buffer_load_dwordx4 v[24:27], v162, s[28:31], s57 offen offset:3328\n"
    "v_ldexp_f32 v180, v180, s90\n"
    "v_ldexp_f32 v181, v181, s90\n"
    "v_ldexp_f32 v182, v182, s90\n"
    "v_ldexp_f32 v183, v183, s90\n"
    "v_fma_f32 v180, v180, 0, v180\n"
    "v_fma_f32 v181, v181, 0, v181\n"
    "v_fma_f32 v182, v182, 0, v182\n"
    "v_fma_f32 v183, v183, 0, v183\n"
    "ds_write_b128 v131, v[180:183] offset:0\n"
    "ds_write_b128 v131, v[184:187] offset:1024\n"
    "ds_read_b128 v[180:183], v132 offset:0\n"
    "ds_read_b128 v[184:187], v132 offset:1024\n"
    "ds_read_b128 v[188:191], v132 offset:128\n"
    "ds_read_b128 v[192:195], v132 offset:1152\n"
    "ds_read_b128 v[196:199], v132 offset:256\n"
    "ds_read_b128 v[200:203], v132 offset:1280\n"
    "s_nop 0\n"
    "s_nop 0\n"
    "s_nop 0\n"
    "s_nop 0\n"
    "s_nop 0\n"
    "s_nop 0\n"
    "s_nop 0\n"
    "s_add_u32 m0, s65, 26624\n"
    "s_add_u32 s72, s85, 2048\n"
    "buffer_load_dwordx4 v90, s[20:23], s72 offen offset:0 nt lds\n"
    "buffer_load_dwordx4 v[100:103], v162, s[28:31], s66 offen offset:0\n"
    "buffer_load_dwordx4 v[104:107], v162, s[28:31], s66 offen offset:3072\n"
    "s_nop 0\n"
    "s_nop 0\n"
    "s_nop 0\n"
    "s_nop 0\n"
    "s_nop 0\n"
    "s_nop 0\n"
    "s_nop 0\n"
    "s_nop 0\n"
    "s_nop 0\n"
    "s_nop 0\n"
    "s_nop 0\n"
    "s_nop 0\n"
    "s_nop 0\n"
    "s_nop 0\n"
    "s_nop 0\n"
    "s_nop 0\n"
    "s_nop 0\n"
    "s_nop 0\n"
    "s_nop 0\n"
    "s_nop 0\n"
    "s_nop 0\n"
    "s_nop 0\n"
    "s_add_u32 m0, s65, 27648\n"
    "s_add_u32 s72, s85, 3072\n"
    "buffer_load_dwordx4 v88, s[20:23], s72 offen offset:0 nt lds\n"
    "buffer_load_dwordx4 v[108:111], v162, s[28:31], s66 offen offset:128\n"
    "s_nop 0\n"
    "s_nop 0\n"
    "s_nop 0\n"
    "s_nop 0\n"
    "s_nop 0\n"
    "s_nop 0\n"
    "s_nop 0\n"
    "s_nop 0\n"
    "s_nop 0\n"
    "s_nop 0\n"
    "s_nop 0\n"
    "s_nop 0\n"
    "s_nop 0\n"
    "s_nop 0\n"
    "s_nop 0\n"
    "s_nop 0\n"
    "s_nop 0\n"
    "s_nop 0\n"
    "s_nop 0\n"
    "s_nop 0\n"
    "s_nop 0\n"
    "s_nop 0\n"
    "s_nop 0\n"
    "s_waitcnt lgkmcnt(0)\n"
    "s_waitcnt vmcnt(10)\n"
    "ds_add_rtn_u32 v245, v247, v246\n"
    "s_lshl_b32 s73, s53, 6\n"
    "s_add_u32 s73, s73, 128\n"
    "s_waitcnt lgkmcnt(0)\n"
    "v_readfirstlane_b32 s74, v245\n"
    "s_nop 0\n"
    "s_cmp_eq_u32 s74, 3\n"
    "s_cselect_b32 s73, s73, 0x7fff0000\n"
    "buffer_store_dwordx2 v[238:239], v91, s[36:39], s73 offen offset:0 sc1\n"
    "s_waitcnt vmcnt(18)\n"
    "s_add_u32 m0, s65, 28672\n"
    "s_add_u32 s72, s85, 4096\n"
    "buffer_load_dwordx4 v89, s[20:23], s72 offen offset:0 nt lds\n"
    "buffer_load_dwordx4 v[112:115], v162, s[28:31], s66 offen offset:3200\n"
    "buffer_load_dwordx4 v[116:119], v162, s[28:31], s66 offen offset:256\n"
    "s_add_u32 s54, s71, s60\n"
    "s_add_u32 s55, s71, s61\n"
    "ds_read_b128 v[92:95], v243 offset:0\n"
    "s_waitcnt lgkmcnt(0)\n"
    "v_fma_f32 v36, v36, v92, -v94\n"
    "v_fma_f32 v37, v37, v93, -v95\n"
    "v_fma_f32 v38, v38, v92, -v94\n"
    "v_fma_f32 v39, v39, v93, -v95\n"
    "v_cvt_pk_bf16_f32 v36, v36, v37\n"
    "v_cvt_pk_bf16_f32 v38, v38, v39\n"
    "buffer_store_dword v36, v244, s[16:19], s54 offen offset:0 sc1\n"
    "buffer_store_dword v38, v244, s[16:19], s54 offen offset:128 sc1\n"
    "v_fma_f32 v40, v40, v92, -v94\n"
    "v_fma_f32 v41, v41, v93, -v95\n"
    "v_fma_f32 v42, v42, v92, -v94\n"
    "v_fma_f32 v43, v43, v93, -v95\n"
    "v_cvt_pk_bf16_f32 v40, v40, v41\n"
    "v_cvt_pk_bf16_f32 v42, v42, v43\n"
    "buffer_store_dword v40, v244, s[16:19], s54 offen offset:1024 sc1\n"
    "buffer_store_dword v42, v244, s[16:19], s54 offen offset:1152 sc1\n"
    "v_fma_f32 v44, v44, v92, -v94\n"
    "v_fma_f32 v45, v45, v93, -v95\n"
    "v_fma_f32 v46, v46, v92, -v94\n"
    "v_fma_f32 v47, v47, v93, -v95\n"
    "v_cvt_pk_bf16_f32 v44, v44, v45\n"
    "v_cvt_pk_bf16_f32 v46, v46, v47\n"
    "buffer_store_dword v44, v244, s[16:19], s54 offen offset:256 sc1\n"
    "buffer_store_dword v46, v244, s[16:19], s54 offen offset:384 sc1\n"
    "v_fma_f32 v48, v48, v92, -v94\n"
    "v_fma_f32 v49, v49, v93, -v95\n"
    "v_fma_f32 v50, v50, v92, -v94\n"
    "v_fma_f32 v51, v51, v93, -v95\n"
    "v_cvt_pk_bf16_f32 v48, v48, v49\n"
    "v_cvt_pk_bf16_f32 v50, v50, v51\n"
    "s_add_u32 m0, s65, 29696\n"
    "s_add_u32 s72, s85, 5120\n"
    "buffer_load_dwordx4 v90, s[20:23], s72 offen offset:0 nt lds\n"
    "buffer_load_dwordx4 v[120:123], v162, s[28:31], s66 offen offset:3328\n"
    "buffer_store_dword v48, v244, s[16:19], s54 offen offset:1280 sc1\n"
    "buffer_store_dword v50, v244, s[16:19], s54 offen offset:1408 sc1\n"
    "v_fma_f32 v52, v52, v92, -v94\n"
    "v_fma_f32 v53, v53, v93, -v95\n"
    "v_fma_f32 v54, v54, v92, -v94\n"
    "v_fma_f32 v55, v55, v93, -v95\n"
    "v_cvt_pk_bf16_f32 v52, v52, v53\n"
    "v_cvt_pk_bf16_f32 v54, v54, v55\n"
    "buffer_store_dword v52, v244, s[16:19], s55 offen offset:0 sc1\n"
    "buffer_store_dword v54, v244, s[16:19], s55 offen offset:128 sc1\n"
    "v_fma_f32 v56, v56, v92, -v94\n"
    "v_fma_f32 v57, v57, v93, -v95\n"
    "v_fma_f32 v58, v58, v92, -v94\n"
    "v_fma_f32 v59, v59, v93, -v95\n"
    "v_cvt_pk_bf16_f32 v56, v56, v57\n"
    "v_cvt_pk_bf16_f32 v58, v58, v59\n"
    "buffer_store_dword v56, v244, s[16:19], s55 offen offset:1024 sc1\n"
    "buffer_store_dword v58, v244, s[16:19], s55 offen offset:1152 sc1\n"
    "v_fma_f32 v60, v60, v92, -v94\n"
    "v_fma_f32 v61, v61, v93, -v95\n"
    "v_fma_f32 v62, v62, v92, -v94\n"
    "v_fma_f32 v63, v63, v93, -v95\n"
    "v_cvt_pk_bf16_f32 v60, v60, v61\n"
    "v_cvt_pk_bf16_f32 v62, v62, v63\n"
    "buffer_store_dword v60, v244, s[16:19], s55 offen offset:256 sc1\n"
    "buffer_store_dword v62, v244, s[16:19], s55 offen offset:384 sc1\n"
    "v_fma_f32 v64, v64, v92, -v94\n"
    "v_fma_f32 v65, v65, v93, -v95\n"
    "v_fma_f32 v66, v66, v92, -v94\n"
    "v_fma_f32 v67, v67, v93, -v95\n"
    "v_cvt_pk_bf16_f32 v64, v64, v65\n"
    "v_cvt_pk_bf16_f32 v66, v66, v67\n"
    "buffer_store_dword v64, v244, s[16:19], s55 offen offset:1280 sc1\n"
    "buffer_store_dword v66, v244, s[16:19], s55 offen offset:1408 sc1\n"
    "v_add_u32 v243, 512, v243\n"
    "s_add_u32 m0, s65, 73728\n"
    "s_add_u32 s72, s85, 196608\n"
    "buffer_load_dwordx4 v88, s[20:23], s72 offen offset:0 nt lds\n"
    "s_waitcnt vmcnt(56)\n"
    "v_lshlrev_b32 v204, 16, v136\n"
    "v_and_b32 v205, 0xffff0000, v136\n"
    "v_lshlrev_b32 v206, 16, v137\n"
    "v_and_b32 v207, 0xffff0000, v137\n"
    "v_lshlrev_b32 v208, 16, v138\n"
    "v_and_b32 v209, 0xffff0000, v138\n"
    "v_lshlrev_b32 v210, 16, v139\n"
    "v_and_b32 v211, 0xffff0000, v139\n"
    "v_log_f32 v68, v204\n"
    "v_log_f32 v69, v205\n"
    "v_log_f32 v70, v206\n"
    "v_log_f32 v71, v207\n"
    "v_log_f32 v72, v208\n"
    "v_log_f32 v73, v209\n"
    "v_log_f32 v74, v210\n"
    "v_log_f32 v75, v211\n"
    "v_rcp_f32 v204, v204\n"
    "v_rcp_f32 v205, v205\n"
    "v_rcp_f32 v206, v206\n"
    "s_add_u32 m0, s65, 74752\n"
    "s_add_u32 s72, s85, 197632\n"
    "buffer_load_dwordx4 v89, s[20:23], s72 offen offset:0 nt lds\n"
    "v_rcp_f32 v207, v207\n"
    "v_rcp_f32 v208, v208\n"
    "v_rcp_f32 v209, v209\n"
    "v_rcp_f32 v210, v210\n"
    "v_rcp_f32 v211, v211\n"
    "s_mov_b32 s73, 0x41a00000\n"
    "v_mov_b32 v228, 0x7fc00000\n"
    "v_cmp_le_f32 vcc, |v68|, s73\n"
    "s_nop 1\n"
    "v_cndmask_b32 v68, v228, v68, vcc\n"
    "v_cmp_le_f32 vcc, |v69|, s73\n"
    "s_nop 1\n"
    "v_cndmask_b32 v69, v228, v69, vcc\n"
    "v_cmp_le_f32 vcc, |v70|, s73\n"
    "s_nop 1\n"
    "v_cndmask_b32 v70, v228, v70, vcc\n"
    "v_cmp_le_f32 vcc, |v71|, s73\n"
    "s_nop 1\n"
    "v_cndmask_b32 v71, v228, v71, vcc\n"
    "v_cmp_le_f32 vcc, |v72|, s73\n"
    "s_nop 1\n"
    "v_cndmask_b32 v72, v228, v72, vcc\n"
    "v_cmp_le_f32 vcc, |v73|, s73\n"
    "s_nop 1\n"
    "v_cndmask_b32 v73, v228, v73, vcc\n"
    "v_cmp_le_f32 vcc, |v74|, s73\n"
    "s_nop 1\n"
    "v_cndmask_b32 v74, v228, v74, vcc\n"
    "s_add_u32 m0, s65, 75776\n"
    "s_add_u32 s72, s85, 198656\n"
    "buffer_load_dwordx4 v90, s[20:23], s72 offen offset:0 nt lds\n"
    "v_cmp_le_f32 vcc, |v75|, s73\n"
    "s_nop 1\n"
    "v_cndmask_b32 v75, v228, v75, vcc\n"
    "v_cvt_pk_bf16_f32 v220, v204, v205\n"
    "v_cvt_pk_bf16_f32 v221, v206, v207\n"
    "v_cvt_pk_bf16_f32 v222, v208, v209\n"
    "v_cvt_pk_bf16_f32 v223, v210, v211\n"
    "v_lshlrev_b32 v212, 16, v220\n"
    "v_and_b32 v213, 0xffff0000, v220\n"
    "v_lshlrev_b32 v214, 16, v221\n"
    "v_and_b32 v215, 0xffff0000, v221\n"
    "v_lshlrev_b32 v216, 16, v222\n"
    "v_and_b32 v217, 0xffff0000, v222\n"
    "v_lshlrev_b32 v218, 16, v223\n"
    "v_and_b32 v219, 0xffff0000, v223\n"
    "v_sub_f32 v204, v204, v212\n"
    "v_sub_f32 v205, v205, v213\n"
    "v_sub_f32 v206, v206, v214\n"
    "v_sub_f32 v207, v207, v215\n"
    "v_sub_f32 v208, v208, v216\n"
    "v_sub_f32 v209, v209, v217\n"
    "s_add_u32 m0, s65, 76800\n"
    "s_add_u32 s72, s85, 199680\n"
    "buffer_load_dwordx4 v88, s[20:23], s72 offen offset:0 nt lds\n"
    "v_sub_f32 v210, v210, v218\n"
    "v_sub_f32 v211, v211, v219\n"
    "v_cvt_pk_bf16_f32 v224, v204, v205\n"
    "v_cvt_pk_bf16_f32 v225, v206, v207\n"
    "v_cvt_pk_bf16_f32 v226, v208, v209\n"
    "v_cvt_pk_bf16_f32 v227, v210, v211\n"
    "v_mov_b32 v204, 0\n"
    "v_mov_b32 v205, 0\n"
    "v_mov_b32 v206, 0\n"
    "v_mov_b32 v207, 0\n"
    "v_swap_b32 v221, v222\n"
    "v_swap_b32 v225, v226\n"
    "v_swap_b32 v141, v142\n"
    "ds_write2_b64 v144, v[220:221], v[222:223] offset0:32 offset1:40\n"
    "ds_write2_b64 v144, v[224:225], v[226:227] offset0:48 offset1:56\n"
    "ds_write2_b64 v144, v[140:141], v[142:143] offset0:64 offset1:72\n"
    "ds_write2_b64 v144, v[204:205], v[206:207] offset0:80 offset1:88\n"
    "s_waitcnt vmcnt(31)\n"
    "v_fma_f32 v4, v4, v180, -v184\n"
    "v_fma_f32 v5, v5, v181, -v185\n"
    "v_fma_f32 v6, v6, v182, -v186\n"
    "s_add_u32 m0, s65, 77824\n"
    "s_add_u32 s72, s85, 200704\n"
    "buffer_load_dwordx4 v89, s[20:23], s72 offen offset:0 nt lds\n"
    "v_fma_f32 v7, v7, v183, -v187\n"
    "v_fma_f32 v8, v8, v180, -v184\n"
    "v_fma_f32 v9, v9, v181, -v185\n"
    "v_fma_f32 v10, v10, v182, -v186\n"
    "v_fma_f32 v11, v11, v183, -v187\n"
    "v_cvt_pk_bf16_f32 v4, v4, v5\n"
    "v_cvt_pk_bf16_f32 v5, v6, v7\n"
    "v_cvt_pk_bf16_f32 v6, v8, v9\n"
    "v_cvt_pk_bf16_f32 v7, v10, v11\n"
    "ds_write_b64 v163, v[4:5] offset:0\n"
    "ds_write_b64 v163, v[6:7] offset:1024\n"
    "v_fma_f32 v12, v12, v188, -v192\n"
    "v_fma_f32 v13, v13, v189, -v193\n"
    "v_fma_f32 v14, v14, v190, -v194\n"
    "v_fma_f32 v15, v15, v191, -v195\n"
    "v_fma_f32 v16, v16, v188, -v192\n"
    "v_fma_f32 v17, v17, v189, -v193\n"
    "v_fma_f32 v18, v18, v190, -v194\n"
    "v_fma_f32 v19, v19, v191, -v195\n"
    "v_cvt_pk_bf16_f32 v12, v12, v13\n"
    "s_add_u32 m0, s65, 78848\n"
    "s_add_u32 s72, s85, 201728\n"
    "buffer_load_dwordx4 v90, s[20:23], s72 offen offset:0 nt lds\n"
    "v_cvt_pk_bf16_f32 v13, v14, v15\n"
    "v_cvt_pk_bf16_f32 v14, v16, v17\n"
    "v_cvt_pk_bf16_f32 v15, v18, v19\n"
    "ds_write_b64 v163, v[12:13] offset:2048\n"
    "ds_write_b64 v163, v[14:15] offset:3072\n"
    "v_fma_f32 v20, v20, v196, -v200\n"
    "v_fma_f32 v21, v21, v197, -v201\n"
    "v_fma_f32 v22, v22, v198, -v202\n"
    "v_fma_f32 v23, v23, v199, -v203\n"
    "v_fma_f32 v24, v24, v196, -v200\n"
    "v_fma_f32 v25, v25, v197, -v201\n"
    "v_fma_f32 v26, v26, v198, -v202\n"
    "v_fma_f32 v27, v27, v199, -v203\n"
    "v_cvt_pk_bf16_f32 v20, v20, v21\n"
    "v_cvt_pk_bf16_f32 v21, v22, v23\n"
    "v_cvt_pk_bf16_f32 v22, v24, v25\n"
    "v_cvt_pk_bf16_f32 v23, v26, v27\n"
    "ds_write_b64 v163, v[20:21] offset:4096\n"
    "ds_write_b64 v163, v[22:23] offset:5120\n"
    "s_waitcnt vmcnt(16)\n"
    "v_fma_f32 v100, v100, v180, -v184\n"
    "s_add_u32 m0, s65, 49152\n"
    "s_add_u32 s72, s85, 393216\n"
    "buffer_load_dwordx4 v88, s[20:23], s72 offen offset:0 nt lds\n"
    "v_fma_f32 v101, v101, v181, -v185\n"
    "v_fma_f32 v102, v102, v182, -v186\n"
    "v_fma_f32 v103, v103, v183, -v187\n"
    "v_fma_f32 v104, v104, v180, -v184\n"
    "v_fma_f32 v105, v105, v181, -v185\n"
    "v_fma_f32 v106, v106, v182, -v186\n"
    "v_fma_f32 v107, v107, v183, -v187\n"
    "v_cvt_pk_bf16_f32 v100, v100, v101\n"
    "v_cvt_pk_bf16_f32 v101, v102, v103\n"
    "v_cvt_pk_bf16_f32 v102, v104, v105\n"
    "v_cvt_pk_bf16_f32 v103, v106, v107\n"
    "ds_write_b64 v163, v[100:101] offset:12288\n"
    "ds_write_b64 v163, v[102:103] offset:13312\n"
    "v_fma_f32 v108, v108, v188, -v192\n"
    "v_fma_f32 v109, v109, v189, -v193\n"
    "v_fma_f32 v110, v110, v190, -v194\n"
    "v_fma_f32 v111, v111, v191, -v195\n"
    "v_fma_f32 v112, v112, v188, -v192\n"
    "v_fma_f32 v113, v113, v189, -v193\n"
    "v_fma_f32 v114, v114, v190, -v194\n"
    "s_add_u32 m0, s65, 50176\n"
    "s_add_u32 s72, s85, 394240\n"
    "buffer_load_dwordx4 v89, s[20:23], s72 offen offset:0 nt lds\n"
    "v_fma_f32 v115, v115, v191, -v195\n"
    "v_cvt_pk_bf16_f32 v108, v108, v109\n"
    "v_cvt_pk_bf16_f32 v109, v110, v111\n"
    "v_cvt_pk_bf16_f32 v110, v112, v113\n"
    "v_cvt_pk_bf16_f32 v111, v114, v115\n"
    "ds_write_b64 v163, v[108:109] offset:14336\n"
    "ds_write_b64 v163, v[110:111] offset:15360\n"
    "v_fma_f32 v116, v116, v196, -v200\n"
    "v_fma_f32 v117, v117, v197, -v201\n"
    "v_fma_f32 v118, v118, v198, -v202\n"
    "v_fma_f32 v119, v119, v199, -v203\n"
    "v_fma_f32 v120, v120, v196, -v200\n"
    "v_fma_f32 v121, v121, v197, -v201\n"
    "v_fma_f32 v122, v122, v198, -v202\n"
    "v_fma_f32 v123, v123, v199, -v203\n"
    "v_cvt_pk_bf16_f32 v116, v116, v117\n"
    "v_cvt_pk_bf16_f32 v117, v118, v119\n"
    "v_cvt_pk_bf16_f32 v118, v120, v121\n"
    "v_cvt_pk_bf16_f32 v119, v122, v123\n"
    "ds_write_b64 v163, v[116:117] offset:16384\n"
    "ds_write_b64 v163, v[118:119] offset:17408\n"
    "s_waitcnt vmcnt(19)\n"
    "ds_read_b128 v[36:39], v154 offset:0\n"
    "ds_read_b128 v[40:43], v155 offset:0\n"
    "ds_read_b128 v[44:47], v154 offset:128\n"
    "ds_read_b128 v[48:51], v155 offset:128\n"
    "ds_read_b128 v[52:55], v154 offset:256\n"
    "ds_read_b128 v[56:59], v155 offset:256\n"
    "s_waitcnt lgkmcnt(0)\n"
    "s_barrier\n"
    "ds_read_b128 a[112:115], v252 offset:0\n"
    "ds_read_b128 a[116:119], v252 offset:2048\n"
    "ds_read_b128 a[120:123], v252 offset:4096\n"
    "ds_read_b128 a[124:127], v252 offset:6144\n"
    "ds_read_b128 a[128:131], v252 offset:8192\n"
    "ds_read_b128 a[132:135], v252 offset:10240\n"
    "ds_read_b128 a[136:139], v252 offset:1024\n"
    "ds_read_b128 a[140:143], v252 offset:3072\n"
    "ds_read_b128 a[144:147], v252 offset:5120\n"
    "ds_read_b128 a[148:151], v252 offset:7168\n"
    "ds_read_b128 a[152:155], v252 offset:9216\n"
    "ds_read_b128 a[156:159], v252 offset:11264\n"
    "ds_read_b128 a[0:3], v252 offset:12288\n"
    "ds_read_b128 a[4:7], v252 offset:14336\n"
    "ds_read_b128 a[8:11], v252 offset:16384\n"
    "ds_read_b128 a[12:15], v252 offset:18432\n"
    "ds_read_b128 a[16:19], v252 offset:20480\n"
    "ds_read_b128 a[20:23], v252 offset:22528\n"
    "ds_read_b128 a[24:27], v252 offset:13312\n"
    "ds_read_b128 a[28:31], v252 offset:15360\n"
    "ds_read_b128 a[32:35], v252 offset:17408\n"
    "ds_read_b128 a[36:39], v252 offset:19456\n"
    "ds_read_b128 a[40:43], v252 offset:21504\n"
    "ds_read_b128 a[44:47], v252 offset:23552\n"
    "s_add_u32 m0, s65, 51200\n"
    "s_add_u32 s72, s85, 395264\n"
    "buffer_load_dwordx4 v90, s[20:23], s72 offen offset:0 nt lds\n"
    "s_add_u32 m0, s65, 52224\n"
    "s_add_u32 s72, s85, 396288\n"
    "buffer_load_dwordx4 v88, s[20:23], s72 offen offset:0 nt lds\n"
    "v_pk_maximum3_f16 v98, v36, v37, v98\n"
    "v_pk_maximum3_f16 v98, v38, v39, v98\n"
    "v_pk_add_u16 v36, s89, v36 clamp\n"
    "v_pk_add_u16 v37, s89, v37 clamp\n"
    "v_pk_add_u16 v38, s89, v38 clamp\n"
    "v_pk_add_u16 v39, s89, v39 clamp\n"
    "v_pk_maximum3_f16 v98, v40, v41, v98\n"
    "v_pk_maximum3_f16 v98, v42, v43, v98\n"
    "v_pk_add_u16 v40, s89, v40 clamp\n"
    "v_pk_add_u16 v41, s89, v41 clamp\n"
    "v_pk_add_u16 v42, s89, v42 clamp\n"
    "v_pk_add_u16 v43, s89, v43 clamp\n"
    "v_pk_maximum3_f16 v98, v44, v45, v98\n"
    "v_pk_maximum3_f16 v98, v46, v47, v98\n"
    "v_pk_add_u16 v44, s89, v44 clamp\n"
    "v_pk_add_u16 v45, s89, v45 clamp\n"
    "v_pk_add_u16 v46, s89, v46 clamp\n"
    "v_pk_add_u16 v47, s89, v47 clamp\n"
    "v_pk_maximum3_f16 v98, v48, v49, v98\n"
    "v_pk_maximum3_f16 v98, v50, v51, v98\n"
    "v_pk_add_u16 v48, s89, v48 clamp\n"
    "v_pk_add_u16 v49, s89, v49 clamp\n"
    "v_pk_add_u16 v50, s89, v50 clamp\n"
    "v_pk_add_u16 v51, s89, v51 clamp\n"
    "v_pk_maximum3_f16 v98, v52, v53, v98\n"
    "v_pk_maximum3_f16 v98, v54, v55, v98\n"
    "v_pk_add_u16 v52, s89, v52 clamp\n"
    "v_pk_add_u16 v53, s89, v53 clamp\n"
    "v_pk_add_u16 v54, s89, v54 clamp\n"
    "v_pk_add_u16 v55, s89, v55 clamp\n"
    "v_pk_maximum3_f16 v98, v56, v57, v98\n"
    "v_pk_maximum3_f16 v98, v58, v59, v98\n"
    "v_pk_add_u16 v56, s89, v56 clamp\n"
    "v_pk_add_u16 v57, s89, v57 clamp\n"
    "v_pk_add_u16 v58, s89, v58 clamp\n"
    "v_pk_add_u16 v59, s89, v59 clamp\n"
    "v_mov_b32 v108, 0\n"
    "v_mov_b32 v109, 0\n"
    "v_mov_b32 v110, 0\n"
    "v_mov_b32 v111, 0\n"
    "v_mov_b32 v112, 0\n"
    "v_mov_b32 v113, 0\n"
    "v_mov_b32 v114, 0\n"
    "v_mov_b32 v115, 0\n"
    "v_mov_b32 v116, 0\n"
    "v_mov_b32 v117, 0\n"
    "v_mov_b32 v118, 0\n"
    "v_mov_b32 v119, 0\n"
    "v_mov_b32 v120, 0\n"
    "v_mov_b32 v121, 0\n"
    "v_mov_b32 v122, 0\n"
    "v_mov_b32 v123, 0\n"
    "s_mul_i32 s73, s79, 768\n"
    "v_add_u32 v78, s73, v250\n"
    "v_mov_b32 v79, 155936\n"
    "s_waitcnt lgkmcnt(15)\n"
    "v_mfma_f32_16x16x32_bf16 v[4:7], a[112:115], v[36:39], 0\n"
    "s_waitcnt lgkmcnt(15)\n"
    "v_mfma_f32_16x16x32_bf16 v[4:7], a[116:119], v[40:43], v[4:7]\n"
    "s_waitcnt lgkmcnt(15)\n"
    "v_mfma_f32_16x16x32_bf16 v[4:7], a[120:123], v[44:47], v[4:7]\n"
    "s_waitcnt lgkmcnt(15)\n"
    "v_mfma_f32_16x16x32_bf16 v[4:7], a[124:127], v[48:51], v[4:7]\n"
    "s_waitcnt lgkmcnt(15)\n"
    "v_mfma_f32_16x16x32_bf16 v[4:7], a[128:131], v[52:55], v[4:7]\n"
    "s_waitcnt lgkmcnt(15)\n"
    "v_mfma_f32_16x16x32_bf16 v[4:7], a[132:135], v[56:59], v[4:7]\n"
    "s_waitcnt lgkmcnt(15)\n"
    "v_mfma_f32_16x16x32_bf16 v[20:23], a[136:139], v[36:39], 0\n"
    "v_accvgpr_write_b32 a192, v36\n"
    "v_accvgpr_write_b32 a193, v37\n"
    "v_accvgpr_write_b32 a194, v38\n"
    "v_accvgpr_write_b32 a195, v39\n"
    "s_waitcnt lgkmcnt(15)\n"
    "v_mfma_f32_16x16x32_bf16 v[20:23], a[140:143], v[40:43], v[20:23]\n"
    "v_accvgpr_write_b32 a196, v40\n"
    "v_accvgpr_write_b32 a197, v41\n"
    "v_accvgpr_write_b32 a198, v42\n"
    "v_accvgpr_write_b32 a199, v43\n"
    "s_waitcnt lgkmcnt(15)\n"
    "v_mfma_f32_16x16x32_bf16 v[20:23], a[144:147], v[44:47], v[20:23]\n"
    "v_accvgpr_write_b32 a200, v44\n"
    "v_accvgpr_write_b32 a201, v45\n"
    "v_accvgpr_write_b32 a202, v46\n"
    "v_accvgpr_write_b32 a203, v47\n"
    "s_waitcnt lgkmcnt(14)\n"
    "v_mfma_f32_16x16x32_bf16 v[20:23], a[148:151], v[48:51], v[20:23]\n"
    "v_accvgpr_write_b32 a204, v48\n"
    "v_accvgpr_write_b32 a205, v49\n"
    "v_accvgpr_write_b32 a206, v50\n"
    "v_accvgpr_write_b32 a207, v51\n"
    "s_waitcnt lgkmcnt(13)\n"
    "v_mfma_f32_16x16x32_bf16 v[20:23], a[152:155], v[52:55], v[20:23]\n"
    "v_accvgpr_write_b32 a208, v52\n"
    "v_accvgpr_write_b32 a209, v53\n"
    "v_accvgpr_write_b32 a210, v54\n"
    "v_accvgpr_write_b32 a211, v55\n"
    "s_waitcnt lgkmcnt(12)\n"
    "v_mfma_f32_16x16x32_bf16 v[20:23], a[156:159], v[56:59], v[20:23]\n"
    "v_accvgpr_write_b32 a212, v56\n"
    "v_accvgpr_write_b32 a213, v57\n"
    "v_accvgpr_write_b32 a214, v58\n"
    "v_accvgpr_write_b32 a215, v59\n"
    "s_add_u32 m0, s65, 53248\n"
    "s_add_u32 s72, s85, 397312\n"
    "buffer_load_dwordx4 v89, s[20:23], s72 offen offset:0 nt lds\n"
    "buffer_load_dwordx2 v[236:237], v87, s[36:39], 0 offen offset:0 sc1\n"
    "ds_read_b128 v[16:19], v78 offset:0\n"
    "ds_read_b128 v[32:35], v78 offset:16\n"
    "ds_read_b128 v[48:51], v78 offset:128\n"
    "ds_read_b128 v[64:67], v78 offset:144\n"
    "ds_read_b128 v[100:103], v78 offset:768\n"
    "ds_read_b128 v[104:107], v78 offset:784\n"
    "s_add_u32 m0, s65, 54272\n"
    "s_add_u32 s72, s85, 398336\n"
    "buffer_load_dwordx4 v90, s[20:23], s72 offen offset:0 nt lds\n"
    "s_add_u32 m0, s65, 98304\n"
    "s_add_u32 s72, s85, 589824\n"
    "buffer_load_dwordx4 v88, s[20:23], s72 offen offset:0 nt lds\n"
    "s_waitcnt lgkmcnt(0)\n"
    "s_barrier\n"
    "ds_read_b128 v[80:83], v79 offset:0\n"
    "s_waitcnt vmcnt(14)\n"
    "buffer_store_dwordx2 v[238:239], v240, s[36:39], s80 offen offset:0 sc1\n"
    "s_lshl_b32 s73, s79, 8\n"
    "s_add_u32 s80, s80, s73\n"
    "s_mul_i32 s73, s79, 98304\n"
    "s_add_u32 s71, s71, s73\n"
    "s_add_u32 m0, s65, 99328\n"
    "s_add_u32 s72, s85, 590848\n"
    "buffer_load_dwordx4 v89, s[20:23], s72 offen offset:0 nt lds\n"
    "s_waitcnt lgkmcnt(0)\n"
    "v_max3_f32 v76, v80, v81, v82\n"
    "v_max_f32 v76, v76, v83\n"
    "v_add_f32 v76, 0xc2b00000, v76\n"
    "s_add_u32 m0, s65, 100352\n"
    "s_add_u32 s72, s85, 591872\n"
    "buffer_load_dwordx4 v90, s[20:23], s72 offen offset:0 nt lds\n"
    "s_add_u32 m0, s65, 101376\n"
    "s_add_u32 s72, s85, 592896\n"
    "buffer_load_dwordx4 v88, s[20:23], s72 offen offset:0 nt lds\n"
    "s_add_u32 m0, s65, 102400\n"
    "s_add_u32 s72, s85, 593920\n"
    "buffer_load_dwordx4 v89, s[20:23], s72 offen offset:0 nt lds\n"
    "s_add_u32 m0, s65, 103424\n"
    "s_add_u32 s72, s85, 594944\n"
    "buffer_load_dwordx4 v90, s[20:23], s72 offen offset:0 nt lds\n"
    "v_sub_f32 v48, v48, v76\n"
    "v_sub_f32 v51, v51, v76\n"
    "v_sub_f32 v49, v49, v76\n"
    "v_sub_f32 v50, v50, v76\n"
    "v_sub_f32 v66, v66, v76\n"
    "v_sub_f32 v65, v65, v76\n"
    "v_mfma_f32_16x16x32_bf16 v[36:39], a[0:3], a[192:195], v[48:51]\n"
    "s_waitcnt vmcnt(14)\n"
    "ds_read_b128 v[200:203], v97 offset:24832\n"
    "v_sub_f32 v67, v67, v76\n"
    "v_mfma_f32_16x16x32_bf16 v[36:39], a[4:7], a[196:199], v[36:39]\n"
    "v_sub_f32 v33, v33, v76\n"
    "v_sub_f32 v32, v32, v76\n"
    "v_mfma_f32_16x16x32_bf16 v[36:39], a[8:11], a[200:203], v[36:39]\n"
    "ds_read_b128 v[184:187], v97 offset:24576\n"
    "ds_read_b128 v[196:199], v96 offset:24832\n"
    "ds_read_b128 v[192:195], v97 offset:24704\n"
    "v_mfma_f32_16x16x32_bf16 v[36:39], a[12:15], a[204:207], v[36:39]\n"
    "v_sub_f32 v64, v64, v76\n"
    "v_sub_f32 v35, v35, v76\n"
    "v_sub_f32 v19, v19, v76\n"
    "v_mfma_f32_16x16x32_bf16 v[52:55], a[24:27], a[192:195], v[64:67]\n"
    "v_add_f32 v21, v21, v33\n"
    "v_sub_f32 v16, v16, v76\n"
    "v_mfma_f32_16x16x32_bf16 v[36:39], a[16:19], a[208:211], v[36:39]\n"
    "v_sub_f32 v18, v18, v76\n"
    "v_add_f32 v20, v20, v32\n"
    "v_sub_f32 v17, v17, v76\n"
    "v_mfma_f32_16x16x32_bf16 v[52:55], a[28:31], a[196:199], v[52:55]\n"
    "ds_read_b128 v[188:191], v96 offset:24704\n"
    "ds_read_b128 v[180:183], v96 offset:24576\n"
    "v_add_f32 v6, v6, v18\n"
    "v_sub_f32 v34, v34, v76\n"
    "v_mfma_f32_16x16x32_bf16 v[36:39], a[20:23], a[212:215], v[36:39]\n"
    "s_waitcnt lgkmcnt(5)\n"
    "v_pk_maximum3_f16 v98, v200, v201, v98\n"
    "v_exp_f32 v20, v20\n"
    "v_pk_maximum3_f16 v98, v202, v203, v98\n"
    "v_pk_add_u16 v203, s89, v203 clamp\n"
    "v_pk_add_u16 v200, s89, v200 clamp\n"
    "v_mfma_f32_16x16x32_bf16 v[52:55], a[32:35], a[200:203], v[52:55]\n"
    "s_waitcnt lgkmcnt(3)\n"
    "v_pk_maximum3_f16 v98, v196, v197, v98\n"
    "v_pk_maximum3_f16 v98, v198, v199, v98\n"
    "v_mfma_f32_16x16x32_bf16 v[52:55], a[36:39], a[204:207], v[52:55]\n"
    "v_exp_f32 v21, v21\n"
    "v_pk_add_u16 v202, s89, v202 clamp\n"
    "v_pk_add_u16 v201, s89, v201 clamp\n"
    "v_mfma_f32_16x16x32_bf16 v[52:55], a[40:43], a[208:211], v[52:55]\n"
    "v_add_f32 v22, v22, v34\n"
    "v_add_f32 v7, v7, v19\n"
    "v_mfma_f32_16x16x32_bf16 v[24:27], a[156:159], v[200:203], v[32:35]\n"
    "s_waitcnt lgkmcnt(2)\n"
    "v_pk_maximum3_f16 v98, v192, v193, v98\n"
    "v_pk_maximum3_f16 v98, v194, v195, v98\n"
    "v_pk_add_u16 v193, s89, v193 clamp\n"
    "v_pk_add_u16 v198, s89, v198 clamp\n"
    "v_add_f32 v5, v5, v17\n"
    "v_mfma_f32_16x16x32_bf16 v[52:55], a[44:47], a[212:215], v[52:55]\n"
    "s_waitcnt lgkmcnt(1)\n"
    "v_pk_add_u16 v194, s89, v194 clamp\n"
    "v_sub_f32 v69, v69, v76\n"
    "v_sub_f32 v73, v73, v76\n"
    "v_pk_maximum3_f16 v98, v188, v189, v98\n"
    "v_pk_maximum3_f16 v98, v190, v191, v98\n"
    "v_pk_add_u16 v196, s89, v196 clamp\n"
    "v_pk_maximum3_f16 v98, v184, v185, v98\n"
    "v_pk_add_u16 v197, s89, v197 clamp\n"
    "v_pk_maximum3_f16 v98, v186, v187, v98\n"
    "v_mfma_f32_16x16x32_bf16 v[8:11], a[132:135], v[200:203], v[16:19]\n"
    "v_sub_f32 v75, v75, v76\n"
    "v_pk_add_u16 v199, s89, v199 clamp\n"
    "v_sub_f32 v101, v101, v76\n"
    "v_exp_f32 v54, v54\n"
    "v_mfma_f32_16x16x32_bf16 v[24:27], a[152:155], v[196:199], v[24:27]\n"
    "s_waitcnt lgkmcnt(0)\n"
    "v_pk_add_u16 v188, s89, v188 clamp\n"
    "v_pk_add_u16 v184, s89, v184 clamp\n"
    "v_sub_f32 v70, v70, v76\n"
    "v_pk_maximum3_f16 v98, v180, v181, v98\n"
    "v_pk_add_u16 v186, s89, v186 clamp\n"
    "v_mfma_f32_16x16x32_bf16 v[56:59], a[44:47], v[200:203], v[64:67]\n"
    "v_pk_add_u16 v189, s89, v189 clamp\n"
    "v_pk_add_u16 v195, s89, v195 clamp\n"
    "v_pk_add_u16 v192, s89, v192 clamp\n"
    "v_pk_add_u16 v185, s89, v185 clamp\n"
    "v_pk_maximum3_f16 v98, v182, v183, v98\n"
    "v_exp_f32 v55, v55\n"
    "v_sub_f32 v68, v68, v76\n"
    "v_mfma_f32_16x16x32_bf16 v[56:59], a[40:43], v[196:199], v[56:59]\n"
    "v_exp_f32 v52, v52\n"
    "v_pk_add_u16 v190, s89, v190 clamp\n"
    "v_mfma_f32_16x16x32_bf16 v[8:11], a[128:131], v[196:199], v[8:11]\n"
    "v_accvgpr_write_b32 a231, v195\n"
    "v_sub_f32 v102, v102, v76\n"
    "v_sub_f32 v71, v71, v76\n"
    "v_sub_f32 v107, v107, v76\n"
    "v_mfma_f32_16x16x32_bf16 v[40:43], a[20:23], v[200:203], v[48:51]\n"
    "v_sub_f32 v74, v74, v76\n"
    "v_add_f32 v23, v23, v35\n"
    "v_mfma_f32_16x16x32_bf16 v[40:43], a[16:19], v[196:199], v[40:43]\n"
    "v_pk_add_u16 v183, s89, v183 clamp\n"
    "v_accvgpr_write_b32 a228, v192\n"
    "v_mfma_f32_16x16x32_bf16 v[56:59], a[36:39], v[192:195], v[56:59]\n"
    "v_add_f32 v4, v4, v16\n"
    "v_exp_f32 v6, v6\n"
    "v_mfma_f32_16x16x32_bf16 v[24:27], a[148:151], v[192:195], v[24:27]\n"
    "v_sub_f32 v72, v72, v76\n"
    "v_pk_add_u16 v187, s89, v187 clamp\n"
    "v_pk_add_u16 v191, s89, v191 clamp\n"
    "v_mfma_f32_16x16x32_bf16 v[8:11], a[124:127], v[192:195], v[8:11]\n"
    "v_exp_f32 v53, v53\n"
    "v_accvgpr_write_b32 a226, v190\n"
    "v_mfma_f32_16x16x32_bf16 v[8:11], a[120:123], v[188:191], v[8:11]\n"
    "v_accvgpr_write_b32 a221, v185\n"
    "v_exp_f32 v39, v39\n"
    "v_exp_f32 v22, v22\n"
    "v_pk_add_u16 v181, s89, v181 clamp\n"
    "v_mfma_f32_16x16x32_bf16 v[56:59], a[32:35], v[188:191], v[56:59]\n"
    "v_exp_f32 v36, v36\n"
    "v_pk_add_u16 v182, s89, v182 clamp\n"
    "v_exp_f32 v23, v23\n"
    "v_mfma_f32_16x16x32_bf16 v[24:27], a[144:147], v[188:191], v[24:27]\n"
    "v_accvgpr_write_b32 a237, v201\n"
    "v_pk_add_u16 v180, s89, v180 clamp\n"
    "v_mfma_f32_16x16x32_bf16 v[40:43], a[12:15], v[192:195], v[40:43]\n"
    "v_accvgpr_write_b32 a239, v203\n"
    "v_accvgpr_write_b32 a223, v187\n"
    "v_exp_f32 v7, v7\n"
    "v_accvgpr_write_b32 a233, v197\n"
    "v_mfma_f32_16x16x32_bf16 v[8:11], a[116:119], v[184:187], v[8:11]\n"
    "v_accvgpr_write_b32 a232, v196\n"
    "v_accvgpr_write_b32 a235, v199\n"
    "v_mfma_f32_16x16x32_bf16 v[40:43], a[8:11], v[188:191], v[40:43]\n"
    "ds_write_b128 v86, v[68:71] offset:0\n"
    "v_mfma_f32_16x16x32_bf16 v[40:43], a[4:7], v[184:187], v[40:43]\n"
    "v_sub_f32 v106, v106, v76\n"
    "v_accvgpr_write_b32 a234, v198\n"
    "v_mfma_f32_16x16x32_bf16 v[8:11], a[112:115], v[180:183], v[8:11]\n"
    "v_cvt_pk_bf16_f32 v70, v20, v21\n"
    "v_exp_f32 v37, v37\n"
    "v_cvt_pk_bf16_f32 v69, v6, v7\n"
    "v_accvgpr_write_b32 a218, v182\n"
    "v_mfma_f32_16x16x32_bf16 v[40:43], a[0:3], v[180:183], v[40:43]\n"
    "ds_write_b128 v86, v[72:75] offset:16\n"
    "v_accvgpr_write_b32 a230, v194\n"
    "v_mfma_f32_16x16x32_bf16 v[24:27], a[140:143], v[184:187], v[24:27]\n"
    "v_exp_f32 v10, v10\n"
    "v_mfma_f32_16x16x32_bf16 v[56:59], a[28:31], v[184:187], v[56:59]\n"
    "v_cvt_pk_bf16_f32 v72, v36, v37\n"
    "v_sub_f32 v103, v103, v76\n"
    "v_mfma_f32_16x16x32_bf16 v[56:59], a[24:27], v[180:183], v[56:59]\n"
    "v_sub_f32 v104, v104, v76\n"
    "v_accvgpr_write_b32 a217, v181\n"
    "v_exp_f32 v4, v4\n"
    "v_sub_f32 v100, v100, v76\n"
    "v_accvgpr_write_b32 a227, v191\n"
    "v_exp_f32 v11, v11\n"
    "v_exp_f32 v5, v5\n"
    "v_exp_f32 v38, v38\n"
    "v_exp_f32 v41, v41\n"
    "v_exp_f32 v9, v9\n"
    "v_exp_f32 v59, v59\n"
    "v_accvgpr_write_b32 a220, v184\n"
    "v_accvgpr_write_b32 a238, v202\n"
    "v_accvgpr_write_b32 a236, v200\n"
    "v_exp_f32 v43, v43\n"
    "v_cvt_pk_bf16_f32 v77, v10, v11\n"
    "v_exp_f32 v8, v8\n"
    "v_cvt_pk_bf16_f32 v75, v54, v55\n"
    "v_mfma_f32_16x16x32_bf16 v[24:27], a[136:139], v[180:183], v[24:27]\n"
    "v_cvt_pk_bf16_f32 v71, v22, v23\n"
    "v_sub_f32 v105, v105, v76\n"
    "v_accvgpr_write_b32 a216, v180\n"
    "v_exp_f32 v58, v58\n"
    "v_cvt_pk_bf16_f32 v73, v38, v39\n"
    "v_exp_f32 v40, v40\n"
    "v_accvgpr_write_b32 a222, v186\n"
    "v_accvgpr_write_b32 a225, v189\n"
    "v_exp_f32 v24, v24\n"
    "v_exp_f32 v57, v57\n"
    "v_cvt_pk_bf16_f32 v68, v4, v5\n"
    "v_cvt_pk_bf16_f32 v80, v40, v41\n"
    "v_exp_f32 v27, v27\n"
    "v_exp_f32 v25, v25\n"
    "v_cvt_pk_bf16_f32 v76, v8, v9\n"
    "v_accvgpr_write_b32 a224, v188\n"
    "v_accvgpr_write_b32 a219, v183\n"
    "v_exp_f32 v26, v26\n"
    "v_exp_f32 v42, v42\n"
    "v_cvt_pk_bf16_f32 v78, v24, v25\n"
    "v_exp_f32 v56, v56\n"
    "v_cvt_pk_bf16_f32 v74, v52, v53\n"
    "v_accvgpr_write_b32 a229, v193\n"
    "v_cvt_pk_bf16_f32 v79, v26, v27\n"
    "v_cvt_pk_bf16_f32 v82, v56, v57\n"
    "v_cvt_pk_bf16_f32 v83, v58, v59\n"
    "v_cvt_pk_bf16_f32 v81, v42, v43\n"
    "s_waitcnt vmcnt(8)\n"
    "v_cmp_eq_u64 s[54:55], v[236:237], v[238:239]\n"
    "s_nop 0\n"
    "s_cmp_eq_u64 s[54:55], -1\n"
    "s_cbranch_scc0 .LpollAK\n"
    ".LpollAKr:\n"
    "s_add_u32 m0, s65, 24576\n"
    "s_add_u32 s72, s65, 49152\n"
    "buffer_load_dwordx4 v1, s[16:19], s72 offen offset:0 sc1 lds\n"
    "s_add_u32 m0, s65, 25600\n"
    "s_add_u32 s72, s65, 50176\n"
    "buffer_load_dwordx4 v1, s[16:19], s72 offen offset:0 sc1 lds\n"
    "s_add_u32 m0, s65, 26624\n"
    "s_add_u32 s72, s65, 51200\n"
    "buffer_load_dwordx4 v1, s[16:19], s72 offen offset:0 sc1 lds\n"
    "s_add_u32 m0, s65, 27648\n"
    "s_add_u32 s72, s65, 52224\n"
    "buffer_load_dwordx4 v1, s[16:19], s72 offen offset:0 sc1 lds\n"
    "s_add_u32 m0, s65, 28672\n"
    "s_add_u32 s72, s65, 53248\n"
    "buffer_load_dwordx4 v1, s[16:19], s72 offen offset:0 sc1 lds\n"
    "s_add_u32 m0, s65, 29696\n"
    "s_add_u32 s72, s65, 54272\n"
    "buffer_load_dwordx4 v1, s[16:19], s72 offen offset:0 sc1 lds\n"
    "s_waitcnt vmcnt(13)\n"
    "ds_read_b128 v[156:159], v96 offset:0\n"
    "ds_read_b128 v[172:175], v96 offset:256\n"
    "ds_read_b128 v[168:171], v97 offset:128\n"
    "ds_read_b128 v[176:179], v97 offset:256\n"
    "ds_read_b128 v[160:163], v97 offset:0\n"
    "ds_read_b128 v[164:167], v96 offset:128\n"
    "s_waitcnt lgkmcnt(5)\n"
    "v_pk_maximum3_f16 v98, v156, v157, v98\n"
    "v_pk_maximum3_f16 v98, v158, v159, v98\n"
    "v_pk_add_u16 v157, s89, v157 clamp\n"
    "v_pk_add_u16 v158, s89, v158 clamp\n"
    "v_pk_add_u16 v156, s89, v156 clamp\n"
    "v_pk_add_u16 v159, s89, v159 clamp\n"
    "s_waitcnt lgkmcnt(1)\n"
    "v_pk_maximum3_f16 v98, v160, v161, v98\n"
    "s_add_u32 m0, s65, 73728\n"
    "s_add_u32 s72, s65, 73728\n"
    "buffer_load_dwordx4 v1, s[16:19], s72 offen offset:0 sc1 lds\n"
    "v_mfma_f32_16x16x32_bf16 v[28:31], a[136:139], v[156:159], v[32:35]\n"
    "s_waitcnt lgkmcnt(0)\n"
    "v_pk_maximum3_f16 v98, v162, v163, v98\n"
    "v_pk_maximum3_f16 v98, v164, v165, v98\n"
    "v_mfma_f32_16x16x32_bf16 v[12:15], a[112:115], v[156:159], v[16:19]\n"
    "v_pk_add_u16 v161, s89, v161 clamp\n"
    "v_pk_maximum3_f16 v98, v166, v167, v98\n"
    "v_pk_add_u16 v160, s89, v160 clamp\n"
    "v_pk_add_u16 v162, s89, v162 clamp\n"
    "v_mfma_f32_16x16x32_bf16 v[60:63], a[24:27], v[156:159], v[64:67]\n"
    "v_pk_maximum3_f16 v98, v168, v169, v98\n"
    "v_pk_add_u16 v163, s89, v163 clamp\n"
    "v_mfma_f32_16x16x32_bf16 v[44:47], a[0:3], v[156:159], v[48:51]\n"
    "v_pk_maximum3_f16 v98, v170, v171, v98\n"
    "v_pk_add_u16 v164, s89, v164 clamp\n"
    "v_mfma_f32_16x16x32_bf16 v[12:15], a[116:119], v[160:163], v[12:15]\n"
    "v_pk_add_u16 v165, s89, v165 clamp\n"
    "s_add_u32 m0, s65, 74752\n"
    "s_add_u32 s72, s65, 74752\n"
    "buffer_load_dwordx4 v1, s[16:19], s72 offen offset:0 sc1 lds\n"
    "v_pk_add_u16 v169, s89, v169 clamp\n"
    "v_pk_add_u16 v168, s89, v168 clamp\n"
    "v_mfma_f32_16x16x32_bf16 v[60:63], a[28:31], v[160:163], v[60:63]\n"
    "v_pk_maximum3_f16 v98, v172, v173, v98\n"
    "v_pk_add_u16 v170, s89, v170 clamp\n"
    "v_mfma_f32_16x16x32_bf16 v[28:31], a[140:143], v[160:163], v[28:31]\n"
    "v_pk_maximum3_f16 v98, v174, v175, v98\n"
    "v_pk_add_u16 v166, s89, v166 clamp\n"
    "v_pk_add_u16 v167, s89, v167 clamp\n"
    "v_mfma_f32_16x16x32_bf16 v[44:47], a[4:7], v[160:163], v[44:47]\n"
    "v_pk_maximum3_f16 v98, v176, v177, v98\n"
    "v_accvgpr_write_b32 a251, v167\n"
    "v_mfma_f32_16x16x32_bf16 v[44:47], a[8:11], v[164:167], v[44:47]\n"
    "v_pk_maximum3_f16 v98, v178, v179, v98\n"
    "v_pk_add_u16 v176, s89, v176 clamp\n"
    "v_mfma_f32_16x16x32_bf16 v[60:63], a[32:35], v[164:167], v[60:63]\n"
    "v_pk_add_u16 v171, s89, v171 clamp\n"
    "s_add_u32 m0, s65, 75776\n"
    "s_add_u32 s72, s65, 75776\n"
    "buffer_load_dwordx4 v1, s[16:19], s72 offen offset:0 sc1 lds\n"
    "v_pk_add_u16 v174, s89, v174 clamp\n"
    "v_mfma_f32_16x16x32_bf16 v[28:31], a[144:147], v[164:167], v[28:31]\n"
    "v_pk_add_u16 v173, s89, v173 clamp\n"
    "v_pk_add_u16 v172, s89, v172 clamp\n"
    "v_mfma_f32_16x16x32_bf16 v[12:15], a[120:123], v[164:167], v[12:15]\n"
    "v_accvgpr_write_b32 a254, v170\n"
    "v_pk_add_u16 v178, s89, v178 clamp\n"
    "v_mfma_f32_16x16x32_bf16 v[28:31], a[148:151], v[168:171], v[28:31]\n"
    "v_accvgpr_write_b32 a243, v159\n"
    "v_accvgpr_write_b32 a162, v174\n"
    "v_mfma_f32_16x16x32_bf16 v[12:15], a[124:127], v[168:171], v[12:15]\n"
    "v_pk_add_u16 v179, s89, v179 clamp\n"
    "v_pk_add_u16 v175, s89, v175 clamp\n"
    "v_mfma_f32_16x16x32_bf16 v[44:47], a[12:15], v[168:171], v[44:47]\n"
    "v_accvgpr_write_b32 a166, v178\n"
    "v_accvgpr_write_b32 a161, v173\n"
    "v_accvgpr_write_b32 a245, v161\n"
    "s_add_u32 m0, s65, 76800\n"
    "s_add_u32 s72, s65, 76800\n"
    "buffer_load_dwordx4 v1, s[16:19], s72 offen offset:0 sc1 lds\n"
    "v_mfma_f32_16x16x32_bf16 v[28:31], a[152:155], v[172:175], v[28:31]\n"
    "v_accvgpr_write_b32 a163, v175\n"
    "v_pk_add_u16 v177, s89, v177 clamp\n"
    "v_mfma_f32_16x16x32_bf16 v[44:47], a[16:19], v[172:175], v[44:47]\n"
    "v_accvgpr_write_b32 a160, v172\n"
    "v_accvgpr_write_b32 a252, v168\n"
    "v_mfma_f32_16x16x32_bf16 v[28:31], a[156:159], v[176:179], v[28:31]\n"
    "v_accvgpr_write_b32 a249, v165\n"
    "v_accvgpr_write_b32 a255, v171\n"
    "v_mfma_f32_16x16x32_bf16 v[12:15], a[128:131], v[172:175], v[12:15]\n"
    "v_accvgpr_write_b32 a247, v163\n"
    "v_accvgpr_write_b32 a164, v176\n"
    "v_mfma_f32_16x16x32_bf16 v[60:63], a[36:39], v[168:171], v[60:63]\n"
    "v_accvgpr_write_b32 a242, v158\n"
    "v_accvgpr_write_b32 a250, v166\n"
    "v_mfma_f32_16x16x32_bf16 v[44:47], a[20:23], v[176:179], v[44:47]\n"
    "v_accvgpr_write_b32 a241, v157\n"
    "s_add_u32 m0, s65, 77824\n"
    "s_add_u32 s72, s65, 77824\n"
    "buffer_load_dwordx4 v1, s[16:19], s72 offen offset:0 sc1 lds\n"
    "v_accvgpr_write_b32 a248, v164\n"
    "v_mfma_f32_16x16x32_bf16 v[12:15], a[132:135], v[176:179], v[12:15]\n"
    "v_accvgpr_write_b32 a246, v162\n"
    "v_exp_f32 v29, v29\n"
    "v_mfma_f32_16x16x32_bf16 v[60:63], a[40:43], v[172:175], v[60:63]\n"
    "v_exp_f32 v28, v28\n"
    "v_mfma_f32_16x16x32_bf16 v[60:63], a[44:47], v[176:179], v[60:63]\n"
    "v_exp_f32 v31, v31\n"
    "v_exp_f32 v30, v30\n"
    "v_exp_f32 v45, v45\n"
    "v_cvt_pk_bf16_f32 v86, v28, v29\n"
    "v_exp_f32 v47, v47\n"
    "v_accvgpr_write_b32 a240, v156\n"
    "v_exp_f32 v12, v12\n"
    "v_exp_f32 v14, v14\n"
    "v_accvgpr_write_b32 a167, v179\n"
    "v_exp_f32 v15, v15\n"
    "s_add_u32 m0, s65, 78848\n"
    "s_add_u32 s72, s65, 78848\n"
    "buffer_load_dwordx4 v1, s[16:19], s72 offen offset:0 sc1 lds\n"
    "v_accvgpr_write_b32 a253, v169\n"
    "v_exp_f32 v63, v63\n"
    "v_exp_f32 v13, v13\n"
    "v_exp_f32 v44, v44\n"
    "v_cvt_pk_bf16_f32 v84, v12, v13\n"
    "v_cvt_pk_bf16_f32 v87, v30, v31\n"
    "v_exp_f32 v46, v46\n"
    "v_exp_f32 v61, v61\n"
    "v_accvgpr_write_b32 a165, v177\n"
    "v_exp_f32 v60, v60\n"
    "v_exp_f32 v62, v62\n"
    "v_cvt_pk_bf16_f32 v90, v60, v61\n"
    "v_accvgpr_write_b32 a244, v160\n"
    "v_cvt_pk_bf16_f32 v85, v14, v15\n"
    "v_cvt_pk_bf16_f32 v89, v46, v47\n"
    "v_cvt_pk_bf16_f32 v88, v44, v45\n"
    "v_cvt_pk_bf16_f32 v91, v62, v63\n"
    "s_bitcmp1_b32 s83, 1\n"
    "s_cbranch_scc1 .Lf3b\n"
    "s_mov_b32 s66, 1\n"
    "s_add_u32 s68, s68, 24576\n"
    "s_add_u32 s81, s81, 128\n"
    "s_add_u32 s80, s80, 256\n"
    "s_add_u32 s71, s71, 98304\n"
    "s_add_u32 s75, s71, s63\n"
    "s_add_u32 s76, s75, 6144\n"
    "s_add_u32 s77, s75, 24576\n"
    "s_add_u32 s78, s75, 30720\n"
    "s_branch .LhKP\n"
    "s_branch .Lf3e\n"
    ".Lf3b:\n"
    "s_add_u32 s75, s71, s63\n"
    "s_add_u32 s76, s75, 6144\n"
    "s_add_u32 s77, s75, 24576\n"
    "s_add_u32 s78, s75, 30720\n"
    "buffer_load_dwordx2 v[204:205], v242, s[28:31], s75 offen offset:0 nt\n"
    "buffer_load_dwordx2 v[206:207], v242, s[28:31], s75 offen offset:768 nt\n"
    "buffer_load_dwordx2 v[208:209], v242, s[28:31], s75 offen offset:3072 nt\n"
    "buffer_load_dwordx2 v[210:211], v242, s[28:31], s75 offen offset:3840 nt\n"
    "buffer_load_dwordx2 v[212:213], v242, s[28:31], s76 offen offset:0 nt\n"
    "buffer_load_dwordx2 v[214:215], v242, s[28:31], s76 offen offset:768 nt\n"
    "buffer_load_dwordx2 v[216:217], v242, s[28:31], s76 offen offset:3072 nt\n"
    "buffer_load_dwordx2 v[218:219], v242, s[28:31], s76 offen offset:3840 nt\n"
    "buffer_load_dwordx2 v[220:221], v242, s[28:31], s77 offen offset:0 nt\n"
    "buffer_load_dwordx2 v[222:223], v242, s[28:31], s77 offen offset:768 nt\n"
    "buffer_load_dwordx2 v[224:225], v242, s[28:31], s77 offen offset:3072 nt\n"
    "buffer_load_dwordx2 v[226:227], v242, s[28:31], s77 offen offset:3840 nt\n"
    "buffer_load_dwordx2 v[228:229], v242, s[28:31], s78 offen offset:0 nt\n"
    "buffer_load_dwordx2 v[230:231], v242, s[28:31], s78 offen offset:768 nt\n"
    "buffer_load_dwordx2 v[232:233], v242, s[28:31], s78 offen offset:3072 nt\n"
    "buffer_load_dwordx2 v[234:235], v242, s[28:31], s78 offen offset:3840 nt\n"
    "s_mov_b32 s66, 1\n"
    "s_add_u32 s68, s68, 24576\n"
    "s_add_u32 s81, s81, 128\n"
    "s_branch .LhKN\n"
    ".Lf3e:\n"
    "s_nop 4\n"
    "s_branch .Lf5e\n"
    ".Lf5b:\n"
    "s_lshl_b32 s73, s79, 8\n"
    "s_mul_i32 s74, s59, 6144\n"
    "s_add_u32 s73, s73, s74\n"
    "v_add_u32 v163, s73, v244\n"
    "s_lshr_b32 s73, s53, 2\n"
    "s_mul_i32 s73, s73, 12288\n"
    "s_and_b32 s74, s53, 3\n"
    "s_lshl_b32 s74, s74, 8\n"
    "s_add_u32 s73, s73, s74\n"
    "s_cmp_eq_u32 s59, 0\n"
    "s_cselect_b32 s73, 0x7f000000, s73\n"
    "s_add_u32 s60, s73, 24576\n"
    "s_add_u32 s61, s73, 28672\n"
    "s_add_u32 s62, s73, 32768\n"
    "v_mov_b32 v244, v243\n"
    "v_mov_b32 v243, 0\n"
    "s_sub_u32 s8, s40, 1572864\n"
    "s_subb_u32 s9, s41, 0\n"
    "buffer_load_dwordx4 v[40:43], v242, s[40:43], s75 offen offset:768 nt\n"
    "v_add_u32 v159, 0, v156\n"
    "v_mul_u32_u24 v159, 2731, v159\n"
    "v_lshrrev_b32 v159, 16, v159\n"
    "v_xor_b32 v159, v159, v156\n"
    "v_and_b32 v159, 7, v159\n"
    "v_and_b32 v88, 0x380, v1\n"
    "v_lshl_add_u32 v88, v159, 4, v88\n"
    "v_add_u32 v159, 64, v156\n"
    "v_mul_u32_u24 v159, 2731, v159\n"
    "v_lshrrev_b32 v159, 16, v159\n"
    "v_xor_b32 v159, v159, v156\n"
    "v_and_b32 v159, 7, v159\n"
    "v_and_b32 v89, 0x380, v1\n"
    "v_lshl_add_u32 v89, v159, 4, v89\n"
    "v_add_u32 v159, 128, v156\n"
    "v_mul_u32_u24 v159, 2731, v159\n"
    "v_lshrrev_b32 v159, 16, v159\n"
    "v_xor_b32 v159, v159, v156\n"
    "v_and_b32 v159, 7, v159\n"
    "v_and_b32 v90, 0x380, v1\n"
    "v_lshl_add_u32 v90, v159, 4, v90\n"
    "v_and_b32 v2, 7, v156\n"
    "v_lshrrev_b32 v144, 7, v130\n"
    "v_mul_u32_u24 v144, 768, v144\n"
    "v_lshl_add_u32 v144, v2, 3, v144\n"
    "buffer_load_dwordx4 v[44:47], v242, s[40:43], s75 offen offset:256 nt\n"
    "v_add_u32 v144, 131072, v144\n"
    "v_mad_u32_u24 v86, v2, 24, v144\n"
    "v_lshl_add_u32 v130, s82, 7, v130\n"
    "v_and_b32 v130, 4095, v130\n"
    "v_and_b32 v96, 15, v156\n"
    "v_lshrrev_b32 v159, 4, v156\n"
    "v_xor_b32 v159, v159, v2\n"
    "v_mul_u32_u24 v96, 384, v96\n"
    "v_lshl_add_u32 v96, v159, 4, v96\n"
    "s_add_u32 s73, s65, 49152\n"
    "v_add_u32 v96, s73, v96\n"
    "v_xor_b32 v97, 64, v96\n"
    "v_subrev_u32 v154, 24576, v96\n"
    "v_subrev_u32 v155, 24576, v97\n"
    "s_add_u32 s67, s65, 98304\n"
    "s_add_u32 s68, s65, 24576\n"
    "s_add_u32 s69, s65, 73728\n"
    "s_mov_b32 s81, 256\n"
    "s_mul_i32 s74, s79, 24576\n"
    "s_add_u32 s70, s74, s65\n"
    "s_add_u32 s70, s70, 73728\n"
    "s_sub_u32 s64, s65, s74\n"
    "s_add_u32 s64, s64, 98304\n"
    "v_and_b32 v249, 0x310, v1\n"
    "v_and_b32 v159, 12, v156\n"
    "v_lshl_add_u32 v249, v159, 3, v249\n"
    "buffer_load_dwordx4 v[48:51], v242, s[40:43], s75 offen offset:1024 nt\n"
    "v_and_b32 v159, 2, v156\n"
    "v_lshl_add_u32 v249, v159, 6, v249\n"
    "s_add_u32 s73, s74, 73728\n"
    "v_add_u32 v248, s73, v249\n"
    "s_sub_u32 s73, 98304, s74\n"
    "v_add_u32 v249, s73, v249\n"
    "s_or_b32 s73, s53, s84\n"
    "s_cmp_eq_u32 s73, 0\n"
    "s_cselect_b32 s82, 0, 0x7fff0000\n"
    "v_and_b32 v252, 12, v156\n"
    "v_bfe_u32 v159, v156, 1, 1\n"
    "v_or_b32 v252, v252, v159\n"
    "v_and_b32 v159, 1, v156\n"
    "v_lshl_or_b32 v252, v159, 1, v252\n"
    "v_lshlrev_b32 v252, 6, v252\n"
    "v_lshlrev_b32 v159, 2, v156\n"
    "v_and_b32 v159, 32, v159\n"
    "v_xor_b32 v159, v159, v156\n"
    "v_and_or_b32 v252, v159, 48, v252\n"
    "v_mov_b32 v2, v252\n"
    "v_add_u32 v3, 24576, v252\n"
    "v_lshrrev_b32 v91, 3, v156\n"
    "v_lshl_add_u32 v87, v91, 6, 64\n"
    "v_add_u32 v87, 64, v87\n"
    "v_and_b32 v241, 1, v156\n"
    "v_lshlrev_b32 v241, 3, v241\n"
    "buffer_load_dwordx4 v[52:55], v242, s[40:43], s75 offen offset:512 nt\n"
    "v_lshl_add_u32 v241, v91, 4, v241\n"
    "v_add_u32 v241, 1152, v241\n"
    "v_lshlrev_b32 v91, 24, v91\n"
    "v_lshl_add_u32 v91, v156, 3, v91\n"
    "s_lshl_b32 s73, s53, 4\n"
    "s_lshl_b32 s74, s59, 3\n"
    "s_add_u32 s73, s73, s74\n"
    "s_add_u32 s73, s73, 1152\n"
    "v_mov_b32 v240, 0x7fff0000\n"
    "v_writelane_b32 v240, s73, 0\n"
    "v_and_b32 v159, 48, v156\n"
    "s_mul_i32 s73, s79, 768\n"
    "s_sub_u32 s73, 131072, s73\n"
    "v_lshlrev_b32 v250, 1, v159\n"
    "v_add_u32 v250, s73, v250\n"
    "v_and_b32 v251, 3, v156\n"
    "v_lshl_add_u32 v251, v251, 7, v159\n"
    "s_add_u32 s73, s73, 256\n"
    "v_add_u32 v251, s73, v251\n"
    "v_and_b32 v159, 64, v1\n"
    "v_add_u32 v251, v159, v251\n"
    "v_bfe_i32 v159, v156, 2, 1\n"
    "v_and_b32 v159, 0xaaaa, v159\n"
    "v_or_b32 v253, 0x4444, v159\n"
    "s_mov_b32 s58, 0\n"
    "s_mov_b32 s59, 8\n"
    "s_waitcnt vmcnt(14)\n"
    "v_pk_min_i16 v4, v4, v10\n"
    "v_pk_min_i16 v5, v5, v11\n"
    "v_pk_min_i16 v6, v6, v12\n"
    "v_pk_min_i16 v7, v7, v13\n"
    "v_pk_min_i16 v8, v8, v14\n"
    "v_pk_min_i16 v9, v9, v15\n"
    "v_pk_min_i16 v16, v16, v18\n"
    "v_pk_min_i16 v17, v17, v19\n"
    "v_pk_min_i16 v4, v4, v7\n"
    "v_pk_min_i16 v5, v5, v8\n"
    "v_pk_min_i16 v6, v6, v9\n"
    "v_pk_min_i16 v16, v16, v17\n"
    "v_pk_min_i16 v4, v4, v5\n"
    "v_lshlrev_b32 v17, 16, v16\n"
    "v_pk_min_i16 v4, v4, v6\n"
    "v_min_i32 v16, v16, v17\n"
    "v_lshlrev_b32 v5, 16, v4\n"
    "v_mov_b32 v7, 155920\n"
    "v_min_i32 v4, v4, v5\n"
    "v_lshl_add_u32 v6, s84, 2, v7\n"
    "v_min_i32_dpp v16, v16, v16 row_shr:1\n"
    "v_min_i32_dpp v4, v4, v4 row_shr:1\n"
    "s_nop 0\n"
    "v_min_i32_dpp v16, v16, v16 row_shr:2\n"
    "v_min_i32_dpp v4, v4, v4 row_shr:2\n"
    "s_nop 0\n"
    "v_min_i32_dpp v16, v16, v16 row_shr:4\n"
    "v_min_i32_dpp v4, v4, v4 row_shr:4\n"
    "s_nop 0\n"
    "v_min_i32_dpp v16, v16, v16 row_shr:8\n"
    "v_min_i32_dpp v4, v4, v4 row_shr:8\n"
    "s_nop 0\n"
    "v_min_i32_dpp v16, v16, v16 row_bcast:15 row_mask:0xa\n"
    "v_min_i32_dpp v4, v4, v4 row_bcast:15 row_mask:0xa\n"
    "s_nop 0\n"
    "v_min_i32_dpp v16, v16, v16 row_bcast:31 row_mask:0xc\n"
    "v_min_i32_dpp v4, v4, v4 row_bcast:31 row_mask:0xc\n"
    "v_readlane_b32 s35, v16, 63\n"
    "v_readlane_b32 s73, v4, 63\n"
    "s_max_i32 s35, s35, 0\n"
    "s_and_b32 s35, s35, 0xffff0000\n"
    "v_mov_b32 v4, s73\n"
    "ds_write_b32 v6, v4\n"
    "v_mov_b32 v14, -32\n"
    "v_mov_b32 v12, -126\n"
    "v_mov_b32 v13, 0x3dd53b95\n"
    "v_mov_b32 v98, 0\n"
    "s_waitcnt lgkmcnt(0)\n"
    "s_barrier\n"
    "ds_read_b128 v[8:11], v7 offset:0\n"
    "buffer_load_dwordx4 v[56:59], v242, s[40:43], s75 offen offset:1280 nt\n"
    "buffer_load_dwordx4 v[136:139], v130, s[92:95], 0 offen offset:0\n"
    "buffer_load_dwordx4 v[140:143], v130, s[96:99], 0 offen offset:0\n"
    "s_nop 0\n"
    "s_nop 0\n"
    "s_nop 0\n"
    "s_nop 0\n"
    "s_nop 0\n"
    "s_nop 0\n"
    "s_nop 0\n"
    "s_nop 0\n"
    "s_nop 0\n"
    "s_nop 0\n"
    "s_nop 0\n"
    "s_nop 0\n"
    "s_nop 0\n"
    "s_nop 0\n"
    "s_nop 0\n"
    "s_nop 0\n"
    "s_nop 0\n"
    "s_nop 0\n"
    "s_nop 0\n"
    "s_nop 0\n"
    "s_nop 0\n"
    "s_nop 0\n"
    "s_nop 0\n"
    "s_nop 0\n"
    "s_nop 0\n"
    "s_nop 0\n"
    "s_nop 0\n"
    "s_nop 0\n"
    "s_nop 0\n"
    "s_nop 0\n"
    "s_nop 0\n"
    "s_nop 0\n"
    "s_nop 0\n"
    "s_nop 0\n"
    "s_nop 0\n"
    "s_nop 0\n"
    "s_nop 0\n"
    "s_nop 0\n"
    "s_nop 0\n"
    "s_nop 0\n"
    "s_nop 0\n"
    "s_nop 0\n"
    "s_nop 0\n"
    "s_nop 0\n"
    "s_nop 0\n"
    "s_nop 0\n"
    "s_nop 0\n"
    "s_nop 0\n"
    "s_nop 0\n"
    "s_nop 0\n"
    "s_nop 0\n"
    "s_nop 0\n"
    "s_nop 0\n"
    "s_nop 0\n"
    "s_nop 0\n"
    "s_nop 0\n"
    "s_nop 0\n"
    "s_nop 0\n"
    "s_nop 0\n"
    "s_nop 0\n"
    "s_waitcnt lgkmcnt(0)\n"
    "v_min3_i32 v4, v8, v9, v10\n"
    "v_min_i32 v4, v4, v11\n"
    "v_max_i32 v4, 0, v4\n"
    "v_and_b32 v4, 0xffff0000, v4\n"
    "v_mul_f32 v4, s35, v4\n"
    "v_mul_f32 v4, 0x3ed53b95, v4\n"
    "v_lshrrev_b32 v4, 23, v4\n"
    "v_sub_u32 v4, 2, v4\n"
    "v_med3_i32 v4, v4, v14, v12\n"
    "v_ldexp_f32 v13, v13, v4\n"
    "v_readfirstlane_b32 s89, v4\n"
    "v_readfirstlane_b32 s88, v13\n"
    "s_mul_i32 s89, s89, 0xff7fff80\n"
    "s_mov_b32 s90, 149\n"
    "s_waitcnt vmcnt(11)\n"
    "s_add_u32 s54, s71, s60\n"
    "s_add_u32 s55, s71, s61\n"
    "s_add_u32 s56, s71, s62\n"
    "v_cvt_pk_u16_u32 v204, v204, v208\n"
    "v_cvt_pk_u16_u32 v205, v205, v209\n"
    "v_cvt_pk_u16_u32 v206, v206, v210\n"
    "v_cvt_pk_u16_u32 v207, v207, v211\n"
    "v_or3_b32 v243, v204, v205, v243\n"
    "v_or3_b32 v243, v206, v207, v243\n"
    "buffer_store_dword v204, v244, s[16:19], s54 offen offset:0 sc1\n"
    "buffer_store_dword v205, v244, s[16:19], s54 offen offset:1024 sc1\n"
    "buffer_load_dwordx4 v[4:7], v162, s[28:31], s57 offen offset:0\n"
    "buffer_load_dwordx4 v[8:11], v162, s[28:31], s57 offen offset:3072\n"
    "buffer_store_dword v206, v244, s[16:19], s54 offen offset:2048 sc1\n"
    "buffer_store_dword v207, v244, s[16:19], s54 offen offset:3072 sc1\n"
    "v_cvt_pk_u16_u32 v212, v212, v216\n"
    "v_cvt_pk_u16_u32 v213, v213, v217\n"
    "v_cvt_pk_u16_u32 v214, v214, v218\n"
    "v_cvt_pk_u16_u32 v215, v215, v219\n"
    "v_or3_b32 v243, v212, v213, v243\n"
    "v_or3_b32 v243, v214, v215, v243\n"
    "buffer_store_dword v212, v244, s[16:19], s55 offen offset:0 sc1\n"
    "buffer_store_dword v213, v244, s[16:19], s55 offen offset:1024 sc1\n"
    "buffer_store_dword v214, v244, s[16:19], s55 offen offset:2048 sc1\n"
    "buffer_load_dwordx4 v[12:15], v162, s[28:31], s57 offen offset:128\n"
    "buffer_store_dword v215, v244, s[16:19], s55 offen offset:3072 sc1\n"
    "v_cvt_pk_u16_u32 v220, v220, v224\n"
    "v_cvt_pk_u16_u32 v221, v221, v225\n"
    "v_cvt_pk_u16_u32 v222, v222, v226\n"
    "v_cvt_pk_u16_u32 v223, v223, v227\n"
    "v_or3_b32 v243, v220, v221, v243\n"
    "v_or3_b32 v243, v222, v223, v243\n"
    "buffer_store_dword v220, v244, s[16:19], s56 offen offset:0 sc1\n"
    "buffer_store_dword v221, v244, s[16:19], s56 offen offset:1024 sc1\n"
    "buffer_store_dword v222, v244, s[16:19], s56 offen offset:2048 sc1\n"
    "buffer_store_dword v223, v244, s[16:19], s56 offen offset:3072 sc1\n"
    "s_add_u32 s71, s71, 98304\n"
    "s_add_u32 m0, s65, 24576\n"
    "s_add_u32 s72, s85, 0\n"
    "buffer_load_dwordx4 v88, s[20:23], s72 offen offset:0 nt lds\n"
    "buffer_load_dwordx4 v[16:19], v162, s[28:31], s57 offen offset:3200\n"
    "buffer_load_dwordx4 v[20:23], v162, s[28:31], s57 offen offset:256\n"
    "s_waitcnt vmcnt(25)\n"
    "v_add_co_u32 v238, vcc, 0x7f4a7c15, v238\n"
    "v_mov_b32 v159, 0x9e3779b9\n"
    "s_nop 0\n"
    "v_addc_co_u32 v239, vcc, v159, v239, vcc\n"
    "v_mov_b32 v246, 1\n"
    "s_waitcnt vmcnt(26)\n"
    "v_lshlrev_b32 v180, 16, v124\n"
    "v_and_b32 v181, 0xffff0000, v124\n"
    "v_lshlrev_b32 v182, 16, v125\n"
    "v_and_b32 v183, 0xffff0000, v125\n"
    "v_lshlrev_b32 v184, 16, v126\n"
    "v_and_b32 v185, 0xffff0000, v126\n"
    "v_lshlrev_b32 v186, 16, v127\n"
    "v_and_b32 v187, 0xffff0000, v127\n"
    "v_mul_f32 v180, s88, v180\n"
    "v_mul_f32 v181, s88, v181\n"
    "v_mul_f32 v182, s88, v182\n"
    "v_mul_f32 v183, s88, v183\n"
    "v_fmaak_f32 v184, v184, v180, 0x80730000\n"
    "v_fmaak_f32 v185, v185, v181, 0x80730000\n"
    "v_fmaak_f32 v186, v186, v182, 0x80730000\n"
    "v_fmaak_f32 v187, v187, v183, 0x80730000\n"
    "s_add_u32 m0, s65, 25600\n"
    "s_add_u32 s72, s85, 1024\n"
    "buffer_load_dwordx4 v89, s[20:23], s72 offen offset:0 nt lds\n"
    "buffer_load_dwordx4 v[24:27], v162, s[28:31], s57 offen offset:3328\n"
    "v_ldexp_f32 v180, v180, s90\n"
    "v_ldexp_f32 v181, v181, s90\n"
    "v_ldexp_f32 v182, v182, s90\n"
    "v_ldexp_f32 v183, v183, s90\n"
    "v_fma_f32 v180, v180, 0, v180\n"
    "v_fma_f32 v181, v181, 0, v181\n"
    "v_fma_f32 v182, v182, 0, v182\n"
    "v_fma_f32 v183, v183, 0, v183\n"
    "ds_write_b128 v131, v[180:183] offset:0\n"
    "ds_write_b128 v131, v[184:187] offset:1024\n"
    "ds_read_b128 v[180:183], v132 offset:0\n"
    "ds_read_b128 v[184:187], v132 offset:1024\n"
    "ds_read_b128 v[188:191], v132 offset:128\n"
    "ds_read_b128 v[192:195], v132 offset:1152\n"
    "ds_read_b128 v[196:199], v132 offset:256\n"
    "ds_read_b128 v[200:203], v132 offset:1280\n"
    "s_nop 0\n"
    "s_nop 0\n"
    "s_nop 0\n"
    "s_nop 0\n"
    "s_nop 0\n"
    "s_nop 0\n"
    "s_nop 0\n"
    "s_add_u32 m0, s65, 26624\n"
    "s_add_u32 s72, s85, 2048\n"
    "buffer_load_dwordx4 v90, s[20:23], s72 offen offset:0 nt lds\n"
    "buffer_load_dwordx4 v[100:103], v162, s[28:31], s66 offen offset:0\n"
    "buffer_load_dwordx4 v[104:107], v162, s[28:31], s66 offen offset:3072\n"
    "s_nop 0\n"
    "s_nop 0\n"
    "s_nop 0\n"
    "s_nop 0\n"
    "s_nop 0\n"
    "s_nop 0\n"
    "s_nop 0\n"
    "s_nop 0\n"
    "s_nop 0\n"
    "s_nop 0\n"
    "s_nop 0\n"
    "s_nop 0\n"
    "s_nop 0\n"
    "s_nop 0\n"
    "s_nop 0\n"
    "s_nop 0\n"
    "s_nop 0\n"
    "s_nop 0\n"
    "s_nop 0\n"
    "s_nop 0\n"
    "s_nop 0\n"
    "s_nop 0\n"
    "s_add_u32 m0, s65, 27648\n"
    "s_add_u32 s72, s85, 3072\n"
    "buffer_load_dwordx4 v88, s[20:23], s72 offen offset:0 nt lds\n"
    "buffer_load_dwordx4 v[108:111], v162, s[28:31], s66 offen offset:128\n"
    "s_nop 0\n"
    "s_nop 0\n"
    "s_nop 0\n"
    "s_nop 0\n"
    "s_nop 0\n"
    "s_nop 0\n"
    "s_nop 0\n"
    "s_nop 0\n"
    "s_nop 0\n"
    "s_nop 0\n"
    "s_nop 0\n"
    "s_nop 0\n"
    "s_nop 0\n"
    "s_nop 0\n"
    "s_nop 0\n"
    "s_nop 0\n"
    "s_nop 0\n"
    "s_nop 0\n"
    "s_nop 0\n"
    "s_nop 0\n"
    "s_nop 0\n"
    "s_nop 0\n"
    "s_nop 0\n"
    "s_waitcnt lgkmcnt(0)\n"
    "s_waitcnt vmcnt(10)\n"
    "ds_add_rtn_u32 v245, v247, v246\n"
    "s_lshl_b32 s73, s53, 6\n"
    "s_add_u32 s73, s73, 128\n"
    "s_waitcnt lgkmcnt(0)\n"
    "v_readfirstlane_b32 s74, v245\n"
    "s_nop 0\n"
    "s_cmp_eq_u32 s74, 3\n"
    "s_cselect_b32 s73, s73, 0x7fff0000\n"
    "buffer_store_dwordx2 v[238:239], v91, s[36:39], s73 offen offset:0 sc1\n"
    "s_waitcnt vmcnt(28)\n"
    "s_add_u32 m0, s65, 28672\n"
    "s_add_u32 s72, s85, 4096\n"
    "buffer_load_dwordx4 v89, s[20:23], s72 offen offset:0 nt lds\n"
    "buffer_load_dwordx4 v[112:115], v162, s[28:31], s66 offen offset:3200\n"
    "buffer_load_dwordx4 v[116:119], v162, s[28:31], s66 offen offset:256\n"
    "s_add_u32 s54, s71, s60\n"
    "s_add_u32 s55, s71, s61\n"
    "s_add_u32 s56, s71, s62\n"
    "v_cvt_pk_u16_u32 v36, v36, v40\n"
    "v_cvt_pk_u16_u32 v37, v37, v41\n"
    "v_cvt_pk_u16_u32 v38, v38, v42\n"
    "v_cvt_pk_u16_u32 v39, v39, v43\n"
    "v_or3_b32 v243, v36, v37, v243\n"
    "v_or3_b32 v243, v38, v39, v243\n"
    "buffer_store_dword v36, v244, s[16:19], s54 offen offset:0 sc1\n"
    "buffer_store_dword v37, v244, s[16:19], s54 offen offset:1024 sc1\n"
    "buffer_store_dword v38, v244, s[16:19], s54 offen offset:2048 sc1\n"
    "buffer_store_dword v39, v244, s[16:19], s54 offen offset:3072 sc1\n"
    "v_cvt_pk_u16_u32 v44, v44, v48\n"
    "v_cvt_pk_u16_u32 v45, v45, v49\n"
    "v_cvt_pk_u16_u32 v46, v46, v50\n"
    "s_add_u32 m0, s65, 29696\n"
    "s_add_u32 s72, s85, 5120\n"
    "buffer_load_dwordx4 v90, s[20:23], s72 offen offset:0 nt lds\n"
    "buffer_load_dwordx4 v[120:123], v162, s[28:31], s66 offen offset:3328\n"
    "v_cvt_pk_u16_u32 v47, v47, v51\n"
    "v_or3_b32 v243, v44, v45, v243\n"
    "v_or3_b32 v243, v46, v47, v243\n"
    "buffer_store_dword v44, v244, s[16:19], s55 offen offset:0 sc1\n"
    "buffer_store_dword v45, v244, s[16:19], s55 offen offset:1024 sc1\n"
    "buffer_store_dword v46, v244, s[16:19], s55 offen offset:2048 sc1\n"
    "buffer_store_dword v47, v244, s[16:19], s55 offen offset:3072 sc1\n"
    "v_cvt_pk_u16_u32 v52, v52, v56\n"
    "v_cvt_pk_u16_u32 v53, v53, v57\n"
    "v_cvt_pk_u16_u32 v54, v54, v58\n"
    "v_cvt_pk_u16_u32 v55, v55, v59\n"
    "v_or3_b32 v243, v52, v53, v243\n"
    "v_or3_b32 v243, v54, v55, v243\n"
    "buffer_store_dword v52, v244, s[16:19], s56 offen offset:0 sc1\n"
    "buffer_store_dword v53, v244, s[16:19], s56 offen offset:1024 sc1\n"
    "buffer_store_dword v54, v244, s[16:19], s56 offen offset:2048 sc1\n"
    "buffer_store_dword v55, v244, s[16:19], s56 offen offset:3072 sc1\n"
    "s_add_u32 m0, s65, 73728\n"
    "s_add_u32 s72, s85, 196608\n"
    "buffer_load_dwordx4 v88, s[20:23], s72 offen offset:0 nt lds\n"
    "s_waitcnt vmcnt(44)\n"
    "v_lshlrev_b32 v204, 16, v136\n"
    "v_and_b32 v205, 0xffff0000, v136\n"
    "v_lshlrev_b32 v206, 16, v137\n"
    "v_and_b32 v207, 0xffff0000, v137\n"
    "v_lshlrev_b32 v208, 16, v138\n"
    "v_and_b32 v209, 0xffff0000, v138\n"
    "v_lshlrev_b32 v210, 16, v139\n"
    "v_and_b32 v211, 0xffff0000, v139\n"
    "v_log_f32 v68, v204\n"
    "v_log_f32 v69, v205\n"
    "v_log_f32 v70, v206\n"
    "v_log_f32 v71, v207\n"
    "v_log_f32 v72, v208\n"
    "v_log_f32 v73, v209\n"
    "v_log_f32 v74, v210\n"
    "v_log_f32 v75, v211\n"
    "v_rcp_f32 v204, v204\n"
    "v_rcp_f32 v205, v205\n"
    "v_rcp_f32 v206, v206\n"
    "s_add_u32 m0, s65, 74752\n"
    "s_add_u32 s72, s85, 197632\n"
    "buffer_load_dwordx4 v89, s[20:23], s72 offen offset:0 nt lds\n"
    "v_rcp_f32 v207, v207\n"
    "v_rcp_f32 v208, v208\n"
    "v_rcp_f32 v209, v209\n"
    "v_rcp_f32 v210, v210\n"
    "v_rcp_f32 v211, v211\n"
    "s_mov_b32 s73, 0x41a00000\n"
    "v_mov_b32 v228, 0x7fc00000\n"
    "v_cmp_le_f32 vcc, |v68|, s73\n"
    "s_nop 1\n"
    "v_cndmask_b32 v68, v228, v68, vcc\n"
    "v_cmp_le_f32 vcc, |v69|, s73\n"
    "s_nop 1\n"
    "v_cndmask_b32 v69, v228, v69, vcc\n"
    "v_cmp_le_f32 vcc, |v70|, s73\n"
    "s_nop 1\n"
    "v_cndmask_b32 v70, v228, v70, vcc\n"
    "v_cmp_le_f32 vcc, |v71|, s73\n"
    "s_nop 1\n"
    "v_cndmask_b32 v71, v228, v71, vcc\n"
    "v_cmp_le_f32 vcc, |v72|, s73\n"
    "s_nop 1\n"
    "v_cndmask_b32 v72, v228, v72, vcc\n"
    "v_cmp_le_f32 vcc, |v73|, s73\n"
    "s_nop 1\n"
    "v_cndmask_b32 v73, v228, v73, vcc\n"
    "v_cmp_le_f32 vcc, |v74|, s73\n"
    "s_nop 1\n"
    "v_cndmask_b32 v74, v228, v74, vcc\n"
    "s_add_u32 m0, s65, 75776\n"
    "s_add_u32 s72, s85, 198656\n"
    "buffer_load_dwordx4 v90, s[20:23], s72 offen offset:0 nt lds\n"
    "v_cmp_le_f32 vcc, |v75|, s73\n"
    "s_nop 1\n"
    "v_cndmask_b32 v75, v228, v75, vcc\n"
    "ds_write_b128 v86, v[68:71] offset:0\n"
    "ds_write_b128 v86, v[72:75] offset:16\n"
    "v_cvt_pk_bf16_f32 v220, v204, v205\n"
    "v_cvt_pk_bf16_f32 v221, v206, v207\n"
    "v_cvt_pk_bf16_f32 v222, v208, v209\n"
    "v_cvt_pk_bf16_f32 v223, v210, v211\n"
    "v_lshlrev_b32 v212, 16, v220\n"
    "v_and_b32 v213, 0xffff0000, v220\n"
    "v_lshlrev_b32 v214, 16, v221\n"
    "v_and_b32 v215, 0xffff0000, v221\n"
    "v_lshlrev_b32 v216, 16, v222\n"
    "v_and_b32 v217, 0xffff0000, v222\n"
    "v_lshlrev_b32 v218, 16, v223\n"
    "v_and_b32 v219, 0xffff0000, v223\n"
    "v_sub_f32 v204, v204, v212\n"
    "v_sub_f32 v205, v205, v213\n"
    "v_sub_f32 v206, v206, v214\n"
    "v_sub_f32 v207, v207, v215\n"
    "v_sub_f32 v208, v208, v216\n"
    "s_add_u32 m0, s65, 76800\n"
    "s_add_u32 s72, s85, 199680\n"
    "buffer_load_dwordx4 v88, s[20:23], s72 offen offset:0 nt lds\n"
    "v_sub_f32 v209, v209, v217\n"
    "v_sub_f32 v210, v210, v218\n"
    "v_sub_f32 v211, v211, v219\n"
    "v_cvt_pk_bf16_f32 v224, v204, v205\n"
    "v_cvt_pk_bf16_f32 v225, v206, v207\n"
    "v_cvt_pk_bf16_f32 v226, v208, v209\n"
    "v_cvt_pk_bf16_f32 v227, v210, v211\n"
    "v_mov_b32 v204, 0\n"
    "v_mov_b32 v205, 0\n"
    "v_mov_b32 v206, 0\n"
    "v_mov_b32 v207, 0\n"
    "v_swap_b32 v221, v222\n"
    "v_swap_b32 v225, v226\n"
    "v_swap_b32 v141, v142\n"
    "ds_write2_b64 v144, v[220:221], v[222:223] offset0:32 offset1:40\n"
    "ds_write2_b64 v144, v[224:225], v[226:227] offset0:48 offset1:56\n"
    "ds_write2_b64 v144, v[140:141], v[142:143] offset0:64 offset1:72\n"
    "ds_write2_b64 v144, v[204:205], v[206:207] offset0:80 offset1:88\n"
    "s_waitcnt vmcnt(27)\n"
    "v_fma_f32 v4, v4, v180, -v184\n"
    "v_fma_f32 v5, v5, v181, -v185\n"
    "s_add_u32 m0, s65, 77824\n"
    "s_add_u32 s72, s85, 200704\n"
    "buffer_load_dwordx4 v89, s[20:23], s72 offen offset:0 nt lds\n"
    "v_fma_f32 v6, v6, v182, -v186\n"
    "v_fma_f32 v7, v7, v183, -v187\n"
    "v_fma_f32 v8, v8, v180, -v184\n"
    "v_fma_f32 v9, v9, v181, -v185\n"
    "v_fma_f32 v10, v10, v182, -v186\n"
    "v_fma_f32 v11, v11, v183, -v187\n"
    "v_cvt_pk_bf16_f32 v4, v4, v5\n"
    "v_cvt_pk_bf16_f32 v5, v6, v7\n"
    "v_cvt_pk_bf16_f32 v6, v8, v9\n"
    "v_cvt_pk_bf16_f32 v7, v10, v11\n"
    "ds_write_b64 v163, v[4:5] offset:0\n"
    "ds_write_b64 v163, v[6:7] offset:1024\n"
    "v_fma_f32 v12, v12, v188, -v192\n"
    "v_fma_f32 v13, v13, v189, -v193\n"
    "v_fma_f32 v14, v14, v190, -v194\n"
    "v_fma_f32 v15, v15, v191, -v195\n"
    "v_fma_f32 v16, v16, v188, -v192\n"
    "v_fma_f32 v17, v17, v189, -v193\n"
    "v_fma_f32 v18, v18, v190, -v194\n"
    "v_fma_f32 v19, v19, v191, -v195\n"
    "s_add_u32 m0, s65, 78848\n"
    "s_add_u32 s72, s85, 201728\n"
    "buffer_load_dwordx4 v90, s[20:23], s72 offen offset:0 nt lds\n"
    "v_cvt_pk_bf16_f32 v12, v12, v13\n"
    "v_cvt_pk_bf16_f32 v13, v14, v15\n"
    "v_cvt_pk_bf16_f32 v14, v16, v17\n"
    "v_cvt_pk_bf16_f32 v15, v18, v19\n"
    "ds_write_b64 v163, v[12:13] offset:2048\n"
    "ds_write_b64 v163, v[14:15] offset:3072\n"
    "v_fma_f32 v20, v20, v196, -v200\n"
    "v_fma_f32 v21, v21, v197, -v201\n"
    "v_fma_f32 v22, v22, v198, -v202\n"
    "v_fma_f32 v23, v23, v199, -v203\n"
    "v_fma_f32 v24, v24, v196, -v200\n"
    "v_fma_f32 v25, v25, v197, -v201\n"
    "v_fma_f32 v26, v26, v198, -v202\n"
    "v_fma_f32 v27, v27, v199, -v203\n"
    "v_cvt_pk_bf16_f32 v20, v20, v21\n"
    "v_cvt_pk_bf16_f32 v21, v22, v23\n"
    "v_cvt_pk_bf16_f32 v22, v24, v25\n"
    "v_cvt_pk_bf16_f32 v23, v26, v27\n"
    "ds_write_b64 v163, v[20:21] offset:4096\n"
    "ds_write_b64 v163, v[22:23] offset:5120\n"
    "s_waitcnt vmcnt(14)\n"
    "s_add_u32 m0, s65, 49152\n"
    "s_add_u32 s72, s85, 393216\n"
    "buffer_load_dwordx4 v88, s[20:23], s72 offen offset:0 nt lds\n"
    "v_fma_f32 v100, v100, v180, -v184\n"
    "v_fma_f32 v101, v101, v181, -v185\n"
    "v_fma_f32 v102, v102, v182, -v186\n"
    "v_fma_f32 v103, v103, v183, -v187\n"
    "v_fma_f32 v104, v104, v180, -v184\n"
    "v_fma_f32 v105, v105, v181, -v185\n"
    "v_fma_f32 v106, v106, v182, -v186\n"
    "v_fma_f32 v107, v107, v183, -v187\n"
    "v_cvt_pk_bf16_f32 v100, v100, v101\n"
    "v_cvt_pk_bf16_f32 v101, v102, v103\n"
    "v_cvt_pk_bf16_f32 v102, v104, v105\n"
    "v_cvt_pk_bf16_f32 v103, v106, v107\n"
    "ds_write_b64 v163, v[100:101] offset:12288\n"
    "ds_write_b64 v163, v[102:103] offset:13312\n"
    "v_fma_f32 v108, v108, v188, -v192\n"
    "v_fma_f32 v109, v109, v189, -v193\n"
    "v_fma_f32 v110, v110, v190, -v194\n"
    "v_fma_f32 v111, v111, v191, -v195\n"
    "v_fma_f32 v112, v112, v188, -v192\n"
    "v_fma_f32 v113, v113, v189, -v193\n"
    "v_fma_f32 v114, v114, v190, -v194\n"
    "s_add_u32 m0, s65, 50176\n"
    "s_add_u32 s72, s85, 394240\n"
    "buffer_load_dwordx4 v89, s[20:23], s72 offen offset:0 nt lds\n"
    "v_fma_f32 v115, v115, v191, -v195\n"
    "v_cvt_pk_bf16_f32 v108, v108, v109\n"
    "v_cvt_pk_bf16_f32 v109, v110, v111\n"
    "v_cvt_pk_bf16_f32 v110, v112, v113\n"
    "v_cvt_pk_bf16_f32 v111, v114, v115\n"
    "ds_write_b64 v163, v[108:109] offset:14336\n"
    "ds_write_b64 v163, v[110:111] offset:15360\n"
    "v_fma_f32 v116, v116, v196, -v200\n"
    "v_fma_f32 v117, v117, v197, -v201\n"
    "v_fma_f32 v118, v118, v198, -v202\n"
    "v_fma_f32 v119, v119, v199, -v203\n"
    "v_fma_f32 v120, v120, v196, -v200\n"
    "v_fma_f32 v121, v121, v197, -v201\n"
    "v_fma_f32 v122, v122, v198, -v202\n"
    "v_fma_f32 v123, v123, v199, -v203\n"
    "v_cvt_pk_bf16_f32 v116, v116, v117\n"
    "v_cvt_pk_bf16_f32 v117, v118, v119\n"
    "v_cvt_pk_bf16_f32 v118, v120, v121\n"
    "v_cvt_pk_bf16_f32 v119, v122, v123\n"
    "ds_write_b64 v163, v[116:117] offset:16384\n"
    "ds_write_b64 v163, v[118:119] offset:17408\n"
    "s_waitcnt vmcnt(17)\n"
    "ds_read_b128 v[36:39], v154 offset:0\n"
    "ds_read_b128 v[40:43], v155 offset:0\n"
    "ds_read_b128 v[44:47], v154 offset:128\n"
    "ds_read_b128 v[48:51], v155 offset:128\n"
    "ds_read_b128 v[52:55], v154 offset:256\n"
    "ds_read_b128 v[56:59], v155 offset:256\n"
    "s_mov_b64 exec, 0\n"
    "s_bitcmp1_b32 s83, 1\n"
    "s_cbranch_scc1 .LwV\n"
    "s_cmp_eq_u32 0, 0\n"
    "s_branch .LwK\n"
    ".Lw2:\n"
    "s_bitcmp1_b32 s83, 1\n"
    "s_cbranch_scc1 .Lwp1\n"
    "s_branch .Lwp0\n"
    ".Lw3:\n"
    "s_mov_b64 exec, -1\n"
    "s_waitcnt lgkmcnt(5)\n"
    "v_pk_maximum3_f16 v98, v36, v37, v98\n"
    "v_pk_maximum3_f16 v98, v38, v39, v98\n"
    "v_pk_add_u16 v36, s89, v36 clamp\n"
    "v_pk_add_u16 v37, s89, v37 clamp\n"
    "v_pk_add_u16 v38, s89, v38 clamp\n"
    "v_pk_add_u16 v39, s89, v39 clamp\n"
    "s_waitcnt lgkmcnt(4)\n"
    "v_pk_maximum3_f16 v98, v40, v41, v98\n"
    "v_pk_maximum3_f16 v98, v42, v43, v98\n"
    "v_pk_add_u16 v40, s89, v40 clamp\n"
    "v_pk_add_u16 v41, s89, v41 clamp\n"
    "v_pk_add_u16 v42, s89, v42 clamp\n"
    "v_pk_add_u16 v43, s89, v43 clamp\n"
    "s_waitcnt lgkmcnt(3)\n"
    "v_pk_maximum3_f16 v98, v44, v45, v98\n"
    "v_pk_maximum3_f16 v98, v46, v47, v98\n"
    "v_pk_add_u16 v44, s89, v44 clamp\n"
    "v_pk_add_u16 v45, s89, v45 clamp\n"
    "v_pk_add_u16 v46, s89, v46 clamp\n"
    "v_pk_add_u16 v47, s89, v47 clamp\n"
    "s_waitcnt lgkmcnt(2)\n"
    "v_pk_maximum3_f16 v98, v48, v49, v98\n"
    "v_pk_maximum3_f16 v98, v50, v51, v98\n"
    "v_pk_add_u16 v48, s89, v48 clamp\n"
    "v_pk_add_u16 v49, s89, v49 clamp\n"
    "v_pk_add_u16 v50, s89, v50 clamp\n"
    "v_pk_add_u16 v51, s89, v51 clamp\n"
    "s_waitcnt lgkmcnt(1)\n"
    "v_pk_maximum3_f16 v98, v52, v53, v98\n"
    "v_pk_maximum3_f16 v98, v54, v55, v98\n"
    "v_pk_add_u16 v52, s89, v52 clamp\n"
    "v_pk_add_u16 v53, s89, v53 clamp\n"
    "v_pk_add_u16 v54, s89, v54 clamp\n"
    "v_pk_add_u16 v55, s89, v55 clamp\n"
    "s_waitcnt lgkmcnt(0)\n"
    "v_pk_maximum3_f16 v98, v56, v57, v98\n"
    "v_pk_maximum3_f16 v98, v58, v59, v98\n"
    "v_pk_add_u16 v56, s89, v56 clamp\n"
    "v_pk_add_u16 v57, s89, v57 clamp\n"
    "v_pk_add_u16 v58, s89, v58 clamp\n"
    "v_pk_add_u16 v59, s89, v59 clamp\n"
    "v_mov_b32 v108, 0\n"
    "v_mov_b32 v109, 0\n"
    "v_mov_b32 v110, 0\n"
    "v_mov_b32 v111, 0\n"
    "v_mov_b32 v112, 0\n"
    "v_mov_b32 v113, 0\n"
    "v_mov_b32 v114, 0\n"
    "v_mov_b32 v115, 0\n"
    "v_mov_b32 v116, 0\n"
    "v_mov_b32 v117, 0\n"
    "v_mov_b32 v118, 0\n"
    "v_mov_b32 v119, 0\n"
    "v_mov_b32 v120, 0\n"
    "v_mov_b32 v121, 0\n"
    "v_mov_b32 v122, 0\n"
    "v_mov_b32 v123, 0\n"
    "s_mul_i32 s73, s79, 768\n"
    "v_add_u32 v78, s73, v250\n"
    "v_mov_b32 v79, 155936\n"
    "s_lshl2_add_u32 s74, s84, 155936\n"
    "v_mov_b32 v77, 155952\n"
    "v_writelane_b32 v77, s74, 63\n"
    "s_waitcnt lgkmcnt(0)\n"
    "s_barrier\n"
    "ds_read_b128 a[112:115], v252 offset:0\n"
    "ds_read_b128 a[116:119], v252 offset:2048\n"
    "ds_read_b128 a[120:123], v252 offset:4096\n"
    "ds_read_b128 a[124:127], v252 offset:6144\n"
    "ds_read_b128 a[128:131], v252 offset:8192\n"
    "ds_read_b128 a[132:135], v252 offset:10240\n"
    "ds_read_b128 a[136:139], v252 offset:1024\n"
    "ds_read_b128 a[140:143], v252 offset:3072\n"
    "ds_read_b128 a[144:147], v252 offset:5120\n"
    "ds_read_b128 a[148:151], v252 offset:7168\n"
    "ds_read_b128 a[152:155], v252 offset:9216\n"
    "ds_read_b128 a[156:159], v252 offset:11264\n"
    "s_add_u32 m0, s65, 51200\n"
    "s_add_u32 s72, s85, 395264\n"
    "buffer_load_dwordx4 v90, s[20:23], s72 offen offset:0 nt lds\n"
    "s_add_u32 m0, s65, 52224\n"
    "s_add_u32 s72, s85, 396288\n"
    "buffer_load_dwordx4 v88, s[20:23], s72 offen offset:0 nt lds\n"
    "s_waitcnt lgkmcnt(11)\n"
    "v_mfma_f32_16x16x32_bf16 v[4:7], a[112:115], v[36:39], 0\n"
    "s_waitcnt lgkmcnt(10)\n"
    "v_mfma_f32_16x16x32_bf16 v[4:7], a[116:119], v[40:43], v[4:7]\n"
    "s_waitcnt lgkmcnt(9)\n"
    "v_mfma_f32_16x16x32_bf16 v[4:7], a[120:123], v[44:47], v[4:7]\n"
    "s_waitcnt lgkmcnt(8)\n"
    "v_mfma_f32_16x16x32_bf16 v[4:7], a[124:127], v[48:51], v[4:7]\n"
    "s_waitcnt lgkmcnt(7)\n"
    "v_mfma_f32_16x16x32_bf16 v[4:7], a[128:131], v[52:55], v[4:7]\n"
    "s_waitcnt lgkmcnt(6)\n"
    "v_mfma_f32_16x16x32_bf16 v[4:7], a[132:135], v[56:59], v[4:7]\n"
    "s_waitcnt lgkmcnt(5)\n"
    "v_mfma_f32_16x16x32_bf16 v[20:23], a[136:139], v[36:39], 0\n"
    "v_accvgpr_write_b32 a192, v36\n"
    "v_accvgpr_write_b32 a193, v37\n"
    "v_accvgpr_write_b32 a194, v38\n"
    "v_accvgpr_write_b32 a195, v39\n"
    "s_waitcnt lgkmcnt(4)\n"
    "v_mfma_f32_16x16x32_bf16 v[20:23], a[140:143], v[40:43], v[20:23]\n"
    "v_accvgpr_write_b32 a196, v40\n"
    "v_accvgpr_write_b32 a197, v41\n"
    "v_accvgpr_write_b32 a198, v42\n"
    "v_accvgpr_write_b32 a199, v43\n"
    "s_waitcnt lgkmcnt(3)\n"
    "v_mfma_f32_16x16x32_bf16 v[20:23], a[144:147], v[44:47], v[20:23]\n"
    "v_accvgpr_write_b32 a200, v44\n"
    "v_accvgpr_write_b32 a201, v45\n"
    "v_accvgpr_write_b32 a202, v46\n"
    "v_accvgpr_write_b32 a203, v47\n"
    "s_waitcnt lgkmcnt(2)\n"
    "v_mfma_f32_16x16x32_bf16 v[20:23], a[148:151], v[48:51], v[20:23]\n"
    "v_accvgpr_write_b32 a204, v48\n"
    "v_accvgpr_write_b32 a205, v49\n"
    "v_accvgpr_write_b32 a206, v50\n"
    "v_accvgpr_write_b32 a207, v51\n"
    "s_waitcnt lgkmcnt(1)\n"
    "v_mfma_f32_16x16x32_bf16 v[20:23], a[152:155], v[52:55], v[20:23]\n"
    "v_accvgpr_write_b32 a208, v52\n"
    "v_accvgpr_write_b32 a209, v53\n"
    "v_accvgpr_write_b32 a210, v54\n"
    "v_accvgpr_write_b32 a211, v55\n"
    "s_waitcnt lgkmcnt(0)\n"
    "v_mfma_f32_16x16x32_bf16 v[20:23], a[156:159], v[56:59], v[20:23]\n"
    "v_accvgpr_write_b32 a212, v56\n"
    "v_accvgpr_write_b32 a213, v57\n"
    "v_accvgpr_write_b32 a214, v58\n"
    "v_accvgpr_write_b32 a215, v59\n"
    "s_add_u32 m0, s65, 53248\n"
    "s_add_u32 s72, s85, 397312\n"
    "buffer_load_dwordx4 v89, s[20:23], s72 offen offset:0 nt lds\n"
    "buffer_load_dwordx2 v[236:237], v87, s[36:39], 0 offen offset:0 sc1\n"
    "ds_read_b128 v[16:19], v78 offset:0\n"
    "ds_read_b128 v[32:35], v78 offset:16\n"
    "ds_read_b128 v[48:51], v78 offset:128\n"
    "ds_read_b128 v[64:67], v78 offset:144\n"
    "ds_read_b128 v[100:103], v78 offset:768\n"
    "ds_read_b128 v[104:107], v78 offset:784\n"
    "v_max3_f32 v76, v4, v5, v6\n"
    "ds_read_b128 a[0:3], v252 offset:12288\n"
    "v_max3_f32 v76, v76, v7, v20\n"
    "ds_read_b128 a[4:7], v252 offset:14336\n"
    "v_max3_f32 v76, v76, v21, v22\n"
    "ds_read_b128 a[8:11], v252 offset:16384\n"
    "v_max_f32 v76, v76, v23\n"
    "ds_read_b128 a[12:15], v252 offset:18432\n"
    "s_nop 0\n"
    "v_max_f32_dpp v76, v76, v76 row_shr:1\n"
    "ds_read_b128 a[16:19], v252 offset:20480\n"
    "ds_read_b128 a[20:23], v252 offset:22528\n"
    "v_max_f32_dpp v76, v76, v76 row_shr:2\n"
    "ds_read_b128 a[24:27], v252 offset:13312\n"
    "s_nop 0\n"
    "v_max_f32_dpp v76, v76, v76 row_shr:4\n"
    "ds_read_b128 a[28:31], v252 offset:15360\n"
    "s_nop 0\n"
    "v_max_f32_dpp v76, v76, v76 row_shr:8\n"
    "ds_read_b128 a[32:35], v252 offset:17408\n"
    "s_nop 0\n"
    "v_max_f32_dpp v76, v76, v76 row_bcast:15 row_mask:0xa\n"
    "ds_read_b128 a[36:39], v252 offset:19456\n"
    "s_nop 0\n"
    "v_max_f32_dpp v76, v76, v76 row_bcast:31 row_mask:0xc\n"
    "ds_read_b128 a[40:43], v252 offset:21504\n"
    "ds_read_b128 a[44:47], v252 offset:23552\n"
    "ds_write_b32 v77, v76\n"
    "s_add_u32 m0, s65, 54272\n"
    "s_add_u32 s72, s85, 398336\n"
    "buffer_load_dwordx4 v90, s[20:23], s72 offen offset:0 nt lds\n"
    "s_add_u32 m0, s65, 98304\n"
    "s_add_u32 s72, s85, 589824\n"
    "buffer_load_dwordx4 v88, s[20:23], s72 offen offset:0 nt lds\n"
    "s_waitcnt lgkmcnt(0)\n"
    "s_barrier\n"
    "ds_read_b128 v[80:83], v79 offset:0\n"
    "s_waitcnt vmcnt(14)\n"
    "buffer_store_dwordx2 v[238:239], v240, s[36:39], s80 offen offset:0 sc1\n"
    "s_lshl_b32 s73, s79, 8\n"
    "s_add_u32 s80, s80, s73\n"
    "s_mul_i32 s73, s79, 98304\n"
    "s_add_u32 s71, s71, s73\n"
    "s_add_u32 m0, s65, 99328\n"
    "s_add_u32 s72, s85, 590848\n"
    "buffer_load_dwordx4 v89, s[20:23], s72 offen offset:0 nt lds\n"
    "s_waitcnt lgkmcnt(0)\n"
    "v_max3_f32 v76, v80, v81, v82\n"
    "v_max_f32 v76, v76, v83\n"
    "v_add_f32 v76, 0xc2b00000, v76\n"
    "s_add_u32 m0, s65, 100352\n"
    "s_add_u32 s72, s85, 591872\n"
    "buffer_load_dwordx4 v90, s[20:23], s72 offen offset:0 nt lds\n"
    "s_add_u32 m0, s65, 101376\n"
    "s_add_u32 s72, s85, 592896\n"
    "buffer_load_dwordx4 v88, s[20:23], s72 offen offset:0 nt lds\n"
    "s_add_u32 m0, s65, 102400\n"
    "s_add_u32 s72, s85, 593920\n"
    "buffer_load_dwordx4 v89, s[20:23], s72 offen offset:0 nt lds\n"
    "s_add_u32 m0, s65, 103424\n"
    "s_add_u32 s72, s85, 594944\n"
    "buffer_load_dwordx4 v90, s[20:23], s72 offen offset:0 nt lds\n"
    "v_sub_f32 v48, v48, v76\n"
    "v_sub_f32 v51, v51, v76\n"
    "v_sub_f32 v49, v49, v76\n"
    "v_sub_f32 v50, v50, v76\n"
    "v_sub_f32 v66, v66, v76\n"
    "v_sub_f32 v65, v65, v76\n"
    "v_mfma_f32_16x16x32_bf16 v[36:39], a[0:3], a[192:195], v[48:51]\n"
    "s_waitcnt vmcnt(14)\n"
    "ds_read_b128 v[200:203], v97 offset:24832\n"
    "v_sub_f32 v67, v67, v76\n"
    "v_mfma_f32_16x16x32_bf16 v[36:39], a[4:7], a[196:199], v[36:39]\n"
    "v_sub_f32 v33, v33, v76\n"
    "v_sub_f32 v32, v32, v76\n"
    "v_mfma_f32_16x16x32_bf16 v[36:39], a[8:11], a[200:203], v[36:39]\n"
    "ds_read_b128 v[184:187], v97 offset:24576\n"
    "ds_read_b128 v[196:199], v96 offset:24832\n"
    "ds_read_b128 v[192:195], v97 offset:24704\n"
    "v_mfma_f32_16x16x32_bf16 v[36:39], a[12:15], a[204:207], v[36:39]\n"
    "v_sub_f32 v64, v64, v76\n"
    "v_sub_f32 v35, v35, v76\n"
    "v_sub_f32 v19, v19, v76\n"
    "v_mfma_f32_16x16x32_bf16 v[52:55], a[24:27], a[192:195], v[64:67]\n"
    "v_add_f32 v21, v21, v33\n"
    "v_sub_f32 v16, v16, v76\n"
    "v_mfma_f32_16x16x32_bf16 v[36:39], a[16:19], a[208:211], v[36:39]\n"
    "v_sub_f32 v18, v18, v76\n"
    "v_add_f32 v20, v20, v32\n"
    "v_sub_f32 v17, v17, v76\n"
    "v_mfma_f32_16x16x32_bf16 v[52:55], a[28:31], a[196:199], v[52:55]\n"
    "ds_read_b128 v[188:191], v96 offset:24704\n"
    "ds_read_b128 v[180:183], v96 offset:24576\n"
    "v_add_f32 v6, v6, v18\n"
    "v_sub_f32 v34, v34, v76\n"
    "v_mfma_f32_16x16x32_bf16 v[36:39], a[20:23], a[212:215], v[36:39]\n"
    "s_waitcnt lgkmcnt(5)\n"
    "v_pk_maximum3_f16 v98, v200, v201, v98\n"
    "v_exp_f32 v20, v20\n"
    "v_pk_maximum3_f16 v98, v202, v203, v98\n"
    "v_pk_add_u16 v203, s89, v203 clamp\n"
    "v_pk_add_u16 v200, s89, v200 clamp\n"
    "v_mfma_f32_16x16x32_bf16 v[52:55], a[32:35], a[200:203], v[52:55]\n"
    "s_waitcnt lgkmcnt(3)\n"
    "v_pk_maximum3_f16 v98, v196, v197, v98\n"
    "v_pk_maximum3_f16 v98, v198, v199, v98\n"
    "v_mfma_f32_16x16x32_bf16 v[52:55], a[36:39], a[204:207], v[52:55]\n"
    "v_exp_f32 v21, v21\n"
    "v_pk_add_u16 v202, s89, v202 clamp\n"
    "v_pk_add_u16 v201, s89, v201 clamp\n"
    "v_mfma_f32_16x16x32_bf16 v[52:55], a[40:43], a[208:211], v[52:55]\n"
    "v_add_f32 v22, v22, v34\n"
    "v_add_f32 v7, v7, v19\n"
    "v_mfma_f32_16x16x32_bf16 v[24:27], a[156:159], v[200:203], v[32:35]\n"
    "s_waitcnt lgkmcnt(2)\n"
    "v_pk_maximum3_f16 v98, v192, v193, v98\n"
    "v_pk_maximum3_f16 v98, v194, v195, v98\n"
    "v_pk_add_u16 v193, s89, v193 clamp\n"
    "v_pk_add_u16 v198, s89, v198 clamp\n"
    "v_add_f32 v5, v5, v17\n"
    "v_mfma_f32_16x16x32_bf16 v[52:55], a[44:47], a[212:215], v[52:55]\n"
    "s_waitcnt lgkmcnt(1)\n"
    "v_pk_add_u16 v194, s89, v194 clamp\n"
    "v_sub_f32 v69, v69, v76\n"
    "v_sub_f32 v73, v73, v76\n"
    "v_pk_maximum3_f16 v98, v188, v189, v98\n"
    "v_pk_maximum3_f16 v98, v190, v191, v98\n"
    "v_pk_add_u16 v196, s89, v196 clamp\n"
    "v_pk_maximum3_f16 v98, v184, v185, v98\n"
    "v_pk_add_u16 v197, s89, v197 clamp\n"
    "v_pk_maximum3_f16 v98, v186, v187, v98\n"
    "v_mfma_f32_16x16x32_bf16 v[8:11], a[132:135], v[200:203], v[16:19]\n"
    "v_sub_f32 v75, v75, v76\n"
    "v_pk_add_u16 v199, s89, v199 clamp\n"
    "v_sub_f32 v101, v101, v76\n"
    "v_exp_f32 v54, v54\n"
    "v_mfma_f32_16x16x32_bf16 v[24:27], a[152:155], v[196:199], v[24:27]\n"
    "s_waitcnt lgkmcnt(0)\n"
    "v_pk_add_u16 v188, s89, v188 clamp\n"
    "v_pk_add_u16 v184, s89, v184 clamp\n"
    "v_sub_f32 v70, v70, v76\n"
    "v_pk_maximum3_f16 v98, v180, v181, v98\n"
    "v_pk_add_u16 v186, s89, v186 clamp\n"
    "v_mfma_f32_16x16x32_bf16 v[56:59], a[44:47], v[200:203], v[64:67]\n"
    "v_pk_add_u16 v189, s89, v189 clamp\n"
    "v_pk_add_u16 v195, s89, v195 clamp\n"
    "v_pk_add_u16 v192, s89, v192 clamp\n"
    "v_pk_add_u16 v185, s89, v185 clamp\n"
    "v_pk_maximum3_f16 v98, v182, v183, v98\n"
    "v_exp_f32 v55, v55\n"
    "v_sub_f32 v68, v68, v76\n"
    "v_mfma_f32_16x16x32_bf16 v[56:59], a[40:43], v[196:199], v[56:59]\n"
    "v_exp_f32 v52, v52\n"
    "v_pk_add_u16 v190, s89, v190 clamp\n"
    "v_mfma_f32_16x16x32_bf16 v[8:11], a[128:131], v[196:199], v[8:11]\n"
    "v_accvgpr_write_b32 a231, v195\n"
    "v_sub_f32 v102, v102, v76\n"
    "v_sub_f32 v71, v71, v76\n"
    "v_sub_f32 v107, v107, v76\n"
    "v_mfma_f32_16x16x32_bf16 v[40:43], a[20:23], v[200:203], v[48:51]\n"
    "v_sub_f32 v74, v74, v76\n"
    "v_add_f32 v23, v23, v35\n"
    "v_mfma_f32_16x16x32_bf16 v[40:43], a[16:19], v[196:199], v[40:43]\n"
    "v_pk_add_u16 v183, s89, v183 clamp\n"
    "v_accvgpr_write_b32 a228, v192\n"
    "v_mfma_f32_16x16x32_bf16 v[56:59], a[36:39], v[192:195], v[56:59]\n"
    "v_add_f32 v4, v4, v16\n"
    "v_exp_f32 v6, v6\n"
    "v_mfma_f32_16x16x32_bf16 v[24:27], a[148:151], v[192:195], v[24:27]\n"
    "v_sub_f32 v72, v72, v76\n"
    "v_pk_add_u16 v187, s89, v187 clamp\n"
    "v_pk_add_u16 v191, s89, v191 clamp\n"
    "v_mfma_f32_16x16x32_bf16 v[8:11], a[124:127], v[192:195], v[8:11]\n"
    "v_exp_f32 v53, v53\n"
    "v_accvgpr_write_b32 a226, v190\n"
    "v_mfma_f32_16x16x32_bf16 v[8:11], a[120:123], v[188:191], v[8:11]\n"
    "v_accvgpr_write_b32 a221, v185\n"
    "v_exp_f32 v39, v39\n"
    "v_exp_f32 v22, v22\n"
    "v_pk_add_u16 v181, s89, v181 clamp\n"
    "v_mfma_f32_16x16x32_bf16 v[56:59], a[32:35], v[188:191], v[56:59]\n"
    "v_exp_f32 v36, v36\n"
    "v_pk_add_u16 v182, s89, v182 clamp\n"
    "v_exp_f32 v23, v23\n"
    "v_mfma_f32_16x16x32_bf16 v[24:27], a[144:147], v[188:191], v[24:27]\n"
    "v_accvgpr_write_b32 a237, v201\n"
    "v_pk_add_u16 v180, s89, v180 clamp\n"
    "v_mfma_f32_16x16x32_bf16 v[40:43], a[12:15], v[192:195], v[40:43]\n"
    "v_accvgpr_write_b32 a239, v203\n"
    "v_accvgpr_write_b32 a223, v187\n"
    "v_exp_f32 v7, v7\n"
    "v_accvgpr_write_b32 a233, v197\n"
    "v_mfma_f32_16x16x32_bf16 v[8:11], a[116:119], v[184:187], v[8:11]\n"
    "v_accvgpr_write_b32 a232, v196\n"
    "v_accvgpr_write_b32 a235, v199\n"
    "v_mfma_f32_16x16x32_bf16 v[40:43], a[8:11], v[188:191], v[40:43]\n"
    "ds_write_b128 v86, v[68:71] offset:0\n"
    "v_mfma_f32_16x16x32_bf16 v[40:43], a[4:7], v[184:187], v[40:43]\n"
    "v_sub_f32 v106, v106, v76\n"
    "v_accvgpr_write_b32 a234, v198\n"
    "v_mfma_f32_16x16x32_bf16 v[8:11], a[112:115], v[180:183], v[8:11]\n"
    "v_cvt_pk_bf16_f32 v70, v20, v21\n"
    "v_exp_f32 v37, v37\n"
    "v_cvt_pk_bf16_f32 v69, v6, v7\n"
    "v_accvgpr_write_b32 a218, v182\n"
    "v_mfma_f32_16x16x32_bf16 v[40:43], a[0:3], v[180:183], v[40:43]\n"
    "ds_write_b128 v86, v[72:75] offset:16\n"
    "v_accvgpr_write_b32 a230, v194\n"
    "v_mfma_f32_16x16x32_bf16 v[24:27], a[140:143], v[184:187], v[24:27]\n"
    "v_exp_f32 v10, v10\n"
    "v_mfma_f32_16x16x32_bf16 v[56:59], a[28:31], v[184:187], v[56:59]\n"
    "v_cvt_pk_bf16_f32 v72, v36, v37\n"
    "v_sub_f32 v103, v103, v76\n"
    "v_mfma_f32_16x16x32_bf16 v[56:59], a[24:27], v[180:183], v[56:59]\n"
    "v_sub_f32 v104, v104, v76\n"
    "v_accvgpr_write_b32 a217, v181\n"
    "v_exp_f32 v4, v4\n"
    "v_sub_f32 v100, v100, v76\n"
    "v_accvgpr_write_b32 a227, v191\n"
    "v_exp_f32 v11, v11\n"
    "v_exp_f32 v5, v5\n"
    "v_exp_f32 v38, v38\n"
    "v_exp_f32 v41, v41\n"
    "v_exp_f32 v9, v9\n"
    "v_exp_f32 v59, v59\n"
    "v_accvgpr_write_b32 a220, v184\n"
    "v_accvgpr_write_b32 a238, v202\n"
    "v_accvgpr_write_b32 a236, v200\n"
    "v_exp_f32 v43, v43\n"
    "v_cvt_pk_bf16_f32 v77, v10, v11\n"
    "v_exp_f32 v8, v8\n"
    "v_cvt_pk_bf16_f32 v75, v54, v55\n"
    "v_mfma_f32_16x16x32_bf16 v[24:27], a[136:139], v[180:183], v[24:27]\n"
    "v_cvt_pk_bf16_f32 v71, v22, v23\n"
    "v_sub_f32 v105, v105, v76\n"
    "v_accvgpr_write_b32 a216, v180\n"
    "v_exp_f32 v58, v58\n"
    "v_cvt_pk_bf16_f32 v73, v38, v39\n"
    "v_exp_f32 v40, v40\n"
    "v_accvgpr_write_b32 a222, v186\n"
    "v_accvgpr_write_b32 a225, v189\n"
    "v_exp_f32 v24, v24\n"
    "v_exp_f32 v57, v57\n"
    "v_cvt_pk_bf16_f32 v68, v4, v5\n"
    "v_cvt_pk_bf16_f32 v80, v40, v41\n"
    "v_exp_f32 v27, v27\n"
    "v_exp_f32 v25, v25\n"
    "v_cvt_pk_bf16_f32 v76, v8, v9\n"
    "v_accvgpr_write_b32 a224, v188\n"
    "v_accvgpr_write_b32 a219, v183\n"
    "v_exp_f32 v26, v26\n"
    "v_exp_f32 v42, v42\n"
    "v_cvt_pk_bf16_f32 v78, v24, v25\n"
    "v_exp_f32 v56, v56\n"
    "v_cvt_pk_bf16_f32 v74, v52, v53\n"
    "v_accvgpr_write_b32 a229, v193\n"
    "v_cvt_pk_bf16_f32 v79, v26, v27\n"
    "v_cvt_pk_bf16_f32 v82, v56, v57\n"
    "v_cvt_pk_bf16_f32 v83, v58, v59\n"
    "v_cvt_pk_bf16_f32 v81, v42, v43\n"
    "s_waitcnt vmcnt(8)\n"
    "v_cmp_eq_u64 s[54:55], v[236:237], v[238:239]\n"
    "s_nop 0\n"
    "s_cmp_eq_u64 s[54:55], -1\n"
    "s_cbranch_scc0 .LpollAV\n"
    ".LpollAVr:\n"
    "s_add_u32 m0, s65, 24576\n"
    "s_add_u32 s72, s65, 49152\n"
    "buffer_load_dwordx4 v1, s[16:19], s72 offen offset:0 sc1 lds\n"
    "s_add_u32 m0, s65, 25600\n"
    "s_add_u32 s72, s65, 50176\n"
    "buffer_load_dwordx4 v1, s[16:19], s72 offen offset:0 sc1 lds\n"
    "s_add_u32 m0, s65, 26624\n"
    "s_add_u32 s72, s65, 51200\n"
    "buffer_load_dwordx4 v1, s[16:19], s72 offen offset:0 sc1 lds\n"
    "s_add_u32 m0, s65, 27648\n"
    "s_add_u32 s72, s65, 52224\n"
    "buffer_load_dwordx4 v1, s[16:19], s72 offen offset:0 sc1 lds\n"
    "s_add_u32 m0, s65, 28672\n"
    "s_add_u32 s72, s65, 53248\n"
    "buffer_load_dwordx4 v1, s[16:19], s72 offen offset:0 sc1 lds\n"
    "s_add_u32 m0, s65, 29696\n"
    "s_add_u32 s72, s65, 54272\n"
    "buffer_load_dwordx4 v1, s[16:19], s72 offen offset:0 sc1 lds\n"
    "s_waitcnt vmcnt(13)\n"
    "ds_read_b128 v[156:159], v96 offset:0\n"
    "ds_read_b128 v[172:175], v96 offset:256\n"
    "ds_read_b128 v[168:171], v97 offset:128\n"
    "ds_read_b128 v[176:179], v97 offset:256\n"
    "ds_read_b128 v[160:163], v97 offset:0\n"
    "ds_read_b128 v[164:167], v96 offset:128\n"
    "s_waitcnt lgkmcnt(5)\n"
    "v_pk_maximum3_f16 v98, v156, v157, v98\n"
    "v_pk_maximum3_f16 v98, v158, v159, v98\n"
    "v_pk_add_u16 v157, s89, v157 clamp\n"
    "v_pk_add_u16 v158, s89, v158 clamp\n"
    "v_pk_add_u16 v156, s89, v156 clamp\n"
    "v_pk_add_u16 v159, s89, v159 clamp\n"
    "s_waitcnt lgkmcnt(1)\n"
    "v_pk_maximum3_f16 v98, v160, v161, v98\n"
    "s_add_u32 m0, s65, 73728\n"
    "s_add_u32 s72, s65, 73728\n"
    "buffer_load_dwordx4 v1, s[16:19], s72 offen offset:0 sc1 lds\n"
    "v_mfma_f32_16x16x32_bf16 v[28:31], a[136:139], v[156:159], v[32:35]\n"
    "s_waitcnt lgkmcnt(0)\n"
    "v_pk_maximum3_f16 v98, v162, v163, v98\n"
    "v_pk_maximum3_f16 v98, v164, v165, v98\n"
    "v_mfma_f32_16x16x32_bf16 v[12:15], a[112:115], v[156:159], v[16:19]\n"
    "v_pk_add_u16 v161, s89, v161 clamp\n"
    "v_pk_maximum3_f16 v98, v166, v167, v98\n"
    "v_pk_add_u16 v160, s89, v160 clamp\n"
    "v_pk_add_u16 v162, s89, v162 clamp\n"
    "v_mfma_f32_16x16x32_bf16 v[60:63], a[24:27], v[156:159], v[64:67]\n"
    "v_pk_maximum3_f16 v98, v168, v169, v98\n"
    "v_pk_add_u16 v163, s89, v163 clamp\n"
    "v_mfma_f32_16x16x32_bf16 v[44:47], a[0:3], v[156:159], v[48:51]\n"
    "v_pk_maximum3_f16 v98, v170, v171, v98\n"
    "v_pk_add_u16 v164, s89, v164 clamp\n"
    "v_mfma_f32_16x16x32_bf16 v[12:15], a[116:119], v[160:163], v[12:15]\n"
    "v_pk_add_u16 v165, s89, v165 clamp\n"
    "s_add_u32 m0, s65, 74752\n"
    "s_add_u32 s72, s65, 74752\n"
    "buffer_load_dwordx4 v1, s[16:19], s72 offen offset:0 sc1 lds\n"
    "v_pk_add_u16 v169, s89, v169 clamp\n"
    "v_pk_add_u16 v168, s89, v168 clamp\n"
    "v_mfma_f32_16x16x32_bf16 v[60:63], a[28:31], v[160:163], v[60:63]\n"
    "v_pk_maximum3_f16 v98, v172, v173, v98\n"
    "v_pk_add_u16 v170, s89, v170 clamp\n"
    "v_mfma_f32_16x16x32_bf16 v[28:31], a[140:143], v[160:163], v[28:31]\n"
    "v_pk_maximum3_f16 v98, v174, v175, v98\n"
    "v_pk_add_u16 v166, s89, v166 clamp\n"
    "v_pk_add_u16 v167, s89, v167 clamp\n"
    "v_mfma_f32_16x16x32_bf16 v[44:47], a[4:7], v[160:163], v[44:47]\n"
    "v_pk_maximum3_f16 v98, v176, v177, v98\n"
    "v_accvgpr_write_b32 a251, v167\n"
    "v_mfma_f32_16x16x32_bf16 v[44:47], a[8:11], v[164:167], v[44:47]\n"
    "v_pk_maximum3_f16 v98, v178, v179, v98\n"
    "v_pk_add_u16 v176, s89, v176 clamp\n"
    "v_mfma_f32_16x16x32_bf16 v[60:63], a[32:35], v[164:167], v[60:63]\n"
    "v_pk_add_u16 v171, s89, v171 clamp\n"
    "s_add_u32 m0, s65, 75776\n"
    "s_add_u32 s72, s65, 75776\n"
    "buffer_load_dwordx4 v1, s[16:19], s72 offen offset:0 sc1 lds\n"
    "v_pk_add_u16 v174, s89, v174 clamp\n"
    "v_mfma_f32_16x16x32_bf16 v[28:31], a[144:147], v[164:167], v[28:31]\n"
    "v_pk_add_u16 v173, s89, v173 clamp\n"
    "v_pk_add_u16 v172, s89, v172 clamp\n"
    "v_mfma_f32_16x16x32_bf16 v[12:15], a[120:123], v[164:167], v[12:15]\n"
    "v_accvgpr_write_b32 a254, v170\n"
    "v_pk_add_u16 v178, s89, v178 clamp\n"
    "v_mfma_f32_16x16x32_bf16 v[28:31], a[148:151], v[168:171], v[28:31]\n"
    "v_accvgpr_write_b32 a243, v159\n"
    "v_accvgpr_write_b32 a162, v174\n"
    "v_mfma_f32_16x16x32_bf16 v[12:15], a[124:127], v[168:171], v[12:15]\n"
    "v_pk_add_u16 v179, s89, v179 clamp\n"
    "v_pk_add_u16 v175, s89, v175 clamp\n"
    "v_mfma_f32_16x16x32_bf16 v[44:47], a[12:15], v[168:171], v[44:47]\n"
    "v_accvgpr_write_b32 a166, v178\n"
    "v_accvgpr_write_b32 a161, v173\n"
    "v_accvgpr_write_b32 a245, v161\n"
    "s_add_u32 m0, s65, 76800\n"
    "s_add_u32 s72, s65, 76800\n"
    "buffer_load_dwordx4 v1, s[16:19], s72 offen offset:0 sc1 lds\n"
    "v_mfma_f32_16x16x32_bf16 v[28:31], a[152:155], v[172:175], v[28:31]\n"
    "v_accvgpr_write_b32 a163, v175\n"
    "v_pk_add_u16 v177, s89, v177 clamp\n"
    "v_mfma_f32_16x16x32_bf16 v[44:47], a[16:19], v[172:175], v[44:47]\n"
    "v_accvgpr_write_b32 a160, v172\n"
    "v_accvgpr_write_b32 a252, v168\n"
    "v_mfma_f32_16x16x32_bf16 v[28:31], a[156:159], v[176:179], v[28:31]\n"
    "v_accvgpr_write_b32 a249, v165\n"
    "v_accvgpr_write_b32 a255, v171\n"
    "v_mfma_f32_16x16x32_bf16 v[12:15], a[128:131], v[172:175], v[12:15]\n"
    "v_accvgpr_write_b32 a247, v163\n"
    "v_accvgpr_write_b32 a164, v176\n"
    "v_mfma_f32_16x16x32_bf16 v[60:63], a[36:39], v[168:171], v[60:63]\n"
    "v_accvgpr_write_b32 a242, v158\n"
    "v_accvgpr_write_b32 a250, v166\n"
    "v_mfma_f32_16x16x32_bf16 v[44:47], a[20:23], v[176:179], v[44:47]\n"
    "v_accvgpr_write_b32 a241, v157\n"
    "s_add_u32 m0, s65, 77824\n"
    "s_add_u32 s72, s65, 77824\n"
    "buffer_load_dwordx4 v1, s[16:19], s72 offen offset:0 sc1 lds\n"
    "v_accvgpr_write_b32 a248, v164\n"
    "v_mfma_f32_16x16x32_bf16 v[12:15], a[132:135], v[176:179], v[12:15]\n"
    "v_accvgpr_write_b32 a246, v162\n"
    "v_exp_f32 v29, v29\n"
    "v_mfma_f32_16x16x32_bf16 v[60:63], a[40:43], v[172:175], v[60:63]\n"
    "v_exp_f32 v28, v28\n"
    "v_mfma_f32_16x16x32_bf16 v[60:63], a[44:47], v[176:179], v[60:63]\n"
    "v_exp_f32 v31, v31\n"
    "v_exp_f32 v30, v30\n"
    "v_exp_f32 v45, v45\n"
    "v_cvt_pk_bf16_f32 v86, v28, v29\n"
    "v_exp_f32 v47, v47\n"
    "v_accvgpr_write_b32 a240, v156\n"
    "v_exp_f32 v12, v12\n"
    "v_exp_f32 v14, v14\n"
    "v_accvgpr_write_b32 a167, v179\n"
    "v_exp_f32 v15, v15\n"
    "s_add_u32 m0, s65, 78848\n"
    "s_add_u32 s72, s65, 78848\n"
    "buffer_load_dwordx4 v1, s[16:19], s72 offen offset:0 sc1 lds\n"
    "v_accvgpr_write_b32 a253, v169\n"
    "v_exp_f32 v63, v63\n"
    "v_exp_f32 v13, v13\n"
    "v_exp_f32 v44, v44\n"
    "v_cvt_pk_bf16_f32 v84, v12, v13\n"
    "v_cvt_pk_bf16_f32 v87, v30, v31\n"
    "v_exp_f32 v46, v46\n"
    "v_exp_f32 v61, v61\n"
    "v_accvgpr_write_b32 a165, v177\n"
    "v_exp_f32 v60, v60\n"
    "v_exp_f32 v62, v62\n"
    "v_cvt_pk_bf16_f32 v90, v60, v61\n"
    "v_accvgpr_write_b32 a244, v160\n"
    "v_cvt_pk_bf16_f32 v85, v14, v15\n"
    "v_cvt_pk_bf16_f32 v89, v46, v47\n"
    "v_cvt_pk_bf16_f32 v88, v44, v45\n"
    "v_cvt_pk_bf16_f32 v91, v62, v63\n"
    "s_bitcmp1_b32 s83, 1\n"
    "s_cbranch_scc1 .Lf4b\n"
    "s_lshl_b32 s73, s53, 1\n"
    "s_add_u32 s73, s73, s79\n"
    "s_mul_i32 s74, s73, 6144\n"
    "s_add_u32 s74, s74, 1425408\n"
    "s_lshl_b32 s73, s73, 8\n"
    "s_add_u32 s73, s73, 1557504\n"
    "s_mov_b64 s[20:21], s[16:17]\n"
    "s_mov_b32 s44, 98304\n"
    "s_mov_b32 s22, 1572864\n"
    "s_sub_u32 s72, s42, 1572864\n"
    "s_sub_u32 s45, s42, 49152\n"
    "s_bitcmp1_b32 s84, 0\n"
    "s_cselect_b32 s52, s71, s73\n"
    "s_cselect_b32 s71, s71, s74\n"
    "s_cselect_b32 s63, s63, s72\n"
    "s_cselect_b32 s42, s45, s42\n"
    "s_cselect_b32 s45, s44, 12288\n"
    "s_cselect_b32 s44, s44, 24576\n"
    "s_cselect_b32 s22, s22, s18\n"
    "s_cselect_b64 s[40:41], s[40:41], s[8:9]\n"
    "s_cselect_b32 s60, s60, 24576\n"
    "s_cselect_b32 s61, s61, 28672\n"
    "s_cselect_b32 s62, s62, 32768\n"
    "s_mov_b32 s66, 1\n"
    "s_add_u32 s68, s68, 24576\n"
    "s_add_u32 s81, s81, 128\n"
    "s_add_u32 s80, s80, 256\n"
    "s_add_u32 s71, s71, s44\n"
    "s_add_u32 s52, s52, s45\n"
    "s_add_u32 s75, s71, s63\n"
    "s_branch .LhVP\n"
    "s_branch .Lf4e\n"
    ".Lf4b:\n"
    "s_add_u32 s75, s71, s63\n"
    "buffer_load_dwordx4 v[204:207], v242, s[40:43], s75 offen offset:0 nt\n"
    "buffer_load_dwordx4 v[208:211], v242, s[40:43], s75 offen offset:768 nt\n"
    "buffer_load_dwordx4 v[212:215], v242, s[40:43], s75 offen offset:256 nt\n"
    "buffer_load_dwordx4 v[216:219], v242, s[40:43], s75 offen offset:1024 nt\n"
    "buffer_load_dwordx4 v[220:223], v242, s[40:43], s75 offen offset:512 nt\n"
    "buffer_load_dwordx4 v[224:227], v242, s[40:43], s75 offen offset:1280 nt\n"
    "s_lshl_b32 s73, s53, 1\n"
    "s_add_u32 s73, s73, s79\n"
    "s_mul_i32 s74, s73, 6144\n"
    "s_add_u32 s74, s74, 1425408\n"
    "s_lshl_b32 s73, s73, 8\n"
    "s_add_u32 s73, s73, 1557504\n"
    "s_mov_b64 s[20:21], s[16:17]\n"
    "s_mov_b32 s44, 98304\n"
    "s_mov_b32 s22, 1572864\n"
    "s_sub_u32 s72, s42, 1572864\n"
    "s_sub_u32 s45, s42, 49152\n"
    "s_bitcmp1_b32 s84, 0\n"
    "s_cselect_b32 s52, s71, s73\n"
    "s_cselect_b32 s71, s71, s74\n"
    "s_cselect_b32 s63, s63, s72\n"
    "s_cselect_b32 s42, s45, s42\n"
    "s_cselect_b32 s45, s44, 12288\n"
    "s_cselect_b32 s44, s44, 24576\n"
    "s_cselect_b32 s22, s22, s18\n"
    "s_cselect_b64 s[40:41], s[40:41], s[8:9]\n"
    "s_cselect_b32 s60, s60, 24576\n"
    "s_cselect_b32 s61, s61, 28672\n"
    "s_cselect_b32 s62, s62, 32768\n"
    "s_mov_b32 s66, 1\n"
    "s_add_u32 s68, s68, 24576\n"
    "s_add_u32 s81, s81, 128\n"
    "s_branch .LhVN\n"
    ".Lf4e:\n"
    "s_nop 4\n"
    ".Lf5e:\n"
    "s_nop 4\n"
    ".fill ((0x20000 - (. - _Z8attn_fwdPKtPKiS2_S0_S0_S0_S0_PcPt)) & 4095) / 4, 4, 0xBF800000\n"
    ".LhKP:\n"
    "s_waitcnt vmcnt(12)\n"
    "ds_read_b128 v[52:55], v251 offset:0\n"
    "ds_read_b128 v[192:195], v97 offset:49280\n"
    "ds_read_b128 v[196:199], v96 offset:49408\n"
    "ds_read_b128 v[200:203], v97 offset:49408\n"
    "s_add_u32 s83, s67, 4096\n"
    "ds_read_b128 v[188:191], v96 offset:49280\n"
    "ds_read_b128 v[180:183], v96 offset:49152\n"
    "s_nop 0\n"
    "ds_read_b128 v[184:187], v97 offset:49152\n"
    "s_waitcnt lgkmcnt(6)\n"
    "v_smfmac_f32_16x16x64_bf16 v[108:111], v[52:55], v[68:75], v253\n"
    "s_waitcnt lgkmcnt(3)\n"
    "v_pk_maximum3_f16 v98, v200, v201, v98\n"
    "v_pk_maximum3_f16 v98, v202, v203, v98\n"
    "v_pk_maximum3_f16 v98, v196, v197, v98\n"
    "v_pk_add_u16 v203, s89, v203 clamp\n"
    "v_pk_maximum3_f16 v98, v198, v199, v98\n"
    "v_pk_add_u16 v200, s89, v200 clamp\n"
    "v_pk_add_u16 v202, s89, v202 clamp\n"
    "v_pk_add_u16 v201, s89, v201 clamp\n"
    "v_pk_maximum3_f16 v98, v192, v193, v98\n"
    "v_pk_maximum3_f16 v98, v194, v195, v98\n"
    "v_mfma_f32_16x16x32_bf16 v[16:19], a[132:135], v[200:203], v[16:19]\n"
    "s_add_u32 m0, s68, .Lend-.Lend2+(0)\n"
    "v_pk_add_u16 v199, s89, v199 clamp\n"
    "v_pk_add_u16 v197, s89, v197 clamp\n"
    "v_mfma_f32_16x16x32_bf16 v[64:67], a[44:47], v[200:203], v[64:67]\n"
    "buffer_load_dwordx4 v1, s[16:19], s67 offen offset:0 sc1 lds\n"
    "v_pk_add_u16 v198, s89, v198 clamp\n"
    "v_mfma_f32_16x16x32_bf16 v[32:35], a[156:159], v[200:203], v[32:35]\n"
    "v_pk_add_u16 v196, s89, v196 clamp\n"
    "v_accvgpr_write_b32 a191, v203\n"
    "v_mfma_f32_16x16x32_bf16 v[48:51], a[20:23], v[200:203], v[48:51]\n"
    "s_waitcnt lgkmcnt(2)\n"
    "v_pk_maximum3_f16 v98, v188, v189, v98\n"
    "v_pk_add_u16 v195, s89, v195 clamp\n"
    "v_mfma_f32_16x16x32_bf16 v[64:67], a[40:43], v[196:199], v[64:67]\n"
    "v_pk_add_u16 v192, s89, v192 clamp\n"
    "v_pk_add_u16 v193, s89, v193 clamp\n"
    "v_mfma_f32_16x16x32_bf16 v[32:35], a[152:155], v[196:199], v[32:35]\n"
    "v_pk_maximum3_f16 v98, v190, v191, v98\n"
    "v_pk_add_u16 v194, s89, v194 clamp\n"
    "v_smfmac_f32_16x16x64_bf16 v[116:119], v[52:55], v[84:91], v253\n"
    "v_pk_add_u16 v191, s89, v191 clamp\n"
    "v_mfma_f32_16x16x32_bf16 v[48:51], a[16:19], v[196:199], v[48:51]\n"
    "buffer_load_dwordx4 v1, s[16:19], s67 offen offset:1024 sc1 lds\n"
    "v_mfma_f32_16x16x32_bf16 v[16:19], a[128:131], v[196:199], v[16:19]\n"
    "s_waitcnt lgkmcnt(0)\n"
    "v_pk_maximum3_f16 v98, v184, v185, v98\n"
    "v_pk_add_u16 v189, s89, v189 clamp\n"
    "v_mfma_f32_16x16x32_bf16 v[48:51], a[12:15], v[192:195], v[48:51]\n"
    "v_pk_add_u16 v190, s89, v190 clamp\n"
    "v_pk_add_u16 v184, s89, v184 clamp\n"
    "v_accvgpr_write_b32 a188, v200\n"
    "v_mfma_f32_16x16x32_bf16 v[64:67], a[36:39], v[192:195], v[64:67]\n"
    "v_pk_maximum3_f16 v98, v186, v187, v98\n"
    "v_pk_add_u16 v188, s89, v188 clamp\n"
    "v_mfma_f32_16x16x32_bf16 v[32:35], a[148:151], v[192:195], v[32:35]\n"
    "v_pk_add_u16 v187, s89, v187 clamp\n"
    "v_pk_maximum3_f16 v98, v180, v181, v98\n"
    "v_mfma_f32_16x16x32_bf16 v[32:35], a[144:147], v[188:191], v[32:35]\n"
    "v_pk_maximum3_f16 v98, v182, v183, v98\n"
    "v_pk_add_u16 v185, s89, v185 clamp\n"
    "v_mfma_f32_16x16x32_bf16 v[16:19], a[124:127], v[192:195], v[16:19]\n"
    "buffer_load_dwordx4 v1, s[16:19], s67 offen offset:2048 sc1 lds\n"
    "v_mfma_f32_16x16x32_bf16 v[16:19], a[120:123], v[188:191], v[16:19]\n"
    "s_waitcnt vmcnt(9)\n"
    "v_pk_add_u16 v186, s89, v186 clamp\n"
    "v_pk_add_u16 v181, s89, v181 clamp\n"
    "v_mfma_f32_16x16x32_bf16 v[64:67], a[32:35], v[188:191], v[64:67]\n"
    "s_barrier\n"
    "ds_read_b128 a[48:51], v252 offset:24576\n"
    "v_mfma_f32_16x16x32_bf16 v[48:51], a[8:11], v[188:191], v[48:51]\n"
    "ds_read_b128 a[72:75], v252 offset:25600\n"
    "ds_read_b128 a[92:95], v252 offset:35840\n"
    "v_mfma_f32_16x16x32_bf16 v[32:35], a[140:143], v[184:187], v[32:35]\n"
    "v_pk_add_u16 v183, s89, v183 clamp\n"
    "v_pk_add_u16 v180, s89, v180 clamp\n"
    "v_pk_add_u16 v182, s89, v182 clamp\n"
    "v_mfma_f32_16x16x32_bf16 v[16:19], a[116:119], v[184:187], v[16:19]\n"
    "ds_read_b128 a[84:87], v252 offset:31744\n"
    "ds_read_b128 a[52:55], v252 offset:26624\n"
    "v_mfma_f32_16x16x32_bf16 v[32:35], a[136:139], v[180:183], v[32:35]\n"
    "buffer_load_dwordx4 v1, s[16:19], s67 offen offset:3072 sc1 lds\n"
    "v_mfma_f32_16x16x32_bf16 v[48:51], a[4:7], v[184:187], v[48:51]\n"
    "ds_read_b128 a[88:91], v252 offset:33792\n"
    "ds_read_b128 a[56:59], v252 offset:28672\n"
    "v_mfma_f32_16x16x32_bf16 v[64:67], a[28:31], v[184:187], v[64:67]\n"
    "v_accvgpr_write_b32 a170, v182\n"
    "v_accvgpr_write_b32 a177, v189\n"
    "v_mfma_f32_16x16x32_bf16 v[16:19], a[112:115], v[180:183], v[16:19]\n"
    "s_add_u32 m0, s68, 4096\n"
    "ds_read_b128 a[76:79], v252 offset:27648\n"
    "ds_read_b128 a[68:71], v252 offset:34816\n"
    "ds_read_b128 a[60:63], v252 offset:30720\n"
    "v_mfma_f32_16x16x32_bf16 v[48:51], a[0:3], v[180:183], v[48:51]\n"
    "s_waitcnt lgkmcnt(9)\n"
    "ds_read_b128 a[80:83], v252 offset:29696\n"
    "v_accvgpr_write_b32 a179, v191\n"
    "v_mfma_f32_16x16x32_bf16 v[4:7], a[48:51], a[192:195], v[100:103]\n"
    "s_waitcnt lgkmcnt(8)\n"
    "v_pk_add_u16 v98, s89, v98\n"
    "v_accvgpr_write_b32 a174, v186\n"
    "v_mfma_f32_16x16x32_bf16 v[24:27], a[92:95], a[236:239], v[104:107]\n"
    "buffer_load_dwordx4 v1, s[16:19], s83 offen offset:0 sc1 lds\n"
    "v_mfma_f32_16x16x32_bf16 v[12:15], a[48:51], a[240:243], v[100:103]\n"
    "v_accvgpr_write_b32 a186, v198\n"
    "v_accvgpr_write_b32 a180, v192\n"
    "v_mfma_f32_16x16x32_bf16 v[20:23], a[72:75], a[192:195], v[104:107]\n"
    "v_accvgpr_write_b32 a181, v193\n"
    "v_accvgpr_write_b32 a172, v184\n"
    "v_mfma_f32_16x16x32_bf16 v[28:31], a[72:75], a[240:243], v[104:107]\n"
    "s_waitcnt lgkmcnt(6)\n"
    "v_exp_f32 v33, v33\n"
    "v_mfma_f32_16x16x32_bf16 v[4:7], a[52:55], a[196:199], v[4:7]\n"
    "ds_read_b128 a[64:67], v252 offset:32768\n"
    "v_accvgpr_write_b32 a184, v196\n"
    "v_mfma_f32_16x16x32_bf16 v[12:15], a[52:55], a[244:247], v[12:15]\n"
    "s_waitcnt lgkmcnt(6)\n"
    "v_accvgpr_write_b32 a168, v180\n"
    "v_accvgpr_write_b32 a182, v194\n"
    "v_mfma_f32_16x16x32_bf16 v[24:27], a[88:91], a[232:235], v[24:27]\n"
    "s_waitcnt lgkmcnt(3)\n"
    "v_accvgpr_write_b32 a183, v195\n"
    "v_accvgpr_write_b32 a190, v202\n"
    "v_mfma_f32_16x16x32_bf16 v[8:11], a[68:71], a[236:239], v[100:103]\n"
    "buffer_load_dwordx4 v1, s[16:19], s83 offen offset:1024 sc1 lds\n"
    "v_mfma_f32_16x16x32_bf16 v[4:7], a[56:59], a[200:203], v[4:7]\n"
    "v_accvgpr_write_b32 a173, v185\n"
    "v_accvgpr_write_b32 a171, v183\n"
    "v_mfma_f32_16x16x32_bf16 v[12:15], a[56:59], a[248:251], v[12:15]\n"
    "s_add_u32 m0, s64, 0\n"
    "v_accvgpr_write_b32 a176, v188\n"
    "v_accvgpr_write_b32 a189, v201\n"
    "v_accvgpr_write_b32 a169, v181\n"
    "v_mfma_f32_16x16x32_bf16 v[64:67], a[24:27], v[180:183], v[64:67]\n"
    "v_accvgpr_write_b32 a175, v187\n"
    "v_and_b32 v98, 0x80008000, v98\n"
    "v_mfma_f32_16x16x32_bf16 v[20:23], a[76:79], a[196:199], v[20:23]\n"
    "s_waitcnt lgkmcnt(0)\n"
    "v_cmp_ne_u32 s[34:35], 0, v98\n"
    "v_accvgpr_write_b32 a187, v199\n"
    "v_mfma_f32_16x16x32_bf16 v[8:11], a[64:67], a[232:235], v[8:11]\n"
    "s_add_u32 s83, s67, 24576\n"
    "v_accvgpr_write_b32 a185, v197\n"
    "v_accvgpr_write_b32 a178, v190\n"
    "v_mfma_f32_16x16x32_bf16 v[24:27], a[84:87], a[228:231], v[24:27]\n"
    "buffer_load_dwordx4 v1, s[16:19], s83 offen offset:0 sc1 lds\n"
    "v_mfma_f32_16x16x32_bf16 v[12:15], a[60:63], a[252:255], v[12:15]\n"
    "v_exp_f32 v32, v32\n"
    "v_mfma_f32_16x16x32_bf16 v[4:7], a[60:63], a[204:207], v[4:7]\n"
    "v_exp_f32 v18, v18\n"
    "v_mfma_f32_16x16x32_bf16 v[12:15], a[64:67], a[160:163], v[12:15]\n"
    "v_exp_f32 v48, v48\n"
    "v_mfma_f32_16x16x32_bf16 v[4:7], a[64:67], a[208:211], v[4:7]\n"
    "v_exp_f32 v19, v19\n"
    "v_mfma_f32_16x16x32_bf16 v[4:7], a[68:71], a[212:215], v[4:7]\n"
    "v_cvt_pk_bf16_f32 v93, v18, v19\n"
    "v_exp_f32 v17, v17\n"
    "v_mfma_f32_16x16x32_bf16 v[28:31], a[76:79], a[244:247], v[28:31]\n"
    "buffer_load_dwordx4 v1, s[16:19], s83 offen offset:1024 sc1 lds\n"
    "v_mfma_f32_16x16x32_bf16 v[24:27], a[80:83], a[224:227], v[24:27]\n"
    "v_exp_f32 v35, v35\n"
    "v_mfma_f32_16x16x32_bf16 v[20:23], a[80:83], a[200:203], v[20:23]\n"
    "v_exp_f32 v16, v16\n"
    "v_mfma_f32_16x16x32_bf16 v[8:11], a[60:63], a[228:231], v[8:11]\n"
    "v_exp_f32 v34, v34\n"
    "v_mfma_f32_16x16x32_bf16 v[20:23], a[84:87], a[204:207], v[20:23]\n"
    "v_cvt_pk_bf16_f32 v95, v34, v35\n"
    "v_mfma_f32_16x16x32_bf16 v[8:11], a[56:59], a[224:227], v[8:11]\n"
    "v_exp_f32 v66, v66\n"
    "v_mfma_f32_16x16x32_bf16 v[24:27], a[76:79], a[220:223], v[24:27]\n"
    "v_cvt_pk_bf16_f32 v94, v32, v33\n"
    "v_mfma_f32_16x16x32_bf16 v[8:11], a[52:55], a[220:223], v[8:11]\n"
    "buffer_load_dwordx4 v1, s[16:19], s83 offen offset:2048 sc1 lds\n"
    "v_mfma_f32_16x16x32_bf16 v[32:35], a[92:95], a[188:191], v[104:107]\n"
    "v_exp_f32 v49, v49\n"
    "v_mfma_f32_16x16x32_bf16 v[12:15], a[68:71], a[164:167], v[12:15]\n"
    "v_exp_f32 v67, v67\n"
    "v_mfma_f32_16x16x32_bf16 v[32:35], a[88:91], a[184:187], v[32:35]\n"
    "s_waitcnt vmcnt(9)\n"
    "s_barrier\n"
    "ds_read_b128 a[116:119], v248 offset:12288\n"
    "v_mfma_f32_16x16x32_bf16 v[8:11], a[48:51], a[216:219], v[8:11]\n"
    "ds_read_b128 a[144:147], v248 offset:4096\n"
    "v_cvt_pk_bf16_f32 v99, v66, v67\n"
    "v_mfma_f32_16x16x32_bf16 v[32:35], a[84:87], a[180:183], v[32:35]\n"
    "ds_read_b128 a[124:127], v248 offset:13312\n"
    "v_cvt_pk_bf16_f32 v92, v16, v17\n"
    "v_mfma_f32_16x16x32_bf16 v[16:19], a[68:71], a[188:191], v[100:103]\n"
    "buffer_load_dwordx4 v1, s[16:19], s83 offen offset:3072 sc1 lds\n"
    "v_mfma_f32_16x16x32_bf16 v[20:23], a[88:91], a[208:211], v[20:23]\n"
    "s_add_u32 s83, s67, 28672\n"
    "ds_read_b128 a[136:139], v248 offset:3072\n"
    "ds_read_b128 a[140:143], v248 offset:15360\n"
    "v_mfma_f32_16x16x32_bf16 v[32:35], a[80:83], a[176:179], v[32:35]\n"
    "ds_read_b128 a[120:123], v248 offset:1024\n"
    "ds_read_b128 a[148:151], v248 offset:16384\n"
    "v_mfma_f32_16x16x32_bf16 v[28:31], a[80:83], a[248:251], v[28:31]\n"
    "v_exp_f32 v64, v64\n"
    "v_mfma_f32_16x16x32_bf16 v[28:31], a[84:87], a[252:255], v[28:31]\n"
    "s_add_u32 m0, s64, 4096\n"
    "v_exp_f32 v50, v50\n"
    "v_mfma_f32_16x16x32_bf16 v[16:19], a[64:67], a[184:187], v[16:19]\n"
    "v_cvt_pk_bf16_f32 v96, v48, v49\n"
    "v_mfma_f32_16x16x32_bf16 v[16:19], a[60:63], a[180:183], v[16:19]\n"
    "ds_read_b128 a[152:155], v248 offset:5120\n"
    "ds_read_b128 a[132:135], v248 offset:14336\n"
    "v_mfma_f32_16x16x32_bf16 v[32:35], a[76:79], a[172:175], v[32:35]\n"
    "s_waitcnt lgkmcnt(7)\n"
    "buffer_load_dwordx4 v1, s[16:19], s83 offen offset:0 sc1 lds\n"
    "v_mfma_f32_16x16x32_bf16 a[32:35], a[144:147], v[92:95], 0\n"
    "ds_read_b128 a[128:131], v248 offset:2048\n"
    "v_smfmac_f32_16x16x64_bf16 v[112:115], v[52:55], v[76:83], v253\n"
    "v_mfma_f32_16x16x32_bf16 v[24:27], a[72:75], a[216:219], v[24:27]\n"
    "s_waitcnt lgkmcnt(5)\n"
    "v_exp_f32 v65, v65\n"
    "v_mfma_f32_16x16x32_bf16 v[152:155], a[140:143], v[80:83], 0\n"
    "s_waitcnt lgkmcnt(4)\n"
    "v_mfma_f32_16x16x32_bf16 v[144:147], a[120:123], v[76:79], 0\n"
    "v_exp_f32 v51, v51\n"
    "v_mfma_f32_16x16x32_bf16 v[16:19], a[56:59], a[176:179], v[16:19]\n"
    "s_waitcnt lgkmcnt(3)\n"
    "buffer_load_dwordx4 v1, s[16:19], s83 offen offset:1024 sc1 lds\n"
    "v_mfma_f32_16x16x32_bf16 v[188:191], a[148:151], v[72:75], 0\n"
    "s_waitcnt lgkmcnt(1)\n"
    "v_cvt_pk_bf16_f32 v97, v50, v51\n"
    "v_cvt_pk_bf16_f32 v98, v64, v65\n"
    "v_mfma_f32_16x16x32_bf16 v[148:151], a[132:135], v[80:83], 0\n"
    "s_waitcnt lgkmcnt(0)\n"
    "ds_read_b128 a[112:115], v248 offset:0\n"
    "v_mfma_f32_16x16x32_bf16 v[148:151], a[128:131], v[76:79], v[148:151]\n"
    "v_exp_f32 v4, v4\n"
    "v_mfma_f32_16x16x32_bf16 v[180:183], a[132:135], v[96:99], 0\n"
    "v_mfma_f32_16x16x32_bf16 v[176:179], a[120:123], v[92:95], 0\n"
    "v_exp_f32 v14, v14\n"
    "v_mfma_f32_16x16x32_bf16 v[28:31], a[88:91], a[160:163], v[28:31]\n"
    "v_exp_f32 v10, v10\n"
    "v_mfma_f32_16x16x32_bf16 v[164:167], a[128:131], v[84:87], 0\n"
    "buffer_load_dwordx2 v[204:205], v242, s[28:31], s75 offen offset:0 nt\n"
    "v_mfma_f32_16x16x32_bf16 v[176:179], a[124:127], v[96:99], v[176:179]\n"
    "v_mfma_f32_16x16x32_bf16 v[20:23], a[92:95], a[212:215], v[20:23]\n"
    "buffer_load_dwordx2 v[206:207], v242, s[28:31], s75 offen offset:768 nt\n"
    "v_mfma_f32_16x16x32_bf16 v[16:19], a[52:55], a[172:175], v[16:19]\n"
    "v_mfma_f32_16x16x32_bf16 v[132:135], a[128:131], v[68:71], 0\n"
    "buffer_load_dwordx2 v[208:209], v242, s[28:31], s75 offen offset:3072 nt\n"
    "v_mfma_f32_16x16x32_bf16 v[136:139], a[136:139], v[68:71], 0\n"
    "v_mfma_f32_16x16x32_bf16 v[16:19], a[48:51], a[168:171], v[16:19]\n"
    "buffer_load_dwordx2 v[210:211], v242, s[28:31], s75 offen offset:3840 nt\n"
    "v_mfma_f32_16x16x32_bf16 v[180:183], a[128:131], v[92:95], v[180:183]\n"
    "v_mfma_f32_16x16x32_bf16 v[184:187], a[140:143], v[96:99], 0\n"
    "s_waitcnt lgkmcnt(0)\n"
    "buffer_load_dwordx2 v[212:213], v242, s[28:31], s76 offen offset:0 nt\n"
    "v_mfma_f32_16x16x32_bf16 v[156:159], a[112:115], v[84:87], 0\n"
    "v_mfma_f32_16x16x32_bf16 a[16:19], a[148:151], v[88:91], 0\n"
    "buffer_load_dwordx2 v[214:215], v242, s[28:31], s76 offen offset:768 nt\n"
    "v_mfma_f32_16x16x32_bf16 v[168:171], a[136:139], v[84:87], 0\n"
    "v_exp_f32 v22, v22\n"
    "v_mfma_f32_16x16x32_bf16 v[152:155], a[136:139], v[76:79], v[152:155]\n"
    "buffer_load_dwordx2 v[216:217], v242, s[28:31], s76 offen offset:3072 nt\n"
    "v_mfma_f32_16x16x32_bf16 v[160:163], a[124:127], v[88:91], 0\n"
    "v_mfma_f32_16x16x32_bf16 v[140:143], a[116:119], v[80:83], 0\n"
    "buffer_load_dwordx2 v[218:219], v242, s[28:31], s76 offen offset:3840 nt\n"
    "v_mfma_f32_16x16x32_bf16 v[132:135], a[132:135], v[72:75], v[132:135]\n"
    "ds_read_b128 v[100:103], v250 offset:896\n"
    "ds_read_b128 a[128:131], v248 offset:8192\n"
    "v_mfma_f32_16x16x32_bf16 a[16:19], a[144:147], v[84:87], a[16:19]\n"
    "buffer_load_dwordx2 v[220:221], v242, s[28:31], s77 offen offset:0 nt\n"
    "v_mfma_f32_16x16x32_bf16 v[140:143], a[112:115], v[76:79], v[140:143]\n"
    "v_mfma_f32_16x16x32_bf16 v[128:131], a[124:127], v[72:75], 0\n"
    "buffer_load_dwordx2 v[222:223], v242, s[28:31], s77 offen offset:768 nt\n"
    "v_mfma_f32_16x16x32_bf16 a[0:3], a[144:147], v[76:79], 0\n"
    "v_exp_f32 v8, v8\n"
    "v_mfma_f32_16x16x32_bf16 a[20:23], a[152:155], v[84:87], 0\n"
    "buffer_load_dwordx2 v[224:225], v242, s[28:31], s77 offen offset:3072 nt\n"
    "v_mfma_f32_16x16x32_bf16 v[188:191], a[144:147], v[68:71], v[188:191]\n"
    "v_mfma_f32_16x16x32_bf16 v[144:147], a[124:127], v[80:83], v[144:147]\n"
    "buffer_load_dwordx2 v[226:227], v242, s[28:31], s77 offen offset:3840 nt\n"
    "v_mfma_f32_16x16x32_bf16 a[0:3], a[148:151], v[80:83], a[0:3]\n"
    "v_exp_f32 v15, v15\n"
    "v_mfma_f32_16x16x32_bf16 v[172:175], a[116:119], v[96:99], 0\n"
    "s_waitcnt lgkmcnt(0)\n"
    "buffer_load_dwordx2 v[228:229], v242, s[28:31], s78 offen offset:0 nt\n"
    "v_mfma_f32_16x16x32_bf16 a[48:51], a[128:131], v[68:71], 0\n"
    "ds_read_b128 a[156:159], v248 offset:17408\n"
    "v_mfma_f32_16x16x32_bf16 v[164:167], a[132:135], v[88:91], v[164:167]\n"
    "buffer_load_dwordx2 v[230:231], v242, s[28:31], s78 offen offset:768 nt\n"
    "v_mfma_f32_16x16x32_bf16 v[124:127], a[112:115], v[68:71], 0\n"
    "v_mfma_f32_16x16x32_bf16 v[172:175], a[112:115], v[92:95], v[172:175]\n"
    "buffer_load_dwordx2 v[232:233], v242, s[28:31], s78 offen offset:3072 nt\n"
    "v_mfma_f32_16x16x32_bf16 a[32:35], a[148:151], v[96:99], a[32:35]\n"
    "v_mfma_f32_16x16x32_bf16 v[128:131], a[120:123], v[68:71], v[128:131]\n"
    "buffer_load_dwordx2 v[234:235], v242, s[28:31], s78 offen offset:3840 nt\n"
    "v_mfma_f32_16x16x32_bf16 v[192:195], a[152:155], v[68:71], 0\n"
    "ds_read_b128 a[132:135], v248 offset:20480\n"
    "ds_read_b128 a[112:115], v248 offset:6144\n"
    "v_mfma_f32_16x16x32_bf16 v[160:163], a[120:123], v[84:87], v[160:163]\n"
    "s_waitcnt lgkmcnt(2)\n"
    "v_mfma_f32_16x16x32_bf16 a[36:39], a[156:159], v[96:99], 0\n"
    "ds_read_b128 a[144:147], v248 offset:10240\n"
    "ds_read_b128 a[124:127], v248 offset:19456\n"
    "v_mfma_f32_16x16x32_bf16 v[32:35], a[72:75], a[168:171], v[32:35]\n"
    "v_exp_f32 v7, v7\n"
    "v_mfma_f32_16x16x32_bf16 v[192:195], a[156:159], v[72:75], v[192:195]\n"
    "v_exp_f32 v21, v21\n"
    "v_mfma_f32_16x16x32_bf16 v[184:187], a[136:139], v[92:95], v[184:187]\n"
    "ds_read_b128 a[120:123], v248 offset:7168\n"
    "v_mfma_f32_16x16x32_bf16 v[28:31], a[92:95], a[164:167], v[28:31]\n"
    "v_mfma_f32_16x16x32_bf16 v[168:171], a[140:143], v[88:91], v[168:171]\n"
    "ds_read_b128 a[148:151], v248 offset:22528\n"
    "ds_read_b128 a[136:139], v248 offset:9216\n"
    "v_mfma_f32_16x16x32_bf16 a[4:7], a[156:159], v[80:83], 0\n"
    "v_exp_f32 v33, v33\n"
    "v_mfma_f32_16x16x32_bf16 a[36:39], a[152:155], v[92:95], a[36:39]\n"
    "v_mfma_f32_16x16x32_bf16 v[124:127], a[116:119], v[72:75], v[124:127]\n"
    "v_exp_f32 v16, v16\n"
    "v_mfma_f32_16x16x32_bf16 a[4:7], a[152:155], v[76:79], a[4:7]\n"
    "v_exp_f32 v5, v5\n"
    "v_mfma_f32_16x16x32_bf16 v[136:139], a[140:143], v[72:75], v[136:139]\n"
    "ds_read_b128 a[140:143], v248 offset:21504\n"
    "ds_read_b128 a[152:155], v248 offset:11264\n"
    "v_mfma_f32_16x16x32_bf16 v[156:159], a[116:119], v[88:91], v[156:159]\n"
    "v_smfmac_f32_16x16x64_bf16 v[120:123], v[52:55], v[92:99], v253\n"
    "s_waitcnt lgkmcnt(8)\n"
    "v_mfma_f32_16x16x32_bf16 a[64:67], a[132:135], v[80:83], 0\n"
    "s_waitcnt lgkmcnt(6)\n"
    "v_mfma_f32_16x16x32_bf16 a[104:107], a[144:147], v[92:95], 0\n"
    "s_waitcnt lgkmcnt(2)\n"
    "v_exp_f32 v20, v20\n"
    "v_mfma_f32_16x16x32_bf16 a[52:55], a[136:139], v[68:71], 0\n"
    "ds_read_b128 a[116:119], v248 offset:18432\n"
    "v_mfma_f32_16x16x32_bf16 a[20:23], a[156:159], v[88:91], a[20:23]\n"
    "ds_read_b128 a[156:159], v248 offset:23552\n"
    "v_mfma_f32_16x16x32_bf16 v[196:199], a[112:115], v[68:71], 0\n"
    "buffer_load_dwordx2 v[236:237], v241, s[36:39], s81 offen offset:0 sc1\n"
    "v_mfma_f32_16x16x32_bf16 v[200:203], a[124:127], v[72:75], 0\n"
    "s_waitcnt lgkmcnt(2)\n"
    "v_exp_f32 v27, v27\n"
    "v_mfma_f32_16x16x32_bf16 a[60:63], a[152:155], v[68:71], 0\n"
    "v_mfma_f32_16x16x32_bf16 a[96:99], a[132:135], v[96:99], 0\n"
    "v_exp_f32 v17, v17\n"
    "v_mfma_f32_16x16x32_bf16 a[72:75], a[144:147], v[76:79], 0\n"
    "v_exp_f32 v24, v24\n"
    "v_mfma_f32_16x16x32_bf16 a[24:27], a[112:115], v[84:87], 0\n"
    "v_exp_f32 v35, v35\n"
    "v_mfma_f32_16x16x32_bf16 a[64:67], a[128:131], v[76:79], a[64:67]\n"
    "v_mfma_f32_16x16x32_bf16 a[80:83], a[128:131], v[84:87], 0\n"
    "s_add_u32 s67, s67, 49152\n"
    "v_exp_f32 v32, v32\n"
    "v_mfma_f32_16x16x32_bf16 a[12:15], a[120:123], v[76:79], 0\n"
    "v_mfma_f32_16x16x32_bf16 a[104:107], a[148:151], v[96:99], a[104:107]\n"
    "v_mov_b32 v2, v3\n"
    "v_mfma_f32_16x16x32_bf16 a[28:31], a[124:127], v[88:91], 0\n"
    "v_mfma_f32_16x16x32_bf16 a[28:31], a[120:123], v[84:87], a[28:31]\n"
    "v_mfma_f32_16x16x32_bf16 a[92:95], a[152:155], v[84:87], 0\n"
    "v_mfma_f32_16x16x32_bf16 v[200:203], a[120:123], v[68:71], v[200:203]\n"
    "s_waitcnt lgkmcnt(0)\n"
    "v_exp_f32 v12, v12\n"
    "v_mfma_f32_16x16x32_bf16 a[108:111], a[156:159], v[96:99], 0\n"
    "v_exp_f32 v18, v18\n"
    "v_mfma_f32_16x16x32_bf16 a[80:83], a[132:135], v[88:91], a[80:83]\n"
    "v_mfma_f32_16x16x32_bf16 a[44:47], a[120:123], v[92:95], 0\n"
    "v_exp_f32 v29, v29\n"
    "v_mfma_f32_16x16x32_bf16 a[12:15], a[124:127], v[80:83], a[12:15]\n"
    "ds_read_b128 a[120:123], v3 offset:16384\n"
    "v_mfma_f32_16x16x32_bf16 a[44:47], a[124:127], v[96:99], a[44:47]\n"
    "v_mfma_f32_16x16x32_bf16 a[56:59], a[148:151], v[72:75], 0\n"
    "v_exp_f32 v28, v28\n"
    "v_mfma_f32_16x16x32_bf16 a[96:99], a[128:131], v[92:95], a[96:99]\n"
    "v_exp_f32 v19, v19\n"
    "v_mfma_f32_16x16x32_bf16 a[40:43], a[116:119], v[96:99], 0\n"
    "v_exp_f32 v31, v31\n"
    "v_mfma_f32_16x16x32_bf16 a[60:63], a[156:159], v[72:75], a[60:63]\n"
    "v_exp_f32 v26, v26\n"
    "v_mfma_f32_16x16x32_bf16 a[72:75], a[148:151], v[80:83], a[72:75]\n"
    "v_mfma_f32_16x16x32_bf16 a[8:11], a[116:119], v[80:83], 0\n"
    "v_exp_f32 v6, v6\n"
    "v_mfma_f32_16x16x32_bf16 a[68:71], a[140:143], v[80:83], 0\n"
    "v_exp_f32 v25, v25\n"
    "v_mfma_f32_16x16x32_bf16 a[48:51], a[132:135], v[72:75], a[48:51]\n"
    "ds_read_b128 a[124:127], v3 offset:18432\n"
    "v_mfma_f32_16x16x32_bf16 a[40:43], a[112:115], v[92:95], a[40:43]\n"
    "ds_read_b128 a[132:135], v3 offset:22528\n"
    "v_mfma_f32_16x16x32_bf16 a[100:103], a[140:143], v[96:99], 0\n"
    "v_mfma_f32_16x16x32_bf16 v[196:199], a[116:119], v[72:75], v[196:199]\n"
    "v_exp_f32 v30, v30\n"
    "v_mfma_f32_16x16x32_bf16 a[108:111], a[152:155], v[92:95], a[108:111]\n"
    "v_mfma_f32_16x16x32_bf16 a[88:91], a[148:151], v[88:91], 0\n"
    "s_waitcnt vmcnt(17)\n"
    "v_exp_f32 v11, v11\n"
    "v_mfma_f32_16x16x32_bf16 a[8:11], a[112:115], v[76:79], a[8:11]\n"
    "ds_read_b128 a[128:131], v3 offset:20480\n"
    "ds_read_b128 a[112:115], v3 offset:12288\n"
    "v_mfma_f32_16x16x32_bf16 a[76:79], a[156:159], v[80:83], 0\n"
    "ds_add_u32 v247, v246\n"
    "v_mfma_f32_16x16x32_bf16 a[24:27], a[116:119], v[88:91], a[24:27]\n"
    "ds_read_b128 a[116:119], v3 offset:14336\n"
    "v_mfma_f32_16x16x32_bf16 a[56:59], a[144:147], v[68:71], a[56:59]\n"
    "v_exp_f32 v9, v9\n"
    "v_mfma_f32_16x16x32_bf16 a[84:87], a[136:139], v[84:87], 0\n"
    "v_exp_f32 v23, v23\n"
    "v_mfma_f32_16x16x32_bf16 a[88:91], a[144:147], v[84:87], a[88:91]\n"
    "v_add_u32 v3, 49152, v252\n"
    "v_exp_f32 v34, v34\n"
    "v_mfma_f32_16x16x32_bf16 a[84:87], a[140:143], v[88:91], a[84:87]\n"
    "v_mfma_f32_16x16x32_bf16 a[100:103], a[136:139], v[92:95], a[100:103]\n"
    "v_exp_f32 v13, v13\n"
    "v_mfma_f32_16x16x32_bf16 a[76:79], a[152:155], v[76:79], a[76:79]\n"
    "v_mfma_f32_16x16x32_bf16 a[68:71], a[136:139], v[76:79], a[68:71]\n"
    "s_waitcnt lgkmcnt(0)\n"
    "v_mfma_f32_16x16x32_bf16 a[92:95], a[156:159], v[88:91], a[92:95]\n"
    "v_mfma_f32_16x16x32_bf16 a[52:55], a[140:143], v[72:75], a[52:55]\n"
    "s_branch .LNK\n"
    ".fill ((0x20000 - (. - _Z8attn_fwdPKtPKiS2_S0_S0_S0_S0_PcPt)) & 4095) / 4, 4, 0xBF800000\n"
    ".LhKN:\n"
    "s_waitcnt vmcnt(28)\n"
    "ds_read_b128 v[52:55], v251 offset:768\n"
    "ds_read_b128 v[180:183], v96 offset:49152\n"
    "ds_read_b128 v[200:203], v97 offset:49408\n"
    "ds_read_b128 v[184:187], v97 offset:49152\n"
    "s_add_u32 m0, s68, 0\n"
    "ds_read_b128 v[196:199], v96 offset:49408\n"
    "ds_read_b128 v[188:191], v96 offset:49280\n"
    "ds_read_b128 v[192:195], v97 offset:49280\n"
    "s_waitcnt lgkmcnt(6)\n"
    "v_smfmac_f32_16x16x64_bf16 v[108:111], v[52:55], v[68:75], v253\n"
    "s_waitcnt lgkmcnt(4)\n"
    "v_pk_maximum3_f16 v98, v200, v201, v98\n"
    "v_pk_maximum3_f16 v98, v202, v203, v98\n"
    "v_pk_add_u16 v200, s89, v200 clamp\n"
    "v_pk_add_u16 v203, s89, v203 clamp\n"
    "v_smfmac_f32_16x16x64_bf16 v[116:119], v[52:55], v[84:91], v253\n"
    "s_waitcnt lgkmcnt(2)\n"
    "s_nop 0\n"
    "v_pk_add_u16 v202, s89, v202 clamp\n"
    "v_pk_add_u16 v201, s89, v201 clamp\n"
    "v_pk_maximum3_f16 v98, v196, v197, v98\n"
    "v_pk_maximum3_f16 v98, v198, v199, v98\n"
    "v_mfma_f32_16x16x32_bf16 v[64:67], a[44:47], v[200:203], v[64:67]\n"
    "v_pk_add_u16 v198, s89, v198 clamp\n"
    "v_pk_add_u16 v196, s89, v196 clamp\n"
    "v_mfma_f32_16x16x32_bf16 v[48:51], a[20:23], v[200:203], v[48:51]\n"
    "v_pk_add_u16 v199, s89, v199 clamp\n"
    "v_pk_add_u16 v197, s89, v197 clamp\n"
    "v_mfma_f32_16x16x32_bf16 v[32:35], a[156:159], v[200:203], v[32:35]\n"
    "s_waitcnt lgkmcnt(0)\n"
    "v_pk_maximum3_f16 v98, v192, v193, v98\n"
    "v_pk_maximum3_f16 v98, v194, v195, v98\n"
    "v_mfma_f32_16x16x32_bf16 v[64:67], a[40:43], v[196:199], v[64:67]\n"
    "v_pk_maximum3_f16 v98, v188, v189, v98\n"
    "v_pk_add_u16 v193, s89, v193 clamp\n"
    "v_mfma_f32_16x16x32_bf16 v[48:51], a[16:19], v[196:199], v[48:51]\n"
    "buffer_load_dwordx4 v1, s[16:19], s67 offen offset:0 sc1 lds\n"
    "v_mfma_f32_16x16x32_bf16 v[16:19], a[132:135], v[200:203], v[16:19]\n"
    "v_pk_add_u16 v194, s89, v194 clamp\n"
    "v_pk_maximum3_f16 v98, v190, v191, v98\n"
    "v_pk_add_u16 v192, s89, v192 clamp\n"
    "v_smfmac_f32_16x16x64_bf16 v[112:115], v[52:55], v[76:83], v253\n"
    "v_pk_add_u16 v191, s89, v191 clamp\n"
    "v_mfma_f32_16x16x32_bf16 v[16:19], a[128:131], v[196:199], v[16:19]\n"
    "v_pk_add_u16 v195, s89, v195 clamp\n"
    "v_pk_maximum3_f16 v98, v184, v185, v98\n"
    "v_mfma_f32_16x16x32_bf16 v[32:35], a[152:155], v[196:199], v[32:35]\n"
    "v_pk_add_u16 v185, s89, v185 clamp\n"
    "v_pk_add_u16 v188, s89, v188 clamp\n"
    "v_mfma_f32_16x16x32_bf16 v[64:67], a[36:39], v[192:195], v[64:67]\n"
    "v_pk_add_u16 v189, s89, v189 clamp\n"
    "v_pk_maximum3_f16 v98, v186, v187, v98\n"
    "v_mfma_f32_16x16x32_bf16 v[32:35], a[148:151], v[192:195], v[32:35]\n"
    "buffer_load_dwordx4 v1, s[16:19], s67 offen offset:1024 sc1 lds\n"
    "v_mfma_f32_16x16x32_bf16 v[16:19], a[124:127], v[192:195], v[16:19]\n"
    "v_pk_add_u16 v190, s89, v190 clamp\n"
    "v_pk_add_u16 v184, s89, v184 clamp\n"
    "v_pk_maximum3_f16 v98, v180, v181, v98\n"
    "v_mfma_f32_16x16x32_bf16 v[32:35], a[144:147], v[188:191], v[32:35]\n"
    "v_pk_add_u16 v187, s89, v187 clamp\n"
    "v_pk_add_u16 v186, s89, v186 clamp\n"
    "v_mfma_f32_16x16x32_bf16 v[48:51], a[12:15], v[192:195], v[48:51]\n"
    "s_waitcnt vmcnt(24)\n"
    "v_pk_maximum3_f16 v98, v182, v183, v98\n"
    "v_pk_add_u16 v182, s89, v182 clamp\n"
    "v_mfma_f32_16x16x32_bf16 v[48:51], a[8:11], v[188:191], v[48:51]\n"
    "s_barrier\n"
    "v_pk_add_u16 v180, s89, v180 clamp\n"
    "v_mfma_f32_16x16x32_bf16 v[64:67], a[32:35], v[188:191], v[64:67]\n"
    "ds_read_b128 a[72:75], v252 offset:25600\n"
    "v_pk_add_u16 v183, s89, v183 clamp\n"
    "v_mfma_f32_16x16x32_bf16 v[32:35], a[140:143], v[184:187], v[32:35]\n"
    "ds_read_b128 a[48:51], v252 offset:24576\n"
    "v_pk_add_u16 v181, s89, v181 clamp\n"
    "v_mfma_f32_16x16x32_bf16 v[16:19], a[120:123], v[188:191], v[16:19]\n"
    "buffer_load_dwordx4 v1, s[16:19], s67 offen offset:2048 sc1 lds\n"
    "v_mfma_f32_16x16x32_bf16 v[32:35], a[136:139], v[180:183], v[32:35]\n"
    "v_accvgpr_write_b32 a187, v199\n"
    "v_accvgpr_write_b32 a186, v198\n"
    "v_mfma_f32_16x16x32_bf16 v[64:67], a[28:31], v[184:187], v[64:67]\n"
    "ds_read_b128 a[92:95], v252 offset:35840\n"
    "ds_read_b128 a[64:67], v252 offset:32768\n"
    "v_mfma_f32_16x16x32_bf16 v[48:51], a[4:7], v[184:187], v[48:51]\n"
    "s_add_u32 s83, s67, 4096\n"
    "ds_read_b128 a[68:71], v252 offset:34816\n"
    "ds_read_b128 a[88:91], v252 offset:33792\n"
    "v_mfma_f32_16x16x32_bf16 v[16:19], a[116:119], v[184:187], v[16:19]\n"
    "v_accvgpr_write_b32 a178, v190\n"
    "v_accvgpr_write_b32 a173, v185\n"
    "v_mfma_f32_16x16x32_bf16 v[64:67], a[24:27], v[180:183], v[64:67]\n"
    "ds_read_b128 a[52:55], v252 offset:26624\n"
    "ds_read_b128 a[56:59], v252 offset:28672\n"
    "v_mfma_f32_16x16x32_bf16 v[48:51], a[0:3], v[180:183], v[48:51]\n"
    "ds_read_b128 a[84:87], v252 offset:31744\n"
    "v_accvgpr_write_b32 a177, v189\n"
    "v_mfma_f32_16x16x32_bf16 v[16:19], a[112:115], v[180:183], v[16:19]\n"
    "s_waitcnt lgkmcnt(8)\n"
    "buffer_load_dwordx4 v1, s[16:19], s67 offen offset:3072 sc1 lds\n"
    "v_mfma_f32_16x16x32_bf16 v[28:31], a[72:75], a[240:243], v[104:107]\n"
    "s_waitcnt lgkmcnt(7)\n"
    "ds_read_b128 a[60:63], v252 offset:30720\n"
    "v_accvgpr_write_b32 a170, v182\n"
    "v_mfma_f32_16x16x32_bf16 v[4:7], a[48:51], a[192:195], v[100:103]\n"
    "ds_read_b128 a[76:79], v252 offset:27648\n"
    "ds_read_b128 a[80:83], v252 offset:29696\n"
    "v_mfma_f32_16x16x32_bf16 v[12:15], a[48:51], a[240:243], v[100:103]\n"
    "s_waitcnt lgkmcnt(9)\n"
    "v_exp_f32 v16, v16\n"
    "v_mfma_f32_16x16x32_bf16 v[24:27], a[92:95], a[236:239], v[104:107]\n"
    "s_waitcnt lgkmcnt(6)\n"
    "v_accvgpr_write_b32 a183, v195\n"
    "v_accvgpr_write_b32 a190, v202\n"
    "v_mfma_f32_16x16x32_bf16 v[24:27], a[88:91], a[232:235], v[24:27]\n"
    "v_exp_f32 v17, v17\n"
    "v_mfma_f32_16x16x32_bf16 v[20:23], a[72:75], a[192:195], v[104:107]\n"
    "s_waitcnt lgkmcnt(5)\n"
    "v_accvgpr_write_b32 a172, v184\n"
    "v_accvgpr_write_b32 a168, v180\n"
    "v_mfma_f32_16x16x32_bf16 v[4:7], a[52:55], a[196:199], v[4:7]\n"
    "s_add_u32 m0, s68, 4096\n"
    "v_exp_f32 v34, v34\n"
    "v_mfma_f32_16x16x32_bf16 v[12:15], a[52:55], a[244:247], v[12:15]\n"
    "buffer_load_dwordx4 v1, s[16:19], s83 offen offset:0 sc1 lds\n"
    "v_mfma_f32_16x16x32_bf16 v[8:11], a[68:71], a[236:239], v[100:103]\n"
    "s_waitcnt lgkmcnt(4)\n"
    "v_accvgpr_write_b32 a182, v194\n"
    "v_accvgpr_write_b32 a189, v201\n"
    "v_mfma_f32_16x16x32_bf16 v[4:7], a[56:59], a[200:203], v[4:7]\n"
    "s_waitcnt lgkmcnt(2)\n"
    "v_accvgpr_write_b32 a175, v187\n"
    "v_accvgpr_write_b32 a180, v192\n"
    "v_mfma_f32_16x16x32_bf16 v[4:7], a[60:63], a[204:207], v[4:7]\n"
    "s_waitcnt lgkmcnt(1)\n"
    "v_accvgpr_write_b32 a176, v188\n"
    "v_pk_add_u16 v98, s89, v98\n"
    "v_mfma_f32_16x16x32_bf16 v[28:31], a[76:79], a[244:247], v[28:31]\n"
    "v_accvgpr_write_b32 a188, v200\n"
    "v_accvgpr_write_b32 a191, v203\n"
    "v_mfma_f32_16x16x32_bf16 v[20:23], a[76:79], a[196:199], v[20:23]\n"
    "s_waitcnt lgkmcnt(0)\n"
    "v_accvgpr_write_b32 a184, v196\n"
    "v_mfma_f32_16x16x32_bf16 v[20:23], a[80:83], a[200:203], v[20:23]\n"
    "v_accvgpr_write_b32 a181, v193\n"
    "v_mfma_f32_16x16x32_bf16 v[12:15], a[56:59], a[248:251], v[12:15]\n"
    "buffer_load_dwordx4 v1, s[16:19], s83 offen offset:1024 sc1 lds\n"
    "v_mfma_f32_16x16x32_bf16 v[20:23], a[84:87], a[204:207], v[20:23]\n"
    "v_accvgpr_write_b32 a171, v183\n"
    "v_accvgpr_write_b32 a185, v197\n"
    "v_mfma_f32_16x16x32_bf16 v[4:7], a[64:67], a[208:211], v[4:7]\n"
    "s_add_u32 m0, s70, 0\n"
    "v_accvgpr_write_b32 a179, v191\n"
    "v_accvgpr_write_b32 a169, v181\n"
    "v_mfma_f32_16x16x32_bf16 v[4:7], a[68:71], a[212:215], v[4:7]\n"
    "s_add_u32 s83, s67, 24576\n"
    "v_mfma_f32_16x16x32_bf16 v[28:31], a[80:83], a[248:251], v[28:31]\n"
    "v_exp_f32 v19, v19\n"
    "v_mfma_f32_16x16x32_bf16 v[8:11], a[64:67], a[232:235], v[8:11]\n"
    "v_accvgpr_write_b32 a174, v186\n"
    "v_mfma_f32_16x16x32_bf16 v[24:27], a[84:87], a[228:231], v[24:27]\n"
    "v_exp_f32 v33, v33\n"
    "v_mfma_f32_16x16x32_bf16 v[20:23], a[88:91], a[208:211], v[20:23]\n"
    "buffer_load_dwordx4 v1, s[16:19], s83 offen offset:0 sc1 lds\n"
    "v_mfma_f32_16x16x32_bf16 v[28:31], a[84:87], a[252:255], v[28:31]\n"
    "v_exp_f32 v32, v32\n"
    "v_mfma_f32_16x16x32_bf16 v[28:31], a[88:91], a[160:163], v[28:31]\n"
    "v_exp_f32 v18, v18\n"
    "v_mfma_f32_16x16x32_bf16 v[24:27], a[80:83], a[224:227], v[24:27]\n"
    "v_exp_f32 v49, v49\n"
    "v_mfma_f32_16x16x32_bf16 v[8:11], a[60:63], a[228:231], v[8:11]\n"
    "v_exp_f32 v35, v35\n"
    "v_mfma_f32_16x16x32_bf16 v[24:27], a[76:79], a[220:223], v[24:27]\n"
    "v_cvt_pk_bf16_f32 v92, v16, v17\n"
    "v_cvt_pk_bf16_f32 v93, v18, v19\n"
    "v_mfma_f32_16x16x32_bf16 v[12:15], a[60:63], a[252:255], v[12:15]\n"
    "v_and_b32 v98, 0x80008000, v98\n"
    "v_cvt_pk_bf16_f32 v94, v32, v33\n"
    "v_mfma_f32_16x16x32_bf16 v[24:27], a[72:75], a[216:219], v[24:27]\n"
    "buffer_load_dwordx4 v1, s[16:19], s83 offen offset:1024 sc1 lds\n"
    "v_mfma_f32_16x16x32_bf16 v[16:19], a[68:71], a[188:191], v[100:103]\n"
    "v_cvt_pk_bf16_f32 v95, v34, v35\n"
    "v_mfma_f32_16x16x32_bf16 v[16:19], a[64:67], a[184:187], v[16:19]\n"
    "s_waitcnt vmcnt(24)\n"
    "s_barrier\n"
    "v_mfma_f32_16x16x32_bf16 v[8:11], a[56:59], a[224:227], v[8:11]\n"
    "ds_read_b128 a[124:127], v249 offset:13312\n"
    "ds_read_b128 a[120:123], v249 offset:1024\n"
    "v_mfma_f32_16x16x32_bf16 v[32:35], a[92:95], a[188:191], v[104:107]\n"
    "ds_read_b128 a[116:119], v249 offset:12288\n"
    "ds_read_b128 a[112:115], v249 offset:0\n"
    "v_mfma_f32_16x16x32_bf16 v[32:35], a[88:91], a[184:187], v[32:35]\n"
    "ds_read_b128 a[132:135], v249 offset:14336\n"
    "ds_read_b128 a[144:147], v249 offset:4096\n"
    "v_mfma_f32_16x16x32_bf16 v[8:11], a[52:55], a[220:223], v[8:11]\n"
    "s_add_u32 s76, s71, s60\n"
    "v_exp_f32 v51, v51\n"
    "v_mfma_f32_16x16x32_bf16 v[12:15], a[64:67], a[160:163], v[12:15]\n"
    "buffer_load_dwordx4 v1, s[16:19], s83 offen offset:2048 sc1 lds\n"
    "v_mfma_f32_16x16x32_bf16 v[28:31], a[92:95], a[164:167], v[28:31]\n"
    "v_exp_f32 v66, v66\n"
    "v_mfma_f32_16x16x32_bf16 v[16:19], a[60:63], a[180:183], v[16:19]\n"
    "v_exp_f32 v50, v50\n"
    "v_mfma_f32_16x16x32_bf16 v[12:15], a[68:71], a[164:167], v[12:15]\n"
    "v_exp_f32 v67, v67\n"
    "v_mfma_f32_16x16x32_bf16 v[8:11], a[48:51], a[216:219], v[8:11]\n"
    "s_add_u32 s77, s71, s61\n"
    "ds_read_b128 a[156:159], v249 offset:17408\n"
    "s_waitcnt lgkmcnt(6)\n"
    "v_mfma_f32_16x16x32_bf16 v[128:131], a[124:127], v[72:75], 0\n"
    "ds_read_b128 a[136:139], v249 offset:3072\n"
    "v_mfma_f32_16x16x32_bf16 v[160:163], a[124:127], v[88:91], 0\n"
    "s_waitcnt lgkmcnt(4)\n"
    "ds_read_b128 a[148:151], v249 offset:16384\n"
    "ds_read_b128 a[128:131], v249 offset:2048\n"
    "v_mfma_f32_16x16x32_bf16 v[156:159], a[112:115], v[84:87], 0\n"
    "buffer_load_dwordx4 v1, s[16:19], s83 offen offset:3072 sc1 lds\n"
    "v_mfma_f32_16x16x32_bf16 v[16:19], a[56:59], a[176:179], v[16:19]\n"
    "s_add_u32 s83, s67, 28672\n"
    "ds_read_b128 a[152:155], v249 offset:5120\n"
    "v_mfma_f32_16x16x32_bf16 v[16:19], a[52:55], a[172:175], v[16:19]\n"
    "s_add_u32 m0, s70, 4096\n"
    "v_exp_f32 v7, v7\n"
    "v_mfma_f32_16x16x32_bf16 v[20:23], a[92:95], a[212:215], v[20:23]\n"
    "v_mfma_f32_16x16x32_bf16 v[156:159], a[116:119], v[88:91], v[156:159]\n"
    "v_exp_f32 v48, v48\n"
    "v_mfma_f32_16x16x32_bf16 v[32:35], a[84:87], a[180:183], v[32:35]\n"
    "s_waitcnt lgkmcnt(6)\n"
    "v_exp_f32 v65, v65\n"
    "v_mfma_f32_16x16x32_bf16 v[148:151], a[132:135], v[80:83], 0\n"
    "ds_read_b128 v[36:39], v243 offset:0\n"
    "v_cmp_ne_u32 s[34:35], 0, v98\n"
    "v_mfma_f32_16x16x32_bf16 v[124:127], a[112:115], v[68:71], 0\n"
    "buffer_load_dwordx4 v1, s[16:19], s83 offen offset:0 sc1 lds\n"
    "v_mfma_f32_16x16x32_bf16 v[176:179], a[120:123], v[92:95], 0\n"
    "s_waitcnt lgkmcnt(3)\n"
    "v_exp_f32 v10, v10\n"
    "v_mfma_f32_16x16x32_bf16 a[16:19], a[148:151], v[88:91], 0\n"
    "v_exp_f32 v64, v64\n"
    "v_mfma_f32_16x16x32_bf16 v[136:139], a[136:139], v[68:71], 0\n"
    "v_exp_f32 v4, v4\n"
    "v_mfma_f32_16x16x32_bf16 v[32:35], a[80:83], a[176:179], v[32:35]\n"
    "ds_read_b128 a[140:143], v249 offset:15360\n"
    "v_add_u32 v251, 1536, v251\n"
    "v_mfma_f32_16x16x32_bf16 v[140:143], a[116:119], v[80:83], 0\n"
    "v_cvt_pk_bf16_f32 v97, v50, v51\n"
    "v_mfma_f32_16x16x32_bf16 v[16:19], a[48:51], a[168:171], v[16:19]\n"
    "s_waitcnt lgkmcnt(3)\n"
    "v_cvt_pk_bf16_f32 v98, v64, v65\n"
    "v_cvt_pk_bf16_f32 v99, v66, v67\n"
    "v_mfma_f32_16x16x32_bf16 v[164:167], a[128:131], v[84:87], 0\n"
    "buffer_load_dwordx4 v1, s[16:19], s83 offen offset:1024 sc1 lds\n"
    "v_mfma_f32_16x16x32_bf16 a[4:7], a[156:159], v[80:83], 0\n"
    "v_exp_f32 v27, v27\n"
    "v_mfma_f32_16x16x32_bf16 v[128:131], a[120:123], v[68:71], v[128:131]\n"
    "s_waitcnt lgkmcnt(2)\n"
    "v_mfma_f32_16x16x32_bf16 v[192:195], a[152:155], v[68:71], 0\n"
    "v_exp_f32 v26, v26\n"
    "v_mfma_f32_16x16x32_bf16 a[32:35], a[144:147], v[92:95], 0\n"
    "ds_read_b128 v[100:103], v250 offset:1664\n"
    "s_waitcnt lgkmcnt(1)\n"
    "v_mfma_f32_16x16x32_bf16 v[152:155], a[140:143], v[80:83], 0\n"
    "v_exp_f32 v22, v22\n"
    "v_mfma_f32_16x16x32_bf16 v[32:35], a[76:79], a[172:175], v[32:35]\n"
    "v_cvt_pk_bf16_f32 v96, v48, v49\n"
    "v_mfma_f32_16x16x32_bf16 v[144:147], a[120:123], v[76:79], 0\n"
    "s_waitcnt vmcnt(12)\n"
    "v_fma_f32 v216, v216, v36, -v38\n"
    "v_fma_f32 v229, v229, v37, -v39\n"
    "v_mfma_f32_16x16x32_bf16 v[160:163], a[120:123], v[84:87], v[160:163]\n"
    "v_fma_f32 v206, v206, v36, -v38\n"
    "v_fma_f32 v213, v213, v37, -v39\n"
    "v_mfma_f32_16x16x32_bf16 v[172:175], a[116:119], v[96:99], 0\n"
    "v_fma_f32 v214, v214, v36, -v38\n"
    "v_mfma_f32_16x16x32_bf16 v[124:127], a[116:119], v[72:75], v[124:127]\n"
    "v_fma_f32 v207, v207, v37, -v39\n"
    "v_fma_f32 v205, v205, v37, -v39\n"
    "v_mfma_f32_16x16x32_bf16 v[176:179], a[124:127], v[96:99], v[176:179]\n"
    "ds_read_b128 a[116:119], v249 offset:18432\n"
    "v_fma_f32 v209, v209, v37, -v39\n"
    "v_mfma_f32_16x16x32_bf16 v[144:147], a[124:127], v[80:83], v[144:147]\n"
    "v_fma_f32 v215, v215, v37, -v39\n"
    "v_fma_f32 v217, v217, v37, -v39\n"
    "v_mfma_f32_16x16x32_bf16 a[4:7], a[152:155], v[76:79], a[4:7]\n"
    "v_exp_f32 v13, v13\n"
    "v_mfma_f32_16x16x32_bf16 v[140:143], a[112:115], v[76:79], v[140:143]\n"
    "v_fma_f32 v212, v212, v36, -v38\n"
    "v_mfma_f32_16x16x32_bf16 v[184:187], a[140:143], v[96:99], 0\n"
    "v_cvt_pk_bf16_f32 v216, v216, v217\n"
    "v_fma_f32 v223, v223, v37, -v39\n"
    "v_mfma_f32_16x16x32_bf16 v[164:167], a[132:135], v[88:91], v[164:167]\n"
    "v_fma_f32 v222, v222, v36, -v38\n"
    "v_fma_f32 v231, v231, v37, -v39\n"
    "v_mfma_f32_16x16x32_bf16 v[188:191], a[148:151], v[72:75], 0\n"
    "v_exp_f32 v21, v21\n"
    "v_mfma_f32_16x16x32_bf16 v[172:175], a[112:115], v[92:95], v[172:175]\n"
    "v_fma_f32 v235, v235, v37, -v39\n"
    "v_fma_f32 v211, v211, v37, -v39\n"
    "v_mfma_f32_16x16x32_bf16 v[132:135], a[128:131], v[68:71], 0\n"
    "ds_read_b128 a[112:115], v249 offset:6144\n"
    "v_mfma_f32_16x16x32_bf16 a[32:35], a[148:151], v[96:99], a[32:35]\n"
    "v_exp_f32 v5, v5\n"
    "v_mfma_f32_16x16x32_bf16 v[180:183], a[132:135], v[96:99], 0\n"
    "ds_read_b128 a[120:123], v249 offset:7168\n"
    "v_cvt_pk_bf16_f32 v206, v206, v207\n"
    "v_mfma_f32_16x16x32_bf16 v[132:135], a[132:135], v[72:75], v[132:135]\n"
    "ds_read_b128 a[124:127], v249 offset:19456\n"
    "v_fma_f32 v221, v221, v37, -v39\n"
    "v_mfma_f32_16x16x32_bf16 v[180:183], a[128:131], v[92:95], v[180:183]\n"
    "ds_read_b128 a[132:135], v249 offset:20480\n"
    "v_fma_f32 v233, v233, v37, -v39\n"
    "v_mfma_f32_16x16x32_bf16 a[0:3], a[144:147], v[76:79], 0\n"
    "v_exp_f32 v12, v12\n"
    "v_mfma_f32_16x16x32_bf16 a[0:3], a[148:151], v[80:83], a[0:3]\n"
    "s_waitcnt lgkmcnt(4)\n"
    "v_fma_f32 v204, v204, v36, -v38\n"
    "v_fma_f32 v218, v218, v36, -v38\n"
    "v_mfma_f32_16x16x32_bf16 a[8:11], a[116:119], v[80:83], 0\n"
    "v_cvt_pk_bf16_f32 v212, v212, v213\n"
    "v_fma_f32 v219, v219, v37, -v39\n"
    "v_mfma_f32_16x16x32_bf16 a[20:23], a[152:155], v[84:87], 0\n"
    "v_cvt_pk_bf16_f32 v204, v204, v205\n"
    "v_fma_f32 v220, v220, v36, -v38\n"
    "v_mfma_f32_16x16x32_bf16 a[36:39], a[156:159], v[96:99], 0\n"
    "v_mfma_f32_16x16x32_bf16 a[16:19], a[144:147], v[84:87], a[16:19]\n"
    "s_waitcnt lgkmcnt(2)\n"
    "buffer_store_dword v204, v244, s[16:19], s76 offen offset:0 sc1\n"
    "v_exp_f32 v16, v16\n"
    "v_mfma_f32_16x16x32_bf16 a[12:15], a[120:123], v[76:79], 0\n"
    "v_mfma_f32_16x16x32_bf16 v[32:35], a[72:75], a[168:171], v[32:35]\n"
    "v_fma_f32 v234, v234, v36, -v38\n"
    "v_fma_f32 v208, v208, v36, -v38\n"
    "v_mfma_f32_16x16x32_bf16 a[36:39], a[152:155], v[92:95], a[36:39]\n"
    "buffer_store_dword v206, v244, s[16:19], s76 offen offset:128 sc1\n"
    "v_fma_f32 v225, v225, v37, -v39\n"
    "v_fma_f32 v224, v224, v36, -v38\n"
    "v_mfma_f32_16x16x32_bf16 v[168:171], a[136:139], v[84:87], 0\n"
    "s_waitcnt lgkmcnt(1)\n"
    "v_cvt_pk_bf16_f32 v208, v208, v209\n"
    "v_fma_f32 v230, v230, v36, -v38\n"
    "v_mfma_f32_16x16x32_bf16 v[200:203], a[124:127], v[72:75], 0\n"
    "v_fma_f32 v226, v226, v36, -v38\n"
    "v_fma_f32 v210, v210, v36, -v38\n"
    "v_mfma_f32_16x16x32_bf16 a[44:47], a[120:123], v[92:95], 0\n"
    "buffer_store_dword v208, v244, s[16:19], s76 offen offset:1024 sc1\n"
    "v_smfmac_f32_16x16x64_bf16 v[120:123], v[52:55], v[92:99], v253\n"
    "v_cvt_pk_bf16_f32 v210, v210, v211\n"
    "v_mfma_f32_16x16x32_bf16 v[188:191], a[144:147], v[68:71], v[188:191]\n"
    "buffer_store_dword v210, v244, s[16:19], s76 offen offset:1152 sc1\n"
    "v_mfma_f32_16x16x32_bf16 v[148:151], a[128:131], v[76:79], v[148:151]\n"
    "v_cvt_pk_bf16_f32 v214, v214, v215\n"
    "v_fma_f32 v227, v227, v37, -v39\n"
    "v_mfma_f32_16x16x32_bf16 a[8:11], a[112:115], v[76:79], a[8:11]\n"
    "ds_read_b128 a[144:147], v249 offset:10240\n"
    "ds_read_b128 a[128:131], v249 offset:8192\n"
    "v_mfma_f32_16x16x32_bf16 a[28:31], a[124:127], v[88:91], 0\n"
    "v_exp_f32 v24, v24\n"
    "v_mfma_f32_16x16x32_bf16 a[24:27], a[112:115], v[84:87], 0\n"
    "v_mov_b32 v2, v3\n"
    "v_cvt_pk_bf16_f32 v222, v222, v223\n"
    "v_mfma_f32_16x16x32_bf16 a[40:43], a[116:119], v[96:99], 0\n"
    "ds_read_b128 a[148:151], v249 offset:22528\n"
    "v_fma_f32 v232, v232, v36, -v38\n"
    "v_mfma_f32_16x16x32_bf16 v[200:203], a[120:123], v[68:71], v[200:203]\n"
    "buffer_store_dword v212, v244, s[16:19], s76 offen offset:256 sc1\n"
    "v_cvt_pk_bf16_f32 v224, v224, v225\n"
    "v_mfma_f32_16x16x32_bf16 v[152:155], a[136:139], v[76:79], v[152:155]\n"
    "s_waitcnt lgkmcnt(3)\n"
    "v_exp_f32 v14, v14\n"
    "v_mfma_f32_16x16x32_bf16 a[96:99], a[132:135], v[96:99], 0\n"
    "v_exp_f32 v31, v31\n"
    "v_mfma_f32_16x16x32_bf16 v[196:199], a[112:115], v[68:71], 0\n"
    "s_waitcnt lgkmcnt(2)\n"
    "buffer_store_dword v214, v244, s[16:19], s76 offen offset:384 sc1\n"
    "v_exp_f32 v6, v6\n"
    "v_mfma_f32_16x16x32_bf16 a[104:107], a[144:147], v[92:95], 0\n"
    "v_exp_f32 v18, v18\n"
    "v_mfma_f32_16x16x32_bf16 a[28:31], a[120:123], v[84:87], a[28:31]\n"
    "v_exp_f32 v33, v33\n"
    "v_mfma_f32_16x16x32_bf16 a[64:67], a[132:135], v[80:83], 0\n"
    "v_cvt_pk_bf16_f32 v230, v230, v231\n"
    "v_mfma_f32_16x16x32_bf16 v[168:171], a[140:143], v[88:91], v[168:171]\n"
    "s_waitcnt lgkmcnt(1)\n"
    "buffer_load_dwordx2 v[236:237], v241, s[36:39], s81 offen offset:0 sc1\n"
    "v_mfma_f32_16x16x32_bf16 a[48:51], a[128:131], v[68:71], 0\n"
    "v_exp_f32 v19, v19\n"
    "v_mfma_f32_16x16x32_bf16 v[136:139], a[140:143], v[72:75], v[136:139]\n"
    "s_waitcnt lgkmcnt(0)\n"
    "buffer_store_dword v216, v244, s[16:19], s76 offen offset:1280 sc1\n"
    "v_mfma_f32_16x16x32_bf16 a[104:107], a[148:151], v[96:99], a[104:107]\n"
    "v_exp_f32 v29, v29\n"
    "v_mfma_f32_16x16x32_bf16 a[88:91], a[148:151], v[88:91], 0\n"
    "v_fma_f32 v228, v228, v36, -v38\n"
    "v_cvt_pk_bf16_f32 v226, v226, v227\n"
    "v_mfma_f32_16x16x32_bf16 v[196:199], a[116:119], v[72:75], v[196:199]\n"
    "v_cvt_pk_bf16_f32 v218, v218, v219\n"
    "v_cvt_pk_bf16_f32 v220, v220, v221\n"
    "v_mfma_f32_16x16x32_bf16 v[184:187], a[136:139], v[92:95], v[184:187]\n"
    "buffer_store_dword v218, v244, s[16:19], s76 offen offset:1408 sc1\n"
    "ds_read_b128 a[140:143], v249 offset:21504\n"
    "v_mfma_f32_16x16x32_bf16 a[24:27], a[116:119], v[88:91], a[24:27]\n"
    "v_exp_f32 v32, v32\n"
    "v_mfma_f32_16x16x32_bf16 v[192:195], a[156:159], v[72:75], v[192:195]\n"
    "ds_read_b128 a[152:155], v249 offset:11264\n"
    "v_cvt_pk_bf16_f32 v232, v232, v233\n"
    "v_mfma_f32_16x16x32_bf16 a[20:23], a[156:159], v[88:91], a[20:23]\n"
    "v_exp_f32 v9, v9\n"
    "v_mfma_f32_16x16x32_bf16 a[80:83], a[128:131], v[84:87], 0\n"
    "v_exp_f32 v11, v11\n"
    "v_mfma_f32_16x16x32_bf16 a[12:15], a[124:127], v[80:83], a[12:15]\n"
    "buffer_store_dword v220, v244, s[16:19], s77 offen offset:0 sc1\n"
    "ds_read_b128 a[156:159], v249 offset:23552\n"
    "v_mfma_f32_16x16x32_bf16 a[72:75], a[144:147], v[76:79], 0\n"
    "v_exp_f32 v28, v28\n"
    "v_mfma_f32_16x16x32_bf16 a[64:67], a[128:131], v[76:79], a[64:67]\n"
    "buffer_store_dword v222, v244, s[16:19], s77 offen offset:128 sc1\n"
    "v_mfma_f32_16x16x32_bf16 a[80:83], a[132:135], v[88:91], a[80:83]\n"
    "v_mfma_f32_16x16x32_bf16 a[88:91], a[144:147], v[84:87], a[88:91]\n"
    "v_exp_f32_e64 v30, v30\n"
    "v_mfma_f32_16x16x32_bf16 a[56:59], a[148:151], v[72:75], 0\n"
    "v_add_u32 v243, 512, v243\n"
    "v_cvt_pk_bf16_f32 v228, v228, v229\n"
    "v_mfma_f32_16x16x32_bf16 a[56:59], a[144:147], v[68:71], a[56:59]\n"
    "s_add_u32 s67, s67, 49152\n"
    "buffer_store_dword v224, v244, s[16:19], s77 offen offset:1024 sc1\n"
    "v_mfma_f32_16x16x32_bf16 a[48:51], a[132:135], v[72:75], a[48:51]\n"
    "v_exp_f32 v20, v20\n"
    "v_mfma_f32_16x16x32_bf16 a[44:47], a[124:127], v[96:99], a[44:47]\n"
    "s_waitcnt lgkmcnt(0)\n"
    "buffer_store_dword v226, v244, s[16:19], s77 offen offset:1152 sc1\n"
    "ds_read_b128 a[136:139], v249 offset:9216\n"
    "v_mfma_f32_16x16x32_bf16 a[108:111], a[156:159], v[96:99], 0\n"
    "v_mfma_f32_16x16x32_bf16 a[96:99], a[128:131], v[92:95], a[96:99]\n"
    "v_exp_f32 v15, v15\n"
    "v_mfma_f32_16x16x32_bf16 a[100:103], a[140:143], v[96:99], 0\n"
    "buffer_store_dword v228, v244, s[16:19], s77 offen offset:256 sc1\n"
    "v_exp_f32 v35, v35\n"
    "v_mfma_f32_16x16x32_bf16 a[68:71], a[140:143], v[80:83], 0\n"
    "ds_read_b128 a[120:123], v3 offset:16384\n"
    "ds_read_b128 a[116:119], v3 offset:14336\n"
    "v_mfma_f32_16x16x32_bf16 a[40:43], a[112:115], v[92:95], a[40:43]\n"
    "v_exp_f32 v8, v8\n"
    "v_mfma_f32_16x16x32_bf16 a[76:79], a[156:159], v[80:83], 0\n"
    "buffer_store_dword v230, v244, s[16:19], s77 offen offset:384 sc1\n"
    "v_mfma_f32_16x16x32_bf16 a[76:79], a[152:155], v[76:79], a[76:79]\n"
    "v_exp_f32 v34, v34\n"
    "v_mfma_f32_16x16x32_bf16 a[60:63], a[152:155], v[68:71], 0\n"
    "s_waitcnt lgkmcnt(2)\n"
    "ds_read_b128 a[124:127], v3 offset:18432\n"
    "ds_read_b128 a[128:131], v3 offset:20480\n"
    "v_mfma_f32_16x16x32_bf16 a[92:95], a[152:155], v[84:87], 0\n"
    "s_waitcnt vmcnt(15)\n"
    "ds_add_u32 v247, v246\n"
    "v_mfma_f32_16x16x32_bf16 a[108:111], a[152:155], v[92:95], a[108:111]\n"
    "ds_read_b128 a[132:135], v3 offset:22528\n"
    "v_add_u32 v250, 1536, v250\n"
    "v_mfma_f32_16x16x32_bf16 a[52:55], a[136:139], v[68:71], 0\n"
    "ds_read_b128 a[112:115], v3 offset:12288\n"
    "v_mfma_f32_16x16x32_bf16 a[84:87], a[136:139], v[84:87], 0\n"
    "v_add_u32 v3, 49152, v252\n"
    "v_mfma_f32_16x16x32_bf16 a[72:75], a[148:151], v[80:83], a[72:75]\n"
    "buffer_store_dword v232, v244, s[16:19], s77 offen offset:1280 sc1\n"
    "v_cvt_pk_bf16_f32 v234, v234, v235\n"
    "v_mfma_f32_16x16x32_bf16 a[100:103], a[136:139], v[92:95], a[100:103]\n"
    "v_exp_f32 v17, v17\n"
    "v_mfma_f32_16x16x32_bf16 a[84:87], a[140:143], v[88:91], a[84:87]\n"
    "v_exp_f32 v23, v23\n"
    "v_mfma_f32_16x16x32_bf16 a[60:63], a[156:159], v[72:75], a[60:63]\n"
    "buffer_store_dword v234, v244, s[16:19], s77 offen offset:1408 sc1\n"
    "v_mfma_f32_16x16x32_bf16 a[52:55], a[140:143], v[72:75], a[52:55]\n"
    "v_exp_f32 v25, v25\n"
    "v_mfma_f32_16x16x32_bf16 a[68:71], a[136:139], v[76:79], a[68:71]\n"
    "s_waitcnt lgkmcnt(0)\n"
    "v_mfma_f32_16x16x32_bf16 a[92:95], a[156:159], v[88:91], a[92:95]\n"
    "s_branch .LPK\n"
    ".fill ((0x20000 - (. - _Z8attn_fwdPKtPKiS2_S0_S0_S0_S0_PcPt)) & 4095) / 4, 4, 0xBF800000\n"
    ".LhVP:\n"
    "s_waitcnt vmcnt(12)\n"
    "ds_read_b128 v[52:55], v251 offset:0\n"
    "s_add_u32 s83, s67, 4096\n"
    "ds_read_b128 v[200:203], v97 offset:49408\n"
    "ds_read_b128 v[196:199], v96 offset:49408\n"
    "v_mov_b32 v2, v3\n"
    "ds_read_b128 v[192:195], v97 offset:49280\n"
    "ds_read_b128 v[188:191], v96 offset:49280\n"
    "ds_read_b128 v[184:187], v97 offset:49152\n"
    "s_add_u32 m0, s68, .Lend-.Lend2+(0)\n"
    "ds_read_b128 v[180:183], v96 offset:49152\n"
    "s_waitcnt lgkmcnt(6)\n"
    "v_smfmac_f32_16x16x64_bf16 v[108:111], v[52:55], v[68:75], v253\n"
    "s_waitcnt lgkmcnt(4)\n"
    "v_pk_maximum3_f16 v98, v200, v201, v98\n"
    "v_pk_maximum3_f16 v98, v202, v203, v98\n"
    "v_pk_add_u16 v203, s89, v203 clamp\n"
    "v_pk_add_u16 v201, s89, v201 clamp\n"
    "v_pk_add_u16 v200, s89, v200 clamp\n"
    "v_pk_add_u16 v202, s89, v202 clamp\n"
    "v_pk_maximum3_f16 v98, v196, v197, v98\n"
    "v_smfmac_f32_16x16x64_bf16 v[116:119], v[52:55], v[84:91], v253\n"
    "v_pk_maximum3_f16 v98, v198, v199, v98\n"
    "v_mfma_f32_16x16x32_bf16 v[64:67], a[44:47], v[200:203], v[64:67]\n"
    "s_waitcnt lgkmcnt(3)\n"
    "v_pk_add_u16 v197, s89, v197 clamp\n"
    "v_pk_maximum3_f16 v98, v192, v193, v98\n"
    "v_mfma_f32_16x16x32_bf16 v[48:51], a[20:23], v[200:203], v[48:51]\n"
    "buffer_load_dwordx4 v1, s[16:19], s67 offen offset:0 sc1 lds\n"
    "v_mfma_f32_16x16x32_bf16 v[16:19], a[132:135], v[200:203], v[16:19]\n"
    "v_pk_add_u16 v199, s89, v199 clamp\n"
    "v_pk_add_u16 v198, s89, v198 clamp\n"
    "v_smfmac_f32_16x16x64_bf16 v[112:115], v[52:55], v[76:83], v253\n"
    "v_pk_add_u16 v196, s89, v196 clamp\n"
    "v_mfma_f32_16x16x32_bf16 v[32:35], a[156:159], v[200:203], v[32:35]\n"
    "s_waitcnt lgkmcnt(2)\n"
    "v_pk_add_u16 v193, s89, v193 clamp\n"
    "v_pk_maximum3_f16 v98, v194, v195, v98\n"
    "v_pk_maximum3_f16 v98, v188, v189, v98\n"
    "v_mfma_f32_16x16x32_bf16 v[32:35], a[152:155], v[196:199], v[32:35]\n"
    "v_pk_maximum3_f16 v98, v190, v191, v98\n"
    "v_pk_add_u16 v192, s89, v192 clamp\n"
    "v_mfma_f32_16x16x32_bf16 v[16:19], a[128:131], v[196:199], v[16:19]\n"
    "v_pk_add_u16 v194, s89, v194 clamp\n"
    "v_pk_add_u16 v195, s89, v195 clamp\n"
    "v_mfma_f32_16x16x32_bf16 v[48:51], a[16:19], v[196:199], v[48:51]\n"
    "buffer_load_dwordx4 v1, s[16:19], s67 offen offset:1024 sc1 lds\n"
    "v_mfma_f32_16x16x32_bf16 v[48:51], a[12:15], v[192:195], v[48:51]\n"
    "s_waitcnt lgkmcnt(1)\n"
    "v_pk_maximum3_f16 v98, v184, v185, v98\n"
    "v_pk_add_u16 v188, s89, v188 clamp\n"
    "v_pk_maximum3_f16 v98, v186, v187, v98\n"
    "v_mfma_f32_16x16x32_bf16 v[32:35], a[148:151], v[192:195], v[32:35]\n"
    "v_pk_add_u16 v190, s89, v190 clamp\n"
    "v_accvgpr_write_b32 a184, v196\n"
    "v_mfma_f32_16x16x32_bf16 v[16:19], a[124:127], v[192:195], v[16:19]\n"
    "v_pk_add_u16 v189, s89, v189 clamp\n"
    "v_pk_add_u16 v191, s89, v191 clamp\n"
    "v_mfma_f32_16x16x32_bf16 v[64:67], a[40:43], v[196:199], v[64:67]\n"
    "v_pk_add_u16 v186, s89, v186 clamp\n"
    "v_pk_add_u16 v187, s89, v187 clamp\n"
    "v_mfma_f32_16x16x32_bf16 v[32:35], a[144:147], v[188:191], v[32:35]\n"
    "s_waitcnt lgkmcnt(0)\n"
    "v_pk_add_u16 v185, s89, v185 clamp\n"
    "v_pk_maximum3_f16 v98, v180, v181, v98\n"
    "v_mfma_f32_16x16x32_bf16 v[48:51], a[8:11], v[188:191], v[48:51]\n"
    "s_waitcnt vmcnt(8)\n"
    "buffer_load_dwordx4 v1, s[16:19], s67 offen offset:2048 sc1 lds\n"
    "v_mfma_f32_16x16x32_bf16 v[64:67], a[36:39], v[192:195], v[64:67]\n"
    "s_barrier\n"
    "v_pk_add_u16 v184, s89, v184 clamp\n"
    "v_mfma_f32_16x16x32_bf16 v[16:19], a[120:123], v[188:191], v[16:19]\n"
    "ds_read_b128 a[48:51], v252 offset:24576\n"
    "ds_read_b128 a[68:71], v252 offset:34816\n"
    "v_mfma_f32_16x16x32_bf16 v[32:35], a[140:143], v[184:187], v[32:35]\n"
    "ds_read_b128 a[76:79], v252 offset:27648\n"
    "ds_read_b128 a[72:75], v252 offset:25600\n"
    "v_mfma_f32_16x16x32_bf16 v[48:51], a[4:7], v[184:187], v[48:51]\n"
    "ds_read_b128 a[92:95], v252 offset:35840\n"
    "v_pk_maximum3_f16 v98, v182, v183, v98\n"
    "v_mfma_f32_16x16x32_bf16 v[64:67], a[32:35], v[188:191], v[64:67]\n"
    "v_pk_add_u16 v182, s89, v182 clamp\n"
    "v_pk_add_u16 v181, s89, v181 clamp\n"
    "v_mfma_f32_16x16x32_bf16 v[64:67], a[28:31], v[184:187], v[64:67]\n"
    "v_pk_add_u16 v183, s89, v183 clamp\n"
    "v_pk_add_u16 v180, s89, v180 clamp\n"
    "v_mfma_f32_16x16x32_bf16 v[16:19], a[116:119], v[184:187], v[16:19]\n"
    "buffer_load_dwordx4 v1, s[16:19], s67 offen offset:3072 sc1 lds\n"
    "v_mfma_f32_16x16x32_bf16 v[32:35], a[136:139], v[180:183], v[32:35]\n"
    "ds_read_b128 a[64:67], v252 offset:32768\n"
    "v_accvgpr_write_b32 a176, v188\n"
    "v_mfma_f32_16x16x32_bf16 v[16:19], a[112:115], v[180:183], v[16:19]\n"
    "v_accvgpr_write_b32 a175, v187\n"
    "s_waitcnt lgkmcnt(5)\n"
    "v_mfma_f32_16x16x32_bf16 v[12:15], a[48:51], a[240:243], v[100:103]\n"
    "s_waitcnt lgkmcnt(4)\n"
    "ds_read_b128 a[56:59], v252 offset:28672\n"
    "v_accvgpr_write_b32 a177, v189\n"
    "v_mfma_f32_16x16x32_bf16 v[48:51], a[0:3], v[180:183], v[48:51]\n"
    "s_add_u32 m0, s68, 4096\n"
    "ds_read_b128 a[80:83], v252 offset:29696\n"
    "ds_read_b128 a[88:91], v252 offset:33792\n"
    "v_mfma_f32_16x16x32_bf16 v[8:11], a[68:71], a[236:239], v[100:103]\n"
    "v_accvgpr_write_b32 a188, v200\n"
    "v_accvgpr_write_b32 a190, v202\n"
    "v_mfma_f32_16x16x32_bf16 v[4:7], a[48:51], a[192:195], v[100:103]\n"
    "s_waitcnt lgkmcnt(5)\n"
    "ds_read_b128 a[60:63], v252 offset:30720\n"
    "ds_read_b128 a[52:55], v252 offset:26624\n"
    "v_mfma_f32_16x16x32_bf16 v[20:23], a[72:75], a[192:195], v[104:107]\n"
    "buffer_load_dwordx4 v1, s[16:19], s83 offen offset:0 sc1 lds\n"
    "v_mfma_f32_16x16x32_bf16 v[28:31], a[72:75], a[240:243], v[104:107]\n"
    "s_waitcnt lgkmcnt(6)\n"
    "v_accvgpr_write_b32 a171, v183\n"
    "v_accvgpr_write_b32 a186, v198\n"
    "v_mfma_f32_16x16x32_bf16 v[24:27], a[92:95], a[236:239], v[104:107]\n"
    "v_exp_f32 v18, v18\n"
    "v_mfma_f32_16x16x32_bf16 v[64:67], a[24:27], v[180:183], v[64:67]\n"
    "v_accvgpr_write_b32 a174, v186\n"
    "v_accvgpr_write_b32 a178, v190\n"
    "v_mfma_f32_16x16x32_bf16 v[20:23], a[76:79], a[196:199], v[20:23]\n"
    "v_accvgpr_write_b32 a191, v203\n"
    "v_accvgpr_write_b32 a179, v191\n"
    "v_mfma_f32_16x16x32_bf16 v[28:31], a[76:79], a[244:247], v[28:31]\n"
    "s_waitcnt lgkmcnt(5)\n"
    "v_accvgpr_write_b32 a180, v192\n"
    "v_mfma_f32_16x16x32_bf16 v[8:11], a[64:67], a[232:235], v[8:11]\n"
    "s_waitcnt lgkmcnt(3)\n"
    "ds_read_b128 a[84:87], v252 offset:31744\n"
    "v_accvgpr_write_b32 a187, v199\n"
    "v_mfma_f32_16x16x32_bf16 v[28:31], a[80:83], a[248:251], v[28:31]\n"
    "v_accvgpr_write_b32 a169, v181\n"
    "v_accvgpr_write_b32 a173, v185\n"
    "v_mfma_f32_16x16x32_bf16 v[20:23], a[80:83], a[200:203], v[20:23]\n"
    "s_waitcnt lgkmcnt(1)\n"
    "buffer_load_dwordx4 v1, s[16:19], s83 offen offset:1024 sc1 lds\n"
    "v_mfma_f32_16x16x32_bf16 v[4:7], a[52:55], a[196:199], v[4:7]\n"
    "v_exp_f32 v16, v16\n"
    "v_mfma_f32_16x16x32_bf16 v[24:27], a[88:91], a[232:235], v[24:27]\n"
    "v_accvgpr_write_b32 a168, v180\n"
    "v_accvgpr_write_b32 a182, v194\n"
    "v_mfma_f32_16x16x32_bf16 v[8:11], a[60:63], a[228:231], v[8:11]\n"
    "v_accvgpr_write_b32 a181, v193\n"
    "v_accvgpr_write_b32 a189, v201\n"
    "v_mfma_f32_16x16x32_bf16 v[8:11], a[56:59], a[224:227], v[8:11]\n"
    "s_add_u32 m0, s64, 0\n"
    "v_accvgpr_write_b32 a170, v182\n"
    "v_accvgpr_write_b32 a172, v184\n"
    "v_mfma_f32_16x16x32_bf16 v[4:7], a[56:59], a[200:203], v[4:7]\n"
    "v_exp_f32 v32, v32\n"
    "v_mfma_f32_16x16x32_bf16 v[4:7], a[60:63], a[204:207], v[4:7]\n"
    "s_waitcnt lgkmcnt(0)\n"
    "v_accvgpr_write_b32 a183, v195\n"
    "v_accvgpr_write_b32 a185, v197\n"
    "v_mfma_f32_16x16x32_bf16 v[28:31], a[84:87], a[252:255], v[28:31]\n"
    "s_add_u32 s83, s67, 24576\n"
    "buffer_load_dwordx4 v1, s[16:19], s83 offen offset:0 sc1 lds\n"
    "v_mfma_f32_16x16x32_bf16 v[12:15], a[52:55], a[244:247], v[12:15]\n"
    "v_exp_f32 v17, v17\n"
    "v_mfma_f32_16x16x32_bf16 v[12:15], a[56:59], a[248:251], v[12:15]\n"
    "v_exp_f32 v19, v19\n"
    "v_mfma_f32_16x16x32_bf16 v[8:11], a[52:55], a[220:223], v[8:11]\n"
    "v_exp_f32 v35, v35\n"
    "v_mfma_f32_16x16x32_bf16 v[20:23], a[84:87], a[204:207], v[20:23]\n"
    "v_exp_f32 v51, v51\n"
    "v_mfma_f32_16x16x32_bf16 v[12:15], a[60:63], a[252:255], v[12:15]\n"
    "v_exp_f32 v65, v65\n"
    "v_mfma_f32_16x16x32_bf16 v[4:7], a[64:67], a[208:211], v[4:7]\n"
    "v_mfma_f32_16x16x32_bf16 v[12:15], a[64:67], a[160:163], v[12:15]\n"
    "buffer_load_dwordx4 v1, s[16:19], s83 offen offset:1024 sc1 lds\n"
    "v_mfma_f32_16x16x32_bf16 v[4:7], a[68:71], a[212:215], v[4:7]\n"
    "v_cvt_pk_bf16_f32 v93, v18, v19\n"
    "v_mfma_f32_16x16x32_bf16 v[24:27], a[84:87], a[228:231], v[24:27]\n"
    "v_exp_f32 v34, v34\n"
    "v_mfma_f32_16x16x32_bf16 v[8:11], a[48:51], a[216:219], v[8:11]\n"
    "v_exp_f32 v33, v33\n"
    "v_mfma_f32_16x16x32_bf16 v[12:15], a[68:71], a[164:167], v[12:15]\n"
    "v_cvt_pk_bf16_f32 v92, v16, v17\n"
    "v_mfma_f32_16x16x32_bf16 v[28:31], a[88:91], a[160:163], v[28:31]\n"
    "s_waitcnt vmcnt(8)\n"
    "v_exp_f32 v48, v48\n"
    "v_mfma_f32_16x16x32_bf16 v[16:19], a[68:71], a[188:191], v[100:103]\n"
    "s_barrier\n"
    "ds_read_b128 a[120:123], v248 offset:1024\n"
    "v_mfma_f32_16x16x32_bf16 v[24:27], a[80:83], a[224:227], v[24:27]\n"
    "buffer_load_dwordx4 v1, s[16:19], s83 offen offset:2048 sc1 lds\n"
    "v_mfma_f32_16x16x32_bf16 v[16:19], a[64:67], a[184:187], v[16:19]\n"
    "ds_read_b128 a[124:127], v248 offset:13312\n"
    "v_cvt_pk_bf16_f32 v94, v32, v33\n"
    "v_mfma_f32_16x16x32_bf16 v[16:19], a[60:63], a[180:183], v[16:19]\n"
    "ds_read_b128 a[136:139], v248 offset:3072\n"
    "ds_read_b128 a[128:131], v248 offset:2048\n"
    "v_mfma_f32_16x16x32_bf16 v[24:27], a[76:79], a[220:223], v[24:27]\n"
    "v_exp_f32 v50, v50\n"
    "v_mfma_f32_16x16x32_bf16 v[20:23], a[88:91], a[208:211], v[20:23]\n"
    "v_cvt_pk_bf16_f32 v97, v50, v51\n"
    "v_cvt_pk_bf16_f32 v95, v34, v35\n"
    "v_mfma_f32_16x16x32_bf16 v[32:35], a[92:95], a[188:191], v[104:107]\n"
    "v_exp_f32 v67, v67\n"
    "v_mfma_f32_16x16x32_bf16 v[16:19], a[56:59], a[176:179], v[16:19]\n"
    "v_mfma_f32_16x16x32_bf16 v[32:35], a[88:91], a[184:187], v[32:35]\n"
    "buffer_load_dwordx4 v1, s[16:19], s83 offen offset:3072 sc1 lds\n"
    "v_mfma_f32_16x16x32_bf16 v[20:23], a[92:95], a[212:215], v[20:23]\n"
    "v_pk_add_u16 v98, s89, v98\n"
    "s_waitcnt lgkmcnt(2)\n"
    "v_mfma_f32_16x16x32_bf16 v[160:163], a[124:127], v[88:91], 0\n"
    "ds_read_b128 a[132:135], v248 offset:14336\n"
    "s_waitcnt lgkmcnt(2)\n"
    "v_mfma_f32_16x16x32_bf16 v[136:139], a[136:139], v[68:71], 0\n"
    "ds_read_b128 a[144:147], v248 offset:4096\n"
    "v_mfma_f32_16x16x32_bf16 v[128:131], a[124:127], v[72:75], 0\n"
    "s_add_u32 s83, s67, 28672\n"
    "ds_read_b128 a[148:151], v248 offset:16384\n"
    "ds_read_b128 a[112:115], v248 offset:0\n"
    "v_mfma_f32_16x16x32_bf16 v[144:147], a[120:123], v[76:79], 0\n"
    "v_exp_f32 v64, v64\n"
    "v_mfma_f32_16x16x32_bf16 v[32:35], a[84:87], a[180:183], v[32:35]\n"
    "s_add_u32 m0, s64, 4096\n"
    "ds_read_b128 a[152:155], v248 offset:5120\n"
    "v_mfma_f32_16x16x32_bf16 v[16:19], a[52:55], a[172:175], v[16:19]\n"
    "buffer_load_dwordx4 v1, s[16:19], s83 offen offset:0 sc1 lds\n"
    "v_mfma_f32_16x16x32_bf16 v[128:131], a[120:123], v[68:71], v[128:131]\n"
    "v_exp_f32 v66, v66\n"
    "v_mfma_f32_16x16x32_bf16 v[176:179], a[120:123], v[92:95], 0\n"
    "s_waitcnt lgkmcnt(5)\n"
    "ds_read_b128 a[116:119], v248 offset:12288\n"
    "v_cvt_pk_bf16_f32 v99, v66, v67\n"
    "v_mfma_f32_16x16x32_bf16 v[132:135], a[128:131], v[68:71], 0\n"
    "v_exp_f32 v49, v49\n"
    "v_mfma_f32_16x16x32_bf16 v[28:31], a[92:95], a[164:167], v[28:31]\n"
    "s_waitcnt lgkmcnt(5)\n"
    "v_cvt_pk_bf16_f32 v96, v48, v49\n"
    "v_mfma_f32_16x16x32_bf16 v[148:151], a[132:135], v[80:83], 0\n"
    "v_and_b32 v98, 0x80008000, v98\n"
    "v_mfma_f32_16x16x32_bf16 v[164:167], a[128:131], v[84:87], 0\n"
    "v_exp_f32 v4, v4\n"
    "v_mfma_f32_16x16x32_bf16 v[24:27], a[72:75], a[216:219], v[24:27]\n"
    "buffer_load_dwordx4 v1, s[16:19], s83 offen offset:1024 sc1 lds\n"
    "v_mfma_f32_16x16x32_bf16 v[32:35], a[80:83], a[176:179], v[32:35]\n"
    "v_exp_f32 v12, v12\n"
    "v_mfma_f32_16x16x32_bf16 v[144:147], a[124:127], v[80:83], v[144:147]\n"
    "v_exp_f32 v31, v31\n"
    "v_mfma_f32_16x16x32_bf16 v[160:163], a[120:123], v[84:87], v[160:163]\n"
    "s_waitcnt lgkmcnt(2)\n"
    "v_mfma_f32_16x16x32_bf16 v[124:127], a[112:115], v[68:71], 0\n"
    "v_exp_f32 v27, v27\n"
    "v_mfma_f32_16x16x32_bf16 v[188:191], a[148:151], v[72:75], 0\n"
    "ds_read_b128 a[140:143], v248 offset:15360\n"
    "v_mfma_f32_16x16x32_bf16 v[16:19], a[48:51], a[168:171], v[16:19]\n"
    "s_waitcnt lgkmcnt(2)\n"
    "ds_read_b128 a[120:123], v248 offset:7168\n"
    "v_cmp_ne_u32 s[34:35], 0, v98\n"
    "v_mfma_f32_16x16x32_bf16 a[20:23], a[152:155], v[84:87], 0\n"
    "s_waitcnt lgkmcnt(2)\n"
    "v_exp_f32 v29, v29\n"
    "v_mfma_f32_16x16x32_bf16 v[140:143], a[116:119], v[80:83], 0\n"
    "buffer_load_dwordx4 v[204:207], v242, s[40:43], s75 offen offset:0 nt\n"
    "v_mfma_f32_16x16x32_bf16 v[188:191], a[144:147], v[68:71], v[188:191]\n"
    "v_cvt_pk_bf16_f32 v98, v64, v65\n"
    "v_mfma_f32_16x16x32_bf16 v[124:127], a[116:119], v[72:75], v[124:127]\n"
    "ds_read_b128 a[156:159], v248 offset:17408\n"
    "v_mfma_f32_16x16x32_bf16 v[132:135], a[132:135], v[72:75], v[132:135]\n"
    "v_exp_f32 v5, v5\n"
    "v_mfma_f32_16x16x32_bf16 v[192:195], a[152:155], v[68:71], 0\n"
    "buffer_load_dwordx4 v[208:211], v242, s[40:43], s75 offen offset:768 nt\n"
    "v_mfma_f32_16x16x32_bf16 v[176:179], a[124:127], v[96:99], v[176:179]\n"
    "ds_read_b128 a[124:127], v248 offset:19456\n"
    "v_mfma_f32_16x16x32_bf16 v[140:143], a[112:115], v[76:79], v[140:143]\n"
    "v_exp_f32 v26, v26\n"
    "v_mfma_f32_16x16x32_bf16 v[172:175], a[116:119], v[96:99], 0\n"
    "s_waitcnt lgkmcnt(2)\n"
    "buffer_load_dwordx4 v[212:215], v242, s[40:43], s75 offen offset:256 nt\n"
    "v_mfma_f32_16x16x32_bf16 a[12:15], a[120:123], v[76:79], 0\n"
    "v_exp_f32 v20, v20\n"
    "v_mfma_f32_16x16x32_bf16 a[0:3], a[144:147], v[76:79], 0\n"
    "v_mfma_f32_16x16x32_bf16 v[152:155], a[140:143], v[80:83], 0\n"
    "v_mfma_f32_16x16x32_bf16 v[172:175], a[112:115], v[92:95], v[172:175]\n"
    "v_exp_f32 v16, v16\n"
    "v_mfma_f32_16x16x32_bf16 a[16:19], a[148:151], v[88:91], 0\n"
    "v_exp_f32 v17, v17\n"
    "v_mfma_f32_16x16x32_bf16 v[164:167], a[132:135], v[88:91], v[164:167]\n"
    "v_exp_f32 v24, v24\n"
    "v_mfma_f32_16x16x32_bf16 v[152:155], a[136:139], v[76:79], v[152:155]\n"
    "buffer_load_dwordx4 v[216:219], v242, s[40:43], s75 offen offset:1024 nt\n"
    "v_mfma_f32_16x16x32_bf16 v[180:183], a[132:135], v[96:99], 0\n"
    "v_exp_f32 v19, v19\n"
    "v_mfma_f32_16x16x32_bf16 v[156:159], a[112:115], v[84:87], 0\n"
    "v_exp_f32 v14, v14\n"
    "v_mfma_f32_16x16x32_bf16 a[44:47], a[120:123], v[92:95], 0\n"
    "v_exp_f32 v9, v9\n"
    "v_mfma_f32_16x16x32_bf16 a[32:35], a[144:147], v[92:95], 0\n"
    "s_waitcnt lgkmcnt(1)\n"
    "buffer_load_dwordx4 v[220:223], v242, s[40:43], s75 offen offset:512 nt\n"
    "v_mfma_f32_16x16x32_bf16 a[36:39], a[156:159], v[96:99], 0\n"
    "v_mfma_f32_16x16x32_bf16 a[0:3], a[148:151], v[80:83], a[0:3]\n"
    "buffer_load_dwordx4 v[224:227], v242, s[40:43], s75 offen offset:1280 nt\n"
    "v_mfma_f32_16x16x32_bf16 a[32:35], a[148:151], v[96:99], a[32:35]\n"
    "ds_read_b128 a[132:135], v248 offset:20480\n"
    "v_mfma_f32_16x16x32_bf16 v[148:151], a[128:131], v[76:79], v[148:151]\n"
    "v_exp_f32 v25, v25\n"
    "v_mfma_f32_16x16x32_bf16 a[4:7], a[156:159], v[80:83], 0\n"
    "ds_read_b128 a[112:115], v248 offset:6144\n"
    "v_mfma_f32_16x16x32_bf16 v[180:183], a[128:131], v[92:95], v[180:183]\n"
    "ds_read_b128 a[148:151], v248 offset:22528\n"
    "v_mfma_f32_16x16x32_bf16 v[184:187], a[140:143], v[96:99], 0\n"
    "v_mfma_f32_16x16x32_bf16 v[156:159], a[116:119], v[88:91], v[156:159]\n"
    "v_exp_f32 v10, v10\n"
    "v_mfma_f32_16x16x32_bf16 v[136:139], a[140:143], v[72:75], v[136:139]\n"
    "s_waitcnt lgkmcnt(3)\n"
    "v_mfma_f32_16x16x32_bf16 v[200:203], a[124:127], v[72:75], 0\n"
    "ds_read_b128 a[116:119], v248 offset:18432\n"
    "ds_read_b128 a[128:131], v248 offset:8192\n"
    "v_mfma_f32_16x16x32_bf16 a[20:23], a[156:159], v[88:91], a[20:23]\n"
    "v_exp_f32 v7, v7\n"
    "v_mfma_f32_16x16x32_bf16 a[4:7], a[152:155], v[76:79], a[4:7]\n"
    "v_mfma_f32_16x16x32_bf16 v[192:195], a[156:159], v[72:75], v[192:195]\n"
    "v_exp_f32 v21, v21\n"
    "v_mfma_f32_16x16x32_bf16 a[36:39], a[152:155], v[92:95], a[36:39]\n"
    "ds_read_b128 a[156:159], v248 offset:23552\n"
    "v_mfma_f32_16x16x32_bf16 v[184:187], a[136:139], v[92:95], v[184:187]\n"
    "s_waitcnt lgkmcnt(5)\n"
    "v_mfma_f32_16x16x32_bf16 a[64:67], a[132:135], v[80:83], 0\n"
    "v_mfma_f32_16x16x32_bf16 v[168:171], a[136:139], v[84:87], 0\n"
    "s_waitcnt lgkmcnt(3)\n"
    "v_mfma_f32_16x16x32_bf16 a[88:91], a[148:151], v[88:91], 0\n"
    "v_mfma_f32_16x16x32_bf16 a[16:19], a[144:147], v[84:87], a[16:19]\n"
    "s_waitcnt lgkmcnt(2)\n"
    "ds_read_b128 a[152:155], v248 offset:11264\n"
    "v_mfma_f32_16x16x32_bf16 a[8:11], a[116:119], v[80:83], 0\n"
    "v_mfma_f32_16x16x32_bf16 v[168:171], a[140:143], v[88:91], v[168:171]\n"
    "v_exp_f32 v15, v15\n"
    "v_mfma_f32_16x16x32_bf16 a[28:31], a[124:127], v[88:91], 0\n"
    "s_waitcnt lgkmcnt(2)\n"
    "v_exp_f32 v11, v11\n"
    "v_mfma_f32_16x16x32_bf16 a[48:51], a[128:131], v[68:71], 0\n"
    "v_mfma_f32_16x16x32_bf16 a[56:59], a[148:151], v[72:75], 0\n"
    "v_mfma_f32_16x16x32_bf16 a[28:31], a[120:123], v[84:87], a[28:31]\n"
    "v_mfma_f32_16x16x32_bf16 a[8:11], a[112:115], v[76:79], a[8:11]\n"
    "buffer_load_dwordx2 v[236:237], v241, s[36:39], s81 offen offset:0 sc1\n"
    "v_mfma_f32_16x16x32_bf16 v[200:203], a[120:123], v[68:71], v[200:203]\n"
    "v_exp_f32 v28, v28\n"
    "v_mfma_f32_16x16x32_bf16 v[196:199], a[112:115], v[68:71], 0\n"
    "s_waitcnt lgkmcnt(1)\n"
    "v_mfma_f32_16x16x32_bf16 a[108:111], a[156:159], v[96:99], 0\n"
    "v_mfma_f32_16x16x32_bf16 v[32:35], a[76:79], a[172:175], v[32:35]\n"
    "ds_read_b128 a[136:139], v248 offset:9216\n"
    "ds_read_b128 a[140:143], v248 offset:21504\n"
    "v_mfma_f32_16x16x32_bf16 a[80:83], a[128:131], v[84:87], 0\n"
    "v_mfma_f32_16x16x32_bf16 a[24:27], a[112:115], v[84:87], 0\n"
    "v_mfma_f32_16x16x32_bf16 a[12:15], a[124:127], v[80:83], a[12:15]\n"
    "v_exp_f32 v23, v23\n"
    "v_mfma_f32_16x16x32_bf16 v[32:35], a[72:75], a[168:171], v[32:35]\n"
    "ds_read_b128 a[120:123], v3 offset:16384\n"
    "ds_read_b128 a[144:147], v248 offset:10240\n"
    "v_mfma_f32_16x16x32_bf16 a[40:43], a[116:119], v[96:99], 0\n"
    "v_exp_f32 v22, v22\n"
    "v_mfma_f32_16x16x32_bf16 a[80:83], a[132:135], v[88:91], a[80:83]\n"
    "v_mfma_f32_16x16x32_bf16 a[76:79], a[156:159], v[80:83], 0\n"
    "v_mfma_f32_16x16x32_bf16 a[40:43], a[112:115], v[92:95], a[40:43]\n"
    "s_waitcnt lgkmcnt(3)\n"
    "v_exp_f32 v32, v32\n"
    "v_mfma_f32_16x16x32_bf16 a[52:55], a[136:139], v[68:71], 0\n"
    "v_exp_f32 v33, v33\n"
    "v_mfma_f32_16x16x32_bf16 a[48:51], a[132:135], v[72:75], a[48:51]\n"
    "v_mfma_f32_16x16x32_bf16 a[92:95], a[152:155], v[84:87], 0\n"
    "s_waitcnt lgkmcnt(2)\n"
    "v_mfma_f32_16x16x32_bf16 a[68:71], a[140:143], v[80:83], 0\n"
    "v_mfma_f32_16x16x32_bf16 a[100:103], a[140:143], v[96:99], 0\n"
    "s_waitcnt lgkmcnt(0)\n"
    "ds_read_b128 v[100:103], v250 offset:896\n"
    "v_mfma_f32_16x16x32_bf16 a[88:91], a[144:147], v[84:87], a[88:91]\n"
    "v_mfma_f32_16x16x32_bf16 a[24:27], a[116:119], v[88:91], a[24:27]\n"
    "v_mfma_f32_16x16x32_bf16 a[84:87], a[136:139], v[84:87], 0\n"
    "ds_read_b128 a[112:115], v3 offset:12288\n"
    "v_mfma_f32_16x16x32_bf16 a[96:99], a[132:135], v[96:99], 0\n"
    "v_exp_f32 v34, v34\n"
    "v_mfma_f32_16x16x32_bf16 v[196:199], a[116:119], v[72:75], v[196:199]\n"
    "v_exp_f32 v6, v6\n"
    "v_mfma_f32_16x16x32_bf16 a[44:47], a[124:127], v[96:99], a[44:47]\n"
    "v_mfma_f32_16x16x32_bf16 a[108:111], a[152:155], v[92:95], a[108:111]\n"
    "v_exp_f32 v8, v8\n"
    "v_mfma_f32_16x16x32_bf16 a[76:79], a[152:155], v[76:79], a[76:79]\n"
    "v_mfma_f32_16x16x32_bf16 a[104:107], a[144:147], v[92:95], 0\n"
    "s_add_u32 s67, s67, 49152\n"
    "v_mfma_f32_16x16x32_bf16 a[56:59], a[144:147], v[68:71], a[56:59]\n"
    "v_exp_f32 v30, v30\n"
    "v_mfma_f32_16x16x32_bf16 a[96:99], a[128:131], v[92:95], a[96:99]\n"
    "ds_read_b128 a[124:127], v3 offset:18432\n"
    "v_mfma_f32_16x16x32_bf16 a[72:75], a[144:147], v[76:79], 0\n"
    "ds_read_b128 a[132:135], v3 offset:22528\n"
    "s_waitcnt vmcnt(7)\n"
    "v_mfma_f32_16x16x32_bf16 a[104:107], a[148:151], v[96:99], a[104:107]\n"
    "ds_add_u32 v247, v246\n"
    "v_mfma_f32_16x16x32_bf16 a[64:67], a[128:131], v[76:79], a[64:67]\n"
    "ds_read_b128 a[116:119], v3 offset:14336\n"
    "ds_read_b128 a[128:131], v3 offset:20480\n"
    "v_smfmac_f32_16x16x64_bf16 v[120:123], v[52:55], v[92:99], v253\n"
    "v_add_u32 v3, 49152, v252\n"
    "v_mfma_f32_16x16x32_bf16 a[60:63], a[152:155], v[68:71], 0\n"
    "v_mfma_f32_16x16x32_bf16 a[52:55], a[140:143], v[72:75], a[52:55]\n"
    "v_mfma_f32_16x16x32_bf16 a[84:87], a[140:143], v[88:91], a[84:87]\n"
    "v_exp_f32 v13, v13\n"
    "v_mfma_f32_16x16x32_bf16 a[60:63], a[156:159], v[72:75], a[60:63]\n"
    "v_exp_f32 v35, v35\n"
    "v_mfma_f32_16x16x32_bf16 a[92:95], a[156:159], v[88:91], a[92:95]\n"
    "v_exp_f32 v18, v18\n"
    "v_mfma_f32_16x16x32_bf16 a[72:75], a[148:151], v[80:83], a[72:75]\n"
    "v_mfma_f32_16x16x32_bf16 a[100:103], a[136:139], v[92:95], a[100:103]\n"
    "s_waitcnt lgkmcnt(0)\n"
    "v_mfma_f32_16x16x32_bf16 a[68:71], a[136:139], v[76:79], a[68:71]\n"
    "s_branch .LNV\n"
    ".fill ((0x20000 - (. - _Z8attn_fwdPKtPKiS2_S0_S0_S0_S0_PcPt)) & 4095) / 4, 4, 0xBF800000\n"
    ".LhVN:\n"
    "s_waitcnt vmcnt(18)\n"
    "ds_read_b128 v[52:55], v251 offset:768\n"
    "ds_read_b128 v[184:187], v97 offset:49152\n"
    "ds_read_b128 v[196:199], v96 offset:49408\n"
    "ds_read_b128 v[200:203], v97 offset:49408\n"
    "ds_read_b128 v[188:191], v96 offset:49280\n"
    "ds_read_b128 v[180:183], v96 offset:49152\n"
    "v_mov_b32 v2, v3\n"
    "ds_read_b128 v[192:195], v97 offset:49280\n"
    "s_add_u32 m0, s68, .Lend-.Lend2+(0)\n"
    "s_waitcnt lgkmcnt(6)\n"
    "v_smfmac_f32_16x16x64_bf16 v[108:111], v[52:55], v[68:75], v253\n"
    "s_waitcnt lgkmcnt(3)\n"
    "v_pk_maximum3_f16 v98, v200, v201, v98\n"
    "v_pk_maximum3_f16 v98, v202, v203, v98\n"
    "v_pk_add_u16 v200, s89, v200 clamp\n"
    "v_smfmac_f32_16x16x64_bf16 v[116:119], v[52:55], v[84:91], v253\n"
    "s_add_u32 s83, s67, 4096\n"
    "v_pk_add_u16 v201, s89, v201 clamp\n"
    "v_pk_add_u16 v202, s89, v202 clamp\n"
    "v_pk_add_u16 v203, s89, v203 clamp\n"
    "v_pk_maximum3_f16 v98, v196, v197, v98\n"
    "v_pk_maximum3_f16 v98, v198, v199, v98\n"
    "v_mfma_f32_16x16x32_bf16 v[48:51], a[20:23], v[200:203], v[48:51]\n"
    "v_pk_add_u16 v197, s89, v197 clamp\n"
    "v_pk_add_u16 v198, s89, v198 clamp\n"
    "v_mfma_f32_16x16x32_bf16 v[64:67], a[44:47], v[200:203], v[64:67]\n"
    "buffer_load_dwordx4 v1, s[16:19], s67 offen offset:0 sc1 lds\n"
    "v_mfma_f32_16x16x32_bf16 v[16:19], a[132:135], v[200:203], v[16:19]\n"
    "v_pk_add_u16 v199, s89, v199 clamp\n"
    "v_pk_add_u16 v196, s89, v196 clamp\n"
    "v_mfma_f32_16x16x32_bf16 v[32:35], a[156:159], v[200:203], v[32:35]\n"
    "s_waitcnt lgkmcnt(0)\n"
    "v_pk_maximum3_f16 v98, v192, v193, v98\n"
    "v_pk_maximum3_f16 v98, v194, v195, v98\n"
    "v_pk_add_u16 v194, s89, v194 clamp\n"
    "v_mfma_f32_16x16x32_bf16 v[32:35], a[152:155], v[196:199], v[32:35]\n"
    "v_pk_add_u16 v192, s89, v192 clamp\n"
    "v_pk_add_u16 v193, s89, v193 clamp\n"
    "v_mfma_f32_16x16x32_bf16 v[64:67], a[40:43], v[196:199], v[64:67]\n"
    "v_pk_maximum3_f16 v98, v188, v189, v98\n"
    "v_pk_add_u16 v195, s89, v195 clamp\n"
    "v_mfma_f32_16x16x32_bf16 v[16:19], a[128:131], v[196:199], v[16:19]\n"
    "v_pk_add_u16 v188, s89, v188 clamp\n"
    "v_pk_maximum3_f16 v98, v190, v191, v98\n"
    "v_mfma_f32_16x16x32_bf16 v[32:35], a[148:151], v[192:195], v[32:35]\n"
    "buffer_load_dwordx4 v1, s[16:19], s67 offen offset:1024 sc1 lds\n"
    "v_mfma_f32_16x16x32_bf16 v[64:67], a[36:39], v[192:195], v[64:67]\n"
    "v_pk_maximum3_f16 v98, v184, v185, v98\n"
    "v_pk_add_u16 v189, s89, v189 clamp\n"
    "v_mfma_f32_16x16x32_bf16 v[48:51], a[16:19], v[196:199], v[48:51]\n"
    "v_pk_add_u16 v185, s89, v185 clamp\n"
    "v_pk_add_u16 v191, s89, v191 clamp\n"
    "v_pk_add_u16 v190, s89, v190 clamp\n"
    "v_mfma_f32_16x16x32_bf16 v[48:51], a[12:15], v[192:195], v[48:51]\n"
    "v_pk_maximum3_f16 v98, v186, v187, v98\n"
    "v_pk_add_u16 v186, s89, v186 clamp\n"
    "v_mfma_f32_16x16x32_bf16 v[16:19], a[124:127], v[192:195], v[16:19]\n"
    "v_pk_maximum3_f16 v98, v180, v181, v98\n"
    "v_pk_add_u16 v187, s89, v187 clamp\n"
    "v_mfma_f32_16x16x32_bf16 v[16:19], a[120:123], v[188:191], v[16:19]\n"
    "v_pk_add_u16 v180, s89, v180 clamp\n"
    "v_pk_add_u16 v184, s89, v184 clamp\n"
    "v_mfma_f32_16x16x32_bf16 v[64:67], a[32:35], v[188:191], v[64:67]\n"
    "v_accvgpr_write_b32 a177, v189\n"
    "v_accvgpr_write_b32 a190, v202\n"
    "v_mfma_f32_16x16x32_bf16 v[32:35], a[144:147], v[188:191], v[32:35]\n"
    "s_waitcnt vmcnt(14)\n"
    "buffer_load_dwordx4 v1, s[16:19], s67 offen offset:2048 sc1 lds\n"
    "v_smfmac_f32_16x16x64_bf16 v[112:115], v[52:55], v[76:83], v253\n"
    "s_barrier\n"
    "ds_read_b128 a[48:51], v252 offset:24576\n"
    "v_mfma_f32_16x16x32_bf16 v[48:51], a[8:11], v[188:191], v[48:51]\n"
    "ds_read_b128 a[68:71], v252 offset:34816\n"
    "ds_read_b128 a[72:75], v252 offset:25600\n"
    "v_mfma_f32_16x16x32_bf16 v[32:35], a[140:143], v[184:187], v[32:35]\n"
    "v_pk_add_u16 v181, s89, v181 clamp\n"
    "v_accvgpr_write_b32 a188, v200\n"
    "v_mfma_f32_16x16x32_bf16 v[48:51], a[4:7], v[184:187], v[48:51]\n"
    "v_pk_maximum3_f16 v98, v182, v183, v98\n"
    "v_accvgpr_write_b32 a176, v188\n"
    "v_mfma_f32_16x16x32_bf16 v[64:67], a[28:31], v[184:187], v[64:67]\n"
    "v_pk_add_u16 v183, s89, v183 clamp\n"
    "v_pk_add_u16 v182, s89, v182 clamp\n"
    "v_mfma_f32_16x16x32_bf16 v[16:19], a[116:119], v[184:187], v[16:19]\n"
    "ds_read_b128 a[52:55], v252 offset:26624\n"
    "ds_read_b128 a[76:79], v252 offset:27648\n"
    "v_mfma_f32_16x16x32_bf16 v[48:51], a[0:3], v[180:183], v[48:51]\n"
    "buffer_load_dwordx4 v1, s[16:19], s67 offen offset:3072 sc1 lds\n"
    "v_mfma_f32_16x16x32_bf16 v[32:35], a[136:139], v[180:183], v[32:35]\n"
    "ds_read_b128 a[92:95], v252 offset:35840\n"
    "ds_read_b128 a[88:91], v252 offset:33792\n"
    "v_mfma_f32_16x16x32_bf16 v[16:19], a[112:115], v[180:183], v[16:19]\n"
    "s_waitcnt lgkmcnt(6)\n"
    "v_accvgpr_write_b32 a180, v192\n"
    "v_accvgpr_write_b32 a181, v193\n"
    "v_mfma_f32_16x16x32_bf16 v[4:7], a[48:51], a[192:195], v[100:103]\n"
    "s_waitcnt lgkmcnt(4)\n"
    "ds_read_b128 a[60:63], v252 offset:30720\n"
    "ds_read_b128 a[56:59], v252 offset:28672\n"
    "v_mfma_f32_16x16x32_bf16 v[20:23], a[72:75], a[192:195], v[104:107]\n"
    "ds_read_b128 a[64:67], v252 offset:32768\n"
    "v_accvgpr_write_b32 a191, v203\n"
    "v_mfma_f32_16x16x32_bf16 v[28:31], a[72:75], a[240:243], v[104:107]\n"
    "s_add_u32 m0, s68, 4096\n"
    "v_accvgpr_write_b32 a175, v187\n"
    "v_accvgpr_write_b32 a182, v194\n"
    "v_mfma_f32_16x16x32_bf16 v[12:15], a[48:51], a[240:243], v[100:103]\n"
    "v_accvgpr_write_b32 a178, v190\n"
    "v_accvgpr_write_b32 a171, v183\n"
    "v_mfma_f32_16x16x32_bf16 v[8:11], a[68:71], a[236:239], v[100:103]\n"
    "buffer_load_dwordx4 v1, s[16:19], s83 offen offset:0 sc1 lds\n"
    "v_mfma_f32_16x16x32_bf16 v[64:67], a[24:27], v[180:183], v[64:67]\n"
    "s_waitcnt lgkmcnt(6)\n"
    "ds_read_b128 a[80:83], v252 offset:29696\n"
    "v_accvgpr_write_b32 a172, v184\n"
    "v_mfma_f32_16x16x32_bf16 v[4:7], a[52:55], a[196:199], v[4:7]\n"
    "s_waitcnt lgkmcnt(5)\n"
    "v_exp_f32 v16, v16\n"
    "v_mfma_f32_16x16x32_bf16 v[24:27], a[92:95], a[236:239], v[104:107]\n"
    "s_waitcnt lgkmcnt(4)\n"
    "v_accvgpr_write_b32 a174, v186\n"
    "v_accvgpr_write_b32 a169, v181\n"
    "v_mfma_f32_16x16x32_bf16 v[24:27], a[88:91], a[232:235], v[24:27]\n"
    "s_waitcnt lgkmcnt(2)\n"
    "ds_read_b128 a[84:87], v252 offset:31744\n"
    "v_mfma_f32_16x16x32_bf16 v[4:7], a[56:59], a[200:203], v[4:7]\n"
    "s_waitcnt lgkmcnt(2)\n"
    "v_accvgpr_write_b32 a189, v201\n"
    "v_accvgpr_write_b32 a179, v191\n"
    "v_mfma_f32_16x16x32_bf16 v[8:11], a[64:67], a[232:235], v[8:11]\n"
    "v_exp_f32 v17, v17\n"
    "v_mfma_f32_16x16x32_bf16 v[28:31], a[76:79], a[244:247], v[28:31]\n"
    "buffer_load_dwordx4 v1, s[16:19], s83 offen offset:1024 sc1 lds\n"
    "v_mfma_f32_16x16x32_bf16 v[20:23], a[76:79], a[196:199], v[20:23]\n"
    "v_accvgpr_write_b32 a184, v196\n"
    "v_accvgpr_write_b32 a170, v182\n"
    "v_mfma_f32_16x16x32_bf16 v[12:15], a[52:55], a[244:247], v[12:15]\n"
    "v_accvgpr_write_b32 a185, v197\n"
    "v_accvgpr_write_b32 a186, v198\n"
    "v_mfma_f32_16x16x32_bf16 v[12:15], a[56:59], a[248:251], v[12:15]\n"
    "v_cvt_pk_bf16_f32 v92, v16, v17\n"
    "v_mfma_f32_16x16x32_bf16 v[12:15], a[60:63], a[252:255], v[12:15]\n"
    "s_add_u32 m0, s70, 0\n"
    "s_add_u32 s83, s67, 24576\n"
    "v_accvgpr_write_b32 a168, v180\n"
    "v_mfma_f32_16x16x32_bf16 v[12:15], a[64:67], a[160:163], v[12:15]\n"
    "s_waitcnt lgkmcnt(1)\n"
    "v_accvgpr_write_b32 a183, v195\n"
    "v_pk_add_u16 v98, s89, v98\n"
    "v_mfma_f32_16x16x32_bf16 v[28:31], a[80:83], a[248:251], v[28:31]\n"
    "s_waitcnt lgkmcnt(0)\n"
    "v_accvgpr_write_b32 a173, v185\n"
    "v_accvgpr_write_b32 a187, v199\n"
    "v_mfma_f32_16x16x32_bf16 v[28:31], a[84:87], a[252:255], v[28:31]\n"
    "buffer_load_dwordx4 v1, s[16:19], s83 offen offset:0 sc1 lds\n"
    "v_mfma_f32_16x16x32_bf16 v[4:7], a[60:63], a[204:207], v[4:7]\n"
    "v_exp_f32 v18, v18\n"
    "v_mfma_f32_16x16x32_bf16 v[8:11], a[60:63], a[228:231], v[8:11]\n"
    "v_exp_f32 v19, v19\n"
    "v_mfma_f32_16x16x32_bf16 v[8:11], a[56:59], a[224:227], v[8:11]\n"
    "v_exp_f32 v32, v32\n"
    "v_mfma_f32_16x16x32_bf16 v[8:11], a[52:55], a[220:223], v[8:11]\n"
    "v_exp_f32 v35, v35\n"
    "v_mfma_f32_16x16x32_bf16 v[28:31], a[88:91], a[160:163], v[28:31]\n"
    "v_cvt_pk_bf16_f32 v93, v18, v19\n"
    "v_mfma_f32_16x16x32_bf16 v[24:27], a[84:87], a[228:231], v[24:27]\n"
    "v_exp_f32 v34, v34\n"
    "v_mfma_f32_16x16x32_bf16 v[20:23], a[80:83], a[200:203], v[20:23]\n"
    "v_exp_f32 v67, v67\n"
    "v_mfma_f32_16x16x32_bf16 v[16:19], a[68:71], a[188:191], v[100:103]\n"
    "buffer_load_dwordx4 v1, s[16:19], s83 offen offset:1024 sc1 lds\n"
    "v_mfma_f32_16x16x32_bf16 v[4:7], a[64:67], a[208:211], v[4:7]\n"
    "v_exp_f32 v33, v33\n"
    "v_mfma_f32_16x16x32_bf16 v[20:23], a[84:87], a[204:207], v[20:23]\n"
    "v_mfma_f32_16x16x32_bf16 v[20:23], a[88:91], a[208:211], v[20:23]\n"
    "v_mfma_f32_16x16x32_bf16 v[24:27], a[80:83], a[224:227], v[24:27]\n"
    "v_cvt_pk_bf16_f32 v95, v34, v35\n"
    "v_cvt_pk_bf16_f32 v94, v32, v33\n"
    "v_mfma_f32_16x16x32_bf16 v[32:35], a[92:95], a[188:191], v[104:107]\n"
    "s_waitcnt vmcnt(14)\n"
    "v_exp_f32 v66, v66\n"
    "v_mfma_f32_16x16x32_bf16 v[16:19], a[64:67], a[184:187], v[16:19]\n"
    "s_barrier\n"
    "ds_read_b128 a[112:115], v249 offset:0\n"
    "v_mfma_f32_16x16x32_bf16 v[32:35], a[88:91], a[184:187], v[32:35]\n"
    "v_exp_f32 v51, v51\n"
    "v_mfma_f32_16x16x32_bf16 v[32:35], a[84:87], a[180:183], v[32:35]\n"
    "buffer_load_dwordx4 v1, s[16:19], s83 offen offset:2048 sc1 lds\n"
    "v_mfma_f32_16x16x32_bf16 v[16:19], a[60:63], a[180:183], v[16:19]\n"
    "ds_read_b128 a[144:147], v249 offset:4096\n"
    "ds_read_b128 a[140:143], v249 offset:15360\n"
    "v_mfma_f32_16x16x32_bf16 v[16:19], a[56:59], a[176:179], v[16:19]\n"
    "ds_read_b128 a[152:155], v249 offset:5120\n"
    "ds_read_b128 a[132:135], v249 offset:14336\n"
    "v_mfma_f32_16x16x32_bf16 v[4:7], a[68:71], a[212:215], v[4:7]\n"
    "ds_read_b128 a[120:123], v249 offset:1024\n"
    "ds_read_b128 a[128:131], v249 offset:2048\n"
    "v_mfma_f32_16x16x32_bf16 v[8:11], a[48:51], a[216:219], v[8:11]\n"
    "ds_read_b128 a[124:127], v249 offset:13312\n"
    "ds_read_b128 a[136:139], v249 offset:3072\n"
    "v_mfma_f32_16x16x32_bf16 v[24:27], a[76:79], a[220:223], v[24:27]\n"
    "v_exp_f32 v65, v65\n"
    "v_mfma_f32_16x16x32_bf16 v[12:15], a[68:71], a[164:167], v[12:15]\n"
    "ds_read_b128 a[116:119], v249 offset:12288\n"
    "v_mfma_f32_16x16x32_bf16 v[32:35], a[80:83], a[176:179], v[32:35]\n"
    "buffer_load_dwordx4 v1, s[16:19], s83 offen offset:3072 sc1 lds\n"
    "v_mfma_f32_16x16x32_bf16 v[20:23], a[92:95], a[212:215], v[20:23]\n"
    "v_mfma_f32_16x16x32_bf16 v[16:19], a[52:55], a[172:175], v[16:19]\n"
    "s_waitcnt lgkmcnt(7)\n"
    "v_exp_f32 v50, v50\n"
    "v_mfma_f32_16x16x32_bf16 v[152:155], a[140:143], v[80:83], 0\n"
    "v_and_b32 v98, 0x80008000, v98\n"
    "v_mfma_f32_16x16x32_bf16 v[24:27], a[72:75], a[216:219], v[24:27]\n"
    "s_add_u32 m0, s70, 4096\n"
    "s_add_u32 s83, s67, 28672\n"
    "s_waitcnt lgkmcnt(2)\n"
    "v_mfma_f32_16x16x32_bf16 v[128:131], a[124:127], v[72:75], 0\n"
    "ds_read_b128 a[148:151], v249 offset:16384\n"
    "v_mfma_f32_16x16x32_bf16 v[148:151], a[132:135], v[80:83], 0\n"
    "s_add_u32 s76, s52, s60\n"
    "v_mfma_f32_16x16x32_bf16 v[28:31], a[92:95], a[164:167], v[28:31]\n"
    "buffer_load_dwordx4 v1, s[16:19], s83 offen offset:0 sc1 lds\n"
    "v_mfma_f32_16x16x32_bf16 v[160:163], a[124:127], v[88:91], 0\n"
    "s_waitcnt lgkmcnt(2)\n"
    "v_exp_f32 v48, v48\n"
    "v_mfma_f32_16x16x32_bf16 v[136:139], a[136:139], v[68:71], 0\n"
    "v_exp_f32 v49, v49\n"
    "v_mfma_f32_16x16x32_bf16 a[32:35], a[144:147], v[92:95], 0\n"
    "v_exp_f32 v27, v27\n"
    "v_mfma_f32_16x16x32_bf16 v[136:139], a[140:143], v[72:75], v[136:139]\n"
    "v_cmp_ne_u32 s[34:35], 0, v98\n"
    "v_mfma_f32_16x16x32_bf16 v[156:159], a[112:115], v[84:87], 0\n"
    "v_mfma_f32_16x16x32_bf16 v[132:135], a[128:131], v[68:71], 0\n"
    "v_cvt_pk_bf16_f32 v96, v48, v49\n"
    "v_mfma_f32_16x16x32_bf16 v[144:147], a[120:123], v[76:79], 0\n"
    "buffer_load_dwordx4 v1, s[16:19], s83 offen offset:1024 sc1 lds\n"
    "v_mfma_f32_16x16x32_bf16 v[16:19], a[48:51], a[168:171], v[16:19]\n"
    "s_waitcnt lgkmcnt(0)\n"
    "v_exp_f32 v20, v20\n"
    "v_mfma_f32_16x16x32_bf16 a[16:19], a[148:151], v[88:91], 0\n"
    "v_exp_f32 v64, v64\n"
    "v_mfma_f32_16x16x32_bf16 v[176:179], a[120:123], v[92:95], 0\n"
    "v_mfma_f32_16x16x32_bf16 v[124:127], a[112:115], v[68:71], 0\n"
    "v_exp_f32 v4, v4\n"
    "v_mfma_f32_16x16x32_bf16 v[156:159], a[116:119], v[88:91], v[156:159]\n"
    "v_exp_f32 v7, v7\n"
    "v_mfma_f32_16x16x32_bf16 v[188:191], a[148:151], v[72:75], 0\n"
    "v_exp_f32 v10, v10\n"
    "v_mfma_f32_16x16x32_bf16 v[32:35], a[76:79], a[172:175], v[32:35]\n"
    "v_mfma_f32_16x16x32_bf16 v[192:195], a[152:155], v[68:71], 0\n"
    "s_add_u32 s78, s52, s62\n"
    "v_exp_f32 v11, v11\n"
    "v_mfma_f32_16x16x32_bf16 v[124:127], a[116:119], v[72:75], v[124:127]\n"
    "s_waitcnt vmcnt(12)\n"
    "v_cvt_pk_bf16_f32 v97, v50, v51\n"
    "v_cvt_pk_bf16_f32 v99, v66, v67\n"
    "v_mfma_f32_16x16x32_bf16 v[164:167], a[128:131], v[84:87], 0\n"
    "v_exp_f32 v19, v19\n"
    "v_mfma_f32_16x16x32_bf16 v[160:163], a[120:123], v[84:87], v[160:163]\n"
    "v_cvt_pk_u16_u32 v204, v204, v208\n"
    "v_cvt_pk_bf16_f32 v98, v64, v65\n"
    "v_mfma_f32_16x16x32_bf16 a[20:23], a[152:155], v[84:87], 0\n"
    "v_cvt_pk_u16_u32 v214, v214, v218\n"
    "v_mfma_f32_16x16x32_bf16 v[148:151], a[128:131], v[76:79], v[148:151]\n"
    "v_cvt_pk_u16_u32 v207, v207, v211\n"
    "v_cvt_pk_u16_u32 v205, v205, v209\n"
    "v_mfma_f32_16x16x32_bf16 v[128:131], a[120:123], v[68:71], v[128:131]\n"
    "ds_read_b128 a[156:159], v249 offset:17408\n"
    "v_mfma_f32_16x16x32_bf16 v[172:175], a[116:119], v[96:99], 0\n"
    "s_add_u32 s77, s52, s61\n"
    "v_exp_f32 v25, v25\n"
    "v_mfma_f32_16x16x32_bf16 v[132:135], a[132:135], v[72:75], v[132:135]\n"
    "v_mfma_f32_16x16x32_bf16 v[144:147], a[124:127], v[80:83], v[144:147]\n"
    "v_mfma_f32_16x16x32_bf16 v[180:183], a[132:135], v[96:99], 0\n"
    "v_exp_f32 v17, v17\n"
    "v_mfma_f32_16x16x32_bf16 v[168:171], a[136:139], v[84:87], 0\n"
    "v_exp_f32 v9, v9\n"
    "v_mfma_f32_16x16x32_bf16 v[164:167], a[132:135], v[88:91], v[164:167]\n"
    "v_mfma_f32_16x16x32_bf16 v[152:155], a[136:139], v[76:79], v[152:155]\n"
    "v_exp_f32 v18, v18\n"
    "v_mfma_f32_16x16x32_bf16 v[176:179], a[124:127], v[96:99], v[176:179]\n"
    "ds_read_b128 a[132:135], v249 offset:20480\n"
    "v_mfma_f32_16x16x32_bf16 v[140:143], a[116:119], v[80:83], 0\n"
    "v_exp_f32 v31, v31\n"
    "v_mfma_f32_16x16x32_bf16 v[188:191], a[144:147], v[68:71], v[188:191]\n"
    "v_mfma_f32_16x16x32_bf16 v[168:171], a[140:143], v[88:91], v[168:171]\n"
    "v_exp_f32 v12, v12\n"
    "v_mfma_f32_16x16x32_bf16 v[180:183], a[128:131], v[92:95], v[180:183]\n"
    "v_cvt_pk_u16_u32 v212, v212, v216\n"
    "v_cvt_pk_u16_u32 v223, v223, v227\n"
    "v_mfma_f32_16x16x32_bf16 v[140:143], a[112:115], v[76:79], v[140:143]\n"
    "s_waitcnt lgkmcnt(1)\n"
    "v_exp_f32 v21, v21\n"
    "v_mfma_f32_16x16x32_bf16 a[36:39], a[156:159], v[96:99], 0\n"
    "ds_read_b128 a[128:131], v249 offset:8192\n"
    "v_cvt_pk_u16_u32 v222, v222, v226\n"
    "v_mfma_f32_16x16x32_bf16 v[172:175], a[112:115], v[92:95], v[172:175]\n"
    "v_or3_b32 v243, v204, v205, v243\n"
    "s_waitcnt lgkmcnt(1)\n"
    "v_mfma_f32_16x16x32_bf16 a[96:99], a[132:135], v[96:99], 0\n"
    "v_mfma_f32_16x16x32_bf16 a[20:23], a[156:159], v[88:91], a[20:23]\n"
    "ds_read_b128 a[116:119], v249 offset:18432\n"
    "v_mfma_f32_16x16x32_bf16 a[16:19], a[144:147], v[84:87], a[16:19]\n"
    "v_exp_f32 v15, v15\n"
    "v_mfma_f32_16x16x32_bf16 a[0:3], a[144:147], v[76:79], 0\n"
    "ds_read_b128 a[120:123], v249 offset:7168\n"
    "v_mfma_f32_16x16x32_bf16 v[184:187], a[140:143], v[96:99], 0\n"
    "v_exp_f32 v26, v26\n"
    "v_mfma_f32_16x16x32_bf16 a[64:67], a[132:135], v[80:83], 0\n"
    "v_cvt_pk_u16_u32 v206, v206, v210\n"
    "v_mfma_f32_16x16x32_bf16 a[36:39], a[152:155], v[92:95], a[36:39]\n"
    "ds_read_b128 a[124:127], v249 offset:19456\n"
    "ds_read_b128 a[140:143], v249 offset:21504\n"
    "v_mfma_f32_16x16x32_bf16 a[0:3], a[148:151], v[80:83], a[0:3]\n"
    "v_cvt_pk_u16_u32 v220, v220, v224\n"
    "v_cvt_pk_u16_u32 v221, v221, v225\n"
    "v_mfma_f32_16x16x32_bf16 v[184:187], a[136:139], v[92:95], v[184:187]\n"
    "s_waitcnt lgkmcnt(4)\n"
    "v_or3_b32 v243, v206, v207, v243\n"
    "v_cvt_pk_u16_u32 v213, v213, v217\n"
    "v_mfma_f32_16x16x32_bf16 a[80:83], a[128:131], v[84:87], 0\n"
    "s_add_u32 s67, s67, 49152\n"
    "ds_read_b128 a[136:139], v249 offset:9216\n"
    "ds_read_b128 a[144:147], v249 offset:10240\n"
    "v_mfma_f32_16x16x32_bf16 a[64:67], a[128:131], v[76:79], a[64:67]\n"
    "v_exp_f32 v23, v23\n"
    "v_mfma_f32_16x16x32_bf16 a[32:35], a[148:151], v[96:99], a[32:35]\n"
    "v_mfma_f32_16x16x32_bf16 a[4:7], a[156:159], v[80:83], 0\n"
    "v_mfma_f32_16x16x32_bf16 a[4:7], a[152:155], v[76:79], a[4:7]\n"
    "v_or3_b32 v243, v212, v213, v243\n"
    "v_cvt_pk_u16_u32 v215, v215, v219\n"
    "v_mfma_f32_16x16x32_bf16 v[192:195], a[156:159], v[72:75], v[192:195]\n"
    "v_exp_f32 v28, v28\n"
    "v_mfma_f32_16x16x32_bf16 a[96:99], a[128:131], v[92:95], a[96:99]\n"
    "s_waitcnt lgkmcnt(3)\n"
    "ds_read_b128 a[112:115], v249 offset:6144\n"
    "ds_read_b128 a[148:151], v249 offset:22528\n"
    "v_mfma_f32_16x16x32_bf16 v[200:203], a[124:127], v[72:75], 0\n"
    "v_exp_f32 v16, v16\n"
    "v_mfma_f32_16x16x32_bf16 a[12:15], a[120:123], v[76:79], 0\n"
    "buffer_load_dwordx2 v[236:237], v241, s[36:39], s81 offen offset:0 sc1\n"
    "v_mfma_f32_16x16x32_bf16 v[200:203], a[120:123], v[68:71], v[200:203]\n"
    "s_waitcnt lgkmcnt(4)\n"
    "v_add_u32 v251, 1536, v251\n"
    "v_mfma_f32_16x16x32_bf16 a[100:103], a[140:143], v[96:99], 0\n"
    "v_exp_f32 v29, v29\n"
    "v_mfma_f32_16x16x32_bf16 a[28:31], a[124:127], v[88:91], 0\n"
    "buffer_store_dword v204, v244, s[20:23], s76 offen offset:0 sc1\n"
    "v_exp_f32 v30, v30\n"
    "v_mfma_f32_16x16x32_bf16 v[32:35], a[72:75], a[168:171], v[32:35]\n"
    "v_mfma_f32_16x16x32_bf16 a[40:43], a[116:119], v[96:99], 0\n"
    "v_exp_f32 v13, v13\n"
    "v_mfma_f32_16x16x32_bf16 a[68:71], a[140:143], v[80:83], 0\n"
    "s_waitcnt lgkmcnt(3)\n"
    "v_mfma_f32_16x16x32_bf16 a[52:55], a[136:139], v[68:71], 0\n"
    "buffer_store_dword v205, v244, s[20:23], s76 offen offset:1024 sc1\n"
    "v_exp_f32 v22, v22\n"
    "v_mfma_f32_16x16x32_bf16 a[84:87], a[136:139], v[84:87], 0\n"
    "v_exp_f32 v24, v24\n"
    "v_mfma_f32_16x16x32_bf16 a[12:15], a[124:127], v[80:83], a[12:15]\n"
    "ds_read_b128 a[156:159], v249 offset:23552\n"
    "v_mfma_f32_16x16x32_bf16 a[44:47], a[120:123], v[92:95], 0\n"
    "v_exp_f32 v35, v35\n"
    "v_mfma_f32_16x16x32_bf16 a[80:83], a[132:135], v[88:91], a[80:83]\n"
    "buffer_store_dword v206, v244, s[20:23], s76 offen offset:2048 sc1\n"
    "ds_read_b128 v[100:103], v250 offset:1664\n"
    "v_mfma_f32_16x16x32_bf16 a[84:87], a[140:143], v[88:91], a[84:87]\n"
    "v_mfma_f32_16x16x32_bf16 a[8:11], a[116:119], v[80:83], 0\n"
    "s_waitcnt lgkmcnt(2)\n"
    "buffer_store_dword v207, v244, s[20:23], s76 offen offset:3072 sc1\n"
    "v_or3_b32 v243, v214, v215, v243\n"
    "v_or3_b32 v243, v220, v221, v243\n"
    "v_mfma_f32_16x16x32_bf16 a[88:91], a[148:151], v[88:91], 0\n"
    "v_mfma_f32_16x16x32_bf16 a[44:47], a[124:127], v[96:99], a[44:47]\n"
    "buffer_store_dword v212, v244, s[20:23], s77 offen offset:0 sc1\n"
    "v_mfma_f32_16x16x32_bf16 a[28:31], a[120:123], v[84:87], a[28:31]\n"
    "v_mfma_f32_16x16x32_bf16 a[24:27], a[112:115], v[84:87], 0\n"
    "ds_read_b128 a[152:155], v249 offset:11264\n"
    "v_mfma_f32_16x16x32_bf16 a[40:43], a[112:115], v[92:95], a[40:43]\n"
    "buffer_store_dword v213, v244, s[20:23], s77 offen offset:1024 sc1\n"
    "v_exp_f32 v6, v6\n"
    "v_mfma_f32_16x16x32_bf16 a[48:51], a[128:131], v[68:71], 0\n"
    "s_waitcnt lgkmcnt(2)\n"
    "v_exp_f32 v8, v8\n"
    "v_mfma_f32_16x16x32_bf16 a[76:79], a[156:159], v[80:83], 0\n"
    "buffer_store_dword v214, v244, s[20:23], s77 offen offset:2048 sc1\n"
    "v_mfma_f32_16x16x32_bf16 a[104:107], a[144:147], v[92:95], 0\n"
    "v_exp_f32 v34, v34\n"
    "v_mfma_f32_16x16x32_bf16 a[52:55], a[140:143], v[72:75], a[52:55]\n"
    "buffer_store_dword v215, v244, s[20:23], s77 offen offset:3072 sc1\n"
    "v_exp_f32 v14, v14\n"
    "v_mfma_f32_16x16x32_bf16 a[48:51], a[132:135], v[72:75], a[48:51]\n"
    "v_mfma_f32_16x16x32_bf16 a[24:27], a[116:119], v[88:91], a[24:27]\n"
    "buffer_store_dword v220, v244, s[20:23], s78 offen offset:0 sc1\n"
    "ds_read_b128 a[120:123], v3 offset:16384\n"
    "v_mfma_f32_16x16x32_bf16 v[196:199], a[112:115], v[68:71], 0\n"
    "v_mfma_f32_16x16x32_bf16 v[196:199], a[116:119], v[72:75], v[196:199]\n"
    "v_mfma_f32_16x16x32_bf16 a[8:11], a[112:115], v[76:79], a[8:11]\n"
    "s_waitcnt lgkmcnt(1)\n"
    "ds_read_b128 a[132:135], v3 offset:22528\n"
    "v_mfma_f32_16x16x32_bf16 a[76:79], a[152:155], v[76:79], a[76:79]\n"
    "buffer_store_dword v221, v244, s[20:23], s78 offen offset:1024 sc1\n"
    "v_mfma_f32_16x16x32_bf16 a[108:111], a[156:159], v[96:99], 0\n"
    "ds_read_b128 a[124:127], v3 offset:18432\n"
    "ds_read_b128 a[128:131], v3 offset:20480\n"
    "v_mfma_f32_16x16x32_bf16 a[92:95], a[152:155], v[84:87], 0\n"
    "v_or3_b32 v243, v222, v223, v243\n"
    "v_mfma_f32_16x16x32_bf16 a[92:95], a[156:159], v[88:91], a[92:95]\n"
    "ds_read_b128 a[112:115], v3 offset:12288\n"
    "ds_read_b128 a[116:119], v3 offset:14336\n"
    "v_mfma_f32_16x16x32_bf16 a[72:75], a[144:147], v[76:79], 0\n"
    "s_waitcnt vmcnt(11)\n"
    "ds_add_u32 v247, v246\n"
    "v_mfma_f32_16x16x32_bf16 a[56:59], a[148:151], v[72:75], 0\n"
    "v_exp_f32 v32, v32\n"
    "v_mfma_f32_16x16x32_bf16 a[60:63], a[152:155], v[68:71], 0\n"
    "v_and_b32 v208, 0xff00ff00, v243\n"
    "v_add_u32 v250, 1536, v250\n"
    "v_mfma_f32_16x16x32_bf16 a[68:71], a[136:139], v[76:79], a[68:71]\n"
    "v_exp_f32 v33, v33\n"
    "v_mfma_f32_16x16x32_bf16 a[88:91], a[144:147], v[84:87], a[88:91]\n"
    "buffer_store_dword v222, v244, s[20:23], s78 offen offset:2048 sc1\n"
    "v_exp_f32 v5, v5\n"
    "v_mfma_f32_16x16x32_bf16 a[72:75], a[148:151], v[80:83], a[72:75]\n"
    "v_cmp_ne_u32 vcc, 0, v208\n"
    "v_mfma_f32_16x16x32_bf16 a[104:107], a[148:151], v[96:99], a[104:107]\n"
    "v_mfma_f32_16x16x32_bf16 a[108:111], a[152:155], v[92:95], a[108:111]\n"
    "buffer_store_dword v223, v244, s[20:23], s78 offen offset:3072 sc1\n"
    "v_add_u32 v3, 49152, v252\n"
    "v_mfma_f32_16x16x32_bf16 a[56:59], a[144:147], v[68:71], a[56:59]\n"
    "s_waitcnt lgkmcnt(1)\n"
    "v_cndmask_b32 v208, 2.0, v241, vcc\n"
    "v_mfma_f32_16x16x32_bf16 a[60:63], a[156:159], v[72:75], a[60:63]\n"
    "v_mfma_f32_16x16x32_bf16 a[100:103], a[136:139], v[92:95], a[100:103]\n"
    "buffer_store_dwordx2 v[238:239], v208, s[36:39], 0 offen offset:0 sc1\n"
    "v_smfmac_f32_16x16x64_bf16 v[120:123], v[52:55], v[92:99], v253\n"
    "s_branch .LPV\n"
    ".fill ((0x20000 - (.LwKe - .LwK) - (. - _Z8attn_fwdPKtPKiS2_S0_S0_S0_S0_PcPt)) & 4095) / 4, 4, 0xBF800000\n"
    ".LwK:\n"
    "s_branch .LwKe\n"
    ".LPK:\n"
    "v_mfma_f32_16x16x32_bf16 v[36:39], a[112:115], a[192:195], v[100:103]\n"
    "s_add_u32 s71, s71, 98304\n"
    "ds_read_b128 a[136:139], v2 offset:13312\n"
    "v_cvt_pk_bf16_f32 v68, v4, v5\n"
    "v_mfma_f32_16x16x32_bf16 v[36:39], a[116:119], a[196:199], v[36:39]\n"
    "s_add_u32 s75, s71, s63\n"
    "ds_read_b128 a[140:143], v2 offset:15360\n"
    "v_cvt_pk_bf16_f32 v69, v6, v7\n"
    "v_mfma_f32_16x16x32_bf16 v[36:39], a[120:123], a[200:203], v[36:39]\n"
    "s_add_u32 s76, s75, 6144\n"
    "ds_read_b128 a[144:147], v2 offset:17408\n"
    "v_cvt_pk_bf16_f32 v76, v8, v9\n"
    "s_nop 0\n"
    "v_mfma_f32_16x16x32_bf16 v[36:39], a[124:127], a[204:207], v[36:39]\n"
    "s_add_u32 s77, s75, 24576\n"
    "ds_read_b128 a[148:151], v2 offset:19456\n"
    "v_cvt_pk_bf16_f32 v77, v10, v11\n"
    "v_mfma_f32_16x16x32_bf16 v[36:39], a[128:131], a[208:211], v[36:39]\n"
    "s_add_u32 s78, s75, 30720\n"
    "ds_read_b128 a[152:155], v2 offset:21504\n"
    "v_cvt_pk_bf16_f32 v84, v12, v13\n"
    "v_mfma_f32_16x16x32_bf16 v[36:39], a[132:135], a[212:215], v[36:39]\n"
    "ds_read_b128 a[156:159], v2 offset:23552\n"
    "v_cvt_pk_bf16_f32 v85, v14, v15\n"
    "v_mfma_f32_16x16x32_bf16 v[40:43], a[132:135], a[236:239], v[100:103]\n"
    "ds_read_b128 v[104:107], v250 offset:144\n"
    "v_cvt_pk_bf16_f32 v92, v16, v17\n"
    "v_mfma_f32_16x16x32_bf16 v[40:43], a[128:131], a[232:235], v[40:43]\n"
    "s_add_u32 s68, s68, 24576\n"
    "v_cvt_pk_bf16_f32 v93, v18, v19\n"
    "v_cvt_pk_bf16_f32 v70, v20, v21\n"
    "v_mfma_f32_16x16x32_bf16 v[40:43], a[124:127], a[228:231], v[40:43]\n"
    "s_cmp_ge_u32 s68, s69\n"
    "s_cselect_b32 s68, s65, s68\n"
    "v_cvt_pk_bf16_f32 v71, v22, v23\n"
    "v_mfma_f32_16x16x32_bf16 v[40:43], a[120:123], a[224:227], v[40:43]\n"
    "v_cvt_pk_bf16_f32 v78, v24, v25\n"
    "v_cvt_pk_bf16_f32 v79, v26, v27\n"
    "v_mfma_f32_16x16x32_bf16 v[40:43], a[116:119], a[220:223], v[40:43]\n"
    "v_cvt_pk_bf16_f32 v86, v28, v29\n"
    "v_cvt_pk_bf16_f32 v87, v30, v31\n"
    "v_mfma_f32_16x16x32_bf16 v[40:43], a[112:115], a[216:219], v[40:43]\n"
    "v_cvt_pk_bf16_f32 v94, v32, v33\n"
    "v_cvt_pk_bf16_f32 v95, v34, v35\n"
    "v_mfma_f32_16x16x32_bf16 v[44:47], a[112:115], a[240:243], v[100:103]\n"
    "v_exp_f32_e64 v36, v36\n"
    "v_mfma_f32_16x16x32_bf16 v[44:47], a[116:119], a[244:247], v[44:47]\n"
    "v_exp_f32_e64 v37, v37\n"
    "v_mfma_f32_16x16x32_bf16 v[44:47], a[120:123], a[248:251], v[44:47]\n"
    "v_exp_f32_e64 v38, v38\n"
    "v_mfma_f32_16x16x32_bf16 v[44:47], a[124:127], a[252:255], v[44:47]\n"
    "v_exp_f32_e64 v39, v39\n"
    "v_mfma_f32_16x16x32_bf16 v[44:47], a[128:131], a[160:163], v[44:47]\n"
    "v_cvt_pk_bf16_f32 v72, v36, v37\n"
    "v_cvt_pk_bf16_f32 v73, v38, v39\n"
    "v_mfma_f32_16x16x32_bf16 v[44:47], a[132:135], a[164:167], v[44:47]\n"
    "v_exp_f32_e64 v40, v40\n"
    "v_mfma_f32_16x16x32_bf16 v[48:51], a[132:135], a[188:191], v[100:103]\n"
    "ds_read_b32 v245, v247\n"
    "v_mfma_f32_16x16x32_bf16 v[48:51], a[128:131], a[184:187], v[48:51]\n"
    "s_add_u32 m0, s68, .Lend-.Lend2+(0)\n"
    "v_exp_f32_e64 v41, v41\n"
    "v_mfma_f32_16x16x32_bf16 v[48:51], a[124:127], a[180:183], v[48:51]\n"
    "v_exp_f32 v42, v42\n"
    "v_mfma_f32_16x16x32_bf16 v[48:51], a[120:123], a[176:179], v[48:51]\n"
    "v_exp_f32 v43, v43\n"
    "v_mfma_f32_16x16x32_bf16 v[48:51], a[116:119], a[172:175], v[48:51]\n"
    "s_waitcnt vmcnt(10)\n"
    "v_cmp_eq_u64 s[54:55], v[236:237], v[238:239]\n"
    "v_cvt_pk_bf16_f32 v80, v40, v41\n"
    "v_mfma_f32_16x16x32_bf16 v[48:51], a[112:115], a[168:171], v[48:51]\n"
    "s_waitcnt lgkmcnt(1)\n"
    "v_cvt_pk_bf16_f32 v81, v42, v43\n"
    "v_mfma_f32_16x16x32_bf16 v[52:55], a[136:139], a[192:195], v[104:107]\n"
    "s_waitcnt lgkmcnt(0)\n"
    "v_cmp_le_u32 s[56:57], s59, v245\n"
    "v_mfma_f32_16x16x32_bf16 v[52:55], a[140:143], a[196:199], v[52:55]\n"
    "v_exp_f32 v44, v44\n"
    "v_mfma_f32_16x16x32_bf16 v[52:55], a[144:147], a[200:203], v[52:55]\n"
    "s_and_b64 s[54:55], s[54:55], s[56:57]\n"
    "s_cmp_eq_u64 s[54:55], -1\n"
    "v_mfma_f32_16x16x32_bf16 v[52:55], a[148:151], a[204:207], v[52:55]\n"
    "s_cbranch_scc0 .Lpoll1\n"
    ".Lpoll1r:\n"
    "v_mfma_f32_16x16x32_bf16 v[52:55], a[152:155], a[208:211], v[52:55]\n"
    "buffer_load_dwordx4 v1, s[16:19], s67 offen offset:0 sc1 lds\n"
    "v_mfma_f32_16x16x32_bf16 v[52:55], a[156:159], a[212:215], v[52:55]\n"
    "ds_read_b128 a[112:115], v3 offset:0\n"
    "v_mfma_f32_16x16x32_bf16 v[56:59], a[156:159], a[236:239], v[104:107]\n"
    "ds_read_b128 a[116:119], v3 offset:2048\n"
    "v_mfma_f32_16x16x32_bf16 v[56:59], a[152:155], a[232:235], v[56:59]\n"
    "ds_read_b128 a[120:123], v3 offset:4096\n"
    "v_mfma_f32_16x16x32_bf16 v[56:59], a[148:151], a[228:231], v[56:59]\n"
    "buffer_load_dwordx4 v1, s[16:19], s67 offen offset:1024 sc1 lds\n"
    "v_mfma_f32_16x16x32_bf16 v[56:59], a[144:147], a[224:227], v[56:59]\n"
    "ds_read_b128 a[124:127], v3 offset:6144\n"
    "v_mfma_f32_16x16x32_bf16 v[56:59], a[140:143], a[220:223], v[56:59]\n"
    "ds_read_b128 a[128:131], v3 offset:8192\n"
    "v_mfma_f32_16x16x32_bf16 v[56:59], a[136:139], a[216:219], v[56:59]\n"
    "ds_read_b128 a[132:135], v3 offset:10240\n"
    "v_mfma_f32_16x16x32_bf16 v[60:63], a[136:139], a[240:243], v[104:107]\n"
    "buffer_load_dwordx4 v1, s[16:19], s67 offen offset:2048 sc1 lds\n"
    "v_mfma_f32_16x16x32_bf16 v[60:63], a[140:143], a[244:247], v[60:63]\n"
    "ds_read_b128 v[100:103], v250 offset:768\n"
    "v_mfma_f32_16x16x32_bf16 v[60:63], a[144:147], a[248:251], v[60:63]\n"
    "v_exp_f32 v52, v52\n"
    "v_mfma_f32_16x16x32_bf16 v[60:63], a[148:151], a[252:255], v[60:63]\n"
    "v_exp_f32 v53, v53\n"
    "v_mfma_f32_16x16x32_bf16 v[60:63], a[152:155], a[160:163], v[60:63]\n"
    "buffer_load_dwordx4 v1, s[16:19], s67 offen offset:3072 sc1 lds\n"
    "v_mfma_f32_16x16x32_bf16 v[60:63], a[156:159], a[164:167], v[60:63]\n"
    "s_add_u32 m0, s68, 4096\n"
    "v_exp_f32 v54, v54\n"
    "v_mfma_f32_16x16x32_bf16 v[64:67], a[156:159], a[188:191], v[104:107]\n"
    "s_add_u32 s83, s67, 4096\n"
    "v_exp_f32 v55, v55\n"
    "v_mfma_f32_16x16x32_bf16 v[64:67], a[152:155], a[184:187], v[64:67]\n"
    "v_cvt_pk_bf16_f32 v74, v52, v53\n"
    "v_cvt_pk_bf16_f32 v75, v54, v55\n"
    "v_mfma_f32_16x16x32_bf16 v[64:67], a[148:151], a[180:183], v[64:67]\n"
    "buffer_load_dwordx4 v1, s[16:19], s83 offen offset:0 sc1 lds\n"
    "v_mfma_f32_16x16x32_bf16 v[64:67], a[144:147], a[176:179], v[64:67]\n"
    "v_exp_f32 v56, v56\n"
    "v_mfma_f32_16x16x32_bf16 v[64:67], a[140:143], a[172:175], v[64:67]\n"
    "v_exp_f32 v57, v57\n"
    "v_mfma_f32_16x16x32_bf16 v[64:67], a[136:139], a[168:171], v[64:67]\n"
    "s_waitcnt lgkmcnt(0)\n"
    "v_exp_f32 v58, v58\n"
    "v_mfma_f32_16x16x32_bf16 v[4:7], a[112:115], a[192:195], v[100:103]\n"
    "buffer_load_dwordx4 v1, s[16:19], s83 offen offset:1024 sc1 lds\n"
    "v_mfma_f32_16x16x32_bf16 v[4:7], a[116:119], a[196:199], v[4:7]\n"
    "s_add_u32 m0, s64, 0\n"
    "ds_read_b128 a[136:139], v3 offset:1024\n"
    "v_cvt_pk_bf16_f32 v82, v56, v57\n"
    "v_mfma_f32_16x16x32_bf16 v[4:7], a[120:123], a[200:203], v[4:7]\n"
    "s_add_u32 s83, s67, 24576\n"
    "ds_read_b128 a[140:143], v3 offset:3072\n"
    "v_mfma_f32_16x16x32_bf16 v[4:7], a[124:127], a[204:207], v[4:7]\n"
    "ds_read_b128 a[144:147], v3 offset:5120\n"
    "v_mfma_f32_16x16x32_bf16 v[4:7], a[128:131], a[208:211], v[4:7]\n"
    "buffer_load_dwordx4 v1, s[16:19], s83 offen offset:0 sc1 lds\n"
    "v_mfma_f32_16x16x32_bf16 v[4:7], a[132:135], a[212:215], v[4:7]\n"
    "ds_read_b128 a[148:151], v3 offset:7168\n"
    "v_mfma_f32_16x16x32_bf16 v[8:11], a[132:135], a[236:239], v[100:103]\n"
    "ds_read_b128 a[152:155], v3 offset:9216\n"
    "v_mfma_f32_16x16x32_bf16 v[8:11], a[128:131], a[232:235], v[8:11]\n"
    "ds_read_b128 a[156:159], v3 offset:11264\n"
    "v_mfma_f32_16x16x32_bf16 v[8:11], a[124:127], a[228:231], v[8:11]\n"
    "buffer_load_dwordx4 v1, s[16:19], s83 offen offset:1024 sc1 lds\n"
    "v_mfma_f32_16x16x32_bf16 v[8:11], a[120:123], a[224:227], v[8:11]\n"
    "ds_read_b128 v[104:107], v250 offset:784\n"
    "v_mfma_f32_16x16x32_bf16 v[8:11], a[116:119], a[220:223], v[8:11]\n"
    "v_exp_f32 v59, v59\n"
    "v_mfma_f32_16x16x32_bf16 v[8:11], a[112:115], a[216:219], v[8:11]\n"
    "v_cvt_pk_bf16_f32 v83, v58, v59\n"
    "v_mfma_f32_16x16x32_bf16 v[12:15], a[112:115], a[240:243], v[100:103]\n"
    "buffer_load_dwordx4 v1, s[16:19], s83 offen offset:2048 sc1 lds\n"
    "v_mfma_f32_16x16x32_bf16 v[12:15], a[116:119], a[244:247], v[12:15]\n"
    "v_exp_f32 v45, v45\n"
    "v_mfma_f32_16x16x32_bf16 v[12:15], a[120:123], a[248:251], v[12:15]\n"
    "v_exp_f32 v46, v46\n"
    "v_mfma_f32_16x16x32_bf16 v[12:15], a[124:127], a[252:255], v[12:15]\n"
    "v_exp_f32 v47, v47\n"
    "v_mfma_f32_16x16x32_bf16 v[12:15], a[128:131], a[160:163], v[12:15]\n"
    "buffer_load_dwordx4 v1, s[16:19], s83 offen offset:3072 sc1 lds\n"
    "v_mfma_f32_16x16x32_bf16 v[12:15], a[132:135], a[164:167], v[12:15]\n"
    "s_add_u32 m0, s64, 4096\n"
    "v_exp_f32 v60, v60\n"
    "v_mfma_f32_16x16x32_bf16 v[16:19], a[132:135], a[188:191], v[100:103]\n"
    "s_add_u32 s83, s67, 28672\n"
    "v_exp_f32 v61, v61\n"
    "v_mfma_f32_16x16x32_bf16 v[16:19], a[128:131], a[184:187], v[16:19]\n"
    "v_exp_f32 v62, v62\n"
    "v_mfma_f32_16x16x32_bf16 v[16:19], a[124:127], a[180:183], v[16:19]\n"
    "buffer_load_dwordx4 v1, s[16:19], s83 offen offset:0 sc1 lds\n"
    "v_mfma_f32_16x16x32_bf16 v[16:19], a[120:123], a[176:179], v[16:19]\n"
    "v_exp_f32 v63, v63\n"
    "v_mfma_f32_16x16x32_bf16 v[16:19], a[116:119], a[172:175], v[16:19]\n"
    "v_cvt_pk_bf16_f32 v88, v44, v45\n"
    "v_cvt_pk_bf16_f32 v89, v46, v47\n"
    "v_mfma_f32_16x16x32_bf16 v[16:19], a[112:115], a[168:171], v[16:19]\n"
    "s_waitcnt lgkmcnt(0)\n"
    "v_cvt_pk_bf16_f32 v90, v60, v61\n"
    "v_cvt_pk_bf16_f32 v91, v62, v63\n"
    "v_mfma_f32_16x16x32_bf16 v[20:23], a[136:139], a[192:195], v[104:107]\n"
    "buffer_load_dwordx4 v1, s[16:19], s83 offen offset:1024 sc1 lds\n"
    "v_mfma_f32_16x16x32_bf16 v[20:23], a[140:143], a[196:199], v[20:23]\n"
    "s_add_u32 s67, s67, 49152\n"
    "ds_read_b128 a[112:115], v248 offset:0\n"
    "v_mfma_f32_16x16x32_bf16 v[20:23], a[144:147], a[200:203], v[20:23]\n"
    "s_add_u32 s59, s59, 4\n"
    "ds_read_b128 a[116:119], v248 offset:12288\n"
    "v_mfma_f32_16x16x32_bf16 v[20:23], a[148:151], a[204:207], v[20:23]\n"
    "s_waitcnt vmcnt(12)\n"
    "ds_read_b128 a[120:123], v248 offset:1024\n"
    "v_mfma_f32_16x16x32_bf16 v[20:23], a[152:155], a[208:211], v[20:23]\n"
    "buffer_store_dwordx2 v[238:239], v240, s[36:39], s80 offen offset:0 sc1\n"
    "v_mfma_f32_16x16x32_bf16 v[20:23], a[156:159], a[212:215], v[20:23]\n"
    "buffer_load_dwordx2 v[204:205], v242, s[28:31], s75 offen offset:0 nt\n"
    "v_mfma_f32_16x16x32_bf16 v[24:27], a[156:159], a[236:239], v[104:107]\n"
    "s_add_u32 s80, s80, 256\n"
    "ds_read_b128 a[124:127], v248 offset:13312\n"
    "v_mfma_f32_16x16x32_bf16 v[24:27], a[152:155], a[232:235], v[24:27]\n"
    "buffer_load_dwordx2 v[206:207], v242, s[28:31], s75 offen offset:768 nt\n"
    "v_mfma_f32_16x16x32_bf16 v[24:27], a[148:151], a[228:231], v[24:27]\n"
    "ds_read_b128 a[128:131], v248 offset:2048\n"
    "v_mfma_f32_16x16x32_bf16 v[24:27], a[144:147], a[224:227], v[24:27]\n"
    "buffer_load_dwordx2 v[208:209], v242, s[28:31], s75 offen offset:3072 nt\n"
    "v_mfma_f32_16x16x32_bf16 v[24:27], a[140:143], a[220:223], v[24:27]\n"
    "ds_read_b128 a[132:135], v248 offset:14336\n"
    "v_mfma_f32_16x16x32_bf16 v[24:27], a[136:139], a[216:219], v[24:27]\n"
    "buffer_load_dwordx2 v[210:211], v242, s[28:31], s75 offen offset:3840 nt\n"
    "v_mfma_f32_16x16x32_bf16 v[28:31], a[136:139], a[240:243], v[104:107]\n"
    "v_exp_f32 v48, v48\n"
    "v_mfma_f32_16x16x32_bf16 v[28:31], a[140:143], a[244:247], v[28:31]\n"
    "buffer_load_dwordx2 v[212:213], v242, s[28:31], s76 offen offset:0 nt\n"
    "v_mfma_f32_16x16x32_bf16 v[28:31], a[144:147], a[248:251], v[28:31]\n"
    "v_exp_f32 v49, v49\n"
    "v_mfma_f32_16x16x32_bf16 v[28:31], a[148:151], a[252:255], v[28:31]\n"
    "buffer_load_dwordx2 v[214:215], v242, s[28:31], s76 offen offset:768 nt\n"
    "v_mfma_f32_16x16x32_bf16 v[28:31], a[152:155], a[160:163], v[28:31]\n"
    "v_exp_f32 v50, v50\n"
    "v_mfma_f32_16x16x32_bf16 v[28:31], a[156:159], a[164:167], v[28:31]\n"
    "buffer_load_dwordx2 v[216:217], v242, s[28:31], s76 offen offset:3072 nt\n"
    "v_mfma_f32_16x16x32_bf16 v[32:35], a[156:159], a[188:191], v[104:107]\n"
    "v_exp_f32 v51, v51\n"
    "v_mfma_f32_16x16x32_bf16 v[32:35], a[152:155], a[184:187], v[32:35]\n"
    "buffer_load_dwordx2 v[218:219], v242, s[28:31], s76 offen offset:3840 nt\n"
    "v_mfma_f32_16x16x32_bf16 v[32:35], a[148:151], a[180:183], v[32:35]\n"
    "v_exp_f32 v64, v64\n"
    "v_mfma_f32_16x16x32_bf16 v[32:35], a[144:147], a[176:179], v[32:35]\n"
    "buffer_load_dwordx2 v[220:221], v242, s[28:31], s77 offen offset:0 nt\n"
    "v_mfma_f32_16x16x32_bf16 v[32:35], a[140:143], a[172:175], v[32:35]\n"
    "v_exp_f32 v65, v65\n"
    "v_mfma_f32_16x16x32_bf16 v[32:35], a[136:139], a[168:171], v[32:35]\n"
    "s_waitcnt lgkmcnt(0)\n"
    "v_exp_f32 v66, v66\n"
    "v_mfma_f32_16x16x32_bf16 v[124:127], a[112:115], v[68:71], v[124:127]\n"
    "buffer_load_dwordx2 v[222:223], v242, s[28:31], s77 offen offset:768 nt\n"
    "v_mfma_f32_16x16x32_bf16 v[124:127], a[116:119], v[72:75], v[124:127]\n"
    "ds_read_b128 a[136:139], v248 offset:3072\n"
    "v_cvt_pk_bf16_f32 v96, v48, v49\n"
    "v_mfma_f32_16x16x32_bf16 v[128:131], a[124:127], v[72:75], v[128:131]\n"
    "buffer_load_dwordx2 v[224:225], v242, s[28:31], s77 offen offset:3072 nt\n"
    "v_mfma_f32_16x16x32_bf16 v[128:131], a[120:123], v[68:71], v[128:131]\n"
    "ds_read_b128 a[140:143], v248 offset:15360\n"
    "v_cvt_pk_bf16_f32 v97, v50, v51\n"
    "v_mfma_f32_16x16x32_bf16 v[132:135], a[128:131], v[68:71], v[132:135]\n"
    "buffer_load_dwordx2 v[226:227], v242, s[28:31], s77 offen offset:3840 nt\n"
    "v_mfma_f32_16x16x32_bf16 v[132:135], a[132:135], v[72:75], v[132:135]\n"
    "ds_read_b128 a[144:147], v248 offset:4096\n"
    "v_cvt_pk_bf16_f32 v98, v64, v65\n"
    "v_mfma_f32_16x16x32_bf16 v[148:151], a[132:135], v[80:83], v[148:151]\n"
    "buffer_load_dwordx2 v[228:229], v242, s[28:31], s78 offen offset:0 nt\n"
    "v_mfma_f32_16x16x32_bf16 v[148:151], a[128:131], v[76:79], v[148:151]\n"
    "ds_read_b128 a[148:151], v248 offset:16384\n"
    "v_mfma_f32_16x16x32_bf16 v[144:147], a[120:123], v[76:79], v[144:147]\n"
    "buffer_load_dwordx2 v[230:231], v242, s[28:31], s78 offen offset:768 nt\n"
    "v_mfma_f32_16x16x32_bf16 v[144:147], a[124:127], v[80:83], v[144:147]\n"
    "ds_read_b128 a[152:155], v248 offset:5120\n"
    "v_mfma_f32_16x16x32_bf16 v[140:143], a[116:119], v[80:83], v[140:143]\n"
    "buffer_load_dwordx2 v[232:233], v242, s[28:31], s78 offen offset:3072 nt\n"
    "v_mfma_f32_16x16x32_bf16 v[140:143], a[112:115], v[76:79], v[140:143]\n"
    "ds_read_b128 a[156:159], v248 offset:17408\n"
    "v_mfma_f32_16x16x32_bf16 v[156:159], a[112:115], v[84:87], v[156:159]\n"
    "buffer_load_dwordx2 v[234:235], v242, s[28:31], s78 offen offset:3840 nt\n"
    "v_mfma_f32_16x16x32_bf16 v[156:159], a[116:119], v[88:91], v[156:159]\n"
    "ds_read_b128 v[104:107], v251 offset:0\n"
    "v_mfma_f32_16x16x32_bf16 v[160:163], a[124:127], v[88:91], v[160:163]\n"
    "v_exp_f32 v67, v67\n"
    "v_mfma_f32_16x16x32_bf16 v[160:163], a[120:123], v[84:87], v[160:163]\n"
    "v_cvt_pk_bf16_f32 v99, v66, v67\n"
    "v_mfma_f32_16x16x32_bf16 v[164:167], a[128:131], v[84:87], v[164:167]\n"
    "v_exp_f32 v4, v4\n"
    "v_mfma_f32_16x16x32_bf16 v[164:167], a[132:135], v[88:91], v[164:167]\n"
    "v_exp_f32 v5, v5\n"
    "v_mfma_f32_16x16x32_bf16 v[180:183], a[132:135], v[96:99], v[180:183]\n"
    "v_exp_f32 v6, v6\n"
    "v_mfma_f32_16x16x32_bf16 v[180:183], a[128:131], v[92:95], v[180:183]\n"
    "v_exp_f32 v7, v7\n"
    "v_mfma_f32_16x16x32_bf16 v[176:179], a[120:123], v[92:95], v[176:179]\n"
    "v_exp_f32 v8, v8\n"
    "v_mfma_f32_16x16x32_bf16 v[176:179], a[124:127], v[96:99], v[176:179]\n"
    "v_exp_f32 v9, v9\n"
    "v_mfma_f32_16x16x32_bf16 v[172:175], a[116:119], v[96:99], v[172:175]\n"
    "v_exp_f32 v10, v10\n"
    "v_mfma_f32_16x16x32_bf16 v[172:175], a[112:115], v[92:95], v[172:175]\n"
    "s_waitcnt lgkmcnt(0)\n"
    "v_exp_f32 v11, v11\n"
    "v_mfma_f32_16x16x32_bf16 v[136:139], a[136:139], v[68:71], v[136:139]\n"
    "ds_read_b128 a[112:115], v248 offset:6144\n"
    "v_mfma_f32_16x16x32_bf16 v[136:139], a[140:143], v[72:75], v[136:139]\n"
    "ds_read_b128 a[116:119], v248 offset:18432\n"
    "v_mfma_f32_16x16x32_bf16 v[188:191], a[148:151], v[72:75], v[188:191]\n"
    "ds_read_b128 a[120:123], v248 offset:7168\n"
    "v_mfma_f32_16x16x32_bf16 v[188:191], a[144:147], v[68:71], v[188:191]\n"
    "ds_read_b128 a[124:127], v248 offset:19456\n"
    "v_mfma_f32_16x16x32_bf16 v[192:195], a[152:155], v[68:71], v[192:195]\n"
    "ds_read_b128 a[128:131], v248 offset:8192\n"
    "v_mfma_f32_16x16x32_bf16 v[192:195], a[156:159], v[72:75], v[192:195]\n"
    "ds_read_b128 a[132:135], v248 offset:20480\n"
    "v_smfmac_f32_16x16x64_bf16 v[108:111], v[104:107], v[68:75], v253\n"
    "s_add_u32 s81, s81, 128\n"
    "s_cmp_gt_u32 s81, 4096\n"
    "s_cselect_b32 s81, 0, s81\n"
    "v_smfmac_f32_16x16x64_bf16 v[112:115], v[104:107], v[76:83], v253\n"
    "s_cmp_ge_u32 s75, 1474560\n"
    "s_cmov_b64 s[28:29], s[6:7]\n"
    "v_mfma_f32_16x16x32_bf16 a[4:7], a[156:159], v[80:83], a[4:7]\n"
    "v_exp_f32 v12, v12\n"
    "v_mfma_f32_16x16x32_bf16 a[4:7], a[152:155], v[76:79], a[4:7]\n"
    "v_exp_f32 v13, v13\n"
    "v_mfma_f32_16x16x32_bf16 a[0:3], a[144:147], v[76:79], a[0:3]\n"
    "v_exp_f32 v14, v14\n"
    "v_mfma_f32_16x16x32_bf16 a[0:3], a[148:151], v[80:83], a[0:3]\n"
    "v_exp_f32 v15, v15\n"
    "v_mfma_f32_16x16x32_bf16 v[152:155], a[140:143], v[80:83], v[152:155]\n"
    "v_exp_f32 v16, v16\n"
    "v_mfma_f32_16x16x32_bf16 v[152:155], a[136:139], v[76:79], v[152:155]\n"
    "v_mfma_f32_16x16x32_bf16 v[168:171], a[136:139], v[84:87], v[168:171]\n"
    "v_exp_f32 v17, v17\n"
    "v_mfma_f32_16x16x32_bf16 v[168:171], a[140:143], v[88:91], v[168:171]\n"
    "v_mfma_f32_16x16x32_bf16 a[16:19], a[148:151], v[88:91], a[16:19]\n"
    "v_mfma_f32_16x16x32_bf16 a[16:19], a[144:147], v[84:87], a[16:19]\n"
    "v_exp_f32 v18, v18\n"
    "v_mfma_f32_16x16x32_bf16 a[20:23], a[152:155], v[84:87], a[20:23]\n"
    "v_mfma_f32_16x16x32_bf16 a[20:23], a[156:159], v[88:91], a[20:23]\n"
    "v_smfmac_f32_16x16x64_bf16 v[116:119], v[104:107], v[84:91], v253\n"
    "v_smfmac_f32_16x16x64_bf16 v[120:123], v[104:107], v[92:99], v253\n"
    "v_mfma_f32_16x16x32_bf16 a[36:39], a[156:159], v[96:99], a[36:39]\n"
    "v_exp_f32 v19, v19\n"
    "v_mfma_f32_16x16x32_bf16 a[36:39], a[152:155], v[92:95], a[36:39]\n"
    "v_exp_f32 v20, v20\n"
    "v_mfma_f32_16x16x32_bf16 a[32:35], a[144:147], v[92:95], a[32:35]\n"
    "v_mfma_f32_16x16x32_bf16 a[32:35], a[148:151], v[96:99], a[32:35]\n"
    "v_exp_f32 v21, v21\n"
    "v_mfma_f32_16x16x32_bf16 v[184:187], a[140:143], v[96:99], v[184:187]\n"
    "v_mfma_f32_16x16x32_bf16 v[184:187], a[136:139], v[92:95], v[184:187]\n"
    "s_waitcnt lgkmcnt(0)\n"
    "v_mfma_f32_16x16x32_bf16 v[196:199], a[112:115], v[68:71], v[196:199]\n"
    "ds_read_b128 a[136:139], v248 offset:9216\n"
    "v_mfma_f32_16x16x32_bf16 v[196:199], a[116:119], v[72:75], v[196:199]\n"
    "ds_read_b128 a[140:143], v248 offset:21504\n"
    "v_mfma_f32_16x16x32_bf16 v[200:203], a[124:127], v[72:75], v[200:203]\n"
    "ds_read_b128 a[144:147], v248 offset:10240\n"
    "v_mfma_f32_16x16x32_bf16 v[200:203], a[120:123], v[68:71], v[200:203]\n"
    "ds_read_b128 a[148:151], v248 offset:22528\n"
    "v_mfma_f32_16x16x32_bf16 a[48:51], a[128:131], v[68:71], a[48:51]\n"
    "buffer_load_dwordx2 v[236:237], v241, s[36:39], s81 offen offset:0 sc1\n"
    "v_mfma_f32_16x16x32_bf16 a[48:51], a[132:135], v[72:75], a[48:51]\n"
    "ds_read_b128 a[152:155], v248 offset:11264\n"
    "v_mfma_f32_16x16x32_bf16 a[64:67], a[132:135], v[80:83], a[64:67]\n"
    "ds_read_b128 a[156:159], v248 offset:23552\n"
    "v_mfma_f32_16x16x32_bf16 a[64:67], a[128:131], v[76:79], a[64:67]\n"
    "v_exp_f32 v22, v22\n"
    "v_mfma_f32_16x16x32_bf16 a[12:15], a[120:123], v[76:79], a[12:15]\n"
    "v_exp_f32 v23, v23\n"
    "v_mfma_f32_16x16x32_bf16 a[12:15], a[124:127], v[80:83], a[12:15]\n"
    "v_exp_f32 v24, v24\n"
    "v_mfma_f32_16x16x32_bf16 a[8:11], a[116:119], v[80:83], a[8:11]\n"
    "v_exp_f32 v25, v25\n"
    "v_mfma_f32_16x16x32_bf16 a[8:11], a[112:115], v[76:79], a[8:11]\n"
    "v_exp_f32 v26, v26\n"
    "v_mfma_f32_16x16x32_bf16 a[24:27], a[112:115], v[84:87], a[24:27]\n"
    "v_mfma_f32_16x16x32_bf16 a[24:27], a[116:119], v[88:91], a[24:27]\n"
    "v_exp_f32 v27, v27\n"
    "v_mfma_f32_16x16x32_bf16 a[28:31], a[124:127], v[88:91], a[28:31]\n"
    "v_mfma_f32_16x16x32_bf16 a[28:31], a[120:123], v[84:87], a[28:31]\n"
    "v_mfma_f32_16x16x32_bf16 a[80:83], a[128:131], v[84:87], a[80:83]\n"
    "v_exp_f32 v28, v28\n"
    "v_mfma_f32_16x16x32_bf16 a[80:83], a[132:135], v[88:91], a[80:83]\n"
    "v_mfma_f32_16x16x32_bf16 a[96:99], a[132:135], v[96:99], a[96:99]\n"
    "v_mfma_f32_16x16x32_bf16 a[96:99], a[128:131], v[92:95], a[96:99]\n"
    "v_exp_f32 v29, v29\n"
    "v_mfma_f32_16x16x32_bf16 a[44:47], a[120:123], v[92:95], a[44:47]\n"
    "v_mfma_f32_16x16x32_bf16 a[44:47], a[124:127], v[96:99], a[44:47]\n"
    "v_exp_f32 v30, v30\n"
    "v_mfma_f32_16x16x32_bf16 a[40:43], a[116:119], v[96:99], a[40:43]\n"
    "v_mfma_f32_16x16x32_bf16 a[40:43], a[112:115], v[92:95], a[40:43]\n"
    "s_waitcnt lgkmcnt(0)\n"
    "v_mfma_f32_16x16x32_bf16 a[52:55], a[136:139], v[68:71], a[52:55]\n"
    "ds_read_b128 a[112:115], v3 offset:12288\n"
    "v_mfma_f32_16x16x32_bf16 a[52:55], a[140:143], v[72:75], a[52:55]\n"
    "ds_read_b128 a[116:119], v3 offset:14336\n"
    "v_mfma_f32_16x16x32_bf16 a[56:59], a[148:151], v[72:75], a[56:59]\n"
    "ds_read_b128 a[120:123], v3 offset:16384\n"
    "v_mfma_f32_16x16x32_bf16 a[56:59], a[144:147], v[68:71], a[56:59]\n"
    "ds_read_b128 a[124:127], v3 offset:18432\n"
    "v_mfma_f32_16x16x32_bf16 a[60:63], a[152:155], v[68:71], a[60:63]\n"
    "ds_read_b128 a[128:131], v3 offset:20480\n"
    "v_mfma_f32_16x16x32_bf16 a[60:63], a[156:159], v[72:75], a[60:63]\n"
    "ds_read_b128 a[132:135], v3 offset:22528\n"
    "v_mfma_f32_16x16x32_bf16 a[76:79], a[156:159], v[80:83], a[76:79]\n"
    "ds_read_b128 v[100:103], v250 offset:896\n"
    "v_mfma_f32_16x16x32_bf16 a[76:79], a[152:155], v[76:79], a[76:79]\n"
    "v_exp_f32 v31, v31\n"
    "v_mfma_f32_16x16x32_bf16 a[72:75], a[144:147], v[76:79], a[72:75]\n"
    "v_exp_f32 v32, v32\n"
    "v_mfma_f32_16x16x32_bf16 a[72:75], a[148:151], v[80:83], a[72:75]\n"
    "v_exp_f32 v33, v33\n"
    "v_mfma_f32_16x16x32_bf16 a[68:71], a[140:143], v[80:83], a[68:71]\n"
    "v_exp_f32 v34, v34\n"
    "v_mfma_f32_16x16x32_bf16 a[68:71], a[136:139], v[76:79], a[68:71]\n"
    "v_exp_f32 v35, v35\n"
    "v_mfma_f32_16x16x32_bf16 a[84:87], a[136:139], v[84:87], a[84:87]\n"
    "v_mfma_f32_16x16x32_bf16 a[84:87], a[140:143], v[88:91], a[84:87]\n"
    "v_mfma_f32_16x16x32_bf16 a[88:91], a[148:151], v[88:91], a[88:91]\n"
    "s_waitcnt vmcnt(18)\n"
    "ds_add_u32 v247, v246\n"
    "v_mfma_f32_16x16x32_bf16 a[88:91], a[144:147], v[84:87], a[88:91]\n"
    "v_mfma_f32_16x16x32_bf16 a[92:95], a[152:155], v[84:87], a[92:95]\n"
    "v_mfma_f32_16x16x32_bf16 a[92:95], a[156:159], v[88:91], a[92:95]\n"
    "v_mfma_f32_16x16x32_bf16 a[108:111], a[156:159], v[96:99], a[108:111]\n"
    "v_mfma_f32_16x16x32_bf16 a[108:111], a[152:155], v[92:95], a[108:111]\n"
    "v_mfma_f32_16x16x32_bf16 a[104:107], a[144:147], v[92:95], a[104:107]\n"
    "s_sub_u32 s74, s68, s65\n"
    "v_mfma_f32_16x16x32_bf16 a[104:107], a[148:151], v[96:99], a[104:107]\n"
    "s_add_u32 s66, s66, 1\n"
    "v_mov_b32 v2, v3\n"
    "v_add_u32 v3, s74, v252\n"
    "v_mfma_f32_16x16x32_bf16 a[100:103], a[140:143], v[96:99], a[100:103]\n"
    "s_waitcnt lgkmcnt(1)\n"
    "s_cmp_eq_u32 s66, 29\n"
    "v_mfma_f32_16x16x32_bf16 a[100:103], a[136:139], v[92:95], a[100:103]\n"
    ".LwKe:\n"
    "s_cbranch_scc1 .LtailP\n"
    ".LNK:\n"
    "v_mfma_f32_16x16x32_bf16 v[36:39], a[112:115], a[192:195], v[100:103]\n"
    "ds_read_b128 a[136:139], v2 offset:13312\n"
    "v_cvt_pk_bf16_f32 v68, v4, v5\n"
    "v_mfma_f32_16x16x32_bf16 v[36:39], a[116:119], a[196:199], v[36:39]\n"
    "ds_read_b128 a[140:143], v2 offset:15360\n"
    "v_mfma_f32_16x16x32_bf16 v[36:39], a[120:123], a[200:203], v[36:39]\n"
    "ds_read_b128 a[144:147], v2 offset:17408\n"
    "v_mfma_f32_16x16x32_bf16 v[36:39], a[124:127], a[204:207], v[36:39]\n"
    "ds_read_b128 a[148:151], v2 offset:19456\n"
    "v_cvt_pk_bf16_f32 v69, v6, v7\n"
    "v_mfma_f32_16x16x32_bf16 v[36:39], a[128:131], a[208:211], v[36:39]\n"
    "ds_read_b128 a[152:155], v2 offset:21504\n"
    "v_mfma_f32_16x16x32_bf16 v[36:39], a[132:135], a[212:215], v[36:39]\n"
    "ds_read_b128 a[156:159], v2 offset:23552\n"
    "v_mfma_f32_16x16x32_bf16 v[40:43], a[132:135], a[236:239], v[100:103]\n"
    "ds_read_b128 v[104:107], v250 offset:912\n"
    "v_mfma_f32_16x16x32_bf16 v[40:43], a[128:131], a[232:235], v[40:43]\n"
    "s_add_u32 s68, s68, 24576\n"
    "v_cvt_pk_bf16_f32 v76, v8, v9\n"
    "v_mfma_f32_16x16x32_bf16 v[40:43], a[124:127], a[228:231], v[40:43]\n"
    "s_cmp_ge_u32 s68, s69\n"
    "s_cselect_b32 s68, s65, s68\n"
    "v_mfma_f32_16x16x32_bf16 v[40:43], a[120:123], a[224:227], v[40:43]\n"
    "v_exp_f32 v36, v36\n"
    "v_mfma_f32_16x16x32_bf16 v[40:43], a[116:119], a[220:223], v[40:43]\n"
    "v_exp_f32 v37, v37\n"
    "v_mfma_f32_16x16x32_bf16 v[40:43], a[112:115], a[216:219], v[40:43]\n"
    "v_cvt_pk_bf16_f32 v77, v10, v11\n"
    "v_cvt_pk_bf16_f32 v72, v36, v37\n"
    "v_mfma_f32_16x16x32_bf16 v[44:47], a[112:115], a[240:243], v[100:103]\n"
    "v_mfma_f32_16x16x32_bf16 v[44:47], a[116:119], a[244:247], v[44:47]\n"
    "v_exp_f32 v38, v38\n"
    "v_mfma_f32_16x16x32_bf16 v[44:47], a[120:123], a[248:251], v[44:47]\n"
    "v_mfma_f32_16x16x32_bf16 v[44:47], a[124:127], a[252:255], v[44:47]\n"
    "v_cvt_pk_bf16_f32 v84, v12, v13\n"
    "v_mfma_f32_16x16x32_bf16 v[44:47], a[128:131], a[160:163], v[44:47]\n"
    "v_exp_f32 v39, v39\n"
    "v_mfma_f32_16x16x32_bf16 v[44:47], a[132:135], a[164:167], v[44:47]\n"
    "v_cvt_pk_bf16_f32 v73, v38, v39\n"
    "v_mfma_f32_16x16x32_bf16 v[48:51], a[132:135], a[188:191], v[100:103]\n"
    "ds_read_b32 v245, v247\n"
    "v_cvt_pk_bf16_f32 v85, v14, v15\n"
    "v_mfma_f32_16x16x32_bf16 v[48:51], a[128:131], a[184:187], v[48:51]\n"
    "s_add_u32 m0, s68, 0\n"
    "v_exp_f32 v40, v40\n"
    "v_mfma_f32_16x16x32_bf16 v[48:51], a[124:127], a[180:183], v[48:51]\n"
    "v_mfma_f32_16x16x32_bf16 v[48:51], a[120:123], a[176:179], v[48:51]\n"
    "v_mfma_f32_16x16x32_bf16 v[48:51], a[116:119], a[172:175], v[48:51]\n"
    "s_waitcnt vmcnt(0)\n"
    "v_cmp_eq_u64 s[54:55], v[236:237], v[238:239]\n"
    "v_cvt_pk_bf16_f32 v92, v16, v17\n"
    "v_mfma_f32_16x16x32_bf16 v[48:51], a[112:115], a[168:171], v[48:51]\n"
    "s_waitcnt lgkmcnt(1)\n"
    "v_exp_f32 v41, v41\n"
    "v_mfma_f32_16x16x32_bf16 v[52:55], a[136:139], a[192:195], v[104:107]\n"
    "s_waitcnt lgkmcnt(0)\n"
    "v_cmp_le_u32 s[56:57], s59, v245\n"
    "v_cvt_pk_bf16_f32 v80, v40, v41\n"
    "v_mfma_f32_16x16x32_bf16 v[52:55], a[140:143], a[196:199], v[52:55]\n"
    "s_waitcnt vmcnt(1)\n"
    "v_exp_f32 v42, v42\n"
    "v_mfma_f32_16x16x32_bf16 v[52:55], a[144:147], a[200:203], v[52:55]\n"
    "s_and_b64 s[54:55], s[54:55], s[56:57]\n"
    "s_cmp_eq_u64 s[54:55], -1\n"
    "v_mfma_f32_16x16x32_bf16 v[52:55], a[148:151], a[204:207], v[52:55]\n"
    "s_cbranch_scc0 .Lpoll2\n"
    ".Lpoll2r:\n"
    "v_mfma_f32_16x16x32_bf16 v[52:55], a[152:155], a[208:211], v[52:55]\n"
    "buffer_load_dwordx4 v1, s[16:19], s67 offen offset:0 sc1 lds\n"
    "v_mfma_f32_16x16x32_bf16 v[52:55], a[156:159], a[212:215], v[52:55]\n"
    "s_add_u32 s76, s71, s60\n"
    "ds_read_b128 a[112:115], v3 offset:0\n"
    "v_cvt_pk_bf16_f32 v93, v18, v19\n"
    "v_mfma_f32_16x16x32_bf16 v[56:59], a[156:159], a[236:239], v[104:107]\n"
    "s_add_u32 s77, s71, s61\n"
    "ds_read_b128 a[116:119], v3 offset:2048\n"
    "v_cvt_pk_bf16_f32 v70, v20, v21\n"
    "v_mfma_f32_16x16x32_bf16 v[56:59], a[152:155], a[232:235], v[56:59]\n"
    "ds_read_b128 a[120:123], v3 offset:4096\n"
    "v_mfma_f32_16x16x32_bf16 v[56:59], a[148:151], a[228:231], v[56:59]\n"
    "buffer_load_dwordx4 v1, s[16:19], s67 offen offset:1024 sc1 lds\n"
    "v_mfma_f32_16x16x32_bf16 v[56:59], a[144:147], a[224:227], v[56:59]\n"
    "ds_read_b128 a[124:127], v3 offset:6144\n"
    "v_cvt_pk_bf16_f32 v71, v22, v23\n"
    "v_mfma_f32_16x16x32_bf16 v[56:59], a[140:143], a[220:223], v[56:59]\n"
    "ds_read_b128 a[128:131], v3 offset:8192\n"
    "v_mfma_f32_16x16x32_bf16 v[56:59], a[136:139], a[216:219], v[56:59]\n"
    "ds_read_b128 a[132:135], v3 offset:10240\n"
    "v_mfma_f32_16x16x32_bf16 v[60:63], a[136:139], a[240:243], v[104:107]\n"
    "buffer_load_dwordx4 v1, s[16:19], s67 offen offset:2048 sc1 lds\n"
    "v_mfma_f32_16x16x32_bf16 v[60:63], a[140:143], a[244:247], v[60:63]\n"
    "ds_read_b128 v[100:103], v250 offset:1536\n"
    "v_cvt_pk_bf16_f32 v78, v24, v25\n"
    "v_mfma_f32_16x16x32_bf16 v[60:63], a[144:147], a[248:251], v[60:63]\n"
    "v_exp_f32 v43, v43\n"
    "v_mfma_f32_16x16x32_bf16 v[60:63], a[148:151], a[252:255], v[60:63]\n"
    "ds_read_b128 v[36:39], v243 offset:0\n"
    "v_cvt_pk_bf16_f32 v81, v42, v43\n"
    "v_mfma_f32_16x16x32_bf16 v[60:63], a[152:155], a[160:163], v[60:63]\n"
    "buffer_load_dwordx4 v1, s[16:19], s67 offen offset:3072 sc1 lds\n"
    "v_mfma_f32_16x16x32_bf16 v[60:63], a[156:159], a[164:167], v[60:63]\n"
    "s_add_u32 m0, s68, 4096\n"
    "v_cvt_pk_bf16_f32 v79, v26, v27\n"
    "v_add_u32 v243, 512, v243\n"
    "v_mfma_f32_16x16x32_bf16 v[64:67], a[156:159], a[188:191], v[104:107]\n"
    "s_add_u32 s83, s67, 4096\n"
    "v_exp_f32 v44, v44\n"
    "v_mfma_f32_16x16x32_bf16 v[64:67], a[152:155], a[184:187], v[64:67]\n"
    "v_exp_f32 v45, v45\n"
    "v_mfma_f32_16x16x32_bf16 v[64:67], a[148:151], a[180:183], v[64:67]\n"
    "buffer_load_dwordx4 v1, s[16:19], s83 offen offset:0 sc1 lds\n"
    "v_mfma_f32_16x16x32_bf16 v[64:67], a[144:147], a[176:179], v[64:67]\n"
    "v_cvt_pk_bf16_f32 v86, v28, v29\n"
    "v_cvt_pk_bf16_f32 v88, v44, v45\n"
    "v_mfma_f32_16x16x32_bf16 v[64:67], a[140:143], a[172:175], v[64:67]\n"
    "v_exp_f32 v46, v46\n"
    "v_mfma_f32_16x16x32_bf16 v[64:67], a[136:139], a[168:171], v[64:67]\n"
    "s_waitcnt lgkmcnt(1)\n"
    "v_exp_f32 v47, v47\n"
    "v_mfma_f32_16x16x32_bf16 v[4:7], a[112:115], a[192:195], v[100:103]\n"
    "buffer_load_dwordx4 v1, s[16:19], s83 offen offset:1024 sc1 lds\n"
    "v_mfma_f32_16x16x32_bf16 v[4:7], a[116:119], a[196:199], v[4:7]\n"
    "s_add_u32 m0, s70, .Lend-.Lend2+(0)\n"
    "ds_read_b128 a[136:139], v3 offset:1024\n"
    "v_cvt_pk_bf16_f32 v87, v30, v31\n"
    "v_mfma_f32_16x16x32_bf16 v[4:7], a[120:123], a[200:203], v[4:7]\n"
    "s_add_u32 s83, s67, 24576\n"
    "ds_read_b128 a[140:143], v3 offset:3072\n"
    "v_cvt_pk_bf16_f32 v89, v46, v47\n"
    "v_mfma_f32_16x16x32_bf16 v[4:7], a[124:127], a[204:207], v[4:7]\n"
    "s_waitcnt lgkmcnt(2)\n"
    "ds_read_b128 a[144:147], v3 offset:5120\n"
    "v_fma_f32 v204, v204, v36, -v38\n"
    "v_mfma_f32_16x16x32_bf16 v[4:7], a[128:131], a[208:211], v[4:7]\n"
    "buffer_load_dwordx4 v1, s[16:19], s83 offen offset:0 sc1 lds\n"
    "v_mfma_f32_16x16x32_bf16 v[4:7], a[132:135], a[212:215], v[4:7]\n"
    "ds_read_b128 a[148:151], v3 offset:7168\n"
    "v_cvt_pk_bf16_f32 v94, v32, v33\n"
    "v_mfma_f32_16x16x32_bf16 v[8:11], a[132:135], a[236:239], v[100:103]\n"
    "ds_read_b128 a[152:155], v3 offset:9216\n"
    "v_fma_f32 v205, v205, v37, -v39\n"
    "v_mfma_f32_16x16x32_bf16 v[8:11], a[128:131], a[232:235], v[8:11]\n"
    "ds_read_b128 a[156:159], v3 offset:11264\n"
    "v_fma_f32 v206, v206, v36, -v38\n"
    "v_mfma_f32_16x16x32_bf16 v[8:11], a[124:127], a[228:231], v[8:11]\n"
    "buffer_load_dwordx4 v1, s[16:19], s83 offen offset:1024 sc1 lds\n"
    "v_mfma_f32_16x16x32_bf16 v[8:11], a[120:123], a[224:227], v[8:11]\n"
    "ds_read_b128 v[104:107], v250 offset:1552\n"
    "v_cvt_pk_bf16_f32 v95, v34, v35\n"
    "v_mfma_f32_16x16x32_bf16 v[8:11], a[116:119], a[220:223], v[8:11]\n"
    "v_exp_f32 v52, v52\n"
    "v_mfma_f32_16x16x32_bf16 v[8:11], a[112:115], a[216:219], v[8:11]\n"
    "v_exp_f32 v53, v53\n"
    "v_mfma_f32_16x16x32_bf16 v[12:15], a[112:115], a[240:243], v[100:103]\n"
    "buffer_load_dwordx4 v1, s[16:19], s83 offen offset:2048 sc1 lds\n"
    "v_mfma_f32_16x16x32_bf16 v[12:15], a[116:119], a[244:247], v[12:15]\n"
    "v_exp_f32 v54, v54\n"
    "v_mfma_f32_16x16x32_bf16 v[12:15], a[120:123], a[248:251], v[12:15]\n"
    "v_exp_f32 v55, v55\n"
    "v_mfma_f32_16x16x32_bf16 v[12:15], a[124:127], a[252:255], v[12:15]\n"
    "v_cvt_pk_bf16_f32 v74, v52, v53\n"
    "v_cvt_pk_bf16_f32 v75, v54, v55\n"
    "v_mfma_f32_16x16x32_bf16 v[12:15], a[128:131], a[160:163], v[12:15]\n"
    "buffer_load_dwordx4 v1, s[16:19], s83 offen offset:3072 sc1 lds\n"
    "v_mfma_f32_16x16x32_bf16 v[12:15], a[132:135], a[164:167], v[12:15]\n"
    "s_add_u32 m0, s70, 4096\n"
    "v_exp_f32 v56, v56\n"
    "v_mfma_f32_16x16x32_bf16 v[16:19], a[132:135], a[188:191], v[100:103]\n"
    "s_add_u32 s83, s67, 28672\n"
    "v_exp_f32 v48, v48\n"
    "v_mfma_f32_16x16x32_bf16 v[16:19], a[128:131], a[184:187], v[16:19]\n"
    "v_exp_f32 v49, v49\n"
    "v_mfma_f32_16x16x32_bf16 v[16:19], a[124:127], a[180:183], v[16:19]\n"
    "buffer_load_dwordx4 v1, s[16:19], s83 offen offset:0 sc1 lds\n"
    "v_mfma_f32_16x16x32_bf16 v[16:19], a[120:123], a[176:179], v[16:19]\n"
    "v_exp_f32 v57, v57\n"
    "v_mfma_f32_16x16x32_bf16 v[16:19], a[116:119], a[172:175], v[16:19]\n"
    "v_cvt_pk_bf16_f32 v82, v56, v57\n"
    "v_cvt_pk_bf16_f32 v96, v48, v49\n"
    "v_mfma_f32_16x16x32_bf16 v[16:19], a[112:115], a[168:171], v[16:19]\n"
    "s_waitcnt lgkmcnt(0)\n"
    "v_exp_f32 v58, v58\n"
    "v_mfma_f32_16x16x32_bf16 v[20:23], a[136:139], a[192:195], v[104:107]\n"
    "buffer_load_dwordx4 v1, s[16:19], s83 offen offset:1024 sc1 lds\n"
    "v_mfma_f32_16x16x32_bf16 v[20:23], a[140:143], a[196:199], v[20:23]\n"
    "s_add_u32 s67, s67, 49152\n"
    "ds_read_b128 a[112:115], v249 offset:0\n"
    "v_fma_f32 v207, v207, v37, -v39\n"
    "v_mfma_f32_16x16x32_bf16 v[20:23], a[144:147], a[200:203], v[20:23]\n"
    "s_add_u32 s59, s59, 4\n"
    "ds_read_b128 a[116:119], v249 offset:12288\n"
    "v_cvt_pk_bf16_f32 v204, v204, v205\n"
    "v_mfma_f32_16x16x32_bf16 v[20:23], a[148:151], a[204:207], v[20:23]\n"
    "ds_read_b128 a[120:123], v249 offset:1024\n"
    "v_cvt_pk_bf16_f32 v206, v206, v207\n"
    "v_mfma_f32_16x16x32_bf16 v[20:23], a[152:155], a[208:211], v[20:23]\n"
    "ds_read_b128 a[124:127], v249 offset:13312\n"
    "v_fma_f32 v208, v208, v36, -v38\n"
    "v_mfma_f32_16x16x32_bf16 v[20:23], a[156:159], a[212:215], v[20:23]\n"
    "ds_read_b128 a[128:131], v249 offset:2048\n"
    "v_fma_f32 v209, v209, v37, -v39\n"
    "v_mfma_f32_16x16x32_bf16 v[24:27], a[156:159], a[236:239], v[104:107]\n"
    "ds_read_b128 a[132:135], v249 offset:14336\n"
    "v_fma_f32 v210, v210, v36, -v38\n"
    "v_mfma_f32_16x16x32_bf16 v[24:27], a[152:155], a[232:235], v[24:27]\n"
    "v_exp_f32 v59, v59\n"
    "v_mfma_f32_16x16x32_bf16 v[24:27], a[148:151], a[228:231], v[24:27]\n"
    "v_cvt_pk_bf16_f32 v83, v58, v59\n"
    "v_fma_f32 v211, v211, v37, -v39\n"
    "v_mfma_f32_16x16x32_bf16 v[24:27], a[144:147], a[224:227], v[24:27]\n"
    "v_exp_f32 v60, v60\n"
    "v_mfma_f32_16x16x32_bf16 v[24:27], a[140:143], a[220:223], v[24:27]\n"
    "v_exp_f32 v61, v61\n"
    "v_mfma_f32_16x16x32_bf16 v[24:27], a[136:139], a[216:219], v[24:27]\n"
    "v_exp_f32 v62, v62\n"
    "v_mfma_f32_16x16x32_bf16 v[28:31], a[136:139], a[240:243], v[104:107]\n"
    "v_cvt_pk_bf16_f32 v90, v60, v61\n"
    "v_cvt_pk_bf16_f32 v208, v208, v209\n"
    "v_mfma_f32_16x16x32_bf16 v[28:31], a[140:143], a[244:247], v[28:31]\n"
    "v_exp_f32 v63, v63\n"
    "v_mfma_f32_16x16x32_bf16 v[28:31], a[144:147], a[248:251], v[28:31]\n"
    "v_cvt_pk_bf16_f32 v91, v62, v63\n"
    "v_cvt_pk_bf16_f32 v210, v210, v211\n"
    "v_mfma_f32_16x16x32_bf16 v[28:31], a[148:151], a[252:255], v[28:31]\n"
    "v_exp_f32 v50, v50\n"
    "v_mfma_f32_16x16x32_bf16 v[28:31], a[152:155], a[160:163], v[28:31]\n"
    "v_exp_f32 v51, v51\n"
    "v_mfma_f32_16x16x32_bf16 v[28:31], a[156:159], a[164:167], v[28:31]\n"
    "v_exp_f32 v64, v64\n"
    "v_mfma_f32_16x16x32_bf16 v[32:35], a[156:159], a[188:191], v[104:107]\n"
    "v_cvt_pk_bf16_f32 v97, v50, v51\n"
    "v_fma_f32 v212, v212, v36, -v38\n"
    "v_mfma_f32_16x16x32_bf16 v[32:35], a[152:155], a[184:187], v[32:35]\n"
    "v_exp_f32 v65, v65\n"
    "v_mfma_f32_16x16x32_bf16 v[32:35], a[148:151], a[180:183], v[32:35]\n"
    "v_cvt_pk_bf16_f32 v98, v64, v65\n"
    "v_fma_f32 v213, v213, v37, -v39\n"
    "v_mfma_f32_16x16x32_bf16 v[32:35], a[144:147], a[176:179], v[32:35]\n"
    "buffer_store_dword v204, v244, s[16:19], s76 offen offset:0 sc1\n"
    "v_mfma_f32_16x16x32_bf16 v[32:35], a[140:143], a[172:175], v[32:35]\n"
    "v_exp_f32 v66, v66\n"
    "v_mfma_f32_16x16x32_bf16 v[32:35], a[136:139], a[168:171], v[32:35]\n"
    "s_waitcnt lgkmcnt(0)\n"
    "v_fma_f32 v214, v214, v36, -v38\n"
    "v_fma_f32 v215, v215, v37, -v39\n"
    "v_mfma_f32_16x16x32_bf16 v[124:127], a[112:115], v[68:71], v[124:127]\n"
    "ds_read_b128 a[136:139], v249 offset:3072\n"
    "v_cvt_pk_bf16_f32 v212, v212, v213\n"
    "v_mfma_f32_16x16x32_bf16 v[124:127], a[116:119], v[72:75], v[124:127]\n"
    "ds_read_b128 a[140:143], v249 offset:15360\n"
    "v_cvt_pk_bf16_f32 v214, v214, v215\n"
    "v_mfma_f32_16x16x32_bf16 v[128:131], a[124:127], v[72:75], v[128:131]\n"
    "ds_read_b128 a[144:147], v249 offset:4096\n"
    "v_fma_f32 v216, v216, v36, -v38\n"
    "v_mfma_f32_16x16x32_bf16 v[128:131], a[120:123], v[68:71], v[128:131]\n"
    "ds_read_b128 a[148:151], v249 offset:16384\n"
    "v_fma_f32 v217, v217, v37, -v39\n"
    "v_mfma_f32_16x16x32_bf16 v[132:135], a[128:131], v[68:71], v[132:135]\n"
    "ds_read_b128 a[152:155], v249 offset:5120\n"
    "v_fma_f32 v218, v218, v36, -v38\n"
    "v_mfma_f32_16x16x32_bf16 v[132:135], a[132:135], v[72:75], v[132:135]\n"
    "ds_read_b128 a[156:159], v249 offset:17408\n"
    "v_fma_f32 v219, v219, v37, -v39\n"
    "v_mfma_f32_16x16x32_bf16 v[148:151], a[132:135], v[80:83], v[148:151]\n"
    "ds_read_b128 v[104:107], v251 offset:768\n"
    "v_cvt_pk_bf16_f32 v216, v216, v217\n"
    "v_mfma_f32_16x16x32_bf16 v[148:151], a[128:131], v[76:79], v[148:151]\n"
    "v_exp_f32 v67, v67\n"
    "v_mfma_f32_16x16x32_bf16 v[144:147], a[120:123], v[76:79], v[144:147]\n"
    "v_cvt_pk_bf16_f32 v99, v66, v67\n"
    "v_cvt_pk_bf16_f32 v218, v218, v219\n"
    "v_mfma_f32_16x16x32_bf16 v[144:147], a[124:127], v[80:83], v[144:147]\n"
    "buffer_store_dword v206, v244, s[16:19], s76 offen offset:128 sc1\n"
    "v_mfma_f32_16x16x32_bf16 v[140:143], a[116:119], v[80:83], v[140:143]\n"
    "v_exp_f32 v4, v4\n"
    "v_mfma_f32_16x16x32_bf16 v[140:143], a[112:115], v[76:79], v[140:143]\n"
    "buffer_store_dword v208, v244, s[16:19], s76 offen offset:1024 sc1\n"
    "v_mfma_f32_16x16x32_bf16 v[156:159], a[112:115], v[84:87], v[156:159]\n"
    "v_fma_f32 v220, v220, v36, -v38\n"
    "v_fma_f32 v221, v221, v37, -v39\n"
    "v_mfma_f32_16x16x32_bf16 v[156:159], a[116:119], v[88:91], v[156:159]\n"
    "buffer_store_dword v210, v244, s[16:19], s76 offen offset:1152 sc1\n"
    "v_mfma_f32_16x16x32_bf16 v[160:163], a[124:127], v[88:91], v[160:163]\n"
    "v_exp_f32 v5, v5\n"
    "v_mfma_f32_16x16x32_bf16 v[160:163], a[120:123], v[84:87], v[160:163]\n"
    "buffer_store_dword v212, v244, s[16:19], s76 offen offset:256 sc1\n"
    "v_mfma_f32_16x16x32_bf16 v[164:167], a[128:131], v[84:87], v[164:167]\n"
    "v_exp_f32 v6, v6\n"
    "v_mfma_f32_16x16x32_bf16 v[164:167], a[132:135], v[88:91], v[164:167]\n"
    "buffer_store_dword v214, v244, s[16:19], s76 offen offset:384 sc1\n"
    "v_mfma_f32_16x16x32_bf16 v[180:183], a[132:135], v[96:99], v[180:183]\n"
    "v_fma_f32 v222, v222, v36, -v38\n"
    "v_fma_f32 v223, v223, v37, -v39\n"
    "v_mfma_f32_16x16x32_bf16 v[180:183], a[128:131], v[92:95], v[180:183]\n"
    "v_exp_f32 v7, v7\n"
    "v_mfma_f32_16x16x32_bf16 v[176:179], a[120:123], v[92:95], v[176:179]\n"
    "v_exp_f32 v8, v8\n"
    "v_mfma_f32_16x16x32_bf16 v[176:179], a[124:127], v[96:99], v[176:179]\n"
    "v_cvt_pk_bf16_f32 v220, v220, v221\n"
    "v_cvt_pk_bf16_f32 v222, v222, v223\n"
    "v_mfma_f32_16x16x32_bf16 v[172:175], a[116:119], v[96:99], v[172:175]\n"
    "v_exp_f32 v9, v9\n"
    "v_mfma_f32_16x16x32_bf16 v[172:175], a[112:115], v[92:95], v[172:175]\n"
    "s_waitcnt lgkmcnt(0)\n"
    "v_exp_f32 v10, v10\n"
    "v_mfma_f32_16x16x32_bf16 v[136:139], a[136:139], v[68:71], v[136:139]\n"
    "ds_read_b128 a[112:115], v249 offset:6144\n"
    "v_fma_f32 v224, v224, v36, -v38\n"
    "v_mfma_f32_16x16x32_bf16 v[136:139], a[140:143], v[72:75], v[136:139]\n"
    "ds_read_b128 a[116:119], v249 offset:18432\n"
    "v_fma_f32 v225, v225, v37, -v39\n"
    "v_mfma_f32_16x16x32_bf16 v[188:191], a[148:151], v[72:75], v[188:191]\n"
    "ds_read_b128 a[120:123], v249 offset:7168\n"
    "v_fma_f32 v226, v226, v36, -v38\n"
    "v_mfma_f32_16x16x32_bf16 v[188:191], a[144:147], v[68:71], v[188:191]\n"
    "ds_read_b128 a[124:127], v249 offset:19456\n"
    "v_fma_f32 v227, v227, v37, -v39\n"
    "v_mfma_f32_16x16x32_bf16 v[192:195], a[152:155], v[68:71], v[192:195]\n"
    "ds_read_b128 a[128:131], v249 offset:8192\n"
    "v_cvt_pk_bf16_f32 v224, v224, v225\n"
    "v_mfma_f32_16x16x32_bf16 v[192:195], a[156:159], v[72:75], v[192:195]\n"
    "ds_read_b128 a[132:135], v249 offset:20480\n"
    "v_smfmac_f32_16x16x64_bf16 v[108:111], v[104:107], v[68:75], v253\n"
    "s_add_u32 s81, s81, 128\n"
    "s_cmp_gt_u32 s81, 4096\n"
    "s_cselect_b32 s81, 0, s81\n"
    "v_smfmac_f32_16x16x64_bf16 v[112:115], v[104:107], v[76:83], v253\n"
    "v_mfma_f32_16x16x32_bf16 a[4:7], a[156:159], v[80:83], a[4:7]\n"
    "v_exp_f32 v11, v11\n"
    "v_mfma_f32_16x16x32_bf16 a[4:7], a[152:155], v[76:79], a[4:7]\n"
    "v_exp_f32 v12, v12\n"
    "v_mfma_f32_16x16x32_bf16 a[0:3], a[144:147], v[76:79], a[0:3]\n"
    "v_exp_f32 v13, v13\n"
    "v_mfma_f32_16x16x32_bf16 a[0:3], a[148:151], v[80:83], a[0:3]\n"
    "v_exp_f32 v14, v14\n"
    "v_mfma_f32_16x16x32_bf16 v[152:155], a[140:143], v[80:83], v[152:155]\n"
    "v_exp_f32 v15, v15\n"
    "v_mfma_f32_16x16x32_bf16 v[152:155], a[136:139], v[76:79], v[152:155]\n"
    "v_cvt_pk_bf16_f32 v226, v226, v227\n"
    "v_fma_f32 v228, v228, v36, -v38\n"
    "v_mfma_f32_16x16x32_bf16 v[168:171], a[136:139], v[84:87], v[168:171]\n"
    "v_exp_f32 v16, v16\n"
    "v_mfma_f32_16x16x32_bf16 v[168:171], a[140:143], v[88:91], v[168:171]\n"
    "v_exp_f32 v17, v17\n"
    "v_mfma_f32_16x16x32_bf16 a[16:19], a[148:151], v[88:91], a[16:19]\n"
    "v_fma_f32 v229, v229, v37, -v39\n"
    "v_fma_f32 v230, v230, v36, -v38\n"
    "v_mfma_f32_16x16x32_bf16 a[16:19], a[144:147], v[84:87], a[16:19]\n"
    "v_exp_f32 v18, v18\n"
    "v_mfma_f32_16x16x32_bf16 a[20:23], a[152:155], v[84:87], a[20:23]\n"
    "v_fma_f32 v231, v231, v37, -v39\n"
    "v_mfma_f32_16x16x32_bf16 a[20:23], a[156:159], v[88:91], a[20:23]\n"
    "v_smfmac_f32_16x16x64_bf16 v[116:119], v[104:107], v[84:91], v253\n"
    "v_smfmac_f32_16x16x64_bf16 v[120:123], v[104:107], v[92:99], v253\n"
    "v_mfma_f32_16x16x32_bf16 a[36:39], a[156:159], v[96:99], a[36:39]\n"
    "v_exp_f32 v19, v19\n"
    "v_mfma_f32_16x16x32_bf16 a[36:39], a[152:155], v[92:95], a[36:39]\n"
    "v_exp_f32 v20, v20\n"
    "v_mfma_f32_16x16x32_bf16 a[32:35], a[144:147], v[92:95], a[32:35]\n"
    "v_cvt_pk_bf16_f32 v228, v228, v229\n"
    "v_cvt_pk_bf16_f32 v230, v230, v231\n"
    "v_mfma_f32_16x16x32_bf16 a[32:35], a[148:151], v[96:99], a[32:35]\n"
    "v_exp_f32 v21, v21\n"
    "v_mfma_f32_16x16x32_bf16 v[184:187], a[140:143], v[96:99], v[184:187]\n"
    "v_fma_f32 v232, v232, v36, -v38\n"
    "v_mfma_f32_16x16x32_bf16 v[184:187], a[136:139], v[92:95], v[184:187]\n"
    "s_waitcnt lgkmcnt(0)\n"
    "v_mfma_f32_16x16x32_bf16 v[196:199], a[112:115], v[68:71], v[196:199]\n"
    "ds_read_b128 a[136:139], v249 offset:9216\n"
    "v_fma_f32 v233, v233, v37, -v39\n"
    "v_mfma_f32_16x16x32_bf16 v[196:199], a[116:119], v[72:75], v[196:199]\n"
    "ds_read_b128 a[140:143], v249 offset:21504\n"
    "v_mfma_f32_16x16x32_bf16 v[200:203], a[124:127], v[72:75], v[200:203]\n"
    "ds_read_b128 a[144:147], v249 offset:10240\n"
    "v_mfma_f32_16x16x32_bf16 v[200:203], a[120:123], v[68:71], v[200:203]\n"
    "ds_read_b128 a[148:151], v249 offset:22528\n"
    "v_fma_f32 v234, v234, v36, -v38\n"
    "v_mfma_f32_16x16x32_bf16 a[48:51], a[128:131], v[68:71], a[48:51]\n"
    "buffer_load_dwordx2 v[236:237], v241, s[36:39], s81 offen offset:0 sc1\n"
    "v_mfma_f32_16x16x32_bf16 a[48:51], a[132:135], v[72:75], a[48:51]\n"
    "ds_read_b128 a[152:155], v249 offset:11264\n"
    "v_fma_f32 v235, v235, v37, -v39\n"
    "v_mfma_f32_16x16x32_bf16 a[64:67], a[132:135], v[80:83], a[64:67]\n"
    "ds_read_b128 a[156:159], v249 offset:23552\n"
    "v_mfma_f32_16x16x32_bf16 a[64:67], a[128:131], v[76:79], a[64:67]\n"
    "buffer_store_dword v216, v244, s[16:19], s76 offen offset:1280 sc1\n"
    "v_mfma_f32_16x16x32_bf16 a[12:15], a[120:123], v[76:79], a[12:15]\n"
    "v_exp_f32 v22, v22\n"
    "v_mfma_f32_16x16x32_bf16 a[12:15], a[124:127], v[80:83], a[12:15]\n"
    "buffer_store_dword v218, v244, s[16:19], s76 offen offset:1408 sc1\n"
    "v_mfma_f32_16x16x32_bf16 a[8:11], a[116:119], v[80:83], a[8:11]\n"
    "v_exp_f32 v23, v23\n"
    "v_mfma_f32_16x16x32_bf16 a[8:11], a[112:115], v[76:79], a[8:11]\n"
    "buffer_store_dword v220, v244, s[16:19], s77 offen offset:0 sc1\n"
    "v_mfma_f32_16x16x32_bf16 a[24:27], a[112:115], v[84:87], a[24:27]\n"
    "v_exp_f32 v24, v24\n"
    "v_mfma_f32_16x16x32_bf16 a[24:27], a[116:119], v[88:91], a[24:27]\n"
    "buffer_store_dword v222, v244, s[16:19], s77 offen offset:128 sc1\n"
    "v_mfma_f32_16x16x32_bf16 a[28:31], a[124:127], v[88:91], a[28:31]\n"
    "v_exp_f32 v25, v25\n"
    "v_mfma_f32_16x16x32_bf16 a[28:31], a[120:123], v[84:87], a[28:31]\n"
    "buffer_store_dword v224, v244, s[16:19], s77 offen offset:1024 sc1\n"
    "v_mfma_f32_16x16x32_bf16 a[80:83], a[128:131], v[84:87], a[80:83]\n"
    "v_exp_f32 v26, v26\n"
    "v_mfma_f32_16x16x32_bf16 a[80:83], a[132:135], v[88:91], a[80:83]\n"
    "buffer_store_dword v226, v244, s[16:19], s77 offen offset:1152 sc1\n"
    "v_mfma_f32_16x16x32_bf16 a[96:99], a[132:135], v[96:99], a[96:99]\n"
    "v_cvt_pk_bf16_f32 v232, v232, v233\n"
    "v_cvt_pk_bf16_f32 v234, v234, v235\n"
    "v_mfma_f32_16x16x32_bf16 a[96:99], a[128:131], v[92:95], a[96:99]\n"
    "buffer_store_dword v228, v244, s[16:19], s77 offen offset:256 sc1\n"
    "v_mfma_f32_16x16x32_bf16 a[44:47], a[120:123], v[92:95], a[44:47]\n"
    "v_exp_f32 v27, v27\n"
    "v_mfma_f32_16x16x32_bf16 a[44:47], a[124:127], v[96:99], a[44:47]\n"
    "buffer_store_dword v230, v244, s[16:19], s77 offen offset:384 sc1\n"
    "v_mfma_f32_16x16x32_bf16 a[40:43], a[116:119], v[96:99], a[40:43]\n"
    "v_exp_f32 v28, v28\n"
    "v_mfma_f32_16x16x32_bf16 a[40:43], a[112:115], v[92:95], a[40:43]\n"
    "s_waitcnt lgkmcnt(0)\n"
    "v_exp_f32 v29, v29\n"
    "v_mfma_f32_16x16x32_bf16 a[52:55], a[136:139], v[68:71], a[52:55]\n"
    "ds_read_b128 a[112:115], v3 offset:12288\n"
    "v_mfma_f32_16x16x32_bf16 a[52:55], a[140:143], v[72:75], a[52:55]\n"
    "ds_read_b128 a[116:119], v3 offset:14336\n"
    "v_mfma_f32_16x16x32_bf16 a[56:59], a[148:151], v[72:75], a[56:59]\n"
    "ds_read_b128 a[120:123], v3 offset:16384\n"
    "v_mfma_f32_16x16x32_bf16 a[56:59], a[144:147], v[68:71], a[56:59]\n"
    "ds_read_b128 a[124:127], v3 offset:18432\n"
    "v_mfma_f32_16x16x32_bf16 a[60:63], a[152:155], v[68:71], a[60:63]\n"
    "ds_read_b128 a[128:131], v3 offset:20480\n"
    "v_mfma_f32_16x16x32_bf16 a[60:63], a[156:159], v[72:75], a[60:63]\n"
    "ds_read_b128 a[132:135], v3 offset:22528\n"
    "v_mfma_f32_16x16x32_bf16 a[76:79], a[156:159], v[80:83], a[76:79]\n"
    "ds_read_b128 v[100:103], v250 offset:1664\n"
    "v_mfma_f32_16x16x32_bf16 a[76:79], a[152:155], v[76:79], a[76:79]\n"
    "buffer_store_dword v232, v244, s[16:19], s77 offen offset:1280 sc1\n"
    "v_mfma_f32_16x16x32_bf16 a[72:75], a[144:147], v[76:79], a[72:75]\n"
    "v_exp_f32 v30, v30\n"
    "v_mfma_f32_16x16x32_bf16 a[72:75], a[148:151], v[80:83], a[72:75]\n"
    "buffer_store_dword v234, v244, s[16:19], s77 offen offset:1408 sc1\n"
    "v_mfma_f32_16x16x32_bf16 a[68:71], a[140:143], v[80:83], a[68:71]\n"
    "v_exp_f32 v31, v31\n"
    "v_mfma_f32_16x16x32_bf16 a[68:71], a[136:139], v[76:79], a[68:71]\n"
    "v_exp_f32 v32, v32\n"
    "v_mfma_f32_16x16x32_bf16 a[84:87], a[136:139], v[84:87], a[84:87]\n"
    "v_exp_f32 v33, v33\n"
    "v_mfma_f32_16x16x32_bf16 a[84:87], a[140:143], v[88:91], a[84:87]\n"
    "v_exp_f32 v34, v34\n"
    "v_mfma_f32_16x16x32_bf16 a[88:91], a[148:151], v[88:91], a[88:91]\n"
    "s_waitcnt vmcnt(17)\n"
    "ds_add_u32 v247, v246\n"
    "v_mfma_f32_16x16x32_bf16 a[88:91], a[144:147], v[84:87], a[88:91]\n"
    "v_exp_f32 v35, v35\n"
    "v_mfma_f32_16x16x32_bf16 a[92:95], a[152:155], v[84:87], a[92:95]\n"
    "v_mfma_f32_16x16x32_bf16 a[92:95], a[156:159], v[88:91], a[92:95]\n"
    "v_mfma_f32_16x16x32_bf16 a[108:111], a[156:159], v[96:99], a[108:111]\n"
    "v_mfma_f32_16x16x32_bf16 a[108:111], a[152:155], v[92:95], a[108:111]\n"
    "v_mfma_f32_16x16x32_bf16 a[104:107], a[144:147], v[92:95], a[104:107]\n"
    "s_sub_u32 s74, s68, s65\n"
    "v_add_u32 v250, 1536, v250\n"
    "v_add_u32 v251, 1536, v251\n"
    "v_mfma_f32_16x16x32_bf16 a[104:107], a[148:151], v[96:99], a[104:107]\n"
    "s_add_u32 s66, s66, 1\n"
    "v_mov_b32 v2, v3\n"
    "v_add_u32 v3, s74, v252\n"
    "v_mfma_f32_16x16x32_bf16 a[100:103], a[140:143], v[96:99], a[100:103]\n"
    "s_waitcnt lgkmcnt(1)\n"
    "s_cmp_lt_u32 s66, 29\n"
    "v_mfma_f32_16x16x32_bf16 a[100:103], a[136:139], v[92:95], a[100:103]\n"
    "s_cbranch_scc1 .LPK\n"
    "s_branch .Ltail\n"
    "s_nop 0\n"
    ".LpollAK:\n"
    "s_sleep 1\n"
    "buffer_load_dwordx2 v[236:237], v87, s[36:39], 0 offen offset:0 sc1\n"
    "s_waitcnt vmcnt(0)\n"
    "v_cmp_eq_u64 s[54:55], v[236:237], v[238:239]\n"
    "s_add_u32 s58, s58, 1\n"
    "s_cmp_eq_u64 s[54:55], -1\n"
    "s_cbranch_scc1 .LpollAKr\n"
    "s_cmp_lt_u32 s58, 0x80000\n"
    "s_cbranch_scc1 .LpollAK\n"
    "s_branch .LpollAKr\n"
    "s_nop 0\n"
    "s_nop 0\n"
    "s_nop 0\n"
    ".LpollAV:\n"
    "s_sleep 1\n"
    "buffer_load_dwordx2 v[236:237], v87, s[36:39], 0 offen offset:0 sc1\n"
    "s_waitcnt vmcnt(0)\n"
    "v_cmp_eq_u64 s[54:55], v[236:237], v[238:239]\n"
    "s_add_u32 s58, s58, 1\n"
    "s_cmp_eq_u64 s[54:55], -1\n"
    "s_cbranch_scc1 .LpollAVr\n"
    "s_cmp_lt_u32 s58, 0x80000\n"
    "s_cbranch_scc1 .LpollAV\n"
    "s_branch .LpollAVr\n"
    "s_nop 0\n"
    "s_nop 0\n"
    "s_nop 0\n"
    "s_nop 0\n"
    "s_nop 0\n"
    "s_nop 0\n"
    "s_nop 0\n"
    ".Lpoll1:\n"
    "s_sleep 1\n"
    "v_cmp_eq_u64 s[54:55], v[236:237], v[238:239]\n"
    "s_nop 1\n"
    "s_cmp_eq_u64 s[54:55], -1\n"
    "s_cbranch_scc1 .Lpoll1l\n"
    "buffer_load_dwordx2 v[236:237], v241, s[36:39], s81 offen offset:0 sc1\n"
    "s_waitcnt vmcnt(0)\n"
    "v_cmp_eq_u64 s[54:55], v[236:237], v[238:239]\n"
    ".Lpoll1l:\n"
    "ds_read_b32 v245, v247\n"
    "s_waitcnt lgkmcnt(0)\n"
    "v_cmp_le_u32 s[56:57], s59, v245\n"
    "s_add_u32 s58, s58, 1\n"
    "s_nop 1\n"
    "s_and_b64 s[54:55], s[54:55], s[56:57]\n"
    "s_cmp_eq_u64 s[54:55], -1\n"
    "s_cbranch_scc1 .Lpoll1r\n"
    "s_cmp_lt_u32 s58, 0x80000\n"
    "s_cbranch_scc1 .Lpoll1\n"
    "s_branch .Lpoll1r\n"
    "s_nop 0\n"
    "s_nop 0\n"
    "s_nop 0\n"
    "s_nop 0\n"
    "s_nop 0\n"
    "s_nop 0\n"
    "s_nop 0\n"
    ".Lpoll2:\n"
    "s_sleep 1\n"
    "v_cmp_eq_u64 s[54:55], v[236:237], v[238:239]\n"
    "s_nop 1\n"
    "s_cmp_eq_u64 s[54:55], -1\n"
    "s_cbranch_scc1 .Lpoll2l\n"
    "buffer_load_dwordx2 v[236:237], v241, s[36:39], s81 offen offset:0 sc1\n"
    "s_waitcnt vmcnt(0)\n"
    "v_cmp_eq_u64 s[54:55], v[236:237], v[238:239]\n"
    ".Lpoll2l:\n"
    "ds_read_b32 v245, v247\n"
    "s_waitcnt lgkmcnt(0)\n"
    "v_cmp_le_u32 s[56:57], s59, v245\n"
    "s_add_u32 s58, s58, 1\n"
    "s_nop 1\n"
    "s_and_b64 s[54:55], s[54:55], s[56:57]\n"
    "s_cmp_eq_u64 s[54:55], -1\n"
    "s_cbranch_scc1 .Lpoll2r\n"
    "s_cmp_lt_u32 s58, 0x80000\n"
    "s_cbranch_scc1 .Lpoll2\n"
    "s_branch .Lpoll2r\n"
    "s_nop 0\n"
    "s_nop 0\n"
    "s_nop 0\n"
    "s_nop 0\n"
    "s_nop 0\n"
    "s_nop 0\n"
    "s_nop 0\n"
    ".Lpoll3:\n"
    "s_sleep 1\n"
    "v_cmp_eq_u64 s[54:55], v[236:237], v[238:239]\n"
    "s_nop 1\n"
    "s_cmp_eq_u64 s[54:55], -1\n"
    "s_cbranch_scc1 .Lpoll3l\n"
    "buffer_load_dwordx2 v[236:237], v241, s[36:39], s81 offen offset:0 sc1\n"
    "s_waitcnt vmcnt(0)\n"
    "v_cmp_eq_u64 s[54:55], v[236:237], v[238:239]\n"
    ".Lpoll3l:\n"
    "ds_read_b32 v245, v247\n"
    "s_waitcnt lgkmcnt(0)\n"
    "v_cmp_le_u32 s[56:57], s59, v245\n"
    "s_add_u32 s58, s58, 1\n"
    "s_nop 1\n"
    "s_and_b64 s[54:55], s[54:55], s[56:57]\n"
    "s_cmp_eq_u64 s[54:55], -1\n"
    "s_cbranch_scc1 .Lpoll3r\n"
    "s_cmp_lt_u32 s58, 0x80000\n"
    "s_cbranch_scc1 .Lpoll3\n"
    "s_branch .Lpoll3r\n"
    "s_nop 0\n"
    "s_nop 0\n"
    "s_nop 0\n"
    "s_nop 0\n"
    "s_nop 0\n"
    "s_nop 0\n"
    "s_nop 0\n"
    ".Lpoll4:\n"
    "s_sleep 1\n"
    "v_cmp_eq_u64 s[54:55], v[236:237], v[238:239]\n"
    "s_nop 1\n"
    "s_cmp_eq_u64 s[54:55], -1\n"
    "s_cbranch_scc1 .Lpoll4l\n"
    "buffer_load_dwordx2 v[236:237], v241, s[36:39], s81 offen offset:0 sc1\n"
    "s_waitcnt vmcnt(0)\n"
    "v_cmp_eq_u64 s[54:55], v[236:237], v[238:239]\n"
    ".Lpoll4l:\n"
    "ds_read_b32 v245, v247\n"
    "s_waitcnt lgkmcnt(0)\n"
    "v_cmp_le_u32 s[56:57], s59, v245\n"
    "s_add_u32 s58, s58, 1\n"
    "s_nop 1\n"
    "s_and_b64 s[54:55], s[54:55], s[56:57]\n"
    "s_cmp_eq_u64 s[54:55], -1\n"
    "s_cbranch_scc1 .Lpoll4r\n"
    "s_cmp_lt_u32 s58, 0x80000\n"
    "s_cbranch_scc1 .Lpoll4\n"
    "s_branch .Lpoll4r\n"
    "s_nop 0\n"
    "s_nop 0\n"
    "s_nop 0\n"
    "s_nop 0\n"
    "s_nop 0\n"
    "s_nop 0\n"
    "s_nop 0\n"
    ".Lpt0:\n"
    "s_sleep 1\n"
    "v_cmp_eq_u64 s[54:55], v[236:237], v[238:239]\n"
    "s_nop 1\n"
    "s_cmp_eq_u64 s[54:55], -1\n"
    "s_cbranch_scc1 .Lpt0l\n"
    "buffer_load_dwordx2 v[236:237], v241, s[36:39], s81 offen offset:0 sc1\n"
    "s_waitcnt vmcnt(0)\n"
    "v_cmp_eq_u64 s[54:55], v[236:237], v[238:239]\n"
    ".Lpt0l:\n"
    "ds_read_b32 v245, v247\n"
    "s_waitcnt lgkmcnt(0)\n"
    "v_cmp_le_u32 s[56:57], s59, v245\n"
    "s_add_u32 s58, s58, 1\n"
    "s_nop 1\n"
    "s_and_b64 s[54:55], s[54:55], s[56:57]\n"
    "s_cmp_eq_u64 s[54:55], -1\n"
    "s_cbranch_scc1 .Lpt0r\n"
    "s_cmp_lt_u32 s58, 0x80000\n"
    "s_cbranch_scc1 .Lpt0\n"
    "s_branch .Lpt0r\n"
    "s_nop 0\n"
    "s_nop 0\n"
    "s_nop 0\n"
    ".Lpt1:\n"
    "s_sleep 1\n"
    "ds_read_b32 v245, v247\n"
    "s_waitcnt lgkmcnt(0)\n"
    "v_cmp_le_u32 s[56:57], s59, v245\n"
    "s_add_u32 s58, s58, 1\n"
    "s_nop 1\n"
    "s_cmp_eq_u64 s[56:57], -1\n"
    "s_cbranch_scc1 .Lpt1r\n"
    "s_cmp_lt_u32 s58, 0x80000\n"
    "s_cbranch_scc1 .Lpt1\n"
    "s_branch .Lpt1r\n"
    "s_nop 0\n"
    "s_nop 0\n"
    "s_nop 0\n"
    ".Lpt4:\n"
    "s_sleep 1\n"
    "ds_read_b32 v245, v247\n"
    "s_waitcnt lgkmcnt(0)\n"
    "v_cmp_le_u32 s[56:57], s59, v245\n"
    "s_add_u32 s58, s58, 1\n"
    "s_nop 1\n"
    "s_cmp_eq_u64 s[56:57], -1\n"
    "s_cbranch_scc1 .Lpt4r\n"
    "s_cmp_lt_u32 s58, 0x80000\n"
    "s_cbranch_scc1 .Lpt4\n"
    "s_branch .Lpt4r\n"
    "s_nop 0\n"
    ".Lpt3:\n"
    "s_sleep 1\n"
    "ds_read_b32 v245, v247\n"
    "s_waitcnt lgkmcnt(0)\n"
    "v_cmp_le_u32 s[56:57], s59, v245\n"
    "s_add_u32 s58, s58, 1\n"
    "s_nop 1\n"
    "s_cmp_eq_u64 s[56:57], -1\n"
    "s_cbranch_scc1 .Lpt3r\n"
    "s_cmp_lt_u32 s58, 0x80000\n"
    "s_cbranch_scc1 .Lpt3\n"
    "s_branch .Lpt3r\n"
    ".fill ((0x20000 - (.LwVe - .LwV) - (. - _Z8attn_fwdPKtPKiS2_S0_S0_S0_S0_PcPt)) & 4095) / 4, 4, 0xBF800000\n"
    ".LwV:\n"
    "s_branch .LwVe\n"
    ".LPV:\n"
    "v_mfma_f32_16x16x32_bf16 v[36:39], a[112:115], a[192:195], v[100:103]\n"
    "s_add_u32 s71, s71, s44\n"
    "ds_read_b128 a[136:139], v2 offset:13312\n"
    "v_cvt_pk_bf16_f32 v68, v4, v5\n"
    "v_mfma_f32_16x16x32_bf16 v[36:39], a[116:119], a[196:199], v[36:39]\n"
    "s_add_u32 s75, s71, s63\n"
    "ds_read_b128 a[140:143], v2 offset:15360\n"
    "v_cvt_pk_bf16_f32 v69, v6, v7\n"
    "v_mfma_f32_16x16x32_bf16 v[36:39], a[120:123], a[200:203], v[36:39]\n"
    "ds_read_b128 a[144:147], v2 offset:17408\n"
    "v_cvt_pk_bf16_f32 v76, v8, v9\n"
    "v_mfma_f32_16x16x32_bf16 v[36:39], a[124:127], a[204:207], v[36:39]\n"
    "ds_read_b128 a[148:151], v2 offset:19456\n"
    "v_cvt_pk_bf16_f32 v77, v10, v11\n"
    "v_mfma_f32_16x16x32_bf16 v[36:39], a[128:131], a[208:211], v[36:39]\n"
    "ds_read_b128 a[152:155], v2 offset:21504\n"
    "v_cvt_pk_bf16_f32 v84, v12, v13\n"
    "v_mfma_f32_16x16x32_bf16 v[36:39], a[132:135], a[212:215], v[36:39]\n"
    "ds_read_b128 a[156:159], v2 offset:23552\n"
    "v_cvt_pk_bf16_f32 v85, v14, v15\n"
    "v_mfma_f32_16x16x32_bf16 v[40:43], a[132:135], a[236:239], v[100:103]\n"
    "ds_read_b128 v[104:107], v250 offset:144\n"
    "v_cvt_pk_bf16_f32 v92, v16, v17\n"
    "v_mfma_f32_16x16x32_bf16 v[40:43], a[128:131], a[232:235], v[40:43]\n"
    "s_add_u32 s68, s68, 24576\n"
    "v_cvt_pk_bf16_f32 v93, v18, v19\n"
    "v_cvt_pk_bf16_f32 v70, v20, v21\n"
    "v_mfma_f32_16x16x32_bf16 v[40:43], a[124:127], a[228:231], v[40:43]\n"
    "s_cmp_ge_u32 s68, s69\n"
    "s_cselect_b32 s68, s65, s68\n"
    "v_cvt_pk_bf16_f32 v71, v22, v23\n"
    "v_mfma_f32_16x16x32_bf16 v[40:43], a[120:123], a[224:227], v[40:43]\n"
    "v_cvt_pk_bf16_f32 v78, v24, v25\n"
    "v_cvt_pk_bf16_f32 v79, v26, v27\n"
    "v_mfma_f32_16x16x32_bf16 v[40:43], a[116:119], a[220:223], v[40:43]\n"
    "v_cvt_pk_bf16_f32 v86, v28, v29\n"
    "v_cvt_pk_bf16_f32 v87, v30, v31\n"
    "v_mfma_f32_16x16x32_bf16 v[40:43], a[112:115], a[216:219], v[40:43]\n"
    "v_cvt_pk_bf16_f32 v94, v32, v33\n"
    "v_cvt_pk_bf16_f32 v95, v34, v35\n"
    "v_mfma_f32_16x16x32_bf16 v[44:47], a[112:115], a[240:243], v[100:103]\n"
    "v_exp_f32 v36, v36\n"
    "v_mfma_f32_16x16x32_bf16 v[44:47], a[116:119], a[244:247], v[44:47]\n"
    "v_exp_f32 v37, v37\n"
    "v_mfma_f32_16x16x32_bf16 v[44:47], a[120:123], a[248:251], v[44:47]\n"
    "v_exp_f32 v38, v38\n"
    "v_mfma_f32_16x16x32_bf16 v[44:47], a[124:127], a[252:255], v[44:47]\n"
    "v_exp_f32 v39, v39\n"
    "v_mfma_f32_16x16x32_bf16 v[44:47], a[128:131], a[160:163], v[44:47]\n"
    "v_cvt_pk_bf16_f32 v72, v36, v37\n"
    "v_cvt_pk_bf16_f32 v73, v38, v39\n"
    "v_mfma_f32_16x16x32_bf16 v[44:47], a[132:135], a[164:167], v[44:47]\n"
    "v_exp_f32 v40, v40\n"
    "v_mfma_f32_16x16x32_bf16 v[48:51], a[132:135], a[188:191], v[100:103]\n"
    "ds_read_b32 v245, v247\n"
    "v_mfma_f32_16x16x32_bf16 v[48:51], a[128:131], a[184:187], v[48:51]\n"
    "s_add_u32 m0, s68, 0\n"
    "v_exp_f32 v41, v41\n"
    "v_mfma_f32_16x16x32_bf16 v[48:51], a[124:127], a[180:183], v[48:51]\n"
    "v_exp_f32 v42, v42\n"
    "v_mfma_f32_16x16x32_bf16 v[48:51], a[120:123], a[176:179], v[48:51]\n"
    "v_exp_f32 v43, v43\n"
    "v_mfma_f32_16x16x32_bf16 v[48:51], a[116:119], a[172:175], v[48:51]\n"
    "s_waitcnt vmcnt(13)\n"
    "v_cmp_eq_u64 s[54:55], v[236:237], v[238:239]\n"
    "v_cvt_pk_bf16_f32 v80, v40, v41\n"
    "v_mfma_f32_16x16x32_bf16 v[48:51], a[112:115], a[168:171], v[48:51]\n"
    "s_waitcnt lgkmcnt(1)\n"
    "v_cvt_pk_bf16_f32 v81, v42, v43\n"
    "v_mfma_f32_16x16x32_bf16 v[52:55], a[136:139], a[192:195], v[104:107]\n"
    "s_waitcnt lgkmcnt(0)\n"
    "v_cmp_le_u32 s[56:57], s59, v245\n"
    "v_mfma_f32_16x16x32_bf16 v[52:55], a[140:143], a[196:199], v[52:55]\n"
    "v_exp_f32 v44, v44\n"
    "v_mfma_f32_16x16x32_bf16 v[52:55], a[144:147], a[200:203], v[52:55]\n"
    "s_and_b64 s[54:55], s[54:55], s[56:57]\n"
    "s_cmp_eq_u64 s[54:55], -1\n"
    "v_mfma_f32_16x16x32_bf16 v[52:55], a[148:151], a[204:207], v[52:55]\n"
    "s_cbranch_scc0 .Lpoll3\n"
    ".Lpoll3r:\n"
    "v_mfma_f32_16x16x32_bf16 v[52:55], a[152:155], a[208:211], v[52:55]\n"
    "buffer_load_dwordx4 v1, s[16:19], s67 offen offset:0 sc1 lds\n"
    "v_mfma_f32_16x16x32_bf16 v[52:55], a[156:159], a[212:215], v[52:55]\n"
    "ds_read_b128 a[112:115], v3 offset:0\n"
    "v_mfma_f32_16x16x32_bf16 v[56:59], a[156:159], a[236:239], v[104:107]\n"
    "ds_read_b128 a[116:119], v3 offset:2048\n"
    "v_mfma_f32_16x16x32_bf16 v[56:59], a[152:155], a[232:235], v[56:59]\n"
    "ds_read_b128 a[120:123], v3 offset:4096\n"
    "v_mfma_f32_16x16x32_bf16 v[56:59], a[148:151], a[228:231], v[56:59]\n"
    "buffer_load_dwordx4 v1, s[16:19], s67 offen offset:1024 sc1 lds\n"
    "v_mfma_f32_16x16x32_bf16 v[56:59], a[144:147], a[224:227], v[56:59]\n"
    "ds_read_b128 a[124:127], v3 offset:6144\n"
    "v_mfma_f32_16x16x32_bf16 v[56:59], a[140:143], a[220:223], v[56:59]\n"
    "ds_read_b128 a[128:131], v3 offset:8192\n"
    "v_mfma_f32_16x16x32_bf16 v[56:59], a[136:139], a[216:219], v[56:59]\n"
    "ds_read_b128 a[132:135], v3 offset:10240\n"
    "v_mfma_f32_16x16x32_bf16 v[60:63], a[136:139], a[240:243], v[104:107]\n"
    "buffer_load_dwordx4 v1, s[16:19], s67 offen offset:2048 sc1 lds\n"
    "v_mfma_f32_16x16x32_bf16 v[60:63], a[140:143], a[244:247], v[60:63]\n"
    "ds_read_b128 v[100:103], v250 offset:768\n"
    "v_mfma_f32_16x16x32_bf16 v[60:63], a[144:147], a[248:251], v[60:63]\n"
    "v_exp_f32 v52, v52\n"
    "v_mfma_f32_16x16x32_bf16 v[60:63], a[148:151], a[252:255], v[60:63]\n"
    "v_exp_f32 v53, v53\n"
    "v_mfma_f32_16x16x32_bf16 v[60:63], a[152:155], a[160:163], v[60:63]\n"
    "buffer_load_dwordx4 v1, s[16:19], s67 offen offset:3072 sc1 lds\n"
    "v_mfma_f32_16x16x32_bf16 v[60:63], a[156:159], a[164:167], v[60:63]\n"
    "s_add_u32 m0, s68, 4096\n"
    "v_exp_f32 v54, v54\n"
    "v_mfma_f32_16x16x32_bf16 v[64:67], a[156:159], a[188:191], v[104:107]\n"
    "s_add_u32 s83, s67, 4096\n"
    "v_exp_f32 v55, v55\n"
    "v_mfma_f32_16x16x32_bf16 v[64:67], a[152:155], a[184:187], v[64:67]\n"
    "v_cvt_pk_bf16_f32 v74, v52, v53\n"
    "v_cvt_pk_bf16_f32 v75, v54, v55\n"
    "v_mfma_f32_16x16x32_bf16 v[64:67], a[148:151], a[180:183], v[64:67]\n"
    "buffer_load_dwordx4 v1, s[16:19], s83 offen offset:0 sc1 lds\n"
    "v_mfma_f32_16x16x32_bf16 v[64:67], a[144:147], a[176:179], v[64:67]\n"
    "v_exp_f32 v56, v56\n"
    "v_mfma_f32_16x16x32_bf16 v[64:67], a[140:143], a[172:175], v[64:67]\n"
    "v_exp_f32 v57, v57\n"
    "v_mfma_f32_16x16x32_bf16 v[64:67], a[136:139], a[168:171], v[64:67]\n"
    "s_waitcnt lgkmcnt(0)\n"
    "v_exp_f32 v58, v58\n"
    "v_mfma_f32_16x16x32_bf16 v[4:7], a[112:115], a[192:195], v[100:103]\n"
    "buffer_load_dwordx4 v1, s[16:19], s83 offen offset:1024 sc1 lds\n"
    "v_mfma_f32_16x16x32_bf16 v[4:7], a[116:119], a[196:199], v[4:7]\n"
    "s_add_u32 m0, s64, 0\n"
    "ds_read_b128 a[136:139], v3 offset:1024\n"
    "v_cvt_pk_bf16_f32 v82, v56, v57\n"
    "v_mfma_f32_16x16x32_bf16 v[4:7], a[120:123], a[200:203], v[4:7]\n"
    "s_add_u32 s83, s67, 24576\n"
    "ds_read_b128 a[140:143], v3 offset:3072\n"
    "v_mfma_f32_16x16x32_bf16 v[4:7], a[124:127], a[204:207], v[4:7]\n"
    "ds_read_b128 a[144:147], v3 offset:5120\n"
    "v_mfma_f32_16x16x32_bf16 v[4:7], a[128:131], a[208:211], v[4:7]\n"
    "buffer_load_dwordx4 v1, s[16:19], s83 offen offset:0 sc1 lds\n"
    "v_mfma_f32_16x16x32_bf16 v[4:7], a[132:135], a[212:215], v[4:7]\n"
    "ds_read_b128 a[148:151], v3 offset:7168\n"
    "v_mfma_f32_16x16x32_bf16 v[8:11], a[132:135], a[236:239], v[100:103]\n"
    "ds_read_b128 a[152:155], v3 offset:9216\n"
    "v_mfma_f32_16x16x32_bf16 v[8:11], a[128:131], a[232:235], v[8:11]\n"
    "ds_read_b128 a[156:159], v3 offset:11264\n"
    "v_mfma_f32_16x16x32_bf16 v[8:11], a[124:127], a[228:231], v[8:11]\n"
    "buffer_load_dwordx4 v1, s[16:19], s83 offen offset:1024 sc1 lds\n"
    "v_mfma_f32_16x16x32_bf16 v[8:11], a[120:123], a[224:227], v[8:11]\n"
    "ds_read_b128 v[104:107], v250 offset:784\n"
    "v_mfma_f32_16x16x32_bf16 v[8:11], a[116:119], a[220:223], v[8:11]\n"
    "v_exp_f32 v59, v59\n"
    "v_mfma_f32_16x16x32_bf16 v[8:11], a[112:115], a[216:219], v[8:11]\n"
    "v_cvt_pk_bf16_f32 v83, v58, v59\n"
    "v_mfma_f32_16x16x32_bf16 v[12:15], a[112:115], a[240:243], v[100:103]\n"
    "buffer_load_dwordx4 v1, s[16:19], s83 offen offset:2048 sc1 lds\n"
    "v_mfma_f32_16x16x32_bf16 v[12:15], a[116:119], a[244:247], v[12:15]\n"
    "v_exp_f32 v45, v45\n"
    "v_mfma_f32_16x16x32_bf16 v[12:15], a[120:123], a[248:251], v[12:15]\n"
    "v_exp_f32 v46, v46\n"
    "v_mfma_f32_16x16x32_bf16 v[12:15], a[124:127], a[252:255], v[12:15]\n"
    "v_exp_f32 v47, v47\n"
    "v_mfma_f32_16x16x32_bf16 v[12:15], a[128:131], a[160:163], v[12:15]\n"
    "buffer_load_dwordx4 v1, s[16:19], s83 offen offset:3072 sc1 lds\n"
    "v_mfma_f32_16x16x32_bf16 v[12:15], a[132:135], a[164:167], v[12:15]\n"
    "s_add_u32 m0, s64, 4096\n"
    "v_exp_f32 v60, v60\n"
    "v_mfma_f32_16x16x32_bf16 v[16:19], a[132:135], a[188:191], v[100:103]\n"
    "s_add_u32 s83, s67, 28672\n"
    "v_exp_f32 v61, v61\n"
    "v_mfma_f32_16x16x32_bf16 v[16:19], a[128:131], a[184:187], v[16:19]\n"
    "v_exp_f32 v62, v62\n"
    "v_mfma_f32_16x16x32_bf16 v[16:19], a[124:127], a[180:183], v[16:19]\n"
    "buffer_load_dwordx4 v1, s[16:19], s83 offen offset:0 sc1 lds\n"
    "v_mfma_f32_16x16x32_bf16 v[16:19], a[120:123], a[176:179], v[16:19]\n"
    "v_exp_f32 v63, v63\n"
    "v_mfma_f32_16x16x32_bf16 v[16:19], a[116:119], a[172:175], v[16:19]\n"
    "v_cvt_pk_bf16_f32 v88, v44, v45\n"
    "v_cvt_pk_bf16_f32 v89, v46, v47\n"
    "v_mfma_f32_16x16x32_bf16 v[16:19], a[112:115], a[168:171], v[16:19]\n"
    "s_waitcnt lgkmcnt(0)\n"
    "v_cvt_pk_bf16_f32 v90, v60, v61\n"
    "v_cvt_pk_bf16_f32 v91, v62, v63\n"
    "v_mfma_f32_16x16x32_bf16 v[20:23], a[136:139], a[192:195], v[104:107]\n"
    "buffer_load_dwordx4 v1, s[16:19], s83 offen offset:1024 sc1 lds\n"
    "v_mfma_f32_16x16x32_bf16 v[20:23], a[140:143], a[196:199], v[20:23]\n"
    "s_add_u32 s67, s67, 49152\n"
    "ds_read_b128 a[112:115], v248 offset:0\n"
    "v_mfma_f32_16x16x32_bf16 v[20:23], a[144:147], a[200:203], v[20:23]\n"
    "s_add_u32 s59, s59, 4\n"
    "ds_read_b128 a[116:119], v248 offset:12288\n"
    "v_mfma_f32_16x16x32_bf16 v[20:23], a[148:151], a[204:207], v[20:23]\n"
    "s_waitcnt vmcnt(12)\n"
    "ds_read_b128 a[120:123], v248 offset:1024\n"
    "v_mfma_f32_16x16x32_bf16 v[20:23], a[152:155], a[208:211], v[20:23]\n"
    "buffer_store_dwordx2 v[238:239], v240, s[36:39], s80 offen offset:0 sc1\n"
    "v_mfma_f32_16x16x32_bf16 v[20:23], a[156:159], a[212:215], v[20:23]\n"
    "buffer_load_dwordx4 v[204:207], v242, s[40:43], s75 offen offset:0 nt\n"
    "v_mfma_f32_16x16x32_bf16 v[24:27], a[156:159], a[236:239], v[104:107]\n"
    "s_add_u32 s80, s80, 256\n"
    "ds_read_b128 a[124:127], v248 offset:13312\n"
    "v_mfma_f32_16x16x32_bf16 v[24:27], a[152:155], a[232:235], v[24:27]\n"
    "buffer_load_dwordx4 v[208:211], v242, s[40:43], s75 offen offset:768 nt\n"
    "v_mfma_f32_16x16x32_bf16 v[24:27], a[148:151], a[228:231], v[24:27]\n"
    "ds_read_b128 a[128:131], v248 offset:2048\n"
    "v_mfma_f32_16x16x32_bf16 v[24:27], a[144:147], a[224:227], v[24:27]\n"
    "buffer_load_dwordx4 v[212:215], v242, s[40:43], s75 offen offset:256 nt\n"
    "v_mfma_f32_16x16x32_bf16 v[24:27], a[140:143], a[220:223], v[24:27]\n"
    "ds_read_b128 a[132:135], v248 offset:14336\n"
    "v_mfma_f32_16x16x32_bf16 v[24:27], a[136:139], a[216:219], v[24:27]\n"
    "buffer_load_dwordx4 v[216:219], v242, s[40:43], s75 offen offset:1024 nt\n"
    "v_mfma_f32_16x16x32_bf16 v[28:31], a[136:139], a[240:243], v[104:107]\n"
    "v_exp_f32 v48, v48\n"
    "v_mfma_f32_16x16x32_bf16 v[28:31], a[140:143], a[244:247], v[28:31]\n"
    "buffer_load_dwordx4 v[220:223], v242, s[40:43], s75 offen offset:512 nt\n"
    "v_mfma_f32_16x16x32_bf16 v[28:31], a[144:147], a[248:251], v[28:31]\n"
    "v_exp_f32 v49, v49\n"
    "v_mfma_f32_16x16x32_bf16 v[28:31], a[148:151], a[252:255], v[28:31]\n"
    "buffer_load_dwordx4 v[224:227], v242, s[40:43], s75 offen offset:1280 nt\n"
    "v_mfma_f32_16x16x32_bf16 v[28:31], a[152:155], a[160:163], v[28:31]\n"
    "v_exp_f32 v50, v50\n"
    "v_mfma_f32_16x16x32_bf16 v[28:31], a[156:159], a[164:167], v[28:31]\n"
    "v_exp_f32 v51, v51\n"
    "v_mfma_f32_16x16x32_bf16 v[32:35], a[156:159], a[188:191], v[104:107]\n"
    "v_exp_f32 v64, v64\n"
    "v_mfma_f32_16x16x32_bf16 v[32:35], a[152:155], a[184:187], v[32:35]\n"
    "v_exp_f32 v65, v65\n"
    "v_mfma_f32_16x16x32_bf16 v[32:35], a[148:151], a[180:183], v[32:35]\n"
    "v_exp_f32 v66, v66\n"
    "v_mfma_f32_16x16x32_bf16 v[32:35], a[144:147], a[176:179], v[32:35]\n"
    "v_exp_f32 v67, v67\n"
    "v_mfma_f32_16x16x32_bf16 v[32:35], a[140:143], a[172:175], v[32:35]\n"
    "v_cvt_pk_bf16_f32 v96, v48, v49\n"
    "v_cvt_pk_bf16_f32 v97, v50, v51\n"
    "v_mfma_f32_16x16x32_bf16 v[32:35], a[136:139], a[168:171], v[32:35]\n"
    "s_waitcnt lgkmcnt(0)\n"
    "v_cvt_pk_bf16_f32 v98, v64, v65\n"
    "v_cvt_pk_bf16_f32 v99, v66, v67\n"
    "v_mfma_f32_16x16x32_bf16 v[124:127], a[112:115], v[68:71], v[124:127]\n"
    "ds_read_b128 a[136:139], v248 offset:3072\n"
    "v_mfma_f32_16x16x32_bf16 v[124:127], a[116:119], v[72:75], v[124:127]\n"
    "ds_read_b128 a[140:143], v248 offset:15360\n"
    "v_mfma_f32_16x16x32_bf16 v[128:131], a[124:127], v[72:75], v[128:131]\n"
    "ds_read_b128 a[144:147], v248 offset:4096\n"
    "v_mfma_f32_16x16x32_bf16 v[128:131], a[120:123], v[68:71], v[128:131]\n"
    "ds_read_b128 a[148:151], v248 offset:16384\n"
    "v_mfma_f32_16x16x32_bf16 v[132:135], a[128:131], v[68:71], v[132:135]\n"
    "ds_read_b128 a[152:155], v248 offset:5120\n"
    "v_mfma_f32_16x16x32_bf16 v[132:135], a[132:135], v[72:75], v[132:135]\n"
    "ds_read_b128 a[156:159], v248 offset:17408\n"
    "v_mfma_f32_16x16x32_bf16 v[148:151], a[132:135], v[80:83], v[148:151]\n"
    "ds_read_b128 v[104:107], v251 offset:0\n"
    "v_mfma_f32_16x16x32_bf16 v[148:151], a[128:131], v[76:79], v[148:151]\n"
    "v_exp_f32 v4, v4\n"
    "v_mfma_f32_16x16x32_bf16 v[144:147], a[120:123], v[76:79], v[144:147]\n"
    "v_exp_f32 v5, v5\n"
    "v_mfma_f32_16x16x32_bf16 v[144:147], a[124:127], v[80:83], v[144:147]\n"
    "v_exp_f32 v6, v6\n"
    "v_mfma_f32_16x16x32_bf16 v[140:143], a[116:119], v[80:83], v[140:143]\n"
    "v_mfma_f32_16x16x32_bf16 v[140:143], a[112:115], v[76:79], v[140:143]\n"
    "v_mfma_f32_16x16x32_bf16 v[156:159], a[112:115], v[84:87], v[156:159]\n"
    "v_exp_f32 v7, v7\n"
    "v_mfma_f32_16x16x32_bf16 v[156:159], a[116:119], v[88:91], v[156:159]\n"
    "v_mfma_f32_16x16x32_bf16 v[160:163], a[124:127], v[88:91], v[160:163]\n"
    "v_exp_f32 v8, v8\n"
    "v_mfma_f32_16x16x32_bf16 v[160:163], a[120:123], v[84:87], v[160:163]\n"
    "v_mfma_f32_16x16x32_bf16 v[164:167], a[128:131], v[84:87], v[164:167]\n"
    "v_mfma_f32_16x16x32_bf16 v[164:167], a[132:135], v[88:91], v[164:167]\n"
    "v_exp_f32 v9, v9\n"
    "v_mfma_f32_16x16x32_bf16 v[180:183], a[132:135], v[96:99], v[180:183]\n"
    "v_mfma_f32_16x16x32_bf16 v[180:183], a[128:131], v[92:95], v[180:183]\n"
    "v_mfma_f32_16x16x32_bf16 v[176:179], a[120:123], v[92:95], v[176:179]\n"
    "v_exp_f32 v10, v10\n"
    "v_mfma_f32_16x16x32_bf16 v[176:179], a[124:127], v[96:99], v[176:179]\n"
    "v_mfma_f32_16x16x32_bf16 v[172:175], a[116:119], v[96:99], v[172:175]\n"
    "v_exp_f32 v11, v11\n"
    "v_mfma_f32_16x16x32_bf16 v[172:175], a[112:115], v[92:95], v[172:175]\n"
    "s_waitcnt lgkmcnt(0)\n"
    "v_mfma_f32_16x16x32_bf16 v[136:139], a[136:139], v[68:71], v[136:139]\n"
    "ds_read_b128 a[112:115], v248 offset:6144\n"
    "v_mfma_f32_16x16x32_bf16 v[136:139], a[140:143], v[72:75], v[136:139]\n"
    "ds_read_b128 a[116:119], v248 offset:18432\n"
    "v_mfma_f32_16x16x32_bf16 v[188:191], a[148:151], v[72:75], v[188:191]\n"
    "ds_read_b128 a[120:123], v248 offset:7168\n"
    "v_mfma_f32_16x16x32_bf16 v[188:191], a[144:147], v[68:71], v[188:191]\n"
    "ds_read_b128 a[124:127], v248 offset:19456\n"
    "v_mfma_f32_16x16x32_bf16 v[192:195], a[152:155], v[68:71], v[192:195]\n"
    "ds_read_b128 a[128:131], v248 offset:8192\n"
    "v_mfma_f32_16x16x32_bf16 v[192:195], a[156:159], v[72:75], v[192:195]\n"
    "ds_read_b128 a[132:135], v248 offset:20480\n"
    "v_smfmac_f32_16x16x64_bf16 v[108:111], v[104:107], v[68:75], v253\n"
    "s_add_u32 s81, s81, 128\n"
    "s_cmp_gt_u32 s81, 4096\n"
    "s_cselect_b32 s81, 0, s81\n"
    "v_smfmac_f32_16x16x64_bf16 v[112:115], v[104:107], v[76:83], v253\n"
    "s_cmp_ge_u32 s75, 1474560\n"
    "s_cmov_b64 s[40:41], s[8:9]\n"
    "v_mfma_f32_16x16x32_bf16 a[4:7], a[156:159], v[80:83], a[4:7]\n"
    "v_exp_f32 v12, v12\n"
    "v_mfma_f32_16x16x32_bf16 a[4:7], a[152:155], v[76:79], a[4:7]\n"
    "v_exp_f32 v13, v13\n"
    "v_mfma_f32_16x16x32_bf16 a[0:3], a[144:147], v[76:79], a[0:3]\n"
    "v_exp_f32 v14, v14\n"
    "v_mfma_f32_16x16x32_bf16 a[0:3], a[148:151], v[80:83], a[0:3]\n"
    "v_exp_f32 v15, v15\n"
    "v_mfma_f32_16x16x32_bf16 v[152:155], a[140:143], v[80:83], v[152:155]\n"
    "v_exp_f32 v16, v16\n"
    "v_mfma_f32_16x16x32_bf16 v[152:155], a[136:139], v[76:79], v[152:155]\n"
    "v_mfma_f32_16x16x32_bf16 v[168:171], a[136:139], v[84:87], v[168:171]\n"
    "v_exp_f32 v17, v17\n"
    "v_mfma_f32_16x16x32_bf16 v[168:171], a[140:143], v[88:91], v[168:171]\n"
    "v_mfma_f32_16x16x32_bf16 a[16:19], a[148:151], v[88:91], a[16:19]\n"
    "v_mfma_f32_16x16x32_bf16 a[16:19], a[144:147], v[84:87], a[16:19]\n"
    "v_exp_f32 v18, v18\n"
    "v_mfma_f32_16x16x32_bf16 a[20:23], a[152:155], v[84:87], a[20:23]\n"
    "v_mfma_f32_16x16x32_bf16 a[20:23], a[156:159], v[88:91], a[20:23]\n"
    "v_smfmac_f32_16x16x64_bf16 v[116:119], v[104:107], v[84:91], v253\n"
    "s_add_u32 s52, s52, s45\n"
    "v_smfmac_f32_16x16x64_bf16 v[120:123], v[104:107], v[92:99], v253\n"
    "v_mfma_f32_16x16x32_bf16 a[36:39], a[156:159], v[96:99], a[36:39]\n"
    "v_exp_f32 v19, v19\n"
    "v_mfma_f32_16x16x32_bf16 a[36:39], a[152:155], v[92:95], a[36:39]\n"
    "v_exp_f32 v20, v20\n"
    "v_mfma_f32_16x16x32_bf16 a[32:35], a[144:147], v[92:95], a[32:35]\n"
    "v_mfma_f32_16x16x32_bf16 a[32:35], a[148:151], v[96:99], a[32:35]\n"
    "v_exp_f32 v21, v21\n"
    "v_mfma_f32_16x16x32_bf16 v[184:187], a[140:143], v[96:99], v[184:187]\n"
    "v_mfma_f32_16x16x32_bf16 v[184:187], a[136:139], v[92:95], v[184:187]\n"
    "s_waitcnt lgkmcnt(0)\n"
    "v_mfma_f32_16x16x32_bf16 v[196:199], a[112:115], v[68:71], v[196:199]\n"
    "ds_read_b128 a[136:139], v248 offset:9216\n"
    "v_mfma_f32_16x16x32_bf16 v[196:199], a[116:119], v[72:75], v[196:199]\n"
    "ds_read_b128 a[140:143], v248 offset:21504\n"
    "v_mfma_f32_16x16x32_bf16 v[200:203], a[124:127], v[72:75], v[200:203]\n"
    "ds_read_b128 a[144:147], v248 offset:10240\n"
    "v_mfma_f32_16x16x32_bf16 v[200:203], a[120:123], v[68:71], v[200:203]\n"
    "ds_read_b128 a[148:151], v248 offset:22528\n"
    "v_mfma_f32_16x16x32_bf16 a[48:51], a[128:131], v[68:71], a[48:51]\n"
    "buffer_load_dwordx2 v[236:237], v241, s[36:39], s81 offen offset:0 sc1\n"
    "v_mfma_f32_16x16x32_bf16 a[48:51], a[132:135], v[72:75], a[48:51]\n"
    "ds_read_b128 a[152:155], v248 offset:11264\n"
    "v_mfma_f32_16x16x32_bf16 a[64:67], a[132:135], v[80:83], a[64:67]\n"
    "ds_read_b128 a[156:159], v248 offset:23552\n"
    "v_mfma_f32_16x16x32_bf16 a[64:67], a[128:131], v[76:79], a[64:67]\n"
    "v_exp_f32 v22, v22\n"
    "v_mfma_f32_16x16x32_bf16 a[12:15], a[120:123], v[76:79], a[12:15]\n"
    "v_exp_f32 v23, v23\n"
    "v_mfma_f32_16x16x32_bf16 a[12:15], a[124:127], v[80:83], a[12:15]\n"
    "v_exp_f32 v24, v24\n"
    "v_mfma_f32_16x16x32_bf16 a[8:11], a[116:119], v[80:83], a[8:11]\n"
    "v_exp_f32 v25, v25\n"
    "v_mfma_f32_16x16x32_bf16 a[8:11], a[112:115], v[76:79], a[8:11]\n"
    "v_exp_f32 v26, v26\n"
    "v_mfma_f32_16x16x32_bf16 a[24:27], a[112:115], v[84:87], a[24:27]\n"
    "v_mfma_f32_16x16x32_bf16 a[24:27], a[116:119], v[88:91], a[24:27]\n"
    "v_exp_f32 v27, v27\n"
    "v_mfma_f32_16x16x32_bf16 a[28:31], a[124:127], v[88:91], a[28:31]\n"
    "v_mfma_f32_16x16x32_bf16 a[28:31], a[120:123], v[84:87], a[28:31]\n"
    "v_mfma_f32_16x16x32_bf16 a[80:83], a[128:131], v[84:87], a[80:83]\n"
    "v_exp_f32 v28, v28\n"
    "v_mfma_f32_16x16x32_bf16 a[80:83], a[132:135], v[88:91], a[80:83]\n"
    "v_mfma_f32_16x16x32_bf16 a[96:99], a[132:135], v[96:99], a[96:99]\n"
    "v_mfma_f32_16x16x32_bf16 a[96:99], a[128:131], v[92:95], a[96:99]\n"
    "v_exp_f32 v29, v29\n"
    "v_mfma_f32_16x16x32_bf16 a[44:47], a[120:123], v[92:95], a[44:47]\n"
    "v_mfma_f32_16x16x32_bf16 a[44:47], a[124:127], v[96:99], a[44:47]\n"
    "v_exp_f32 v30, v30\n"
    "v_mfma_f32_16x16x32_bf16 a[40:43], a[116:119], v[96:99], a[40:43]\n"
    "v_mfma_f32_16x16x32_bf16 a[40:43], a[112:115], v[92:95], a[40:43]\n"
    "s_waitcnt lgkmcnt(0)\n"
    "v_mfma_f32_16x16x32_bf16 a[52:55], a[136:139], v[68:71], a[52:55]\n"
    "ds_read_b128 a[112:115], v3 offset:12288\n"
    "v_mfma_f32_16x16x32_bf16 a[52:55], a[140:143], v[72:75], a[52:55]\n"
    "ds_read_b128 a[116:119], v3 offset:14336\n"
    "v_mfma_f32_16x16x32_bf16 a[56:59], a[148:151], v[72:75], a[56:59]\n"
    "ds_read_b128 a[120:123], v3 offset:16384\n"
    "v_mfma_f32_16x16x32_bf16 a[56:59], a[144:147], v[68:71], a[56:59]\n"
    "ds_read_b128 a[124:127], v3 offset:18432\n"
    "v_mfma_f32_16x16x32_bf16 a[60:63], a[152:155], v[68:71], a[60:63]\n"
    "ds_read_b128 a[128:131], v3 offset:20480\n"
    "v_mfma_f32_16x16x32_bf16 a[60:63], a[156:159], v[72:75], a[60:63]\n"
    "ds_read_b128 a[132:135], v3 offset:22528\n"
    "v_mfma_f32_16x16x32_bf16 a[76:79], a[156:159], v[80:83], a[76:79]\n"
    "ds_read_b128 v[100:103], v250 offset:896\n"
    "v_mfma_f32_16x16x32_bf16 a[76:79], a[152:155], v[76:79], a[76:79]\n"
    "v_exp_f32 v31, v31\n"
    "v_mfma_f32_16x16x32_bf16 a[72:75], a[144:147], v[76:79], a[72:75]\n"
    "v_exp_f32 v32, v32\n"
    "v_mfma_f32_16x16x32_bf16 a[72:75], a[148:151], v[80:83], a[72:75]\n"
    "v_exp_f32 v33, v33\n"
    "v_mfma_f32_16x16x32_bf16 a[68:71], a[140:143], v[80:83], a[68:71]\n"
    "v_exp_f32 v34, v34\n"
    "v_mfma_f32_16x16x32_bf16 a[68:71], a[136:139], v[76:79], a[68:71]\n"
    "v_exp_f32 v35, v35\n"
    "v_mfma_f32_16x16x32_bf16 a[84:87], a[136:139], v[84:87], a[84:87]\n"
    "v_mfma_f32_16x16x32_bf16 a[84:87], a[140:143], v[88:91], a[84:87]\n"
    "v_mfma_f32_16x16x32_bf16 a[88:91], a[148:151], v[88:91], a[88:91]\n"
    "s_waitcnt vmcnt(8)\n"
    "ds_add_u32 v247, v246\n"
    "v_mfma_f32_16x16x32_bf16 a[88:91], a[144:147], v[84:87], a[88:91]\n"
    "v_mfma_f32_16x16x32_bf16 a[92:95], a[152:155], v[84:87], a[92:95]\n"
    "v_mfma_f32_16x16x32_bf16 a[92:95], a[156:159], v[88:91], a[92:95]\n"
    "v_mfma_f32_16x16x32_bf16 a[108:111], a[156:159], v[96:99], a[108:111]\n"
    "v_mfma_f32_16x16x32_bf16 a[108:111], a[152:155], v[92:95], a[108:111]\n"
    "v_mfma_f32_16x16x32_bf16 a[104:107], a[144:147], v[92:95], a[104:107]\n"
    "s_sub_u32 s74, s68, s65\n"
    "v_mfma_f32_16x16x32_bf16 a[104:107], a[148:151], v[96:99], a[104:107]\n"
    "s_add_u32 s66, s66, 1\n"
    "v_mov_b32 v2, v3\n"
    "v_add_u32_e64 v3, s74, v252\n"
    "v_mfma_f32_16x16x32_bf16 a[100:103], a[140:143], v[96:99], a[100:103]\n"
    "s_waitcnt lgkmcnt(1)\n"
    "s_cmp_eq_u32 s66, .Lend-.Lend2+(29)\n"
    "v_mfma_f32_16x16x32_bf16 a[100:103], a[136:139], v[92:95], a[100:103]\n"
    ".LwVe:\n"
    "s_cbranch_scc1 .LtailP\n"
    ".LNV:\n"
    "v_mfma_f32_16x16x32_bf16 v[36:39], a[112:115], a[192:195], v[100:103]\n"
    "ds_read_b128 a[136:139], v2 offset:13312\n"
    "v_cvt_pk_bf16_f32 v68, v4, v5\n"
    "v_mfma_f32_16x16x32_bf16 v[36:39], a[116:119], a[196:199], v[36:39]\n"
    "ds_read_b128 a[140:143], v2 offset:15360\n"
    "v_mfma_f32_16x16x32_bf16 v[36:39], a[120:123], a[200:203], v[36:39]\n"
    "ds_read_b128 a[144:147], v2 offset:17408\n"
    "v_mfma_f32_16x16x32_bf16 v[36:39], a[124:127], a[204:207], v[36:39]\n"
    "ds_read_b128 a[148:151], v2 offset:19456\n"
    "v_cvt_pk_bf16_f32 v69, v6, v7\n"
    "v_mfma_f32_16x16x32_bf16 v[36:39], a[128:131], a[208:211], v[36:39]\n"
    "ds_read_b128 a[152:155], v2 offset:21504\n"
    "v_mfma_f32_16x16x32_bf16 v[36:39], a[132:135], a[212:215], v[36:39]\n"
    "ds_read_b128 a[156:159], v2 offset:23552\n"
    "v_mfma_f32_16x16x32_bf16 v[40:43], a[132:135], a[236:239], v[100:103]\n"
    "ds_read_b128 v[104:107], v250 offset:912\n"
    "v_mfma_f32_16x16x32_bf16 v[40:43], a[128:131], a[232:235], v[40:43]\n"
    "s_add_u32 s68, s68, 24576\n"
    "v_cvt_pk_bf16_f32 v76, v8, v9\n"
    "v_mfma_f32_16x16x32_bf16 v[40:43], a[124:127], a[228:231], v[40:43]\n"
    "s_cmp_ge_u32 s68, s69\n"
    "s_cselect_b32 s68, s65, s68\n"
    "v_mfma_f32_16x16x32_bf16 v[40:43], a[120:123], a[224:227], v[40:43]\n"
    "v_exp_f32 v36, v36\n"
    "v_mfma_f32_16x16x32_bf16 v[40:43], a[116:119], a[220:223], v[40:43]\n"
    "v_exp_f32 v37, v37\n"
    "v_mfma_f32_16x16x32_bf16 v[40:43], a[112:115], a[216:219], v[40:43]\n"
    "v_cvt_pk_bf16_f32 v77, v10, v11\n"
    "v_cvt_pk_bf16_f32 v72, v36, v37\n"
    "v_mfma_f32_16x16x32_bf16 v[44:47], a[112:115], a[240:243], v[100:103]\n"
    "v_mfma_f32_16x16x32_bf16 v[44:47], a[116:119], a[244:247], v[44:47]\n"
    "v_exp_f32 v38, v38\n"
    "v_mfma_f32_16x16x32_bf16 v[44:47], a[120:123], a[248:251], v[44:47]\n"
    "v_mfma_f32_16x16x32_bf16 v[44:47], a[124:127], a[252:255], v[44:47]\n"
    "v_cvt_pk_bf16_f32 v84, v12, v13\n"
    "v_mfma_f32_16x16x32_bf16 v[44:47], a[128:131], a[160:163], v[44:47]\n"
    "v_exp_f32 v39, v39\n"
    "v_mfma_f32_16x16x32_bf16 v[44:47], a[132:135], a[164:167], v[44:47]\n"
    "v_cvt_pk_bf16_f32 v73, v38, v39\n"
    "v_mfma_f32_16x16x32_bf16 v[48:51], a[132:135], a[188:191], v[100:103]\n"
    "ds_read_b32 v245, v247\n"
    "v_cvt_pk_bf16_f32 v85, v14, v15\n"
    "v_mfma_f32_16x16x32_bf16 v[48:51], a[128:131], a[184:187], v[48:51]\n"
    "s_add_u32 m0, s68, 0\n"
    "v_exp_f32 v40, v40\n"
    "v_mfma_f32_16x16x32_bf16 v[48:51], a[124:127], a[180:183], v[48:51]\n"
    "v_mfma_f32_16x16x32_bf16 v[48:51], a[120:123], a[176:179], v[48:51]\n"
    "v_mfma_f32_16x16x32_bf16 v[48:51], a[116:119], a[172:175], v[48:51]\n"
    "s_waitcnt vmcnt(0)\n"
    "v_cmp_eq_u64 s[54:55], v[236:237], v[238:239]\n"
    "v_cvt_pk_bf16_f32 v92, v16, v17\n"
    "v_mfma_f32_16x16x32_bf16 v[48:51], a[112:115], a[168:171], v[48:51]\n"
    "s_waitcnt lgkmcnt(1)\n"
    "v_exp_f32 v41, v41\n"
    "v_mfma_f32_16x16x32_bf16 v[52:55], a[136:139], a[192:195], v[104:107]\n"
    "s_waitcnt lgkmcnt(0)\n"
    "v_cmp_le_u32 s[56:57], s59, v245\n"
    "v_cvt_pk_bf16_f32 v80, v40, v41\n"
    "v_mfma_f32_16x16x32_bf16 v[52:55], a[140:143], a[196:199], v[52:55]\n"
    "s_waitcnt vmcnt(1)\n"
    "v_exp_f32 v42, v42\n"
    "v_mfma_f32_16x16x32_bf16 v[52:55], a[144:147], a[200:203], v[52:55]\n"
    "s_and_b64 s[54:55], s[54:55], s[56:57]\n"
    "s_cmp_eq_u64 s[54:55], -1\n"
    "v_mfma_f32_16x16x32_bf16 v[52:55], a[148:151], a[204:207], v[52:55]\n"
    "s_cbranch_scc0 .Lpoll4\n"
    ".Lpoll4r:\n"
    "v_mfma_f32_16x16x32_bf16 v[52:55], a[152:155], a[208:211], v[52:55]\n"
    "buffer_load_dwordx4 v1, s[16:19], s67 offen offset:0 sc1 lds\n"
    "v_mfma_f32_16x16x32_bf16 v[52:55], a[156:159], a[212:215], v[52:55]\n"
    "s_add_u32 s76, s52, s60\n"
    "ds_read_b128 a[112:115], v3 offset:0\n"
    "v_cvt_pk_bf16_f32 v93, v18, v19\n"
    "v_mfma_f32_16x16x32_bf16 v[56:59], a[156:159], a[236:239], v[104:107]\n"
    "s_add_u32 s77, s52, s61\n"
    "ds_read_b128 a[116:119], v3 offset:2048\n"
    "v_cvt_pk_bf16_f32 v70, v20, v21\n"
    "v_mfma_f32_16x16x32_bf16 v[56:59], a[152:155], a[232:235], v[56:59]\n"
    "ds_read_b128 a[120:123], v3 offset:4096\n"
    "v_mfma_f32_16x16x32_bf16 v[56:59], a[148:151], a[228:231], v[56:59]\n"
    "buffer_load_dwordx4 v1, s[16:19], s67 offen offset:1024 sc1 lds\n"
    "v_mfma_f32_16x16x32_bf16 v[56:59], a[144:147], a[224:227], v[56:59]\n"
    "ds_read_b128 a[124:127], v3 offset:6144\n"
    "v_cvt_pk_bf16_f32 v71, v22, v23\n"
    "v_mfma_f32_16x16x32_bf16 v[56:59], a[140:143], a[220:223], v[56:59]\n"
    "s_add_u32 s78, s52, s62\n"
    "ds_read_b128 a[128:131], v3 offset:8192\n"
    "v_mfma_f32_16x16x32_bf16 v[56:59], a[136:139], a[216:219], v[56:59]\n"
    "ds_read_b128 a[132:135], v3 offset:10240\n"
    "v_mfma_f32_16x16x32_bf16 v[60:63], a[136:139], a[240:243], v[104:107]\n"
    "buffer_load_dwordx4 v1, s[16:19], s67 offen offset:2048 sc1 lds\n"
    "v_mfma_f32_16x16x32_bf16 v[60:63], a[140:143], a[244:247], v[60:63]\n"
    "ds_read_b128 v[100:103], v250 offset:1536\n"
    "v_cvt_pk_bf16_f32 v78, v24, v25\n"
    "v_mfma_f32_16x16x32_bf16 v[60:63], a[144:147], a[248:251], v[60:63]\n"
    "v_exp_f32 v43, v43\n"
    "v_mfma_f32_16x16x32_bf16 v[60:63], a[148:151], a[252:255], v[60:63]\n"
    "v_cvt_pk_bf16_f32 v81, v42, v43\n"
    "v_cvt_pk_u16_u32 v204, v204, v208\n"
    "v_mfma_f32_16x16x32_bf16 v[60:63], a[152:155], a[160:163], v[60:63]\n"
    "buffer_load_dwordx4 v1, s[16:19], s67 offen offset:3072 sc1 lds\n"
    "v_mfma_f32_16x16x32_bf16 v[60:63], a[156:159], a[164:167], v[60:63]\n"
    "s_add_u32 m0, s68, 4096\n"
    "v_cvt_pk_bf16_f32 v79, v26, v27\n"
    "v_mfma_f32_16x16x32_bf16 v[64:67], a[156:159], a[188:191], v[104:107]\n"
    "s_add_u32 s83, s67, 4096\n"
    "v_exp_f32 v44, v44\n"
    "v_mfma_f32_16x16x32_bf16 v[64:67], a[152:155], a[184:187], v[64:67]\n"
    "v_exp_f32 v45, v45\n"
    "v_mfma_f32_16x16x32_bf16 v[64:67], a[148:151], a[180:183], v[64:67]\n"
    "buffer_load_dwordx4 v1, s[16:19], s83 offen offset:0 sc1 lds\n"
    "v_mfma_f32_16x16x32_bf16 v[64:67], a[144:147], a[176:179], v[64:67]\n"
    "v_cvt_pk_bf16_f32 v86, v28, v29\n"
    "v_cvt_pk_bf16_f32 v88, v44, v45\n"
    "v_mfma_f32_16x16x32_bf16 v[64:67], a[140:143], a[172:175], v[64:67]\n"
    "v_exp_f32 v46, v46\n"
    "v_mfma_f32_16x16x32_bf16 v[64:67], a[136:139], a[168:171], v[64:67]\n"
    "s_waitcnt lgkmcnt(0)\n"
    "v_exp_f32 v47, v47\n"
    "v_mfma_f32_16x16x32_bf16 v[4:7], a[112:115], a[192:195], v[100:103]\n"
    "buffer_load_dwordx4 v1, s[16:19], s83 offen offset:1024 sc1 lds\n"
    "v_mfma_f32_16x16x32_bf16 v[4:7], a[116:119], a[196:199], v[4:7]\n"
    "s_add_u32 m0, s70, 0\n"
    "ds_read_b128 a[136:139], v3 offset:1024\n"
    "v_cvt_pk_bf16_f32 v87, v30, v31\n"
    "v_mfma_f32_16x16x32_bf16 v[4:7], a[120:123], a[200:203], v[4:7]\n"
    "s_add_u32 s83, s67, 24576\n"
    "ds_read_b128 a[140:143], v3 offset:3072\n"
    "v_cvt_pk_bf16_f32 v89, v46, v47\n"
    "v_mfma_f32_16x16x32_bf16 v[4:7], a[124:127], a[204:207], v[4:7]\n"
    "ds_read_b128 a[144:147], v3 offset:5120\n"
    "v_cvt_pk_u16_u32 v205, v205, v209\n"
    "v_mfma_f32_16x16x32_bf16 v[4:7], a[128:131], a[208:211], v[4:7]\n"
    "buffer_load_dwordx4 v1, s[16:19], s83 offen offset:0 sc1 lds\n"
    "v_mfma_f32_16x16x32_bf16 v[4:7], a[132:135], a[212:215], v[4:7]\n"
    "ds_read_b128 a[148:151], v3 offset:7168\n"
    "v_cvt_pk_bf16_f32 v94, v32, v33\n"
    "v_mfma_f32_16x16x32_bf16 v[8:11], a[132:135], a[236:239], v[100:103]\n"
    "ds_read_b128 a[152:155], v3 offset:9216\n"
    "v_cvt_pk_u16_u32 v206, v206, v210\n"
    "v_mfma_f32_16x16x32_bf16 v[8:11], a[128:131], a[232:235], v[8:11]\n"
    "ds_read_b128 a[156:159], v3 offset:11264\n"
    "v_cvt_pk_u16_u32 v207, v207, v211\n"
    "v_mfma_f32_16x16x32_bf16 v[8:11], a[124:127], a[228:231], v[8:11]\n"
    "buffer_load_dwordx4 v1, s[16:19], s83 offen offset:1024 sc1 lds\n"
    "v_mfma_f32_16x16x32_bf16 v[8:11], a[120:123], a[224:227], v[8:11]\n"
    "ds_read_b128 v[104:107], v250 offset:1552\n"
    "v_cvt_pk_bf16_f32 v95, v34, v35\n"
    "v_mfma_f32_16x16x32_bf16 v[8:11], a[116:119], a[220:223], v[8:11]\n"
    "v_exp_f32 v52, v52\n"
    "v_mfma_f32_16x16x32_bf16 v[8:11], a[112:115], a[216:219], v[8:11]\n"
    "v_exp_f32 v53, v53\n"
    "v_mfma_f32_16x16x32_bf16 v[12:15], a[112:115], a[240:243], v[100:103]\n"
    "buffer_load_dwordx4 v1, s[16:19], s83 offen offset:2048 sc1 lds\n"
    "v_mfma_f32_16x16x32_bf16 v[12:15], a[116:119], a[244:247], v[12:15]\n"
    "v_exp_f32 v54, v54\n"
    "v_mfma_f32_16x16x32_bf16 v[12:15], a[120:123], a[248:251], v[12:15]\n"
    "v_exp_f32 v55, v55\n"
    "v_mfma_f32_16x16x32_bf16 v[12:15], a[124:127], a[252:255], v[12:15]\n"
    "v_cvt_pk_bf16_f32 v74, v52, v53\n"
    "v_cvt_pk_bf16_f32 v75, v54, v55\n"
    "v_mfma_f32_16x16x32_bf16 v[12:15], a[128:131], a[160:163], v[12:15]\n"
    "buffer_load_dwordx4 v1, s[16:19], s83 offen offset:3072 sc1 lds\n"
    "v_mfma_f32_16x16x32_bf16 v[12:15], a[132:135], a[164:167], v[12:15]\n"
    "s_add_u32 m0, s70, 4096\n"
    "v_exp_f32 v56, v56\n"
    "v_mfma_f32_16x16x32_bf16 v[16:19], a[132:135], a[188:191], v[100:103]\n"
    "s_add_u32 s83, s67, 28672\n"
    "v_exp_f32 v48, v48\n"
    "v_mfma_f32_16x16x32_bf16 v[16:19], a[128:131], a[184:187], v[16:19]\n"
    "v_exp_f32 v49, v49\n"
    "v_mfma_f32_16x16x32_bf16 v[16:19], a[124:127], a[180:183], v[16:19]\n"
    "buffer_load_dwordx4 v1, s[16:19], s83 offen offset:0 sc1 lds\n"
    "v_mfma_f32_16x16x32_bf16 v[16:19], a[120:123], a[176:179], v[16:19]\n"
    "v_exp_f32 v57, v57\n"
    "v_mfma_f32_16x16x32_bf16 v[16:19], a[116:119], a[172:175], v[16:19]\n"
    "v_cvt_pk_bf16_f32 v82, v56, v57\n"
    "v_cvt_pk_bf16_f32 v96, v48, v49\n"
    "v_mfma_f32_16x16x32_bf16 v[16:19], a[112:115], a[168:171], v[16:19]\n"
    "s_waitcnt lgkmcnt(0)\n"
    "v_exp_f32 v58, v58\n"
    "v_mfma_f32_16x16x32_bf16 v[20:23], a[136:139], a[192:195], v[104:107]\n"
    "buffer_load_dwordx4 v1, s[16:19], s83 offen offset:1024 sc1 lds\n"
    "v_mfma_f32_16x16x32_bf16 v[20:23], a[140:143], a[196:199], v[20:23]\n"
    "s_add_u32 s67, s67, 49152\n"
    "ds_read_b128 a[112:115], v249 offset:0\n"
    "v_or3_b32 v243, v204, v205, v243\n"
    "v_mfma_f32_16x16x32_bf16 v[20:23], a[144:147], a[200:203], v[20:23]\n"
    "s_add_u32 s59, s59, 4\n"
    "ds_read_b128 a[116:119], v249 offset:12288\n"
    "v_or3_b32 v243, v206, v207, v243\n"
    "v_mfma_f32_16x16x32_bf16 v[20:23], a[148:151], a[204:207], v[20:23]\n"
    "ds_read_b128 a[120:123], v249 offset:1024\n"
    "v_cvt_pk_u16_u32 v212, v212, v216\n"
    "v_mfma_f32_16x16x32_bf16 v[20:23], a[152:155], a[208:211], v[20:23]\n"
    "ds_read_b128 a[124:127], v249 offset:13312\n"
    "v_cvt_pk_u16_u32 v213, v213, v217\n"
    "v_mfma_f32_16x16x32_bf16 v[20:23], a[156:159], a[212:215], v[20:23]\n"
    "ds_read_b128 a[128:131], v249 offset:2048\n"
    "v_mfma_f32_16x16x32_bf16 v[24:27], a[156:159], a[236:239], v[104:107]\n"
    "ds_read_b128 a[132:135], v249 offset:14336\n"
    "v_mfma_f32_16x16x32_bf16 v[24:27], a[152:155], a[232:235], v[24:27]\n"
    "v_exp_f32 v59, v59\n"
    "v_mfma_f32_16x16x32_bf16 v[24:27], a[148:151], a[228:231], v[24:27]\n"
    "v_cvt_pk_bf16_f32 v83, v58, v59\n"
    "v_cvt_pk_u16_u32 v214, v214, v218\n"
    "v_mfma_f32_16x16x32_bf16 v[24:27], a[144:147], a[224:227], v[24:27]\n"
    "v_exp_f32 v60, v60\n"
    "v_mfma_f32_16x16x32_bf16 v[24:27], a[140:143], a[220:223], v[24:27]\n"
    "v_exp_f32 v61, v61\n"
    "v_mfma_f32_16x16x32_bf16 v[24:27], a[136:139], a[216:219], v[24:27]\n"
    "v_exp_f32 v62, v62\n"
    "v_mfma_f32_16x16x32_bf16 v[28:31], a[136:139], a[240:243], v[104:107]\n"
    "v_cvt_pk_bf16_f32 v90, v60, v61\n"
    "v_mfma_f32_16x16x32_bf16 v[28:31], a[140:143], a[244:247], v[28:31]\n"
    "v_exp_f32 v63, v63\n"
    "v_mfma_f32_16x16x32_bf16 v[28:31], a[144:147], a[248:251], v[28:31]\n"
    "v_cvt_pk_bf16_f32 v91, v62, v63\n"
    "v_cvt_pk_u16_u32 v215, v215, v219\n"
    "v_mfma_f32_16x16x32_bf16 v[28:31], a[148:151], a[252:255], v[28:31]\n"
    "v_exp_f32 v50, v50\n"
    "v_mfma_f32_16x16x32_bf16 v[28:31], a[152:155], a[160:163], v[28:31]\n"
    "v_exp_f32 v51, v51\n"
    "v_mfma_f32_16x16x32_bf16 v[28:31], a[156:159], a[164:167], v[28:31]\n"
    "v_exp_f32 v64, v64\n"
    "v_mfma_f32_16x16x32_bf16 v[32:35], a[156:159], a[188:191], v[104:107]\n"
    "v_cvt_pk_bf16_f32 v97, v50, v51\n"
    "v_mfma_f32_16x16x32_bf16 v[32:35], a[152:155], a[184:187], v[32:35]\n"
    "v_exp_f32 v65, v65\n"
    "v_mfma_f32_16x16x32_bf16 v[32:35], a[148:151], a[180:183], v[32:35]\n"
    "v_cvt_pk_bf16_f32 v98, v64, v65\n"
    "v_or3_b32 v243, v212, v213, v243\n"
    "v_mfma_f32_16x16x32_bf16 v[32:35], a[144:147], a[176:179], v[32:35]\n"
    "v_mfma_f32_16x16x32_bf16 v[32:35], a[140:143], a[172:175], v[32:35]\n"
    "v_exp_f32 v66, v66\n"
    "v_mfma_f32_16x16x32_bf16 v[32:35], a[136:139], a[168:171], v[32:35]\n"
    "s_waitcnt lgkmcnt(0)\n"
    "v_mfma_f32_16x16x32_bf16 v[124:127], a[112:115], v[68:71], v[124:127]\n"
    "ds_read_b128 a[136:139], v249 offset:3072\n"
    "v_or3_b32 v243, v214, v215, v243\n"
    "v_mfma_f32_16x16x32_bf16 v[124:127], a[116:119], v[72:75], v[124:127]\n"
    "ds_read_b128 a[140:143], v249 offset:15360\n"
    "v_mfma_f32_16x16x32_bf16 v[128:131], a[124:127], v[72:75], v[128:131]\n"
    "ds_read_b128 a[144:147], v249 offset:4096\n"
    "v_mfma_f32_16x16x32_bf16 v[128:131], a[120:123], v[68:71], v[128:131]\n"
    "ds_read_b128 a[148:151], v249 offset:16384\n"
    "v_mfma_f32_16x16x32_bf16 v[132:135], a[128:131], v[68:71], v[132:135]\n"
    "ds_read_b128 a[152:155], v249 offset:5120\n"
    "v_mfma_f32_16x16x32_bf16 v[132:135], a[132:135], v[72:75], v[132:135]\n"
    "ds_read_b128 a[156:159], v249 offset:17408\n"
    "v_cvt_pk_u16_u32 v220, v220, v224\n"
    "v_mfma_f32_16x16x32_bf16 v[148:151], a[132:135], v[80:83], v[148:151]\n"
    "ds_read_b128 v[104:107], v251 offset:768\n"
    "v_mfma_f32_16x16x32_bf16 v[148:151], a[128:131], v[76:79], v[148:151]\n"
    "v_exp_f32 v67, v67\n"
    "v_mfma_f32_16x16x32_bf16 v[144:147], a[120:123], v[76:79], v[144:147]\n"
    "v_cvt_pk_bf16_f32 v99, v66, v67\n"
    "v_mfma_f32_16x16x32_bf16 v[144:147], a[124:127], v[80:83], v[144:147]\n"
    "v_exp_f32 v4, v4\n"
    "v_mfma_f32_16x16x32_bf16 v[140:143], a[116:119], v[80:83], v[140:143]\n"
    "v_exp_f32 v5, v5\n"
    "v_mfma_f32_16x16x32_bf16 v[140:143], a[112:115], v[76:79], v[140:143]\n"
    "v_exp_f32 v6, v6\n"
    "v_mfma_f32_16x16x32_bf16 v[156:159], a[112:115], v[84:87], v[156:159]\n"
    "v_exp_f32 v7, v7\n"
    "v_mfma_f32_16x16x32_bf16 v[156:159], a[116:119], v[88:91], v[156:159]\n"
    "v_cvt_pk_u16_u32 v221, v221, v225\n"
    "v_mfma_f32_16x16x32_bf16 v[160:163], a[124:127], v[88:91], v[160:163]\n"
    "v_exp_f32 v8, v8\n"
    "v_mfma_f32_16x16x32_bf16 v[160:163], a[120:123], v[84:87], v[160:163]\n"
    "v_cvt_pk_u16_u32 v222, v222, v226\n"
    "v_mfma_f32_16x16x32_bf16 v[164:167], a[128:131], v[84:87], v[164:167]\n"
    "v_mfma_f32_16x16x32_bf16 v[164:167], a[132:135], v[88:91], v[164:167]\n"
    "v_exp_f32 v9, v9\n"
    "v_mfma_f32_16x16x32_bf16 v[180:183], a[132:135], v[96:99], v[180:183]\n"
    "v_mfma_f32_16x16x32_bf16 v[180:183], a[128:131], v[92:95], v[180:183]\n"
    "v_mfma_f32_16x16x32_bf16 v[176:179], a[120:123], v[92:95], v[176:179]\n"
    "v_exp_f32 v10, v10\n"
    "v_mfma_f32_16x16x32_bf16 v[176:179], a[124:127], v[96:99], v[176:179]\n"
    "v_cvt_pk_u16_u32 v223, v223, v227\n"
    "v_mfma_f32_16x16x32_bf16 v[172:175], a[116:119], v[96:99], v[172:175]\n"
    "v_exp_f32 v11, v11\n"
    "v_mfma_f32_16x16x32_bf16 v[172:175], a[112:115], v[92:95], v[172:175]\n"
    "s_waitcnt lgkmcnt(0)\n"
    "v_mfma_f32_16x16x32_bf16 v[136:139], a[136:139], v[68:71], v[136:139]\n"
    "ds_read_b128 a[112:115], v249 offset:6144\n"
    "v_mfma_f32_16x16x32_bf16 v[136:139], a[140:143], v[72:75], v[136:139]\n"
    "ds_read_b128 a[116:119], v249 offset:18432\n"
    "v_mfma_f32_16x16x32_bf16 v[188:191], a[148:151], v[72:75], v[188:191]\n"
    "ds_read_b128 a[120:123], v249 offset:7168\n"
    "v_or3_b32 v243, v220, v221, v243\n"
    "v_mfma_f32_16x16x32_bf16 v[188:191], a[144:147], v[68:71], v[188:191]\n"
    "ds_read_b128 a[124:127], v249 offset:19456\n"
    "v_mfma_f32_16x16x32_bf16 v[192:195], a[152:155], v[68:71], v[192:195]\n"
    "ds_read_b128 a[128:131], v249 offset:8192\n"
    "v_mfma_f32_16x16x32_bf16 v[192:195], a[156:159], v[72:75], v[192:195]\n"
    "ds_read_b128 a[132:135], v249 offset:20480\n"
    "v_smfmac_f32_16x16x64_bf16 v[108:111], v[104:107], v[68:75], v253\n"
    "s_add_u32 s81, s81, 128\n"
    "s_cmp_gt_u32 s81, 4096\n"
    "s_cselect_b32 s81, 0, s81\n"
    "v_smfmac_f32_16x16x64_bf16 v[112:115], v[104:107], v[76:83], v253\n"
    "v_mfma_f32_16x16x32_bf16 a[4:7], a[156:159], v[80:83], a[4:7]\n"
    "v_exp_f32 v12, v12\n"
    "v_mfma_f32_16x16x32_bf16 a[4:7], a[152:155], v[76:79], a[4:7]\n"
    "v_exp_f32 v13, v13\n"
    "v_mfma_f32_16x16x32_bf16 a[0:3], a[144:147], v[76:79], a[0:3]\n"
    "v_exp_f32 v14, v14\n"
    "v_mfma_f32_16x16x32_bf16 a[0:3], a[148:151], v[80:83], a[0:3]\n"
    "v_exp_f32 v15, v15\n"
    "v_mfma_f32_16x16x32_bf16 v[152:155], a[140:143], v[80:83], v[152:155]\n"
    "v_or3_b32 v243, v222, v223, v243\n"
    "v_and_b32 v208, 0xff00ff00, v243\n"
    "v_mfma_f32_16x16x32_bf16 v[152:155], a[136:139], v[76:79], v[152:155]\n"
    "v_exp_f32 v16, v16\n"
    "v_mfma_f32_16x16x32_bf16 v[168:171], a[136:139], v[84:87], v[168:171]\n"
    "v_exp_f32 v17, v17\n"
    "v_mfma_f32_16x16x32_bf16 v[168:171], a[140:143], v[88:91], v[168:171]\n"
    "v_mfma_f32_16x16x32_bf16 a[16:19], a[148:151], v[88:91], a[16:19]\n"
    "v_mfma_f32_16x16x32_bf16 a[16:19], a[144:147], v[84:87], a[16:19]\n"
    "v_exp_f32 v18, v18\n"
    "v_mfma_f32_16x16x32_bf16 a[20:23], a[152:155], v[84:87], a[20:23]\n"
    "v_cmp_ne_u32 vcc, 0, v208\n"
    "v_mfma_f32_16x16x32_bf16 a[20:23], a[156:159], v[88:91], a[20:23]\n"
    "v_smfmac_f32_16x16x64_bf16 v[116:119], v[104:107], v[84:91], v253\n"
    "v_smfmac_f32_16x16x64_bf16 v[120:123], v[104:107], v[92:99], v253\n"
    "v_mfma_f32_16x16x32_bf16 a[36:39], a[156:159], v[96:99], a[36:39]\n"
    "v_exp_f32 v19, v19\n"
    "v_mfma_f32_16x16x32_bf16 a[36:39], a[152:155], v[92:95], a[36:39]\n"
    "v_exp_f32 v20, v20\n"
    "v_mfma_f32_16x16x32_bf16 a[32:35], a[144:147], v[92:95], a[32:35]\n"
    "v_cndmask_b32 v208, 2.0, v241, vcc\n"
    "v_mfma_f32_16x16x32_bf16 a[32:35], a[148:151], v[96:99], a[32:35]\n"
    "v_exp_f32 v21, v21\n"
    "v_mfma_f32_16x16x32_bf16 v[184:187], a[140:143], v[96:99], v[184:187]\n"
    "v_mfma_f32_16x16x32_bf16 v[184:187], a[136:139], v[92:95], v[184:187]\n"
    "s_waitcnt lgkmcnt(0)\n"
    "v_mfma_f32_16x16x32_bf16 v[196:199], a[112:115], v[68:71], v[196:199]\n"
    "ds_read_b128 a[136:139], v249 offset:9216\n"
    "v_mfma_f32_16x16x32_bf16 v[196:199], a[116:119], v[72:75], v[196:199]\n"
    "ds_read_b128 a[140:143], v249 offset:21504\n"
    "v_mfma_f32_16x16x32_bf16 v[200:203], a[124:127], v[72:75], v[200:203]\n"
    "ds_read_b128 a[144:147], v249 offset:10240\n"
    "v_mfma_f32_16x16x32_bf16 v[200:203], a[120:123], v[68:71], v[200:203]\n"
    "ds_read_b128 a[148:151], v249 offset:22528\n"
    "v_mfma_f32_16x16x32_bf16 a[48:51], a[128:131], v[68:71], a[48:51]\n"
    "buffer_load_dwordx2 v[236:237], v241, s[36:39], s81 offen offset:0 sc1\n"
    "v_mfma_f32_16x16x32_bf16 a[48:51], a[132:135], v[72:75], a[48:51]\n"
    "ds_read_b128 a[152:155], v249 offset:11264\n"
    "v_mfma_f32_16x16x32_bf16 a[64:67], a[132:135], v[80:83], a[64:67]\n"
    "ds_read_b128 a[156:159], v249 offset:23552\n"
    "v_mfma_f32_16x16x32_bf16 a[64:67], a[128:131], v[76:79], a[64:67]\n"
    "buffer_store_dword v204, v244, s[20:23], s76 offen offset:0 sc1\n"
    "v_mfma_f32_16x16x32_bf16 a[12:15], a[120:123], v[76:79], a[12:15]\n"
    "v_exp_f32 v22, v22\n"
    "v_mfma_f32_16x16x32_bf16 a[12:15], a[124:127], v[80:83], a[12:15]\n"
    "buffer_store_dword v205, v244, s[20:23], s76 offen offset:1024 sc1\n"
    "v_mfma_f32_16x16x32_bf16 a[8:11], a[116:119], v[80:83], a[8:11]\n"
    "v_exp_f32 v23, v23\n"
    "v_mfma_f32_16x16x32_bf16 a[8:11], a[112:115], v[76:79], a[8:11]\n"
    "buffer_store_dword v206, v244, s[20:23], s76 offen offset:2048 sc1\n"
    "v_mfma_f32_16x16x32_bf16 a[24:27], a[112:115], v[84:87], a[24:27]\n"
    "v_exp_f32 v24, v24\n"
    "v_mfma_f32_16x16x32_bf16 a[24:27], a[116:119], v[88:91], a[24:27]\n"
    "buffer_store_dword v207, v244, s[20:23], s76 offen offset:3072 sc1\n"
    "v_mfma_f32_16x16x32_bf16 a[28:31], a[124:127], v[88:91], a[28:31]\n"
    "v_exp_f32 v25, v25\n"
    "v_mfma_f32_16x16x32_bf16 a[28:31], a[120:123], v[84:87], a[28:31]\n"
    "buffer_store_dword v212, v244, s[20:23], s77 offen offset:0 sc1\n"
    "v_mfma_f32_16x16x32_bf16 a[80:83], a[128:131], v[84:87], a[80:83]\n"
    "v_exp_f32 v26, v26\n"
    "v_mfma_f32_16x16x32_bf16 a[80:83], a[132:135], v[88:91], a[80:83]\n"
    "buffer_store_dword v213, v244, s[20:23], s77 offen offset:1024 sc1\n"
    "v_mfma_f32_16x16x32_bf16 a[96:99], a[132:135], v[96:99], a[96:99]\n"
    "v_exp_f32 v27, v27\n"
    "v_mfma_f32_16x16x32_bf16 a[96:99], a[128:131], v[92:95], a[96:99]\n"
    "buffer_store_dword v214, v244, s[20:23], s77 offen offset:2048 sc1\n"
    "v_mfma_f32_16x16x32_bf16 a[44:47], a[120:123], v[92:95], a[44:47]\n"
    "v_exp_f32 v28, v28\n"
    "v_mfma_f32_16x16x32_bf16 a[44:47], a[124:127], v[96:99], a[44:47]\n"
    "buffer_store_dword v215, v244, s[20:23], s77 offen offset:3072 sc1\n"
    "v_mfma_f32_16x16x32_bf16 a[40:43], a[116:119], v[96:99], a[40:43]\n"
    "v_exp_f32 v29, v29\n"
    "v_mfma_f32_16x16x32_bf16 a[40:43], a[112:115], v[92:95], a[40:43]\n"
    "s_waitcnt lgkmcnt(0)\n"
    "v_exp_f32 v30, v30\n"
    "v_mfma_f32_16x16x32_bf16 a[52:55], a[136:139], v[68:71], a[52:55]\n"
    "ds_read_b128 a[112:115], v3 offset:12288\n"
    "v_mfma_f32_16x16x32_bf16 a[52:55], a[140:143], v[72:75], a[52:55]\n"
    "ds_read_b128 a[116:119], v3 offset:14336\n"
    "v_mfma_f32_16x16x32_bf16 a[56:59], a[148:151], v[72:75], a[56:59]\n"
    "ds_read_b128 a[120:123], v3 offset:16384\n"
    "v_mfma_f32_16x16x32_bf16 a[56:59], a[144:147], v[68:71], a[56:59]\n"
    "ds_read_b128 a[124:127], v3 offset:18432\n"
    "v_mfma_f32_16x16x32_bf16 a[60:63], a[152:155], v[68:71], a[60:63]\n"
    "ds_read_b128 a[128:131], v3 offset:20480\n"
    "v_mfma_f32_16x16x32_bf16 a[60:63], a[156:159], v[72:75], a[60:63]\n"
    "ds_read_b128 a[132:135], v3 offset:22528\n"
    "v_mfma_f32_16x16x32_bf16 a[76:79], a[156:159], v[80:83], a[76:79]\n"
    "ds_read_b128 v[100:103], v250 offset:1664\n"
    "v_mfma_f32_16x16x32_bf16 a[76:79], a[152:155], v[76:79], a[76:79]\n"
    "buffer_store_dword v220, v244, s[20:23], s78 offen offset:0 sc1\n"
    "v_mfma_f32_16x16x32_bf16 a[72:75], a[144:147], v[76:79], a[72:75]\n"
    "v_exp_f32 v31, v31\n"
    "v_mfma_f32_16x16x32_bf16 a[72:75], a[148:151], v[80:83], a[72:75]\n"
    "buffer_store_dword v221, v244, s[20:23], s78 offen offset:1024 sc1\n"
    "v_mfma_f32_16x16x32_bf16 a[68:71], a[140:143], v[80:83], a[68:71]\n"
    "v_exp_f32 v32, v32\n"
    "v_mfma_f32_16x16x32_bf16 a[68:71], a[136:139], v[76:79], a[68:71]\n"
    "buffer_store_dword v222, v244, s[20:23], s78 offen offset:2048 sc1\n"
    "v_mfma_f32_16x16x32_bf16 a[84:87], a[136:139], v[84:87], a[84:87]\n"
    "v_exp_f32 v33, v33\n"
    "v_mfma_f32_16x16x32_bf16 a[84:87], a[140:143], v[88:91], a[84:87]\n"
    "buffer_store_dword v223, v244, s[20:23], s78 offen offset:3072 sc1\n"
    "v_mfma_f32_16x16x32_bf16 a[88:91], a[148:151], v[88:91], a[88:91]\n"
    "s_waitcnt vmcnt(13)\n"
    "ds_add_u32 v247, v246\n"
    "v_mfma_f32_16x16x32_bf16 a[88:91], a[144:147], v[84:87], a[88:91]\n"
    "buffer_store_dwordx2 v[238:239], v208, s[36:39], 0 offen offset:0 sc1\n"
    "v_mfma_f32_16x16x32_bf16 a[92:95], a[152:155], v[84:87], a[92:95]\n"
    "v_exp_f32 v34, v34\n"
    "v_mfma_f32_16x16x32_bf16 a[92:95], a[156:159], v[88:91], a[92:95]\n"
    "v_exp_f32 v35, v35\n"
    "v_mfma_f32_16x16x32_bf16 a[108:111], a[156:159], v[96:99], a[108:111]\n"
    "v_mfma_f32_16x16x32_bf16 a[108:111], a[152:155], v[92:95], a[108:111]\n"
    "v_mfma_f32_16x16x32_bf16 a[104:107], a[144:147], v[92:95], a[104:107]\n"
    "s_sub_u32 s74, s68, s65\n"
    "v_add_u32 v250, 1536, v250\n"
    "v_add_u32 v251, 1536, v251\n"
    "v_mfma_f32_16x16x32_bf16 a[104:107], a[148:151], v[96:99], a[104:107]\n"
    "s_add_u32 s66, s66, 1\n"
    "v_mov_b32 v2, v3\n"
    "v_add_u32 v3, s74, v252\n"
    "v_mfma_f32_16x16x32_bf16 a[100:103], a[140:143], v[96:99], a[100:103]\n"
    "s_waitcnt lgkmcnt(1)\n"
    "s_cmp_lt_u32 s66, 29\n"
    "v_mfma_f32_16x16x32_bf16 a[100:103], a[136:139], v[92:95], a[100:103]\n"
    "s_cbranch_scc1 .LPV\n"
    "s_branch .Ltail\n"
    "s_nop 0\n"
    "s_nop 0\n"
    "s_nop 0\n"
    "s_nop 0\n"
    "s_nop 0\n"
    "s_nop 0\n"
    ".LtailP:\n"
    "s_cbranch_execz .Lw2\n"
    "v_swap_b32 v248, v249\n"
    "s_mov_b32 s64, s70\n"
    "v_add_u32 v250, 768, v250\n"
    "v_add_u32 v251, 768, v251\n"
    ".Ltail:\n"
    "v_mfma_f32_16x16x32_bf16 v[36:39], a[112:115], a[192:195], v[100:103]\n"
    "v_cvt_pk_bf16_f32 v93, v18, v19\n"
    "v_cvt_pk_bf16_f32 v84, v12, v13\n"
    "v_mfma_f32_16x16x32_bf16 v[36:39], a[116:119], a[196:199], v[36:39]\n"
    "ds_read_b128 a[136:139], v2 offset:13312\n"
    "ds_read_b128 a[140:143], v2 offset:15360\n"
    "v_mfma_f32_16x16x32_bf16 v[36:39], a[120:123], a[200:203], v[36:39]\n"
    "s_mul_i32 s75, s84, 49152\n"
    "v_mfma_f32_16x16x32_bf16 v[36:39], a[124:127], a[204:207], v[36:39]\n"
    "v_cvt_pk_bf16_f32 v77, v10, v11\n"
    "v_subrev_u32 v249, 24576, v249\n"
    "v_mfma_f32_16x16x32_bf16 v[36:39], a[128:131], a[208:211], v[36:39]\n"
    "ds_read_b128 a[152:155], v2 offset:21504\n"
    "v_mfma_f32_16x16x32_bf16 v[40:43], a[132:135], a[236:239], v[100:103]\n"
    "v_cvt_pk_bf16_f32 v69, v6, v7\n"
    "v_lshlrev_b32 v240, 16, v1\n"
    "v_mfma_f32_16x16x32_bf16 v[36:39], a[132:135], a[212:215], v[36:39]\n"
    "ds_read_b128 a[156:159], v2 offset:23552\n"
    "ds_read_b128 v[104:107], v250 offset:144\n"
    "v_mfma_f32_16x16x32_bf16 v[48:51], a[132:135], a[188:191], v[100:103]\n"
    "s_add_u32 s76, s75, 196608\n"
    "ds_read_b128 a[144:147], v2 offset:17408\n"
    "v_and_b32 v3, 0xf0, v1\n"
    "v_mfma_f32_16x16x32_bf16 v[48:51], a[128:131], a[184:187], v[48:51]\n"
    "ds_read_b128 a[148:151], v2 offset:19456\n"
    "s_add_u32 s77, s76, 196608\n"
    "v_mfma_f32_16x16x32_bf16 v[40:43], a[128:131], a[232:235], v[40:43]\n"
    "v_exp_f32 v37, v37\n"
    "v_mfma_f32_16x16x32_bf16 v[48:51], a[124:127], a[180:183], v[48:51]\n"
    "v_exp_f32 v38, v38\n"
    "v_mfma_f32_16x16x32_bf16 v[48:51], a[120:123], a[176:179], v[48:51]\n"
    "s_add_u32 s78, s77, 196608\n"
    "v_cvt_pk_bf16_f32 v76, v8, v9\n"
    "v_cvt_pk_bf16_f32 v68, v4, v5\n"
    "v_mfma_f32_16x16x32_bf16 v[40:43], a[124:127], a[228:231], v[40:43]\n"
    "v_cvt_pk_bf16_f32 v85, v14, v15\n"
    "v_cvt_pk_bf16_f32 v71, v22, v23\n"
    "v_mfma_f32_16x16x32_bf16 v[48:51], a[116:119], a[172:175], v[48:51]\n"
    "v_and_b32 v2, 0x70, v1\n"
    "v_cvt_pk_bf16_f32 v94, v32, v33\n"
    "v_mfma_f32_16x16x32_bf16 v[44:47], a[112:115], a[240:243], v[100:103]\n"
    "v_cvt_pk_bf16_f32 v95, v34, v35\n"
    "v_cvt_pk_bf16_f32 v86, v28, v29\n"
    "v_mfma_f32_16x16x32_bf16 v[48:51], a[112:115], a[168:171], v[48:51]\n"
    "v_exp_f32 v39, v39\n"
    "v_mfma_f32_16x16x32_bf16 v[44:47], a[116:119], a[244:247], v[44:47]\n"
    "s_waitcnt lgkmcnt(2)\n"
    "v_cvt_pk_bf16_f32 v70, v20, v21\n"
    "v_cvt_pk_bf16_f32 v92, v16, v17\n"
    "v_mfma_f32_16x16x32_bf16 v[60:63], a[136:139], a[240:243], v[104:107]\n"
    "v_exp_f32 v36, v36\n"
    "v_mfma_f32_16x16x32_bf16 v[56:59], a[156:159], a[236:239], v[104:107]\n"
    "s_add_u32 s72, s65, 122880\n"
    "ds_read_b32 v245, v247\n"
    "v_mfma_f32_16x16x32_bf16 v[40:43], a[120:123], a[224:227], v[40:43]\n"
    "v_exp_f32 v51, v51\n"
    "v_mfma_f32_16x16x32_bf16 v[60:63], a[140:143], a[244:247], v[60:63]\n"
    "v_exp_f32 v48, v48\n"
    "v_mfma_f32_16x16x32_bf16 v[44:47], a[120:123], a[248:251], v[44:47]\n"
    "v_exp_f32 v50, v50\n"
    "v_mfma_f32_16x16x32_bf16 v[40:43], a[116:119], a[220:223], v[40:43]\n"
    "s_waitcnt vmcnt(0)\n"
    "v_exp_f32 v49, v49\n"
    "v_mfma_f32_16x16x32_bf16 v[44:47], a[124:127], a[252:255], v[44:47]\n"
    "ds_read_b128 v[232:235], v251 offset:0\n"
    "ds_read_b128 v[100:103], v250 offset:768\n"
    "v_mfma_f32_16x16x32_bf16 v[52:55], a[136:139], a[192:195], v[104:107]\n"
    "v_cvt_pk_bf16_f32 v79, v26, v27\n"
    "v_lshrrev_b32 v224, 4, v1\n"
    "v_mfma_f32_16x16x32_bf16 v[64:67], a[156:159], a[188:191], v[104:107]\n"
    "v_lshl_add_u32 v3, v3, 3, s72\n"
    "v_cmp_eq_u64 s[54:55], v[236:237], v[238:239]\n"
    "v_mfma_f32_16x16x32_bf16 v[52:55], a[140:143], a[196:199], v[52:55]\n"
    "s_waitcnt lgkmcnt(2)\n"
    "v_cvt_pk_bf16_f32 v78, v24, v25\n"
    "v_cmp_le_u32 s[56:57], s59, v245\n"
    "v_mfma_f32_16x16x32_bf16 v[40:43], a[112:115], a[216:219], v[40:43]\n"
    "s_and_b64 s[54:55], s[54:55], s[56:57]\n"
    "v_and_b32 v224, 48, v224\n"
    "s_cmp_eq_u64 s[54:55], -1\n"
    "v_mfma_f32_16x16x32_bf16 v[60:63], a[144:147], a[248:251], v[60:63]\n"
    "s_cbranch_scc0 .Lpt0\n"
    ".Lpt0r:\n"
    "v_mfma_f32_16x16x32_bf16 v[44:47], a[128:131], a[160:163], v[44:47]\n"
    "s_add_u32 s81, s81, 128\n"
    "ds_read_b128 a[116:119], v252 offset:2048\n"
    "s_add_u32 m0, s65, 24576\n"
    "v_mfma_f32_16x16x32_bf16 v[52:55], a[144:147], a[200:203], v[52:55]\n"
    "s_add_u32 s83, s67, .Lend-.Lend2+(0)\n"
    "buffer_load_dwordx4 v1, s[16:19], s83 offen offset:0 sc1 lds\n"
    "v_mfma_f32_16x16x32_bf16 v[60:63], a[148:151], a[252:255], v[60:63]\n"
    "ds_read_b128 a[120:123], v252 offset:4096\n"
    "ds_read_b128 a[128:131], v252 offset:8192\n"
    "v_mfma_f32_16x16x32_bf16 v[56:59], a[152:155], a[232:235], v[56:59]\n"
    "buffer_load_dwordx4 v1, s[16:19], s83 offen offset:1024 sc1 lds\n"
    "v_mfma_f32_16x16x32_bf16 v[60:63], a[152:155], a[160:163], v[60:63]\n"
    "ds_read_b128 v[104:107], v250 offset:784\n"
    "ds_read_b128 a[112:115], v252 offset:0\n"
    "v_mfma_f32_16x16x32_bf16 v[44:47], a[132:135], a[164:167], v[44:47]\n"
    "buffer_load_dwordx4 v1, s[16:19], s83 offen offset:2048 sc1 lds\n"
    "v_mfma_f32_16x16x32_bf16 v[52:55], a[148:151], a[204:207], v[52:55]\n"
    "ds_read_b128 a[132:135], v252 offset:10240\n"
    "v_mfma_f32_16x16x32_bf16 v[52:55], a[152:155], a[208:211], v[52:55]\n"
    "buffer_load_dwordx4 v1, s[16:19], s83 offen offset:3072 sc1 lds\n"
    "v_mfma_f32_16x16x32_bf16 v[60:63], a[156:159], a[164:167], v[60:63]\n"
    "s_add_u32 m0, s65, 28672\n"
    "v_exp_f32_e64 v40, v40\n"
    "v_mfma_f32_16x16x32_bf16 v[56:59], a[148:151], a[228:231], v[56:59]\n"
    "s_add_u32 s83, s67, 4096\n"
    "buffer_load_dwordx4 v1, s[16:19], s83 offen offset:0 sc1 lds\n"
    "v_mfma_f32_16x16x32_bf16 v[56:59], a[144:147], a[224:227], v[56:59]\n"
    "ds_read_b128 a[124:127], v252 offset:6144\n"
    "v_cvt_pk_bf16_f32 v72, v36, v37\n"
    "v_mfma_f32_16x16x32_bf16 v[56:59], a[140:143], a[220:223], v[56:59]\n"
    "buffer_load_dwordx4 v1, s[16:19], s83 offen offset:1024 sc1 lds\n"
    "v_mfma_f32_16x16x32_bf16 v[52:55], a[156:159], a[212:215], v[52:55]\n"
    "v_exp_f32 v45, v45\n"
    "v_mfma_f32_16x16x32_bf16 v[56:59], a[136:139], a[216:219], v[56:59]\n"
    "v_exp_f32 v62, v62\n"
    "v_mfma_f32_16x16x32_bf16 v[64:67], a[152:155], a[184:187], v[64:67]\n"
    "s_waitcnt lgkmcnt(2)\n"
    "v_xor_b32 v224, v224, v2\n"
    "v_cvt_pk_bf16_f32 v87, v30, v31\n"
    "v_mfma_f32_16x16x32_bf16 v[12:15], a[112:115], a[240:243], v[100:103]\n"
    "s_waitcnt lgkmcnt(1)\n"
    "ds_read_b128 a[156:159], v252 offset:11264\n"
    "ds_read_b128 a[152:155], v252 offset:9216\n"
    "v_mfma_f32_16x16x32_bf16 v[16:19], a[132:135], a[188:191], v[100:103]\n"
    ".Lwp0:\n"
    "s_cbranch_execz .Lw3\n"
    "s_add_u32 m0, s65, 73728\n"
    "v_exp_f32 v56, v56\n"
    "v_mfma_f32_16x16x32_bf16 v[64:67], a[148:151], a[180:183], v[64:67]\n"
    "v_exp_f32 v58, v58\n"
    "v_mfma_f32_16x16x32_bf16 v[4:7], a[112:115], a[192:195], v[100:103]\n"
    "s_add_u32 s83, s67, 24576\n"
    "v_exp_f32 v61, v61\n"
    "v_mfma_f32_16x16x32_bf16 v[16:19], a[128:131], a[184:187], v[16:19]\n"
    "v_exp_f32 v53, v53\n"
    "v_mfma_f32_16x16x32_bf16 v[64:67], a[144:147], a[176:179], v[64:67]\n"
    "v_exp_f32 v54, v54\n"
    "v_mfma_f32_16x16x32_bf16 v[64:67], a[140:143], a[172:175], v[64:67]\n"
    "v_exp_f32 v52, v52\n"
    "v_mfma_f32_16x16x32_bf16 v[8:11], a[132:135], a[236:239], v[100:103]\n"
    "s_waitcnt lgkmcnt(2)\n"
    "v_exp_f32 v63, v63\n"
    "v_mfma_f32_16x16x32_bf16 v[16:19], a[124:127], a[180:183], v[16:19]\n"
    "v_exp_f32 v41, v41\n"
    "v_mfma_f32_16x16x32_bf16 v[64:67], a[136:139], a[168:171], v[64:67]\n"
    "v_exp_f32 v43, v43\n"
    "v_mfma_f32_16x16x32_bf16 v[8:11], a[128:131], a[232:235], v[8:11]\n"
    "ds_read_b128 a[144:147], v252 offset:5120\n"
    "ds_read_b128 a[148:151], v252 offset:7168\n"
    "v_mfma_f32_16x16x32_bf16 v[12:15], a[116:119], a[244:247], v[12:15]\n"
    "v_exp_f32 v44, v44\n"
    "v_mfma_f32_16x16x32_bf16 v[4:7], a[116:119], a[196:199], v[4:7]\n"
    "v_exp_f32 v55, v55\n"
    "v_mfma_f32_16x16x32_bf16 v[16:19], a[120:123], a[176:179], v[16:19]\n"
    "ds_read_b128 a[136:139], v252 offset:1024\n"
    "v_cvt_pk_bf16_f32 v80, v40, v41\n"
    "v_mfma_f32_16x16x32_bf16 v[12:15], a[120:123], a[248:251], v[12:15]\n"
    "buffer_load_dwordx4 v1, s[16:19], s83 offen offset:0 sc1 lds\n"
    "v_mfma_f32_16x16x32_bf16 v[4:7], a[120:123], a[200:203], v[4:7]\n"
    "v_exp_f32 v42, v42\n"
    "v_mfma_f32_16x16x32_bf16 v[16:19], a[116:119], a[172:175], v[16:19]\n"
    "v_exp_f32 v66, v66\n"
    "v_mfma_f32_16x16x32_bf16 v[4:7], a[124:127], a[204:207], v[4:7]\n"
    "v_exp_f32 v59, v59\n"
    "v_mfma_f32_16x16x32_bf16 v[4:7], a[128:131], a[208:211], v[4:7]\n"
    "buffer_load_dwordx4 v1, s[16:19], s83 offen offset:1024 sc1 lds\n"
    "v_mfma_f32_16x16x32_bf16 v[4:7], a[132:135], a[212:215], v[4:7]\n"
    "v_exp_f32 v57, v57\n"
    "v_mfma_f32_16x16x32_bf16 v[12:15], a[124:127], a[252:255], v[12:15]\n"
    "ds_read_b128 v[100:103], v250 offset:896\n"
    "ds_read_b128 a[140:143], v252 offset:3072\n"
    "v_mfma_f32_16x16x32_bf16 v[12:15], a[128:131], a[160:163], v[12:15]\n"
    "v_exp_f32 v65, v65\n"
    "v_mfma_f32_16x16x32_bf16 v[8:11], a[124:127], a[228:231], v[8:11]\n"
    "v_exp_f32 v47, v47\n"
    "v_mfma_f32_16x16x32_bf16 v[12:15], a[132:135], a[164:167], v[12:15]\n"
    "s_waitcnt lgkmcnt(6)\n"
    "buffer_load_dwordx4 v1, s[16:19], s83 offen offset:2048 sc1 lds\n"
    "v_mfma_f32_16x16x32_bf16 v[32:35], a[156:159], a[188:191], v[104:107]\n"
    "v_exp_f32 v60, v60\n"
    "v_mfma_f32_16x16x32_bf16 v[24:27], a[156:159], a[236:239], v[104:107]\n"
    "s_waitcnt lgkmcnt(2)\n"
    "v_cvt_pk_bf16_f32 v81, v42, v43\n"
    "v_mfma_f32_16x16x32_bf16 v[28:31], a[136:139], a[240:243], v[104:107]\n"
    "v_cvt_pk_bf16_f32 v96, v48, v49\n"
    "v_cvt_pk_bf16_f32 v90, v60, v61\n"
    "v_mfma_f32_16x16x32_bf16 v[8:11], a[120:123], a[224:227], v[8:11]\n"
    "v_exp_f32 v7, v7\n"
    "v_mfma_f32_16x16x32_bf16 v[24:27], a[152:155], a[232:235], v[24:27]\n"
    "v_exp_f32 v67, v67\n"
    "v_mfma_f32_16x16x32_bf16 v[8:11], a[116:119], a[220:223], v[8:11]\n"
    "ds_read_b128 a[124:127], v252 offset:18432\n"
    "v_mfma_f32_16x16x32_bf16 v[16:19], a[112:115], a[168:171], v[16:19]\n"
    "buffer_load_dwordx4 v1, s[16:19], s83 offen offset:3072 sc1 lds\n"
    "v_mfma_f32_16x16x32_bf16 v[32:35], a[152:155], a[184:187], v[32:35]\n"
    "s_waitcnt lgkmcnt(1)\n"
    "ds_read_b128 a[132:135], v252 offset:22528\n"
    "v_cvt_pk_bf16_f32 v75, v54, v55\n"
    "v_mfma_f32_16x16x32_bf16 v[24:27], a[148:151], a[228:231], v[24:27]\n"
    "s_add_u32 m0, s65, 77824\n"
    "v_cvt_pk_bf16_f32 v74, v52, v53\n"
    "v_cvt_pk_bf16_f32 v97, v50, v51\n"
    "v_mfma_f32_16x16x32_bf16 v[20:23], a[136:139], a[192:195], v[104:107]\n"
    "s_add_u32 s83, s67, 28672\n"
    "v_exp_f32 v6, v6\n"
    "v_mfma_f32_16x16x32_bf16 v[20:23], a[140:143], a[196:199], v[20:23]\n"
    "v_cvt_pk_bf16_f32 v99, v66, v67\n"
    "v_exp_f32 v14, v14\n"
    "v_mfma_f32_16x16x32_bf16 v[24:27], a[144:147], a[224:227], v[24:27]\n"
    "ds_read_b128 a[128:131], v252 offset:20480\n"
    "ds_read_b128 v[104:107], v250 offset:912\n"
    "v_mfma_f32_16x16x32_bf16 v[8:11], a[112:115], a[216:219], v[8:11]\n"
    "v_exp_f32 v46, v46\n"
    "v_mfma_f32_16x16x32_bf16 v[28:31], a[140:143], a[244:247], v[28:31]\n"
    "v_exp_f32 v13, v13\n"
    "v_mfma_f32_16x16x32_bf16 v[32:35], a[148:151], a[180:183], v[32:35]\n"
    "s_waitcnt lgkmcnt(2)\n"
    "v_exp_f32 v64, v64\n"
    "v_mfma_f32_16x16x32_bf16 v[40:43], a[132:135], a[236:239], v[100:103]\n"
    "ds_read_b128 a[120:123], v252 offset:16384\n"
    "v_cvt_pk_bf16_f32 v91, v62, v63\n"
    "v_mfma_f32_16x16x32_bf16 v[32:35], a[144:147], a[176:179], v[32:35]\n"
    "ds_read_b128 a[112:115], v252 offset:12288\n"
    "v_cvt_pk_bf16_f32 v73, v38, v39\n"
    "v_mfma_f32_16x16x32_bf16 v[28:31], a[144:147], a[248:251], v[28:31]\n"
    "v_exp_f32 v11, v11\n"
    "v_mfma_f32_16x16x32_bf16 v[32:35], a[140:143], a[172:175], v[32:35]\n"
    "v_cvt_pk_bf16_f32 v88, v44, v45\n"
    "v_cvt_pk_bf16_f32 v89, v46, v47\n"
    "v_mfma_f32_16x16x32_bf16 v[32:35], a[136:139], a[168:171], v[32:35]\n"
    "buffer_load_dwordx4 v1, s[16:19], s83 offen offset:0 sc1 lds\n"
    "v_mfma_f32_16x16x32_bf16 v[24:27], a[140:143], a[220:223], v[24:27]\n"
    "v_exp_f32 v9, v9\n"
    "v_mfma_f32_16x16x32_bf16 v[20:23], a[144:147], a[200:203], v[20:23]\n"
    "buffer_load_dwordx4 v1, s[16:19], s83 offen offset:1024 sc1 lds\n"
    "v_mfma_f32_16x16x32_bf16 v[20:23], a[148:151], a[204:207], v[20:23]\n"
    "v_exp_f32 v15, v15\n"
    "v_mfma_f32_16x16x32_bf16 v[20:23], a[152:155], a[208:211], v[20:23]\n"
    "v_exp_f32 v34, v34\n"
    "v_mfma_f32_16x16x32_bf16 v[20:23], a[156:159], a[212:215], v[20:23]\n"
    "v_exp_f32 v10, v10\n"
    "v_mfma_f32_16x16x32_bf16 v[28:31], a[148:151], a[252:255], v[28:31]\n"
    "s_add_u32 s59, s59, 4\n"
    "ds_read_b128 a[140:143], v252 offset:15360\n"
    "ds_read_b128 a[116:119], v252 offset:14336\n"
    "v_mfma_f32_16x16x32_bf16 v[28:31], a[152:155], a[160:163], v[28:31]\n"
    "s_waitcnt lgkmcnt(5)\n"
    "v_exp_f32 v4, v4\n"
    "v_mfma_f32_16x16x32_bf16 v[40:43], a[128:131], a[232:235], v[40:43]\n"
    "v_exp_f32 v12, v12\n"
    "v_mfma_f32_16x16x32_bf16 v[24:27], a[136:139], a[216:219], v[24:27]\n"
    "ds_read_b128 a[136:139], v252 offset:13312\n"
    "v_add_u32 v3, v3, v224\n"
    "v_mfma_f32_16x16x32_bf16 v[48:51], a[132:135], a[188:191], v[100:103]\n"
    "v_exp_f32 v23, v23\n"
    "v_smfmac_f32_16x16x64_bf16 v[116:119], v[232:235], v[84:91], v253\n"
    "ds_read_b128 a[152:155], v252 offset:21504\n"
    "v_mfma_f32_16x16x32_bf16 v[28:31], a[156:159], a[164:167], v[28:31]\n"
    "s_waitcnt lgkmcnt(5)\n"
    "v_exp_f32 v5, v5\n"
    "v_mfma_f32_16x16x32_bf16 v[40:43], a[124:127], a[228:231], v[40:43]\n"
    "s_mov_b32 s74, 0x47000000\n"
    "v_exp_f32 v20, v20\n"
    "v_mfma_f32_16x16x32_bf16 v[40:43], a[120:123], a[224:227], v[40:43]\n"
    "s_waitcnt lgkmcnt(4)\n"
    "ds_read_b128 a[156:159], v252 offset:23552\n"
    "v_cvt_pk_bf16_f32 v204, v4, v5\n"
    "v_mfma_f32_16x16x32_bf16 v[36:39], a[112:115], a[192:195], v[100:103]\n"
    "s_waitcnt lgkmcnt(3)\n"
    "v_exp_f32 v32, v32\n"
    "v_mfma_f32_16x16x32_bf16 v[36:39], a[116:119], a[196:199], v[36:39]\n"
    "v_cvt_pk_bf16_f32 v220, v12, v13\n"
    "v_cvt_pk_bf16_f32 v82, v56, v57\n"
    "v_mfma_f32_16x16x32_bf16 v[40:43], a[116:119], a[220:223], v[40:43]\n"
    "v_exp_f32 v16, v16\n"
    "v_mfma_f32_16x16x32_bf16 v[40:43], a[112:115], a[216:219], v[40:43]\n"
    "s_waitcnt lgkmcnt(2)\n"
    "v_exp_f32 v28, v28\n"
    "v_mfma_f32_16x16x32_bf16 v[52:55], a[136:139], a[192:195], v[104:107]\n"
    "v_exp_f32 v33, v33\n"
    "v_mfma_f32_16x16x32_bf16 v[48:51], a[128:131], a[184:187], v[48:51]\n"
    "v_exp_f32 v8, v8\n"
    "v_mfma_f32_16x16x32_bf16 v[44:47], a[112:115], a[240:243], v[100:103]\n"
    "v_exp_f32 v17, v17\n"
    "v_mfma_f32_16x16x32_bf16 v[60:63], a[136:139], a[240:243], v[104:107]\n"
    "v_cvt_pk_bf16_f32 v98, v64, v65\n"
    "v_cvt_pk_bf16_f32 v83, v58, v59\n"
    "v_mfma_f32_16x16x32_bf16 v[48:51], a[124:127], a[180:183], v[48:51]\n"
    "v_exp_f32 v35, v35\n"
    "v_mfma_f32_16x16x32_bf16 v[44:47], a[116:119], a[244:247], v[44:47]\n"
    "v_exp_f32 v41, v41\n"
    "v_mfma_f32_16x16x32_bf16 v[44:47], a[120:123], a[248:251], v[44:47]\n"
    "v_exp_f32 v29, v29\n"
    "v_mfma_f32_16x16x32_bf16 v[60:63], a[140:143], a[244:247], v[60:63]\n"
    "v_exp_f32 v31, v31\n"
    "v_mfma_f32_16x16x32_bf16 v[36:39], a[120:123], a[200:203], v[36:39]\n"
    "v_lshrrev_b32 v224, 3, v1\n"
    "v_cvt_pk_bf16_f32 v205, v6, v7\n"
    "v_mfma_f32_16x16x32_bf16 v[48:51], a[120:123], a[176:179], v[48:51]\n"
    "s_waitcnt vmcnt(6)\n"
    "ds_add_u32 v247, v246\n"
    "v_mfma_f32_16x16x32_bf16 v[48:51], a[116:119], a[172:175], v[48:51]\n"
    "s_waitcnt lgkmcnt(1)\n"
    "ds_read_b128 a[144:147], v252 offset:17408\n"
    "ds_read_b128 a[148:151], v252 offset:19456\n"
    "v_mfma_f32_16x16x32_bf16 v[56:59], a[156:159], a[236:239], v[104:107]\n"
    "v_exp_f32 v25, v25\n"
    "v_cvt_pk_bf16_f32 v228, v16, v17\n"
    "v_mfma_f32_16x16x32_bf16 v[52:55], a[140:143], a[196:199], v[52:55]\n"
    "v_exp_f32 v18, v18\n"
    "v_mfma_f32_16x16x32_bf16 v[44:47], a[124:127], a[252:255], v[44:47]\n"
    "v_exp_f32 v19, v19\n"
    "v_mfma_f32_16x16x32_bf16 v[64:67], a[156:159], a[188:191], v[104:107]\n"
    "v_exp_f32 v43, v43\n"
    "v_mfma_f32_16x16x32_bf16 v[48:51], a[112:115], a[168:171], v[48:51]\n"
    "v_exp_f32 v27, v27\n"
    "v_mfma_f32_16x16x32_bf16 v[36:39], a[124:127], a[204:207], v[36:39]\n"
    "v_exp_f32 v30, v30\n"
    "v_mfma_f32_16x16x32_bf16 v[64:67], a[152:155], a[184:187], v[64:67]\n"
    "v_exp_f32 v22, v22\n"
    "v_mfma_f32_16x16x32_bf16 v[36:39], a[128:131], a[208:211], v[36:39]\n"
    "v_exp_f32 v42, v42\n"
    "v_mfma_f32_16x16x32_bf16 v[44:47], a[128:131], a[160:163], v[44:47]\n"
    "s_waitcnt lgkmcnt(1)\n"
    "v_exp_f32 v26, v26\n"
    "v_mfma_f32_16x16x32_bf16 v[60:63], a[144:147], a[248:251], v[60:63]\n"
    "s_waitcnt lgkmcnt(0)\n"
    "v_exp_f32 v40, v40\n"
    "v_mfma_f32_16x16x32_bf16 v[64:67], a[148:151], a[180:183], v[64:67]\n"
    "ds_read_b128 v[100:103], v250 offset:1536\n"
    "ds_read_b32 v245, v247\n"
    "v_mfma_f32_16x16x32_bf16 v[56:59], a[152:155], a[232:235], v[56:59]\n"
    "v_cvt_pk_bf16_f32 v216, v40, v41\n"
    "v_cvt_pk_bf16_f32 v231, v34, v35\n"
    "v_mfma_f32_16x16x32_bf16 v[64:67], a[144:147], a[176:179], v[64:67]\n"
    "v_exp_f32 v21, v21\n"
    "v_smfmac_f32_16x16x64_bf16 v[120:123], v[232:235], v[92:99], v253\n"
    "v_cvt_pk_bf16_f32 v221, v14, v15\n"
    "v_mfma_f32_16x16x32_bf16 v[44:47], a[132:135], a[164:167], v[44:47]\n"
    "v_cvt_pk_bf16_f32 v207, v22, v23\n"
    "v_cvt_pk_bf16_f32 v229, v18, v19\n"
    "v_mfma_f32_16x16x32_bf16 v[64:67], a[140:143], a[172:175], v[64:67]\n"
    "v_cvt_pk_bf16_f32 v206, v20, v21\n"
    "v_cvt_pk_bf16_f32 v212, v8, v9\n"
    "v_mfma_f32_16x16x32_bf16 v[52:55], a[144:147], a[200:203], v[52:55]\n"
    "v_exp_f32 v50, v50\n"
    "v_mfma_f32_16x16x32_bf16 v[60:63], a[148:151], a[252:255], v[60:63]\n"
    "v_exp_f32 v24, v24\n"
    "v_mfma_f32_16x16x32_bf16 v[36:39], a[132:135], a[212:215], v[36:39]\n"
    "s_waitcnt lgkmcnt(0)\n"
    "v_cmp_le_u32 s[56:57], s59, v245\n"
    "v_cvt_pk_bf16_f32 v213, v10, v11\n"
    "v_smfmac_f32_16x16x64_bf16 v[108:111], v[232:235], v[68:75], v253\n"
    "s_cmp_eq_u64 s[56:57], -1\n"
    "v_cvt_pk_bf16_f32 v214, v24, v25\n"
    "v_smfmac_f32_16x16x64_bf16 v[112:115], v[232:235], v[76:83], v253\n"
    "s_cbranch_scc0 .Lpt1\n"
    ".Lpt1r:\n"
    "v_mfma_f32_16x16x32_bf16 v[52:55], a[148:151], a[204:207], v[52:55]\n"
    "s_add_u32 m0, s65, 49152\n"
    "ds_read_b128 a[128:131], v252 offset:32768\n"
    "ds_read_b128 a[124:127], v252 offset:30720\n"
    "v_mfma_f32_16x16x32_bf16 v[60:63], a[152:155], a[160:163], v[60:63]\n"
    "ds_read_b128 a[112:115], v252 offset:24576\n"
    "v_cvt_pk_bf16_f32 v222, v28, v29\n"
    "v_mfma_f32_16x16x32_bf16 v[60:63], a[156:159], a[164:167], v[60:63]\n"
    "s_add_u32 s83, s67, 73728\n"
    "ds_read_b128 a[132:135], v252 offset:34816\n"
    "v_cvt_pk_bf16_f32 v215, v26, v27\n"
    "v_mfma_f32_16x16x32_bf16 v[56:59], a[148:151], a[228:231], v[56:59]\n"
    "buffer_load_dwordx4 v1, s[16:19], s83 offen offset:0 sc1 lds\n"
    "v_mfma_f32_16x16x32_bf16 v[56:59], a[144:147], a[224:227], v[56:59]\n"
    "ds_read_b128 a[116:119], v252 offset:26624\n"
    "v_cvt_pk_bf16_f32 v223, v30, v31\n"
    "v_mfma_f32_16x16x32_bf16 v[56:59], a[140:143], a[220:223], v[56:59]\n"
    "v_exp_f32 v38, v38\n"
    "v_exp_f32 v47, v47\n"
    "v_mfma_f32_16x16x32_bf16 v[52:55], a[152:155], a[208:211], v[52:55]\n"
    "ds_read_b128 a[120:123], v252 offset:28672\n"
    "ds_read_b128 v[104:107], v250 offset:1552\n"
    "v_mfma_f32_16x16x32_bf16 v[64:67], a[136:139], a[168:171], v[64:67]\n"
    "buffer_load_dwordx4 v1, s[16:19], s83 offen offset:1024 sc1 lds\n"
    "v_mfma_f32_16x16x32_bf16 v[56:59], a[136:139], a[216:219], v[56:59]\n"
    "v_exp_f32 v37, v37\n"
    "v_mfma_f32_16x16x32_bf16 v[52:55], a[156:159], a[212:215], v[52:55]\n"
    "s_waitcnt lgkmcnt(4)\n"
    "buffer_load_dwordx4 v1, s[16:19], s83 offen offset:2048 sc1 lds\n"
    "v_mfma_f32_16x16x32_bf16 v[4:7], a[112:115], a[192:195], v[100:103]\n"
    "ds_read_b128 a[140:143], v252 offset:27648\n"
    "v_cvt_pk_bf16_f32 v230, v32, v33\n"
    "v_mfma_f32_16x16x32_bf16 v[20:23], a[112:115], a[240:243], v[100:103]\n"
    "s_waitcnt lgkmcnt(3)\n"
    "buffer_load_dwordx4 v1, s[16:19], s83 offen offset:3072 sc1 lds\n"
    "v_mfma_f32_16x16x32_bf16 v[20:23], a[116:119], a[244:247], v[20:23]\n"
    "s_add_u32 m0, s65, 53248\n"
    "v_exp_f32 v58, v58\n"
    "v_mfma_f32_16x16x32_bf16 v[28:31], a[132:135], a[188:191], v[100:103]\n"
    "s_add_u32 s83, s67, 77824\n"
    "v_exp_f32 v44, v44\n"
    "v_mfma_f32_16x16x32_bf16 v[28:31], a[128:131], a[184:187], v[28:31]\n"
    "ds_read_b128 a[148:151], v252 offset:31744\n"
    "ds_read_b128 a[152:155], v252 offset:33792\n"
    "v_mfma_f32_16x16x32_bf16 v[12:15], a[132:135], a[236:239], v[100:103]\n"
    "ds_read_b128 a[156:159], v252 offset:35840\n"
    "v_and_b32 v224, 0x70, v224\n"
    "v_mfma_f32_16x16x32_bf16 v[12:15], a[128:131], a[232:235], v[12:15]\n"
    "buffer_load_dwordx4 v1, s[16:19], s83 offen offset:0 sc1 lds\n"
    "v_mfma_f32_16x16x32_bf16 v[28:31], a[124:127], a[180:183], v[28:31]\n"
    "v_exp_f32 v55, v55\n"
    "v_mfma_f32_16x16x32_bf16 v[4:7], a[116:119], a[196:199], v[4:7]\n"
    "s_waitcnt lgkmcnt(5)\n"
    "buffer_load_dwordx4 v1, s[16:19], s83 offen offset:1024 sc1 lds\n"
    "v_mfma_f32_16x16x32_bf16 v[28:31], a[120:123], a[176:179], v[28:31]\n"
    "v_exp_f32 v65, v65\n"
    "v_mfma_f32_16x16x32_bf16 v[28:31], a[116:119], a[172:175], v[28:31]\n"
    "v_exp_f32 v61, v61\n"
    "v_mfma_f32_16x16x32_bf16 v[12:15], a[124:127], a[228:231], v[12:15]\n"
    "v_exp_f32 v53, v53\n"
    "v_mfma_f32_16x16x32_bf16 v[4:7], a[120:123], a[200:203], v[4:7]\n"
    "buffer_load_dwordx2 v[236:237], v241, s[36:39], 0 offen offset:0 sc1\n"
    "v_mfma_f32_16x16x32_bf16 v[20:23], a[120:123], a[248:251], v[20:23]\n"
    "s_waitcnt lgkmcnt(0)\n"
    "v_exp_f32 v67, v67\n"
    "v_mfma_f32_16x16x32_bf16 v[16:19], a[156:159], a[236:239], v[104:107]\n"
    "ds_read_b128 a[136:139], v252 offset:25600\n"
    "ds_read_b128 v[100:103], v250 offset:1664\n"
    "v_mfma_f32_16x16x32_bf16 v[4:7], a[124:127], a[204:207], v[4:7]\n"
    "v_exp_f32 v36, v36\n"
    "v_mfma_f32_16x16x32_bf16 v[28:31], a[112:115], a[168:171], v[28:31]\n"
    "s_waitcnt vmcnt(7)\n"
    "buffer_store_dwordx2 v[238:239], v240, s[36:39], s82 offen offset:0 sc1\n"
    "v_exp_f32 v46, v46\n"
    "v_mfma_f32_16x16x32_bf16 v[32:35], a[156:159], a[188:191], v[104:107]\n"
    "v_exp_f32 v60, v60\n"
    "v_mfma_f32_16x16x32_bf16 v[12:15], a[120:123], a[224:227], v[12:15]\n"
    "v_exp_f32 v64, v64\n"
    "v_mfma_f32_16x16x32_bf16 v[4:7], a[128:131], a[208:211], v[4:7]\n"
    "v_exp_f32 v66, v66\n"
    "v_mfma_f32_16x16x32_bf16 v[12:15], a[116:119], a[220:223], v[12:15]\n"
    "v_exp_f32 v49, v49\n"
    "v_mfma_f32_16x16x32_bf16 v[20:23], a[124:127], a[252:255], v[20:23]\n"
    "ds_read_b128 a[116:119], v252 offset:38912\n"
    "v_cvt_pk_bf16_f32 v235, v66, v67\n"
    "v_mfma_f32_16x16x32_bf16 v[32:35], a[152:155], a[184:187], v[32:35]\n"
    "ds_read_b128 a[120:123], v252 offset:40960\n"
    "v_cvt_pk_bf16_f32 v208, v36, v37\n"
    "v_mfma_f32_16x16x32_bf16 v[12:15], a[112:115], a[216:219], v[12:15]\n"
    "s_mov_b32 s73, 0x7f800000\n"
    "ds_read_b128 a[144:147], v252 offset:29696\n"
    "ds_read_b128 a[112:115], v252 offset:36864\n"
    "v_mfma_f32_16x16x32_bf16 v[4:7], a[132:135], a[212:215], v[4:7]\n"
    "s_waitcnt lgkmcnt(5)\n"
    "ds_add_u32 v247, v246\n"
    "v_mfma_f32_16x16x32_bf16 v[24:27], a[136:139], a[240:243], v[104:107]\n"
    "v_exp_f32 v39, v39\n"
    "v_mfma_f32_16x16x32_bf16 v[16:19], a[152:155], a[232:235], v[16:19]\n"
    "v_cvt_pk_bf16_f32 v226, v60, v61\n"
    "v_cvt_pk_bf16_f32 v234, v64, v65\n"
    "v_mfma_f32_16x16x32_bf16 v[8:11], a[136:139], a[192:195], v[104:107]\n"
    "v_exp_f32 v48, v48\n"
    "v_mfma_f32_16x16x32_bf16 v[24:27], a[140:143], a[244:247], v[24:27]\n"
    "v_exp_f32 v56, v56\n"
    "v_mfma_f32_16x16x32_bf16 v[20:23], a[128:131], a[160:163], v[20:23]\n"
    "ds_read_b128 v[240:243], v251 offset:768\n"
    "v_cvt_pk_bf16_f32 v209, v38, v39\n"
    "v_mfma_f32_16x16x32_bf16 v[16:19], a[148:151], a[228:231], v[16:19]\n"
    "v_exp_f32 v62, v62\n"
    "v_mfma_f32_16x16x32_bf16 v[20:23], a[132:135], a[164:167], v[20:23]\n"
    "v_exp_f32 v63, v63\n"
    "v_mfma_f32_16x16x32_bf16 v[8:11], a[140:143], a[196:199], v[8:11]\n"
    "s_waitcnt lgkmcnt(3)\n"
    "ds_read_b128 a[128:131], v252 offset:45056\n"
    "ds_read_b128 a[132:135], v252 offset:47104\n"
    "v_mfma_f32_16x16x32_bf16 v[24:27], a[144:147], a[248:251], v[24:27]\n"
    "v_exp_f32 v12, v12\n"
    "v_mfma_f32_16x16x32_bf16 v[8:11], a[144:147], a[200:203], v[8:11]\n"
    "v_exp_f32 v13, v13\n"
    "v_mfma_f32_16x16x32_bf16 v[24:27], a[148:151], a[252:255], v[24:27]\n"
    "v_exp_f32 v4, v4\n"
    "v_mfma_f32_16x16x32_bf16 v[24:27], a[152:155], a[160:163], v[24:27]\n"
    "v_exp_f32 v59, v59\n"
    "v_mfma_f32_16x16x32_bf16 v[16:19], a[144:147], a[224:227], v[16:19]\n"
    "s_mov_b32 s79, 133\n"
    "v_exp_f32 v5, v5\n"
    "v_mfma_f32_16x16x32_bf16 v[16:19], a[140:143], a[220:223], v[16:19]\n"
    "v_exp_f32 v54, v54\n"
    "v_mfma_f32_16x16x32_bf16 v[8:11], a[148:151], a[204:207], v[8:11]\n"
    "v_exp_f32 v45, v45\n"
    "v_mfma_f32_16x16x32_bf16 v[16:19], a[136:139], a[216:219], v[16:19]\n"
    "ds_read_b128 a[124:127], v252 offset:43008\n"
    "ds_read_b32 v245, v247\n"
    "v_mfma_f32_16x16x32_bf16 v[32:35], a[148:151], a[180:183], v[32:35]\n"
    "v_exp_f32 v6, v6\n"
    "v_mfma_f32_16x16x32_bf16 v[8:11], a[152:155], a[208:211], v[8:11]\n"
    "ds_read_b128 a[152:155], v252 offset:46080\n"
    "v_cvt_pk_bf16_f32 v217, v42, v43\n"
    "v_mfma_f32_16x16x32_bf16 v[8:11], a[156:159], a[212:215], v[8:11]\n"
    "v_exp_f32 v19, v19\n"
    "v_mfma_f32_16x16x32_bf16 v[24:27], a[156:159], a[164:167], v[24:27]\n"
    "v_exp_f32 v51, v51\n"
    "v_mfma_f32_16x16x32_bf16 v[32:35], a[144:147], a[176:179], v[32:35]\n"
    "s_waitcnt lgkmcnt(7)\n"
    "v_cvt_pk_bf16_f32 v219, v58, v59\n"
    "v_cvt_pk_bf16_f32 v233, v50, v51\n"
    "v_mfma_f32_16x16x32_bf16 v[36:39], a[112:115], a[192:195], v[100:103]\n"
    "s_waitcnt lgkmcnt(3)\n"
    "v_exp_f32 v21, v21\n"
    "v_mfma_f32_16x16x32_bf16 v[40:43], a[132:135], a[236:239], v[100:103]\n"
    "ds_read_b128 a[156:159], v252 offset:48128\n"
    "v_cvt_pk_bf16_f32 v227, v62, v63\n"
    "v_mfma_f32_16x16x32_bf16 v[36:39], a[116:119], a[196:199], v[36:39]\n"
    "s_add_u32 s59, s59, 4\n"
    "v_exp_f32 v28, v28\n"
    "v_mfma_f32_16x16x32_bf16 v[32:35], a[140:143], a[172:175], v[32:35]\n"
    "v_exp_f32 v52, v52\n"
    "v_mfma_f32_16x16x32_bf16 v[32:35], a[136:139], a[168:171], v[32:35]\n"
    "v_cvt_pk_bf16_f32 v232, v48, v49\n"
    "v_mad_u32_u24 v244, v224, 24, v2\n"
    "v_mfma_f32_16x16x32_bf16 v[48:51], a[132:135], a[188:191], v[100:103]\n"
    "ds_read_b128 a[136:139], v252 offset:37888\n"
    "ds_read_b128 v[104:107], v250 offset:1680\n"
    "v_mfma_f32_16x16x32_bf16 v[36:39], a[120:123], a[200:203], v[36:39]\n"
    "s_waitcnt lgkmcnt(5)\n"
    "v_exp_f32 v57, v57\n"
    "v_mfma_f32_16x16x32_bf16 v[36:39], a[124:127], a[204:207], v[36:39]\n"
    "v_cvt_pk_bf16_f32 v225, v46, v47\n"
    "v_cvt_pk_bf16_f32 v210, v52, v53\n"
    "v_mfma_f32_16x16x32_bf16 v[36:39], a[128:131], a[208:211], v[36:39]\n"
    "v_cvt_pk_bf16_f32 v224, v44, v45\n"
    "v_cvt_pk_bf16_f32 v211, v54, v55\n"
    "v_mfma_f32_16x16x32_bf16 v[40:43], a[128:131], a[232:235], v[40:43]\n"
    "ds_read_b128 a[144:147], v252 offset:41984\n"
    "ds_read_b128 a[140:143], v252 offset:39936\n"
    "v_mfma_f32_16x16x32_bf16 v[44:47], a[112:115], a[240:243], v[100:103]\n"
    "v_exp_f32 v20, v20\n"
    "v_mfma_f32_16x16x32_bf16 v[40:43], a[124:127], a[228:231], v[40:43]\n"
    "v_cvt_pk_bf16_f32 v12, v12, v13\n"
    "v_cvt_pk_bf16_f32 v218, v56, v57\n"
    "v_mfma_f32_16x16x32_bf16 v[36:39], a[132:135], a[212:215], v[36:39]\n"
    "v_exp_f32 v32, v32\n"
    "v_mfma_f32_16x16x32_bf16 v[40:43], a[120:123], a[224:227], v[40:43]\n"
    "v_exp_f32 v30, v30\n"
    "v_smfmac_f32_16x16x64_bf16 v[120:123], v[240:243], v[228:235], v253\n"
    "ds_read_b128 a[148:151], v252 offset:44032\n"
    "v_mfma_f32_16x16x32_bf16 v[44:47], a[116:119], a[244:247], v[44:47]\n"
    "s_waitcnt lgkmcnt(3)\n"
    "v_exp_f32 v26, v26\n"
    "v_mfma_f32_16x16x32_bf16 v[52:55], a[136:139], a[192:195], v[104:107]\n"
    "v_exp_f32 v15, v15\n"
    "v_mfma_f32_16x16x32_bf16 v[56:59], a[156:159], a[236:239], v[104:107]\n"
    "s_waitcnt lgkmcnt(1)\n"
    "v_cvt_pk_bf16_f32 v4, v4, v5\n"
    "v_cmp_le_u32 s[56:57], s59, v245\n"
    "v_smfmac_f32_16x16x64_bf16 v[108:111], v[240:243], v[204:211], v253\n"
    "s_cmp_eq_u64 s[56:57], -1\n"
    "v_lshrrev_b32 v252, 3, v1\n"
    "v_mfma_f32_16x16x32_bf16 v[52:55], a[140:143], a[196:199], v[52:55]\n"
    "v_exp_f32 v7, v7\n"
    "v_mfma_f32_16x16x32_bf16 v[60:63], a[136:139], a[240:243], v[104:107]\n"
    "v_exp_f32 v17, v17\n"
    "v_mfma_f32_16x16x32_bf16 v[60:63], a[140:143], a[244:247], v[60:63]\n"
    "v_exp_f32 v14, v14\n"
    "v_mfma_f32_16x16x32_bf16 v[56:59], a[152:155], a[232:235], v[56:59]\n"
    "v_exp_f32 v39, v39\n"
    "v_mfma_f32_16x16x32_bf16 v[64:67], a[156:159], a[188:191], v[104:107]\n"
    "v_exp_f32 v11, v11\n"
    "v_mfma_f32_16x16x32_bf16 v[60:63], a[144:147], a[248:251], v[60:63]\n"
    "v_exp_f32 v22, v22\n"
    "v_mfma_f32_16x16x32_bf16 v[52:55], a[144:147], a[200:203], v[52:55]\n"
    "s_waitcnt lgkmcnt(0)\n"
    "v_exp_f32 v27, v27\n"
    "v_mfma_f32_16x16x32_bf16 v[52:55], a[148:151], a[204:207], v[52:55]\n"
    "v_exp_f32 v8, v8\n"
    "v_mfma_f32_16x16x32_bf16 v[52:55], a[152:155], a[208:211], v[52:55]\n"
    "v_exp_f32 v9, v9\n"
    "v_mfma_f32_16x16x32_bf16 v[56:59], a[148:151], a[228:231], v[56:59]\n"
    "ds_read_b128 a[204:207], v248 offset:13312\n"
    "v_cvt_pk_bf16_f32 v5, v6, v7\n"
    "v_mfma_f32_16x16x32_bf16 v[64:67], a[152:155], a[184:187], v[64:67]\n"
    "v_exp_f32 v16, v16\n"
    "v_mfma_f32_16x16x32_bf16 v[56:59], a[144:147], a[224:227], v[56:59]\n"
    "ds_read_b128 a[208:211], v248 offset:2048\n"
    "ds_read_b128 a[196:199], v248 offset:12288\n"
    "v_mfma_f32_16x16x32_bf16 v[40:43], a[116:119], a[220:223], v[40:43]\n"
    "v_exp_f32_e64 v38, v38\n"
    "v_mfma_f32_16x16x32_bf16 v[52:55], a[156:159], a[212:215], v[52:55]\n"
    "v_exp_f32_e64 v37, v37\n"
    "v_smfmac_f32_16x16x64_bf16 v[116:119], v[240:243], v[220:227], v253\n"
    "v_cvt_pk_bf16_f32 v6, v8, v9\n"
    "v_smfmac_f32_16x16x64_bf16 v[112:115], v[240:243], v[212:219], v253\n"
    "s_cbranch_scc0 .Lpt4\n"
    ".Lpt4r:\n"
    "v_mfma_f32_16x16x32_bf16 v[44:47], a[120:123], a[248:251], v[44:47]\n"
    "v_exp_f32 v10, v10\n"
    "v_mfma_f32_16x16x32_bf16 v[40:43], a[112:115], a[216:219], v[40:43]\n"
    "v_exp_f32 v25, v25\n"
    "v_mfma_f32_16x16x32_bf16 v[48:51], a[128:131], a[184:187], v[48:51]\n"
    "v_exp_f32 v54, v54\n"
    "v_mfma_f32_16x16x32_bf16 v[56:59], a[140:143], a[220:223], v[56:59]\n"
    "s_waitcnt lgkmcnt(1)\n"
    "ds_read_b128 a[212:215], v248 offset:14336\n"
    "v_cvt_pk_bf16_f32 v20, v20, v21\n"
    "v_mfma_f32_16x16x32_bf16 v[148:151], a[208:211], v[76:79], v[148:151]\n"
    "s_waitcnt vmcnt(0)\n"
    "ds_add_u32 v247, v246\n"
    "v_mfma_f32_16x16x32_bf16 v[64:67], a[148:151], a[180:183], v[64:67]\n"
    "ds_read_b128 a[192:195], v248 offset:0\n"
    "ds_read_b128 a[200:203], v248 offset:1024\n"
    "v_mfma_f32_16x16x32_bf16 v[48:51], a[124:127], a[180:183], v[48:51]\n"
    "v_exp_f32 v52, v52\n"
    "v_mfma_f32_16x16x32_bf16 v[128:131], a[204:207], v[72:75], v[128:131]\n"
    "v_exp_f32 v41, v41\n"
    "v_mfma_f32_16x16x32_bf16 v[144:147], a[204:207], v[80:83], v[144:147]\n"
    "v_exp_f32 v36, v36\n"
    "v_mfma_f32_16x16x32_bf16 v[44:47], a[124:127], a[252:255], v[44:47]\n"
    "v_exp_f32 v24, v24\n"
    "v_mfma_f32_16x16x32_bf16 v[60:63], a[148:151], a[252:255], v[60:63]\n"
    "v_exp_f32 v23, v23\n"
    "v_mfma_f32_16x16x32_bf16 v[44:47], a[128:131], a[160:163], v[44:47]\n"
    "v_exp_f32 v55, v55\n"
    "v_mfma_f32_16x16x32_bf16 v[48:51], a[120:123], a[176:179], v[48:51]\n"
    "v_exp_f32 v29, v29\n"
    "v_mfma_f32_16x16x32_bf16 v[64:67], a[144:147], a[176:179], v[64:67]\n"
    "ds_read_b128 a[236:239], v249 offset:37888\n"
    "v_cvt_pk_bf16_f32 v9, v38, v39\n"
    "v_mfma_f32_16x16x32_bf16 v[56:59], a[136:139], a[216:219], v[56:59]\n"
    "ds_read_b128 a[220:223], v248 offset:15360\n"
    "ds_read_b128 a[244:247], v249 offset:38912\n"
    "v_mfma_f32_16x16x32_bf16 v[48:51], a[116:119], a[172:175], v[48:51]\n"
    "s_waitcnt lgkmcnt(6)\n"
    "ds_read_b128 a[232:235], v249 offset:25600\n"
    "ds_read_b128 a[240:243], v249 offset:26624\n"
    "v_mfma_f32_16x16x32_bf16 v[64:67], a[140:143], a[172:175], v[64:67]\n"
    "s_add_u32 s59, s59, 4\n"
    "v_cvt_pk_bf16_f32 v7, v10, v11\n"
    "v_cvt_pk_bf16_f32 v13, v14, v15\n"
    "v_mfma_f32_16x16x32_bf16 v[148:151], a[212:215], v[80:83], v[148:151]\n"
    "v_exp_f32 v53, v53\n"
    "v_mfma_f32_16x16x32_bf16 v[44:47], a[132:135], a[164:167], v[44:47]\n"
    "ds_read_b32 v245, v247\n"
    "v_cvt_pk_bf16_f32 v11, v54, v55\n"
    "v_mfma_f32_16x16x32_bf16 v[60:63], a[152:155], a[160:163], v[60:63]\n"
    "ds_read_b128 v[240:243], v251 offset:1536\n"
    "ds_read_b128 a[216:219], v248 offset:3072\n"
    "v_mfma_f32_16x16x32_bf16 v[64:67], a[136:139], a[168:171], v[64:67]\n"
    "s_waitcnt lgkmcnt(8)\n"
    "v_cvt_pk_bf16_f32 v8, v36, v37\n"
    "v_cvt_pk_bf16_f32 v10, v52, v53\n"
    "v_mfma_f32_16x16x32_bf16 v[128:131], a[200:203], v[68:71], v[128:131]\n"
    "v_exp_f32 v43, v43\n"
    "v_mfma_f32_16x16x32_bf16 v[60:63], a[156:159], a[164:167], v[60:63]\n"
    "v_exp_f32 v45, v45\n"
    "v_mfma_f32_16x16x32_bf16 v[140:143], a[192:195], v[76:79], v[140:143]\n"
    "ds_read_b128 a[252:255], v249 offset:39936\n"
    "ds_read_b128 a[224:227], v249 offset:24576\n"
    "v_mfma_f32_16x16x32_bf16 v[124:127], a[192:195], v[68:71], v[124:127]\n"
    "s_waitcnt lgkmcnt(9)\n"
    "v_cvt_pk_bf16_f32 v14, v16, v17\n"
    "v_cvt_pk_bf16_f32 v21, v22, v23\n"
    "v_mfma_f32_16x16x32_bf16 v[128:131], a[236:239], v[208:211], v[128:131]\n"
    "v_exp_f32 v58, v58\n"
    "v_mfma_f32_16x16x32_bf16 v[140:143], a[196:199], v[80:83], v[140:143]\n"
    "s_waitcnt lgkmcnt(4)\n"
    "ds_read_b128 a[228:231], v249 offset:36864\n"
    "v_cmp_le_u32 s[56:57], s59, v245\n"
    "v_mfma_f32_16x16x32_bf16 v[124:127], a[196:199], v[72:75], v[124:127]\n"
    "v_exp_f32 v62, v62\n"
    "v_mfma_f32_16x16x32_bf16 v[148:151], a[240:243], v[212:215], v[148:151]\n"
    "s_cmp_eq_u64 s[56:57], -1\n"
    "v_exp_f32 v40, v40\n"
    "v_mfma_f32_16x16x32_bf16 v[132:135], a[208:211], v[68:71], v[132:135]\n"
    "s_waitcnt lgkmcnt(4)\n"
    "v_exp_f32 v42, v42\n"
    "v_smfmac_f32_16x16x64_bf16 v[108:111], v[240:243], v[4:11], v253\n"
    "s_cbranch_scc0 .Lpt3\n"
    ".Lpt3r:\n"
    "v_mfma_f32_16x16x32_bf16 v[48:51], a[112:115], a[168:171], v[48:51]\n"
    "s_waitcnt lgkmcnt(1)\n"
    "ds_read_b128 a[176:179], v249 offset:2048\n"
    "ds_read_b128 a[168:171], v249 offset:1024\n"
    "v_mfma_f32_16x16x32_bf16 v[124:127], a[224:227], v[204:207], v[124:127]\n"
    "v_exp_f32 v18, v18\n"
    "v_mfma_f32_16x16x32_bf16 v[136:139], a[220:223], v[72:75], v[136:139]\n"
    "v_cvt_pk_bf16_f32 v16, v40, v41\n"
    "v_cvt_pk_bf16_f32 v15, v18, v19\n"
    "v_mfma_f32_16x16x32_bf16 v[152:155], a[220:223], v[80:83], v[152:155]\n"
    "s_waitcnt lgkmcnt(2)\n"
    "v_exp_f32 v61, v61\n"
    "v_mfma_f32_16x16x32_bf16 v[124:127], a[228:231], v[208:211], v[124:127]\n"
    "v_exp_f32 v63, v63\n"
    "v_mov_b32 v111, v110\n"
    "v_mfma_f32_16x16x32_bf16 v[152:155], a[216:219], v[76:79], v[152:155]\n"
    "v_add_f32 v108, v108, v109\n"
    "v_cvt_pk_bf16_f32 v22, v24, v25\n"
    "v_mfma_f32_16x16x32_bf16 v[132:135], a[212:215], v[72:75], v[132:135]\n"
    "ds_read_b128 a[188:191], v249 offset:15360\n"
    "ds_read_b128 a[248:251], v249 offset:27648\n"
    "v_mfma_f32_16x16x32_bf16 v[136:139], a[216:219], v[68:71], v[136:139]\n"
    "ds_read_b128 a[172:175], v249 offset:13312\n"
    "ds_read_b128 a[180:183], v249 offset:14336\n"
    "v_mfma_f32_16x16x32_bf16 v[144:147], a[200:203], v[76:79], v[144:147]\n"
    "v_exp_f32 v35, v35\n"
    "v_mfma_f32_16x16x32_bf16 v[132:135], a[240:243], v[204:207], v[132:135]\n"
    "ds_read_b128 a[160:163], v249 offset:0\n"
    "v_mov_b32 v109, v108\n"
    "v_mfma_f32_16x16x32_bf16 v[136:139], a[252:255], v[208:211], v[136:139]\n"
    "ds_read_b128 a[164:167], v249 offset:12288\n"
    "ds_read_b128 a[184:187], v249 offset:3072\n"
    "v_mfma_f32_16x16x32_bf16 v[132:135], a[244:247], v[208:211], v[132:135]\n"
    "s_waitcnt lgkmcnt(8)\n"
    "v_exp_f32 v46, v46\n"
    "v_mfma_f32_16x16x32_bf16 v[132:135], a[176:179], v[4:7], v[132:135]\n"
    "v_exp_f32 v59, v59\n"
    "v_mfma_f32_16x16x32_bf16 v[144:147], a[236:239], v[216:219], v[144:147]\n"
    "v_exp_f32 v67, v67\n"
    "v_mfma_f32_16x16x32_bf16 v[144:147], a[232:235], v[212:215], v[144:147]\n"
    "v_permlane16_swap_b32 v108, v109\n"
    "v_mfma_f32_16x16x32_bf16 v[128:131], a[232:235], v[204:207], v[128:131]\n"
    "s_waitcnt lgkmcnt(3)\n"
    "v_exp_f32 v44, v44\n"
    "v_mfma_f32_16x16x32_bf16 v[132:135], a[180:183], v[8:11], v[132:135]\n"
    "v_cvt_pk_bf16_f32 v23, v26, v27\n"
    "v_cvt_pk_bf16_f32 v27, v62, v63\n"
    "v_mfma_f32_16x16x32_bf16 v[136:139], a[248:251], v[204:207], v[136:139]\n"
    "s_waitcnt lgkmcnt(2)\n"
    "v_exp_f32 v57, v57\n"
    "v_mfma_f32_16x16x32_bf16 v[124:127], a[160:163], v[4:7], v[124:127]\n"
    "v_exp_f32 v56, v56\n"
    "v_mfma_f32_16x16x32_bf16 v[140:143], a[224:227], v[212:215], v[140:143]\n"
    "s_waitcnt lgkmcnt(1)\n"
    "v_and_b32 v252, 0x70, v252\n"
    "v_add_f32 v108, v108, v109\n"
    "v_mfma_f32_16x16x32_bf16 v[124:127], a[164:167], v[8:11], v[124:127]\n"
    "v_permlane16_swap_b32 v110, v111\n"
    "v_mfma_f32_16x16x32_bf16 v[128:131], a[172:175], v[8:11], v[128:131]\n"
    "v_add_f32 v110, v110, v111\n"
    "v_cvt_pk_bf16_f32 v18, v56, v57\n"
    "v_mfma_f32_16x16x32_bf16 v[128:131], a[168:171], v[4:7], v[128:131]\n"
    ".Lwp1:\n"
    "s_cbranch_execz .Lwp2\n"
    "v_rcp_f32 v109, v108\n"
    "v_mfma_f32_16x16x32_bf16 v[136:139], a[188:191], v[8:11], v[136:139]\n"
    "s_waitcnt lgkmcnt(0)\n"
    "v_exp_f32 v60, v60\n"
    "v_mfma_f32_16x16x32_bf16 v[136:139], a[184:187], v[4:7], v[136:139]\n"
    "v_mul_f32 v111, -v110, v109\n"
    "v_cvt_pk_bf16_f32 v19, v58, v59\n"
    "v_cvt_pk_bf16_f32 v17, v42, v43\n"
    "v_xor_b32 v252, v252, v1\n"
    "v_ldexp_f32 v109, v109, s79\n"
    "v_pk_fma_f32 v[134:135], v[134:135], v[108:109], v[110:111] op_sel:[0,1,1] op_sel_hi:[1,1,1]\n"
    "v_pk_fma_f32 v[124:125], v[124:125], v[108:109], v[110:111] op_sel:[0,1,1] op_sel_hi:[1,1,1]\n"
    "v_pk_fma_f32 v[126:127], v[126:127], v[108:109], v[110:111] op_sel:[0,1,1] op_sel_hi:[1,1,1]\n"
    "v_pk_fma_f32 v[128:129], v[128:129], v[108:109], v[110:111] op_sel:[0,1,1] op_sel_hi:[1,1,1]\n"
    "v_pk_fma_f32 v[132:133], v[132:133], v[108:109], v[110:111] op_sel:[0,1,1] op_sel_hi:[1,1,1]\n"
    "v_pk_fma_f32 v[130:131], v[130:131], v[108:109], v[110:111] op_sel:[0,1,1] op_sel_hi:[1,1,1]\n"
    "v_pk_fma_f32 v[138:139], v[138:139], v[108:109], v[110:111] op_sel:[0,1,1] op_sel_hi:[1,1,1]\n"
    "v_pk_fma_f32 v[136:137], v[136:137], v[108:109], v[110:111] op_sel:[0,1,1] op_sel_hi:[1,1,1]\n"
    "v_cvt_pk_bf16_f32 v124, v124, v128\n"
    "v_cvt_pk_bf16_f32 v128, v126, v130\n"
    "v_mfma_f32_16x16x32_bf16 v[152:155], a[252:255], v[216:219], v[152:155]\n"
    "v_cvt_pk_bf16_f32 v130, v127, v131\n"
    "v_cvt_pk_bf16_f32 v126, v125, v129\n"
    "v_cvt_pk_bf16_f32 v127, v133, v137\n"
    "v_mfma_f32_16x16x32_bf16 v[148:151], a[244:247], v[216:219], v[148:151]\n"
    "v_add_u32 v252, s72, v252\n"
    "v_cvt_pk_bf16_f32 v125, v132, v136\n"
    "v_mfma_f32_16x16x32_bf16 v[140:143], a[228:231], v[216:219], v[140:143]\n"
    "ds_write_b128 v3, v[124:127] offset:0\n"
    "v_smfmac_f32_16x16x64_bf16 v[112:115], v[240:243], v[12:19], v253\n"
    "v_cvt_pk_bf16_f32 v129, v134, v138\n"
    "v_cvt_pk_bf16_f32 v26, v60, v61\n"
    "v_mfma_f32_16x16x32_bf16 v[148:151], a[176:179], v[12:15], v[148:151]\n"
    "v_exp_f32 v51, v51\n"
    "v_mfma_f32_16x16x32_bf16 v[140:143], a[160:163], v[12:15], v[140:143]\n"
    "v_cvt_pk_bf16_f32 v131, v135, v139\n"
    "v_xor_b32 v2, 64, v3\n"
    "v_mfma_f32_16x16x32_bf16 v[156:159], a[192:195], v[84:87], v[156:159]\n"
    "ds_write_b128 v2, v[128:131] offset:0\n"
    "v_mfma_f32_16x16x32_bf16 v[156:159], a[196:199], v[88:91], v[156:159]\n"
    "v_exp_f32 v47, v47\n"
    "v_mfma_f32_16x16x32_bf16 v[152:155], a[248:251], v[212:215], v[152:155]\n"
    "v_cvt_pk_bf16_f32 v24, v44, v45\n"
    "v_cvt_pk_bf16_f32 v25, v46, v47\n"
    "v_mfma_f32_16x16x32_bf16 v[156:159], a[224:227], v[220:223], v[156:159]\n"
    "ds_read_b128 v[132:135], v252 offset:0\n"
    "v_add_f32 v112, v112, v113\n"
    "v_mfma_f32_16x16x32_bf16 v[140:143], a[164:167], v[16:19], v[140:143]\n"
    "v_exp_f32 v34, v34\n"
    "v_mfma_f32_16x16x32_bf16 v[144:147], a[172:175], v[16:19], v[144:147]\n"
    "v_mov_b32 v113, v112\n"
    "v_mov_b32 v115, v114\n"
    "v_mfma_f32_16x16x32_bf16 v[144:147], a[168:171], v[12:15], v[144:147]\n"
    "ds_read_b128 v[136:139], v252 offset:1024\n"
    "v_permlane16_swap_b32 v112, v113\n"
    "v_mfma_f32_16x16x32_bf16 v[164:167], a[208:211], v[84:87], v[164:167]\n"
    "v_permlane16_swap_b32 v114, v115\n"
    "v_mfma_f32_16x16x32_bf16 v[152:155], a[188:191], v[16:19], v[152:155]\n"
    "v_exp_f32_e64 v48, v48\n"
    "v_mfma_f32_16x16x32_bf16 v[148:151], a[180:183], v[16:19], v[148:151]\n"
    "v_add_f32 v112, v112, v113\n"
    "v_cvt_pk_bf16_f32 v28, v28, v29\n"
    "v_mfma_f32_16x16x32_bf16 v[152:155], a[184:187], v[12:15], v[152:155]\n"
    "v_rcp_f32 v113, v112\n"
    "v_smfmac_f32_16x16x64_bf16 v[116:119], v[240:243], v[20:27], v253\n"
    "s_waitcnt lgkmcnt(1)\n"
    "buffer_store_dwordx4 v[132:135], v244, s[24:27], s75 offen offset:0 sc1\n"
    "v_add_f32 v114, v114, v115\n"
    "v_mul_f32 v115, -v114, v113\n"
    "v_ldexp_f32 v113, v113, s79\n"
    "v_pk_fma_f32 v[142:143], v[142:143], v[112:113], v[114:115] op_sel:[0,1,1] op_sel_hi:[1,1,1]\n"
    "v_pk_fma_f32 v[144:145], v[144:145], v[112:113], v[114:115] op_sel:[0,1,1] op_sel_hi:[1,1,1]\n"
    "v_pk_fma_f32 v[140:141], v[140:141], v[112:113], v[114:115] op_sel:[0,1,1] op_sel_hi:[1,1,1]\n"
    "v_pk_fma_f32 v[146:147], v[146:147], v[112:113], v[114:115] op_sel:[0,1,1] op_sel_hi:[1,1,1]\n"
    "v_pk_fma_f32 v[150:151], v[150:151], v[112:113], v[114:115] op_sel:[0,1,1] op_sel_hi:[1,1,1]\n"
    "v_add_f32_e64 v116, v116, v117\n"
    "v_pk_fma_f32 v[152:153], v[152:153], v[112:113], v[114:115] op_sel:[0,1,1] op_sel_hi:[1,1,1]\n"
    "v_pk_fma_f32 v[148:149], v[148:149], v[112:113], v[114:115] op_sel:[0,1,1] op_sel_hi:[1,1,1]\n"
    "v_pk_fma_f32 v[154:155], v[154:155], v[112:113], v[114:115] op_sel:[0,1,1] op_sel_hi:[1,1,1]\n"
    "v_cvt_pk_bf16_f32 v140, v140, v144\n"
    "v_cvt_pk_bf16_f32 v144, v142, v146\n"
    "v_mfma_f32_16x16x32_bf16 v[160:163], a[204:207], v[88:91], v[160:163]\n"
    "v_cvt_pk_bf16_f32 v146, v143, v147\n"
    "v_cvt_pk_bf16_f32 v142, v141, v145\n"
    "v_mfma_f32_16x16x32_bf16 v[156:159], a[228:231], v[224:227], v[156:159]\n"
    "v_cvt_pk_bf16_f32 v141, v148, v152\n"
    "v_cvt_pk_bf16_f32 v143, v149, v153\n"
    "v_mfma_f32_16x16x32_bf16 v[156:159], a[160:163], v[20:23], v[156:159]\n"
    "s_waitcnt lgkmcnt(0)\n"
    "buffer_store_dwordx4 v[136:139], v244, s[24:27], s75 offen offset:3072 sc1\n"
    "ds_write_b128 v3, v[140:143] offset:2048\n"
    "v_mfma_f32_16x16x32_bf16 v[172:175], a[192:195], v[92:95], v[172:175]\n"
    "v_mov_b32 v119, v118\n"
    "v_cvt_pk_bf16_f32 v145, v150, v154\n"
    "v_mfma_f32_16x16x32_bf16 v[156:159], a[164:167], v[24:27], v[156:159]\n"
    "v_cvt_pk_bf16_f32 v147, v151, v155\n"
    "v_mov_b32 v117, v116\n"
    "v_mfma_f32_16x16x32_bf16 v[180:183], a[208:211], v[92:95], v[180:183]\n"
    "ds_write_b128 v2, v[144:147] offset:2048\n"
    "v_mfma_f32_16x16x32_bf16 v[160:163], a[200:203], v[84:87], v[160:163]\n"
    "v_permlane16_swap_b32 v118, v119\n"
    "v_mfma_f32_16x16x32_bf16 v[168:171], a[220:223], v[88:91], v[168:171]\n"
    "v_exp_f32 v33, v33\n"
    "v_mfma_f32_16x16x32_bf16 v[172:175], a[196:199], v[96:99], v[172:175]\n"
    "ds_read_b128 v[148:151], v252 offset:2048\n"
    "ds_read_b128 v[152:155], v252 offset:3072\n"
    "v_mfma_f32_16x16x32_bf16 v[180:183], a[212:215], v[96:99], v[180:183]\n"
    "v_exp_f32 v49, v49\n"
    "v_mfma_f32_16x16x32_bf16 v[160:163], a[236:239], v[224:227], v[160:163]\n"
    "v_permlane16_swap_b32 v116, v117\n"
    "v_mfma_f32_16x16x32_bf16 v[168:171], a[216:219], v[84:87], v[168:171]\n"
    "v_exp_f32 v31, v31\n"
    "v_mfma_f32_16x16x32_bf16 v[176:179], a[204:207], v[96:99], v[176:179]\n"
    "v_exp_f32 v66, v66\n"
    "v_mfma_f32_16x16x32_bf16 v[164:167], a[212:215], v[88:91], v[164:167]\n"
    "ds_read_b128 a[204:207], v248 offset:17408\n"
    "v_add_f32 v118, v118, v119\n"
    "v_mfma_f32_16x16x32_bf16 v[160:163], a[232:235], v[220:223], v[160:163]\n"
    "v_cvt_pk_bf16_f32 v29, v30, v31\n"
    "v_add_f32 v116, v116, v117\n"
    "v_mfma_f32_16x16x32_bf16 v[168:171], a[252:255], v[224:227], v[168:171]\n"
    "v_rcp_f32 v117, v116\n"
    "v_mfma_f32_16x16x32_bf16 v[160:163], a[172:175], v[24:27], v[160:163]\n"
    "s_waitcnt lgkmcnt(1)\n"
    "buffer_store_dwordx4 v[152:155], v244, s[24:27], s76 offen offset:3072 sc1\n"
    "v_exp_f32 v50, v50\n"
    "v_exp_f32 v64, v64\n"
    "v_mfma_f32_16x16x32_bf16 v[176:179], a[200:203], v[92:95], v[176:179]\n"
    "ds_read_b128 a[196:199], v248 offset:16384\n"
    "v_cvt_pk_bf16_f32 v31, v34, v35\n"
    "v_mfma_f32_16x16x32_bf16 v[160:163], a[168:171], v[20:23], v[160:163]\n"
    "v_mul_f32 v119, -v118, v117\n"
    "v_cvt_pk_bf16_f32 v30, v32, v33\n"
    "v_mfma_f32_16x16x32_bf16 v[164:167], a[240:243], v[220:223], v[164:167]\n"
    "buffer_store_dwordx4 v[148:151], v244, s[24:27], s76 offen offset:0 sc1\n"
    "v_cvt_pk_bf16_f32 v35, v66, v67\n"
    "v_ldexp_f32 v117, v117, s79\n"
    "v_exp_f32 v65, v65\n"
    "v_mfma_f32_16x16x32_bf16 v[184:187], a[220:223], v[96:99], v[184:187]\n"
    "v_fma_f32 v156, v156, v117, v119\n"
    "v_fma_f32 v157, v157, v117, v119\n"
    "v_mfma_f32_16x16x32_bf16 v[168:171], a[248:251], v[220:223], v[168:171]\n"
    "v_fma_f32 v158, v158, v117, v119\n"
    "v_fma_f32 v159, v159, v117, v119\n"
    "v_mfma_f32_16x16x32_bf16 v[164:167], a[244:247], v[224:227], v[164:167]\n"
    "v_fma_f32 v160, v160, v117, v119\n"
    "v_fma_f32 v161, v161, v117, v119\n"
    "v_mfma_f32_16x16x32_bf16 v[168:171], a[188:191], v[24:27], v[168:171]\n"
    "v_fma_f32 v162, v162, v117, v119\n"
    "v_fma_f32 v163, v163, v117, v119\n"
    "v_mfma_f32_16x16x32_bf16 v[172:175], a[224:227], v[228:231], v[172:175]\n"
    "ds_read_b128 a[224:227], v249 offset:28672\n"
    "v_cvt_pk_bf16_f32 v34, v64, v65\n"
    "v_mfma_f32_16x16x32_bf16 v[168:171], a[184:187], v[20:23], v[168:171]\n"
    "v_cvt_pk_bf16_f32 v156, v156, v160\n"
    "v_cvt_pk_bf16_f32 v160, v158, v162\n"
    "v_mfma_f32_16x16x32_bf16 v[164:167], a[176:179], v[20:23], v[164:167]\n"
    "v_cvt_pk_bf16_f32 v162, v159, v163\n"
    "v_cvt_pk_bf16_f32 v32, v48, v49\n"
    "v_mfma_f32_16x16x32_bf16 v[164:167], a[180:183], v[24:27], v[164:167]\n"
    "ds_read_b128 a[192:195], v248 offset:4096\n"
    "v_cvt_pk_bf16_f32 v158, v157, v161\n"
    "v_mfma_f32_16x16x32_bf16 v[180:183], a[240:243], v[228:231], v[180:183]\n"
    "v_cvt_pk_bf16_f32 v33, v50, v51\n"
    "v_fma_f32 v168, v168, v117, v119\n"
    "v_fma_f32 v169, v169, v117, v119\n"
    "v_mfma_f32_16x16x32_bf16 v[184:187], a[216:219], v[92:95], v[184:187]\n"
    "v_fma_f32 v170, v170, v117, v119\n"
    "v_fma_f32 v171, v171, v117, v119\n"
    "v_smfmac_f32_16x16x64_bf16 v[120:123], v[240:243], v[28:35], v253\n"
    "ds_read_b128 a[220:223], v248 offset:19456\n"
    "v_mfma_f32_16x16x32_bf16 v[184:187], a[252:255], v[232:235], v[184:187]\n"
    "v_fma_f32 v164, v164, v117, v119\n"
    "v_fma_f32 v165, v165, v117, v119\n"
    "v_mfma_f32_16x16x32_bf16 v[172:175], a[228:231], v[232:235], v[172:175]\n"
    "v_cvt_pk_bf16_f32 v157, v164, v168\n"
    "v_cvt_pk_bf16_f32 v159, v165, v169\n"
    "v_mfma_f32_16x16x32_bf16 v[176:179], a[236:239], v[232:235], v[176:179]\n"
    "ds_write_b128 v3, v[156:159] offset:4096\n"
    "v_mfma_f32_16x16x32_bf16 v[180:183], a[244:247], v[232:235], v[180:183]\n"
    "s_waitcnt lgkmcnt(2)\n"
    "v_fma_f32 v166, v166, v117, v119\n"
    "v_fma_f32 v167, v167, v117, v119\n"
    "v_mfma_f32_16x16x32_bf16 v[188:191], a[192:195], v[68:71], v[188:191]\n"
    "v_cvt_pk_bf16_f32 v163, v167, v171\n"
    "v_cvt_pk_bf16_f32 v161, v166, v170\n"
    "v_mfma_f32_16x16x32_bf16 v[184:187], a[248:251], v[228:231], v[184:187]\n"
    "ds_write_b128 v2, v[160:163] offset:4096\n"
    "v_mfma_f32_16x16x32_bf16 v[192:195], a[204:207], v[72:75], v[192:195]\n"
    "v_add_f32_e64 v120, v120, v121\n"
    "v_mov_b32_e64 v121, v120\n"
    "v_mfma_f32_16x16x32_bf16 v[188:191], a[196:199], v[72:75], v[188:191]\n"
    "s_nop 0\n"
    "v_permlane16_swap_b32 v120, v121\n"
    "v_mfma_f32_16x16x32_bf16 v[176:179], a[232:235], v[228:231], v[176:179]\n"
    "s_waitcnt lgkmcnt(2)\n"
    "ds_read_b128 v[164:167], v252 offset:4096\n"
    "ds_read_b128 a[228:231], v249 offset:40960\n"
    "v_mfma_f32_16x16x32_bf16 v[200:203], a[220:223], v[72:75], v[200:203]\n"
    "ds_read_b128 a[212:215], v248 offset:18432\n"
    "ds_read_b128 v[168:171], v252 offset:5120\n"
    "v_mfma_f32_16x16x32_bf16 v[180:183], a[176:179], v[28:31], v[180:183]\n"
    "ds_read_b128 a[132:135], v249 offset:18432\n"
    "v_mov_b32 v123, v122\n"
    "v_mfma_f32_16x16x32_bf16 v[176:179], a[172:175], v[32:35], v[176:179]\n"
    "ds_read_b128 a[208:211], v248 offset:6144\n"
    "ds_read_b128 a[216:219], v248 offset:7168\n"
    "v_mfma_f32_16x16x32_bf16 v[188:191], a[224:227], v[204:207], v[188:191]\n"
    "ds_read_b128 a[248:251], v249 offset:31744\n"
    "ds_read_b128 a[236:239], v249 offset:41984\n"
    "v_mfma_f32_16x16x32_bf16 v[184:187], a[188:191], v[32:35], v[184:187]\n"
    "v_permlane16_swap_b32_e64 v122, v123\n"
    "v_mfma_f32_16x16x32_bf16 v[172:175], a[160:163], v[28:31], v[172:175]\n"
    "ds_read_b128 a[140:143], v249 offset:19456\n"
    "ds_read_b128 a[200:203], v248 offset:5120\n"
    "v_mfma_f32_16x16x32_bf16 v[180:183], a[180:183], v[32:35], v[180:183]\n"
    "ds_read_b128 a[136:139], v249 offset:7168\n"
    "ds_read_b128 a[124:127], v249 offset:17408\n"
    "v_mfma_f32_16x16x32_bf16 v[184:187], a[184:187], v[28:31], v[184:187]\n"
    "s_waitcnt lgkmcnt(12)\n"
    "buffer_store_dwordx4 v[164:167], v244, s[24:27], s77 offen offset:0 sc1\n"
    "ds_read_b128 a[240:243], v249 offset:30720\n"
    "ds_read_b128 a[252:255], v249 offset:44032\n"
    "v_add_f32 v122, v122, v123\n"
    "v_add_f32 v120, v120, v121\n"
    "v_mfma_f32_16x16x32_bf16 v[176:179], a[168:171], v[28:31], v[176:179]\n"
    "ds_read_b128 a[112:115], v249 offset:4096\n"
    "ds_read_b128 a[128:131], v249 offset:6144\n"
    "v_mfma_f32_16x16x32_bf16 v[172:175], a[164:167], v[32:35], v[172:175]\n"
    "s_waitcnt lgkmcnt(11)\n"
    "v_rcp_f32 v121, v120\n"
    "v_mfma_f32_16x16x32_bf16 v[196:199], a[208:211], v[68:71], v[196:199]\n"
    "ds_read_b128 a[244:247], v249 offset:43008\n"
    "v_mul_f32 v123, -v122, v121\n"
    "v_mfma_f32_16x16x32_bf16 v[188:191], a[228:231], v[208:211], v[188:191]\n"
    "buffer_store_dwordx4 v[168:171], v244, s[24:27], s77 offen offset:3072 sc1\n"
    "ds_read_b128 a[232:235], v249 offset:29696\n"
    "ds_read_b128 a[116:119], v249 offset:16384\n"
    "ds_read_b128 a[120:123], v249 offset:5120\n"
    "v_ldexp_f32 v121, v121, s79\n"
    "v_mfma_f32_16x16x32_bf16 v[196:199], a[212:215], v[72:75], v[196:199]\n"
    "s_waitcnt lgkmcnt(10)\n"
    "v_fma_f32 v174, v174, v121, v123\n"
    "v_fma_f32 v175, v175, v121, v123\n"
    "v_mfma_f32_16x16x32_bf16 v[192:195], a[200:203], v[68:71], v[192:195]\n"
    "v_fma_f32 v176, v176, v121, v123\n"
    "v_fma_f32 v177, v177, v121, v123\n"
    "v_mfma_f32_16x16x32_bf16 v[200:203], a[216:219], v[68:71], v[200:203]\n"
    "s_waitcnt lgkmcnt(7)\n"
    "v_fma_f32 v186, v186, v121, v123\n"
    "v_fma_f32 v187, v187, v121, v123\n"
    "v_mfma_f32_16x16x32_bf16 v[196:199], a[240:243], v[204:207], v[196:199]\n"
    "s_waitcnt lgkmcnt(6)\n"
    "v_fma_f32 v182, v182, v121, v123\n"
    "v_fma_f32 v183, v183, v121, v123\n"
    "v_mfma_f32_16x16x32_bf16 v[200:203], a[252:255], v[208:211], v[200:203]\n"
    "v_fma_f32 v178, v178, v121, v123\n"
    "v_fma_f32 v179, v179, v121, v123\n"
    "v_mfma_f32_16x16x32_bf16 v[200:203], a[248:251], v[204:207], v[200:203]\n"
    "s_waitcnt lgkmcnt(3)\n"
    "v_fma_f32 v172, v172, v121, v123\n"
    "v_fma_f32 v173, v173, v121, v123\n"
    "v_mfma_f32_16x16x32_bf16 v[196:199], a[244:247], v[208:211], v[196:199]\n"
    "v_cvt_pk_bf16_f32 v172, v172, v176\n"
    "v_cvt_pk_bf16_f32 v176, v174, v178\n"
    "v_mfma_f32_16x16x32_bf16 v[192:195], a[236:239], v[208:211], v[192:195]\n"
    "s_waitcnt lgkmcnt(2)\n"
    "v_cvt_pk_bf16_f32 v174, v173, v177\n"
    "v_cvt_pk_bf16_f32 v178, v175, v179\n"
    "v_mfma_f32_16x16x32_bf16 v[192:195], a[232:235], v[204:207], v[192:195]\n"
    "v_cvt_pk_bf16_f32 v177, v182, v186\n"
    "v_cvt_pk_bf16_f32 v179, v183, v187\n"
    "v_mfma_f32_16x16x32_bf16 v[196:199], a[128:131], v[4:7], v[196:199]\n"
    "v_fma_f32 v180, v180, v121, v123\n"
    "v_fma_f32 v181, v181, v121, v123\n"
    "v_mfma_f32_16x16x32_bf16 v[188:191], a[112:115], v[4:7], v[188:191]\n"
    "s_waitcnt lgkmcnt(1)\n"
    "ds_write_b128 v2, v[176:179] offset:0\n"
    "v_mfma_f32_16x16x32_bf16 v[188:191], a[116:119], v[8:11], v[188:191]\n"
    "v_fma_f32 v184, v184, v121, v123\n"
    "v_fma_f32 v185, v185, v121, v123\n"
    "v_mfma_f32_16x16x32_bf16 v[192:195], a[124:127], v[8:11], v[192:195]\n"
    "v_cvt_pk_bf16_f32 v175, v181, v185\n"
    "v_cvt_pk_bf16_f32 v173, v180, v184\n"
    "v_mfma_f32_16x16x32_bf16 v[196:199], a[132:135], v[8:11], v[196:199]\n"
    "ds_write_b128 v3, v[172:175] offset:0\n"
    "v_mfma_f32_16x16x32_bf16 a[4:7], a[204:207], v[80:83], a[4:7]\n"
    "v_fma_f32 v188, v188, v109, v111\n"
    "v_fma_f32 v189, v189, v109, v111\n"
    "v_mfma_f32_16x16x32_bf16 a[0:3], a[192:195], v[76:79], a[0:3]\n"
    "s_waitcnt lgkmcnt(2)\n"
    "v_fma_f32 v190, v190, v109, v111\n"
    "v_fma_f32 v191, v191, v109, v111\n"
    "v_mfma_f32_16x16x32_bf16 v[192:195], a[120:123], v[4:7], v[192:195]\n"
    "ds_read_b128 v[180:183], v252 offset:0\n"
    "ds_read_b128 v[184:187], v252 offset:1024\n"
    "v_mfma_f32_16x16x32_bf16 v[200:203], a[140:143], v[8:11], v[200:203]\n"
    "v_fma_f32 v198, v198, v109, v111\n"
    "v_fma_f32 v199, v199, v109, v111\n"
    "v_mfma_f32_16x16x32_bf16 a[0:3], a[196:199], v[80:83], a[0:3]\n"
    "v_fma_f32 v196, v196, v109, v111\n"
    "v_fma_f32 v197, v197, v109, v111\n"
    "v_mfma_f32_16x16x32_bf16 a[12:15], a[220:223], v[80:83], a[12:15]\n"
    "v_fma_f32 v192, v192, v109, v111\n"
    "v_fma_f32 v193, v193, v109, v111\n"
    "v_mfma_f32_16x16x32_bf16 a[4:7], a[200:203], v[76:79], a[4:7]\n"
    "ds_read_b128 a[164:167], v249 offset:20480\n"
    "ds_read_b128 a[180:183], v249 offset:22528\n"
    "v_mfma_f32_16x16x32_bf16 v[200:203], a[136:139], v[4:7], v[200:203]\n"
    "v_fma_f32 v194, v194, v109, v111\n"
    "v_fma_f32 v195, v195, v109, v111\n"
    "v_mfma_f32_16x16x32_bf16 a[0:3], a[224:227], v[212:215], a[0:3]\n"
    "ds_read_b128 a[172:175], v249 offset:21504\n"
    "v_mfma_f32_16x16x32_bf16 a[4:7], a[236:239], v[216:219], a[4:7]\n"
    "v_cvt_pk_bf16_f32 v188, v188, v192\n"
    "v_cvt_pk_bf16_f32 v192, v190, v194\n"
    "v_mfma_f32_16x16x32_bf16 a[0:3], a[228:231], v[216:219], a[0:3]\n"
    "s_waitcnt lgkmcnt(4)\n"
    "buffer_store_dwordx4 v[180:183], v244, s[24:27], s78 offen offset:0 sc1\n"
    "v_cvt_pk_bf16_f32 v190, v189, v193\n"
    "v_cvt_pk_bf16_f32 v194, v191, v195\n"
    "v_fma_f32 v200, v200, v109, v111\n"
    "v_fma_f32 v201, v201, v109, v111\n"
    "v_mfma_f32_16x16x32_bf16 a[0:3], a[112:115], v[12:15], a[0:3]\n"
    "v_cvt_pk_bf16_f32 v189, v196, v200\n"
    "v_cvt_pk_bf16_f32 v191, v197, v201\n"
    "v_mfma_f32_16x16x32_bf16 a[0:3], a[116:119], v[16:19], a[0:3]\n"
    "ds_read_b128 a[176:179], v249 offset:10240\n"
    "v_mfma_f32_16x16x32_bf16 a[4:7], a[232:235], v[212:215], a[4:7]\n"
    "s_waitcnt lgkmcnt(4)\n"
    "buffer_store_dwordx4 v[184:187], v244, s[24:27], s78 offen offset:3072 sc1\n"
    "ds_write_b128 v3, v[188:191] offset:2048\n"
    "v_mfma_f32_16x16x32_bf16 a[8:11], a[208:211], v[76:79], a[8:11]\n"
    "v_fma_f32 v202, v202, v109, v111\n"
    "v_fma_f32 v203, v203, v109, v111\n"
    "v_mfma_f32_16x16x32_bf16 a[12:15], a[216:219], v[76:79], a[12:15]\n"
    "v_cvt_pk_bf16_f32 v195, v199, v203\n"
    "v_cvt_pk_bf16_f32 v193, v198, v202\n"
    "v_mfma_f32_16x16x32_bf16 a[12:15], a[252:255], v[216:219], a[12:15]\n"
    "ds_write_b128 v2, v[192:195] offset:2048\n"
    "v_mfma_f32_16x16x32_bf16 a[4:7], a[124:127], v[16:19], a[4:7]\n"
    "v_accvgpr_read_b32 v40, a1\n"
    "v_mfma_f32_16x16x32_bf16 a[20:23], a[204:207], v[88:91], a[20:23]\n"
    "v_accvgpr_read_b32 v56, a3\n"
    "v_mov_b32_e64 v250, 0\n"
    "v_mfma_f32_16x16x32_bf16 a[4:7], a[120:123], v[12:15], a[4:7]\n"
    "ds_read_b128 v[200:203], v252 offset:3072\n"
    "ds_read_b128 v[196:199], v252 offset:2048\n"
    "v_mfma_f32_16x16x32_bf16 a[8:11], a[212:215], v[80:83], a[8:11]\n"
    "ds_read_b128 a[188:191], v249 offset:23552\n"
    "v_mfma_f32_16x16x32_bf16 a[8:11], a[240:243], v[212:215], a[8:11]\n"
    "v_accvgpr_read_b32 v36, a0\n"
    "v_accvgpr_read_b32 v52, a2\n"
    "v_mfma_f32_16x16x32_bf16 a[8:11], a[244:247], v[216:219], a[8:11]\n"
    "v_accvgpr_read_b32 v41, a5\n"
    "v_accvgpr_read_b32 v57, a7\n"
    "v_mfma_f32_16x16x32_bf16 a[8:11], a[128:131], v[12:15], a[8:11]\n"
    "v_accvgpr_read_b32 v37, a4\n"
    "v_accvgpr_read_b32 v53, a6\n"
    "v_mfma_f32_16x16x32_bf16 a[8:11], a[132:135], v[16:19], a[8:11]\n"
    "v_fma_f32 v40, v40, v113, v115\n"
    "v_fma_f32 v41, v41, v113, v115\n"
    "v_mfma_f32_16x16x32_bf16 a[12:15], a[248:251], v[212:215], a[12:15]\n"
    "ds_read_b128 a[160:163], v249 offset:8192\n"
    "v_mfma_f32_16x16x32_bf16 a[12:15], a[140:143], v[16:19], a[12:15]\n"
    "v_fma_f32 v52, v52, v113, v115\n"
    "v_fma_f32 v53, v53, v113, v115\n"
    "v_mfma_f32_16x16x32_bf16 a[12:15], a[136:139], v[12:15], a[12:15]\n"
    "s_waitcnt lgkmcnt(3)\n"
    "buffer_store_dwordx4 v[200:203], v244, s[24:27], s75 offen offset:3200 sc1\n"
    "v_cvt_pk_bf16_f32 v52, v52, v53\n"
    "v_fma_f32 v36, v36, v113, v115\n"
    "v_fma_f32 v37, v37, v113, v115\n"
    "v_accvgpr_read_b32 v38, a8\n"
    "v_mfma_f32_16x16x32_bf16 a[16:19], a[192:195], v[84:87], a[16:19]\n"
    "ds_read_b128 a[168:171], v249 offset:9216\n"
    "v_accvgpr_read_b32 v42, a9\n"
    "v_mfma_f32_16x16x32_bf16 a[16:19], a[196:199], v[88:91], a[16:19]\n"
    "v_accvgpr_read_b32 v54, a10\n"
    "v_accvgpr_read_b32 v39, a12\n"
    "v_mfma_f32_16x16x32_bf16 a[20:23], a[200:203], v[84:87], a[20:23]\n"
    "v_accvgpr_read_b32 v55, a14\n"
    "v_accvgpr_read_b32 v58, a11\n"
    "v_mfma_f32_16x16x32_bf16 a[16:19], a[224:227], v[220:223], a[16:19]\n"
    "v_accvgpr_read_b32 v59, a15\n"
    "v_cvt_pk_bf16_f32 v36, v36, v37\n"
    "v_mfma_f32_16x16x32_bf16 a[20:23], a[236:239], v[224:227], a[20:23]\n"
    "v_fma_f32 v38, v38, v113, v115\n"
    "v_fma_f32 v39, v39, v113, v115\n"
    "v_mfma_f32_16x16x32_bf16 a[24:27], a[208:211], v[84:87], a[24:27]\n"
    "v_fma_f32 v54, v54, v113, v115\n"
    "v_fma_f32 v55, v55, v113, v115\n"
    "v_mfma_f32_16x16x32_bf16 a[20:23], a[232:235], v[220:223], a[20:23]\n"
    "v_accvgpr_read_b32 v43, a13\n"
    "v_cvt_pk_bf16_f32 v53, v54, v55\n"
    "v_mfma_f32_16x16x32_bf16 a[20:23], a[124:127], v[24:27], a[20:23]\n"
    "v_fma_f32 v42, v42, v113, v115\n"
    "v_fma_f32 v43, v43, v113, v115\n"
    "v_mfma_f32_16x16x32_bf16 a[16:19], a[228:231], v[224:227], a[16:19]\n"
    "ds_read_b128 a[184:187], v249 offset:11264\n"
    "v_cvt_pk_bf16_f32 v37, v38, v39\n"
    "v_mfma_f32_16x16x32_bf16 a[20:23], a[120:123], v[20:23], a[20:23]\n"
    "v_cvt_pk_bf16_f32 v39, v42, v43\n"
    "v_cvt_pk_bf16_f32 v38, v40, v41\n"
    "v_mfma_f32_16x16x32_bf16 a[28:31], a[220:223], v[88:91], a[28:31]\n"
    "s_waitcnt lgkmcnt(4)\n"
    "buffer_store_dwordx4 v[196:199], v244, s[24:27], s75 offen offset:128 sc1\n"
    "ds_write_b128 v3, v[36:39] offset:4096\n"
    "v_mfma_f32_16x16x32_bf16 a[16:19], a[112:115], v[20:23], a[16:19]\n"
    "v_fma_f32 v58, v58, v113, v115\n"
    "v_fma_f32 v59, v59, v113, v115\n"
    "v_mfma_f32_16x16x32_bf16 a[16:19], a[116:119], v[24:27], a[16:19]\n"
    "v_fma_f32 v56, v56, v113, v115\n"
    "v_fma_f32 v57, v57, v113, v115\n"
    "v_cvt_pk_bf16_f32 v55, v58, v59\n"
    "v_mfma_f32_16x16x32_bf16 a[28:31], a[216:219], v[84:87], a[28:31]\n"
    "v_accvgpr_read_b32 v49, a21\n"
    "v_cvt_pk_bf16_f32 v54, v56, v57\n"
    "v_mfma_f32_16x16x32_bf16 a[24:27], a[212:215], v[88:91], a[24:27]\n"
    "ds_write_b128 v2, v[52:55] offset:4096\n"
    "v_mfma_f32_16x16x32_bf16 a[32:35], a[192:195], v[92:95], a[32:35]\n"
    "v_accvgpr_read_b32 v64, a19\n"
    "v_accvgpr_read_b32 v45, a20\n"
    "v_mfma_f32_16x16x32_bf16 a[24:27], a[240:243], v[220:223], a[24:27]\n"
    "v_accvgpr_read_b32 v44, a16\n"
    "v_accvgpr_read_b32 v65, a23\n"
    "v_mfma_f32_16x16x32_bf16 a[24:27], a[244:247], v[224:227], a[24:27]\n"
    "ds_read_b128 v[56:59], v252 offset:5120\n"
    "v_accvgpr_read_b32 v48, a17\n"
    "v_mfma_f32_16x16x32_bf16 a[28:31], a[252:255], v[224:227], a[28:31]\n"
    "v_fma_f32 v64, v64, v117, v119\n"
    "v_fma_f32 v65, v65, v117, v119\n"
    "v_mfma_f32_16x16x32_bf16 a[24:27], a[128:131], v[20:23], a[24:27]\n"
    "ds_read_b128 v[40:43], v252 offset:4096\n"
    "v_accvgpr_read_b32 v60, a18\n"
    "v_mfma_f32_16x16x32_bf16 a[24:27], a[132:135], v[24:27], a[24:27]\n"
    "v_fma_f32 v44, v44, v117, v119\n"
    "v_fma_f32 v45, v45, v117, v119\n"
    "v_mfma_f32_16x16x32_bf16 a[28:31], a[248:251], v[220:223], a[28:31]\n"
    "ds_read_b128 a[192:195], v248 offset:8192\n"
    "v_accvgpr_read_b32 v61, a22\n"
    "v_mfma_f32_16x16x32_bf16 a[28:31], a[140:143], v[24:27], a[28:31]\n"
    "v_fma_f32 v48, v48, v117, v119\n"
    "v_fma_f32 v49, v49, v117, v119\n"
    "v_mfma_f32_16x16x32_bf16 a[28:31], a[136:139], v[20:23], a[28:31]\n"
    "v_fma_f32 v60, v60, v117, v119\n"
    "v_fma_f32 v61, v61, v117, v119\n"
    "v_mfma_f32_16x16x32_bf16 a[40:43], a[208:211], v[92:95], a[40:43]\n"
    "v_accvgpr_read_b32 v62, a26\n"
    "v_accvgpr_read_b32 v66, a27\n"
    "v_mfma_f32_16x16x32_bf16 a[36:39], a[204:207], v[96:99], a[36:39]\n"
    "s_waitcnt lgkmcnt(2)\n"
    "buffer_store_dwordx4 v[56:59], v244, s[24:27], s76 offen offset:3200 sc1\n"
    "v_accvgpr_read_b32 v46, a24\n"
    "v_accvgpr_read_b32 v63, a30\n"
    "v_accvgpr_read_b32 v47, a28\n"
    "v_accvgpr_read_b32 v51, a29\n"
    "v_mfma_f32_16x16x32_bf16 a[40:43], a[212:215], v[96:99], a[40:43]\n"
    "v_fma_f32 v62, v62, v117, v119\n"
    "v_fma_f32 v63, v63, v117, v119\n"
    "v_mfma_f32_16x16x32_bf16 a[44:47], a[220:223], v[96:99], a[44:47]\n"
    "v_accvgpr_read_b32 v50, a25\n"
    "v_cvt_pk_bf16_f32 v44, v44, v45\n"
    "v_cvt_pk_bf16_f32 v60, v60, v61\n"
    "v_mfma_f32_16x16x32_bf16 a[36:39], a[200:203], v[92:95], a[36:39]\n"
    "v_fma_f32 v50, v50, v117, v119\n"
    "v_fma_f32 v51, v51, v117, v119\n"
    "v_mfma_f32_16x16x32_bf16 a[44:47], a[216:219], v[92:95], a[44:47]\n"
    "v_fma_f32 v46, v46, v117, v119\n"
    "v_fma_f32 v47, v47, v117, v119\n"
    "v_mfma_f32_16x16x32_bf16 a[36:39], a[236:239], v[232:235], a[36:39]\n"
    "v_cvt_pk_bf16_f32 v61, v62, v63\n"
    "v_cvt_pk_bf16_f32 v45, v46, v47\n"
    "v_mfma_f32_16x16x32_bf16 a[40:43], a[240:243], v[228:231], a[40:43]\n"
    "v_accvgpr_read_b32 v67, a31\n"
    "v_cvt_pk_bf16_f32 v47, v50, v51\n"
    "v_mfma_f32_16x16x32_bf16 a[36:39], a[232:235], v[228:231], a[36:39]\n"
    "ds_read_b128 a[208:211], v248 offset:10240\n"
    "v_cvt_pk_bf16_f32 v46, v48, v49\n"
    "v_mfma_f32_16x16x32_bf16 a[36:39], a[124:127], v[32:35], a[36:39]\n"
    "s_waitcnt lgkmcnt(2)\n"
    "buffer_store_dwordx4 v[40:43], v244, s[24:27], s76 offen offset:128 sc1\n"
    "ds_write_b128 v3, v[44:47] offset:0\n"
    "v_mfma_f32_16x16x32_bf16 a[36:39], a[120:123], v[28:31], a[36:39]\n"
    "v_fma_f32 v66, v66, v117, v119\n"
    "v_fma_f32 v67, v67, v117, v119\n"
    "v_mfma_f32_16x16x32_bf16 a[40:43], a[244:247], v[232:235], a[40:43]\n"
    "v_cvt_pk_bf16_f32 v62, v64, v65\n"
    "v_cvt_pk_bf16_f32 v63, v66, v67\n"
    "v_mfma_f32_16x16x32_bf16 a[44:47], a[252:255], v[232:235], a[44:47]\n"
    "ds_write_b128 v2, v[60:63] offset:0\n"
    "v_mfma_f32_16x16x32_bf16 a[32:35], a[196:199], v[96:99], a[32:35]\n"
    "v_accvgpr_read_b32 v125, a36\n"
    "v_fma_f32 v250, |v110|, 4.0, v250\n"
    "v_mfma_f32_16x16x32_bf16 a[44:47], a[248:251], v[228:231], a[44:47]\n"
    "v_fma_f32 v250, v108, 4.0, v250\n"
    "v_accvgpr_read_b32 v137, a39\n"
    "v_mfma_f32_16x16x32_bf16 a[32:35], a[224:227], v[228:231], a[32:35]\n"
    "ds_read_b128 a[236:239], v249 offset:46080\n"
    "ds_read_b128 v[48:51], v252 offset:0\n"
    "ds_read_b128 a[212:215], v248 offset:22528\n"
    "v_mfma_f32_16x16x32_bf16 a[32:35], a[228:231], v[232:235], a[32:35]\n"
    "ds_read_b128 a[224:227], v249 offset:32768\n"
    "v_accvgpr_read_b32 v133, a38\n"
    "v_mfma_f32_16x16x32_bf16 a[40:43], a[128:131], v[28:31], a[40:43]\n"
    "ds_read_b128 a[204:207], v248 offset:21504\n"
    "v_mfma_f32_16x16x32_bf16 a[44:47], a[140:143], v[32:35], a[44:47]\n"
    "ds_read_b128 v[64:67], v252 offset:1024\n"
    "ds_read_b128 a[240:243], v249 offset:34816\n"
    "v_mfma_f32_16x16x32_bf16 a[32:35], a[112:115], v[28:31], a[32:35]\n"
    "ds_read_b128 a[216:219], v248 offset:11264\n"
    "ds_read_b128 a[196:199], v248 offset:20480\n"
    "v_mfma_f32_16x16x32_bf16 a[32:35], a[116:119], v[32:35], a[32:35]\n"
    "s_waitcnt lgkmcnt(12)\n"
    "ds_read_b128 a[244:247], v249 offset:47104\n"
    "ds_read_b128 a[200:203], v248 offset:9216\n"
    "v_mfma_f32_16x16x32_bf16 a[48:51], a[192:195], v[68:71], a[48:51]\n"
    "ds_read_b128 a[232:235], v249 offset:33792\n"
    "ds_read_b128 a[252:255], v249 offset:48128\n"
    "v_mfma_f32_16x16x32_bf16 a[40:43], a[132:135], v[32:35], a[40:43]\n"
    "s_nop 0\n"
    "v_accvgpr_read_b32 v124, a32\n"
    "v_mfma_f32_16x16x32_bf16 a[44:47], a[136:139], v[28:31], a[44:47]\n"
    "s_waitcnt lgkmcnt(11)\n"
    "buffer_store_dwordx4 v[48:51], v244, s[24:27], s77 offen offset:128 sc1\n"
    "v_accvgpr_read_b32 v128, a33\n"
    "v_accvgpr_read_b32 v132, a34\n"
    "v_fma_f32 v132, v132, v121, v123\n"
    "v_fma_f32 v133, v133, v121, v123\n"
    "v_accvgpr_read_b32 v134, a42\n"
    "v_mfma_f32_16x16x32_bf16 a[56:59], a[208:211], v[68:71], a[56:59]\n"
    "s_waitcnt lgkmcnt(8)\n"
    "v_accvgpr_read_b32 v129, a37\n"
    "v_accvgpr_read_b32 v131, a45\n"
    "v_mfma_f32_16x16x32_bf16 a[52:55], a[204:207], v[72:75], a[52:55]\n"
    "v_accvgpr_read_b32 v130, a41\n"
    "v_accvgpr_read_b32 v138, a43\n"
    "v_mfma_f32_16x16x32_bf16 a[56:59], a[212:215], v[72:75], a[56:59]\n"
    "s_waitcnt lgkmcnt(4)\n"
    "v_accvgpr_read_b32 v135, a46\n"
    "v_fma_f32 v124, v124, v121, v123\n"
    "v_fma_f32 v125, v125, v121, v123\n"
    "v_mfma_f32_16x16x32_bf16 a[48:51], a[196:199], v[72:75], a[48:51]\n"
    "s_waitcnt lgkmcnt(2)\n"
    "ds_read_b128 a[228:231], v249 offset:45056\n"
    "ds_read_b128 a[248:251], v249 offset:35840\n"
    "v_mfma_f32_16x16x32_bf16 a[52:55], a[200:203], v[68:71], a[52:55]\n"
    ".Lwp2:\n"
    "s_cbranch_execz .Lw3\n"
    "v_fma_f32 v130, v130, v121, v123\n"
    "v_fma_f32 v131, v131, v121, v123\n"
    "v_mfma_f32_16x16x32_bf16 a[56:59], a[240:243], v[204:207], a[56:59]\n"
    "v_accvgpr_read_b32 v126, a40\n"
    "v_fma_f32 v134, v134, v121, v123\n"
    "v_fma_f32 v135, v135, v121, v123\n"
    "v_mfma_f32_16x16x32_bf16 a[48:51], a[224:227], v[204:207], a[48:51]\n"
    "ds_read_b128 a[220:223], v248 offset:23552\n"
    "v_accvgpr_read_b32 v139, a47\n"
    "v_mfma_f32_16x16x32_bf16 a[56:59], a[244:247], v[208:211], a[56:59]\n"
    "buffer_store_dwordx4 v[64:67], v244, s[24:27], s77 offen offset:3200 sc1\n"
    "v_accvgpr_read_b32 v136, a35\n"
    "v_accvgpr_read_b32 v127, a44\n"
    "v_fma_f32 v138, v138, v121, v123\n"
    "v_fma_f32 v139, v139, v121, v123\n"
    "v_mfma_f32_16x16x32_bf16 a[56:59], a[176:179], v[4:7], a[56:59]\n"
    "v_fma_f32 v126, v126, v121, v123\n"
    "v_fma_f32 v127, v127, v121, v123\n"
    "v_mfma_f32_16x16x32_bf16 a[56:59], a[180:183], v[8:11], a[56:59]\n"
    "v_cvt_pk_bf16_f32 v124, v124, v125\n"
    "v_cvt_pk_bf16_f32 v125, v126, v127\n"
    "v_mfma_f32_16x16x32_bf16 a[52:55], a[236:239], v[208:211], a[52:55]\n"
    "s_waitcnt lgkmcnt(2)\n"
    "v_fma_f32 v128, v128, v121, v123\n"
    "v_fma_f32 v129, v129, v121, v123\n"
    "v_mfma_f32_16x16x32_bf16 a[48:51], a[228:231], v[208:211], a[48:51]\n"
    "v_cvt_pk_bf16_f32 v132, v132, v133\n"
    "v_cvt_pk_bf16_f32 v126, v128, v129\n"
    "v_mfma_f32_16x16x32_bf16 a[52:55], a[232:235], v[204:207], a[52:55]\n"
    "v_cvt_pk_bf16_f32 v133, v134, v135\n"
    "v_cvt_pk_bf16_f32 v127, v130, v131\n"
    "v_mfma_f32_16x16x32_bf16 a[52:55], a[172:175], v[8:11], a[52:55]\n"
    "s_waitcnt lgkmcnt(0)\n"
    "ds_write_b128 v3, v[124:127] offset:2048\n"
    "v_mfma_f32_16x16x32_bf16 a[60:63], a[220:223], v[72:75], a[60:63]\n"
    "v_fma_f32 v136, v136, v121, v123\n"
    "v_fma_f32 v137, v137, v121, v123\n"
    "v_mfma_f32_16x16x32_bf16 a[52:55], a[168:171], v[4:7], a[52:55]\n"
    "v_cvt_pk_bf16_f32 v135, v138, v139\n"
    "v_cvt_pk_bf16_f32 v134, v136, v137\n"
    "v_mfma_f32_16x16x32_bf16 a[60:63], a[216:219], v[68:71], a[60:63]\n"
    "ds_write_b128 v2, v[132:135] offset:2048\n"
    "v_mfma_f32_16x16x32_bf16 a[48:51], a[160:163], v[4:7], a[48:51]\n"
    "v_accvgpr_read_b32 v146, a57\n"
    "v_mfma_f32_16x16x32_bf16 a[48:51], a[164:167], v[8:11], a[48:51]\n"
    "ds_read_b128 v[128:131], v252 offset:2048\n"
    "v_mfma_f32_16x16x32_bf16 a[60:63], a[252:255], v[208:211], a[60:63]\n"
    "v_accvgpr_read_b32 v141, a52\n"
    "v_accvgpr_read_b32 v153, a55\n"
    "v_mfma_f32_16x16x32_bf16 a[64:67], a[192:195], v[76:79], a[64:67]\n"
    "v_accvgpr_read_b32 v149, a54\n"
    "v_accvgpr_read_b32 v150, a58\n"
    "v_mfma_f32_16x16x32_bf16 a[60:63], a[248:251], v[204:207], a[60:63]\n"
    "ds_read_b128 v[136:139], v252 offset:3072\n"
    "v_accvgpr_read_b32 v142, a56\n"
    "v_mfma_f32_16x16x32_bf16 a[60:63], a[188:191], v[8:11], a[60:63]\n"
    "v_accvgpr_read_b32 v145, a53\n"
    "v_accvgpr_read_b32 v148, a50\n"
    "v_mfma_f32_16x16x32_bf16 a[60:63], a[184:187], v[4:7], a[60:63]\n"
    "v_accvgpr_read_b32 v154, a59\n"
    "v_accvgpr_read_b32 v140, a48\n"
    "v_mfma_f32_16x16x32_bf16 a[64:67], a[196:199], v[80:83], a[64:67]\n"
    "v_fma_f32 v140, v140, v109, v111\n"
    "v_fma_f32 v141, v141, v109, v111\n"
    "v_mfma_f32_16x16x32_bf16 a[68:71], a[204:207], v[80:83], a[68:71]\n"
    "s_nop 1\n"
    "v_accvgpr_read_b32 v143, a60\n"
    "v_mfma_f32_16x16x32_bf16 a[68:71], a[200:203], v[76:79], a[68:71]\n"
    "s_waitcnt lgkmcnt(1)\n"
    "buffer_store_dwordx4 v[128:131], v244, s[24:27], s78 offen offset:128 sc1\n"
    "v_fma_f32 v148, v148, v109, v111\n"
    "v_fma_f32 v149, v149, v109, v111\n"
    "v_accvgpr_read_b32 v144, a49\n"
    "v_cvt_pk_bf16_f32 v148, v148, v149\n"
    "v_mfma_f32_16x16x32_bf16 a[76:79], a[220:223], v[80:83], a[76:79]\n"
    "v_cvt_pk_bf16_f32 v140, v140, v141\n"
    "v_accvgpr_read_b32 v152, a51\n"
    "v_mfma_f32_16x16x32_bf16 a[76:79], a[216:219], v[76:79], a[76:79]\n"
    "v_fma_f32 v142, v142, v109, v111\n"
    "v_fma_f32 v143, v143, v109, v111\n"
    "v_mfma_f32_16x16x32_bf16 a[64:67], a[224:227], v[212:215], a[64:67]\n"
    "v_fma_f32 v144, v144, v109, v111\n"
    "v_fma_f32 v145, v145, v109, v111\n"
    "v_accvgpr_read_b32 v151, a62\n"
    "v_mfma_f32_16x16x32_bf16 a[68:71], a[236:239], v[216:219], a[68:71]\n"
    "v_accvgpr_read_b32 v155, a63\n"
    "s_nop 0\n"
    "v_mfma_f32_16x16x32_bf16 a[68:71], a[232:235], v[212:215], a[68:71]\n"
    "s_waitcnt lgkmcnt(0)\n"
    "buffer_store_dwordx4 v[136:139], v244, s[24:27], s78 offen offset:3200 sc1\n"
    "v_fma_f32 v150, v150, v109, v111\n"
    "v_fma_f32 v151, v151, v109, v111\n"
    "v_cvt_pk_bf16_f32 v141, v142, v143\n"
    "v_accvgpr_read_b32 v147, a61\n"
    "v_mfma_f32_16x16x32_bf16 a[72:75], a[208:211], v[76:79], a[72:75]\n"
    "v_fma_f32 v146, v146, v109, v111\n"
    "v_fma_f32 v147, v147, v109, v111\n"
    "v_mfma_f32_16x16x32_bf16 a[64:67], a[228:231], v[216:219], a[64:67]\n"
    "v_mfma_f32_16x16x32_bf16 a[68:71], a[172:175], v[16:19], a[68:71]\n"
    "v_fma_f32 v154, v154, v109, v111\n"
    "v_fma_f32 v155, v155, v109, v111\n"
    "v_mfma_f32_16x16x32_bf16 a[64:67], a[160:163], v[12:15], a[64:67]\n"
    "v_cvt_pk_bf16_f32 v149, v150, v151\n"
    "v_cvt_pk_bf16_f32 v142, v144, v145\n"
    "v_cvt_pk_bf16_f32 v143, v146, v147\n"
    "v_mfma_f32_16x16x32_bf16 a[68:71], a[168:171], v[12:15], a[68:71]\n"
    "ds_write_b128 v3, v[140:143] offset:4096\n"
    "v_mfma_f32_16x16x32_bf16 a[72:75], a[212:215], v[80:83], a[72:75]\n"
    "v_fma_f32 v152, v152, v109, v111\n"
    "v_fma_f32 v153, v153, v109, v111\n"
    "v_mfma_f32_16x16x32_bf16 a[72:75], a[240:243], v[212:215], a[72:75]\n"
    "v_cvt_pk_bf16_f32 v150, v152, v153\n"
    "v_cvt_pk_bf16_f32 v151, v154, v155\n"
    "v_mfma_f32_16x16x32_bf16 a[92:95], a[220:223], v[88:91], a[92:95]\n"
    "ds_write_b128 v2, v[148:151] offset:4096\n"
    "v_mfma_f32_16x16x32_bf16 a[92:95], a[216:219], v[84:87], a[92:95]\n"
    "v_accvgpr_read_b32 v161, a69\n"
    "v_fma_f32 v250, v109, s74, v250\n"
    "v_mfma_f32_16x16x32_bf16 a[64:67], a[164:167], v[16:19], a[64:67]\n"
    "v_fma_f32 v250, |v114|, 4.0, v250\n"
    "v_mfma_f32_16x16x32_bf16 a[72:75], a[244:247], v[216:219], a[72:75]\n"
    "ds_read_b128 v[144:147], v252 offset:4096\n"
    "v_mfma_f32_16x16x32_bf16 a[72:75], a[176:179], v[12:15], a[72:75]\n"
    "ds_read_b128 v[152:155], v252 offset:5120\n"
    "v_accvgpr_read_b32 v165, a70\n"
    "v_mfma_f32_16x16x32_bf16 a[72:75], a[180:183], v[16:19], a[72:75]\n"
    "s_nop 0\n"
    "v_accvgpr_read_b32 v156, a64\n"
    "v_accvgpr_read_b32 v160, a65\n"
    "v_mfma_f32_16x16x32_bf16 a[76:79], a[252:255], v[216:219], a[76:79]\n"
    "v_fma_f32 v160, v160, v113, v115\n"
    "v_fma_f32 v161, v161, v113, v115\n"
    "v_mfma_f32_16x16x32_bf16 a[84:87], a[204:207], v[88:91], a[84:87]\n"
    "s_nop 0\n"
    "v_accvgpr_read_b32 v162, a73\n"
    "v_accvgpr_read_b32 v169, a71\n"
    "v_mfma_f32_16x16x32_bf16 a[80:83], a[192:195], v[84:87], a[80:83]\n"
    "v_fma_f32 v250, v112, 4.0, v250\n"
    "s_nop 0\n"
    "v_mfma_f32_16x16x32_bf16 a[76:79], a[248:251], v[212:215], a[76:79]\n"
    "v_accvgpr_read_b32 v166, a74\n"
    "v_accvgpr_read_b32 v157, a68\n"
    "v_mfma_f32_16x16x32_bf16 a[76:79], a[188:191], v[16:19], a[76:79]\n"
    "v_accvgpr_read_b32 v164, a66\n"
    "v_accvgpr_read_b32 v168, a67\n"
    "v_mfma_f32_16x16x32_bf16 a[76:79], a[184:187], v[12:15], a[76:79]\n"
    "s_waitcnt lgkmcnt(1)\n"
    "buffer_store_dwordx4 v[144:147], v244, s[24:27], s75 offen offset:256 sc1\n"
    "v_fma_f32 v164, v164, v113, v115\n"
    "v_fma_f32 v165, v165, v113, v115\n"
    "v_accvgpr_read_b32 v158, a72\n"
    "v_fma_f32 v156, v156, v113, v115\n"
    "v_fma_f32 v157, v157, v113, v115\n"
    "v_mfma_f32_16x16x32_bf16 a[80:83], a[196:199], v[88:91], a[80:83]\n"
    "v_accvgpr_read_b32 v167, a78\n"
    "v_cvt_pk_bf16_f32 v156, v156, v157\n"
    "v_fma_f32 v250, v113, s74, v250\n"
    "v_mfma_f32_16x16x32_bf16 a[88:91], a[208:211], v[84:87], a[88:91]\n"
    "s_nop 0\n"
    "v_accvgpr_read_b32 v159, a76\n"
    "v_fma_f32 v166, v166, v113, v115\n"
    "v_fma_f32 v167, v167, v113, v115\n"
    "v_mfma_f32_16x16x32_bf16 a[84:87], a[200:203], v[84:87], a[84:87]\n"
    "s_waitcnt lgkmcnt(0)\n"
    "buffer_store_dwordx4 v[152:155], v244, s[24:27], s75 offen offset:3328 sc1\n"
    "v_fma_f32 v158, v158, v113, v115\n"
    "v_fma_f32 v159, v159, v113, v115\n"
    "v_accvgpr_read_b32 v170, a75\n"
    "v_accvgpr_read_b32 v171, a79\n"
    "v_mfma_f32_16x16x32_bf16 a[84:87], a[236:239], v[224:227], a[84:87]\n"
    "v_cvt_pk_bf16_f32 v157, v158, v159\n"
    "v_cvt_pk_bf16_f32 v158, v160, v161\n"
    "v_mfma_f32_16x16x32_bf16 a[84:87], a[232:235], v[220:223], a[84:87]\n"
    "v_cvt_pk_bf16_f32 v164, v164, v165\n"
    "v_accvgpr_read_b32 v163, a77\n"
    "v_mfma_f32_16x16x32_bf16 a[80:83], a[224:227], v[220:223], a[80:83]\n"
    "v_fma_f32 v162, v162, v113, v115\n"
    "v_fma_f32 v163, v163, v113, v115\n"
    "v_mfma_f32_16x16x32_bf16 a[88:91], a[212:215], v[88:91], a[88:91]\n"
    "v_cvt_pk_bf16_f32 v159, v162, v163\n"
    "v_cvt_pk_bf16_f32 v165, v166, v167\n"
    "v_mfma_f32_16x16x32_bf16 a[84:87], a[172:175], v[24:27], a[84:87]\n"
    "ds_write_b128 v3, v[156:159] offset:0\n"
    "v_mfma_f32_16x16x32_bf16 a[84:87], a[168:171], v[20:23], a[84:87]\n"
    "v_fma_f32 v168, v168, v113, v115\n"
    "v_fma_f32 v169, v169, v113, v115\n"
    "v_mfma_f32_16x16x32_bf16 a[92:95], a[252:255], v[224:227], a[92:95]\n"
    "v_fma_f32 v170, v170, v113, v115\n"
    "v_fma_f32 v171, v171, v113, v115\n"
    "v_fma_f32 v250, |v118|, 4.0, v250\n"
    "v_cvt_pk_bf16_f32 v167, v170, v171\n"
    "v_cvt_pk_bf16_f32 v166, v168, v169\n"
    "v_mfma_f32_16x16x32_bf16 a[80:83], a[228:231], v[224:227], a[80:83]\n"
    "ds_write_b128 v2, v[164:167] offset:0\n"
    "v_mfma_f32_16x16x32_bf16 a[80:83], a[160:163], v[20:23], a[80:83]\n"
    "v_mfma_f32_16x16x32_bf16 a[92:95], a[248:251], v[220:223], a[92:95]\n"
    "ds_read_b128 v[160:163], v252 offset:0\n"
    "v_mfma_f32_16x16x32_bf16 a[80:83], a[164:167], v[24:27], a[80:83]\n"
    "ds_read_b128 v[168:171], v252 offset:1024\n"
    "v_accvgpr_read_b32 v173, a84\n"
    "v_mfma_f32_16x16x32_bf16 a[92:95], a[188:191], v[24:27], a[92:95]\n"
    "v_accvgpr_read_b32 v177, a85\n"
    "v_mfma_f32_16x16x32_bf16 a[88:91], a[240:243], v[220:223], a[88:91]\n"
    "v_mfma_f32_16x16x32_bf16 a[88:91], a[244:247], v[224:227], a[88:91]\n"
    "s_nop 1\n"
    "v_accvgpr_read_b32 v176, a81\n"
    "v_mfma_f32_16x16x32_bf16 a[92:95], a[184:187], v[20:23], a[92:95]\n"
    "v_accvgpr_read_b32 v172, a80\n"
    "v_accvgpr_read_b32 v184, a83\n"
    "v_mfma_f32_16x16x32_bf16 a[88:91], a[176:179], v[20:23], a[88:91]\n"
    "v_fma_f32 v172, v172, v117, v119\n"
    "v_fma_f32 v173, v173, v117, v119\n"
    "v_mfma_f32_16x16x32_bf16 a[88:91], a[180:183], v[24:27], a[88:91]\n"
    "v_fma_f32 v176, v176, v117, v119\n"
    "v_fma_f32 v177, v177, v117, v119\n"
    "v_mfma_f32_16x16x32_bf16 a[96:99], a[192:195], v[92:95], a[96:99]\n"
    "s_waitcnt lgkmcnt(1)\n"
    "buffer_store_dwordx4 v[160:163], v244, s[24:27], s76 offen offset:256 sc1\n"
    "v_accvgpr_read_b32 v175, a92\n"
    "v_accvgpr_read_b32 v180, a82\n"
    "v_accvgpr_read_b32 v181, a86\n"
    "v_cvt_pk_bf16_f32 v172, v172, v173\n"
    "v_mfma_f32_16x16x32_bf16 a[108:111], a[220:223], v[96:99], a[108:111]\n"
    "v_accvgpr_read_b32 v179, a93\n"
    "v_accvgpr_read_b32 v174, a88\n"
    "v_mfma_f32_16x16x32_bf16 a[108:111], a[216:219], v[92:95], a[108:111]\n"
    "v_fma_f32 v250, v116, 4.0, v250\n"
    "v_accvgpr_read_b32 v183, a94\n"
    "v_mfma_f32_16x16x32_bf16 a[96:99], a[196:199], v[96:99], a[96:99]\n"
    "v_accvgpr_read_b32 v187, a95\n"
    "v_accvgpr_read_b32 v178, a89\n"
    "v_mfma_f32_16x16x32_bf16 a[100:103], a[204:207], v[96:99], a[100:103]\n"
    "v_mfma_f32_16x16x32_bf16 a[104:107], a[208:211], v[92:95], a[104:107]\n"
    "s_nop 0\n"
    "v_fma_f32 v174, v174, v117, v119\n"
    "v_fma_f32 v175, v175, v117, v119\n"
    "v_mfma_f32_16x16x32_bf16 a[108:111], a[252:255], v[232:235], a[108:111]\n"
    "s_waitcnt lgkmcnt(0)\n"
    "buffer_store_dwordx4 v[168:171], v244, s[24:27], s76 offen offset:3328 sc1\n"
    "v_cvt_pk_bf16_f32 v173, v174, v175\n"
    "v_accvgpr_read_b32 v182, a90\n"
    "v_accvgpr_read_b32 v185, a87\n"
    "v_cvt_pk_bf16_f32 v174, v176, v177\n"
    "v_pk_fma_f32 v[178:179], v[178:179], v[116:117], v[118:119] op_sel:[0,1,1] op_sel_hi:[1,1,1]\n"
    "v_accvgpr_read_b32 v186, a91\n"
    "v_cvt_pk_bf16_f32 v175, v178, v179\n"
    "v_fma_f32 v186, v186, v117, v119\n"
    "v_fma_f32 v187, v187, v117, v119\n"
    "v_pk_fma_f32 v[180:181], v[180:181], v[116:117], v[118:119] op_sel:[0,1,1] op_sel_hi:[1,1,1]\n"
    "v_cmp_eq_u64 s[56:57], v[236:237], v[238:239]\n"
    "v_fma_f32 v182, v182, v117, v119\n"
    "v_fma_f32 v183, v183, v117, v119\n"
    "v_mfma_f32_16x16x32_bf16 a[108:111], a[248:251], v[228:231], a[108:111]\n"
    "ds_write_b128 v3, v[172:175] offset:2048\n"
    "v_cvt_pk_bf16_f32 v180, v180, v181\n"
    "v_mfma_f32_16x16x32_bf16 a[96:99], a[224:227], v[228:231], a[96:99]\n"
    "v_fma_f32 v184, v184, v117, v119\n"
    "v_fma_f32 v185, v185, v117, v119\n"
    "v_cvt_pk_bf16_f32 v181, v182, v183\n"
    "v_mfma_f32_16x16x32_bf16 a[104:107], a[212:215], v[96:99], a[104:107]\n"
    "v_cvt_pk_bf16_f32 v182, v184, v185\n"
    "v_cvt_pk_bf16_f32 v183, v186, v187\n"
    "v_mfma_f32_16x16x32_bf16 a[96:99], a[228:231], v[232:235], a[96:99]\n"
    "ds_write_b128 v2, v[180:183] offset:2048\n"
    "v_mfma_f32_16x16x32_bf16 a[96:99], a[160:163], v[28:31], a[96:99]\n"
    "v_mfma_f32_16x16x32_bf16 a[104:107], a[240:243], v[228:231], a[104:107]\n"
    "ds_read_b128 v[176:179], v252 offset:2048\n"
    "v_mfma_f32_16x16x32_bf16 a[104:107], a[244:247], v[232:235], a[104:107]\n"
    "v_mfma_f32_16x16x32_bf16 a[100:103], a[200:203], v[92:95], a[100:103]\n"
    "v_mfma_f32_16x16x32_bf16 a[104:107], a[176:179], v[28:31], a[104:107]\n"
    "v_mfma_f32_16x16x32_bf16 a[104:107], a[180:183], v[32:35], a[104:107]\n"
    "v_mfma_f32_16x16x32_bf16 a[108:111], a[188:191], v[32:35], a[108:111]\n"
    "s_or_b64 s[56:57], s[56:57], s[34:35]\n"
    "ds_read_b128 v[184:187], v252 offset:3072\n"
    "v_mfma_f32_16x16x32_bf16 a[100:103], a[236:239], v[232:235], a[100:103]\n"
    "v_mfma_f32_16x16x32_bf16 a[96:99], a[164:167], v[32:35], a[96:99]\n"
    "v_mfma_f32_16x16x32_bf16 a[100:103], a[232:235], v[228:231], a[100:103]\n"
    "s_waitcnt lgkmcnt(1)\n"
    "buffer_store_dwordx4 v[176:179], v244, s[24:27], s77 offen offset:256 sc1\n"
    "v_accvgpr_read_b32 v198, a106\n"
    "v_fma_f32 v250, v117, s74, v250\n"
    "v_fma_f32 v250, |v122|, 4.0, v250\n"
    "v_accvgpr_read_b32 v194, a105\n"
    "s_nop 0\n"
    "v_accvgpr_read_b32 v196, a98\n"
    "v_mfma_f32_16x16x32_bf16 a[108:111], a[184:187], v[28:31], a[108:111]\n"
    "v_accvgpr_read_b32 v202, a107\n"
    "v_mfma_f32_16x16x32_bf16 a[100:103], a[172:175], v[32:35], a[100:103]\n"
    "s_nop 0\n"
    "v_mfma_f32_16x16x32_bf16 a[100:103], a[168:171], v[28:31], a[100:103]\n"
    "s_waitcnt lgkmcnt(0)\n"
    "buffer_store_dwordx4 v[184:187], v244, s[24:27], s77 offen offset:3328 sc1\n"
    "v_accvgpr_read_b32 v190, a104\n"
    "s_nop 1\n"
    "v_accvgpr_read_b32 v191, a108\n"
    "v_accvgpr_read_b32 v199, a110\n"
    "v_fma_f32 v250, v120, 4.0, v250\n"
    "v_accvgpr_read_b32 v197, a102\n"
    "v_accvgpr_read_b32 v200, a99\n"
    "v_accvgpr_read_b32 v195, a109\n"
    "v_accvgpr_read_b32 v188, a96\n"
    "v_pk_fma_f32 v[190:191], v[190:191], v[120:121], v[122:123] op_sel:[0,1,1] op_sel_hi:[1,1,1]\n"
    "v_accvgpr_read_b32 v201, a103\n"
    "v_accvgpr_read_b32 v203, a111\n"
    "v_accvgpr_read_b32 v189, a100\n"
    "v_pk_fma_f32 v[194:195], v[194:195], v[120:121], v[122:123] op_sel:[0,1,1] op_sel_hi:[1,1,1]\n"
    "v_fma_f32 v198, v198, v121, v123\n"
    "v_fma_f32 v199, v199, v121, v123\n"
    "v_fma_f32 v188, v188, v121, v123\n"
    "v_fma_f32 v189, v189, v121, v123\n"
    "v_accvgpr_read_b32 v192, a97\n"
    "v_cvt_pk_bf16_f32 v188, v188, v189\n"
    "v_accvgpr_read_b32 v193, a101\n"
    "v_cvt_pk_bf16_f32 v189, v190, v191\n"
    "v_cvt_pk_bf16_f32 v191, v194, v195\n"
    "v_fma_f32 v192, v192, v121, v123\n"
    "v_fma_f32 v193, v193, v121, v123\n"
    "v_cvt_pk_bf16_f32 v190, v192, v193\n"
    "ds_write_b128 v3, v[188:191] offset:4096\n"
    "v_pk_fma_f32 v[196:197], v[196:197], v[120:121], v[122:123] op_sel:[0,1,1] op_sel_hi:[1,1,1]\n"
    "v_cvt_pk_bf16_f32 v196, v196, v197\n"
    "v_cvt_pk_bf16_f32 v197, v198, v199\n"
    "v_pk_fma_f32 v[200:201], v[200:201], v[120:121], v[122:123] op_sel:[0,1,1] op_sel_hi:[1,1,1]\n"
    "v_fma_f32 v250, v121, s74, v250\n"
    "v_pk_fma_f32 v[202:203], v[202:203], v[120:121], v[122:123] op_sel:[0,1,1] op_sel_hi:[1,1,1]\n"
    "v_cvt_pk_bf16_f32 v199, v202, v203\n"
    "v_cvt_pk_bf16_f32 v198, v200, v201\n"
    "ds_write_b128 v2, v[196:199] offset:4096\n"
    "v_cmp_nlt_f32 s[54:55], |v250|, s73\n"
    "ds_read_b128 v[200:203], v252 offset:5120\n"
    "ds_read_b128 v[192:195], v252 offset:4096\n"
    "s_waitcnt lgkmcnt(1)\n"
    "buffer_store_dwordx4 v[200:203], v244, s[24:27], s78 offen offset:3328 sc1\n"
    "s_or_b64 s[54:55], s[54:55], s[56:57]\n"
    "s_waitcnt lgkmcnt(0)\n"
    "buffer_store_dwordx4 v[192:195], v244, s[24:27], s78 offen offset:256 sc1\n"
    "s_or_b32 %0, s54, s55\n"
    "s_cmp_eq_u32 %0, 0\n"
    "s_cbranch_scc1 .Lfin\n"
    "s_waitcnt vmcnt(0)\n"
    ".Lfin:\n"
    ".Lend:\n"
    ".Lend2:\n"
    : "=&s"(bad), "+s"(ka) : "s"(b), "v"(tid)
    : "memory", "vcc", "s4", "s5", "s6", "s7", "s8", "s9", "s10", "s11", "s12", "s13", "s14", "s15", "s16", "s17", "s18", "s19", "s20", "s21", "s22", "s23", "s24", "s25", "s26", "s27", "s28", "s29", "s30", "s31", "s34", "s35", "s36", "s37", "s38", "s39", "s40", "s41", "s42", "s43", "s44", "s45", "s46", "s47", "s48", "s49", "s50", "s51", "s52", "s53", "s54", "s55", "s56", "s57", "s58", "s59", "s60", "s61", "s62", "s63", "s64", "s65", "s66", "s67", "s68", "s69", "s70", "s71", "s72", "s73", "s74", "s75", "s76", "s77", "s78", "s79", "s80", "s81", "s82", "s83", "s84", "s85", "s86", "s87", "s88", "s89", "s90", "s91", "s92", "s93", "s94", "s95", "s96", "s97", "s98", "s99", "v1", "v2", "v3", "v4", "v5", "v6", "v7", "v8", "v9", "v10", "v11", "v12", "v13", "v14", "v15", "v16", "v17", "v18", "v19", "v20", "v21", "v22", "v23", "v24", "v25", "v26", "v27", "v28", "v29", "v30", "v31", "v32", "v33", "v34", "v35", "v36", "v37", "v38", "v39", "v40", "v41", "v42", "v43", "v44", "v45", "v46", "v47", "v48", "v49", "v50", "v51", "v52", "v53", "v54", "v55", "v56", "v57", "v58", "v59", "v60", "v61", "v62", "v63", "v64", "v65", "v66", "v67", "v68", "v69", "v70", "v71", "v72", "v73", "v74", "v75", "v76", "v77", "v78", "v79", "v80", "v81", "v82", "v83", "v84", "v85", "v86", "v87", "v88", "v89", "v90", "v91", "v92", "v93", "v94", "v95", "v96", "v97", "v98", "v99", "v100", "v101", "v102", "v103", "v104", "v105", "v106", "v107", "v108", "v109", "v110", "v111", "v112", "v113", "v114", "v115", "v116", "v117", "v118", "v119", "v120", "v121", "v122", "v123", "v124", "v125", "v126", "v127", "v128", "v129", "v130", "v131", "v132", "v133", "v134", "v135", "v136", "v137", "v138", "v139", "v140", "v141", "v142", "v143", "v144", "v145", "v146", "v147", "v148", "v149", "v150", "v151", "v152", "v153", "v154", "v155", "v156", "v157", "v158", "v159", "v160", "v161", "v162", "v163", "v164", "v165", "v166", "v167", "v168", "v169", "v170", "v171", "v172", "v173", "v174", "v175", "v176", "v177", "v178", "v179", "v180", "v181", "v182", "v183", "v184", "v185", "v186", "v187", "v188", "v189", "v190", "v191", "v192", "v193", "v194", "v195", "v196", "v197", "v198", "v199", "v200", "v201", "v202", "v203", "v204", "v205", "v206", "v207", "v208", "v209", "v210", "v211", "v212", "v213", "v214", "v215", "v216", "v217", "v218", "v219", "v220", "v221", "v222", "v223", "v224", "v225", "v226", "v227", "v228", "v229", "v230", "v231", "v232", "v233", "v234", "v235", "v236", "v237", "v238", "v239", "v240", "v241", "v242", "v243", "v244", "v245", "v246", "v247", "v248", "v249", "v250", "v251", "v252", "v253", "v254", "v255", "a0", "a1", "a2", "a3", "a4", "a5", "a6", "a7", "a8", "a9", "a10", "a11", "a12", "a13", "a14", "a15", "a16", "a17", "a18", "a19", "a20", "a21", "a22", "a23", "a24", "a25", "a26", "a27", "a28", "a29", "a30", "a31", "a32", "a33", "a34", "a35", "a36", "a37", "a38", "a39", "a40", "a41", "a42", "a43", "a44", "a45", "a46", "a47", "a48", "a49", "a50", "a51", "a52", "a53", "a54", "a55", "a56", "a57", "a58", "a59", "a60", "a61", "a62", "a63", "a64", "a65", "a66", "a67", "a68", "a69", "a70", "a71", "a72", "a73", "a74", "a75", "a76", "a77", "a78", "a79", "a80", "a81", "a82", "a83", "a84", "a85", "a86", "a87", "a88", "a89", "a90", "a91", "a92", "a93", "a94", "a95", "a96", "a97", "a98", "a99", "a100", "a101", "a102", "a103", "a104", "a105", "a106", "a107", "a108", "a109", "a110", "a111", "a112", "a113", "a114", "a115", "a116", "a117", "a118", "a119", "a120", "a121", "a122", "a123", "a124", "a125", "a126", "a127", "a128", "a129", "a130", "a131", "a132", "a133", "a134", "a135", "a136", "a137", "a138", "a139", "a140", "a141", "a142", "a143", "a144", "a145", "a146", "a147", "a148", "a149", "a150", "a151", "a152", "a153", "a154", "a155", "a156", "a157", "a158", "a159", "a160", "a161", "a162", "a163", "a164", "a165", "a166", "a167", "a168", "a169", "a170", "a171", "a172", "a173", "a174", "a175", "a176", "a177", "a178", "a179", "a180", "a181", "a182", "a183", "a184", "a185", "a186", "a187", "a188", "a189", "a190", "a191", "a192", "a193", "a194", "a195", "a196", "a197", "a198", "a199", "a200", "a201", "a202", "a203", "a204", "a205", "a206", "a207", "a208", "a209", "a210", "a211", "a212", "a213", "a214", "a215", "a216", "a217", "a218", "a219", "a220", "a221", "a222", "a223", "a224", "a225", "a226", "a227", "a228", "a229", "a230", "a231", "a232", "a233", "a234", "a235", "a236", "a237", "a238", "a239", "a240", "a241", "a242", "a243", "a244", "a245", "a246", "a247", "a248", "a249", "a250", "a251", "a252", "a253", "a254", "a255");
  if (bad) {
    slow_rows((const Args*)ka, (b & 7) * 4 + ((b >> 3) & 3), (b >> 5) * 16 + (tid >> 6) * 128, tid & 63);
  }
}

extern "C" void kernel_launch(void* const* d_in, const int* in_sizes, int n_in,
                              void* d_out, int out_size, void* d_ws, size_t ws_size,
                              hipStream_t stream) {
  (void)hipFuncSetAttribute((const void*)attn_fwd, hipFuncAttributeMaxDynamicSharedMemorySize, SHM);
  attn_fwd<<<256, 256, SHM, stream>>>((const unsigned short*)d_in[0], (const int*)d_in[1], (const int*)d_in[2], (const unsigned short*)d_in[3], (const unsigned short*)d_in[4],
                                      (const unsigned short*)d_in[5], (const unsigned short*)d_in[6], (char*)d_ws, (unsigned short*)d_out);
}
```

```cpp
#include <hip/hip_runtime.h>

constexpr int D = 192, NH = 32, S = 2048;
constexpr int SHM = 155968;

struct Args { const unsigned short* q; const int* qk; const int* qv; const unsigned short* ksc; const unsigned short* kzr;
              const unsigned short* vsc; const unsigned short* vzr; char* ws; unsigned short* o; };

__device__ __forceinline__ float bf2f(unsigned short x) { return __uint_as_float((unsigned)x << 16); }
__device__ __forceinline__ float rbf(float x) { return (float)(__bf16)x; }

__device__ __forceinline__ void slow_rows(const Args* a, unsigned h, unsigned row0, unsigned lane) {
  const unsigned short* q = a->q + (size_t)h * S * D;
  unsigned short* o = a->o + (size_t)h * S * D;
  const int* qk = a->qk + (size_t)h * S * D; const int* qv = a->qv + (size_t)h * S * D;
  const unsigned short* ks = a->ksc + h * 32 * D; const unsigned short* kz = a->kzr + h * 32 * D;
  const unsigned short* vs = a->vsc + h * S; const unsigned short* vz = a->vzr + h * S;
#pragma unroll 1
  for (int n = 0; n < 64; ++n) {
    const int r = (row0 + h * 0 + (n >> 4) * 512 + (n & 15)) % S;
    float m = -INFINITY, l = 0.f, a0 = 0.f, a1 = 0.f, a2 = 0.f;
#pragma unroll 1
    for (int t = 0; t < 32; ++t) {
      const int* kr = qk + (size_t)(t * 64 + lane) * D;
      float s = 0.f;
#pragma unroll 4
      for (int d = 0; d < D; ++d) s = fmaf(bf2f(q[r * D + d]), rbf(rbf(rbf((float)kr[d]) - bf2f(kz[t * D + d])) * bf2f(ks[t * D + d])), s);
      s *= 0.07216878364870322f;
      float mx = s;
      for (int x = 32; x; x >>= 1) mx = fmaxf(mx, __shfl_xor(mx, x));
      const float mn = fmaxf(m, mx), c = __expf(m - mn), p = __expf(s - mn);
      float ps = p;
      for (int x = 32; x; x >>= 1) ps += __shfl_xor(ps, x);
      l = l * c + ps; a0 *= c; a1 *= c; a2 *= c; m = mn;
#pragma unroll 2
      for (int k = 0; k < 64; ++k) {
        const float pk = __shfl(p, k), z = bf2f(vz[t * 64 + k]), sc = bf2f(vs[t * 64 + k]);
        const int* vr = qv + (size_t)(t * 64 + k) * D + lane;
        a0 = fmaf(pk, rbf(rbf(rbf((float)vr[0]) - z) * sc), a0); a1 = fmaf(pk, rbf(rbf(rbf((float)vr[64]) - z) * sc), a1); a2 = fmaf(pk, rbf(rbf(rbf((float)vr[128]) - z) * sc), a2);
      }
    }
    const float i = 1.f / l;
    o[r * D + lane] = __builtin_bit_cast(unsigned short, (__bf16)(a0 * i)); o[r * D + 64 + lane] = __builtin_bit_cast(unsigned short, (__bf16)(a1 * i));
    o[r * D + 128 + lane] = __builtin_bit_cast(unsigned short, (__bf16)(a2 * i));
  }
}

__global__ __launch_bounds__(256, 1) __attribute__((aligned(4096))) void attn_fwd(const unsigned short* q, const int* qk, const int* qv, const unsigned short* ksc, const unsigned short* kzr,
                                                   const unsigned short* vsc, const unsigned short* vzr, char* ws, unsigned short* o) {
  unsigned long long ka = (unsigned long long)__builtin_amdgcn_kernarg_segment_ptr();
  unsigned bad, b = blockIdx.x;
  const unsigned tid = threadIdx.x;
  asm volatile(
    "s_load_dwordx16 s[4:19], %1, 0\n"
    "s_load_dwordx2 s[24:25], %1, 64\n"
    "s_getpc_b64 s[54:55]\n"
    ".Lpc:\n"
    "s_and_b32 s72, %2, 24\n"
    "s_lshl_b32 s72, s72, 4\n"
    "v_lshl_add_u32 v246, %3, 9, s72\n"
    "v_min_u32 v246, .Lend-.Lpc-4, v246\n"
    "global_load_dword v246, v246, s[54:55]\n"
    "s_getreg_b32 s72, hwreg(HW_REG_XCC_ID, 0, 1)\n"
    "s_cmp_eq_u32 s72, 1\n"
    "s_cbranch_scc1 .Ldly\n"
    "s_sleep 20\n"
    ".Ldly:\n"
    "v_readfirstlane_b32 s84, %3\n"
    "v_and_b32 v156, 63, %3\n"
    "s_and_b32 s52, %2, 7\n"
    "s_bfe_u32 s72, %2, 0x20003\n"
    "s_lshr_b32 s53, %2, 5\n"
    "s_lshl_b32 s52, s52, 2\n"
    "s_lshr_b32 s84, s84, 6\n"
    "s_or_b32 s52, s52, s72\n"
    "v_lshlrev_b32 v1, 4, v156\n"
    "s_mov_b32 s30, 1572864\n"
    "s_mov_b32 s31, 0x20000\n"
    "s_mov_b32 s42, 1572864\n"
    "s_mov_b32 s43, 0x20000\n"
    "s_mov_b32 s46, 12288\n"
    "s_mov_b32 s47, 0x20000\n"
    "s_mov_b32 s90, 12288\n"
    "s_mov_b32 s91, 0x20000\n"
    "s_lshr_b32 s79, s84, 1\n"
    "s_and_b32 s59, s84, 1\n"
    "s_cmp_ge_u32 s53, 6\n"
    "s_cselect_b32 s83, 1, 0\n"
    "s_or_b32 s83, s83, s84\n"
    "s_sub_u32 s58, 2, s79\n"
    "s_mul_i32 s71, s58, 49152\n"
    "s_mul_i32 s72, s52, 1572864\n"
    "s_mul_i32 s73, s52, 12288\n"
    "s_mul_i32 s82, s52, 3\n"
    "s_and_b32 s82, s82, 31\n"
    "s_mul_i32 s82, s82, 23\n"
    "s_lshr_b32 s82, s82, 5\n"
    "s_add_u32 s82, s82, 1\n"
    "s_mul_i32 s67, s82, 49152\n"
    "s_add_u32 s30, s30, s67\n"
    "s_add_u32 s42, s42, s67\n"
    "v_lshrrev_b32 v157, 2, v156\n"
    "v_and_b32 v158, 3, v156\n"
    "s_mul_i32 s35, s84, 3072\n"
    "s_bitcmp1_b32 s83, 0\n"
    "s_cbranch_scc1 .Lf2b\n"
    "v_and_b32 v159, 32, v156\n"
    "v_mul_u32_u24 v242, 376, v159\n"
    "v_and_b32 v159, 16, v156\n"
    "v_mul_u32_u24 v159, 88, v159\n"
    "v_lshl_add_u32 v242, v156, 3, v242\n"
    "v_add_u32 v242, v159, v242\n"
    "s_lshl_b32 s63, s53, 7\n"
    "s_add_u32 s63, s63, s67\n"
    "s_and_b32 s81, s58, 1\n"
    "s_add_u32 s81, s81, s82\n"
    "v_lshl_add_u32 v160, v157, 1, s81\n"
    "v_and_b32 v160, 31, v160\n"
    "v_mul_u32_u24 v160, 384, v160\n"
    "v_lshl_add_u32 v160, v158, 4, v160\n"
    "s_lshl_b32 s81, s53, 6\n"
    "s_add_u32 s75, s71, s63\n"
    "s_add_u32 s76, s75, 6144\n"
    "s_add_u32 s77, s75, 24576\n"
    "s_add_u32 s78, s75, 30720\n"
    "s_waitcnt lgkmcnt(0)\n"
    "s_add_u32 s44, s10, s73\n"
    "s_addc_u32 s45, s11, 0\n"
    "s_add_u32 s88, s12, s73\n"
    "s_addc_u32 s89, s13, 0\n"
    "buffer_load_dwordx4 v[4:7], v1, s[44:47], s35 offen offset:0\n"
    "buffer_load_dwordx4 v[8:11], v1, s[44:47], s35 offen offset:1024\n"
    "buffer_load_dwordx4 v[12:15], v1, s[44:47], s35 offen offset:2048\n"
    "buffer_load_dwordx4 v[16:19], v1, s[88:91], 0 offen offset:0\n"
    "buffer_load_dwordx4 v[164:167], v160, s[44:47], s81 offen offset:0\n"
    "buffer_load_dwordx4 v[168:171], v160, s[88:91], s81 offen offset:0\n"
    "s_add_u32 s28, s6, s72\n"
    "s_addc_u32 s29, s7, 0\n"
    "buffer_load_dwordx2 v[204:205], v242, s[28:31], s75 offen offset:0 nt\n"
    "buffer_load_dwordx2 v[206:207], v242, s[28:31], s75 offen offset:768 nt\n"
    "buffer_load_dwordx2 v[208:209], v242, s[28:31], s75 offen offset:3072 nt\n"
    "buffer_load_dwordx2 v[210:211], v242, s[28:31], s75 offen offset:3840 nt\n"
    "buffer_load_dwordx2 v[212:213], v242, s[28:31], s76 offen offset:0 nt\n"
    "buffer_load_dwordx2 v[214:215], v242, s[28:31], s76 offen offset:768 nt\n"
    "buffer_load_dwordx2 v[216:217], v242, s[28:31], s76 offen offset:3072 nt\n"
    "buffer_load_dwordx2 v[218:219], v242, s[28:31], s76 offen offset:3840 nt\n"
    "buffer_load_dwordx2 v[220:221], v242, s[28:31], s77 offen offset:0 nt\n"
    "buffer_load_dwordx2 v[222:223], v242, s[28:31], s77 offen offset:768 nt\n"
    "buffer_load_dwordx2 v[224:225], v242, s[28:31], s77 offen offset:3072 nt\n"
    "buffer_load_dwordx2 v[226:227], v242, s[28:31], s77 offen offset:3840 nt\n"
    "buffer_load_dwordx2 v[228:229], v242, s[28:31], s78 offen offset:0 nt\n"
    "buffer_load_dwordx2 v[230:231], v242, s[28:31], s78 offen offset:768 nt\n"
    "buffer_load_dwordx2 v[232:233], v242, s[28:31], s78 offen offset:3072 nt\n"
    "buffer_load_dwordx2 v[234:235], v242, s[28:31], s78 offen offset:3840 nt\n"
    "s_add_u32 s75, s71, s63\n"
    "s_add_u32 s75, s75, 98304\n"
    "s_add_u32 s76, s75, 6144\n"
    "s_add_u32 s77, s75, 24576\n"
    "s_add_u32 s78, s75, 30720\n"
    "buffer_load_dwordx2 v[36:37], v242, s[28:31], s75 offen offset:0 nt\n"
    "buffer_load_dwordx2 v[38:39], v242, s[28:31], s75 offen offset:768 nt\n"
    "v_lshrrev_b32 v162, 1, v156\n"
    "v_and_b32 v162, 16, v162\n"
    "v_bfe_u32 v159, v156, 4, 1\n"
    "v_or_b32 v162, v162, v159\n"
    "v_and_or_b32 v162, v157, 2, v162\n"
    "v_mul_u32_u24 v162, 768, v162\n"
    "v_and_b32 v163, 0x70, v1\n"
    "v_add_u32 v162, v162, v163\n"
    "s_branch .Lf2e\n"
    ".Lf2b:\n"
    "v_and_b32 v159, 48, v156\n"
    "v_lshlrev_b32 v242, 2, v159\n"
    "v_lshl_add_u32 v243, v158, 4, v242\n"
    "v_and_b32 v159, 12, v156\n"
    "v_mul_u32_u24 v242, 384, v159\n"
    "v_add_u32 v242, v243, v242\n"
    "v_add_u32 v243, v243, v159\n"
    "s_mul_i32 s63, s53, 6144\n"
    "s_sub_u32 s63, s63, 49152\n"
    "s_add_u32 s63, s63, s67\n"
    "s_cmp_eq_u32 s59, 0\n"
    "s_cselect_b32 s63, 0x7f000000, s63\n"
    "s_add_u32 s75, s71, s63\n"
    "s_waitcnt lgkmcnt(0)\n"
    "s_add_u32 s44, s10, s73\n"
    "s_addc_u32 s45, s11, 0\n"
    "s_add_u32 s88, s12, s73\n"
    "s_addc_u32 s89, s13, 0\n"
    "buffer_load_dwordx4 v[4:7], v1, s[44:47], s35 offen offset:0\n"
    "buffer_load_dwordx4 v[8:11], v1, s[44:47], s35 offen offset:1024\n"
    "buffer_load_dwordx4 v[12:15], v1, s[44:47], s35 offen offset:2048\n"
    "buffer_load_dwordx4 v[16:19], v1, s[88:91], 0 offen offset:0\n"
    "s_add_u32 s40, s8, s72\n"
    "s_addc_u32 s41, s9, 0\n"
    "buffer_load_dwordx4 v[204:207], v242, s[40:43], s75 offen offset:0 nt\n"
    "buffer_load_dwordx4 v[208:211], v242, s[40:43], s75 offen offset:768 nt\n"
    "buffer_load_dwordx4 v[212:215], v242, s[40:43], s75 offen offset:256 nt\n"
    "buffer_load_dwordx4 v[216:219], v242, s[40:43], s75 offen offset:1024 nt\n"
    "buffer_load_dwordx4 v[220:223], v242, s[40:43], s75 offen offset:512 nt\n"
    "buffer_load_dwordx4 v[224:227], v242, s[40:43], s75 offen offset:1280 nt\n"
    "s_add_u32 s75, s71, s63\n"
    "s_add_u32 s75, s75, 98304\n"
    "buffer_load_dwordx4 v[36:39], v242, s[40:43], s75 offen offset:0 nt\n"
    "s_add_u32 s28, s6, s72\n"
    "s_addc_u32 s29, s7, 0\n"
    "v_lshrrev_b32 v162, 1, v156\n"
    "v_and_b32 v162, 16, v162\n"
    "v_bfe_u32 v159, v156, 4, 1\n"
    "v_or_b32 v162, v162, v159\n"
    "v_and_or_b32 v162, v157, 2, v162\n"
    "v_mul_u32_u24 v162, 768, v162\n"
    "v_and_b32 v163, 0x70, v1\n"
    "v_add_u32 v162, v162, v163\n"
    ".Lf2e:\n"
    "s_nop 4\n"
    "v_lshlrev_b32 v247, 2, v156\n"
    "v_add_u32 v247, 155648, v247\n"
    "v_mov_b32 v159, 0\n"
    "ds_write_b32 v247, v159\n"
    "v_mov_b32 v159, 0xff800000\n"
    "s_lshl2_add_u32 s74, s84, 155936\n"
    "v_mov_b32 v160, s74\n"
    "ds_write_b32 v160, v159\n"
    "s_mov_b32 s38, 5376\n"
    "s_mov_b32 s39, 0x20000\n"
    "s_mov_b32 s94, 4096\n"
    "s_mov_b32 s95, 0x20000\n"
    "s_mov_b32 s98, 4096\n"
    "s_mov_b32 s99, 0x20000\n"
    "s_mov_b32 s22, 743424\n"
    "s_mov_b32 s23, 0x20000\n"
    "s_mov_b32 s26, 743424\n"
    "s_mov_b32 s27, 0x20000\n"
    "s_lshl_b32 s80, s58, 7\n"
    "s_add_u32 s80, s80, 256\n"
    "s_mul_i32 s74, s52, 65536\n"
    "s_mul_i32 s69, s59, 384\n"
    "s_mul_i32 s57, s79, 6144\n"
    "s_add_u32 s57, s57, s69\n"
    "s_add_u32 s57, s57, s67\n"
    "s_mul_i32 s67, s82, 384\n"
    "s_add_u32 s66, s57, 24576\n"
    "s_add_u32 s36, s18, s74\n"
    "s_addc_u32 s37, s19, 0\n"
    "s_lshl_b32 s74, s52, 12\n"
    "s_add_u32 s92, s14, s74\n"
    "s_addc_u32 s93, s15, 0\n"
    "s_add_u32 s96, s16, s74\n"
    "s_addc_u32 s97, s17, 0\n"
    "v_lshlrev_b32 v128, 3, v156\n"
    "v_mov_b32 v129, 0\n"
    "v_lshlrev_b32 v130, 4, %3\n"
    "v_xor_b32 v130, 1024, v130\n"
    "buffer_load_dwordx2 v[124:125], v128, s[44:47], s67 offen offset:0\n"
    "buffer_load_dwordx2 v[126:127], v128, s[88:91], s67 offen offset:0\n"
    "buffer_load_dwordx2 v[238:239], v129, s[36:39], 0 offen offset:0 sc1\n"
    "s_mul_i32 s74, s52, 786432\n"
    "s_mul_i32 s73, s53, 6144\n"
    "s_add_u32 s74, s74, s73\n"
    "s_add_u32 s20, s4, s74\n"
    "s_addc_u32 s21, s5, 0\n"
    "s_add_u32 s24, s24, s74\n"
    "s_addc_u32 s25, s25, 0\n"
    "s_mul_i32 s72, s52, 1626112\n"
    "s_add_u32 s72, s72, 2097152\n"
    "s_add_u32 s16, s18, s72\n"
    "s_addc_u32 s17, s19, 0\n"
    "s_mov_b32 s18, 1622016\n"
    "s_mov_b32 s19, 0x20000\n"
    "s_mul_i32 s65, s84, 6144\n"
    "s_mul_i32 s85, s84, 49152\n"
    "s_getreg_b32 s10, hwreg(HW_REG_XCC_ID, 0, 1)\n"
    "s_lshl2_add_u32 s83, s10, s83\n"
    "s_cmp_eq_u32 s10, 1\n"
    "s_cselect_b32 s10, s85, 2.0\n"
    "s_waitcnt lgkmcnt(0)\n"
    "s_barrier\n"
    "s_add_u32 s73, s65, 49152\n"
    "v_add_u32 v131, s73, v1\n"
    "s_add_u32 s73, s73, s69\n"
    "v_add_u32 v132, s73, v163\n"
    "v_and_b32 v159, 32, v156\n"
    "v_lshlrev_b32 v244, 3, v156\n"
    "v_xor_b32 v244, v244, v159\n"
    "v_lshl_add_u32 v244, v159, 3, v244\n"
    "s_bitcmp1_b32 s83, 0\n"
    "s_cbranch_scc1 .Lf7b\n"
    "s_lshl_b32 s60, s53, 11\n"
    "s_add_u32 s61, s60, 12288\n"
    "s_lshl_b32 s73, s58, 8\n"
    "s_add_u32 s73, s73, 122880\n"
    "v_and_b32 v243, 0xf0, v1\n"
    "v_add_u32 v243, s73, v243\n"
    "s_and_b32 s73, s73, 0x1fd00\n"
    "v_lshlrev_b32 v160, 9, v157\n"
    "v_lshl_add_u32 v160, v158, 6, v160\n"
    "v_add_u32 v160, s73, v160\n"
    "s_lshl_b32 s73, s79, 8\n"
    "s_mul_i32 s74, s59, 6144\n"
    "s_add_u32 s73, s73, s74\n"
    "v_add_u32 v163, s73, v244\n"
    "v_and_b32 v159, 32, v156\n"
    "v_lshlrev_b32 v244, 2, v156\n"
    "v_xor_b32 v244, v244, v159\n"
    "v_mad_u32_u24 v244, v159, 12, v244\n"
    "s_sub_u32 s6, s28, 1572864\n"
    "s_subb_u32 s7, s29, 0\n"
    "buffer_load_dwordx2 v[40:41], v242, s[28:31], s75 offen offset:3072 nt\n"
    "buffer_load_dwordx2 v[42:43], v242, s[28:31], s75 offen offset:3840 nt\n"
    "v_add_u32 v159, 0, v156\n"
    "v_mul_u32_u24 v159, 2731, v159\n"
    "v_lshrrev_b32 v159, 16, v159\n"
    "v_xor_b32 v159, v159, v156\n"
    "v_and_b32 v159, 7, v159\n"
    "v_and_b32 v88, 0x380, v1\n"
    "v_lshl_add_u32 v88, v159, 4, v88\n"
    "v_add_u32 v159, 64, v156\n"
    "v_mul_u32_u24 v159, 2731, v159\n"
    "v_lshrrev_b32 v159, 16, v159\n"
    "v_xor_b32 v159, v159, v156\n"
    "v_and_b32 v159, 7, v159\n"
    "v_and_b32 v89, 0x380, v1\n"
    "v_lshl_add_u32 v89, v159, 4, v89\n"
    "v_add_u32 v159, 128, v156\n"
    "v_mul_u32_u24 v159, 2731, v159\n"
    "v_lshrrev_b32 v159, 16, v159\n"
    "v_xor_b32 v159, v159, v156\n"
    "v_and_b32 v159, 7, v159\n"
    "v_and_b32 v90, 0x380, v1\n"
    "v_lshl_add_u32 v90, v159, 4, v90\n"
    "v_and_b32 v2, 7, v156\n"
    "v_lshrrev_b32 v144, 7, v130\n"
    "v_mul_u32_u24 v144, 768, v144\n"
    "v_lshl_add_u32 v144, v2, 3, v144\n"
    "buffer_load_dwordx2 v[44:45], v242, s[28:31], s76 offen offset:0 nt\n"
    "buffer_load_dwordx2 v[46:47], v242, s[28:31], s76 offen offset:768 nt\n"
    "v_add_u32 v144, 131072, v144\n"
    "v_mad_u32_u24 v86, v2, 24, v144\n"
    "v_lshl_add_u32 v130, s82, 7, v130\n"
    "v_and_b32 v130, 4095, v130\n"
    "v_and_b32 v96, 15, v156\n"
    "v_lshrrev_b32 v159, 4, v156\n"
    "v_xor_b32 v159, v159, v2\n"
    "v_mul_u32_u24 v96, 384, v96\n"
    "v_lshl_add_u32 v96, v159, 4, v96\n"
    "s_add_u32 s73, s65, 49152\n"
    "v_add_u32 v96, s73, v96\n"
    "v_xor_b32 v97, 64, v96\n"
    "v_subrev_u32 v154, 24576, v96\n"
    "v_subrev_u32 v155, 24576, v97\n"
    "s_add_u32 s67, s65, 98304\n"
    "s_add_u32 s68, s65, 24576\n"
    "s_add_u32 s69, s65, 73728\n"
    "s_mov_b32 s81, 256\n"
    "s_mul_i32 s74, s79, 24576\n"
    "s_add_u32 s70, s74, s65\n"
    "s_add_u32 s70, s70, 73728\n"
    "s_sub_u32 s64, s65, s74\n"
    "s_add_u32 s64, s64, 98304\n"
    "v_and_b32 v249, 0x310, v1\n"
    "v_and_b32 v159, 12, v156\n"
    "v_lshl_add_u32 v249, v159, 3, v249\n"
    "buffer_load_dwordx2 v[48:49], v242, s[28:31], s76 offen offset:3072 nt\n"
    "buffer_load_dwordx2 v[50:51], v242, s[28:31], s76 offen offset:3840 nt\n"
    "v_and_b32 v159, 2, v156\n"
    "v_lshl_add_u32 v249, v159, 6, v249\n"
    "s_add_u32 s73, s74, 73728\n"
    "v_add_u32 v248, s73, v249\n"
    "s_sub_u32 s73, 98304, s74\n"
    "v_add_u32 v249, s73, v249\n"
    "s_or_b32 s73, s53, s84\n"
    "s_cmp_eq_u32 s73, 0\n"
    "s_cselect_b32 s82, 0, 0x7fff0000\n"
    "v_and_b32 v252, 12, v156\n"
    "v_bfe_u32 v159, v156, 1, 1\n"
    "v_or_b32 v252, v252, v159\n"
    "v_and_b32 v159, 1, v156\n"
    "v_lshl_or_b32 v252, v159, 1, v252\n"
    "v_lshlrev_b32 v252, 6, v252\n"
    "v_lshlrev_b32 v159, 2, v156\n"
    "v_and_b32 v159, 32, v159\n"
    "v_xor_b32 v159, v159, v156\n"
    "v_and_or_b32 v252, v159, 48, v252\n"
    "v_mov_b32 v2, v252\n"
    "v_add_u32 v3, 24576, v252\n"
    "v_lshrrev_b32 v91, 3, v156\n"
    "v_lshl_add_u32 v87, v91, 6, 64\n"
    "v_add_u32 v87, 64, v87\n"
    "v_and_b32 v241, 1, v156\n"
    "v_lshlrev_b32 v241, 3, v241\n"
    "buffer_load_dwordx2 v[52:53], v242, s[28:31], s77 offen offset:0 nt\n"
    "buffer_load_dwordx2 v[54:55], v242, s[28:31], s77 offen offset:768 nt\n"
    "v_lshl_add_u32 v241, v91, 4, v241\n"
    "v_add_u32 v241, 1152, v241\n"
    "v_lshlrev_b32 v91, 24, v91\n"
    "v_lshl_add_u32 v91, v156, 3, v91\n"
    "s_lshl_b32 s73, s53, 4\n"
    "s_lshl_b32 s74, s59, 3\n"
    "s_add_u32 s73, s73, s74\n"
    "s_add_u32 s73, s73, 1152\n"
    "v_mov_b32 v240, 0x7fff0000\n"
    "v_writelane_b32 v240, s73, 0\n"
    "v_and_b32 v159, 48, v156\n"
    "s_mul_i32 s73, s79, 768\n"
    "s_sub_u32 s73, 131072, s73\n"
    "v_lshlrev_b32 v250, 1, v159\n"
    "v_add_u32 v250, s73, v250\n"
    "v_and_b32 v251, 3, v156\n"
    "v_lshl_add_u32 v251, v251, 7, v159\n"
    "s_add_u32 s73, s73, 256\n"
    "v_add_u32 v251, s73, v251\n"
    "v_and_b32 v159, 64, v1\n"
    "v_add_u32 v251, v159, v251\n"
    "v_bfe_i32 v159, v156, 2, 1\n"
    "v_and_b32 v159, 0xaaaa, v159\n"
    "v_or_b32 v253, 0x4444, v159\n"
    "s_mov_b32 s58, 0\n"
    "s_mov_b32 s59, 8\n"
    "s_waitcnt vmcnt(31)\n"
    "v_pk_min_i16 v4, v4, v10\n"
    "v_pk_min_i16 v5, v5, v11\n"
    "v_pk_min_i16 v6, v6, v12\n"
    "v_pk_min_i16 v7, v7, v13\n"
    "v_pk_min_i16 v8, v8, v14\n"
    "v_pk_min_i16 v9, v9, v15\n"
    "v_pk_min_i16 v16, v16, v18\n"
    "v_pk_min_i16 v17, v17, v19\n"
    "v_pk_min_i16 v4, v4, v7\n"
    "v_pk_min_i16 v5, v5, v8\n"
    "v_pk_min_i16 v6, v6, v9\n"
    "v_pk_min_i16 v16, v16, v17\n"
    "v_pk_min_i16 v4, v4, v5\n"
    "v_lshlrev_b32 v17, 16, v16\n"
    "v_pk_min_i16 v4, v4, v6\n"
    "v_min_i32 v16, v16, v17\n"
    "v_lshlrev_b32 v5, 16, v4\n"
    "v_mov_b32 v7, 155920\n"
    "v_min_i32 v4, v4, v5\n"
    "v_lshl_add_u32 v6, s84, 2, v7\n"
    "v_min_i32_dpp v16, v16, v16 row_shr:1\n"
    "v_min_i32_dpp v4, v4, v4 row_shr:1\n"
    "s_nop 0\n"
    "v_min_i32_dpp v16, v16, v16 row_shr:2\n"
    "v_min_i32_dpp v4, v4, v4 row_shr:2\n"
    "s_nop 0\n"
    "v_min_i32_dpp v16, v16, v16 row_shr:4\n"
    "v_min_i32_dpp v4, v4, v4 row_shr:4\n"
    "s_nop 0\n"
    "v_min_i32_dpp v16, v16, v16 row_shr:8\n"
    "v_min_i32_dpp v4, v4, v4 row_shr:8\n"
    "s_nop 0\n"
    "v_min_i32_dpp v16, v16, v16 row_bcast:15 row_mask:0xa\n"
    "v_min_i32_dpp v4, v4, v4 row_bcast:15 row_mask:0xa\n"
    "s_nop 0\n"
    "v_min_i32_dpp v16, v16, v16 row_bcast:31 row_mask:0xc\n"
    "v_min_i32_dpp v4, v4, v4 row_bcast:31 row_mask:0xc\n"
    "v_readlane_b32 s35, v16, 63\n"
    "v_readlane_b32 s73, v4, 63\n"
    "s_max_i32 s35, s35, 0\n"
    "s_and_b32 s35, s35, 0xffff0000\n"
    "v_mov_b32 v4, s73\n"
    "ds_write_b32 v6, v4\n"
    "v_mov_b32 v14, -32\n"
    "v_mov_b32 v12, -126\n"
    "v_mov_b32 v13, 0x3dd53b95\n"
    "v_mov_b32 v98, 0\n"
    "s_waitcnt vmcnt(29)\n"
    "v_lshlrev_b32 v180, 16, v164\n"
    "v_and_b32 v181, 0xffff0000, v164\n"
    "v_lshlrev_b32 v182, 16, v168\n"
    "v_and_b32 v183, 0xffff0000, v168\n"
    "v_lshlrev_b32 v184, 16, v165\n"
    "v_and_b32 v185, 0xffff0000, v165\n"
    "v_lshlrev_b32 v186, 16, v169\n"
    "v_and_b32 v187, 0xffff0000, v169\n"
    "v_lshlrev_b32 v188, 16, v166\n"
    "v_and_b32 v189, 0xffff0000, v166\n"
    "v_lshlrev_b32 v190, 16, v170\n"
    "v_and_b32 v191, 0xffff0000, v170\n"
    "v_lshlrev_b32 v192, 16, v167\n"
    "v_and_b32 v193, 0xffff0000, v167\n"
    "v_lshlrev_b32 v194, 16, v171\n"
    "v_and_b32 v195, 0xffff0000, v171\n"
    "s_waitcnt lgkmcnt(0)\n"
    "s_barrier\n"
    "ds_read_b128 v[8:11], v7 offset:0\n"
    "buffer_load_dwordx2 v[56:57], v242, s[28:31], s77 offen offset:3072 nt\n"
    "buffer_load_dwordx2 v[58:59], v242, s[28:31], s77 offen offset:3840 nt\n"
    "buffer_load_dwordx4 v[136:139], v130, s[92:95], 0 offen offset:0\n"
    "buffer_load_dwordx4 v[140:143], v130, s[96:99], 0 offen offset:0\n"
    "s_nop 0\n"
    "s_nop 0\n"
    "s_nop 0\n"
    "s_nop 0\n"
    "s_nop 0\n"
    "s_nop 0\n"
    "s_nop 0\n"
    "s_nop 0\n"
    "s_nop 0\n"
    "s_nop 0\n"
    "s_nop 0\n"
    "s_nop 0\n"
    "s_nop 0\n"
    "s_nop 0\n"
    "s_nop 0\n"
    "s_nop 0\n"
    "s_nop 0\n"
    "s_nop 0\n"
    "s_nop 0\n"
    "s_nop 0\n"
    "s_nop 0\n"
    "s_nop 0\n"
    "s_nop 0\n"
    "s_nop 0\n"
    "s_nop 0\n"
    "s_nop 0\n"
    "s_nop 0\n"
    "s_nop 0\n"
    "s_nop 0\n"
    "s_nop 0\n"
    "s_nop 0\n"
    "s_nop 0\n"
    "s_nop 0\n"
    "s_nop 0\n"
    "s_nop 0\n"
    "s_nop 0\n"
    "s_nop 0\n"
    "s_nop 0\n"
    "s_nop 0\n"
    "s_nop 0\n"
    "s_nop 0\n"
    "s_nop 0\n"
    "s_nop 0\n"
    "s_nop 0\n"
    "s_nop 0\n"
    "s_nop 0\n"
    "s_nop 0\n"
    "s_nop 0\n"
    "s_nop 0\n"
    "s_nop 0\n"
    "s_nop 0\n"
    "s_nop 0\n"
    "s_nop 0\n"
    "s_nop 0\n"
    "s_nop 0\n"
    "s_nop 0\n"
    "s_nop 0\n"
    "s_nop 0\n"
    "s_nop 0\n"
    "s_nop 0\n"
    "s_waitcnt lgkmcnt(0)\n"
    "v_min3_i32 v4, v8, v9, v10\n"
    "v_min_i32 v4, v4, v11\n"
    "v_max_i32 v4, 0, v4\n"
    "v_and_b32 v4, 0xffff0000, v4\n"
    "v_mul_f32 v4, s35, v4\n"
    "v_mul_f32 v4, 0x3ed53b95, v4\n"
    "v_lshrrev_b32 v4, 23, v4\n"
    "v_sub_u32 v4, 2, v4\n"
    "v_med3_i32 v4, v4, v14, v12\n"
    "v_ldexp_f32 v13, v13, v4\n"
    "v_readfirstlane_b32 s89, v4\n"
    "v_readfirstlane_b32 s88, v13\n"
    "s_mul_i32 s89, s89, 0xff7fff80\n"
    "s_mov_b32 s90, 149\n"
    "v_mul_f32 v180, s88, v180\n"
    "v_mul_f32 v181, s88, v181\n"
    "v_mul_f32 v184, s88, v184\n"
    "v_mul_f32 v185, s88, v185\n"
    "v_mul_f32 v188, s88, v188\n"
    "v_mul_f32 v189, s88, v189\n"
    "v_mul_f32 v192, s88, v192\n"
    "v_mul_f32 v193, s88, v193\n"
    "v_fmaak_f32 v182, v182, v180, 0x80730000\n"
    "v_fmaak_f32 v183, v183, v181, 0x80730000\n"
    "v_fmaak_f32 v186, v186, v184, 0x80730000\n"
    "v_fmaak_f32 v187, v187, v185, 0x80730000\n"
    "v_fmaak_f32 v190, v190, v188, 0x80730000\n"
    "v_fmaak_f32 v191, v191, v189, 0x80730000\n"
    "v_fmaak_f32 v194, v194, v192, 0x80730000\n"
    "v_fmaak_f32 v195, v195, v193, 0x80730000\n"
    "v_ldexp_f32 v180, v180, s90\n"
    "v_ldexp_f32 v181, v181, s90\n"
    "v_ldexp_f32 v184, v184, s90\n"
    "v_ldexp_f32 v185, v185, s90\n"
    "v_ldexp_f32 v188, v188, s90\n"
    "v_ldexp_f32 v189, v189, s90\n"
    "v_ldexp_f32 v192, v192, s90\n"
    "v_ldexp_f32 v193, v193, s90\n"
    "v_fma_f32 v180, v180, 0, v180\n"
    "v_fma_f32 v181, v181, 0, v181\n"
    "v_fma_f32 v184, v184, 0, v184\n"
    "v_fma_f32 v185, v185, 0, v185\n"
    "v_fma_f32 v188, v188, 0, v188\n"
    "v_fma_f32 v189, v189, 0, v189\n"
    "v_fma_f32 v192, v192, 0, v192\n"
    "v_fma_f32 v193, v193, 0, v193\n"
    "ds_write_b128 v160, v[180:183] offset:0\n"
    "ds_write_b128 v160, v[184:187] offset:16\n"
    "ds_write_b128 v160, v[188:191] offset:32\n"
    "ds_write_b128 v160, v[192:195] offset:48\n"
    "s_waitcnt vmcnt(17)\n"
    "s_add_u32 s54, s71, s60\n"
    "s_add_u32 s55, s71, s61\n"
    "ds_read_b128 v[92:95], v243 offset:0\n"
    "s_waitcnt lgkmcnt(0)\n"
    "v_fma_f32 v204, v204, v92, -v94\n"
    "v_fma_f32 v205, v205, v93, -v95\n"
    "v_fma_f32 v206, v206, v92, -v94\n"
    "v_fma_f32 v207, v207, v93, -v95\n"
    "v_cvt_pk_bf16_f32 v204, v204, v205\n"
    "v_cvt_pk_bf16_f32 v206, v206, v207\n"
    "buffer_store_dword v204, v244, s[16:19], s54 offen offset:0 sc1\n"
    "buffer_store_dword v206, v244, s[16:19], s54 offen offset:128 sc1\n"
    "v_fma_f32 v208, v208, v92, -v94\n"
    "v_fma_f32 v209, v209, v93, -v95\n"
    "v_fma_f32 v210, v210, v92, -v94\n"
    "v_fma_f32 v211, v211, v93, -v95\n"
    "v_cvt_pk_bf16_f32 v208, v208, v209\n"
    "v_cvt_pk_bf16_f32 v210, v210, v211\n"
    "buffer_store_dword v208, v244, s[16:19], s54 offen offset:1024 sc1\n"
    "buffer_store_dword v210, v244, s[16:19], s54 offen offset:1152 sc1\n"
    "v_fma_f32 v212, v212, v92, -v94\n"
    "v_fma_f32 v213, v213, v93, -v95\n"
    "buffer_load_dwordx2 v[60:61], v242, s[28:31], s78 offen offset:0 nt\n"
    "buffer_load_dwordx2 v[62:63], v242, s[28:31], s78 offen offset:768 nt\n"
    "buffer_load_dwordx4 v[4:7], v162, s[28:31], s57 offen offset:0\n"
    "buffer_load_dwordx4 v[8:11], v162, s[28:31], s57 offen offset:3072\n"
    "v_fma_f32 v214, v214, v92, -v94\n"
    "v_fma_f32 v215, v215, v93, -v95\n"
    "v_cvt_pk_bf16_f32 v212, v212, v213\n"
    "v_cvt_pk_bf16_f32 v214, v214, v215\n"
    "buffer_store_dword v212, v244, s[16:19], s54 offen offset:256 sc1\n"
    "buffer_store_dword v214, v244, s[16:19], s54 offen offset:384 sc1\n"
    "v_fma_f32 v216, v216, v92, -v94\n"
    "v_fma_f32 v217, v217, v93, -v95\n"
    "v_fma_f32 v218, v218, v92, -v94\n"
    "v_fma_f32 v219, v219, v93, -v95\n"
    "v_cvt_pk_bf16_f32 v216, v216, v217\n"
    "v_cvt_pk_bf16_f32 v218, v218, v219\n"
    "buffer_store_dword v216, v244, s[16:19], s54 offen offset:1280 sc1\n"
    "buffer_store_dword v218, v244, s[16:19], s54 offen offset:1408 sc1\n"
    "v_fma_f32 v220, v220, v92, -v94\n"
    "v_fma_f32 v221, v221, v93, -v95\n"
    "v_fma_f32 v222, v222, v92, -v94\n"
    "v_fma_f32 v223, v223, v93, -v95\n"
    "v_cvt_pk_bf16_f32 v220, v220, v221\n"
    "v_cvt_pk_bf16_f32 v222, v222, v223\n"
    "buffer_store_dword v220, v244, s[16:19], s55 offen offset:0 sc1\n"
    "buffer_store_dword v222, v244, s[16:19], s55 offen offset:128 sc1\n"
    "v_fma_f32 v224, v224, v92, -v94\n"
    "buffer_load_dwordx2 v[64:65], v242, s[28:31], s78 offen offset:3072 nt\n"
    "buffer_load_dwordx2 v[66:67], v242, s[28:31], s78 offen offset:3840 nt\n"
    "buffer_load_dwordx4 v[12:15], v162, s[28:31], s57 offen offset:128\n"
    "v_fma_f32 v225, v225, v93, -v95\n"
    "v_fma_f32 v226, v226, v92, -v94\n"
    "v_fma_f32 v227, v227, v93, -v95\n"
    "v_cvt_pk_bf16_f32 v224, v224, v225\n"
    "v_cvt_pk_bf16_f32 v226, v226, v227\n"
    "buffer_store_dword v224, v244, s[16:19], s55 offen offset:1024 sc1\n"
    "buffer_store_dword v226, v244, s[16:19], s55 offen offset:1152 sc1\n"
    "v_fma_f32 v228, v228, v92, -v94\n"
    "v_fma_f32 v229, v229, v93, -v95\n"
    "v_fma_f32 v230, v230, v92, -v94\n"
    "v_fma_f32 v231, v231, v93, -v95\n"
    "v_cvt_pk_bf16_f32 v228, v228, v229\n"
    "v_cvt_pk_bf16_f32 v230, v230, v231\n"
    "buffer_store_dword v228, v244, s[16:19], s55 offen offset:256 sc1\n"
    "buffer_store_dword v230, v244, s[16:19], s55 offen offset:384 sc1\n"
    "v_fma_f32 v232, v232, v92, -v94\n"
    "v_fma_f32 v233, v233, v93, -v95\n"
    "v_fma_f32 v234, v234, v92, -v94\n"
    "v_fma_f32 v235, v235, v93, -v95\n"
    "v_cvt_pk_bf16_f32 v232, v232, v233\n"
    "v_cvt_pk_bf16_f32 v234, v234, v235\n"
    "buffer_store_dword v232, v244, s[16:19], s55 offen offset:1280 sc1\n"
    "buffer_store_dword v234, v244, s[16:19], s55 offen offset:1408 sc1\n"
    "v_add_u32 v243, 512, v243\n"
    "s_add_u32 s71, s71, 98304\n"
    "s_add_u32 m0, s65, 24576\n"
    "s_add_u32 s72, s85, 0\n"
    "buffer_load_dwordx4 v88, s[20:23], s72 offen offset:0 nt lds\n"
    "buffer_load_dwordx4 v[16:19], v162, s[28:31], s57 offen offset:3200\n"
    "buffer_load_dwordx4 v[20:23], v162, s[28:31], s57 offen offset:256\n"
    "s_waitcnt vmcnt(38)\n"
    "v_add_co_u32 v238, vcc, 0x7f4a7c15, v238\n"
    "v_mov_b32 v159, 0x9e3779b9\n"
    "s_nop 0\n"
    "v_addc_co_u32 v239, vcc, v159, v239, vcc\n"
    "v_mov_b32 v246, 1\n"
    "s_waitcnt vmcnt(39)\n"
    "v_lshlrev_b32 v180, 16, v124\n"
    "v_and_b32 v181, 0xffff0000, v124\n"
    "v_lshlrev_b32 v182, 16, v125\n"
    "v_and_b32 v183, 0xffff0000, v125\n"
    "v_lshlrev_b32 v184, 16, v126\n"
    "v_and_b32 v185, 0xffff0000, v126\n"
    "v_lshlrev_b32 v186, 16, v127\n"
    "v_and_b32 v187, 0xffff0000, v127\n"
    "v_mul_f32 v180, s88, v180\n"
    "v_mul_f32 v181, s88, v181\n"
    "v_mul_f32 v182, s88, v182\n"
    "v_mul_f32 v183, s88, v183\n"
    "v_fmaak_f32 v184, v184, v180, 0x80730000\n"
    "v_fmaak_f32 v185, v185, v181, 0x80730000\n"
    "v_fmaak_f32 v186, v186, v182, 0x80730000\n"
    "v_fmaak_f32 v187, v187, v183, 0x80730000\n"
    "s_add_u32 m0, s65, 25600\n"
    "s_add_u32 s72, s85, 1024\n"
    "buffer_load_dwordx4 v89, s[20:23], s72 offen offset:0 nt lds\n"
    "buffer_load_dwordx4 v[24:27], v162, s[28:31], s57 offen offset:3328\n"
    "v_ldexp_f32 v180, v180, s90\n"
    "v_ldexp_f32 v181, v181, s90\n"
    "v_ldexp_f32 v182, v182, s90\n"
    "v_ldexp_f32 v183, v183, s90\n"
    "v_fma_f32 v180, v180, 0, v180\n"
    "v_fma_f32 v181, v181, 0, v181\n"
    "v_fma_f32 v182, v182, 0, v182\n"
    "v_fma_f32 v183, v183, 0, v183\n"
    "ds_write_b128 v131, v[180:183] offset:0\n"
    "ds_write_b128 v131, v[184:187] offset:1024\n"
    "ds_read_b128 v[180:183], v132 offset:0\n"
    "ds_read_b128 v[184:187], v132 offset:1024\n"
    "ds_read_b128 v[188:191], v132 offset:128\n"
    "ds_read_b128 v[192:195], v132 offset:1152\n"
    "ds_read_b128 v[196:199], v132 offset:256\n"
    "ds_read_b128 v[200:203], v132 offset:1280\n"
    "s_nop 0\n"
    "s_nop 0\n"
    "s_nop 0\n"
    "s_nop 0\n"
    "s_nop 0\n"
    "s_nop 0\n"
    "s_nop 0\n"
    "s_add_u32 m0, s65, 26624\n"
    "s_add_u32 s72, s85, 2048\n"
    "buffer_load_dwordx4 v90, s[20:23], s72 offen offset:0 nt lds\n"
    "buffer_load_dwordx4 v[100:103], v162, s[28:31], s66 offen offset:0\n"
    "buffer_load_dwordx4 v[104:107], v162, s[28:31], s66 offen offset:3072\n"
    "s_nop 0\n"
    "s_nop 0\n"
    "s_nop 0\n"
    "s_nop 0\n"
    "s_nop 0\n"
    "s_nop 0\n"
    "s_nop 0\n"
    "s_nop 0\n"
    "s_nop 0\n"
    "s_nop 0\n"
    "s_nop 0\n"
    "s_nop 0\n"
    "s_nop 0\n"
    "s_nop 0\n"
    "s_nop 0\n"
    "s_nop 0\n"
    "s_nop 0\n"
    "s_nop 0\n"
    "s_nop 0\n"
    "s_nop 0\n"
    "s_nop 0\n"
    "s_nop 0\n"
    "s_add_u32 m0, s65, 27648\n"
    "s_add_u32 s72, s85, 3072\n"
    "buffer_load_dwordx4 v88, s[20:23], s72 offen offset:0 nt lds\n"
    "buffer_load_dwordx4 v[108:111], v162, s[28:31], s66 offen offset:128\n"
    "s_nop 0\n"
    "s_nop 0\n"
    "s_nop 0\n"
    "s_nop 0\n"
    "s_nop 0\n"
    "s_nop 0\n"
    "s_nop 0\n"
    "s_nop 0\n"
    "s_nop 0\n"
    "s_nop 0\n"
    "s_nop 0\n"
    "s_nop 0\n"
    "s_nop 0\n"
    "s_nop 0\n"
    "s_nop 0\n"
    "s_nop 0\n"
    "s_nop 0\n"
    "s_nop 0\n"
    "s_nop 0\n"
    "s_nop 0\n"
    "s_nop 0\n"
    "s_nop 0\n"
    "s_nop 0\n"
    "s_waitcnt lgkmcnt(0)\n"
    "s_waitcnt vmcnt(10)\n"
    "ds_add_rtn_u32 v245, v247, v246\n"
    "s_lshl_b32 s73, s53, 6\n"
    "s_add_u32 s73, s73, 128\n"
    "s_waitcnt lgkmcnt(0)\n"
    "v_readfirstlane_b32 s74, v245\n"
    "s_nop 0\n"
    "s_cmp_eq_u32 s74, 3\n"
    "s_cselect_b32 s73, s73, 0x7fff0000\n"
    "buffer_store_dwordx2 v[238:239], v91, s[36:39], s73 offen offset:0 sc1\n"
    "s_waitcnt vmcnt(18)\n"
    "s_add_u32 m0, s65, 28672\n"
    "s_add_u32 s72, s85, 4096\n"
    "buffer_load_dwordx4 v89, s[20:23], s72 offen offset:0 nt lds\n"
    "buffer_load_dwordx4 v[112:115], v162, s[28:31], s66 offen offset:3200\n"
    "buffer_load_dwordx4 v[116:119], v162, s[28:31], s66 offen offset:256\n"
    "s_add_u32 s54, s71, s60\n"
    "s_add_u32 s55, s71, s61\n"
    "ds_read_b128 v[92:95], v243 offset:0\n"
    "s_waitcnt lgkmcnt(0)\n"
    "v_fma_f32 v36, v36, v92, -v94\n"
    "v_fma_f32 v37, v37, v93, -v95\n"
    "v_fma_f32 v38, v38, v92, -v94\n"
    "v_fma_f32 v39, v39, v93, -v95\n"
    "v_cvt_pk_bf16_f32 v36, v36, v37\n"
    "v_cvt_pk_bf16_f32 v38, v38, v39\n"
    "buffer_store_dword v36, v244, s[16:19], s54 offen offset:0 sc1\n"
    "buffer_store_dword v38, v244, s[16:19], s54 offen offset:128 sc1\n"
    "v_fma_f32 v40, v40, v92, -v94\n"
    "v_fma_f32 v41, v41, v93, -v95\n"
    "v_fma_f32 v42, v42, v92, -v94\n"
    "v_fma_f32 v43, v43, v93, -v95\n"
    "v_cvt_pk_bf16_f32 v40, v40, v41\n"
    "v_cvt_pk_bf16_f32 v42, v42, v43\n"
    "buffer_store_dword v40, v244, s[16:19], s54 offen offset:1024 sc1\n"
    "buffer_store_dword v42, v244, s[16:19], s54 offen offset:1152 sc1\n"
    "v_fma_f32 v44, v44, v92, -v94\n"
    "v_fma_f32 v45, v45, v93, -v95\n"
    "v_fma_f32 v46, v46, v92, -v94\n"
    "v_fma_f32 v47, v47, v93, -v95\n"
    "v_cvt_pk_bf16_f32 v44, v44, v45\n"
    "v_cvt_pk_bf16_f32 v46, v46, v47\n"
    "buffer_store_dword v44, v244, s[16:19], s54 offen offset:256 sc1\n"
    "buffer_store_dword v46, v244, s[16:19], s54 offen offset:384 sc1\n"
    "v_fma_f32 v48, v48, v92, -v94\n"
    "v_fma_f32 v49, v49, v93, -v95\n"
    "v_fma_f32 v50, v50, v92, -v94\n"
    "v_fma_f32 v51, v51, v93, -v95\n"
    "v_cvt_pk_bf16_f32 v48, v48, v49\n"
    "v_cvt_pk_bf16_f32 v50, v50, v51\n"
    "s_add_u32 m0, s65, 29696\n"
    "s_add_u32 s72, s85, 5120\n"
    "buffer_load_dwordx4 v90, s[20:23], s72 offen offset:0 nt lds\n"
    "buffer_load_dwordx4 v[120:123], v162, s[28:31], s66 offen offset:3328\n"
    "buffer_store_dword v48, v244, s[16:19], s54 offen offset:1280 sc1\n"
    "buffer_store_dword v50, v244, s[16:19], s54 offen offset:1408 sc1\n"
    "v_fma_f32 v52, v52, v92, -v94\n"
    "v_fma_f32 v53, v53, v93, -v95\n"
    "v_fma_f32 v54, v54, v92, -v94\n"
    "v_fma_f32 v55, v55, v93, -v95\n"
    "v_cvt_pk_bf16_f32 v52, v52, v53\n"
    "v_cvt_pk_bf16_f32 v54, v54, v55\n"
    "buffer_store_dword v52, v244, s[16:19], s55 offen offset:0 sc1\n"
    "buffer_store_dword v54, v244, s[16:19], s55 offen offset:128 sc1\n"
    "v_fma_f32 v56, v56, v92, -v94\n"
    "v_fma_f32 v57, v57, v93, -v95\n"
    "v_fma_f32 v58, v58, v92, -v94\n"
    "v_fma_f32 v59, v59, v93, -v95\n"
    "v_cvt_pk_bf16_f32 v56, v56, v57\n"
    "v_cvt_pk_bf16_f32 v58, v58, v59\n"
    "buffer_store_dword v56, v244, s[16:19], s55 offen offset:1024 sc1\n"
    "buffer_store_dword v58, v244, s[16:19], s55 offen offset:1152 sc1\n"
    "v_fma_f32 v60, v60, v92, -v94\n"
    "v_fma_f32 v61, v61, v93, -v95\n"
    "v_fma_f32 v62, v62, v92, -v94\n"
    "v_fma_f32 v63, v63, v93, -v95\n"
    "v_cvt_pk_bf16_f32 v60, v60, v61\n"
    "v_cvt_pk_bf16_f32 v62, v62, v63\n"
    "buffer_store_dword v60, v244, s[16:19], s55 offen offset:256 sc1\n"
    "buffer_store_dword v62, v244, s[16:19], s55 offen offset:384 sc1\n"
    "v_fma_f32 v64, v64, v92, -v94\n"
    "v_fma_f32 v65, v65, v93, -v95\n"
    "v_fma_f32 v66, v66, v92, -v94\n"
    "v_fma_f32 v67, v67, v93, -v95\n"
    "v_cvt_pk_bf16_f32 v64, v64, v65\n"
    "v_cvt_pk_bf16_f32 v66, v66, v67\n"
    "buffer_store_dword v64, v244, s[16:19], s55 offen offset:1280 sc1\n"
    "buffer_store_dword v66, v244, s[16:19], s55 offen offset:1408 sc1\n"
    "v_add_u32 v243, 512, v243\n"
    "s_add_u32 m0, s65, 73728\n"
    "s_add_u32 s72, s10, 196608\n"
    "buffer_load_dwordx4 v88, s[20:23], s72 offen offset:0 nt lds\n"
    "s_waitcnt vmcnt(56)\n"
    "v_lshlrev_b32 v204, 16, v136\n"
    "v_and_b32 v205, 0xffff0000, v136\n"
    "v_lshlrev_b32 v206, 16, v137\n"
    "v_and_b32 v207, 0xffff0000, v137\n"
    "v_lshlrev_b32 v208, 16, v138\n"
    "v_and_b32 v209, 0xffff0000, v138\n"
    "v_lshlrev_b32 v210, 16, v139\n"
    "v_and_b32 v211, 0xffff0000, v139\n"
    "v_log_f32 v68, v204\n"
    "v_log_f32 v69, v205\n"
    "v_log_f32 v70, v206\n"
    "v_log_f32 v71, v207\n"
    "v_log_f32 v72, v208\n"
    "v_log_f32 v73, v209\n"
    "v_log_f32 v74, v210\n"
    "v_log_f32 v75, v211\n"
    "v_rcp_f32 v204, v204\n"
    "v_rcp_f32 v205, v205\n"
    "v_rcp_f32 v206, v206\n"
    "s_add_u32 m0, s65, 74752\n"
    "s_add_u32 s72, s10, 197632\n"
    "buffer_load_dwordx4 v89, s[20:23], s72 offen offset:0 nt lds\n"
    "v_rcp_f32 v207, v207\n"
    "v_rcp_f32 v208, v208\n"
    "v_rcp_f32 v209, v209\n"
    "v_rcp_f32 v210, v210\n"
    "v_rcp_f32 v211, v211\n"
    "s_mov_b32 s73, 0x41a00000\n"
    "v_mov_b32 v228, 0x7fc00000\n"
    "v_cmp_le_f32 vcc, |v68|, s73\n"
    "s_nop 1\n"
    "v_cndmask_b32 v68, v228, v68, vcc\n"
    "v_cmp_le_f32 vcc, |v69|, s73\n"
    "s_nop 1\n"
    "v_cndmask_b32 v69, v228, v69, vcc\n"
    "v_cmp_le_f32 vcc, |v70|, s73\n"
    "s_nop 1\n"
    "v_cndmask_b32 v70, v228, v70, vcc\n"
    "v_cmp_le_f32 vcc, |v71|, s73\n"
    "s_nop 1\n"
    "v_cndmask_b32 v71, v228, v71, vcc\n"
    "v_cmp_le_f32 vcc, |v72|, s73\n"
    "s_nop 1\n"
    "v_cndmask_b32 v72, v228, v72, vcc\n"
    "v_cmp_le_f32 vcc, |v73|, s73\n"
    "s_nop 1\n"
    "v_cndmask_b32 v73, v228, v73, vcc\n"
    "v_cmp_le_f32 vcc, |v74|, s73\n"
    "s_nop 1\n"
    "v_cndmask_b32 v74, v228, v74, vcc\n"
    "s_add_u32 m0, s65, 75776\n"
    "s_add_u32 s72, s10, 198656\n"
    "buffer_load_dwordx4 v90, s[20:23], s72 offen offset:0 nt lds\n"
    "v_cmp_le_f32 vcc, |v75|, s73\n"
    "s_nop 1\n"
    "v_cndmask_b32 v75, v228, v75, vcc\n"
    "v_cvt_pk_bf16_f32 v220, v204, v205\n"
    "v_cvt_pk_bf16_f32 v221, v206, v207\n"
    "v_cvt_pk_bf16_f32 v222, v208, v209\n"
    "v_cvt_pk_bf16_f32 v223, v210, v211\n"
    "v_lshlrev_b32 v212, 16, v220\n"
    "v_and_b32 v213, 0xffff0000, v220\n"
    "v_lshlrev_b32 v214, 16, v221\n"
    "v_and_b32 v215, 0xffff0000, v221\n"
    "v_lshlrev_b32 v216, 16, v222\n"
    "v_and_b32 v217, 0xffff0000, v222\n"
    "v_lshlrev_b32 v218, 16, v223\n"
    "v_and_b32 v219, 0xffff0000, v223\n"
    "v_sub_f32 v204, v204, v212\n"
    "v_sub_f32 v205, v205, v213\n"
    "v_sub_f32 v206, v206, v214\n"
    "v_sub_f32 v207, v207, v215\n"
    "v_sub_f32 v208, v208, v216\n"
    "v_sub_f32 v209, v209, v217\n"
    "s_add_u32 m0, s65, 76800\n"
    "s_add_u32 s72, s10, 199680\n"
    "buffer_load_dwordx4 v88, s[20:23], s72 offen offset:0 nt lds\n"
    "v_sub_f32 v210, v210, v218\n"
    "v_sub_f32 v211, v211, v219\n"
    "v_cvt_pk_bf16_f32 v224, v204, v205\n"
    "v_cvt_pk_bf16_f32 v225, v206, v207\n"
    "v_cvt_pk_bf16_f32 v226, v208, v209\n"
    "v_cvt_pk_bf16_f32 v227, v210, v211\n"
    "v_mov_b32 v204, 0\n"
    "v_mov_b32 v205, 0\n"
    "v_mov_b32 v206, 0\n"
    "v_mov_b32 v207, 0\n"
    "v_swap_b32 v221, v222\n"
    "v_swap_b32 v225, v226\n"
    "v_swap_b32 v141, v142\n"
    "ds_write2_b64 v144, v[220:221], v[222:223] offset0:32 offset1:40\n"
    "ds_write2_b64 v144, v[224:225], v[226:227] offset0:48 offset1:56\n"
    "ds_write2_b64 v144, v[140:141], v[142:143] offset0:64 offset1:72\n"
    "ds_write2_b64 v144, v[204:205], v[206:207] offset0:80 offset1:88\n"
    "s_waitcnt vmcnt(31)\n"
    "v_fma_f32 v4, v4, v180, -v184\n"
    "v_fma_f32 v5, v5, v181, -v185\n"
    "v_fma_f32 v6, v6, v182, -v186\n"
    "s_add_u32 m0, s65, 77824\n"
    "s_add_u32 s72, s10, 200704\n"
    "buffer_load_dwordx4 v89, s[20:23], s72 offen offset:0 nt lds\n"
    "v_fma_f32 v7, v7, v183, -v187\n"
    "v_fma_f32 v8, v8, v180, -v184\n"
    "v_fma_f32 v9, v9, v181, -v185\n"
    "v_fma_f32 v10, v10, v182, -v186\n"
    "v_fma_f32 v11, v11, v183, -v187\n"
    "v_cvt_pk_bf16_f32 v4, v4, v5\n"
    "v_cvt_pk_bf16_f32 v5, v6, v7\n"
    "v_cvt_pk_bf16_f32 v6, v8, v9\n"
    "v_cvt_pk_bf16_f32 v7, v10, v11\n"
    "ds_write_b64 v163, v[4:5] offset:0\n"
    "ds_write_b64 v163, v[6:7] offset:1024\n"
    "v_fma_f32 v12, v12, v188, -v192\n"
    "v_fma_f32 v13, v13, v189, -v193\n"
    "v_fma_f32 v14, v14, v190, -v194\n"
    "v_fma_f32 v15, v15, v191, -v195\n"
    "v_fma_f32 v16, v16, v188, -v192\n"
    "v_fma_f32 v17, v17, v189, -v193\n"
    "v_fma_f32 v18, v18, v190, -v194\n"
    "v_fma_f32 v19, v19, v191, -v195\n"
    "v_cvt_pk_bf16_f32 v12, v12, v13\n"
    "s_add_u32 m0, s65, 78848\n"
    "s_add_u32 s72, s10, 201728\n"
    "buffer_load_dwordx4 v90, s[20:23], s72 offen offset:0 nt lds\n"
    "v_cvt_pk_bf16_f32 v13, v14, v15\n"
    "v_cvt_pk_bf16_f32 v14, v16, v17\n"
    "v_cvt_pk_bf16_f32 v15, v18, v19\n"
    "ds_write_b64 v163, v[12:13] offset:2048\n"
    "ds_write_b64 v163, v[14:15] offset:3072\n"
    "v_fma_f32 v20, v20, v196, -v200\n"
    "v_fma_f32 v21, v21, v197, -v201\n"
    "v_fma_f32 v22, v22, v198, -v202\n"
    "v_fma_f32 v23, v23, v199, -v203\n"
    "v_fma_f32 v24, v24, v196, -v200\n"
    "v_fma_f32 v25, v25, v197, -v201\n"
    "v_fma_f32 v26, v26, v198, -v202\n"
    "v_fma_f32 v27, v27, v199, -v203\n"
    "v_cvt_pk_bf16_f32 v20, v20, v21\n"
    "v_cvt_pk_bf16_f32 v21, v22, v23\n"
    "v_cvt_pk_bf16_f32 v22, v24, v25\n"
    "v_cvt_pk_bf16_f32 v23, v26, v27\n"
    "ds_write_b64 v163, v[20:21] offset:4096\n"
    "ds_write_b64 v163, v[22:23] offset:5120\n"
    "s_waitcnt vmcnt(16)\n"
    "v_fma_f32 v100, v100, v180, -v184\n"
    "s_add_u32 m0, s65, 49152\n"
    "s_add_u32 s72, s10, 393216\n"
    "buffer_load_dwordx4 v88, s[20:23], s72 offen offset:0 nt lds\n"
    "v_fma_f32 v101, v101, v181, -v185\n"
    "v_fma_f32 v102, v102, v182, -v186\n"
    "v_fma_f32 v103, v103, v183, -v187\n"
    "v_fma_f32 v104, v104, v180, -v184\n"
    "v_fma_f32 v105, v105, v181, -v185\n"
    "v_fma_f32 v106, v106, v182, -v186\n"
    "v_fma_f32 v107, v107, v183, -v187\n"
    "v_cvt_pk_bf16_f32 v100, v100, v101\n"
    "v_cvt_pk_bf16_f32 v101, v102, v103\n"
    "v_cvt_pk_bf16_f32 v102, v104, v105\n"
    "v_cvt_pk_bf16_f32 v103, v106, v107\n"
    "ds_write_b64 v163, v[100:101] offset:12288\n"
    "ds_write_b64 v163, v[102:103] offset:13312\n"
    "v_fma_f32 v108, v108, v188, -v192\n"
    "v_fma_f32 v109, v109, v189, -v193\n"
    "v_fma_f32 v110, v110, v190, -v194\n"
    "v_fma_f32 v111, v111, v191, -v195\n"
    "v_fma_f32 v112, v112, v188, -v192\n"
    "v_fma_f32 v113, v113, v189, -v193\n"
    "v_fma_f32 v114, v114, v190, -v194\n"
    "s_add_u32 m0, s65, 50176\n"
    "s_add_u32 s72, s10, 394240\n"
    "buffer_load_dwordx4 v89, s[20:23], s72 offen offset:0 nt lds\n"
    "v_fma_f32 v115, v115, v191, -v195\n"
    "v_cvt_pk_bf16_f32 v108, v108, v109\n"
    "v_cvt_pk_bf16_f32 v109, v110, v111\n"
    "v_cvt_pk_bf16_f32 v110, v112, v113\n"
    "v_cvt_pk_bf16_f32 v111, v114, v115\n"
    "ds_write_b64 v163, v[108:109] offset:14336\n"
    "ds_write_b64 v163, v[110:111] offset:15360\n"
    "v_fma_f32 v116, v116, v196, -v200\n"
    "v_fma_f32 v117, v117, v197, -v201\n"
    "v_fma_f32 v118, v118, v198, -v202\n"
    "v_fma_f32 v119, v119, v199, -v203\n"
    "v_fma_f32 v120, v120, v196, -v200\n"
    "v_fma_f32 v121, v121, v197, -v201\n"
    "v_fma_f32 v122, v122, v198, -v202\n"
    "v_fma_f32 v123, v123, v199, -v203\n"
    "v_cvt_pk_bf16_f32 v116, v116, v117\n"
    "v_cvt_pk_bf16_f32 v117, v118, v119\n"
    "v_cvt_pk_bf16_f32 v118, v120, v121\n"
    "v_cvt_pk_bf16_f32 v119, v122, v123\n"
    "ds_write_b64 v163, v[116:117] offset:16384\n"
    "ds_write_b64 v163, v[118:119] offset:17408\n"
    "s_waitcnt vmcnt(19)\n"
    "ds_read_b128 v[36:39], v154 offset:0\n"
    "ds_read_b128 v[40:43], v155 offset:0\n"
    "ds_read_b128 v[44:47], v154 offset:128\n"
    "ds_read_b128 v[48:51], v155 offset:128\n"
    "ds_read_b128 v[52:55], v154 offset:256\n"
    "ds_read_b128 v[56:59], v155 offset:256\n"
    "s_waitcnt lgkmcnt(0)\n"
    "s_barrier\n"
    "ds_read_b128 a[112:115], v252 offset:0\n"
    "ds_read_b128 a[116:119], v252 offset:2048\n"
    "ds_read_b128 a[120:123], v252 offset:4096\n"
    "ds_read_b128 a[124:127], v252 offset:6144\n"
    "ds_read_b128 a[128:131], v252 offset:8192\n"
    "ds_read_b128 a[132:135], v252 offset:10240\n"
    "ds_read_b128 a[136:139], v252 offset:1024\n"
    "ds_read_b128 a[140:143], v252 offset:3072\n"
    "ds_read_b128 a[144:147], v252 offset:5120\n"
    "ds_read_b128 a[148:151], v252 offset:7168\n"
    "ds_read_b128 a[152:155], v252 offset:9216\n"
    "ds_read_b128 a[156:159], v252 offset:11264\n"
    "ds_read_b128 a[0:3], v252 offset:12288\n"
    "ds_read_b128 a[4:7], v252 offset:14336\n"
    "ds_read_b128 a[8:11], v252 offset:16384\n"
    "ds_read_b128 a[12:15], v252 offset:18432\n"
    "ds_read_b128 a[16:19], v252 offset:20480\n"
    "ds_read_b128 a[20:23], v252 offset:22528\n"
    "ds_read_b128 a[24:27], v252 offset:13312\n"
    "ds_read_b128 a[28:31], v252 offset:15360\n"
    "ds_read_b128 a[32:35], v252 offset:17408\n"
    "ds_read_b128 a[36:39], v252 offset:19456\n"
    "ds_read_b128 a[40:43], v252 offset:21504\n"
    "ds_read_b128 a[44:47], v252 offset:23552\n"
    "s_add_u32 m0, s65, 51200\n"
    "s_add_u32 s72, s10, 395264\n"
    "buffer_load_dwordx4 v90, s[20:23], s72 offen offset:0 nt lds\n"
    "s_add_u32 m0, s65, 52224\n"
    "s_add_u32 s72, s10, 396288\n"
    "buffer_load_dwordx4 v88, s[20:23], s72 offen offset:0 nt lds\n"
    "v_pk_maximum3_f16 v98, v36, v37, v98\n"
    "v_pk_maximum3_f16 v98, v38, v39, v98\n"
    "v_pk_add_u16 v36, s89, v36 clamp\n"
    "v_pk_add_u16 v37, s89, v37 clamp\n"
    "v_pk_add_u16 v38, s89, v38 clamp\n"
    "v_pk_add_u16 v39, s89, v39 clamp\n"
    "v_pk_maximum3_f16 v98, v40, v41, v98\n"
    "v_pk_maximum3_f16 v98, v42, v43, v98\n"
    "v_pk_add_u16 v40, s89, v40 clamp\n"
    "v_pk_add_u16 v41, s89, v41 clamp\n"
    "v_pk_add_u16 v42, s89, v42 clamp\n"
    "v_pk_add_u16 v43, s89, v43 clamp\n"
    "v_pk_maximum3_f16 v98, v44, v45, v98\n"
    "v_pk_maximum3_f16 v98, v46, v47, v98\n"
    "v_pk_add_u16 v44, s89, v44 clamp\n"
    "v_pk_add_u16 v45, s89, v45 clamp\n"
    "v_pk_add_u16 v46, s89, v46 clamp\n"
    "v_pk_add_u16 v47, s89, v47 clamp\n"
    "v_pk_maximum3_f16 v98, v48, v49, v98\n"
    "v_pk_maximum3_f16 v98, v50, v51, v98\n"
    "v_pk_add_u16 v48, s89, v48 clamp\n"
    "v_pk_add_u16 v49, s89, v49 clamp\n"
    "v_pk_add_u16 v50, s89, v50 clamp\n"
    "v_pk_add_u16 v51, s89, v51 clamp\n"
    "v_pk_maximum3_f16 v98, v52, v53, v98\n"
    "v_pk_maximum3_f16 v98, v54, v55, v98\n"
    "v_pk_add_u16 v52, s89, v52 clamp\n"
    "v_pk_add_u16 v53, s89, v53 clamp\n"
    "v_pk_add_u16 v54, s89, v54 clamp\n"
    "v_pk_add_u16 v55, s89, v55 clamp\n"
    "v_pk_maximum3_f16 v98, v56, v57, v98\n"
    "v_pk_maximum3_f16 v98, v58, v59, v98\n"
    "v_pk_add_u16 v56, s89, v56 clamp\n"
    "v_pk_add_u16 v57, s89, v57 clamp\n"
    "v_pk_add_u16 v58, s89, v58 clamp\n"
    "v_pk_add_u16 v59, s89, v59 clamp\n"
    "v_mov_b32 v108, 0\n"
    "v_mov_b32 v109, 0\n"
    "v_mov_b32 v110, 0\n"
    "v_mov_b32 v111, 0\n"
    "v_mov_b32 v112, 0\n"
    "v_mov_b32 v113, 0\n"
    "v_mov_b32 v114, 0\n"
    "v_mov_b32 v115, 0\n"
    "v_mov_b32 v116, 0\n"
    "v_mov_b32 v117, 0\n"
    "v_mov_b32 v118, 0\n"
    "v_mov_b32 v119, 0\n"
    "v_mov_b32 v120, 0\n"
    "v_mov_b32 v121, 0\n"
    "v_mov_b32 v122, 0\n"
    "v_mov_b32 v123, 0\n"
    "s_mul_i32 s73, s79, 768\n"
    "v_add_u32 v78, s73, v250\n"
    "v_mov_b32 v79, 155936\n"
    "s_waitcnt lgkmcnt(15)\n"
    "v_mfma_f32_16x16x32_bf16 v[4:7], a[112:115], v[36:39], 0\n"
    "s_waitcnt lgkmcnt(15)\n"
    "v_mfma_f32_16x16x32_bf16 v[4:7], a[116:119], v[40:43], v[4:7]\n"
    "s_waitcnt lgkmcnt(15)\n"
    "v_mfma_f32_16x16x32_bf16 v[4:7], a[120:123], v[44:47], v[4:7]\n"
    "s_waitcnt lgkmcnt(15)\n"
    "v_mfma_f32_16x16x32_bf16 v[4:7], a[124:127], v[48:51], v[4:7]\n"
    "s_waitcnt lgkmcnt(15)\n"
    "v_mfma_f32_16x16x32_bf16 v[4:7], a[128:131], v[52:55], v[4:7]\n"
    "s_waitcnt lgkmcnt(15)\n"
    "v_mfma_f32_16x16x32_bf16 v[4:7], a[132:135], v[56:59], v[4:7]\n"
    "s_waitcnt lgkmcnt(15)\n"
    "v_mfma_f32_16x16x32_bf16 v[20:23], a[136:139], v[36:39], 0\n"
    "v_accvgpr_write_b32 a192, v36\n"
    "v_accvgpr_write_b32 a193, v37\n"
    "v_accvgpr_write_b32 a194, v38\n"
    "v_accvgpr_write_b32 a195, v39\n"
    "s_waitcnt lgkmcnt(15)\n"
    "v_mfma_f32_16x16x32_bf16 v[20:23], a[140:143], v[40:43], v[20:23]\n"
    "v_accvgpr_write_b32 a196, v40\n"
    "v_accvgpr_write_b32 a197, v41\n"
    "v_accvgpr_write_b32 a198, v42\n"
    "v_accvgpr_write_b32 a199, v43\n"
    "s_waitcnt lgkmcnt(15)\n"
    "v_mfma_f32_16x16x32_bf16 v[20:23], a[144:147], v[44:47], v[20:23]\n"
    "v_accvgpr_write_b32 a200, v44\n"
    "v_accvgpr_write_b32 a201, v45\n"
    "v_accvgpr_write_b32 a202, v46\n"
    "v_accvgpr_write_b32 a203, v47\n"
    "s_waitcnt lgkmcnt(14)\n"
    "v_mfma_f32_16x16x32_bf16 v[20:23], a[148:151], v[48:51], v[20:23]\n"
    "v_accvgpr_write_b32 a204, v48\n"
    "v_accvgpr_write_b32 a205, v49\n"
    "v_accvgpr_write_b32 a206, v50\n"
    "v_accvgpr_write_b32 a207, v51\n"
    "s_waitcnt lgkmcnt(13)\n"
    "v_mfma_f32_16x16x32_bf16 v[20:23], a[152:155], v[52:55], v[20:23]\n"
    "v_accvgpr_write_b32 a208, v52\n"
    "v_accvgpr_write_b32 a209, v53\n"
    "v_accvgpr_write_b32 a210, v54\n"
    "v_accvgpr_write_b32 a211, v55\n"
    "s_waitcnt lgkmcnt(12)\n"
    "v_mfma_f32_16x16x32_bf16 v[20:23], a[156:159], v[56:59], v[20:23]\n"
    "v_accvgpr_write_b32 a212, v56\n"
    "v_accvgpr_write_b32 a213, v57\n"
    "v_accvgpr_write_b32 a214, v58\n"
    "v_accvgpr_write_b32 a215, v59\n"
    "s_add_u32 m0, s65, 53248\n"
    "s_add_u32 s72, s10, 397312\n"
    "buffer_load_dwordx4 v89, s[20:23], s72 offen offset:0 nt lds\n"
    "buffer_load_dwordx2 v[236:237], v87, s[36:39], 0 offen offset:0 sc1\n"
    "ds_read_b128 v[16:19], v78 offset:0\n"
    "ds_read_b128 v[32:35], v78 offset:16\n"
    "ds_read_b128 v[48:51], v78 offset:128\n"
    "ds_read_b128 v[64:67], v78 offset:144\n"
    "ds_read_b128 v[100:103], v78 offset:768\n"
    "ds_read_b128 v[104:107], v78 offset:784\n"
    "s_add_u32 m0, s65, 54272\n"
    "s_add_u32 s72, s10, 398336\n"
    "buffer_load_dwordx4 v90, s[20:23], s72 offen offset:0 nt lds\n"
    "s_add_u32 m0, s65, 98304\n"
    "s_add_u32 s72, s10, 589824\n"
    "buffer_load_dwordx4 v88, s[20:23], s72 offen offset:0 nt lds\n"
    "s_waitcnt lgkmcnt(0)\n"
    "s_barrier\n"
    "ds_read_b128 v[80:83], v79 offset:0\n"
    "s_waitcnt vmcnt(14)\n"
    "buffer_store_dwordx2 v[238:239], v240, s[36:39], s80 offen offset:0 sc1\n"
    "s_lshl_b32 s73, s79, 8\n"
    "s_add_u32 s80, s80, s73\n"
    "s_mul_i32 s73, s79, 98304\n"
    "s_add_u32 s71, s71, s73\n"
    "s_add_u32 m0, s65, 99328\n"
    "s_add_u32 s72, s10, 590848\n"
    "buffer_load_dwordx4 v89, s[20:23], s72 offen offset:0 nt lds\n"
    "s_waitcnt lgkmcnt(0)\n"
    "v_max3_f32 v76, v80, v81, v82\n"
    "v_max_f32 v76, v76, v83\n"
    "v_add_f32 v76, 0xc2b00000, v76\n"
    "s_add_u32 m0, s65, 100352\n"
    "s_add_u32 s72, s10, 591872\n"
    "buffer_load_dwordx4 v90, s[20:23], s72 offen offset:0 nt lds\n"
    "s_bitcmp1_b32 s83, 2\n"
    "s_cbranch_scc1 .Lf3b\n"
    "s_sleep 0\n"
    "s_add_u32 m0, s65, 73728\n"
    "s_add_u32 s72, s85, 196608\n"
    "buffer_load_dwordx4 v88, s[20:23], s72 offen offset:0 nt lds\n"
    "s_add_u32 m0, s65, 74752\n"
    "s_add_u32 s72, s85, 197632\n"
    "buffer_load_dwordx4 v89, s[20:23], s72 offen offset:0 nt lds\n"
    "s_add_u32 m0, s65, 75776\n"
    "s_add_u32 s72, s85, 198656\n"
    "buffer_load_dwordx4 v90, s[20:23], s72 offen offset:0 nt lds\n"
    "s_add_u32 m0, s65, 76800\n"
    "s_add_u32 s72, s85, 199680\n"
    "buffer_load_dwordx4 v88, s[20:23], s72 offen offset:0 nt lds\n"
    "s_add_u32 m0, s65, 77824\n"
    "s_add_u32 s72, s85, 200704\n"
    "buffer_load_dwordx4 v89, s[20:23], s72 offen offset:0 nt lds\n"
    "s_add_u32 m0, s65, 78848\n"
    "s_add_u32 s72, s85, 201728\n"
    "buffer_load_dwordx4 v90, s[20:23], s72 offen offset:0 nt lds\n"
    "s_add_u32 m0, s65, 49152\n"
    "s_add_u32 s72, s85, 393216\n"
    "buffer_load_dwordx4 v88, s[20:23], s72 offen offset:0 nt lds\n"
    "s_add_u32 m0, s65, 50176\n"
    "s_add_u32 s72, s85, 394240\n"
    "buffer_load_dwordx4 v89, s[20:23], s72 offen offset:0 nt lds\n"
    "s_add_u32 m0, s65, 51200\n"
    "s_add_u32 s72, s85, 395264\n"
    "buffer_load_dwordx4 v90, s[20:23], s72 offen offset:0 nt lds\n"
    "s_add_u32 m0, s65, 52224\n"
    "s_add_u32 s72, s85, 396288\n"
    "buffer_load_dwordx4 v88, s[20:23], s72 offen offset:0 nt lds\n"
    "s_add_u32 m0, s65, 53248\n"
    "s_add_u32 s72, s85, 397312\n"
    "buffer_load_dwordx4 v89, s[20:23], s72 offen offset:0 nt lds\n"
    "s_add_u32 m0, s65, 54272\n"
    "s_add_u32 s72, s85, 398336\n"
    "buffer_load_dwordx4 v90, s[20:23], s72 offen offset:0 nt lds\n"
    "s_add_u32 m0, s65, 98304\n"
    "s_add_u32 s72, s85, 589824\n"
    "buffer_load_dwordx4 v88, s[20:23], s72 offen offset:0 nt lds\n"
    "s_add_u32 m0, s65, 99328\n"
    "s_add_u32 s72, s85, 590848\n"
    "buffer_load_dwordx4 v89, s[20:23], s72 offen offset:0 nt lds\n"
    "s_add_u32 m0, s65, 100352\n"
    "s_add_u32 s72, s85, 591872\n"
    "buffer_load_dwordx4 v90, s[20:23], s72 offen offset:0 nt lds\n"
    "s_branch .Lf3e\n"
    ".Lf3b:\n"
    ".Lf3e:\n"
    "s_nop 4\n"
    "s_add_u32 m0, s65, 101376\n"
    "s_add_u32 s72, s85, 592896\n"
    "buffer_load_dwordx4 v88, s[20:23], s72 offen offset:0 nt lds\n"
    "s_add_u32 m0, s65, 102400\n"
    "s_add_u32 s72, s85, 593920\n"
    "buffer_load_dwordx4 v89, s[20:23], s72 offen offset:0 nt lds\n"
    "s_add_u32 m0, s65, 103424\n"
    "s_add_u32 s72, s85, 594944\n"
    "buffer_load_dwordx4 v90, s[20:23], s72 offen offset:0 nt lds\n"
    "v_sub_f32 v48, v48, v76\n"
    "v_sub_f32 v51, v51, v76\n"
    "v_sub_f32 v49, v49, v76\n"
    "v_sub_f32 v50, v50, v76\n"
    "v_sub_f32 v66, v66, v76\n"
    "v_sub_f32 v65, v65, v76\n"
    "v_mfma_f32_16x16x32_bf16 v[36:39], a[0:3], a[192:195], v[48:51]\n"
    "s_waitcnt vmcnt(12)\n"
    "ds_read_b128 v[200:203], v97 offset:24832\n"
    "v_sub_f32 v67, v67, v76\n"
    "v_mfma_f32_16x16x32_bf16 v[36:39], a[4:7], a[196:199], v[36:39]\n"
    "v_sub_f32 v33, v33, v76\n"
    "v_sub_f32 v32, v32, v76\n"
    "v_mfma_f32_16x16x32_bf16 v[36:39], a[8:11], a[200:203], v[36:39]\n"
    "ds_read_b128 v[184:187], v97 offset:24576\n"
    "ds_read_b128 v[196:199], v96 offset:24832\n"
    "ds_read_b128 v[192:195], v97 offset:24704\n"
    "v_mfma_f32_16x16x32_bf16 v[36:39], a[12:15], a[204:207], v[36:39]\n"
    "v_sub_f32 v64, v64, v76\n"
    "v_sub_f32 v35, v35, v76\n"
    "v_sub_f32 v19, v19, v76\n"
    "v_mfma_f32_16x16x32_bf16 v[52:55], a[24:27], a[192:195], v[64:67]\n"
    "v_add_f32 v21, v21, v33\n"
    "v_sub_f32 v16, v16, v76\n"
    "v_mfma_f32_16x16x32_bf16 v[36:39], a[16:19], a[208:211], v[36:39]\n"
    "v_sub_f32 v18, v18, v76\n"
    "v_add_f32 v20, v20, v32\n"
    "v_sub_f32 v17, v17, v76\n"
    "v_mfma_f32_16x16x32_bf16 v[52:55], a[28:31], a[196:199], v[52:55]\n"
    "ds_read_b128 v[188:191], v96 offset:24704\n"
    "ds_read_b128 v[180:183], v96 offset:24576\n"
    "v_add_f32 v6, v6, v18\n"
    "v_sub_f32 v34, v34, v76\n"
    "v_mfma_f32_16x16x32_bf16 v[36:39], a[20:23], a[212:215], v[36:39]\n"
    "s_waitcnt lgkmcnt(5)\n"
    "v_pk_maximum3_f16 v98, v200, v201, v98\n"
    "v_exp_f32 v20, v20\n"
    "v_pk_maximum3_f16 v98, v202, v203, v98\n"
    "v_pk_add_u16 v203, s89, v203 clamp\n"
    "v_pk_add_u16 v200, s89, v200 clamp\n"
    "v_mfma_f32_16x16x32_bf16 v[52:55], a[32:35], a[200:203], v[52:55]\n"
    "s_waitcnt lgkmcnt(3)\n"
    "v_pk_maximum3_f16 v98, v196, v197, v98\n"
    "v_pk_maximum3_f16 v98, v198, v199, v98\n"
    "v_mfma_f32_16x16x32_bf16 v[52:55], a[36:39], a[204:207], v[52:55]\n"
    "v_exp_f32 v21, v21\n"
    "v_pk_add_u16 v202, s89, v202 clamp\n"
    "v_pk_add_u16 v201, s89, v201 clamp\n"
    "v_mfma_f32_16x16x32_bf16 v[52:55], a[40:43], a[208:211], v[52:55]\n"
    "v_add_f32 v22, v22, v34\n"
    "v_add_f32 v7, v7, v19\n"
    "v_mfma_f32_16x16x32_bf16 v[24:27], a[156:159], v[200:203], v[32:35]\n"
    "s_waitcnt lgkmcnt(2)\n"
    "v_pk_maximum3_f16 v98, v192, v193, v98\n"
    "v_pk_maximum3_f16 v98, v194, v195, v98\n"
    "v_pk_add_u16 v193, s89, v193 clamp\n"
    "v_pk_add_u16 v198, s89, v198 clamp\n"
    "v_add_f32 v5, v5, v17\n"
    "v_mfma_f32_16x16x32_bf16 v[52:55], a[44:47], a[212:215], v[52:55]\n"
    "s_waitcnt lgkmcnt(1)\n"
    "v_pk_add_u16 v194, s89, v194 clamp\n"
    "v_sub_f32 v69, v69, v76\n"
    "v_sub_f32 v73, v73, v76\n"
    "v_pk_maximum3_f16 v98, v188, v189, v98\n"
    "v_pk_maximum3_f16 v98, v190, v191, v98\n"
    "v_pk_add_u16 v196, s89, v196 clamp\n"
    "v_pk_maximum3_f16 v98, v184, v185, v98\n"
    "v_pk_add_u16 v197, s89, v197 clamp\n"
    "v_pk_maximum3_f16 v98, v186, v187, v98\n"
    "v_mfma_f32_16x16x32_bf16 v[8:11], a[132:135], v[200:203], v[16:19]\n"
    "v_sub_f32 v75, v75, v76\n"
    "v_pk_add_u16 v199, s89, v199 clamp\n"
    "v_sub_f32 v101, v101, v76\n"
    "v_exp_f32 v54, v54\n"
    "v_mfma_f32_16x16x32_bf16 v[24:27], a[152:155], v[196:199], v[24:27]\n"
    "s_waitcnt lgkmcnt(0)\n"
    "v_pk_add_u16 v188, s89, v188 clamp\n"
    "v_pk_add_u16 v184, s89, v184 clamp\n"
    "v_sub_f32 v70, v70, v76\n"
    "v_pk_maximum3_f16 v98, v180, v181, v98\n"
    "v_pk_add_u16 v186, s89, v186 clamp\n"
    "v_mfma_f32_16x16x32_bf16 v[56:59], a[44:47], v[200:203], v[64:67]\n"
    "v_pk_add_u16 v189, s89, v189 clamp\n"
    "v_pk_add_u16 v195, s89, v195 clamp\n"
    "v_pk_add_u16 v192, s89, v192 clamp\n"
    "v_pk_add_u16 v185, s89, v185 clamp\n"
    "v_pk_maximum3_f16 v98, v182, v183, v98\n"
    "v_exp_f32 v55, v55\n"
    "v_sub_f32 v68, v68, v76\n"
    "v_mfma_f32_16x16x32_bf16 v[56:59], a[40:43], v[196:199], v[56:59]\n"
    "v_exp_f32 v52, v52\n"
    "v_pk_add_u16 v190, s89, v190 clamp\n"
    "v_mfma_f32_16x16x32_bf16 v[8:11], a[128:131], v[196:199], v[8:11]\n"
    "v_accvgpr_write_b32 a231, v195\n"
    "v_sub_f32 v102, v102, v76\n"
    "v_sub_f32 v71, v71, v76\n"
    "v_sub_f32 v107, v107, v76\n"
    "v_mfma_f32_16x16x32_bf16 v[40:43], a[20:23], v[200:203], v[48:51]\n"
    "v_sub_f32 v74, v74, v76\n"
    "v_add_f32 v23, v23, v35\n"
    "v_mfma_f32_16x16x32_bf16 v[40:43], a[16:19], v[196:199], v[40:43]\n"
    "v_pk_add_u16 v183, s89, v183 clamp\n"
    "v_accvgpr_write_b32 a228, v192\n"
    "v_mfma_f32_16x16x32_bf16 v[56:59], a[36:39], v[192:195], v[56:59]\n"
    "v_add_f32 v4, v4, v16\n"
    "v_exp_f32 v6, v6\n"
    "v_mfma_f32_16x16x32_bf16 v[24:27], a[148:151], v[192:195], v[24:27]\n"
    "v_sub_f32 v72, v72, v76\n"
    "v_pk_add_u16 v187, s89, v187 clamp\n"
    "v_pk_add_u16 v191, s89, v191 clamp\n"
    "v_mfma_f32_16x16x32_bf16 v[8:11], a[124:127], v[192:195], v[8:11]\n"
    "v_exp_f32 v53, v53\n"
    "v_accvgpr_write_b32 a226, v190\n"
    "v_mfma_f32_16x16x32_bf16 v[8:11], a[120:123], v[188:191], v[8:11]\n"
    "v_accvgpr_write_b32 a221, v185\n"
    "v_exp_f32 v39, v39\n"
    "v_exp_f32 v22, v22\n"
    "v_pk_add_u16 v181, s89, v181 clamp\n"
    "v_mfma_f32_16x16x32_bf16 v[56:59], a[32:35], v[188:191], v[56:59]\n"
    "v_exp_f32 v36, v36\n"
    "v_pk_add_u16 v182, s89, v182 clamp\n"
    "v_exp_f32 v23, v23\n"
    "v_mfma_f32_16x16x32_bf16 v[24:27], a[144:147], v[188:191], v[24:27]\n"
    "v_accvgpr_write_b32 a237, v201\n"
    "v_pk_add_u16 v180, s89, v180 clamp\n"
    "v_mfma_f32_16x16x32_bf16 v[40:43], a[12:15], v[192:195], v[40:43]\n"
    "v_accvgpr_write_b32 a239, v203\n"
    "v_accvgpr_write_b32 a223, v187\n"
    "v_exp_f32 v7, v7\n"
    "v_accvgpr_write_b32 a233, v197\n"
    "v_mfma_f32_16x16x32_bf16 v[8:11], a[116:119], v[184:187], v[8:11]\n"
    "v_accvgpr_write_b32 a232, v196\n"
    "v_accvgpr_write_b32 a235, v199\n"
    "v_mfma_f32_16x16x32_bf16 v[40:43], a[8:11], v[188:191], v[40:43]\n"
    "ds_write_b128 v86, v[68:71] offset:0\n"
    "v_mfma_f32_16x16x32_bf16 v[40:43], a[4:7], v[184:187], v[40:43]\n"
    "v_sub_f32 v106, v106, v76\n"
    "v_accvgpr_write_b32 a234, v198\n"
    "v_mfma_f32_16x16x32_bf16 v[8:11], a[112:115], v[180:183], v[8:11]\n"
    "v_cvt_pk_bf16_f32 v70, v20, v21\n"
    "v_exp_f32 v37, v37\n"
    "v_cvt_pk_bf16_f32 v69, v6, v7\n"
    "v_accvgpr_write_b32 a218, v182\n"
    "v_mfma_f32_16x16x32_bf16 v[40:43], a[0:3], v[180:183], v[40:43]\n"
    "ds_write_b128 v86, v[72:75] offset:16\n"
    "v_accvgpr_write_b32 a230, v194\n"
    "v_mfma_f32_16x16x32_bf16 v[24:27], a[140:143], v[184:187], v[24:27]\n"
    "v_exp_f32 v10, v10\n"
    "v_mfma_f32_16x16x32_bf16 v[56:59], a[28:31], v[184:187], v[56:59]\n"
    "v_cvt_pk_bf16_f32 v72, v36, v37\n"
    "v_sub_f32 v103, v103, v76\n"
    "v_mfma_f32_16x16x32_bf16 v[56:59], a[24:27], v[180:183], v[56:59]\n"
    "v_sub_f32 v104, v104, v76\n"
    "v_accvgpr_write_b32 a217, v181\n"
    "v_exp_f32 v4, v4\n"
    "v_sub_f32 v100, v100, v76\n"
    "v_accvgpr_write_b32 a227, v191\n"
    "v_exp_f32 v11, v11\n"
    "v_exp_f32 v5, v5\n"
    "v_exp_f32 v38, v38\n"
    "v_exp_f32 v41, v41\n"
    "v_exp_f32 v9, v9\n"
    "v_exp_f32 v59, v59\n"
    "v_accvgpr_write_b32 a220, v184\n"
    "v_accvgpr_write_b32 a238, v202\n"
    "v_accvgpr_write_b32 a236, v200\n"
    "v_exp_f32 v43, v43\n"
    "v_cvt_pk_bf16_f32 v77, v10, v11\n"
    "v_exp_f32 v8, v8\n"
    "v_cvt_pk_bf16_f32 v75, v54, v55\n"
    "v_mfma_f32_16x16x32_bf16 v[24:27], a[136:139], v[180:183], v[24:27]\n"
    "v_cvt_pk_bf16_f32 v71, v22, v23\n"
    "v_sub_f32 v105, v105, v76\n"
    "v_accvgpr_write_b32 a216, v180\n"
    "v_exp_f32 v58, v58\n"
    "v_cvt_pk_bf16_f32 v73, v38, v39\n"
    "v_exp_f32 v40, v40\n"
    "v_accvgpr_write_b32 a222, v186\n"
    "v_accvgpr_write_b32 a225, v189\n"
    "v_exp_f32 v24, v24\n"
    "v_exp_f32 v57, v57\n"
    "v_cvt_pk_bf16_f32 v68, v4, v5\n"
    "v_cvt_pk_bf16_f32 v80, v40, v41\n"
    "v_exp_f32 v27, v27\n"
    "v_exp_f32 v25, v25\n"
    "v_cvt_pk_bf16_f32 v76, v8, v9\n"
    "v_accvgpr_write_b32 a224, v188\n"
    "v_accvgpr_write_b32 a219, v183\n"
    "v_exp_f32 v26, v26\n"
    "v_exp_f32 v42, v42\n"
    "v_cvt_pk_bf16_f32 v78, v24, v25\n"
    "v_exp_f32 v56, v56\n"
    "v_cvt_pk_bf16_f32 v74, v52, v53\n"
    "v_accvgpr_write_b32 a229, v193\n"
    "v_cvt_pk_bf16_f32 v79, v26, v27\n"
    "v_cvt_pk_bf16_f32 v82, v56, v57\n"
    "v_cvt_pk_bf16_f32 v83, v58, v59\n"
    "v_cvt_pk_bf16_f32 v81, v42, v43\n"
    "s_waitcnt vmcnt(8)\n"
    "v_cmp_eq_u64 s[54:55], v[236:237], v[238:239]\n"
    "s_nop 0\n"
    "s_cmp_eq_u64 s[54:55], -1\n"
    "s_cbranch_scc0 .LpollAK\n"
    ".LpollAKr:\n"
    "s_add_u32 m0, s65, 24576\n"
    "s_add_u32 s72, s65, 49152\n"
    "buffer_load_dwordx4 v1, s[16:19], s72 offen offset:0 sc1 lds\n"
    "s_add_u32 m0, s65, 25600\n"
    "s_add_u32 s72, s65, 50176\n"
    "buffer_load_dwordx4 v1, s[16:19], s72 offen offset:0 sc1 lds\n"
    "s_add_u32 m0, s65, 26624\n"
    "s_add_u32 s72, s65, 51200\n"
    "buffer_load_dwordx4 v1, s[16:19], s72 offen offset:0 sc1 lds\n"
    "s_add_u32 m0, s65, 27648\n"
    "s_add_u32 s72, s65, 52224\n"
    "buffer_load_dwordx4 v1, s[16:19], s72 offen offset:0 sc1 lds\n"
    "s_add_u32 m0, s65, 28672\n"
    "s_add_u32 s72, s65, 53248\n"
    "buffer_load_dwordx4 v1, s[16:19], s72 offen offset:0 sc1 lds\n"
    "s_add_u32 m0, s65, 29696\n"
    "s_add_u32 s72, s65, 54272\n"
    "buffer_load_dwordx4 v1, s[16:19], s72 offen offset:0 sc1 lds\n"
    "s_waitcnt vmcnt(12)\n"
    "ds_read_b128 v[156:159], v96 offset:0\n"
    "ds_read_b128 v[172:175], v96 offset:256\n"
    "ds_read_b128 v[168:171], v97 offset:128\n"
    "ds_read_b128 v[176:179], v97 offset:256\n"
    "ds_read_b128 v[160:163], v97 offset:0\n"
    "ds_read_b128 v[164:167], v96 offset:128\n"
    "s_waitcnt lgkmcnt(5)\n"
    "v_pk_maximum3_f16 v98, v156, v157, v98\n"
    "v_pk_maximum3_f16 v98, v158, v159, v98\n"
    "v_pk_add_u16 v157, s89, v157 clamp\n"
    "v_pk_add_u16 v158, s89, v158 clamp\n"
    "v_pk_add_u16 v156, s89, v156 clamp\n"
    "v_pk_add_u16 v159, s89, v159 clamp\n"
    "s_waitcnt lgkmcnt(1)\n"
    "v_pk_maximum3_f16 v98, v160, v161, v98\n"
    "s_add_u32 m0, s65, 73728\n"
    "s_add_u32 s72, s65, 73728\n"
    "buffer_load_dwordx4 v1, s[16:19], s72 offen offset:0 sc1 lds\n"
    "v_mfma_f32_16x16x32_bf16 v[28:31], a[136:139], v[156:159], v[32:35]\n"
    "s_waitcnt lgkmcnt(0)\n"
    "v_pk_maximum3_f16 v98, v162, v163, v98\n"
    "v_pk_maximum3_f16 v98, v164, v165, v98\n"
    "v_mfma_f32_16x16x32_bf16 v[12:15], a[112:115], v[156:159], v[16:19]\n"
    "v_pk_add_u16 v161, s89, v161 clamp\n"
    "v_pk_maximum3_f16 v98, v166, v167, v98\n"
    "v_pk_add_u16 v160, s89, v160 clamp\n"
    "v_pk_add_u16 v162, s89, v162 clamp\n"
    "v_mfma_f32_16x16x32_bf16 v[60:63], a[24:27], v[156:159], v[64:67]\n"
    "v_pk_maximum3_f16 v98, v168, v169, v98\n"
    "v_pk_add_u16 v163, s89, v163 clamp\n"
    "v_mfma_f32_16x16x32_bf16 v[44:47], a[0:3], v[156:159], v[48:51]\n"
    "v_pk_maximum3_f16 v98, v170, v171, v98\n"
    "v_pk_add_u16 v164, s89, v164 clamp\n"
    "v_mfma_f32_16x16x32_bf16 v[12:15], a[116:119], v[160:163], v[12:15]\n"
    "v_pk_add_u16 v165, s89, v165 clamp\n"
    "s_add_u32 m0, s65, 74752\n"
    "s_add_u32 s72, s65, 74752\n"
    "buffer_load_dwordx4 v1, s[16:19], s72 offen offset:0 sc1 lds\n"
    "v_pk_add_u16 v169, s89, v169 clamp\n"
    "v_pk_add_u16 v168, s89, v168 clamp\n"
    "v_mfma_f32_16x16x32_bf16 v[60:63], a[28:31], v[160:163], v[60:63]\n"
    "v_pk_maximum3_f16 v98, v172, v173, v98\n"
    "v_pk_add_u16 v170, s89, v170 clamp\n"
    "v_mfma_f32_16x16x32_bf16 v[28:31], a[140:143], v[160:163], v[28:31]\n"
    "v_pk_maximum3_f16 v98, v174, v175, v98\n"
    "v_pk_add_u16 v166, s89, v166 clamp\n"
    "v_pk_add_u16 v167, s89, v167 clamp\n"
    "v_mfma_f32_16x16x32_bf16 v[44:47], a[4:7], v[160:163], v[44:47]\n"
    "v_pk_maximum3_f16 v98, v176, v177, v98\n"
    "v_accvgpr_write_b32 a251, v167\n"
    "v_mfma_f32_16x16x32_bf16 v[44:47], a[8:11], v[164:167], v[44:47]\n"
    "v_pk_maximum3_f16 v98, v178, v179, v98\n"
    "v_pk_add_u16 v176, s89, v176 clamp\n"
    "v_mfma_f32_16x16x32_bf16 v[60:63], a[32:35], v[164:167], v[60:63]\n"
    "v_pk_add_u16 v171, s89, v171 clamp\n"
    "s_add_u32 m0, s65, 75776\n"
    "s_add_u32 s72, s65, 75776\n"
    "buffer_load_dwordx4 v1, s[16:19], s72 offen offset:0 sc1 lds\n"
    "v_pk_add_u16 v174, s89, v174 clamp\n"
    "v_mfma_f32_16x16x32_bf16 v[28:31], a[144:147], v[164:167], v[28:31]\n"
    "v_pk_add_u16 v173, s89, v173 clamp\n"
    "v_pk_add_u16 v172, s89, v172 clamp\n"
    "v_mfma_f32_16x16x32_bf16 v[12:15], a[120:123], v[164:167], v[12:15]\n"
    "v_accvgpr_write_b32 a254, v170\n"
    "v_pk_add_u16 v178, s89, v178 clamp\n"
    "v_mfma_f32_16x16x32_bf16 v[28:31], a[148:151], v[168:171], v[28:31]\n"
    "v_accvgpr_write_b32 a243, v159\n"
    "v_accvgpr_write_b32 a162, v174\n"
    "v_mfma_f32_16x16x32_bf16 v[12:15], a[124:127], v[168:171], v[12:15]\n"
    "v_pk_add_u16 v179, s89, v179 clamp\n"
    "v_pk_add_u16 v175, s89, v175 clamp\n"
    "v_mfma_f32_16x16x32_bf16 v[44:47], a[12:15], v[168:171], v[44:47]\n"
    "v_accvgpr_write_b32 a166, v178\n"
    "v_accvgpr_write_b32 a161, v173\n"
    "v_accvgpr_write_b32 a245, v161\n"
    "s_add_u32 m0, s65, 76800\n"
    "s_add_u32 s72, s65, 76800\n"
    "buffer_load_dwordx4 v1, s[16:19], s72 offen offset:0 sc1 lds\n"
    "v_mfma_f32_16x16x32_bf16 v[28:31], a[152:155], v[172:175], v[28:31]\n"
    "v_accvgpr_write_b32 a163, v175\n"
    "v_pk_add_u16 v177, s89, v177 clamp\n"
    "v_mfma_f32_16x16x32_bf16 v[44:47], a[16:19], v[172:175], v[44:47]\n"
    "v_accvgpr_write_b32 a160, v172\n"
    "v_accvgpr_write_b32 a252, v168\n"
    "v_mfma_f32_16x16x32_bf16 v[28:31], a[156:159], v[176:179], v[28:31]\n"
    "v_accvgpr_write_b32 a249, v165\n"
    "v_accvgpr_write_b32 a255, v171\n"
    "v_mfma_f32_16x16x32_bf16 v[12:15], a[128:131], v[172:175], v[12:15]\n"
    "v_accvgpr_write_b32 a247, v163\n"
    "v_accvgpr_write_b32 a164, v176\n"
    "v_mfma_f32_16x16x32_bf16 v[60:63], a[36:39], v[168:171], v[60:63]\n"
    "v_accvgpr_write_b32 a242, v158\n"
    "v_accvgpr_write_b32 a250, v166\n"
    "v_mfma_f32_16x16x32_bf16 v[44:47], a[20:23], v[176:179], v[44:47]\n"
    "v_accvgpr_write_b32 a241, v157\n"
    "s_add_u32 m0, s65, 77824\n"
    "s_add_u32 s72, s65, 77824\n"
    "buffer_load_dwordx4 v1, s[16:19], s72 offen offset:0 sc1 lds\n"
    "v_accvgpr_write_b32 a248, v164\n"
    "v_mfma_f32_16x16x32_bf16 v[12:15], a[132:135], v[176:179], v[12:15]\n"
    "v_accvgpr_write_b32 a246, v162\n"
    "v_exp_f32 v29, v29\n"
    "v_mfma_f32_16x16x32_bf16 v[60:63], a[40:43], v[172:175], v[60:63]\n"
    "v_exp_f32 v28, v28\n"
    "v_mfma_f32_16x16x32_bf16 v[60:63], a[44:47], v[176:179], v[60:63]\n"
    "v_exp_f32 v31, v31\n"
    "v_exp_f32 v30, v30\n"
    "v_exp_f32 v45, v45\n"
    "v_cvt_pk_bf16_f32 v86, v28, v29\n"
    "v_exp_f32 v47, v47\n"
    "v_accvgpr_write_b32 a240, v156\n"
    "v_exp_f32 v12, v12\n"
    "v_exp_f32 v14, v14\n"
    "v_accvgpr_write_b32 a167, v179\n"
    "v_exp_f32 v15, v15\n"
    "s_add_u32 m0, s65, 78848\n"
    "s_add_u32 s72, s65, 78848\n"
    "buffer_load_dwordx4 v1, s[16:19], s72 offen offset:0 sc1 lds\n"
    "v_accvgpr_write_b32 a253, v169\n"
    "v_exp_f32 v63, v63\n"
    "v_exp_f32 v13, v13\n"
    "v_exp_f32 v44, v44\n"
    "v_cvt_pk_bf16_f32 v84, v12, v13\n"
    "v_cvt_pk_bf16_f32 v87, v30, v31\n"
    "v_exp_f32 v46, v46\n"
    "v_exp_f32 v61, v61\n"
    "v_accvgpr_write_b32 a165, v177\n"
    "v_exp_f32 v60, v60\n"
    "v_exp_f32 v62, v62\n"
    "v_cvt_pk_bf16_f32 v90, v60, v61\n"
    "v_accvgpr_write_b32 a244, v160\n"
    "v_cvt_pk_bf16_f32 v85, v14, v15\n"
    "v_cvt_pk_bf16_f32 v89, v46, v47\n"
    "v_cvt_pk_bf16_f32 v88, v44, v45\n"
    "v_cvt_pk_bf16_f32 v91, v62, v63\n"
    "s_bitcmp1_b32 s83, 1\n"
    "s_cbranch_scc1 .Lf4b\n"
    "s_mov_b32 s66, 1\n"
    "s_add_u32 s68, s68, 24576\n"
    "s_add_u32 s81, s81, 128\n"
    "s_add_u32 s80, s80, 256\n"
    "s_add_u32 s71, s71, 98304\n"
    "s_add_u32 s75, s71, s63\n"
    "s_add_u32 s76, s75, 6144\n"
    "s_add_u32 s77, s75, 24576\n"
    "s_add_u32 s78, s75, 30720\n"
    "s_branch .LhKP\n"
    "s_branch .Lf4e\n"
    ".Lf4b:\n"
    "s_add_u32 s75, s71, s63\n"
    "s_add_u32 s76, s75, 6144\n"
    "s_add_u32 s77, s75, 24576\n"
    "s_add_u32 s78, s75, 30720\n"
    "buffer_load_dwordx2 v[204:205], v242, s[28:31], s75 offen offset:0 nt\n"
    "buffer_load_dwordx2 v[206:207], v242, s[28:31], s75 offen offset:768 nt\n"
    "buffer_load_dwordx2 v[208:209], v242, s[28:31], s75 offen offset:3072 nt\n"
    "buffer_load_dwordx2 v[210:211], v242, s[28:31], s75 offen offset:3840 nt\n"
    "buffer_load_dwordx2 v[212:213], v242, s[28:31], s76 offen offset:0 nt\n"
    "buffer_load_dwordx2 v[214:215], v242, s[28:31], s76 offen offset:768 nt\n"
    "buffer_load_dwordx2 v[216:217], v242, s[28:31], s76 offen offset:3072 nt\n"
    "buffer_load_dwordx2 v[218:219], v242, s[28:31], s76 offen offset:3840 nt\n"
    "buffer_load_dwordx2 v[220:221], v242, s[28:31], s77 offen offset:0 nt\n"
    "buffer_load_dwordx2 v[222:223], v242, s[28:31], s77 offen offset:768 nt\n"
    "buffer_load_dwordx2 v[224:225], v242, s[28:31], s77 offen offset:3072 nt\n"
    "buffer_load_dwordx2 v[226:227], v242, s[28:31], s77 offen offset:3840 nt\n"
    "buffer_load_dwordx2 v[228:229], v242, s[28:31], s78 offen offset:0 nt\n"
    "buffer_load_dwordx2 v[230:231], v242, s[28:31], s78 offen offset:768 nt\n"
    "buffer_load_dwordx2 v[232:233], v242, s[28:31], s78 offen offset:3072 nt\n"
    "buffer_load_dwordx2 v[234:235], v242, s[28:31], s78 offen offset:3840 nt\n"
    "s_mov_b32 s66, 1\n"
    "s_add_u32 s68, s68, 24576\n"
    "s_add_u32 s81, s81, 128\n"
    "s_branch .LhKN\n"
    ".Lf4e:\n"
    "s_nop 4\n"
    "s_branch .Lf7e\n"
    ".Lf7b:\n"
    "s_lshl_b32 s73, s79, 8\n"
    "s_mul_i32 s74, s59, 6144\n"
    "s_add_u32 s73, s73, s74\n"
    "v_add_u32 v163, s73, v244\n"
    "s_lshr_b32 s73, s53, 2\n"
    "s_mul_i32 s73, s73, 12288\n"
    "s_and_b32 s74, s53, 3\n"
    "s_lshl_b32 s74, s74, 8\n"
    "s_add_u32 s73, s73, s74\n"
    "s_cmp_eq_u32 s59, 0\n"
    "s_cselect_b32 s73, 0x7f000000, s73\n"
    "s_add_u32 s60, s73, 24576\n"
    "s_add_u32 s61, s73, 28672\n"
    "s_add_u32 s62, s73, 32768\n"
    "v_mov_b32 v244, v243\n"
    "v_mov_b32 v243, 0\n"
    "s_sub_u32 s8, s40, 1572864\n"
    "s_subb_u32 s9, s41, 0\n"
    "buffer_load_dwordx4 v[40:43], v242, s[40:43], s75 offen offset:768 nt\n"
    "v_add_u32 v159, 0, v156\n"
    "v_mul_u32_u24 v159, 2731, v159\n"
    "v_lshrrev_b32 v159, 16, v159\n"
    "v_xor_b32 v159, v159, v156\n"
    "v_and_b32 v159, 7, v159\n"
    "v_and_b32 v88, 0x380, v1\n"
    "v_lshl_add_u32 v88, v159, 4, v88\n"
    "v_add_u32 v159, 64, v156\n"
    "v_mul_u32_u24 v159, 2731, v159\n"
    "v_lshrrev_b32 v159, 16, v159\n"
    "v_xor_b32 v159, v159, v156\n"
    "v_and_b32 v159, 7, v159\n"
    "v_and_b32 v89, 0x380, v1\n"
    "v_lshl_add_u32 v89, v159, 4, v89\n"
    "v_add_u32 v159, 128, v156\n"
    "v_mul_u32_u24 v159, 2731, v159\n"
    "v_lshrrev_b32 v159, 16, v159\n"
    "v_xor_b32 v159, v159, v156\n"
    "v_and_b32 v159, 7, v159\n"
    "v_and_b32 v90, 0x380, v1\n"
    "v_lshl_add_u32 v90, v159, 4, v90\n"
    "v_and_b32 v2, 7, v156\n"
    "v_lshrrev_b32 v144, 7, v130\n"
    "v_mul_u32_u24 v144, 768, v144\n"
    "v_lshl_add_u32 v144, v2, 3, v144\n"
    "buffer_load_dwordx4 v[44:47], v242, s[40:43], s75 offen offset:256 nt\n"
    "v_add_u32 v144, 131072, v144\n"
    "v_mad_u32_u24 v86, v2, 24, v144\n"
    "v_lshl_add_u32 v130, s82, 7, v130\n"
    "v_and_b32 v130, 4095, v130\n"
    "v_and_b32 v96, 15, v156\n"
    "v_lshrrev_b32 v159, 4, v156\n"
    "v_xor_b32 v159, v159, v2\n"
    "v_mul_u32_u24 v96, 384, v96\n"
    "v_lshl_add_u32 v96, v159, 4, v96\n"
    "s_add_u32 s73, s65, 49152\n"
    "v_add_u32 v96, s73, v96\n"
    "v_xor_b32 v97, 64, v96\n"
    "v_subrev_u32 v154, 24576, v96\n"
    "v_subrev_u32 v155, 24576, v97\n"
    "s_add_u32 s67, s65, 98304\n"
    "s_add_u32 s68, s65, 24576\n"
    "s_add_u32 s69, s65, 73728\n"
    "s_mov_b32 s81, 256\n"
    "s_mul_i32 s74, s79, 24576\n"
    "s_add_u32 s70, s74, s65\n"
    "s_add_u32 s70, s70, 73728\n"
    "s_sub_u32 s64, s65, s74\n"
    "s_add_u32 s64, s64, 98304\n"
    "v_and_b32 v249, 0x310, v1\n"
    "v_and_b32 v159, 12, v156\n"
    "v_lshl_add_u32 v249, v159, 3, v249\n"
    "buffer_load_dwordx4 v[48:51], v242, s[40:43], s75 offen offset:1024 nt\n"
    "v_and_b32 v159, 2, v156\n"
    "v_lshl_add_u32 v249, v159, 6, v249\n"
    "s_add_u32 s73, s74, 73728\n"
    "v_add_u32 v248, s73, v249\n"
    "s_sub_u32 s73, 98304, s74\n"
    "v_add_u32 v249, s73, v249\n"
    "s_or_b32 s73, s53, s84\n"
    "s_cmp_eq_u32 s73, 0\n"
    "s_cselect_b32 s82, 0, 0x7fff0000\n"
    "v_and_b32 v252, 12, v156\n"
    "v_bfe_u32 v159, v156, 1, 1\n"
    "v_or_b32 v252, v252, v159\n"
    "v_and_b32 v159, 1, v156\n"
    "v_lshl_or_b32 v252, v159, 1, v252\n"
    "v_lshlrev_b32 v252, 6, v252\n"
    "v_lshlrev_b32 v159, 2, v156\n"
    "v_and_b32 v159, 32, v159\n"
    "v_xor_b32 v159, v159, v156\n"
    "v_and_or_b32 v252, v159, 48, v252\n"
    "v_mov_b32 v2, v252\n"
    "v_add_u32 v3, 24576, v252\n"
    "v_lshrrev_b32 v91, 3, v156\n"
    "v_lshl_add_u32 v87, v91, 6, 64\n"
    "v_add_u32 v87, 64, v87\n"
    "v_and_b32 v241, 1, v156\n"
    "v_lshlrev_b32 v241, 3, v241\n"
    "buffer_load_dwordx4 v[52:55], v242, s[40:43], s75 offen offset:512 nt\n"
    "v_lshl_add_u32 v241, v91, 4, v241\n"
    "v_add_u32 v241, 1152, v241\n"
    "v_lshlrev_b32 v91, 24, v91\n"
    "v_lshl_add_u32 v91, v156, 3, v91\n"
    "s_lshl_b32 s73, s53, 4\n"
    "s_lshl_b32 s74, s59, 3\n"
    "s_add_u32 s73, s73, s74\n"
    "s_add_u32 s73, s73, 1152\n"
    "v_mov_b32 v240, 0x7fff0000\n"
    "v_writelane_b32 v240, s73, 0\n"
    "v_and_b32 v159, 48, v156\n"
    "s_mul_i32 s73, s79, 768\n"
    "s_sub_u32 s73, 131072, s73\n"
    "v_lshlrev_b32 v250, 1, v159\n"
    "v_add_u32 v250, s73, v250\n"
    "v_and_b32 v251, 3, v156\n"
    "v_lshl_add_u32 v251, v251, 7, v159\n"
    "s_add_u32 s73, s73, 256\n"
    "v_add_u32 v251, s73, v251\n"
    "v_and_b32 v159, 64, v1\n"
    "v_add_u32 v251, v159, v251\n"
    "v_bfe_i32 v159, v156, 2, 1\n"
    "v_and_b32 v159, 0xaaaa, v159\n"
    "v_or_b32 v253, 0x4444, v159\n"
    "s_mov_b32 s58, 0\n"
    "s_mov_b32 s59, 8\n"
    "s_waitcnt vmcnt(14)\n"
    "v_pk_min_i16 v4, v4, v10\n"
    "v_pk_min_i16 v5, v5, v11\n"
    "v_pk_min_i16 v6, v6, v12\n"
    "v_pk_min_i16 v7, v7, v13\n"
    "v_pk_min_i16 v8, v8, v14\n"
    "v_pk_min_i16 v9, v9, v15\n"
    "v_pk_min_i16 v16, v16, v18\n"
    "v_pk_min_i16 v17, v17, v19\n"
    "v_pk_min_i16 v4, v4, v7\n"
    "v_pk_min_i16 v5, v5, v8\n"
    "v_pk_min_i16 v6, v6, v9\n"
    "v_pk_min_i16 v16, v16, v17\n"
    "v_pk_min_i16 v4, v4, v5\n"
    "v_lshlrev_b32 v17, 16, v16\n"
    "v_pk_min_i16 v4, v4, v6\n"
    "v_min_i32 v16, v16, v17\n"
    "v_lshlrev_b32 v5, 16, v4\n"
    "v_mov_b32 v7, 155920\n"
    "v_min_i32 v4, v4, v5\n"
    "v_lshl_add_u32 v6, s84, 2, v7\n"
    "v_min_i32_dpp v16, v16, v16 row_shr:1\n"
    "v_min_i32_dpp v4, v4, v4 row_shr:1\n"
    "s_nop 0\n"
    "v_min_i32_dpp v16, v16, v16 row_shr:2\n"
    "v_min_i32_dpp v4, v4, v4 row_shr:2\n"
    "s_nop 0\n"
    "v_min_i32_dpp v16, v16, v16 row_shr:4\n"
    "v_min_i32_dpp v4, v4, v4 row_shr:4\n"
    "s_nop 0\n"
    "v_min_i32_dpp v16, v16, v16 row_shr:8\n"
    "v_min_i32_dpp v4, v4, v4 row_shr:8\n"
    "s_nop 0\n"
    "v_min_i32_dpp v16, v16, v16 row_bcast:15 row_mask:0xa\n"
    "v_min_i32_dpp v4, v4, v4 row_bcast:15 row_mask:0xa\n"
    "s_nop 0\n"
    "v_min_i32_dpp v16, v16, v16 row_bcast:31 row_mask:0xc\n"
    "v_min_i32_dpp v4, v4, v4 row_bcast:31 row_mask:0xc\n"
    "v_readlane_b32 s35, v16, 63\n"
    "v_readlane_b32 s73, v4, 63\n"
    "s_max_i32 s35, s35, 0\n"
    "s_and_b32 s35, s35, 0xffff0000\n"
    "v_mov_b32 v4, s73\n"
    "ds_write_b32 v6, v4\n"
    "v_mov_b32 v14, -32\n"
    "v_mov_b32 v12, -126\n"
    "v_mov_b32 v13, 0x3dd53b95\n"
    "v_mov_b32 v98, 0\n"
    "s_waitcnt lgkmcnt(0)\n"
    "s_barrier\n"
    "ds_read_b128 v[8:11], v7 offset:0\n"
    "buffer_load_dwordx4 v[56:59], v242, s[40:43], s75 offen offset:1280 nt\n"
    "buffer_load_dwordx4 v[136:139], v130, s[92:95], 0 offen offset:0\n"
    "buffer_load_dwordx4 v[140:143], v130, s[96:99], 0 offen offset:0\n"
    "s_nop 0\n"
    "s_nop 0\n"
    "s_nop 0\n"
    "s_nop 0\n"
    "s_nop 0\n"
    "s_nop 0\n"
    "s_nop 0\n"
    "s_nop 0\n"
    "s_nop 0\n"
    "s_nop 0\n"
    "s_nop 0\n"
    "s_nop 0\n"
    "s_nop 0\n"
    "s_nop 0\n"
    "s_nop 0\n"
    "s_nop 0\n"
    "s_nop 0\n"
    "s_nop 0\n"
    "s_nop 0\n"
    "s_nop 0\n"
    "s_nop 0\n"
    "s_nop 0\n"
    "s_nop 0\n"
    "s_nop 0\n"
    "s_nop 0\n"
    "s_nop 0\n"
    "s_nop 0\n"
    "s_nop 0\n"
    "s_nop 0\n"
    "s_nop 0\n"
    "s_nop 0\n"
    "s_nop 0\n"
    "s_nop 0\n"
    "s_nop 0\n"
    "s_nop 0\n"
    "s_nop 0\n"
    "s_nop 0\n"
    "s_nop 0\n"
    "s_nop 0\n"
    "s_nop 0\n"
    "s_nop 0\n"
    "s_nop 0\n"
    "s_nop 0\n"
    "s_nop 0\n"
    "s_nop 0\n"
    "s_nop 0\n"
    "s_nop 0\n"
    "s_nop 0\n"
    "s_nop 0\n"
    "s_nop 0\n"
    "s_nop 0\n"
    "s_nop 0\n"
    "s_nop 0\n"
    "s_nop 0\n"
    "s_nop 0\n"
    "s_nop 0\n"
    "s_nop 0\n"
    "s_nop 0\n"
    "s_nop 0\n"
    "s_nop 0\n"
    "s_waitcnt lgkmcnt(0)\n"
    "v_min3_i32 v4, v8, v9, v10\n"
    "v_min_i32 v4, v4, v11\n"
    "v_max_i32 v4, 0, v4\n"
    "v_and_b32 v4, 0xffff0000, v4\n"
    "v_mul_f32 v4, s35, v4\n"
    "v_mul_f32 v4, 0x3ed53b95, v4\n"
    "v_lshrrev_b32 v4, 23, v4\n"
    "v_sub_u32 v4, 2, v4\n"
    "v_med3_i32 v4, v4, v14, v12\n"
    "v_ldexp_f32 v13, v13, v4\n"
    "v_readfirstlane_b32 s89, v4\n"
    "v_readfirstlane_b32 s88, v13\n"
    "s_mul_i32 s89, s89, 0xff7fff80\n"
    "s_mov_b32 s90, 149\n"
    "s_waitcnt vmcnt(11)\n"
    "s_add_u32 s54, s71, s60\n"
    "s_add_u32 s55, s71, s61\n"
    "s_add_u32 s56, s71, s62\n"
    "v_cvt_pk_u16_u32 v204, v204, v208\n"
    "v_cvt_pk_u16_u32 v205, v205, v209\n"
    "v_cvt_pk_u16_u32 v206, v206, v210\n"
    "v_cvt_pk_u16_u32 v207, v207, v211\n"
    "v_or3_b32 v243, v204, v205, v243\n"
    "v_or3_b32 v243, v206, v207, v243\n"
    "buffer_store_dword v204, v244, s[16:19], s54 offen offset:0 sc1\n"
    "buffer_store_dword v205, v244, s[16:19], s54 offen offset:1024 sc1\n"
    "buffer_load_dwordx4 v[4:7], v162, s[28:31], s57 offen offset:0\n"
    "buffer_load_dwordx4 v[8:11], v162, s[28:31], s57 offen offset:3072\n"
    "buffer_store_dword v206, v244, s[16:19], s54 offen offset:2048 sc1\n"
    "buffer_store_dword v207, v244, s[16:19], s54 offen offset:3072 sc1\n"
    "v_cvt_pk_u16_u32 v212, v212, v216\n"
    "v_cvt_pk_u16_u32 v213, v213, v217\n"
    "v_cvt_pk_u16_u32 v214, v214, v218\n"
    "v_cvt_pk_u16_u32 v215, v215, v219\n"
    "v_or3_b32 v243, v212, v213, v243\n"
    "v_or3_b32 v243, v214, v215, v243\n"
    "buffer_store_dword v212, v244, s[16:19], s55 offen offset:0 sc1\n"
    "buffer_store_dword v213, v244, s[16:19], s55 offen offset:1024 sc1\n"
    "buffer_store_dword v214, v244, s[16:19], s55 offen offset:2048 sc1\n"
    "buffer_load_dwordx4 v[12:15], v162, s[28:31], s57 offen offset:128\n"
    "buffer_store_dword v215, v244, s[16:19], s55 offen offset:3072 sc1\n"
    "v_cvt_pk_u16_u32 v220, v220, v224\n"
    "v_cvt_pk_u16_u32 v221, v221, v225\n"
    "v_cvt_pk_u16_u32 v222, v222, v226\n"
    "v_cvt_pk_u16_u32 v223, v223, v227\n"
    "v_or3_b32 v243, v220, v221, v243\n"
    "v_or3_b32 v243, v222, v223, v243\n"
    "buffer_store_dword v220, v244, s[16:19], s56 offen offset:0 sc1\n"
    "buffer_store_dword v221, v244, s[16:19], s56 offen offset:1024 sc1\n"
    "buffer_store_dword v222, v244, s[16:19], s56 offen offset:2048 sc1\n"
    "buffer_store_dword v223, v244, s[16:19], s56 offen offset:3072 sc1\n"
    "s_add_u32 s71, s71, 98304\n"
    "s_add_u32 m0, s65, 24576\n"
    "s_add_u32 s72, s85, 0\n"
    "buffer_load_dwordx4 v88, s[20:23], s72 offen offset:0 nt lds\n"
    "buffer_load_dwordx4 v[16:19], v162, s[28:31], s57 offen offset:3200\n"
    "buffer_load_dwordx4 v[20:23], v162, s[28:31], s57 offen offset:256\n"
    "s_waitcnt vmcnt(25)\n"
    "v_add_co_u32 v238, vcc, 0x7f4a7c15, v238\n"
    "v_mov_b32 v159, 0x9e3779b9\n"
    "s_nop 0\n"
    "v_addc_co_u32 v239, vcc, v159, v239, vcc\n"
    "v_mov_b32 v246, 1\n"
    "s_waitcnt vmcnt(26)\n"
    "v_lshlrev_b32 v180, 16, v124\n"
    "v_and_b32 v181, 0xffff0000, v124\n"
    "v_lshlrev_b32 v182, 16, v125\n"
    "v_and_b32 v183, 0xffff0000, v125\n"
    "v_lshlrev_b32 v184, 16, v126\n"
    "v_and_b32 v185, 0xffff0000, v126\n"
    "v_lshlrev_b32 v186, 16, v127\n"
    "v_and_b32 v187, 0xffff0000, v127\n"
    "v_mul_f32 v180, s88, v180\n"
    "v_mul_f32 v181, s88, v181\n"
    "v_mul_f32 v182, s88, v182\n"
    "v_mul_f32 v183, s88, v183\n"
    "v_fmaak_f32 v184, v184, v180, 0x80730000\n"
    "v_fmaak_f32 v185, v185, v181, 0x80730000\n"
    "v_fmaak_f32 v186, v186, v182, 0x80730000\n"
    "v_fmaak_f32 v187, v187, v183, 0x80730000\n"
    "s_add_u32 m0, s65, 25600\n"
    "s_add_u32 s72, s85, 1024\n"
    "buffer_load_dwordx4 v89, s[20:23], s72 offen offset:0 nt lds\n"
    "buffer_load_dwordx4 v[24:27], v162, s[28:31], s57 offen offset:3328\n"
    "v_ldexp_f32 v180, v180, s90\n"
    "v_ldexp_f32 v181, v181, s90\n"
    "v_ldexp_f32 v182, v182, s90\n"
    "v_ldexp_f32 v183, v183, s90\n"
    "v_fma_f32 v180, v180, 0, v180\n"
    "v_fma_f32 v181, v181, 0, v181\n"
    "v_fma_f32 v182, v182, 0, v182\n"
    "v_fma_f32 v183, v183, 0, v183\n"
    "ds_write_b128 v131, v[180:183] offset:0\n"
    "ds_write_b128 v131, v[184:187] offset:1024\n"
    "ds_read_b128 v[180:183], v132 offset:0\n"
    "ds_read_b128 v[184:187], v132 offset:1024\n"
    "ds_read_b128 v[188:191], v132 offset:128\n"
    "ds_read_b128 v[192:195], v132 offset:1152\n"
    "ds_read_b128 v[196:199], v132 offset:256\n"
    "ds_read_b128 v[200:203], v132 offset:1280\n"
    "s_nop 0\n"
    "s_nop 0\n"
    "s_nop 0\n"
    "s_nop 0\n"
    "s_nop 0\n"
    "s_nop 0\n"
    "s_nop 0\n"
    "s_add_u32 m0, s65, 26624\n"
    "s_add_u32 s72, s85, 2048\n"
    "buffer_load_dwordx4 v90, s[20:23], s72 offen offset:0 nt lds\n"
    "buffer_load_dwordx4 v[100:103], v162, s[28:31], s66 offen offset:0\n"
    "buffer_load_dwordx4 v[104:107], v162, s[28:31], s66 offen offset:3072\n"
    "s_nop 0\n"
    "s_nop 0\n"
    "s_nop 0\n"
    "s_nop 0\n"
    "s_nop 0\n"
    "s_nop 0\n"
    "s_nop 0\n"
    "s_nop 0\n"
    "s_nop 0\n"
    "s_nop 0\n"
    "s_nop 0\n"
    "s_nop 0\n"
    "s_nop 0\n"
    "s_nop 0\n"
    "s_nop 0\n"
    "s_nop 0\n"
    "s_nop 0\n"
    "s_nop 0\n"
    "s_nop 0\n"
    "s_nop 0\n"
    "s_nop 0\n"
    "s_nop 0\n"
    "s_add_u32 m0, s65, 27648\n"
    "s_add_u32 s72, s85, 3072\n"
    "buffer_load_dwordx4 v88, s[20:23], s72 offen offset:0 nt lds\n"
    "buffer_load_dwordx4 v[108:111], v162, s[28:31], s66 offen offset:128\n"
    "s_nop 0\n"
    "s_nop 0\n"
    "s_nop 0\n"
    "s_nop 0\n"
    "s_nop 0\n"
    "s_nop 0\n"
    "s_nop 0\n"
    "s_nop 0\n"
    "s_nop 0\n"
    "s_nop 0\n"
    "s_nop 0\n"
    "s_nop 0\n"
    "s_nop 0\n"
    "s_nop 0\n"
    "s_nop 0\n"
    "s_nop 0\n"
    "s_nop 0\n"
    "s_nop 0\n"
    "s_nop 0\n"
    "s_nop 0\n"
    "s_nop 0\n"
    "s_nop 0\n"
    "s_nop 0\n"
    "s_waitcnt lgkmcnt(0)\n"
    "s_waitcnt vmcnt(10)\n"
    "ds_add_rtn_u32 v245, v247, v246\n"
    "s_lshl_b32 s73, s53, 6\n"
    "s_add_u32 s73, s73, 128\n"
    "s_waitcnt lgkmcnt(0)\n"
    "v_readfirstlane_b32 s74, v245\n"
    "s_nop 0\n"
    "s_cmp_eq_u32 s74, 3\n"
    "s_cselect_b32 s73, s73, 0x7fff0000\n"
    "buffer_store_dwordx2 v[238:239], v91, s[36:39], s73 offen offset:0 sc1\n"
    "s_waitcnt vmcnt(28)\n"
    "s_add_u32 m0, s65, 28672\n"
    "s_add_u32 s72, s85, 4096\n"
    "buffer_load_dwordx4 v89, s[20:23], s72 offen offset:0 nt lds\n"
    "buffer_load_dwordx4 v[112:115], v162, s[28:31], s66 offen offset:3200\n"
    "buffer_load_dwordx4 v[116:119], v162, s[28:31], s66 offen offset:256\n"
    "s_add_u32 s54, s71, s60\n"
    "s_add_u32 s55, s71, s61\n"
    "s_add_u32 s56, s71, s62\n"
    "v_cvt_pk_u16_u32 v36, v36, v40\n"
    "v_cvt_pk_u16_u32 v37, v37, v41\n"
    "v_cvt_pk_u16_u32 v38, v38, v42\n"
    "v_cvt_pk_u16_u32 v39, v39, v43\n"
    "v_or3_b32 v243, v36, v37, v243\n"
    "v_or3_b32 v243, v38, v39, v243\n"
    "buffer_store_dword v36, v244, s[16:19], s54 offen offset:0 sc1\n"
    "buffer_store_dword v37, v244, s[16:19], s54 offen offset:1024 sc1\n"
    "buffer_store_dword v38, v244, s[16:19], s54 offen offset:2048 sc1\n"
    "buffer_store_dword v39, v244, s[16:19], s54 offen offset:3072 sc1\n"
    "v_cvt_pk_u16_u32 v44, v44, v48\n"
    "v_cvt_pk_u16_u32 v45, v45, v49\n"
    "v_cvt_pk_u16_u32 v46, v46, v50\n"
    "s_add_u32 m0, s65, 29696\n"
    "s_add_u32 s72, s85, 5120\n"
    "buffer_load_dwordx4 v90, s[20:23], s72 offen offset:0 nt lds\n"
    "buffer_load_dwordx4 v[120:123], v162, s[28:31], s66 offen offset:3328\n"
    "v_cvt_pk_u16_u32 v47, v47, v51\n"
    "v_or3_b32 v243, v44, v45, v243\n"
    "v_or3_b32 v243, v46, v47, v243\n"
    "buffer_store_dword v44, v244, s[16:19], s55 offen offset:0 sc1\n"
    "buffer_store_dword v45, v244, s[16:19], s55 offen offset:1024 sc1\n"
    "buffer_store_dword v46, v244, s[16:19], s55 offen offset:2048 sc1\n"
    "buffer_store_dword v47, v244, s[16:19], s55 offen offset:3072 sc1\n"
    "v_cvt_pk_u16_u32 v52, v52, v56\n"
    "v_cvt_pk_u16_u32 v53, v53, v57\n"
    "v_cvt_pk_u16_u32 v54, v54, v58\n"
    "v_cvt_pk_u16_u32 v55, v55, v59\n"
    "v_or3_b32 v243, v52, v53, v243\n"
    "v_or3_b32 v243, v54, v55, v243\n"
    "buffer_store_dword v52, v244, s[16:19], s56 offen offset:0 sc1\n"
    "buffer_store_dword v53, v244, s[16:19], s56 offen offset:1024 sc1\n"
    "buffer_store_dword v54, v244, s[16:19], s56 offen offset:2048 sc1\n"
    "buffer_store_dword v55, v244, s[16:19], s56 offen offset:3072 sc1\n"
    "s_add_u32 m0, s65, 73728\n"
    "s_add_u32 s72, s10, 196608\n"
    "buffer_load_dwordx4 v88, s[20:23], s72 offen offset:0 nt lds\n"
    "s_waitcnt vmcnt(44)\n"
    "v_lshlrev_b32 v204, 16, v136\n"
    "v_and_b32 v205, 0xffff0000, v136\n"
    "v_lshlrev_b32 v206, 16, v137\n"
    "v_and_b32 v207, 0xffff0000, v137\n"
    "v_lshlrev_b32 v208, 16, v138\n"
    "v_and_b32 v209, 0xffff0000, v138\n"
    "v_lshlrev_b32 v210, 16, v139\n"
    "v_and_b32 v211, 0xffff0000, v139\n"
    "v_log_f32 v68, v204\n"
    "v_log_f32 v69, v205\n"
    "v_log_f32 v70, v206\n"
    "v_log_f32 v71, v207\n"
    "v_log_f32 v72, v208\n"
    "v_log_f32 v73, v209\n"
    "v_log_f32 v74, v210\n"
    "v_log_f32 v75, v211\n"
    "v_rcp_f32 v204, v204\n"
    "v_rcp_f32 v205, v205\n"
    "v_rcp_f32 v206, v206\n"
    "s_add_u32 m0, s65, 74752\n"
    "s_add_u32 s72, s10, 197632\n"
    "buffer_load_dwordx4 v89, s[20:23], s72 offen offset:0 nt lds\n"
    "v_rcp_f32 v207, v207\n"
    "v_rcp_f32 v208, v208\n"
    "v_rcp_f32 v209, v209\n"
    "v_rcp_f32 v210, v210\n"
    "v_rcp_f32 v211, v211\n"
    "s_mov_b32 s73, 0x41a00000\n"
    "v_mov_b32 v228, 0x7fc00000\n"
    "v_cmp_le_f32 vcc, |v68|, s73\n"
    "s_nop 1\n"
    "v_cndmask_b32 v68, v228, v68, vcc\n"
    "v_cmp_le_f32 vcc, |v69|, s73\n"
    "s_nop 1\n"
    "v_cndmask_b32 v69, v228, v69, vcc\n"
    "v_cmp_le_f32 vcc, |v70|, s73\n"
    "s_nop 1\n"
    "v_cndmask_b32 v70, v228, v70, vcc\n"
    "v_cmp_le_f32 vcc, |v71|, s73\n"
    "s_nop 1\n"
    "v_cndmask_b32 v71, v228, v71, vcc\n"
    "v_cmp_le_f32 vcc, |v72|, s73\n"
    "s_nop 1\n"
    "v_cndmask_b32 v72, v228, v72, vcc\n"
    "v_cmp_le_f32 vcc, |v73|, s73\n"
    "s_nop 1\n"
    "v_cndmask_b32 v73, v228, v73, vcc\n"
    "v_cmp_le_f32 vcc, |v74|, s73\n"
    "s_nop 1\n"
    "v_cndmask_b32 v74, v228, v74, vcc\n"
    "s_add_u32 m0, s65, 75776\n"
    "s_add_u32 s72, s10, 198656\n"
    "buffer_load_dwordx4 v90, s[20:23], s72 offen offset:0 nt lds\n"
    "v_cmp_le_f32 vcc, |v75|, s73\n"
    "s_nop 1\n"
    "v_cndmask_b32 v75, v228, v75, vcc\n"
    "ds_write_b128 v86, v[68:71] offset:0\n"
    "ds_write_b128 v86, v[72:75] offset:16\n"
    "v_cvt_pk_bf16_f32 v220, v204, v205\n"
    "v_cvt_pk_bf16_f32 v221, v206, v207\n"
    "v_cvt_pk_bf16_f32 v222, v208, v209\n"
    "v_cvt_pk_bf16_f32 v223, v210, v211\n"
    "v_lshlrev_b32 v212, 16, v220\n"
    "v_and_b32 v213, 0xffff0000, v220\n"
    "v_lshlrev_b32 v214, 16, v221\n"
    "v_and_b32 v215, 0xffff0000, v221\n"
    "v_lshlrev_b32 v216, 16, v222\n"
    "v_and_b32 v217, 0xffff0000, v222\n"
    "v_lshlrev_b32 v218, 16, v223\n"
    "v_and_b32 v219, 0xffff0000, v223\n"
    "v_sub_f32 v204, v204, v212\n"
    "v_sub_f32 v205, v205, v213\n"
    "v_sub_f32 v206, v206, v214\n"
    "v_sub_f32 v207, v207, v215\n"
    "v_sub_f32 v208, v208, v216\n"
    "s_add_u32 m0, s65, 76800\n"
    "s_add_u32 s72, s10, 199680\n"
    "buffer_load_dwordx4 v88, s[20:23], s72 offen offset:0 nt lds\n"
    "v_sub_f32 v209, v209, v217\n"
    "v_sub_f32 v210, v210, v218\n"
    "v_sub_f32 v211, v211, v219\n"
    "v_cvt_pk_bf16_f32 v224, v204, v205\n"
    "v_cvt_pk_bf16_f32 v225, v206, v207\n"
    "v_cvt_pk_bf16_f32 v226, v208, v209\n"
    "v_cvt_pk_bf16_f32 v227, v210, v211\n"
    "v_mov_b32 v204, 0\n"
    "v_mov_b32 v205, 0\n"
    "v_mov_b32 v206, 0\n"
    "v_mov_b32 v207, 0\n"
    "v_swap_b32 v221, v222\n"
    "v_swap_b32 v225, v226\n"
    "v_swap_b32 v141, v142\n"
    "ds_write2_b64 v144, v[220:221], v[222:223] offset0:32 offset1:40\n"
    "ds_write2_b64 v144, v[224:225], v[226:227] offset0:48 offset1:56\n"
    "ds_write2_b64 v144, v[140:141], v[142:143] offset0:64 offset1:72\n"
    "ds_write2_b64 v144, v[204:205], v[206:207] offset0:80 offset1:88\n"
    "s_waitcnt vmcnt(27)\n"
    "v_fma_f32 v4, v4, v180, -v184\n"
    "v_fma_f32 v5, v5, v181, -v185\n"
    "s_add_u32 m0, s65, 77824\n"
    "s_add_u32 s72, s10, 200704\n"
    "buffer_load_dwordx4 v89, s[20:23], s72 offen offset:0 nt lds\n"
    "v_fma_f32 v6, v6, v182, -v186\n"
    "v_fma_f32 v7, v7, v183, -v187\n"
    "v_fma_f32 v8, v8, v180, -v184\n"
    "v_fma_f32 v9, v9, v181, -v185\n"
    "v_fma_f32 v10, v10, v182, -v186\n"
    "v_fma_f32 v11, v11, v183, -v187\n"
    "v_cvt_pk_bf16_f32 v4, v4, v5\n"
    "v_cvt_pk_bf16_f32 v5, v6, v7\n"
    "v_cvt_pk_bf16_f32 v6, v8, v9\n"
    "v_cvt_pk_bf16_f32 v7, v10, v11\n"
    "ds_write_b64 v163, v[4:5] offset:0\n"
    "ds_write_b64 v163, v[6:7] offset:1024\n"
    "v_fma_f32 v12, v12, v188, -v192\n"
    "v_fma_f32 v13, v13, v189, -v193\n"
    "v_fma_f32 v14, v14, v190, -v194\n"
    "v_fma_f32 v15, v15, v191, -v195\n"
    "v_fma_f32 v16, v16, v188, -v192\n"
    "v_fma_f32 v17, v17, v189, -v193\n"
    "v_fma_f32 v18, v18, v190, -v194\n"
    "v_fma_f32 v19, v19, v191, -v195\n"
    "s_add_u32 m0, s65, 78848\n"
    "s_add_u32 s72, s10, 201728\n"
    "buffer_load_dwordx4 v90, s[20:23], s72 offen offset:0 nt lds\n"
    "v_cvt_pk_bf16_f32 v12, v12, v13\n"
    "v_cvt_pk_bf16_f32 v13, v14, v15\n"
    "v_cvt_pk_bf16_f32 v14, v16, v17\n"
    "v_cvt_pk_bf16_f32 v15, v18, v19\n"
    "ds_write_b64 v163, v[12:13] offset:2048\n"
    "ds_write_b64 v163, v[14:15] offset:3072\n"
    "v_fma_f32 v20, v20, v196, -v200\n"
    "v_fma_f32 v21, v21, v197, -v201\n"
    "v_fma_f32 v22, v22, v198, -v202\n"
    "v_fma_f32 v23, v23, v199, -v203\n"
    "v_fma_f32 v24, v24, v196, -v200\n"
    "v_fma_f32 v25, v25, v197, -v201\n"
    "v_fma_f32 v26, v26, v198, -v202\n"
    "v_fma_f32 v27, v27, v199, -v203\n"
    "v_cvt_pk_bf16_f32 v20, v20, v21\n"
    "v_cvt_pk_bf16_f32 v21, v22, v23\n"
    "v_cvt_pk_bf16_f32 v22, v24, v25\n"
    "v_cvt_pk_bf16_f32 v23, v26, v27\n"
    "ds_write_b64 v163, v[20:21] offset:4096\n"
    "ds_write_b64 v163, v[22:23] offset:5120\n"
    "s_waitcnt vmcnt(14)\n"
    "s_add_u32 m0, s65, 49152\n"
    "s_add_u32 s72, s10, 393216\n"
    "buffer_load_dwordx4 v88, s[20:23], s72 offen offset:0 nt lds\n"
    "v_fma_f32 v100, v100, v180, -v184\n"
    "v_fma_f32 v101, v101, v181, -v185\n"
    "v_fma_f32 v102, v102, v182, -v186\n"
    "v_fma_f32 v103, v103, v183, -v187\n"
    "v_fma_f32 v104, v104, v180, -v184\n"
    "v_fma_f32 v105, v105, v181, -v185\n"
    "v_fma_f32 v106, v106, v182, -v186\n"
    "v_fma_f32 v107, v107, v183, -v187\n"
    "v_cvt_pk_bf16_f32 v100, v100, v101\n"
    "v_cvt_pk_bf16_f32 v101, v102, v103\n"
    "v_cvt_pk_bf16_f32 v102, v104, v105\n"
    "v_cvt_pk_bf16_f32 v103, v106, v107\n"
    "ds_write_b64 v163, v[100:101] offset:12288\n"
    "ds_write_b64 v163, v[102:103] offset:13312\n"
    "v_fma_f32 v108, v108, v188, -v192\n"
    "v_fma_f32 v109, v109, v189, -v193\n"
    "v_fma_f32 v110, v110, v190, -v194\n"
    "v_fma_f32 v111, v111, v191, -v195\n"
    "v_fma_f32 v112, v112, v188, -v192\n"
    "v_fma_f32 v113, v113, v189, -v193\n"
    "v_fma_f32 v114, v114, v190, -v194\n"
    "s_add_u32 m0, s65, 50176\n"
    "s_add_u32 s72, s10, 394240\n"
    "buffer_load_dwordx4 v89, s[20:23], s72 offen offset:0 nt lds\n"
    "v_fma_f32 v115, v115, v191, -v195\n"
    "v_cvt_pk_bf16_f32 v108, v108, v109\n"
    "v_cvt_pk_bf16_f32 v109, v110, v111\n"
    "v_cvt_pk_bf16_f32 v110, v112, v113\n"
    "v_cvt_pk_bf16_f32 v111, v114, v115\n"
    "ds_write_b64 v163, v[108:109] offset:14336\n"
    "ds_write_b64 v163, v[110:111] offset:15360\n"
    "v_fma_f32 v116, v116, v196, -v200\n"
    "v_fma_f32 v117, v117, v197, -v201\n"
    "v_fma_f32 v118, v118, v198, -v202\n"
    "v_fma_f32 v119, v119, v199, -v203\n"
    "v_fma_f32 v120, v120, v196, -v200\n"
    "v_fma_f32 v121, v121, v197, -v201\n"
    "v_fma_f32 v122, v122, v198, -v202\n"
    "v_fma_f32 v123, v123, v199, -v203\n"
    "v_cvt_pk_bf16_f32 v116, v116, v117\n"
    "v_cvt_pk_bf16_f32 v117, v118, v119\n"
    "v_cvt_pk_bf16_f32 v118, v120, v121\n"
    "v_cvt_pk_bf16_f32 v119, v122, v123\n"
    "ds_write_b64 v163, v[116:117] offset:16384\n"
    "ds_write_b64 v163, v[118:119] offset:17408\n"
    "s_waitcnt vmcnt(17)\n"
    "ds_read_b128 v[36:39], v154 offset:0\n"
    "ds_read_b128 v[40:43], v155 offset:0\n"
    "ds_read_b128 v[44:47], v154 offset:128\n"
    "ds_read_b128 v[48:51], v155 offset:128\n"
    "ds_read_b128 v[52:55], v154 offset:256\n"
    "ds_read_b128 v[56:59], v155 offset:256\n"
    "s_mov_b64 exec, 0\n"
    "s_bitcmp1_b32 s83, 1\n"
    "s_cbranch_scc1 .LwV\n"
    "s_cmp_eq_u32 0, 0\n"
    "s_branch .LwK\n"
    ".Lw2:\n"
    "s_bitcmp1_b32 s83, 1\n"
    "s_cbranch_scc1 .Lwp1\n"
    "s_branch .Lwp0\n"
    ".Lw3:\n"
    "s_mov_b64 exec, -1\n"
    "s_waitcnt lgkmcnt(5)\n"
    "v_pk_maximum3_f16 v98, v36, v37, v98\n"
    "v_pk_maximum3_f16 v98, v38, v39, v98\n"
    "v_pk_add_u16 v36, s89, v36 clamp\n"
    "v_pk_add_u16 v37, s89, v37 clamp\n"
    "v_pk_add_u16 v38, s89, v38 clamp\n"
    "v_pk_add_u16 v39, s89, v39 clamp\n"
    "s_waitcnt lgkmcnt(4)\n"
    "v_pk_maximum3_f16 v98, v40, v41, v98\n"
    "v_pk_maximum3_f16 v98, v42, v43, v98\n"
    "v_pk_add_u16 v40, s89, v40 clamp\n"
    "v_pk_add_u16 v41, s89, v41 clamp\n"
    "v_pk_add_u16 v42, s89, v42 clamp\n"
    "v_pk_add_u16 v43, s89, v43 clamp\n"
    "s_waitcnt lgkmcnt(3)\n"
    "v_pk_maximum3_f16 v98, v44, v45, v98\n"
    "v_pk_maximum3_f16 v98, v46, v47, v98\n"
    "v_pk_add_u16 v44, s89, v44 clamp\n"
    "v_pk_add_u16 v45, s89, v45 clamp\n"
    "v_pk_add_u16 v46, s89, v46 clamp\n"
    "v_pk_add_u16 v47, s89, v47 clamp\n"
    "s_waitcnt lgkmcnt(2)\n"
    "v_pk_maximum3_f16 v98, v48, v49, v98\n"
    "v_pk_maximum3_f16 v98, v50, v51, v98\n"
    "v_pk_add_u16 v48, s89, v48 clamp\n"
    "v_pk_add_u16 v49, s89, v49 clamp\n"
    "v_pk_add_u16 v50, s89, v50 clamp\n"
    "v_pk_add_u16 v51, s89, v51 clamp\n"
    "s_waitcnt lgkmcnt(1)\n"
    "v_pk_maximum3_f16 v98, v52, v53, v98\n"
    "v_pk_maximum3_f16 v98, v54, v55, v98\n"
    "v_pk_add_u16 v52, s89, v52 clamp\n"
    "v_pk_add_u16 v53, s89, v53 clamp\n"
    "v_pk_add_u16 v54, s89, v54 clamp\n"
    "v_pk_add_u16 v55, s89, v55 clamp\n"
    "s_waitcnt lgkmcnt(0)\n"
    "v_pk_maximum3_f16 v98, v56, v57, v98\n"
    "v_pk_maximum3_f16 v98, v58, v59, v98\n"
    "v_pk_add_u16 v56, s89, v56 clamp\n"
    "v_pk_add_u16 v57, s89, v57 clamp\n"
    "v_pk_add_u16 v58, s89, v58 clamp\n"
    "v_pk_add_u16 v59, s89, v59 clamp\n"
    "v_mov_b32 v108, 0\n"
    "v_mov_b32 v109, 0\n"
    "v_mov_b32 v110, 0\n"
    "v_mov_b32 v111, 0\n"
    "v_mov_b32 v112, 0\n"
    "v_mov_b32 v113, 0\n"
    "v_mov_b32 v114, 0\n"
    "v_mov_b32 v115, 0\n"
    "v_mov_b32 v116, 0\n"
    "v_mov_b32 v117, 0\n"
    "v_mov_b32 v118, 0\n"
    "v_mov_b32 v119, 0\n"
    "v_mov_b32 v120, 0\n"
    "v_mov_b32 v121, 0\n"
    "v_mov_b32 v122, 0\n"
    "v_mov_b32 v123, 0\n"
    "s_mul_i32 s73, s79, 768\n"
    "v_add_u32 v78, s73, v250\n"
    "v_mov_b32 v79, 155936\n"
    "s_lshl2_add_u32 s74, s84, 155936\n"
    "v_mov_b32 v77, 155952\n"
    "v_writelane_b32 v77, s74, 63\n"
    "s_waitcnt lgkmcnt(0)\n"
    "s_barrier\n"
    "ds_read_b128 a[112:115], v252 offset:0\n"
    "ds_read_b128 a[116:119], v252 offset:2048\n"
    "ds_read_b128 a[120:123], v252 offset:4096\n"
    "ds_read_b128 a[124:127], v252 offset:6144\n"
    "ds_read_b128 a[128:131], v252 offset:8192\n"
    "ds_read_b128 a[132:135], v252 offset:10240\n"
    "ds_read_b128 a[136:139], v252 offset:1024\n"
    "ds_read_b128 a[140:143], v252 offset:3072\n"
    "ds_read_b128 a[144:147], v252 offset:5120\n"
    "ds_read_b128 a[148:151], v252 offset:7168\n"
    "ds_read_b128 a[152:155], v252 offset:9216\n"
    "ds_read_b128 a[156:159], v252 offset:11264\n"
    "s_add_u32 m0, s65, 51200\n"
    "s_add_u32 s72, s10, 395264\n"
    "buffer_load_dwordx4 v90, s[20:23], s72 offen offset:0 nt lds\n"
    "s_add_u32 m0, s65, 52224\n"
    "s_add_u32 s72, s10, 396288\n"
    "buffer_load_dwordx4 v88, s[20:23], s72 offen offset:0 nt lds\n"
    "s_waitcnt lgkmcnt(11)\n"
    "v_mfma_f32_16x16x32_bf16 v[4:7], a[112:115], v[36:39], 0\n"
    "s_waitcnt lgkmcnt(10)\n"
    "v_mfma_f32_16x16x32_bf16 v[4:7], a[116:119], v[40:43], v[4:7]\n"
    "s_waitcnt lgkmcnt(9)\n"
    "v_mfma_f32_16x16x32_bf16 v[4:7], a[120:123], v[44:47], v[4:7]\n"
    "s_waitcnt lgkmcnt(8)\n"
    "v_mfma_f32_16x16x32_bf16 v[4:7], a[124:127], v[48:51], v[4:7]\n"
    "s_waitcnt lgkmcnt(7)\n"
    "v_mfma_f32_16x16x32_bf16 v[4:7], a[128:131], v[52:55], v[4:7]\n"
    "s_waitcnt lgkmcnt(6)\n"
    "v_mfma_f32_16x16x32_bf16 v[4:7], a[132:135], v[56:59], v[4:7]\n"
    "s_waitcnt lgkmcnt(5)\n"
    "v_mfma_f32_16x16x32_bf16 v[20:23], a[136:139], v[36:39], 0\n"
    "v_accvgpr_write_b32 a192, v36\n"
    "v_accvgpr_write_b32 a193, v37\n"
    "v_accvgpr_write_b32 a194, v38\n"
    "v_accvgpr_write_b32 a195, v39\n"
    "s_waitcnt lgkmcnt(4)\n"
    "v_mfma_f32_16x16x32_bf16 v[20:23], a[140:143], v[40:43], v[20:23]\n"
    "v_accvgpr_write_b32 a196, v40\n"
    "v_accvgpr_write_b32 a197, v41\n"
    "v_accvgpr_write_b32 a198, v42\n"
    "v_accvgpr_write_b32 a199, v43\n"
    "s_waitcnt lgkmcnt(3)\n"
    "v_mfma_f32_16x16x32_bf16 v[20:23], a[144:147], v[44:47], v[20:23]\n"
    "v_accvgpr_write_b32 a200, v44\n"
    "v_accvgpr_write_b32 a201, v45\n"
    "v_accvgpr_write_b32 a202, v46\n"
    "v_accvgpr_write_b32 a203, v47\n"
    "s_waitcnt lgkmcnt(2)\n"
    "v_mfma_f32_16x16x32_bf16 v[20:23], a[148:151], v[48:51], v[20:23]\n"
    "v_accvgpr_write_b32 a204, v48\n"
    "v_accvgpr_write_b32 a205, v49\n"
    "v_accvgpr_write_b32 a206, v50\n"
    "v_accvgpr_write_b32 a207, v51\n"
    "s_waitcnt lgkmcnt(1)\n"
    "v_mfma_f32_16x16x32_bf16 v[20:23], a[152:155], v[52:55], v[20:23]\n"
    "v_accvgpr_write_b32 a208, v52\n"
    "v_accvgpr_write_b32 a209, v53\n"
    "v_accvgpr_write_b32 a210, v54\n"
    "v_accvgpr_write_b32 a211, v55\n"
    "s_waitcnt lgkmcnt(0)\n"
    "v_mfma_f32_16x16x32_bf16 v[20:23], a[156:159], v[56:59], v[20:23]\n"
    "v_accvgpr_write_b32 a212, v56\n"
    "v_accvgpr_write_b32 a213, v57\n"
    "v_accvgpr_write_b32 a214, v58\n"
    "v_accvgpr_write_b32 a215, v59\n"
    "s_add_u32 m0, s65, 53248\n"
    "s_add_u32 s72, s10, 397312\n"
    "buffer_load_dwordx4 v89, s[20:23], s72 offen offset:0 nt lds\n"
    "buffer_load_dwordx2 v[236:237], v87, s[36:39], 0 offen offset:0 sc1\n"
    "ds_read_b128 v[16:19], v78 offset:0\n"
    "ds_read_b128 v[32:35], v78 offset:16\n"
    "ds_read_b128 v[48:51], v78 offset:128\n"
    "ds_read_b128 v[64:67], v78 offset:144\n"
    "ds_read_b128 v[100:103], v78 offset:768\n"
    "ds_read_b128 v[104:107], v78 offset:784\n"
    "v_max3_f32 v76, v4, v5, v6\n"
    "ds_read_b128 a[0:3], v252 offset:12288\n"
    "v_max3_f32 v76, v76, v7, v20\n"
    "ds_read_b128 a[4:7], v252 offset:14336\n"
    "v_max3_f32 v76, v76, v21, v22\n"
    "ds_read_b128 a[8:11], v252 offset:16384\n"
    "v_max_f32 v76, v76, v23\n"
    "ds_read_b128 a[12:15], v252 offset:18432\n"
    "s_nop 0\n"
    "v_max_f32_dpp v76, v76, v76 row_shr:1\n"
    "ds_read_b128 a[16:19], v252 offset:20480\n"
    "ds_read_b128 a[20:23], v252 offset:22528\n"
    "v_max_f32_dpp v76, v76, v76 row_shr:2\n"
    "ds_read_b128 a[24:27], v252 offset:13312\n"
    "s_nop 0\n"
    "v_max_f32_dpp v76, v76, v76 row_shr:4\n"
    "ds_read_b128 a[28:31], v252 offset:15360\n"
    "s_nop 0\n"
    "v_max_f32_dpp v76, v76, v76 row_shr:8\n"
    "ds_read_b128 a[32:35], v252 offset:17408\n"
    "s_nop 0\n"
    "v_max_f32_dpp v76, v76, v76 row_bcast:15 row_mask:0xa\n"
    "ds_read_b128 a[36:39], v252 offset:19456\n"
    "s_nop 0\n"
    "v_max_f32_dpp v76, v76, v76 row_bcast:31 row_mask:0xc\n"
    "ds_read_b128 a[40:43], v252 offset:21504\n"
    "ds_read_b128 a[44:47], v252 offset:23552\n"
    "ds_write_b32 v77, v76\n"
    "s_add_u32 m0, s65, 54272\n"
    "s_add_u32 s72, s10, 398336\n"
    "buffer_load_dwordx4 v90, s[20:23], s72 offen offset:0 nt lds\n"
    "s_add_u32 m0, s65, 98304\n"
    "s_add_u32 s72, s10, 589824\n"
    "buffer_load_dwordx4 v88, s[20:23], s72 offen offset:0 nt lds\n"
    "s_waitcnt lgkmcnt(0)\n"
    "s_barrier\n"
    "ds_read_b128 v[80:83], v79 offset:0\n"
    "s_waitcnt vmcnt(14)\n"
    "buffer_store_dwordx2 v[238:239], v240, s[36:39], s80 offen offset:0 sc1\n"
    "s_lshl_b32 s73, s79, 8\n"
    "s_add_u32 s80, s80, s73\n"
    "s_mul_i32 s73, s79, 98304\n"
    "s_add_u32 s71, s71, s73\n"
    "s_add_u32 m0, s65, 99328\n"
    "s_add_u32 s72, s10, 590848\n"
    "buffer_load_dwordx4 v89, s[20:23], s72 offen offset:0 nt lds\n"
    "s_waitcnt lgkmcnt(0)\n"
    "v_max3_f32 v76, v80, v81, v82\n"
    "v_max_f32 v76, v76, v83\n"
    "v_add_f32 v76, 0xc2b00000, v76\n"
    "s_add_u32 m0, s65, 100352\n"
    "s_add_u32 s72, s10, 591872\n"
    "buffer_load_dwordx4 v90, s[20:23], s72 offen offset:0 nt lds\n"
    "s_bitcmp1_b32 s83, 2\n"
    "s_cbranch_scc1 .Lf5b\n"
    "s_sleep 0\n"
    "s_add_u32 m0, s65, 73728\n"
    "s_add_u32 s72, s85, 196608\n"
    "buffer_load_dwordx4 v88, s[20:23], s72 offen offset:0 nt lds\n"
    "s_add_u32 m0, s65, 74752\n"
    "s_add_u32 s72, s85, 197632\n"
    "buffer_load_dwordx4 v89, s[20:23], s72 offen offset:0 nt lds\n"
    "s_add_u32 m0, s65, 75776\n"
    "s_add_u32 s72, s85, 198656\n"
    "buffer_load_dwordx4 v90, s[20:23], s72 offen offset:0 nt lds\n"
    "s_add_u32 m0, s65, 76800\n"
    "s_add_u32 s72, s85, 199680\n"
    "buffer_load_dwordx4 v88, s[20:23], s72 offen offset:0 nt lds\n"
    "s_add_u32 m0, s65, 77824\n"
    "s_add_u32 s72, s85, 200704\n"
    "buffer_load_dwordx4 v89, s[20:23], s72 offen offset:0 nt lds\n"
    "s_add_u32 m0, s65, 78848\n"
    "s_add_u32 s72, s85, 201728\n"
    "buffer_load_dwordx4 v90, s[20:23], s72 offen offset:0 nt lds\n"
    "s_add_u32 m0, s65, 49152\n"
    "s_add_u32 s72, s85, 393216\n"
    "buffer_load_dwordx4 v88, s[20:23], s72 offen offset:0 nt lds\n"
    "s_add_u32 m0, s65, 50176\n"
    "s_add_u32 s72, s85, 394240\n"
    "buffer_load_dwordx4 v89, s[20:23], s72 offen offset:0 nt lds\n"
    "s_add_u32 m0, s65, 51200\n"
    "s_add_u32 s72, s85, 395264\n"
    "buffer_load_dwordx4 v90, s[20:23], s72 offen offset:0 nt lds\n"
    "s_add_u32 m0, s65, 52224\n"
    "s_add_u32 s72, s85, 396288\n"
    "buffer_load_dwordx4 v88, s[20:23], s72 offen offset:0 nt lds\n"
    "s_add_u32 m0, s65, 53248\n"
    "s_add_u32 s72, s85, 397312\n"
    "buffer_load_dwordx4 v89, s[20:23], s72 offen offset:0 nt lds\n"
    "s_add_u32 m0, s65, 54272\n"
    "s_add_u32 s72, s85, 398336\n"
    "buffer_load_dwordx4 v90, s[20:23], s72 offen offset:0 nt lds\n"
    "s_add_u32 m0, s65, 98304\n"
    "s_add_u32 s72, s85, 589824\n"
    "buffer_load_dwordx4 v88, s[20:23], s72 offen offset:0 nt lds\n"
    "s_add_u32 m0, s65, 99328\n"
    "s_add_u32 s72, s85, 590848\n"
    "buffer_load_dwordx4 v89, s[20:23], s72 offen offset:0 nt lds\n"
    "s_add_u32 m0, s65, 100352\n"
    "s_add_u32 s72, s85, 591872\n"
    "buffer_load_dwordx4 v90, s[20:23], s72 offen offset:0 nt lds\n"
    "s_branch .Lf5e\n"
    ".Lf5b:\n"
    ".Lf5e:\n"
    "s_nop 4\n"
    "s_add_u32 m0, s65, 101376\n"
    "s_add_u32 s72, s85, 592896\n"
    "buffer_load_dwordx4 v88, s[20:23], s72 offen offset:0 nt lds\n"
    "s_add_u32 m0, s65, 102400\n"
    "s_add_u32 s72, s85, 593920\n"
    "buffer_load_dwordx4 v89, s[20:23], s72 offen offset:0 nt lds\n"
    "s_add_u32 m0, s65, 103424\n"
    "s_add_u32 s72, s85, 594944\n"
    "buffer_load_dwordx4 v90, s[20:23], s72 offen offset:0 nt lds\n"
    "v_sub_f32 v48, v48, v76\n"
    "v_sub_f32 v51, v51, v76\n"
    "v_sub_f32 v49, v49, v76\n"
    "v_sub_f32 v50, v50, v76\n"
    "v_sub_f32 v66, v66, v76\n"
    "v_sub_f32 v65, v65, v76\n"
    "v_mfma_f32_16x16x32_bf16 v[36:39], a[0:3], a[192:195], v[48:51]\n"
    "s_waitcnt vmcnt(12)\n"
    "ds_read_b128 v[200:203], v97 offset:24832\n"
    "v_sub_f32 v67, v67, v76\n"
    "v_mfma_f32_16x16x32_bf16 v[36:39], a[4:7], a[196:199], v[36:39]\n"
    "v_sub_f32 v33, v33, v76\n"
    "v_sub_f32 v32, v32, v76\n"
    "v_mfma_f32_16x16x32_bf16 v[36:39], a[8:11], a[200:203], v[36:39]\n"
    "ds_read_b128 v[184:187], v97 offset:24576\n"
    "ds_read_b128 v[196:199], v96 offset:24832\n"
    "ds_read_b128 v[192:195], v97 offset:24704\n"
    "v_mfma_f32_16x16x32_bf16 v[36:39], a[12:15], a[204:207], v[36:39]\n"
    "v_sub_f32 v64, v64, v76\n"
    "v_sub_f32 v35, v35, v76\n"
    "v_sub_f32 v19, v19, v76\n"
    "v_mfma_f32_16x16x32_bf16 v[52:55], a[24:27], a[192:195], v[64:67]\n"
    "v_add_f32 v21, v21, v33\n"
    "v_sub_f32 v16, v16, v76\n"
    "v_mfma_f32_16x16x32_bf16 v[36:39], a[16:19], a[208:211], v[36:39]\n"
    "v_sub_f32 v18, v18, v76\n"
    "v_add_f32 v20, v20, v32\n"
    "v_sub_f32 v17, v17, v76\n"
    "v_mfma_f32_16x16x32_bf16 v[52:55], a[28:31], a[196:199], v[52:55]\n"
    "ds_read_b128 v[188:191], v96 offset:24704\n"
    "ds_read_b128 v[180:183], v96 offset:24576\n"
    "v_add_f32 v6, v6, v18\n"
    "v_sub_f32 v34, v34, v76\n"
    "v_mfma_f32_16x16x32_bf16 v[36:39], a[20:23], a[212:215], v[36:39]\n"
    "s_waitcnt lgkmcnt(5)\n"
    "v_pk_maximum3_f16 v98, v200, v201, v98\n"
    "v_exp_f32 v20, v20\n"
    "v_pk_maximum3_f16 v98, v202, v203, v98\n"
    "v_pk_add_u16 v203, s89, v203 clamp\n"
    "v_pk_add_u16 v200, s89, v200 clamp\n"
    "v_mfma_f32_16x16x32_bf16 v[52:55], a[32:35], a[200:203], v[52:55]\n"
    "s_waitcnt lgkmcnt(3)\n"
    "v_pk_maximum3_f16 v98, v196, v197, v98\n"
    "v_pk_maximum3_f16 v98, v198, v199, v98\n"
    "v_mfma_f32_16x16x32_bf16 v[52:55], a[36:39], a[204:207], v[52:55]\n"
    "v_exp_f32 v21, v21\n"
    "v_pk_add_u16 v202, s89, v202 clamp\n"
    "v_pk_add_u16 v201, s89, v201 clamp\n"
    "v_mfma_f32_16x16x32_bf16 v[52:55], a[40:43], a[208:211], v[52:55]\n"
    "v_add_f32 v22, v22, v34\n"
    "v_add_f32 v7, v7, v19\n"
    "v_mfma_f32_16x16x32_bf16 v[24:27], a[156:159], v[200:203], v[32:35]\n"
    "s_waitcnt lgkmcnt(2)\n"
    "v_pk_maximum3_f16 v98, v192, v193, v98\n"
    "v_pk_maximum3_f16 v98, v194, v195, v98\n"
    "v_pk_add_u16 v193, s89, v193 clamp\n"
    "v_pk_add_u16 v198, s89, v198 clamp\n"
    "v_add_f32 v5, v5, v17\n"
    "v_mfma_f32_16x16x32_bf16 v[52:55], a[44:47], a[212:215], v[52:55]\n"
    "s_waitcnt lgkmcnt(1)\n"
    "v_pk_add_u16 v194, s89, v194 clamp\n"
    "v_sub_f32 v69, v69, v76\n"
    "v_sub_f32 v73, v73, v76\n"
    "v_pk_maximum3_f16 v98, v188, v189, v98\n"
    "v_pk_maximum3_f16 v98, v190, v191, v98\n"
    "v_pk_add_u16 v196, s89, v196 clamp\n"
    "v_pk_maximum3_f16 v98, v184, v185, v98\n"
    "v_pk_add_u16 v197, s89, v197 clamp\n"
    "v_pk_maximum3_f16 v98, v186, v187, v98\n"
    "v_mfma_f32_16x16x32_bf16 v[8:11], a[132:135], v[200:203], v[16:19]\n"
    "v_sub_f32 v75, v75, v76\n"
    "v_pk_add_u16 v199, s89, v199 clamp\n"
    "v_sub_f32 v101, v101, v76\n"
    "v_exp_f32 v54, v54\n"
    "v_mfma_f32_16x16x32_bf16 v[24:27], a[152:155], v[196:199], v[24:27]\n"
    "s_waitcnt lgkmcnt(0)\n"
    "v_pk_add_u16 v188, s89, v188 clamp\n"
    "v_pk_add_u16 v184, s89, v184 clamp\n"
    "v_sub_f32 v70, v70, v76\n"
    "v_pk_maximum3_f16 v98, v180, v181, v98\n"
    "v_pk_add_u16 v186, s89, v186 clamp\n"
    "v_mfma_f32_16x16x32_bf16 v[56:59], a[44:47], v[200:203], v[64:67]\n"
    "v_pk_add_u16 v189, s89, v189 clamp\n"
    "v_pk_add_u16 v195, s89, v195 clamp\n"
    "v_pk_add_u16 v192, s89, v192 clamp\n"
    "v_pk_add_u16 v185, s89, v185 clamp\n"
    "v_pk_maximum3_f16 v98, v182, v183, v98\n"
    "v_exp_f32 v55, v55\n"
    "v_sub_f32 v68, v68, v76\n"
    "v_mfma_f32_16x16x32_bf16 v[56:59], a[40:43], v[196:199], v[56:59]\n"
    "v_exp_f32 v52, v52\n"
    "v_pk_add_u16 v190, s89, v190 clamp\n"
    "v_mfma_f32_16x16x32_bf16 v[8:11], a[128:131], v[196:199], v[8:11]\n"
    "v_accvgpr_write_b32 a231, v195\n"
    "v_sub_f32 v102, v102, v76\n"
    "v_sub_f32 v71, v71, v76\n"
    "v_sub_f32 v107, v107, v76\n"
    "v_mfma_f32_16x16x32_bf16 v[40:43], a[20:23], v[200:203], v[48:51]\n"
    "v_sub_f32 v74, v74, v76\n"
    "v_add_f32 v23, v23, v35\n"
    "v_mfma_f32_16x16x32_bf16 v[40:43], a[16:19], v[196:199], v[40:43]\n"
    "v_pk_add_u16 v183, s89, v183 clamp\n"
    "v_accvgpr_write_b32 a228, v192\n"
    "v_mfma_f32_16x16x32_bf16 v[56:59], a[36:39], v[192:195], v[56:59]\n"
    "v_add_f32 v4, v4, v16\n"
    "v_exp_f32 v6, v6\n"
    "v_mfma_f32_16x16x32_bf16 v[24:27], a[148:151], v[192:195], v[24:27]\n"
    "v_sub_f32 v72, v72, v76\n"
    "v_pk_add_u16 v187, s89, v187 clamp\n"
    "v_pk_add_u16 v191, s89, v191 clamp\n"
    "v_mfma_f32_16x16x32_bf16 v[8:11], a[124:127], v[192:195], v[8:11]\n"
    "v_exp_f32 v53, v53\n"
    "v_accvgpr_write_b32 a226, v190\n"
    "v_mfma_f32_16x16x32_bf16 v[8:11], a[120:123], v[188:191], v[8:11]\n"
    "v_accvgpr_write_b32 a221, v185\n"
    "v_exp_f32 v39, v39\n"
    "v_exp_f32 v22, v22\n"
    "v_pk_add_u16 v181, s89, v181 clamp\n"
    "v_mfma_f32_16x16x32_bf16 v[56:59], a[32:35], v[188:191], v[56:59]\n"
    "v_exp_f32 v36, v36\n"
    "v_pk_add_u16 v182, s89, v182 clamp\n"
    "v_exp_f32 v23, v23\n"
    "v_mfma_f32_16x16x32_bf16 v[24:27], a[144:147], v[188:191], v[24:27]\n"
    "v_accvgpr_write_b32 a237, v201\n"
    "v_pk_add_u16 v180, s89, v180 clamp\n"
    "v_mfma_f32_16x16x32_bf16 v[40:43], a[12:15], v[192:195], v[40:43]\n"
    "v_accvgpr_write_b32 a239, v203\n"
    "v_accvgpr_write_b32 a223, v187\n"
    "v_exp_f32 v7, v7\n"
    "v_accvgpr_write_b32 a233, v197\n"
    "v_mfma_f32_16x16x32_bf16 v[8:11], a[116:119], v[184:187], v[8:11]\n"
    "v_accvgpr_write_b32 a232, v196\n"
    "v_accvgpr_write_b32 a235, v199\n"
    "v_mfma_f32_16x16x32_bf16 v[40:43], a[8:11], v[188:191], v[40:43]\n"
    "ds_write_b128 v86, v[68:71] offset:0\n"
    "v_mfma_f32_16x16x32_bf16 v[40:43], a[4:7], v[184:187], v[40:43]\n"
    "v_sub_f32 v106, v106, v76\n"
    "v_accvgpr_write_b32 a234, v198\n"
    "v_mfma_f32_16x16x32_bf16 v[8:11], a[112:115], v[180:183], v[8:11]\n"
    "v_cvt_pk_bf16_f32 v70, v20, v21\n"
    "v_exp_f32 v37, v37\n"
    "v_cvt_pk_bf16_f32 v69, v6, v7\n"
    "v_accvgpr_write_b32 a218, v182\n"
    "v_mfma_f32_16x16x32_bf16 v[40:43], a[0:3], v[180:183], v[40:43]\n"
    "ds_write_b128 v86, v[72:75] offset:16\n"
    "v_accvgpr_write_b32 a230, v194\n"
    "v_mfma_f32_16x16x32_bf16 v[24:27], a[140:143], v[184:187], v[24:27]\n"
    "v_exp_f32 v10, v10\n"
    "v_mfma_f32_16x16x32_bf16 v[56:59], a[28:31], v[184:187], v[56:59]\n"
    "v_cvt_pk_bf16_f32 v72, v36, v37\n"
    "v_sub_f32 v103, v103, v76\n"
    "v_mfma_f32_16x16x32_bf16 v[56:59], a[24:27], v[180:183], v[56:59]\n"
    "v_sub_f32 v104, v104, v76\n"
    "v_accvgpr_write_b32 a217, v181\n"
    "v_exp_f32 v4, v4\n"
    "v_sub_f32 v100, v100, v76\n"
    "v_accvgpr_write_b32 a227, v191\n"
    "v_exp_f32 v11, v11\n"
    "v_exp_f32 v5, v5\n"
    "v_exp_f32 v38, v38\n"
    "v_exp_f32 v41, v41\n"
    "v_exp_f32 v9, v9\n"
    "v_exp_f32 v59, v59\n"
    "v_accvgpr_write_b32 a220, v184\n"
    "v_accvgpr_write_b32 a238, v202\n"
    "v_accvgpr_write_b32 a236, v200\n"
    "v_exp_f32 v43, v43\n"
    "v_cvt_pk_bf16_f32 v77, v10, v11\n"
    "v_exp_f32 v8, v8\n"
    "v_cvt_pk_bf16_f32 v75, v54, v55\n"
    "v_mfma_f32_16x16x32_bf16 v[24:27], a[136:139], v[180:183], v[24:27]\n"
    "v_cvt_pk_bf16_f32 v71, v22, v23\n"
    "v_sub_f32 v105, v105, v76\n"
    "v_accvgpr_write_b32 a216, v180\n"
    "v_exp_f32 v58, v58\n"
    "v_cvt_pk_bf16_f32 v73, v38, v39\n"
    "v_exp_f32 v40, v40\n"
    "v_accvgpr_write_b32 a222, v186\n"
    "v_accvgpr_write_b32 a225, v189\n"
    "v_exp_f32 v24, v24\n"
    "v_exp_f32 v57, v57\n"
    "v_cvt_pk_bf16_f32 v68, v4, v5\n"
    "v_cvt_pk_bf16_f32 v80, v40, v41\n"
    "v_exp_f32 v27, v27\n"
    "v_exp_f32 v25, v25\n"
    "v_cvt_pk_bf16_f32 v76, v8, v9\n"
    "v_accvgpr_write_b32 a224, v188\n"
    "v_accvgpr_write_b32 a219, v183\n"
    "v_exp_f32 v26, v26\n"
    "v_exp_f32 v42, v42\n"
    "v_cvt_pk_bf16_f32 v78, v24, v25\n"
    "v_exp_f32 v56, v56\n"
    "v_cvt_pk_bf16_f32 v74, v52, v53\n"
    "v_accvgpr_write_b32 a229, v193\n"
    "v_cvt_pk_bf16_f32 v79, v26, v27\n"
    "v_cvt_pk_bf16_f32 v82, v56, v57\n"
    "v_cvt_pk_bf16_f32 v83, v58, v59\n"
    "v_cvt_pk_bf16_f32 v81, v42, v43\n"
    "s_waitcnt vmcnt(8)\n"
    "v_cmp_eq_u64 s[54:55], v[236:237], v[238:239]\n"
    "s_nop 0\n"
    "s_cmp_eq_u64 s[54:55], -1\n"
    "s_cbranch_scc0 .LpollAV\n"
    ".LpollAVr:\n"
    "s_add_u32 m0, s65, 24576\n"
    "s_add_u32 s72, s65, 49152\n"
    "buffer_load_dwordx4 v1, s[16:19], s72 offen offset:0 sc1 lds\n"
    "s_add_u32 m0, s65, 25600\n"
    "s_add_u32 s72, s65, 50176\n"
    "buffer_load_dwordx4 v1, s[16:19], s72 offen offset:0 sc1 lds\n"
    "s_add_u32 m0, s65, 26624\n"
    "s_add_u32 s72, s65, 51200\n"
    "buffer_load_dwordx4 v1, s[16:19], s72 offen offset:0 sc1 lds\n"
    "s_add_u32 m0, s65, 27648\n"
    "s_add_u32 s72, s65, 52224\n"
    "buffer_load_dwordx4 v1, s[16:19], s72 offen offset:0 sc1 lds\n"
    "s_add_u32 m0, s65, 28672\n"
    "s_add_u32 s72, s65, 53248\n"
    "buffer_load_dwordx4 v1, s[16:19], s72 offen offset:0 sc1 lds\n"
    "s_add_u32 m0, s65, 29696\n"
    "s_add_u32 s72, s65, 54272\n"
    "buffer_load_dwordx4 v1, s[16:19], s72 offen offset:0 sc1 lds\n"
    "s_waitcnt vmcnt(12)\n"
    "ds_read_b128 v[156:159], v96 offset:0\n"
    "ds_read_b128 v[172:175], v96 offset:256\n"
    "ds_read_b128 v[168:171], v97 offset:128\n"
    "ds_read_b128 v[176:179], v97 offset:256\n"
    "ds_read_b128 v[160:163], v97 offset:0\n"
    "ds_read_b128 v[164:167], v96 offset:128\n"
    "s_waitcnt lgkmcnt(5)\n"
    "v_pk_maximum3_f16 v98, v156, v157, v98\n"
    "v_pk_maximum3_f16 v98, v158, v159, v98\n"
    "v_pk_add_u16 v157, s89, v157 clamp\n"
    "v_pk_add_u16 v158, s89, v158 clamp\n"
    "v_pk_add_u16 v156, s89, v156 clamp\n"
    "v_pk_add_u16 v159, s89, v159 clamp\n"
    "s_waitcnt lgkmcnt(1)\n"
    "v_pk_maximum3_f16 v98, v160, v161, v98\n"
    "s_add_u32 m0, s65, 73728\n"
    "s_add_u32 s72, s65, 73728\n"
    "buffer_load_dwordx4 v1, s[16:19], s72 offen offset:0 sc1 lds\n"
    "v_mfma_f32_16x16x32_bf16 v[28:31], a[136:139], v[156:159], v[32:35]\n"
    "s_waitcnt lgkmcnt(0)\n"
    "v_pk_maximum3_f16 v98, v162, v163, v98\n"
    "v_pk_maximum3_f16 v98, v164, v165, v98\n"
    "v_mfma_f32_16x16x32_bf16 v[12:15], a[112:115], v[156:159], v[16:19]\n"
    "v_pk_add_u16 v161, s89, v161 clamp\n"
    "v_pk_maximum3_f16 v98, v166, v167, v98\n"
    "v_pk_add_u16 v160, s89, v160 clamp\n"
    "v_pk_add_u16 v162, s89, v162 clamp\n"
    "v_mfma_f32_16x16x32_bf16 v[60:63], a[24:27], v[156:159], v[64:67]\n"
    "v_pk_maximum3_f16 v98, v168, v169, v98\n"
    "v_pk_add_u16 v163, s89, v163 clamp\n"
    "v_mfma_f32_16x16x32_bf16 v[44:47], a[0:3], v[156:159], v[48:51]\n"
    "v_pk_maximum3_f16 v98, v170, v171, v98\n"
    "v_pk_add_u16 v164, s89, v164 clamp\n"
    "v_mfma_f32_16x16x32_bf16 v[12:15], a[116:119], v[160:163], v[12:15]\n"
    "v_pk_add_u16 v165, s89, v165 clamp\n"
    "s_add_u32 m0, s65, 74752\n"
    "s_add_u32 s72, s65, 74752\n"
    "buffer_load_dwordx4 v1, s[16:19], s72 offen offset:0 sc1 lds\n"
    "v_pk_add_u16 v169, s89, v169 clamp\n"
    "v_pk_add_u16 v168, s89, v168 clamp\n"
    "v_mfma_f32_16x16x32_bf16 v[60:63], a[28:31], v[160:163], v[60:63]\n"
    "v_pk_maximum3_f16 v98, v172, v173, v98\n"
    "v_pk_add_u16 v170, s89, v170 clamp\n"
    "v_mfma_f32_16x16x32_bf16 v[28:31], a[140:143], v[160:163], v[28:31]\n"
    "v_pk_maximum3_f16 v98, v174, v175, v98\n"
    "v_pk_add_u16 v166, s89, v166 clamp\n"
    "v_pk_add_u16 v167, s89, v167 clamp\n"
    "v_mfma_f32_16x16x32_bf16 v[44:47], a[4:7], v[160:163], v[44:47]\n"
    "v_pk_maximum3_f16 v98, v176, v177, v98\n"
    "v_accvgpr_write_b32 a251, v167\n"
    "v_mfma_f32_16x16x32_bf16 v[44:47], a[8:11], v[164:167], v[44:47]\n"
    "v_pk_maximum3_f16 v98, v178, v179, v98\n"
    "v_pk_add_u16 v176, s89, v176 clamp\n"
    "v_mfma_f32_16x16x32_bf16 v[60:63], a[32:35], v[164:167], v[60:63]\n"
    "v_pk_add_u16 v171, s89, v171 clamp\n"
    "s_add_u32 m0, s65, 75776\n"
    "s_add_u32 s72, s65, 75776\n"
    "buffer_load_dwordx4 v1, s[16:19], s72 offen offset:0 sc1 lds\n"
    "v_pk_add_u16 v174, s89, v174 clamp\n"
    "v_mfma_f32_16x16x32_bf16 v[28:31], a[144:147], v[164:167], v[28:31]\n"
    "v_pk_add_u16 v173, s89, v173 clamp\n"
    "v_pk_add_u16 v172, s89, v172 clamp\n"
    "v_mfma_f32_16x16x32_bf16 v[12:15], a[120:123], v[164:167], v[12:15]\n"
    "v_accvgpr_write_b32 a254, v170\n"
    "v_pk_add_u16 v178, s89, v178 clamp\n"
    "v_mfma_f32_16x16x32_bf16 v[28:31], a[148:151], v[168:171], v[28:31]\n"
    "v_accvgpr_write_b32 a243, v159\n"
    "v_accvgpr_write_b32 a162, v174\n"
    "v_mfma_f32_16x16x32_bf16 v[12:15], a[124:127], v[168:171], v[12:15]\n"
    "v_pk_add_u16 v179, s89, v179 clamp\n"
    "v_pk_add_u16 v175, s89, v175 clamp\n"
    "v_mfma_f32_16x16x32_bf16 v[44:47], a[12:15], v[168:171], v[44:47]\n"
    "v_accvgpr_write_b32 a166, v178\n"
    "v_accvgpr_write_b32 a161, v173\n"
    "v_accvgpr_write_b32 a245, v161\n"
    "s_add_u32 m0, s65, 76800\n"
    "s_add_u32 s72, s65, 76800\n"
    "buffer_load_dwordx4 v1, s[16:19], s72 offen offset:0 sc1 lds\n"
    "v_mfma_f32_16x16x32_bf16 v[28:31], a[152:155], v[172:175], v[28:31]\n"
    "v_accvgpr_write_b32 a163, v175\n"
    "v_pk_add_u16 v177, s89, v177 clamp\n"
    "v_mfma_f32_16x16x32_bf16 v[44:47], a[16:19], v[172:175], v[44:47]\n"
    "v_accvgpr_write_b32 a160, v172\n"
    "v_accvgpr_write_b32 a252, v168\n"
    "v_mfma_f32_16x16x32_bf16 v[28:31], a[156:159], v[176:179], v[28:31]\n"
    "v_accvgpr_write_b32 a249, v165\n"
    "v_accvgpr_write_b32 a255, v171\n"
    "v_mfma_f32_16x16x32_bf16 v[12:15], a[128:131], v[172:175], v[12:15]\n"
    "v_accvgpr_write_b32 a247, v163\n"
    "v_accvgpr_write_b32 a164, v176\n"
    "v_mfma_f32_16x16x32_bf16 v[60:63], a[36:39], v[168:171], v[60:63]\n"
    "v_accvgpr_write_b32 a242, v158\n"
    "v_accvgpr_write_b32 a250, v166\n"
    "v_mfma_f32_16x16x32_bf16 v[44:47], a[20:23], v[176:179], v[44:47]\n"
    "v_accvgpr_write_b32 a241, v157\n"
    "s_add_u32 m0, s65, 77824\n"
    "s_add_u32 s72, s65, 77824\n"
    "buffer_load_dwordx4 v1, s[16:19], s72 offen offset:0 sc1 lds\n"
    "v_accvgpr_write_b32 a248, v164\n"
    "v_mfma_f32_16x16x32_bf16 v[12:15], a[132:135], v[176:179], v[12:15]\n"
    "v_accvgpr_write_b32 a246, v162\n"
    "v_exp_f32 v29, v29\n"
    "v_mfma_f32_16x16x32_bf16 v[60:63], a[40:43], v[172:175], v[60:63]\n"
    "v_exp_f32 v28, v28\n"
    "v_mfma_f32_16x16x32_bf16 v[60:63], a[44:47], v[176:179], v[60:63]\n"
    "v_exp_f32 v31, v31\n"
    "v_exp_f32 v30, v30\n"
    "v_exp_f32 v45, v45\n"
    "v_cvt_pk_bf16_f32 v86, v28, v29\n"
    "v_exp_f32 v47, v47\n"
    "v_accvgpr_write_b32 a240, v156\n"
    "v_exp_f32 v12, v12\n"
    "v_exp_f32 v14, v14\n"
    "v_accvgpr_write_b32 a167, v179\n"
    "v_exp_f32 v15, v15\n"
    "s_add_u32 m0, s65, 78848\n"
    "s_add_u32 s72, s65, 78848\n"
    "buffer_load_dwordx4 v1, s[16:19], s72 offen offset:0 sc1 lds\n"
    "v_accvgpr_write_b32 a253, v169\n"
    "v_exp_f32 v63, v63\n"
    "v_exp_f32 v13, v13\n"
    "v_exp_f32 v44, v44\n"
    "v_cvt_pk_bf16_f32 v84, v12, v13\n"
    "v_cvt_pk_bf16_f32 v87, v30, v31\n"
    "v_exp_f32 v46, v46\n"
    "v_exp_f32 v61, v61\n"
    "v_accvgpr_write_b32 a165, v177\n"
    "v_exp_f32 v60, v60\n"
    "v_exp_f32 v62, v62\n"
    "v_cvt_pk_bf16_f32 v90, v60, v61\n"
    "v_accvgpr_write_b32 a244, v160\n"
    "v_cvt_pk_bf16_f32 v85, v14, v15\n"
    "v_cvt_pk_bf16_f32 v89, v46, v47\n"
    "v_cvt_pk_bf16_f32 v88, v44, v45\n"
    "v_cvt_pk_bf16_f32 v91, v62, v63\n"
    "s_bitcmp1_b32 s83, 1\n"
    "s_cbranch_scc1 .Lf6b\n"
    "s_lshl_b32 s73, s53, 1\n"
    "s_add_u32 s73, s73, s79\n"
    "s_mul_i32 s74, s73, 6144\n"
    "s_add_u32 s74, s74, 1425408\n"
    "s_lshl_b32 s73, s73, 8\n"
    "s_add_u32 s73, s73, 1557504\n"
    "s_mov_b64 s[20:21], s[16:17]\n"
    "s_mov_b32 s44, 98304\n"
    "s_mov_b32 s22, 1572864\n"
    "s_sub_u32 s72, s42, 1572864\n"
    "s_sub_u32 s45, s42, 49152\n"
    "s_bitcmp1_b32 s84, 0\n"
    "s_cselect_b32 s52, s71, s73\n"
    "s_cselect_b32 s71, s71, s74\n"
    "s_cselect_b32 s63, s63, s72\n"
    "s_cselect_b32 s42, s45, s42\n"
    "s_cselect_b32 s45, s44, 12288\n"
    "s_cselect_b32 s44, s44, 24576\n"
    "s_cselect_b32 s22, s22, s18\n"
    "s_cselect_b64 s[40:41], s[40:41], s[8:9]\n"
    "s_cselect_b32 s60, s60, 24576\n"
    "s_cselect_b32 s61, s61, 28672\n"
    "s_cselect_b32 s62, s62, 32768\n"
    "s_mov_b32 s66, 1\n"
    "s_add_u32 s68, s68, 24576\n"
    "s_add_u32 s81, s81, 128\n"
    "s_add_u32 s80, s80, 256\n"
    "s_add_u32 s71, s71, s44\n"
    "s_add_u32 s52, s52, s45\n"
    "s_add_u32 s75, s71, s63\n"
    "s_branch .LhVP\n"
    "s_branch .Lf6e\n"
    ".Lf6b:\n"
    "s_add_u32 s75, s71, s63\n"
    "buffer_load_dwordx4 v[204:207], v242, s[40:43], s75 offen offset:0 nt\n"
    "buffer_load_dwordx4 v[208:211], v242, s[40:43], s75 offen offset:768 nt\n"
    "buffer_load_dwordx4 v[212:215], v242, s[40:43], s75 offen offset:256 nt\n"
    "buffer_load_dwordx4 v[216:219], v242, s[40:43], s75 offen offset:1024 nt\n"
    "buffer_load_dwordx4 v[220:223], v242, s[40:43], s75 offen offset:512 nt\n"
    "buffer_load_dwordx4 v[224:227], v242, s[40:43], s75 offen offset:1280 nt\n"
    "s_lshl_b32 s73, s53, 1\n"
    "s_add_u32 s73, s73, s79\n"
    "s_mul_i32 s74, s73, 6144\n"
    "s_add_u32 s74, s74, 1425408\n"
    "s_lshl_b32 s73, s73, 8\n"
    "s_add_u32 s73, s73, 1557504\n"
    "s_mov_b64 s[20:21], s[16:17]\n"
    "s_mov_b32 s44, 98304\n"
    "s_mov_b32 s22, 1572864\n"
    "s_sub_u32 s72, s42, 1572864\n"
    "s_sub_u32 s45, s42, 49152\n"
    "s_bitcmp1_b32 s84, 0\n"
    "s_cselect_b32 s52, s71, s73\n"
    "s_cselect_b32 s71, s71, s74\n"
    "s_cselect_b32 s63, s63, s72\n"
    "s_cselect_b32 s42, s45, s42\n"
    "s_cselect_b32 s45, s44, 12288\n"
    "s_cselect_b32 s44, s44, 24576\n"
    "s_cselect_b32 s22, s22, s18\n"
    "s_cselect_b64 s[40:41], s[40:41], s[8:9]\n"
    "s_cselect_b32 s60, s60, 24576\n"
    "s_cselect_b32 s61, s61, 28672\n"
    "s_cselect_b32 s62, s62, 32768\n"
    "s_mov_b32 s66, 1\n"
    "s_add_u32 s68, s68, 24576\n"
    "s_add_u32 s81, s81, 128\n"
    "s_branch .LhVN\n"
    ".Lf6e:\n"
    "s_nop 4\n"
    ".Lf7e:\n"
    "s_nop 4\n"
    ".fill ((0x20000 - (. - _Z8attn_fwdPKtPKiS2_S0_S0_S0_S0_PcPt)) & 4095) / 4, 4, 0xBF800000\n"
    ".LhKP:\n"
    "s_waitcnt vmcnt(12)\n"
    "ds_read_b128 v[52:55], v251 offset:0\n"
    "ds_read_b128 v[192:195], v97 offset:49280\n"
    "ds_read_b128 v[196:199], v96 offset:49408\n"
    "ds_read_b128 v[200:203], v97 offset:49408\n"
    "s_add_u32 s83, s67, 4096\n"
    "ds_read_b128 v[188:191], v96 offset:49280\n"
    "ds_read_b128 v[180:183], v96 offset:49152\n"
    "s_nop 0\n"
    "ds_read_b128 v[184:187], v97 offset:49152\n"
    "s_waitcnt lgkmcnt(6)\n"
    "v_smfmac_f32_16x16x64_bf16 v[108:111], v[52:55], v[68:75], v253\n"
    "s_waitcnt lgkmcnt(3)\n"
    "v_pk_maximum3_f16 v98, v200, v201, v98\n"
    "v_pk_maximum3_f16 v98, v202, v203, v98\n"
    "v_pk_maximum3_f16 v98, v196, v197, v98\n"
    "v_pk_add_u16 v203, s89, v203 clamp\n"
    "v_pk_maximum3_f16 v98, v198, v199, v98\n"
    "v_pk_add_u16 v200, s89, v200 clamp\n"
    "v_pk_add_u16 v202, s89, v202 clamp\n"
    "v_pk_add_u16 v201, s89, v201 clamp\n"
    "v_pk_maximum3_f16 v98, v192, v193, v98\n"
    "v_pk_maximum3_f16 v98, v194, v195, v98\n"
    "v_mfma_f32_16x16x32_bf16 v[16:19], a[132:135], v[200:203], v[16:19]\n"
    "s_add_u32 m0, s68, .Lend-.Lend2+(0)\n"
    "v_pk_add_u16 v199, s89, v199 clamp\n"
    "v_pk_add_u16 v197, s89, v197 clamp\n"
    "v_mfma_f32_16x16x32_bf16 v[64:67], a[44:47], v[200:203], v[64:67]\n"
    "buffer_load_dwordx4 v1, s[16:19], s67 offen offset:0 sc1 lds\n"
    "v_pk_add_u16 v198, s89, v198 clamp\n"
    "v_mfma_f32_16x16x32_bf16 v[32:35], a[156:159], v[200:203], v[32:35]\n"
    "v_pk_add_u16 v196, s89, v196 clamp\n"
    "v_accvgpr_write_b32 a191, v203\n"
    "v_mfma_f32_16x16x32_bf16 v[48:51], a[20:23], v[200:203], v[48:51]\n"
    "s_waitcnt lgkmcnt(2)\n"
    "v_pk_maximum3_f16 v98, v188, v189, v98\n"
    "v_pk_add_u16 v195, s89, v195 clamp\n"
    "v_mfma_f32_16x16x32_bf16 v[64:67], a[40:43], v[196:199], v[64:67]\n"
    "v_pk_add_u16 v192, s89, v192 clamp\n"
    "v_pk_add_u16 v193, s89, v193 clamp\n"
    "v_mfma_f32_16x16x32_bf16 v[32:35], a[152:155], v[196:199], v[32:35]\n"
    "v_pk_maximum3_f16 v98, v190, v191, v98\n"
    "v_pk_add_u16 v194, s89, v194 clamp\n"
    "v_smfmac_f32_16x16x64_bf16 v[116:119], v[52:55], v[84:91], v253\n"
    "v_pk_add_u16 v191, s89, v191 clamp\n"
    "v_mfma_f32_16x16x32_bf16 v[48:51], a[16:19], v[196:199], v[48:51]\n"
    "buffer_load_dwordx4 v1, s[16:19], s67 offen offset:1024 sc1 lds\n"
    "v_mfma_f32_16x16x32_bf16 v[16:19], a[128:131], v[196:199], v[16:19]\n"
    "s_waitcnt lgkmcnt(0)\n"
    "v_pk_maximum3_f16 v98, v184, v185, v98\n"
    "v_pk_add_u16 v189, s89, v189 clamp\n"
    "v_mfma_f32_16x16x32_bf16 v[48:51], a[12:15], v[192:195], v[48:51]\n"
    "v_pk_add_u16 v190, s89, v190 clamp\n"
    "v_pk_add_u16 v184, s89, v184 clamp\n"
    "v_accvgpr_write_b32 a188, v200\n"
    "v_mfma_f32_16x16x32_bf16 v[64:67], a[36:39], v[192:195], v[64:67]\n"
    "v_pk_maximum3_f16 v98, v186, v187, v98\n"
    "v_pk_add_u16 v188, s89, v188 clamp\n"
    "v_mfma_f32_16x16x32_bf16 v[32:35], a[148:151], v[192:195], v[32:35]\n"
    "v_pk_add_u16 v187, s89, v187 clamp\n"
    "v_pk_maximum3_f16 v98, v180, v181, v98\n"
    "v_mfma_f32_16x16x32_bf16 v[32:35], a[144:147], v[188:191], v[32:35]\n"
    "v_pk_maximum3_f16 v98, v182, v183, v98\n"
    "v_pk_add_u16 v185, s89, v185 clamp\n"
    "v_mfma_f32_16x16x32_bf16 v[16:19], a[124:127], v[192:195], v[16:19]\n"
    "buffer_load_dwordx4 v1, s[16:19], s67 offen offset:2048 sc1 lds\n"
    "v_mfma_f32_16x16x32_bf16 v[16:19], a[120:123], v[188:191], v[16:19]\n"
    "s_waitcnt vmcnt(9)\n"
    "v_pk_add_u16 v186, s89, v186 clamp\n"
    "v_pk_add_u16 v181, s89, v181 clamp\n"
    "v_mfma_f32_16x16x32_bf16 v[64:67], a[32:35], v[188:191], v[64:67]\n"
    "s_barrier\n"
    "ds_read_b128 a[48:51], v252 offset:24576\n"
    "v_mfma_f32_16x16x32_bf16 v[48:51], a[8:11], v[188:191], v[48:51]\n"
    "ds_read_b128 a[72:75], v252 offset:25600\n"
    "ds_read_b128 a[92:95], v252 offset:35840\n"
    "v_mfma_f32_16x16x32_bf16 v[32:35], a[140:143], v[184:187], v[32:35]\n"
    "v_pk_add_u16 v183, s89, v183 clamp\n"
    "v_pk_add_u16 v180, s89, v180 clamp\n"
    "v_pk_add_u16 v182, s89, v182 clamp\n"
    "v_mfma_f32_16x16x32_bf16 v[16:19], a[116:119], v[184:187], v[16:19]\n"
    "ds_read_b128 a[84:87], v252 offset:31744\n"
    "ds_read_b128 a[52:55], v252 offset:26624\n"
    "v_mfma_f32_16x16x32_bf16 v[32:35], a[136:139], v[180:183], v[32:35]\n"
    "buffer_load_dwordx4 v1, s[16:19], s67 offen offset:3072 sc1 lds\n"
    "v_mfma_f32_16x16x32_bf16 v[48:51], a[4:7], v[184:187], v[48:51]\n"
    "ds_read_b128 a[88:91], v252 offset:33792\n"
    "ds_read_b128 a[56:59], v252 offset:28672\n"
    "v_mfma_f32_16x16x32_bf16 v[64:67], a[28:31], v[184:187], v[64:67]\n"
    "v_accvgpr_write_b32 a170, v182\n"
    "v_accvgpr_write_b32 a177, v189\n"
    "v_mfma_f32_16x16x32_bf16 v[16:19], a[112:115], v[180:183], v[16:19]\n"
    "s_add_u32 m0, s68, 4096\n"
    "ds_read_b128 a[76:79], v252 offset:27648\n"
    "ds_read_b128 a[68:71], v252 offset:34816\n"
    "ds_read_b128 a[60:63], v252 offset:30720\n"
    "v_mfma_f32_16x16x32_bf16 v[48:51], a[0:3], v[180:183], v[48:51]\n"
    "s_waitcnt lgkmcnt(9)\n"
    "ds_read_b128 a[80:83], v252 offset:29696\n"
    "v_accvgpr_write_b32 a179, v191\n"
    "v_mfma_f32_16x16x32_bf16 v[4:7], a[48:51], a[192:195], v[100:103]\n"
    "s_waitcnt lgkmcnt(8)\n"
    "v_pk_add_u16 v98, s89, v98\n"
    "v_accvgpr_write_b32 a174, v186\n"
    "v_mfma_f32_16x16x32_bf16 v[24:27], a[92:95], a[236:239], v[104:107]\n"
    "buffer_load_dwordx4 v1, s[16:19], s83 offen offset:0 sc1 lds\n"
    "v_mfma_f32_16x16x32_bf16 v[12:15], a[48:51], a[240:243], v[100:103]\n"
    "v_accvgpr_write_b32 a186, v198\n"
    "v_accvgpr_write_b32 a180, v192\n"
    "v_mfma_f32_16x16x32_bf16 v[20:23], a[72:75], a[192:195], v[104:107]\n"
    "v_accvgpr_write_b32 a181, v193\n"
    "v_accvgpr_write_b32 a172, v184\n"
    "v_mfma_f32_16x16x32_bf16 v[28:31], a[72:75], a[240:243], v[104:107]\n"
    "s_waitcnt lgkmcnt(6)\n"
    "v_exp_f32 v33, v33\n"
    "v_mfma_f32_16x16x32_bf16 v[4:7], a[52:55], a[196:199], v[4:7]\n"
    "ds_read_b128 a[64:67], v252 offset:32768\n"
    "v_accvgpr_write_b32 a184, v196\n"
    "v_mfma_f32_16x16x32_bf16 v[12:15], a[52:55], a[244:247], v[12:15]\n"
    "s_waitcnt lgkmcnt(6)\n"
    "v_accvgpr_write_b32 a168, v180\n"
    "v_accvgpr_write_b32 a182, v194\n"
    "v_mfma_f32_16x16x32_bf16 v[24:27], a[88:91], a[232:235], v[24:27]\n"
    "s_waitcnt lgkmcnt(3)\n"
    "v_accvgpr_write_b32 a183, v195\n"
    "v_accvgpr_write_b32 a190, v202\n"
    "v_mfma_f32_16x16x32_bf16 v[8:11], a[68:71], a[236:239], v[100:103]\n"
    "buffer_load_dwordx4 v1, s[16:19], s83 offen offset:1024 sc1 lds\n"
    "v_mfma_f32_16x16x32_bf16 v[4:7], a[56:59], a[200:203], v[4:7]\n"
    "v_accvgpr_write_b32 a173, v185\n"
    "v_accvgpr_write_b32 a171, v183\n"
    "v_mfma_f32_16x16x32_bf16 v[12:15], a[56:59], a[248:251], v[12:15]\n"
    "s_add_u32 m0, s64, 0\n"
    "v_accvgpr_write_b32 a176, v188\n"
    "v_accvgpr_write_b32 a189, v201\n"
    "v_accvgpr_write_b32 a169, v181\n"
    "v_mfma_f32_16x16x32_bf16 v[64:67], a[24:27], v[180:183], v[64:67]\n"
    "v_accvgpr_write_b32 a175, v187\n"
    "v_and_b32 v98, 0x80008000, v98\n"
    "v_mfma_f32_16x16x32_bf16 v[20:23], a[76:79], a[196:199], v[20:23]\n"
    "s_waitcnt lgkmcnt(0)\n"
    "v_cmp_ne_u32 s[34:35], 0, v98\n"
    "v_accvgpr_write_b32 a187, v199\n"
    "v_mfma_f32_16x16x32_bf16 v[8:11], a[64:67], a[232:235], v[8:11]\n"
    "s_add_u32 s83, s67, 24576\n"
    "v_accvgpr_write_b32 a185, v197\n"
    "v_accvgpr_write_b32 a178, v190\n"
    "v_mfma_f32_16x16x32_bf16 v[24:27], a[84:87], a[228:231], v[24:27]\n"
    "buffer_load_dwordx4 v1, s[16:19], s83 offen offset:0 sc1 lds\n"
    "v_mfma_f32_16x16x32_bf16 v[12:15], a[60:63], a[252:255], v[12:15]\n"
    "v_exp_f32 v32, v32\n"
    "v_mfma_f32_16x16x32_bf16 v[4:7], a[60:63], a[204:207], v[4:7]\n"
    "v_exp_f32 v18, v18\n"
    "v_mfma_f32_16x16x32_bf16 v[12:15], a[64:67], a[160:163], v[12:15]\n"
    "v_exp_f32 v48, v48\n"
    "v_mfma_f32_16x16x32_bf16 v[4:7], a[64:67], a[208:211], v[4:7]\n"
    "v_exp_f32 v19, v19\n"
    "v_mfma_f32_16x16x32_bf16 v[4:7], a[68:71], a[212:215], v[4:7]\n"
    "v_cvt_pk_bf16_f32 v93, v18, v19\n"
    "v_exp_f32 v17, v17\n"
    "v_mfma_f32_16x16x32_bf16 v[28:31], a[76:79], a[244:247], v[28:31]\n"
    "buffer_load_dwordx4 v1, s[16:19], s83 offen offset:1024 sc1 lds\n"
    "v_mfma_f32_16x16x32_bf16 v[24:27], a[80:83], a[224:227], v[24:27]\n"
    "v_exp_f32 v35, v35\n"
    "v_mfma_f32_16x16x32_bf16 v[20:23], a[80:83], a[200:203], v[20:23]\n"
    "v_exp_f32 v16, v16\n"
    "v_mfma_f32_16x16x32_bf16 v[8:11], a[60:63], a[228:231], v[8:11]\n"
    "v_exp_f32 v34, v34\n"
    "v_mfma_f32_16x16x32_bf16 v[20:23], a[84:87], a[204:207], v[20:23]\n"
    "v_cvt_pk_bf16_f32 v95, v34, v35\n"
    "v_mfma_f32_16x16x32_bf16 v[8:11], a[56:59], a[224:227], v[8:11]\n"
    "v_exp_f32 v66, v66\n"
    "v_mfma_f32_16x16x32_bf16 v[24:27], a[76:79], a[220:223], v[24:27]\n"
    "v_cvt_pk_bf16_f32 v94, v32, v33\n"
    "v_mfma_f32_16x16x32_bf16 v[8:11], a[52:55], a[220:223], v[8:11]\n"
    "buffer_load_dwordx4 v1, s[16:19], s83 offen offset:2048 sc1 lds\n"
    "v_mfma_f32_16x16x32_bf16 v[32:35], a[92:95], a[188:191], v[104:107]\n"
    "v_exp_f32 v49, v49\n"
    "v_mfma_f32_16x16x32_bf16 v[12:15], a[68:71], a[164:167], v[12:15]\n"
    "v_exp_f32 v67, v67\n"
    "v_mfma_f32_16x16x32_bf16 v[32:35], a[88:91], a[184:187], v[32:35]\n"
    "s_waitcnt vmcnt(9)\n"
    "s_barrier\n"
    "ds_read_b128 a[116:119], v248 offset:12288\n"
    "v_mfma_f32_16x16x32_bf16 v[8:11], a[48:51], a[216:219], v[8:11]\n"
    "ds_read_b128 a[144:147], v248 offset:4096\n"
    "v_cvt_pk_bf16_f32 v99, v66, v67\n"
    "v_mfma_f32_16x16x32_bf16 v[32:35], a[84:87], a[180:183], v[32:35]\n"
    "ds_read_b128 a[124:127], v248 offset:13312\n"
    "v_cvt_pk_bf16_f32 v92, v16, v17\n"
    "v_mfma_f32_16x16x32_bf16 v[16:19], a[68:71], a[188:191], v[100:103]\n"
    "buffer_load_dwordx4 v1, s[16:19], s83 offen offset:3072 sc1 lds\n"
    "v_mfma_f32_16x16x32_bf16 v[20:23], a[88:91], a[208:211], v[20:23]\n"
    "s_add_u32 s83, s67, 28672\n"
    "ds_read_b128 a[136:139], v248 offset:3072\n"
    "ds_read_b128 a[140:143], v248 offset:15360\n"
    "v_mfma_f32_16x16x32_bf16 v[32:35], a[80:83], a[176:179], v[32:35]\n"
    "ds_read_b128 a[120:123], v248 offset:1024\n"
    "ds_read_b128 a[148:151], v248 offset:16384\n"
    "v_mfma_f32_16x16x32_bf16 v[28:31], a[80:83], a[248:251], v[28:31]\n"
    "v_exp_f32 v64, v64\n"
    "v_mfma_f32_16x16x32_bf16 v[28:31], a[84:87], a[252:255], v[28:31]\n"
    "s_add_u32 m0, s64, 4096\n"
    "v_exp_f32 v50, v50\n"
    "v_mfma_f32_16x16x32_bf16 v[16:19], a[64:67], a[184:187], v[16:19]\n"
    "v_cvt_pk_bf16_f32 v96, v48, v49\n"
    "v_mfma_f32_16x16x32_bf16 v[16:19], a[60:63], a[180:183], v[16:19]\n"
    "ds_read_b128 a[152:155], v248 offset:5120\n"
    "ds_read_b128 a[132:135], v248 offset:14336\n"
    "v_mfma_f32_16x16x32_bf16 v[32:35], a[76:79], a[172:175], v[32:35]\n"
    "s_waitcnt lgkmcnt(7)\n"
    "buffer_load_dwordx4 v1, s[16:19], s83 offen offset:0 sc1 lds\n"
    "v_mfma_f32_16x16x32_bf16 a[32:35], a[144:147], v[92:95], 0\n"
    "ds_read_b128 a[128:131], v248 offset:2048\n"
    "v_smfmac_f32_16x16x64_bf16 v[112:115], v[52:55], v[76:83], v253\n"
    "v_mfma_f32_16x16x32_bf16 v[24:27], a[72:75], a[216:219], v[24:27]\n"
    "s_waitcnt lgkmcnt(5)\n"
    "v_exp_f32 v65, v65\n"
    "v_mfma_f32_16x16x32_bf16 v[152:155], a[140:143], v[80:83], 0\n"
    "s_waitcnt lgkmcnt(4)\n"
    "v_mfma_f32_16x16x32_bf16 v[144:147], a[120:123], v[76:79], 0\n"
    "v_exp_f32 v51, v51\n"
    "v_mfma_f32_16x16x32_bf16 v[16:19], a[56:59], a[176:179], v[16:19]\n"
    "s_waitcnt lgkmcnt(3)\n"
    "buffer_load_dwordx4 v1, s[16:19], s83 offen offset:1024 sc1 lds\n"
    "v_mfma_f32_16x16x32_bf16 v[188:191], a[148:151], v[72:75], 0\n"
    "s_waitcnt lgkmcnt(1)\n"
    "v_cvt_pk_bf16_f32 v97, v50, v51\n"
    "v_cvt_pk_bf16_f32 v98, v64, v65\n"
    "v_mfma_f32_16x16x32_bf16 v[148:151], a[132:135], v[80:83], 0\n"
    "s_waitcnt lgkmcnt(0)\n"
    "ds_read_b128 a[112:115], v248 offset:0\n"
    "v_mfma_f32_16x16x32_bf16 v[148:151], a[128:131], v[76:79], v[148:151]\n"
    "v_exp_f32 v4, v4\n"
    "v_mfma_f32_16x16x32_bf16 v[180:183], a[132:135], v[96:99], 0\n"
    "v_mfma_f32_16x16x32_bf16 v[176:179], a[120:123], v[92:95], 0\n"
    "v_exp_f32 v14, v14\n"
    "v_mfma_f32_16x16x32_bf16 v[28:31], a[88:91], a[160:163], v[28:31]\n"
    "v_exp_f32 v10, v10\n"
    "v_mfma_f32_16x16x32_bf16 v[164:167], a[128:131], v[84:87], 0\n"
    "buffer_load_dwordx2 v[204:205], v242, s[28:31], s75 offen offset:0 nt\n"
    "v_mfma_f32_16x16x32_bf16 v[176:179], a[124:127], v[96:99], v[176:179]\n"
    "v_mfma_f32_16x16x32_bf16 v[20:23], a[92:95], a[212:215], v[20:23]\n"
    "buffer_load_dwordx2 v[206:207], v242, s[28:31], s75 offen offset:768 nt\n"
    "v_mfma_f32_16x16x32_bf16 v[16:19], a[52:55], a[172:175], v[16:19]\n"
    "v_mfma_f32_16x16x32_bf16 v[132:135], a[128:131], v[68:71], 0\n"
    "buffer_load_dwordx2 v[208:209], v242, s[28:31], s75 offen offset:3072 nt\n"
    "v_mfma_f32_16x16x32_bf16 v[136:139], a[136:139], v[68:71], 0\n"
    "v_mfma_f32_16x16x32_bf16 v[16:19], a[48:51], a[168:171], v[16:19]\n"
    "buffer_load_dwordx2 v[210:211], v242, s[28:31], s75 offen offset:3840 nt\n"
    "v_mfma_f32_16x16x32_bf16 v[180:183], a[128:131], v[92:95], v[180:183]\n"
    "v_mfma_f32_16x16x32_bf16 v[184:187], a[140:143], v[96:99], 0\n"
    "s_waitcnt lgkmcnt(0)\n"
    "buffer_load_dwordx2 v[212:213], v242, s[28:31], s76 offen offset:0 nt\n"
    "v_mfma_f32_16x16x32_bf16 v[156:159], a[112:115], v[84:87], 0\n"
    "v_mfma_f32_16x16x32_bf16 a[16:19], a[148:151], v[88:91], 0\n"
    "buffer_load_dwordx2 v[214:215], v242, s[28:31], s76 offen offset:768 nt\n"
    "v_mfma_f32_16x16x32_bf16 v[168:171], a[136:139], v[84:87], 0\n"
    "v_exp_f32 v22, v22\n"
    "v_mfma_f32_16x16x32_bf16 v[152:155], a[136:139], v[76:79], v[152:155]\n"
    "buffer_load_dwordx2 v[216:217], v242, s[28:31], s76 offen offset:3072 nt\n"
    "v_mfma_f32_16x16x32_bf16 v[160:163], a[124:127], v[88:91], 0\n"
    "v_mfma_f32_16x16x32_bf16 v[140:143], a[116:119], v[80:83], 0\n"
    "buffer_load_dwordx2 v[218:219], v242, s[28:31], s76 offen offset:3840 nt\n"
    "v_mfma_f32_16x16x32_bf16 v[132:135], a[132:135], v[72:75], v[132:135]\n"
    "ds_read_b128 v[100:103], v250 offset:896\n"
    "ds_read_b128 a[128:131], v248 offset:8192\n"
    "v_mfma_f32_16x16x32_bf16 a[16:19], a[144:147], v[84:87], a[16:19]\n"
    "buffer_load_dwordx2 v[220:221], v242, s[28:31], s77 offen offset:0 nt\n"
    "v_mfma_f32_16x16x32_bf16 v[140:143], a[112:115], v[76:79], v[140:143]\n"
    "v_mfma_f32_16x16x32_bf16 v[128:131], a[124:127], v[72:75], 0\n"
    "buffer_load_dwordx2 v[222:223], v242, s[28:31], s77 offen offset:768 nt\n"
    "v_mfma_f32_16x16x32_bf16 a[0:3], a[144:147], v[76:79], 0\n"
    "v_exp_f32 v8, v8\n"
    "v_mfma_f32_16x16x32_bf16 a[20:23], a[152:155], v[84:87], 0\n"
    "buffer_load_dwordx2 v[224:225], v242, s[28:31], s77 offen offset:3072 nt\n"
    "v_mfma_f32_16x16x32_bf16 v[188:191], a[144:147], v[68:71], v[188:191]\n"
    "v_mfma_f32_16x16x32_bf16 v[144:147], a[124:127], v[80:83], v[144:147]\n"
    "buffer_load_dwordx2 v[226:227], v242, s[28:31], s77 offen offset:3840 nt\n"
    "v_mfma_f32_16x16x32_bf16 a[0:3], a[148:151], v[80:83], a[0:3]\n"
    "v_exp_f32 v15, v15\n"
    "v_mfma_f32_16x16x32_bf16 v[172:175], a[116:119], v[96:99], 0\n"
    "s_waitcnt lgkmcnt(0)\n"
    "buffer_load_dwordx2 v[228:229], v242, s[28:31], s78 offen offset:0 nt\n"
    "v_mfma_f32_16x16x32_bf16 a[48:51], a[128:131], v[68:71], 0\n"
    "ds_read_b128 a[156:159], v248 offset:17408\n"
    "v_mfma_f32_16x16x32_bf16 v[164:167], a[132:135], v[88:91], v[164:167]\n"
    "buffer_load_dwordx2 v[230:231], v242, s[28:31], s78 offen offset:768 nt\n"
    "v_mfma_f32_16x16x32_bf16 v[124:127], a[112:115], v[68:71], 0\n"
    "v_mfma_f32_16x16x32_bf16 v[172:175], a[112:115], v[92:95], v[172:175]\n"
    "buffer_load_dwordx2 v[232:233], v242, s[28:31], s78 offen offset:3072 nt\n"
    "v_mfma_f32_16x16x32_bf16 a[32:35], a[148:151], v[96:99], a[32:35]\n"
    "v_mfma_f32_16x16x32_bf16 v[128:131], a[120:123], v[68:71], v[128:131]\n"
    "buffer_load_dwordx2 v[234:235], v242, s[28:31], s78 offen offset:3840 nt\n"
    "v_mfma_f32_16x16x32_bf16 v[192:195], a[152:155], v[68:71], 0\n"
    "ds_read_b128 a[132:135], v248 offset:20480\n"
    "ds_read_b128 a[112:115], v248 offset:6144\n"
    "v_mfma_f32_16x16x32_bf16 v[160:163], a[120:123], v[84:87], v[160:163]\n"
    "s_waitcnt lgkmcnt(2)\n"
    "v_mfma_f32_16x16x32_bf16 a[36:39], a[156:159], v[96:99], 0\n"
    "ds_read_b128 a[144:147], v248 offset:10240\n"
    "ds_read_b128 a[124:127], v248 offset:19456\n"
    "v_mfma_f32_16x16x32_bf16 v[32:35], a[72:75], a[168:171], v[32:35]\n"
    "v_exp_f32 v7, v7\n"
    "v_mfma_f32_16x16x32_bf16 v[192:195], a[156:159], v[72:75], v[192:195]\n"
    "v_exp_f32 v21, v21\n"
    "v_mfma_f32_16x16x32_bf16 v[184:187], a[136:139], v[92:95], v[184:187]\n"
    "ds_read_b128 a[120:123], v248 offset:7168\n"
    "v_mfma_f32_16x16x32_bf16 v[28:31], a[92:95], a[164:167], v[28:31]\n"
    "v_mfma_f32_16x16x32_bf16 v[168:171], a[140:143], v[88:91], v[168:171]\n"
    "ds_read_b128 a[148:151], v248 offset:22528\n"
    "ds_read_b128 a[136:139], v248 offset:9216\n"
    "v_mfma_f32_16x16x32_bf16 a[4:7], a[156:159], v[80:83], 0\n"
    "v_exp_f32 v33, v33\n"
    "v_mfma_f32_16x16x32_bf16 a[36:39], a[152:155], v[92:95], a[36:39]\n"
    "v_mfma_f32_16x16x32_bf16 v[124:127], a[116:119], v[72:75], v[124:127]\n"
    "v_exp_f32 v16, v16\n"
    "v_mfma_f32_16x16x32_bf16 a[4:7], a[152:155], v[76:79], a[4:7]\n"
    "v_exp_f32 v5, v5\n"
    "v_mfma_f32_16x16x32_bf16 v[136:139], a[140:143], v[72:75], v[136:139]\n"
    "ds_read_b128 a[140:143], v248 offset:21504\n"
    "ds_read_b128 a[152:155], v248 offset:11264\n"
    "v_mfma_f32_16x16x32_bf16 v[156:159], a[116:119], v[88:91], v[156:159]\n"
    "v_smfmac_f32_16x16x64_bf16 v[120:123], v[52:55], v[92:99], v253\n"
    "s_waitcnt lgkmcnt(8)\n"
    "v_mfma_f32_16x16x32_bf16 a[64:67], a[132:135], v[80:83], 0\n"
    "s_waitcnt lgkmcnt(6)\n"
    "v_mfma_f32_16x16x32_bf16 a[104:107], a[144:147], v[92:95], 0\n"
    "s_waitcnt lgkmcnt(2)\n"
    "v_exp_f32 v20, v20\n"
    "v_mfma_f32_16x16x32_bf16 a[52:55], a[136:139], v[68:71], 0\n"
    "ds_read_b128 a[116:119], v248 offset:18432\n"
    "v_mfma_f32_16x16x32_bf16 a[20:23], a[156:159], v[88:91], a[20:23]\n"
    "ds_read_b128 a[156:159], v248 offset:23552\n"
    "v_mfma_f32_16x16x32_bf16 v[196:199], a[112:115], v[68:71], 0\n"
    "buffer_load_dwordx2 v[236:237], v241, s[36:39], s81 offen offset:0 sc1\n"
    "v_mfma_f32_16x16x32_bf16 v[200:203], a[124:127], v[72:75], 0\n"
    "s_waitcnt lgkmcnt(2)\n"
    "v_exp_f32 v27, v27\n"
    "v_mfma_f32_16x16x32_bf16 a[60:63], a[152:155], v[68:71], 0\n"
    "v_mfma_f32_16x16x32_bf16 a[96:99], a[132:135], v[96:99], 0\n"
    "v_exp_f32 v17, v17\n"
    "v_mfma_f32_16x16x32_bf16 a[72:75], a[144:147], v[76:79], 0\n"
    "v_exp_f32 v24, v24\n"
    "v_mfma_f32_16x16x32_bf16 a[24:27], a[112:115], v[84:87], 0\n"
    "v_exp_f32 v35, v35\n"
    "v_mfma_f32_16x16x32_bf16 a[64:67], a[128:131], v[76:79], a[64:67]\n"
    "v_mfma_f32_16x16x32_bf16 a[80:83], a[128:131], v[84:87], 0\n"
    "s_add_u32 s67, s67, 49152\n"
    "v_exp_f32 v32, v32\n"
    "v_mfma_f32_16x16x32_bf16 a[12:15], a[120:123], v[76:79], 0\n"
    "v_mfma_f32_16x16x32_bf16 a[104:107], a[148:151], v[96:99], a[104:107]\n"
    "v_mov_b32 v2, v3\n"
    "v_mfma_f32_16x16x32_bf16 a[28:31], a[124:127], v[88:91], 0\n"
    "v_mfma_f32_16x16x32_bf16 a[28:31], a[120:123], v[84:87], a[28:31]\n"
    "v_mfma_f32_16x16x32_bf16 a[92:95], a[152:155], v[84:87], 0\n"
    "v_mfma_f32_16x16x32_bf16 v[200:203], a[120:123], v[68:71], v[200:203]\n"
    "s_waitcnt lgkmcnt(0)\n"
    "v_exp_f32 v12, v12\n"
    "v_mfma_f32_16x16x32_bf16 a[108:111], a[156:159], v[96:99], 0\n"
    "v_exp_f32 v18, v18\n"
    "v_mfma_f32_16x16x32_bf16 a[80:83], a[132:135], v[88:91], a[80:83]\n"
    "v_mfma_f32_16x16x32_bf16 a[44:47], a[120:123], v[92:95], 0\n"
    "v_exp_f32 v29, v29\n"
    "v_mfma_f32_16x16x32_bf16 a[12:15], a[124:127], v[80:83], a[12:15]\n"
    "ds_read_b128 a[120:123], v3 offset:16384\n"
    "v_mfma_f32_16x16x32_bf16 a[44:47], a[124:127], v[96:99], a[44:47]\n"
    "v_mfma_f32_16x16x32_bf16 a[56:59], a[148:151], v[72:75], 0\n"
    "v_exp_f32 v28, v28\n"
    "v_mfma_f32_16x16x32_bf16 a[96:99], a[128:131], v[92:95], a[96:99]\n"
    "v_exp_f32 v19, v19\n"
    "v_mfma_f32_16x16x32_bf16 a[40:43], a[116:119], v[96:99], 0\n"
    "v_exp_f32 v31, v31\n"
    "v_mfma_f32_16x16x32_bf16 a[60:63], a[156:159], v[72:75], a[60:63]\n"
    "v_exp_f32 v26, v26\n"
    "v_mfma_f32_16x16x32_bf16 a[72:75], a[148:151], v[80:83], a[72:75]\n"
    "v_mfma_f32_16x16x32_bf16 a[8:11], a[116:119], v[80:83], 0\n"
    "v_exp_f32 v6, v6\n"
    "v_mfma_f32_16x16x32_bf16 a[68:71], a[140:143], v[80:83], 0\n"
    "v_exp_f32 v25, v25\n"
    "v_mfma_f32_16x16x32_bf16 a[48:51], a[132:135], v[72:75], a[48:51]\n"
    "ds_read_b128 a[124:127], v3 offset:18432\n"
    "v_mfma_f32_16x16x32_bf16 a[40:43], a[112:115], v[92:95], a[40:43]\n"
    "ds_read_b128 a[132:135], v3 offset:22528\n"
    "v_mfma_f32_16x16x32_bf16 a[100:103], a[140:143], v[96:99], 0\n"
    "v_mfma_f32_16x16x32_bf16 v[196:199], a[116:119], v[72:75], v[196:199]\n"
    "v_exp_f32 v30, v30\n"
    "v_mfma_f32_16x16x32_bf16 a[108:111], a[152:155], v[92:95], a[108:111]\n"
    "v_mfma_f32_16x16x32_bf16 a[88:91], a[148:151], v[88:91], 0\n"
    "s_waitcnt vmcnt(17)\n"
    "v_exp_f32 v11, v11\n"
    "v_mfma_f32_16x16x32_bf16 a[8:11], a[112:115], v[76:79], a[8:11]\n"
    "ds_read_b128 a[128:131], v3 offset:20480\n"
    "ds_read_b128 a[112:115], v3 offset:12288\n"
    "v_mfma_f32_16x16x32_bf16 a[76:79], a[156:159], v[80:83], 0\n"
    "ds_add_u32 v247, v246\n"
    "v_mfma_f32_16x16x32_bf16 a[24:27], a[116:119], v[88:91], a[24:27]\n"
    "ds_read_b128 a[116:119], v3 offset:14336\n"
    "v_mfma_f32_16x16x32_bf16 a[56:59], a[144:147], v[68:71], a[56:59]\n"
    "v_exp_f32 v9, v9\n"
    "v_mfma_f32_16x16x32_bf16 a[84:87], a[136:139], v[84:87], 0\n"
    "v_exp_f32 v23, v23\n"
    "v_mfma_f32_16x16x32_bf16 a[88:91], a[144:147], v[84:87], a[88:91]\n"
    "v_add_u32 v3, 49152, v252\n"
    "v_exp_f32 v34, v34\n"
    "v_mfma_f32_16x16x32_bf16 a[84:87], a[140:143], v[88:91], a[84:87]\n"
    "v_mfma_f32_16x16x32_bf16 a[100:103], a[136:139], v[92:95], a[100:103]\n"
    "v_exp_f32 v13, v13\n"
    "v_mfma_f32_16x16x32_bf16 a[76:79], a[152:155], v[76:79], a[76:79]\n"
    "v_mfma_f32_16x16x32_bf16 a[68:71], a[136:139], v[76:79], a[68:71]\n"
    "s_waitcnt lgkmcnt(0)\n"
    "v_mfma_f32_16x16x32_bf16 a[92:95], a[156:159], v[88:91], a[92:95]\n"
    "v_mfma_f32_16x16x32_bf16 a[52:55], a[140:143], v[72:75], a[52:55]\n"
    "s_branch .LNK\n"
    ".fill ((0x20000 - (. - _Z8attn_fwdPKtPKiS2_S0_S0_S0_S0_PcPt)) & 4095) / 4, 4, 0xBF800000\n"
    ".LhKN:\n"
    "s_waitcnt vmcnt(28)\n"
    "ds_read_b128 v[52:55], v251 offset:768\n"
    "ds_read_b128 v[180:183], v96 offset:49152\n"
    "ds_read_b128 v[200:203], v97 offset:49408\n"
    "ds_read_b128 v[184:187], v97 offset:49152\n"
    "s_add_u32 m0, s68, 0\n"
    "ds_read_b128 v[196:199], v96 offset:49408\n"
    "ds_read_b128 v[188:191], v96 offset:49280\n"
    "ds_read_b128 v[192:195], v97 offset:49280\n"
    "s_waitcnt lgkmcnt(6)\n"
    "v_smfmac_f32_16x16x64_bf16 v[108:111], v[52:55], v[68:75], v253\n"
    "s_waitcnt lgkmcnt(4)\n"
    "v_pk_maximum3_f16 v98, v200, v201, v98\n"
    "v_pk_maximum3_f16 v98, v202, v203, v98\n"
    "v_pk_add_u16 v200, s89, v200 clamp\n"
    "v_pk_add_u16 v203, s89, v203 clamp\n"
    "v_smfmac_f32_16x16x64_bf16 v[116:119], v[52:55], v[84:91], v253\n"
    "s_waitcnt lgkmcnt(2)\n"
    "s_nop 0\n"
    "v_pk_add_u16 v202, s89, v202 clamp\n"
    "v_pk_add_u16 v201, s89, v201 clamp\n"
    "v_pk_maximum3_f16 v98, v196, v197, v98\n"
    "v_pk_maximum3_f16 v98, v198, v199, v98\n"
    "v_mfma_f32_16x16x32_bf16 v[64:67], a[44:47], v[200:203], v[64:67]\n"
    "v_pk_add_u16 v198, s89, v198 clamp\n"
    "v_pk_add_u16 v196, s89, v196 clamp\n"
    "v_mfma_f32_16x16x32_bf16 v[48:51], a[20:23], v[200:203], v[48:51]\n"
    "v_pk_add_u16 v199, s89, v199 clamp\n"
    "v_pk_add_u16 v197, s89, v197 clamp\n"
    "v_mfma_f32_16x16x32_bf16 v[32:35], a[156:159], v[200:203], v[32:35]\n"
    "s_waitcnt lgkmcnt(0)\n"
    "v_pk_maximum3_f16 v98, v192, v193, v98\n"
    "v_pk_maximum3_f16 v98, v194, v195, v98\n"
    "v_mfma_f32_16x16x32_bf16 v[64:67], a[40:43], v[196:199], v[64:67]\n"
    "v_pk_maximum3_f16 v98, v188, v189, v98\n"
    "v_pk_add_u16 v193, s89, v193 clamp\n"
    "v_mfma_f32_16x16x32_bf16 v[48:51], a[16:19], v[196:199], v[48:51]\n"
    "buffer_load_dwordx4 v1, s[16:19], s67 offen offset:0 sc1 lds\n"
    "v_mfma_f32_16x16x32_bf16 v[16:19], a[132:135], v[200:203], v[16:19]\n"
    "v_pk_add_u16 v194, s89, v194 clamp\n"
    "v_pk_maximum3_f16 v98, v190, v191, v98\n"
    "v_pk_add_u16 v192, s89, v192 clamp\n"
    "v_smfmac_f32_16x16x64_bf16 v[112:115], v[52:55], v[76:83], v253\n"
    "v_pk_add_u16 v191, s89, v191 clamp\n"
    "v_mfma_f32_16x16x32_bf16 v[16:19], a[128:131], v[196:199], v[16:19]\n"
    "v_pk_add_u16 v195, s89, v195 clamp\n"
    "v_pk_maximum3_f16 v98, v184, v185, v98\n"
    "v_mfma_f32_16x16x32_bf16 v[32:35], a[152:155], v[196:199], v[32:35]\n"
    "v_pk_add_u16 v185, s89, v185 clamp\n"
    "v_pk_add_u16 v188, s89, v188 clamp\n"
    "v_mfma_f32_16x16x32_bf16 v[64:67], a[36:39], v[192:195], v[64:67]\n"
    "v_pk_add_u16 v189, s89, v189 clamp\n"
    "v_pk_maximum3_f16 v98, v186, v187, v98\n"
    "v_mfma_f32_16x16x32_bf16 v[32:35], a[148:151], v[192:195], v[32:35]\n"
    "buffer_load_dwordx4 v1, s[16:19], s67 offen offset:1024 sc1 lds\n"
    "v_mfma_f32_16x16x32_bf16 v[16:19], a[124:127], v[192:195], v[16:19]\n"
    "v_pk_add_u16 v190, s89, v190 clamp\n"
    "v_pk_add_u16 v184, s89, v184 clamp\n"
    "v_pk_maximum3_f16 v98, v180, v181, v98\n"
    "v_mfma_f32_16x16x32_bf16 v[32:35], a[144:147], v[188:191], v[32:35]\n"
    "v_pk_add_u16 v187, s89, v187 clamp\n"
    "v_pk_add_u16 v186, s89, v186 clamp\n"
    "v_mfma_f32_16x16x32_bf16 v[48:51], a[12:15], v[192:195], v[48:51]\n"
    "s_waitcnt vmcnt(24)\n"
    "v_pk_maximum3_f16 v98, v182, v183, v98\n"
    "v_pk_add_u16 v182, s89, v182 clamp\n"
    "v_mfma_f32_16x16x32_bf16 v[48:51], a[8:11], v[188:191], v[48:51]\n"
    "s_barrier\n"
    "v_pk_add_u16 v180, s89, v180 clamp\n"
    "v_mfma_f32_16x16x32_bf16 v[64:67], a[32:35], v[188:191], v[64:67]\n"
    "ds_read_b128 a[72:75], v252 offset:25600\n"
    "v_pk_add_u16 v183, s89, v183 clamp\n"
    "v_mfma_f32_16x16x32_bf16 v[32:35], a[140:143], v[184:187], v[32:35]\n"
    "ds_read_b128 a[48:51], v252 offset:24576\n"
    "v_pk_add_u16 v181, s89, v181 clamp\n"
    "v_mfma_f32_16x16x32_bf16 v[16:19], a[120:123], v[188:191], v[16:19]\n"
    "buffer_load_dwordx4 v1, s[16:19], s67 offen offset:2048 sc1 lds\n"
    "v_mfma_f32_16x16x32_bf16 v[32:35], a[136:139], v[180:183], v[32:35]\n"
    "v_accvgpr_write_b32 a187, v199\n"
    "v_accvgpr_write_b32 a186, v198\n"
    "v_mfma_f32_16x16x32_bf16 v[64:67], a[28:31], v[184:187], v[64:67]\n"
    "ds_read_b128 a[92:95], v252 offset:35840\n"
    "ds_read_b128 a[64:67], v252 offset:32768\n"
    "v_mfma_f32_16x16x32_bf16 v[48:51], a[4:7], v[184:187], v[48:51]\n"
    "s_add_u32 s83, s67, 4096\n"
    "ds_read_b128 a[68:71], v252 offset:34816\n"
    "ds_read_b128 a[88:91], v252 offset:33792\n"
    "v_mfma_f32_16x16x32_bf16 v[16:19], a[116:119], v[184:187], v[16:19]\n"
    "v_accvgpr_write_b32 a178, v190\n"
    "v_accvgpr_write_b32 a173, v185\n"
    "v_mfma_f32_16x16x32_bf16 v[64:67], a[24:27], v[180:183], v[64:67]\n"
    "ds_read_b128 a[52:55], v252 offset:26624\n"
    "ds_read_b128 a[56:59], v252 offset:28672\n"
    "v_mfma_f32_16x16x32_bf16 v[48:51], a[0:3], v[180:183], v[48:51]\n"
    "ds_read_b128 a[84:87], v252 offset:31744\n"
    "v_accvgpr_write_b32 a177, v189\n"
    "v_mfma_f32_16x16x32_bf16 v[16:19], a[112:115], v[180:183], v[16:19]\n"
    "s_waitcnt lgkmcnt(8)\n"
    "buffer_load_dwordx4 v1, s[16:19], s67 offen offset:3072 sc1 lds\n"
    "v_mfma_f32_16x16x32_bf16 v[28:31], a[72:75], a[240:243], v[104:107]\n"
    "s_waitcnt lgkmcnt(7)\n"
    "ds_read_b128 a[60:63], v252 offset:30720\n"
    "v_accvgpr_write_b32 a170, v182\n"
    "v_mfma_f32_16x16x32_bf16 v[4:7], a[48:51], a[192:195], v[100:103]\n"
    "ds_read_b128 a[76:79], v252 offset:27648\n"
    "ds_read_b128 a[80:83], v252 offset:29696\n"
    "v_mfma_f32_16x16x32_bf16 v[12:15], a[48:51], a[240:243], v[100:103]\n"
    "s_waitcnt lgkmcnt(9)\n"
    "v_exp_f32 v16, v16\n"
    "v_mfma_f32_16x16x32_bf16 v[24:27], a[92:95], a[236:239], v[104:107]\n"
    "s_waitcnt lgkmcnt(6)\n"
    "v_accvgpr_write_b32 a183, v195\n"
    "v_accvgpr_write_b32 a190, v202\n"
    "v_mfma_f32_16x16x32_bf16 v[24:27], a[88:91], a[232:235], v[24:27]\n"
    "v_exp_f32 v17, v17\n"
    "v_mfma_f32_16x16x32_bf16 v[20:23], a[72:75], a[192:195], v[104:107]\n"
    "s_waitcnt lgkmcnt(5)\n"
    "v_accvgpr_write_b32 a172, v184\n"
    "v_accvgpr_write_b32 a168, v180\n"
    "v_mfma_f32_16x16x32_bf16 v[4:7], a[52:55], a[196:199], v[4:7]\n"
    "s_add_u32 m0, s68, 4096\n"
    "v_exp_f32 v34, v34\n"
    "v_mfma_f32_16x16x32_bf16 v[12:15], a[52:55], a[244:247], v[12:15]\n"
    "buffer_load_dwordx4 v1, s[16:19], s83 offen offset:0 sc1 lds\n"
    "v_mfma_f32_16x16x32_bf16 v[8:11], a[68:71], a[236:239], v[100:103]\n"
    "s_waitcnt lgkmcnt(4)\n"
    "v_accvgpr_write_b32 a182, v194\n"
    "v_accvgpr_write_b32 a189, v201\n"
    "v_mfma_f32_16x16x32_bf16 v[4:7], a[56:59], a[200:203], v[4:7]\n"
    "s_waitcnt lgkmcnt(2)\n"
    "v_accvgpr_write_b32 a175, v187\n"
    "v_accvgpr_write_b32 a180, v192\n"
    "v_mfma_f32_16x16x32_bf16 v[4:7], a[60:63], a[204:207], v[4:7]\n"
    "s_waitcnt lgkmcnt(1)\n"
    "v_accvgpr_write_b32 a176, v188\n"
    "v_pk_add_u16 v98, s89, v98\n"
    "v_mfma_f32_16x16x32_bf16 v[28:31], a[76:79], a[244:247], v[28:31]\n"
    "v_accvgpr_write_b32 a188, v200\n"
    "v_accvgpr_write_b32 a191, v203\n"
    "v_mfma_f32_16x16x32_bf16 v[20:23], a[76:79], a[196:199], v[20:23]\n"
    "s_waitcnt lgkmcnt(0)\n"
    "v_accvgpr_write_b32 a184, v196\n"
    "v_mfma_f32_16x16x32_bf16 v[20:23], a[80:83], a[200:203], v[20:23]\n"
    "v_accvgpr_write_b32 a181, v193\n"
    "v_mfma_f32_16x16x32_bf16 v[12:15], a[56:59], a[248:251], v[12:15]\n"
    "buffer_load_dwordx4 v1, s[16:19], s83 offen offset:1024 sc1 lds\n"
    "v_mfma_f32_16x16x32_bf16 v[20:23], a[84:87], a[204:207], v[20:23]\n"
    "v_accvgpr_write_b32 a171, v183\n"
    "v_accvgpr_write_b32 a185, v197\n"
    "v_mfma_f32_16x16x32_bf16 v[4:7], a[64:67], a[208:211], v[4:7]\n"
    "s_add_u32 m0, s70, 0\n"
    "v_accvgpr_write_b32 a179, v191\n"
    "v_accvgpr_write_b32 a169, v181\n"
    "v_mfma_f32_16x16x32_bf16 v[4:7], a[68:71], a[212:215], v[4:7]\n"
    "s_add_u32 s83, s67, 24576\n"
    "v_mfma_f32_16x16x32_bf16 v[28:31], a[80:83], a[248:251], v[28:31]\n"
    "v_exp_f32 v19, v19\n"
    "v_mfma_f32_16x16x32_bf16 v[8:11], a[64:67], a[232:235], v[8:11]\n"
    "v_accvgpr_write_b32 a174, v186\n"
    "v_mfma_f32_16x16x32_bf16 v[24:27], a[84:87], a[228:231], v[24:27]\n"
    "v_exp_f32 v33, v33\n"
    "v_mfma_f32_16x16x32_bf16 v[20:23], a[88:91], a[208:211], v[20:23]\n"
    "buffer_load_dwordx4 v1, s[16:19], s83 offen offset:0 sc1 lds\n"
    "v_mfma_f32_16x16x32_bf16 v[28:31], a[84:87], a[252:255], v[28:31]\n"
    "v_exp_f32 v32, v32\n"
    "v_mfma_f32_16x16x32_bf16 v[28:31], a[88:91], a[160:163], v[28:31]\n"
    "v_exp_f32 v18, v18\n"
    "v_mfma_f32_16x16x32_bf16 v[24:27], a[80:83], a[224:227], v[24:27]\n"
    "v_exp_f32 v49, v49\n"
    "v_mfma_f32_16x16x32_bf16 v[8:11], a[60:63], a[228:231], v[8:11]\n"
    "v_exp_f32 v35, v35\n"
    "v_mfma_f32_16x16x32_bf16 v[24:27], a[76:79], a[220:223], v[24:27]\n"
    "v_cvt_pk_bf16_f32 v92, v16, v17\n"
    "v_cvt_pk_bf16_f32 v93, v18, v19\n"
    "v_mfma_f32_16x16x32_bf16 v[12:15], a[60:63], a[252:255], v[12:15]\n"
    "v_and_b32 v98, 0x80008000, v98\n"
    "v_cvt_pk_bf16_f32 v94, v32, v33\n"
    "v_mfma_f32_16x16x32_bf16 v[24:27], a[72:75], a[216:219], v[24:27]\n"
    "buffer_load_dwordx4 v1, s[16:19], s83 offen offset:1024 sc1 lds\n"
    "v_mfma_f32_16x16x32_bf16 v[16:19], a[68:71], a[188:191], v[100:103]\n"
    "v_cvt_pk_bf16_f32 v95, v34, v35\n"
    "v_mfma_f32_16x16x32_bf16 v[16:19], a[64:67], a[184:187], v[16:19]\n"
    "s_waitcnt vmcnt(24)\n"
    "s_barrier\n"
    "v_mfma_f32_16x16x32_bf16 v[8:11], a[56:59], a[224:227], v[8:11]\n"
    "ds_read_b128 a[124:127], v249 offset:13312\n"
    "ds_read_b128 a[120:123], v249 offset:1024\n"
    "v_mfma_f32_16x16x32_bf16 v[32:35], a[92:95], a[188:191], v[104:107]\n"
    "ds_read_b128 a[116:119], v249 offset:12288\n"
    "ds_read_b128 a[112:115], v249 offset:0\n"
    "v_mfma_f32_16x16x32_bf16 v[32:35], a[88:91], a[184:187], v[32:35]\n"
    "ds_read_b128 a[132:135], v249 offset:14336\n"
    "ds_read_b128 a[144:147], v249 offset:4096\n"
    "v_mfma_f32_16x16x32_bf16 v[8:11], a[52:55], a[220:223], v[8:11]\n"
    "s_add_u32 s76, s71, s60\n"
    "v_exp_f32 v51, v51\n"
    "v_mfma_f32_16x16x32_bf16 v[12:15], a[64:67], a[160:163], v[12:15]\n"
    "buffer_load_dwordx4 v1, s[16:19], s83 offen offset:2048 sc1 lds\n"
    "v_mfma_f32_16x16x32_bf16 v[28:31], a[92:95], a[164:167], v[28:31]\n"
    "v_exp_f32 v66, v66\n"
    "v_mfma_f32_16x16x32_bf16 v[16:19], a[60:63], a[180:183], v[16:19]\n"
    "v_exp_f32 v50, v50\n"
    "v_mfma_f32_16x16x32_bf16 v[12:15], a[68:71], a[164:167], v[12:15]\n"
    "v_exp_f32 v67, v67\n"
    "v_mfma_f32_16x16x32_bf16 v[8:11], a[48:51], a[216:219], v[8:11]\n"
    "s_add_u32 s77, s71, s61\n"
    "ds_read_b128 a[156:159], v249 offset:17408\n"
    "s_waitcnt lgkmcnt(6)\n"
    "v_mfma_f32_16x16x32_bf16 v[128:131], a[124:127], v[72:75], 0\n"
    "ds_read_b128 a[136:139], v249 offset:3072\n"
    "v_mfma_f32_16x16x32_bf16 v[160:163], a[124:127], v[88:91], 0\n"
    "s_waitcnt lgkmcnt(4)\n"
    "ds_read_b128 a[148:151], v249 offset:16384\n"
    "ds_read_b128 a[128:131], v249 offset:2048\n"
    "v_mfma_f32_16x16x32_bf16 v[156:159], a[112:115], v[84:87], 0\n"
    "buffer_load_dwordx4 v1, s[16:19], s83 offen offset:3072 sc1 lds\n"
    "v_mfma_f32_16x16x32_bf16 v[16:19], a[56:59], a[176:179], v[16:19]\n"
    "s_add_u32 s83, s67, 28672\n"
    "ds_read_b128 a[152:155], v249 offset:5120\n"
    "v_mfma_f32_16x16x32_bf16 v[16:19], a[52:55], a[172:175], v[16:19]\n"
    "s_add_u32 m0, s70, 4096\n"
    "v_exp_f32 v7, v7\n"
    "v_mfma_f32_16x16x32_bf16 v[20:23], a[92:95], a[212:215], v[20:23]\n"
    "v_mfma_f32_16x16x32_bf16 v[156:159], a[116:119], v[88:91], v[156:159]\n"
    "v_exp_f32 v48, v48\n"
    "v_mfma_f32_16x16x32_bf16 v[32:35], a[84:87], a[180:183], v[32:35]\n"
    "s_waitcnt lgkmcnt(6)\n"
    "v_exp_f32 v65, v65\n"
    "v_mfma_f32_16x16x32_bf16 v[148:151], a[132:135], v[80:83], 0\n"
    "ds_read_b128 v[36:39], v243 offset:0\n"
    "v_cmp_ne_u32 s[34:35], 0, v98\n"
    "v_mfma_f32_16x16x32_bf16 v[124:127], a[112:115], v[68:71], 0\n"
    "buffer_load_dwordx4 v1, s[16:19], s83 offen offset:0 sc1 lds\n"
    "v_mfma_f32_16x16x32_bf16 v[176:179], a[120:123], v[92:95], 0\n"
    "s_waitcnt lgkmcnt(3)\n"
    "v_exp_f32 v10, v10\n"
    "v_mfma_f32_16x16x32_bf16 a[16:19], a[148:151], v[88:91], 0\n"
    "v_exp_f32 v64, v64\n"
    "v_mfma_f32_16x16x32_bf16 v[136:139], a[136:139], v[68:71], 0\n"
    "v_exp_f32 v4, v4\n"
    "v_mfma_f32_16x16x32_bf16 v[32:35], a[80:83], a[176:179], v[32:35]\n"
    "ds_read_b128 a[140:143], v249 offset:15360\n"
    "v_add_u32 v251, 1536, v251\n"
    "v_mfma_f32_16x16x32_bf16 v[140:143], a[116:119], v[80:83], 0\n"
    "v_cvt_pk_bf16_f32 v97, v50, v51\n"
    "v_mfma_f32_16x16x32_bf16 v[16:19], a[48:51], a[168:171], v[16:19]\n"
    "s_waitcnt lgkmcnt(3)\n"
    "v_cvt_pk_bf16_f32 v98, v64, v65\n"
    "v_cvt_pk_bf16_f32 v99, v66, v67\n"
    "v_mfma_f32_16x16x32_bf16 v[164:167], a[128:131], v[84:87], 0\n"
    "buffer_load_dwordx4 v1, s[16:19], s83 offen offset:1024 sc1 lds\n"
    "v_mfma_f32_16x16x32_bf16 a[4:7], a[156:159], v[80:83], 0\n"
    "v_exp_f32 v27, v27\n"
    "v_mfma_f32_16x16x32_bf16 v[128:131], a[120:123], v[68:71], v[128:131]\n"
    "s_waitcnt lgkmcnt(2)\n"
    "v_mfma_f32_16x16x32_bf16 v[192:195], a[152:155], v[68:71], 0\n"
    "v_exp_f32 v26, v26\n"
    "v_mfma_f32_16x16x32_bf16 a[32:35], a[144:147], v[92:95], 0\n"
    "ds_read_b128 v[100:103], v250 offset:1664\n"
    "s_waitcnt lgkmcnt(1)\n"
    "v_mfma_f32_16x16x32_bf16 v[152:155], a[140:143], v[80:83], 0\n"
    "v_exp_f32 v22, v22\n"
    "v_mfma_f32_16x16x32_bf16 v[32:35], a[76:79], a[172:175], v[32:35]\n"
    "v_cvt_pk_bf16_f32 v96, v48, v49\n"
    "v_mfma_f32_16x16x32_bf16 v[144:147], a[120:123], v[76:79], 0\n"
    "s_waitcnt vmcnt(12)\n"
    "v_fma_f32 v216, v216, v36, -v38\n"
    "v_fma_f32 v229, v229, v37, -v39\n"
    "v_mfma_f32_16x16x32_bf16 v[160:163], a[120:123], v[84:87], v[160:163]\n"
    "v_fma_f32 v206, v206, v36, -v38\n"
    "v_fma_f32 v213, v213, v37, -v39\n"
    "v_mfma_f32_16x16x32_bf16 v[172:175], a[116:119], v[96:99], 0\n"
    "v_fma_f32 v214, v214, v36, -v38\n"
    "v_mfma_f32_16x16x32_bf16 v[124:127], a[116:119], v[72:75], v[124:127]\n"
    "v_fma_f32 v207, v207, v37, -v39\n"
    "v_fma_f32 v205, v205, v37, -v39\n"
    "v_mfma_f32_16x16x32_bf16 v[176:179], a[124:127], v[96:99], v[176:179]\n"
    "ds_read_b128 a[116:119], v249 offset:18432\n"
    "v_fma_f32 v209, v209, v37, -v39\n"
    "v_mfma_f32_16x16x32_bf16 v[144:147], a[124:127], v[80:83], v[144:147]\n"
    "v_fma_f32 v215, v215, v37, -v39\n"
    "v_fma_f32 v217, v217, v37, -v39\n"
    "v_mfma_f32_16x16x32_bf16 a[4:7], a[152:155], v[76:79], a[4:7]\n"
    "v_exp_f32 v13, v13\n"
    "v_mfma_f32_16x16x32_bf16 v[140:143], a[112:115], v[76:79], v[140:143]\n"
    "v_fma_f32 v212, v212, v36, -v38\n"
    "v_mfma_f32_16x16x32_bf16 v[184:187], a[140:143], v[96:99], 0\n"
    "v_cvt_pk_bf16_f32 v216, v216, v217\n"
    "v_fma_f32 v223, v223, v37, -v39\n"
    "v_mfma_f32_16x16x32_bf16 v[164:167], a[132:135], v[88:91], v[164:167]\n"
    "v_fma_f32 v222, v222, v36, -v38\n"
    "v_fma_f32 v231, v231, v37, -v39\n"
    "v_mfma_f32_16x16x32_bf16 v[188:191], a[148:151], v[72:75], 0\n"
    "v_exp_f32 v21, v21\n"
    "v_mfma_f32_16x16x32_bf16 v[172:175], a[112:115], v[92:95], v[172:175]\n"
    "v_fma_f32 v235, v235, v37, -v39\n"
    "v_fma_f32 v211, v211, v37, -v39\n"
    "v_mfma_f32_16x16x32_bf16 v[132:135], a[128:131], v[68:71], 0\n"
    "ds_read_b128 a[112:115], v249 offset:6144\n"
    "v_mfma_f32_16x16x32_bf16 a[32:35], a[148:151], v[96:99], a[32:35]\n"
    "v_exp_f32 v5, v5\n"
    "v_mfma_f32_16x16x32_bf16 v[180:183], a[132:135], v[96:99], 0\n"
    "ds_read_b128 a[120:123], v249 offset:7168\n"
    "v_cvt_pk_bf16_f32 v206, v206, v207\n"
    "v_mfma_f32_16x16x32_bf16 v[132:135], a[132:135], v[72:75], v[132:135]\n"
    "ds_read_b128 a[124:127], v249 offset:19456\n"
    "v_fma_f32 v221, v221, v37, -v39\n"
    "v_mfma_f32_16x16x32_bf16 v[180:183], a[128:131], v[92:95], v[180:183]\n"
    "ds_read_b128 a[132:135], v249 offset:20480\n"
    "v_fma_f32 v233, v233, v37, -v39\n"
    "v_mfma_f32_16x16x32_bf16 a[0:3], a[144:147], v[76:79], 0\n"
    "v_exp_f32 v12, v12\n"
    "v_mfma_f32_16x16x32_bf16 a[0:3], a[148:151], v[80:83], a[0:3]\n"
    "s_waitcnt lgkmcnt(4)\n"
    "v_fma_f32 v204, v204, v36, -v38\n"
    "v_fma_f32 v218, v218, v36, -v38\n"
    "v_mfma_f32_16x16x32_bf16 a[8:11], a[116:119], v[80:83], 0\n"
    "v_cvt_pk_bf16_f32 v212, v212, v213\n"
    "v_fma_f32 v219, v219, v37, -v39\n"
    "v_mfma_f32_16x16x32_bf16 a[20:23], a[152:155], v[84:87], 0\n"
    "v_cvt_pk_bf16_f32 v204, v204, v205\n"
    "v_fma_f32 v220, v220, v36, -v38\n"
    "v_mfma_f32_16x16x32_bf16 a[36:39], a[156:159], v[96:99], 0\n"
    "v_mfma_f32_16x16x32_bf16 a[16:19], a[144:147], v[84:87], a[16:19]\n"
    "s_waitcnt lgkmcnt(2)\n"
    "buffer_store_dword v204, v244, s[16:19], s76 offen offset:0 sc1\n"
    "v_exp_f32 v16, v16\n"
    "v_mfma_f32_16x16x32_bf16 a[12:15], a[120:123], v[76:79], 0\n"
    "v_mfma_f32_16x16x32_bf16 v[32:35], a[72:75], a[168:171], v[32:35]\n"
    "v_fma_f32 v234, v234, v36, -v38\n"
    "v_fma_f32 v208, v208, v36, -v38\n"
    "v_mfma_f32_16x16x32_bf16 a[36:39], a[152:155], v[92:95], a[36:39]\n"
    "buffer_store_dword v206, v244, s[16:19], s76 offen offset:128 sc1\n"
    "v_fma_f32 v225, v225, v37, -v39\n"
    "v_fma_f32 v224, v224, v36, -v38\n"
    "v_mfma_f32_16x16x32_bf16 v[168:171], a[136:139], v[84:87], 0\n"
    "s_waitcnt lgkmcnt(1)\n"
    "v_cvt_pk_bf16_f32 v208, v208, v209\n"
    "v_fma_f32 v230, v230, v36, -v38\n"
    "v_mfma_f32_16x16x32_bf16 v[200:203], a[124:127], v[72:75], 0\n"
    "v_fma_f32 v226, v226, v36, -v38\n"
    "v_fma_f32 v210, v210, v36, -v38\n"
    "v_mfma_f32_16x16x32_bf16 a[44:47], a[120:123], v[92:95], 0\n"
    "buffer_store_dword v208, v244, s[16:19], s76 offen offset:1024 sc1\n"
    "v_smfmac_f32_16x16x64_bf16 v[120:123], v[52:55], v[92:99], v253\n"
    "v_cvt_pk_bf16_f32 v210, v210, v211\n"
    "v_mfma_f32_16x16x32_bf16 v[188:191], a[144:147], v[68:71], v[188:191]\n"
    "buffer_store_dword v210, v244, s[16:19], s76 offen offset:1152 sc1\n"
    "v_mfma_f32_16x16x32_bf16 v[148:151], a[128:131], v[76:79], v[148:151]\n"
    "v_cvt_pk_bf16_f32 v214, v214, v215\n"
    "v_fma_f32 v227, v227, v37, -v39\n"
    "v_mfma_f32_16x16x32_bf16 a[8:11], a[112:115], v[76:79], a[8:11]\n"
    "ds_read_b128 a[144:147], v249 offset:10240\n"
    "ds_read_b128 a[128:131], v249 offset:8192\n"
    "v_mfma_f32_16x16x32_bf16 a[28:31], a[124:127], v[88:91], 0\n"
    "v_exp_f32 v24, v24\n"
    "v_mfma_f32_16x16x32_bf16 a[24:27], a[112:115], v[84:87], 0\n"
    "v_mov_b32 v2, v3\n"
    "v_cvt_pk_bf16_f32 v222, v222, v223\n"
    "v_mfma_f32_16x16x32_bf16 a[40:43], a[116:119], v[96:99], 0\n"
    "ds_read_b128 a[148:151], v249 offset:22528\n"
    "v_fma_f32 v232, v232, v36, -v38\n"
    "v_mfma_f32_16x16x32_bf16 v[200:203], a[120:123], v[68:71], v[200:203]\n"
    "buffer_store_dword v212, v244, s[16:19], s76 offen offset:256 sc1\n"
    "v_cvt_pk_bf16_f32 v224, v224, v225\n"
    "v_mfma_f32_16x16x32_bf16 v[152:155], a[136:139], v[76:79], v[152:155]\n"
    "s_waitcnt lgkmcnt(3)\n"
    "v_exp_f32 v14, v14\n"
    "v_mfma_f32_16x16x32_bf16 a[96:99], a[132:135], v[96:99], 0\n"
    "v_exp_f32 v31, v31\n"
    "v_mfma_f32_16x16x32_bf16 v[196:199], a[112:115], v[68:71], 0\n"
    "s_waitcnt lgkmcnt(2)\n"
    "buffer_store_dword v214, v244, s[16:19], s76 offen offset:384 sc1\n"
    "v_exp_f32 v6, v6\n"
    "v_mfma_f32_16x16x32_bf16 a[104:107], a[144:147], v[92:95], 0\n"
    "v_exp_f32 v18, v18\n"
    "v_mfma_f32_16x16x32_bf16 a[28:31], a[120:123], v[84:87], a[28:31]\n"
    "v_exp_f32 v33, v33\n"
    "v_mfma_f32_16x16x32_bf16 a[64:67], a[132:135], v[80:83], 0\n"
    "v_cvt_pk_bf16_f32 v230, v230, v231\n"
    "v_mfma_f32_16x16x32_bf16 v[168:171], a[140:143], v[88:91], v[168:171]\n"
    "s_waitcnt lgkmcnt(1)\n"
    "buffer_load_dwordx2 v[236:237], v241, s[36:39], s81 offen offset:0 sc1\n"
    "v_mfma_f32_16x16x32_bf16 a[48:51], a[128:131], v[68:71], 0\n"
    "v_exp_f32 v19, v19\n"
    "v_mfma_f32_16x16x32_bf16 v[136:139], a[140:143], v[72:75], v[136:139]\n"
    "s_waitcnt lgkmcnt(0)\n"
    "buffer_store_dword v216, v244, s[16:19], s76 offen offset:1280 sc1\n"
    "v_mfma_f32_16x16x32_bf16 a[104:107], a[148:151], v[96:99], a[104:107]\n"
    "v_exp_f32 v29, v29\n"
    "v_mfma_f32_16x16x32_bf16 a[88:91], a[148:151], v[88:91], 0\n"
    "v_fma_f32 v228, v228, v36, -v38\n"
    "v_cvt_pk_bf16_f32 v226, v226, v227\n"
    "v_mfma_f32_16x16x32_bf16 v[196:199], a[116:119], v[72:75], v[196:199]\n"
    "v_cvt_pk_bf16_f32 v218, v218, v219\n"
    "v_cvt_pk_bf16_f32 v220, v220, v221\n"
    "v_mfma_f32_16x16x32_bf16 v[184:187], a[136:139], v[92:95], v[184:187]\n"
    "buffer_store_dword v218, v244, s[16:19], s76 offen offset:1408 sc1\n"
    "ds_read_b128 a[140:143], v249 offset:21504\n"
    "v_mfma_f32_16x16x32_bf16 a[24:27], a[116:119], v[88:91], a[24:27]\n"
    "v_exp_f32 v32, v32\n"
    "v_mfma_f32_16x16x32_bf16 v[192:195], a[156:159], v[72:75], v[192:195]\n"
    "ds_read_b128 a[152:155], v249 offset:11264\n"
    "v_cvt_pk_bf16_f32 v232, v232, v233\n"
    "v_mfma_f32_16x16x32_bf16 a[20:23], a[156:159], v[88:91], a[20:23]\n"
    "v_exp_f32 v9, v9\n"
    "v_mfma_f32_16x16x32_bf16 a[80:83], a[128:131], v[84:87], 0\n"
    "v_exp_f32 v11, v11\n"
    "v_mfma_f32_16x16x32_bf16 a[12:15], a[124:127], v[80:83], a[12:15]\n"
    "buffer_store_dword v220, v244, s[16:19], s77 offen offset:0 sc1\n"
    "ds_read_b128 a[156:159], v249 offset:23552\n"
    "v_mfma_f32_16x16x32_bf16 a[72:75], a[144:147], v[76:79], 0\n"
    "v_exp_f32 v28, v28\n"
    "v_mfma_f32_16x16x32_bf16 a[64:67], a[128:131], v[76:79], a[64:67]\n"
    "buffer_store_dword v222, v244, s[16:19], s77 offen offset:128 sc1\n"
    "v_mfma_f32_16x16x32_bf16 a[80:83], a[132:135], v[88:91], a[80:83]\n"
    "v_mfma_f32_16x16x32_bf16 a[88:91], a[144:147], v[84:87], a[88:91]\n"
    "v_exp_f32_e64 v30, v30\n"
    "v_mfma_f32_16x16x32_bf16 a[56:59], a[148:151], v[72:75], 0\n"
    "v_add_u32 v243, 512, v243\n"
    "v_cvt_pk_bf16_f32 v228, v228, v229\n"
    "v_mfma_f32_16x16x32_bf16 a[56:59], a[144:147], v[68:71], a[56:59]\n"
    "s_add_u32 s67, s67, 49152\n"
    "buffer_store_dword v224, v244, s[16:19], s77 offen offset:1024 sc1\n"
    "v_mfma_f32_16x16x32_bf16 a[48:51], a[132:135], v[72:75], a[48:51]\n"
    "v_exp_f32 v20, v20\n"
    "v_mfma_f32_16x16x32_bf16 a[44:47], a[124:127], v[96:99], a[44:47]\n"
    "s_waitcnt lgkmcnt(0)\n"
    "buffer_store_dword v226, v244, s[16:19], s77 offen offset:1152 sc1\n"
    "ds_read_b128 a[136:139], v249 offset:9216\n"
    "v_mfma_f32_16x16x32_bf16 a[108:111], a[156:159], v[96:99], 0\n"
    "v_mfma_f32_16x16x32_bf16 a[96:99], a[128:131], v[92:95], a[96:99]\n"
    "v_exp_f32 v15, v15\n"
    "v_mfma_f32_16x16x32_bf16 a[100:103], a[140:143], v[96:99], 0\n"
    "buffer_store_dword v228, v244, s[16:19], s77 offen offset:256 sc1\n"
    "v_exp_f32 v35, v35\n"
    "v_mfma_f32_16x16x32_bf16 a[68:71], a[140:143], v[80:83], 0\n"
    "ds_read_b128 a[120:123], v3 offset:16384\n"
    "ds_read_b128 a[116:119], v3 offset:14336\n"
    "v_mfma_f32_16x16x32_bf16 a[40:43], a[112:115], v[92:95], a[40:43]\n"
    "v_exp_f32 v8, v8\n"
    "v_mfma_f32_16x16x32_bf16 a[76:79], a[156:159], v[80:83], 0\n"
    "buffer_store_dword v230, v244, s[16:19], s77 offen offset:384 sc1\n"
    "v_mfma_f32_16x16x32_bf16 a[76:79], a[152:155], v[76:79], a[76:79]\n"
    "v_exp_f32 v34, v34\n"
    "v_mfma_f32_16x16x32_bf16 a[60:63], a[152:155], v[68:71], 0\n"
    "s_waitcnt lgkmcnt(2)\n"
    "ds_read_b128 a[124:127], v3 offset:18432\n"
    "ds_read_b128 a[128:131], v3 offset:20480\n"
    "v_mfma_f32_16x16x32_bf16 a[92:95], a[152:155], v[84:87], 0\n"
    "s_waitcnt vmcnt(15)\n"
    "ds_add_u32 v247, v246\n"
    "v_mfma_f32_16x16x32_bf16 a[108:111], a[152:155], v[92:95], a[108:111]\n"
    "ds_read_b128 a[132:135], v3 offset:22528\n"
    "v_add_u32 v250, 1536, v250\n"
    "v_mfma_f32_16x16x32_bf16 a[52:55], a[136:139], v[68:71], 0\n"
    "ds_read_b128 a[112:115], v3 offset:12288\n"
    "v_mfma_f32_16x16x32_bf16 a[84:87], a[136:139], v[84:87], 0\n"
    "v_add_u32 v3, 49152, v252\n"
    "v_mfma_f32_16x16x32_bf16 a[72:75], a[148:151], v[80:83], a[72:75]\n"
    "buffer_store_dword v232, v244, s[16:19], s77 offen offset:1280 sc1\n"
    "v_cvt_pk_bf16_f32 v234, v234, v235\n"
    "v_mfma_f32_16x16x32_bf16 a[100:103], a[136:139], v[92:95], a[100:103]\n"
    "v_exp_f32 v17, v17\n"
    "v_mfma_f32_16x16x32_bf16 a[84:87], a[140:143], v[88:91], a[84:87]\n"
    "v_exp_f32 v23, v23\n"
    "v_mfma_f32_16x16x32_bf16 a[60:63], a[156:159], v[72:75], a[60:63]\n"
    "buffer_store_dword v234, v244, s[16:19], s77 offen offset:1408 sc1\n"
    "v_mfma_f32_16x16x32_bf16 a[52:55], a[140:143], v[72:75], a[52:55]\n"
    "v_exp_f32 v25, v25\n"
    "v_mfma_f32_16x16x32_bf16 a[68:71], a[136:139], v[76:79], a[68:71]\n"
    "s_waitcnt lgkmcnt(0)\n"
    "v_mfma_f32_16x16x32_bf16 a[92:95], a[156:159], v[88:91], a[92:95]\n"
    "s_branch .LPK\n"
    ".fill ((0x20000 - (. - _Z8attn_fwdPKtPKiS2_S0_S0_S0_S0_PcPt)) & 4095) / 4, 4, 0xBF800000\n"
    ".LhVP:\n"
    "s_waitcnt vmcnt(12)\n"
    "ds_read_b128 v[52:55], v251 offset:0\n"
    "s_add_u32 s83, s67, 4096\n"
    "ds_read_b128 v[200:203], v97 offset:49408\n"
    "ds_read_b128 v[196:199], v96 offset:49408\n"
    "v_mov_b32 v2, v3\n"
    "ds_read_b128 v[192:195], v97 offset:49280\n"
    "ds_read_b128 v[188:191], v96 offset:49280\n"
    "ds_read_b128 v[184:187], v97 offset:49152\n"
    "s_add_u32 m0, s68, .Lend-.Lend2+(0)\n"
    "ds_read_b128 v[180:183], v96 offset:49152\n"
    "s_waitcnt lgkmcnt(6)\n"
    "v_smfmac_f32_16x16x64_bf16 v[108:111], v[52:55], v[68:75], v253\n"
    "s_waitcnt lgkmcnt(4)\n"
    "v_pk_maximum3_f16 v98, v200, v201, v98\n"
    "v_pk_maximum3_f16 v98, v202, v203, v98\n"
    "v_pk_add_u16 v203, s89, v203 clamp\n"
    "v_pk_add_u16 v201, s89, v201 clamp\n"
    "v_pk_add_u16 v200, s89, v200 clamp\n"
    "v_pk_add_u16 v202, s89, v202 clamp\n"
    "v_pk_maximum3_f16 v98, v196, v197, v98\n"
    "v_smfmac_f32_16x16x64_bf16 v[116:119], v[52:55], v[84:91], v253\n"
    "v_pk_maximum3_f16 v98, v198, v199, v98\n"
    "v_mfma_f32_16x16x32_bf16 v[64:67], a[44:47], v[200:203], v[64:67]\n"
    "s_waitcnt lgkmcnt(3)\n"
    "v_pk_add_u16 v197, s89, v197 clamp\n"
    "v_pk_maximum3_f16 v98, v192, v193, v98\n"
    "v_mfma_f32_16x16x32_bf16 v[48:51], a[20:23], v[200:203], v[48:51]\n"
    "buffer_load_dwordx4 v1, s[16:19], s67 offen offset:0 sc1 lds\n"
    "v_mfma_f32_16x16x32_bf16 v[16:19], a[132:135], v[200:203], v[16:19]\n"
    "v_pk_add_u16 v199, s89, v199 clamp\n"
    "v_pk_add_u16 v198, s89, v198 clamp\n"
    "v_smfmac_f32_16x16x64_bf16 v[112:115], v[52:55], v[76:83], v253\n"
    "v_pk_add_u16 v196, s89, v196 clamp\n"
    "v_mfma_f32_16x16x32_bf16 v[32:35], a[156:159], v[200:203], v[32:35]\n"
    "s_waitcnt lgkmcnt(2)\n"
    "v_pk_add_u16 v193, s89, v193 clamp\n"
    "v_pk_maximum3_f16 v98, v194, v195, v98\n"
    "v_pk_maximum3_f16 v98, v188, v189, v98\n"
    "v_mfma_f32_16x16x32_bf16 v[32:35], a[152:155], v[196:199], v[32:35]\n"
    "v_pk_maximum3_f16 v98, v190, v191, v98\n"
    "v_pk_add_u16 v192, s89, v192 clamp\n"
    "v_mfma_f32_16x16x32_bf16 v[16:19], a[128:131], v[196:199], v[16:19]\n"
    "v_pk_add_u16 v194, s89, v194 clamp\n"
    "v_pk_add_u16 v195, s89, v195 clamp\n"
    "v_mfma_f32_16x16x32_bf16 v[48:51], a[16:19], v[196:199], v[48:51]\n"
    "buffer_load_dwordx4 v1, s[16:19], s67 offen offset:1024 sc1 lds\n"
    "v_mfma_f32_16x16x32_bf16 v[48:51], a[12:15], v[192:195], v[48:51]\n"
    "s_waitcnt lgkmcnt(1)\n"
    "v_pk_maximum3_f16 v98, v184, v185, v98\n"
    "v_pk_add_u16 v188, s89, v188 clamp\n"
    "v_pk_maximum3_f16 v98, v186, v187, v98\n"
    "v_mfma_f32_16x16x32_bf16 v[32:35], a[148:151], v[192:195], v[32:35]\n"
    "v_pk_add_u16 v190, s89, v190 clamp\n"
    "v_accvgpr_write_b32 a184, v196\n"
    "v_mfma_f32_16x16x32_bf16 v[16:19], a[124:127], v[192:195], v[16:19]\n"
    "v_pk_add_u16 v189, s89, v189 clamp\n"
    "v_pk_add_u16 v191, s89, v191 clamp\n"
    "v_mfma_f32_16x16x32_bf16 v[64:67], a[40:43], v[196:199], v[64:67]\n"
    "v_pk_add_u16 v186, s89, v186 clamp\n"
    "v_pk_add_u16 v187, s89, v187 clamp\n"
    "v_mfma_f32_16x16x32_bf16 v[32:35], a[144:147], v[188:191], v[32:35]\n"
    "s_waitcnt lgkmcnt(0)\n"
    "v_pk_add_u16 v185, s89, v185 clamp\n"
    "v_pk_maximum3_f16 v98, v180, v181, v98\n"
    "v_mfma_f32_16x16x32_bf16 v[48:51], a[8:11], v[188:191], v[48:51]\n"
    "s_waitcnt vmcnt(8)\n"
    "buffer_load_dwordx4 v1, s[16:19], s67 offen offset:2048 sc1 lds\n"
    "v_mfma_f32_16x16x32_bf16 v[64:67], a[36:39], v[192:195], v[64:67]\n"
    "s_barrier\n"
    "v_pk_add_u16 v184, s89, v184 clamp\n"
    "v_mfma_f32_16x16x32_bf16 v[16:19], a[120:123], v[188:191], v[16:19]\n"
    "ds_read_b128 a[48:51], v252 offset:24576\n"
    "ds_read_b128 a[68:71], v252 offset:34816\n"
    "v_mfma_f32_16x16x32_bf16 v[32:35], a[140:143], v[184:187], v[32:35]\n"
    "ds_read_b128 a[76:79], v252 offset:27648\n"
    "ds_read_b128 a[72:75], v252 offset:25600\n"
    "v_mfma_f32_16x16x32_bf16 v[48:51], a[4:7], v[184:187], v[48:51]\n"
    "ds_read_b128 a[92:95], v252 offset:35840\n"
    "v_pk_maximum3_f16 v98, v182, v183, v98\n"
    "v_mfma_f32_16x16x32_bf16 v[64:67], a[32:35], v[188:191], v[64:67]\n"
    "v_pk_add_u16 v182, s89, v182 clamp\n"
    "v_pk_add_u16 v181, s89, v181 clamp\n"
    "v_mfma_f32_16x16x32_bf16 v[64:67], a[28:31], v[184:187], v[64:67]\n"
    "v_pk_add_u16 v183, s89, v183 clamp\n"
    "v_pk_add_u16 v180, s89, v180 clamp\n"
    "v_mfma_f32_16x16x32_bf16 v[16:19], a[116:119], v[184:187], v[16:19]\n"
    "buffer_load_dwordx4 v1, s[16:19], s67 offen offset:3072 sc1 lds\n"
    "v_mfma_f32_16x16x32_bf16 v[32:35], a[136:139], v[180:183], v[32:35]\n"
    "ds_read_b128 a[64:67], v252 offset:32768\n"
    "v_accvgpr_write_b32 a176, v188\n"
    "v_mfma_f32_16x16x32_bf16 v[16:19], a[112:115], v[180:183], v[16:19]\n"
    "v_accvgpr_write_b32 a175, v187\n"
    "s_waitcnt lgkmcnt(5)\n"
    "v_mfma_f32_16x16x32_bf16 v[12:15], a[48:51], a[240:243], v[100:103]\n"
    "s_waitcnt lgkmcnt(4)\n"
    "ds_read_b128 a[56:59], v252 offset:28672\n"
    "v_accvgpr_write_b32 a177, v189\n"
    "v_mfma_f32_16x16x32_bf16 v[48:51], a[0:3], v[180:183], v[48:51]\n"
    "s_add_u32 m0, s68, 4096\n"
    "ds_read_b128 a[80:83], v252 offset:29696\n"
    "ds_read_b128 a[88:91], v252 offset:33792\n"
    "v_mfma_f32_16x16x32_bf16 v[8:11], a[68:71], a[236:239], v[100:103]\n"
    "v_accvgpr_write_b32 a188, v200\n"
    "v_accvgpr_write_b32 a190, v202\n"
    "v_mfma_f32_16x16x32_bf16 v[4:7], a[48:51], a[192:195], v[100:103]\n"
    "s_waitcnt lgkmcnt(5)\n"
    "ds_read_b128 a[60:63], v252 offset:30720\n"
    "ds_read_b128 a[52:55], v252 offset:26624\n"
    "v_mfma_f32_16x16x32_bf16 v[20:23], a[72:75], a[192:195], v[104:107]\n"
    "buffer_load_dwordx4 v1, s[16:19], s83 offen offset:0 sc1 lds\n"
    "v_mfma_f32_16x16x32_bf16 v[28:31], a[72:75], a[240:243], v[104:107]\n"
    "s_waitcnt lgkmcnt(6)\n"
    "v_accvgpr_write_b32 a171, v183\n"
    "v_accvgpr_write_b32 a186, v198\n"
    "v_mfma_f32_16x16x32_bf16 v[24:27], a[92:95], a[236:239], v[104:107]\n"
    "v_exp_f32 v18, v18\n"
    "v_mfma_f32_16x16x32_bf16 v[64:67], a[24:27], v[180:183], v[64:67]\n"
    "v_accvgpr_write_b32 a174, v186\n"
    "v_accvgpr_write_b32 a178, v190\n"
    "v_mfma_f32_16x16x32_bf16 v[20:23], a[76:79], a[196:199], v[20:23]\n"
    "v_accvgpr_write_b32 a191, v203\n"
    "v_accvgpr_write_b32 a179, v191\n"
    "v_mfma_f32_16x16x32_bf16 v[28:31], a[76:79], a[244:247], v[28:31]\n"
    "s_waitcnt lgkmcnt(5)\n"
    "v_accvgpr_write_b32 a180, v192\n"
    "v_mfma_f32_16x16x32_bf16 v[8:11], a[64:67], a[232:235], v[8:11]\n"
    "s_waitcnt lgkmcnt(3)\n"
    "ds_read_b128 a[84:87], v252 offset:31744\n"
    "v_accvgpr_write_b32 a187, v199\n"
    "v_mfma_f32_16x16x32_bf16 v[28:31], a[80:83], a[248:251], v[28:31]\n"
    "v_accvgpr_write_b32 a169, v181\n"
    "v_accvgpr_write_b32 a173, v185\n"
    "v_mfma_f32_16x16x32_bf16 v[20:23], a[80:83], a[200:203], v[20:23]\n"
    "s_waitcnt lgkmcnt(1)\n"
    "buffer_load_dwordx4 v1, s[16:19], s83 offen offset:1024 sc1 lds\n"
    "v_mfma_f32_16x16x32_bf16 v[4:7], a[52:55], a[196:199], v[4:7]\n"
    "v_exp_f32 v16, v16\n"
    "v_mfma_f32_16x16x32_bf16 v[24:27], a[88:91], a[232:235], v[24:27]\n"
    "v_accvgpr_write_b32 a168, v180\n"
    "v_accvgpr_write_b32 a182, v194\n"
    "v_mfma_f32_16x16x32_bf16 v[8:11], a[60:63], a[228:231], v[8:11]\n"
    "v_accvgpr_write_b32 a181, v193\n"
    "v_accvgpr_write_b32 a189, v201\n"
    "v_mfma_f32_16x16x32_bf16 v[8:11], a[56:59], a[224:227], v[8:11]\n"
    "s_add_u32 m0, s64, 0\n"
    "v_accvgpr_write_b32 a170, v182\n"
    "v_accvgpr_write_b32 a172, v184\n"
    "v_mfma_f32_16x16x32_bf16 v[4:7], a[56:59], a[200:203], v[4:7]\n"
    "v_exp_f32 v32, v32\n"
    "v_mfma_f32_16x16x32_bf16 v[4:7], a[60:63], a[204:207], v[4:7]\n"
    "s_waitcnt lgkmcnt(0)\n"
    "v_accvgpr_write_b32 a183, v195\n"
    "v_accvgpr_write_b32 a185, v197\n"
    "v_mfma_f32_16x16x32_bf16 v[28:31], a[84:87], a[252:255], v[28:31]\n"
    "s_add_u32 s83, s67, 24576\n"
    "buffer_load_dwordx4 v1, s[16:19], s83 offen offset:0 sc1 lds\n"
    "v_mfma_f32_16x16x32_bf16 v[12:15], a[52:55], a[244:247], v[12:15]\n"
    "v_exp_f32 v17, v17\n"
    "v_mfma_f32_16x16x32_bf16 v[12:15], a[56:59], a[248:251], v[12:15]\n"
    "v_exp_f32 v19, v19\n"
    "v_mfma_f32_16x16x32_bf16 v[8:11], a[52:55], a[220:223], v[8:11]\n"
    "v_exp_f32 v35, v35\n"
    "v_mfma_f32_16x16x32_bf16 v[20:23], a[84:87], a[204:207], v[20:23]\n"
    "v_exp_f32 v51, v51\n"
    "v_mfma_f32_16x16x32_bf16 v[12:15], a[60:63], a[252:255], v[12:15]\n"
    "v_exp_f32 v65, v65\n"
    "v_mfma_f32_16x16x32_bf16 v[4:7], a[64:67], a[208:211], v[4:7]\n"
    "v_mfma_f32_16x16x32_bf16 v[12:15], a[64:67], a[160:163], v[12:15]\n"
    "buffer_load_dwordx4 v1, s[16:19], s83 offen offset:1024 sc1 lds\n"
    "v_mfma_f32_16x16x32_bf16 v[4:7], a[68:71], a[212:215], v[4:7]\n"
    "v_cvt_pk_bf16_f32 v93, v18, v19\n"
    "v_mfma_f32_16x16x32_bf16 v[24:27], a[84:87], a[228:231], v[24:27]\n"
    "v_exp_f32 v34, v34\n"
    "v_mfma_f32_16x16x32_bf16 v[8:11], a[48:51], a[216:219], v[8:11]\n"
    "v_exp_f32 v33, v33\n"
    "v_mfma_f32_16x16x32_bf16 v[12:15], a[68:71], a[164:167], v[12:15]\n"
    "v_cvt_pk_bf16_f32 v92, v16, v17\n"
    "v_mfma_f32_16x16x32_bf16 v[28:31], a[88:91], a[160:163], v[28:31]\n"
    "s_waitcnt vmcnt(8)\n"
    "v_exp_f32 v48, v48\n"
    "v_mfma_f32_16x16x32_bf16 v[16:19], a[68:71], a[188:191], v[100:103]\n"
    "s_barrier\n"
    "ds_read_b128 a[120:123], v248 offset:1024\n"
    "v_mfma_f32_16x16x32_bf16 v[24:27], a[80:83], a[224:227], v[24:27]\n"
    "buffer_load_dwordx4 v1, s[16:19], s83 offen offset:2048 sc1 lds\n"
    "v_mfma_f32_16x16x32_bf16 v[16:19], a[64:67], a[184:187], v[16:19]\n"
    "ds_read_b128 a[124:127], v248 offset:13312\n"
    "v_cvt_pk_bf16_f32 v94, v32, v33\n"
    "v_mfma_f32_16x16x32_bf16 v[16:19], a[60:63], a[180:183], v[16:19]\n"
    "ds_read_b128 a[136:139], v248 offset:3072\n"
    "ds_read_b128 a[128:131], v248 offset:2048\n"
    "v_mfma_f32_16x16x32_bf16 v[24:27], a[76:79], a[220:223], v[24:27]\n"
    "v_exp_f32 v50, v50\n"
    "v_mfma_f32_16x16x32_bf16 v[20:23], a[88:91], a[208:211], v[20:23]\n"
    "v_cvt_pk_bf16_f32 v97, v50, v51\n"
    "v_cvt_pk_bf16_f32 v95, v34, v35\n"
    "v_mfma_f32_16x16x32_bf16 v[32:35], a[92:95], a[188:191], v[104:107]\n"
    "v_exp_f32 v67, v67\n"
    "v_mfma_f32_16x16x32_bf16 v[16:19], a[56:59], a[176:179], v[16:19]\n"
    "v_mfma_f32_16x16x32_bf16 v[32:35], a[88:91], a[184:187], v[32:35]\n"
    "buffer_load_dwordx4 v1, s[16:19], s83 offen offset:3072 sc1 lds\n"
    "v_mfma_f32_16x16x32_bf16 v[20:23], a[92:95], a[212:215], v[20:23]\n"
    "v_pk_add_u16 v98, s89, v98\n"
    "s_waitcnt lgkmcnt(2)\n"
    "v_mfma_f32_16x16x32_bf16 v[160:163], a[124:127], v[88:91], 0\n"
    "ds_read_b128 a[132:135], v248 offset:14336\n"
    "s_waitcnt lgkmcnt(2)\n"
    "v_mfma_f32_16x16x32_bf16 v[136:139], a[136:139], v[68:71], 0\n"
    "ds_read_b128 a[144:147], v248 offset:4096\n"
    "v_mfma_f32_16x16x32_bf16 v[128:131], a[124:127], v[72:75], 0\n"
    "s_add_u32 s83, s67, 28672\n"
    "ds_read_b128 a[148:151], v248 offset:16384\n"
    "ds_read_b128 a[112:115], v248 offset:0\n"
    "v_mfma_f32_16x16x32_bf16 v[144:147], a[120:123], v[76:79], 0\n"
    "v_exp_f32 v64, v64\n"
    "v_mfma_f32_16x16x32_bf16 v[32:35], a[84:87], a[180:183], v[32:35]\n"
    "s_add_u32 m0, s64, 4096\n"
    "ds_read_b128 a[152:155], v248 offset:5120\n"
    "v_mfma_f32_16x16x32_bf16 v[16:19], a[52:55], a[172:175], v[16:19]\n"
    "buffer_load_dwordx4 v1, s[16:19], s83 offen offset:0 sc1 lds\n"
    "v_mfma_f32_16x16x32_bf16 v[128:131], a[120:123], v[68:71], v[128:131]\n"
    "v_exp_f32 v66, v66\n"
    "v_mfma_f32_16x16x32_bf16 v[176:179], a[120:123], v[92:95], 0\n"
    "s_waitcnt lgkmcnt(5)\n"
    "ds_read_b128 a[116:119], v248 offset:12288\n"
    "v_cvt_pk_bf16_f32 v99, v66, v67\n"
    "v_mfma_f32_16x16x32_bf16 v[132:135], a[128:131], v[68:71], 0\n"
    "v_exp_f32 v49, v49\n"
    "v_mfma_f32_16x16x32_bf16 v[28:31], a[92:95], a[164:167], v[28:31]\n"
    "s_waitcnt lgkmcnt(5)\n"
    "v_cvt_pk_bf16_f32 v96, v48, v49\n"
    "v_mfma_f32_16x16x32_bf16 v[148:151], a[132:135], v[80:83], 0\n"
    "v_and_b32 v98, 0x80008000, v98\n"
    "v_mfma_f32_16x16x32_bf16 v[164:167], a[128:131], v[84:87], 0\n"
    "v_exp_f32 v4, v4\n"
    "v_mfma_f32_16x16x32_bf16 v[24:27], a[72:75], a[216:219], v[24:27]\n"
    "buffer_load_dwordx4 v1, s[16:19], s83 offen offset:1024 sc1 lds\n"
    "v_mfma_f32_16x16x32_bf16 v[32:35], a[80:83], a[176:179], v[32:35]\n"
    "v_exp_f32 v12, v12\n"
    "v_mfma_f32_16x16x32_bf16 v[144:147], a[124:127], v[80:83], v[144:147]\n"
    "v_exp_f32 v31, v31\n"
    "v_mfma_f32_16x16x32_bf16 v[160:163], a[120:123], v[84:87], v[160:163]\n"
    "s_waitcnt lgkmcnt(2)\n"
    "v_mfma_f32_16x16x32_bf16 v[124:127], a[112:115], v[68:71], 0\n"
    "v_exp_f32 v27, v27\n"
    "v_mfma_f32_16x16x32_bf16 v[188:191], a[148:151], v[72:75], 0\n"
    "ds_read_b128 a[140:143], v248 offset:15360\n"
    "v_mfma_f32_16x16x32_bf16 v[16:19], a[48:51], a[168:171], v[16:19]\n"
    "s_waitcnt lgkmcnt(2)\n"
    "ds_read_b128 a[120:123], v248 offset:7168\n"
    "v_cmp_ne_u32 s[34:35], 0, v98\n"
    "v_mfma_f32_16x16x32_bf16 a[20:23], a[152:155], v[84:87], 0\n"
    "s_waitcnt lgkmcnt(2)\n"
    "v_exp_f32 v29, v29\n"
    "v_mfma_f32_16x16x32_bf16 v[140:143], a[116:119], v[80:83], 0\n"
    "buffer_load_dwordx4 v[204:207], v242, s[40:43], s75 offen offset:0 nt\n"
    "v_mfma_f32_16x16x32_bf16 v[188:191], a[144:147], v[68:71], v[188:191]\n"
    "v_cvt_pk_bf16_f32 v98, v64, v65\n"
    "v_mfma_f32_16x16x32_bf16 v[124:127], a[116:119], v[72:75], v[124:127]\n"
    "ds_read_b128 a[156:159], v248 offset:17408\n"
    "v_mfma_f32_16x16x32_bf16 v[132:135], a[132:135], v[72:75], v[132:135]\n"
    "v_exp_f32 v5, v5\n"
    "v_mfma_f32_16x16x32_bf16 v[192:195], a[152:155], v[68:71], 0\n"
    "buffer_load_dwordx4 v[208:211], v242, s[40:43], s75 offen offset:768 nt\n"
    "v_mfma_f32_16x16x32_bf16 v[176:179], a[124:127], v[96:99], v[176:179]\n"
    "ds_read_b128 a[124:127], v248 offset:19456\n"
    "v_mfma_f32_16x16x32_bf16 v[140:143], a[112:115], v[76:79], v[140:143]\n"
    "v_exp_f32 v26, v26\n"
    "v_mfma_f32_16x16x32_bf16 v[172:175], a[116:119], v[96:99], 0\n"
    "s_waitcnt lgkmcnt(2)\n"
    "buffer_load_dwordx4 v[212:215], v242, s[40:43], s75 offen offset:256 nt\n"
    "v_mfma_f32_16x16x32_bf16 a[12:15], a[120:123], v[76:79], 0\n"
    "v_exp_f32 v20, v20\n"
    "v_mfma_f32_16x16x32_bf16 a[0:3], a[144:147], v[76:79], 0\n"
    "v_mfma_f32_16x16x32_bf16 v[152:155], a[140:143], v[80:83], 0\n"
    "v_mfma_f32_16x16x32_bf16 v[172:175], a[112:115], v[92:95], v[172:175]\n"
    "v_exp_f32 v16, v16\n"
    "v_mfma_f32_16x16x32_bf16 a[16:19], a[148:151], v[88:91], 0\n"
    "v_exp_f32 v17, v17\n"
    "v_mfma_f32_16x16x32_bf16 v[164:167], a[132:135], v[88:91], v[164:167]\n"
    "v_exp_f32 v24, v24\n"
    "v_mfma_f32_16x16x32_bf16 v[152:155], a[136:139], v[76:79], v[152:155]\n"
    "buffer_load_dwordx4 v[216:219], v242, s[40:43], s75 offen offset:1024 nt\n"
    "v_mfma_f32_16x16x32_bf16 v[180:183], a[132:135], v[96:99], 0\n"
    "v_exp_f32 v19, v19\n"
    "v_mfma_f32_16x16x32_bf16 v[156:159], a[112:115], v[84:87], 0\n"
    "v_exp_f32 v14, v14\n"
    "v_mfma_f32_16x16x32_bf16 a[44:47], a[120:123], v[92:95], 0\n"
    "v_exp_f32 v9, v9\n"
    "v_mfma_f32_16x16x32_bf16 a[32:35], a[144:147], v[92:95], 0\n"
    "s_waitcnt lgkmcnt(1)\n"
    "buffer_load_dwordx4 v[220:223], v242, s[40:43], s75 offen offset:512 nt\n"
    "v_mfma_f32_16x16x32_bf16 a[36:39], a[156:159], v[96:99], 0\n"
    "v_mfma_f32_16x16x32_bf16 a[0:3], a[148:151], v[80:83], a[0:3]\n"
    "buffer_load_dwordx4 v[224:227], v242, s[40:43], s75 offen offset:1280 nt\n"
    "v_mfma_f32_16x16x32_bf16 a[32:35], a[148:151], v[96:99], a[32:35]\n"
    "ds_read_b128 a[132:135], v248 offset:20480\n"
    "v_mfma_f32_16x16x32_bf16 v[148:151], a[128:131], v[76:79], v[148:151]\n"
    "v_exp_f32 v25, v25\n"
    "v_mfma_f32_16x16x32_bf16 a[4:7], a[156:159], v[80:83], 0\n"
    "ds_read_b128 a[112:115], v248 offset:6144\n"
    "v_mfma_f32_16x16x32_bf16 v[180:183], a[128:131], v[92:95], v[180:183]\n"
    "ds_read_b128 a[148:151], v248 offset:22528\n"
    "v_mfma_f32_16x16x32_bf16 v[184:187], a[140:143], v[96:99], 0\n"
    "v_mfma_f32_16x16x32_bf16 v[156:159], a[116:119], v[88:91], v[156:159]\n"
    "v_exp_f32 v10, v10\n"
    "v_mfma_f32_16x16x32_bf16 v[136:139], a[140:143], v[72:75], v[136:139]\n"
    "s_waitcnt lgkmcnt(3)\n"
    "v_mfma_f32_16x16x32_bf16 v[200:203], a[124:127], v[72:75], 0\n"
    "ds_read_b128 a[116:119], v248 offset:18432\n"
    "ds_read_b128 a[128:131], v248 offset:8192\n"
    "v_mfma_f32_16x16x32_bf16 a[20:23], a[156:159], v[88:91], a[20:23]\n"
    "v_exp_f32 v7, v7\n"
    "v_mfma_f32_16x16x32_bf16 a[4:7], a[152:155], v[76:79], a[4:7]\n"
    "v_mfma_f32_16x16x32_bf16 v[192:195], a[156:159], v[72:75], v[192:195]\n"
    "v_exp_f32 v21, v21\n"
    "v_mfma_f32_16x16x32_bf16 a[36:39], a[152:155], v[92:95], a[36:39]\n"
    "ds_read_b128 a[156:159], v248 offset:23552\n"
    "v_mfma_f32_16x16x32_bf16 v[184:187], a[136:139], v[92:95], v[184:187]\n"
    "s_waitcnt lgkmcnt(5)\n"
    "v_mfma_f32_16x16x32_bf16 a[64:67], a[132:135], v[80:83], 0\n"
    "v_mfma_f32_16x16x32_bf16 v[168:171], a[136:139], v[84:87], 0\n"
    "s_waitcnt lgkmcnt(3)\n"
    "v_mfma_f32_16x16x32_bf16 a[88:91], a[148:151], v[88:91], 0\n"
    "v_mfma_f32_16x16x32_bf16 a[16:19], a[144:147], v[84:87], a[16:19]\n"
    "s_waitcnt lgkmcnt(2)\n"
    "ds_read_b128 a[152:155], v248 offset:11264\n"
    "v_mfma_f32_16x16x32_bf16 a[8:11], a[116:119], v[80:83], 0\n"
    "v_mfma_f32_16x16x32_bf16 v[168:171], a[140:143], v[88:91], v[168:171]\n"
    "v_exp_f32 v15, v15\n"
    "v_mfma_f32_16x16x32_bf16 a[28:31], a[124:127], v[88:91], 0\n"
    "s_waitcnt lgkmcnt(2)\n"
    "v_exp_f32 v11, v11\n"
    "v_mfma_f32_16x16x32_bf16 a[48:51], a[128:131], v[68:71], 0\n"
    "v_mfma_f32_16x16x32_bf16 a[56:59], a[148:151], v[72:75], 0\n"
    "v_mfma_f32_16x16x32_bf16 a[28:31], a[120:123], v[84:87], a[28:31]\n"
    "v_mfma_f32_16x16x32_bf16 a[8:11], a[112:115], v[76:79], a[8:11]\n"
    "buffer_load_dwordx2 v[236:237], v241, s[36:39], s81 offen offset:0 sc1\n"
    "v_mfma_f32_16x16x32_bf16 v[200:203], a[120:123], v[68:71], v[200:203]\n"
    "v_exp_f32 v28, v28\n"
    "v_mfma_f32_16x16x32_bf16 v[196:199], a[112:115], v[68:71], 0\n"
    "s_waitcnt lgkmcnt(1)\n"
    "v_mfma_f32_16x16x32_bf16 a[108:111], a[156:159], v[96:99], 0\n"
    "v_mfma_f32_16x16x32_bf16 v[32:35], a[76:79], a[172:175], v[32:35]\n"
    "ds_read_b128 a[136:139], v248 offset:9216\n"
    "ds_read_b128 a[140:143], v248 offset:21504\n"
    "v_mfma_f32_16x16x32_bf16 a[80:83], a[128:131], v[84:87], 0\n"
    "v_mfma_f32_16x16x32_bf16 a[24:27], a[112:115], v[84:87], 0\n"
    "v_mfma_f32_16x16x32_bf16 a[12:15], a[124:127], v[80:83], a[12:15]\n"
    "v_exp_f32 v23, v23\n"
    "v_mfma_f32_16x16x32_bf16 v[32:35], a[72:75], a[168:171], v[32:35]\n"
    "ds_read_b128 a[120:123], v3 offset:16384\n"
    "ds_read_b128 a[144:147], v248 offset:10240\n"
    "v_mfma_f32_16x16x32_bf16 a[40:43], a[116:119], v[96:99], 0\n"
    "v_exp_f32 v22, v22\n"
    "v_mfma_f32_16x16x32_bf16 a[80:83], a[132:135], v[88:91], a[80:83]\n"
    "v_mfma_f32_16x16x32_bf16 a[76:79], a[156:159], v[80:83], 0\n"
    "v_mfma_f32_16x16x32_bf16 a[40:43], a[112:115], v[92:95], a[40:43]\n"
    "s_waitcnt lgkmcnt(3)\n"
    "v_exp_f32 v32, v32\n"
    "v_mfma_f32_16x16x32_bf16 a[52:55], a[136:139], v[68:71], 0\n"
    "v_exp_f32 v33, v33\n"
    "v_mfma_f32_16x16x32_bf16 a[48:51], a[132:135], v[72:75], a[48:51]\n"
    "v_mfma_f32_16x16x32_bf16 a[92:95], a[152:155], v[84:87], 0\n"
    "s_waitcnt lgkmcnt(2)\n"
    "v_mfma_f32_16x16x32_bf16 a[68:71], a[140:143], v[80:83], 0\n"
    "v_mfma_f32_16x16x32_bf16 a[100:103], a[140:143], v[96:99], 0\n"
    "s_waitcnt lgkmcnt(0)\n"
    "ds_read_b128 v[100:103], v250 offset:896\n"
    "v_mfma_f32_16x16x32_bf16 a[88:91], a[144:147], v[84:87], a[88:91]\n"
    "v_mfma_f32_16x16x32_bf16 a[24:27], a[116:119], v[88:91], a[24:27]\n"
    "v_mfma_f32_16x16x32_bf16 a[84:87], a[136:139], v[84:87], 0\n"
    "ds_read_b128 a[112:115], v3 offset:12288\n"
    "v_mfma_f32_16x16x32_bf16 a[96:99], a[132:135], v[96:99], 0\n"
    "v_exp_f32 v34, v34\n"
    "v_mfma_f32_16x16x32_bf16 v[196:199], a[116:119], v[72:75], v[196:199]\n"
    "v_exp_f32 v6, v6\n"
    "v_mfma_f32_16x16x32_bf16 a[44:47], a[124:127], v[96:99], a[44:47]\n"
    "v_mfma_f32_16x16x32_bf16 a[108:111], a[152:155], v[92:95], a[108:111]\n"
    "v_exp_f32 v8, v8\n"
    "v_mfma_f32_16x16x32_bf16 a[76:79], a[152:155], v[76:79], a[76:79]\n"
    "v_mfma_f32_16x16x32_bf16 a[104:107], a[144:147], v[92:95], 0\n"
    "s_add_u32 s67, s67, 49152\n"
    "v_mfma_f32_16x16x32_bf16 a[56:59], a[144:147], v[68:71], a[56:59]\n"
    "v_exp_f32 v30, v30\n"
    "v_mfma_f32_16x16x32_bf16 a[96:99], a[128:131], v[92:95], a[96:99]\n"
    "ds_read_b128 a[124:127], v3 offset:18432\n"
    "v_mfma_f32_16x16x32_bf16 a[72:75], a[144:147], v[76:79], 0\n"
    "ds_read_b128 a[132:135], v3 offset:22528\n"
    "s_waitcnt vmcnt(7)\n"
    "v_mfma_f32_16x16x32_bf16 a[104:107], a[148:151], v[96:99], a[104:107]\n"
    "ds_add_u32 v247, v246\n"
    "v_mfma_f32_16x16x32_bf16 a[64:67], a[128:131], v[76:79], a[64:67]\n"
    "ds_read_b128 a[116:119], v3 offset:14336\n"
    "ds_read_b128 a[128:131], v3 offset:20480\n"
    "v_smfmac_f32_16x16x64_bf16 v[120:123], v[52:55], v[92:99], v253\n"
    "v_add_u32 v3, 49152, v252\n"
    "v_mfma_f32_16x16x32_bf16 a[60:63], a[152:155], v[68:71], 0\n"
    "v_mfma_f32_16x16x32_bf16 a[52:55], a[140:143], v[72:75], a[52:55]\n"
    "v_mfma_f32_16x16x32_bf16 a[84:87], a[140:143], v[88:91], a[84:87]\n"
    "v_exp_f32 v13, v13\n"
    "v_mfma_f32_16x16x32_bf16 a[60:63], a[156:159], v[72:75], a[60:63]\n"
    "v_exp_f32 v35, v35\n"
    "v_mfma_f32_16x16x32_bf16 a[92:95], a[156:159], v[88:91], a[92:95]\n"
    "v_exp_f32 v18, v18\n"
    "v_mfma_f32_16x16x32_bf16 a[72:75], a[148:151], v[80:83], a[72:75]\n"
    "v_mfma_f32_16x16x32_bf16 a[100:103], a[136:139], v[92:95], a[100:103]\n"
    "s_waitcnt lgkmcnt(0)\n"
    "v_mfma_f32_16x16x32_bf16 a[68:71], a[136:139], v[76:79], a[68:71]\n"
    "s_branch .LNV\n"
    ".fill ((0x20000 - (. - _Z8attn_fwdPKtPKiS2_S0_S0_S0_S0_PcPt)) & 4095) / 4, 4, 0xBF800000\n"
    ".LhVN:\n"
    "s_waitcnt vmcnt(18)\n"
    "ds_read_b128 v[52:55], v251 offset:768\n"
    "ds_read_b128 v[184:187], v97 offset:49152\n"
    "ds_read_b128 v[196:199], v96 offset:49408\n"
    "ds_read_b128 v[200:203], v97 offset:49408\n"
    "ds_read_b128 v[188:191], v96 offset:49280\n"
    "ds_read_b128 v[180:183], v96 offset:49152\n"
    "v_mov_b32 v2, v3\n"
    "ds_read_b128 v[192:195], v97 offset:49280\n"
    "s_add_u32 m0, s68, .Lend-.Lend2+(0)\n"
    "s_waitcnt lgkmcnt(6)\n"
    "v_smfmac_f32_16x16x64_bf16 v[108:111], v[52:55], v[68:75], v253\n"
    "s_waitcnt lgkmcnt(3)\n"
    "v_pk_maximum3_f16 v98, v200, v201, v98\n"
    "v_pk_maximum3_f16 v98, v202, v203, v98\n"
    "v_pk_add_u16 v200, s89, v200 clamp\n"
    "v_smfmac_f32_16x16x64_bf16 v[116:119], v[52:55], v[84:91], v253\n"
    "s_add_u32 s83, s67, 4096\n"
    "v_pk_add_u16 v201, s89, v201 clamp\n"
    "v_pk_add_u16 v202, s89, v202 clamp\n"
    "v_pk_add_u16 v203, s89, v203 clamp\n"
    "v_pk_maximum3_f16 v98, v196, v197, v98\n"
    "v_pk_maximum3_f16 v98, v198, v199, v98\n"
    "v_mfma_f32_16x16x32_bf16 v[48:51], a[20:23], v[200:203], v[48:51]\n"
    "v_pk_add_u16 v197, s89, v197 clamp\n"
    "v_pk_add_u16 v198, s89, v198 clamp\n"
    "v_mfma_f32_16x16x32_bf16 v[64:67], a[44:47], v[200:203], v[64:67]\n"
    "buffer_load_dwordx4 v1, s[16:19], s67 offen offset:0 sc1 lds\n"
    "v_mfma_f32_16x16x32_bf16 v[16:19], a[132:135], v[200:203], v[16:19]\n"
    "v_pk_add_u16 v199, s89, v199 clamp\n"
    "v_pk_add_u16 v196, s89, v196 clamp\n"
    "v_mfma_f32_16x16x32_bf16 v[32:35], a[156:159], v[200:203], v[32:35]\n"
    "s_waitcnt lgkmcnt(0)\n"
    "v_pk_maximum3_f16 v98, v192, v193, v98\n"
    "v_pk_maximum3_f16 v98, v194, v195, v98\n"
    "v_pk_add_u16 v194, s89, v194 clamp\n"
    "v_mfma_f32_16x16x32_bf16 v[32:35], a[152:155], v[196:199], v[32:35]\n"
    "v_pk_add_u16 v192, s89, v192 clamp\n"
    "v_pk_add_u16 v193, s89, v193 clamp\n"
    "v_mfma_f32_16x16x32_bf16 v[64:67], a[40:43], v[196:199], v[64:67]\n"
    "v_pk_maximum3_f16 v98, v188, v189, v98\n"
    "v_pk_add_u16 v195, s89, v195 clamp\n"
    "v_mfma_f32_16x16x32_bf16 v[16:19], a[128:131], v[196:199], v[16:19]\n"
    "v_pk_add_u16 v188, s89, v188 clamp\n"
    "v_pk_maximum3_f16 v98, v190, v191, v98\n"
    "v_mfma_f32_16x16x32_bf16 v[32:35], a[148:151], v[192:195], v[32:35]\n"
    "buffer_load_dwordx4 v1, s[16:19], s67 offen offset:1024 sc1 lds\n"
    "v_mfma_f32_16x16x32_bf16 v[64:67], a[36:39], v[192:195], v[64:67]\n"
    "v_pk_maximum3_f16 v98, v184, v185, v98\n"
    "v_pk_add_u16 v189, s89, v189 clamp\n"
    "v_mfma_f32_16x16x32_bf16 v[48:51], a[16:19], v[196:199], v[48:51]\n"
    "v_pk_add_u16 v185, s89, v185 clamp\n"
    "v_pk_add_u16 v191, s89, v191 clamp\n"
    "v_pk_add_u16 v190, s89, v190 clamp\n"
    "v_mfma_f32_16x16x32_bf16 v[48:51], a[12:15], v[192:195], v[48:51]\n"
    "v_pk_maximum3_f16 v98, v186, v187, v98\n"
    "v_pk_add_u16 v186, s89, v186 clamp\n"
    "v_mfma_f32_16x16x32_bf16 v[16:19], a[124:127], v[192:195], v[16:19]\n"
    "v_pk_maximum3_f16 v98, v180, v181, v98\n"
    "v_pk_add_u16 v187, s89, v187 clamp\n"
    "v_mfma_f32_16x16x32_bf16 v[16:19], a[120:123], v[188:191], v[16:19]\n"
    "v_pk_add_u16 v180, s89, v180 clamp\n"
    "v_pk_add_u16 v184, s89, v184 clamp\n"
    "v_mfma_f32_16x16x32_bf16 v[64:67], a[32:35], v[188:191], v[64:67]\n"
    "v_accvgpr_write_b32 a177, v189\n"
    "v_accvgpr_write_b32 a190, v202\n"
    "v_mfma_f32_16x16x32_bf16 v[32:35], a[144:147], v[188:191], v[32:35]\n"
    "s_waitcnt vmcnt(14)\n"
    "buffer_load_dwordx4 v1, s[16:19], s67 offen offset:2048 sc1 lds\n"
    "v_smfmac_f32_16x16x64_bf16 v[112:115], v[52:55], v[76:83], v253\n"
    "s_barrier\n"
    "ds_read_b128 a[48:51], v252 offset:24576\n"
    "v_mfma_f32_16x16x32_bf16 v[48:51], a[8:11], v[188:191], v[48:51]\n"
    "ds_read_b128 a[68:71], v252 offset:34816\n"
    "ds_read_b128 a[72:75], v252 offset:25600\n"
    "v_mfma_f32_16x16x32_bf16 v[32:35], a[140:143], v[184:187], v[32:35]\n"
    "v_pk_add_u16 v181, s89, v181 clamp\n"
    "v_accvgpr_write_b32 a188, v200\n"
    "v_mfma_f32_16x16x32_bf16 v[48:51], a[4:7], v[184:187], v[48:51]\n"
    "v_pk_maximum3_f16 v98, v182, v183, v98\n"
    "v_accvgpr_write_b32 a176, v188\n"
    "v_mfma_f32_16x16x32_bf16 v[64:67], a[28:31], v[184:187], v[64:67]\n"
    "v_pk_add_u16 v183, s89, v183 clamp\n"
    "v_pk_add_u16 v182, s89, v182 clamp\n"
    "v_mfma_f32_16x16x32_bf16 v[16:19], a[116:119], v[184:187], v[16:19]\n"
    "ds_read_b128 a[52:55], v252 offset:26624\n"
    "ds_read_b128 a[76:79], v252 offset:27648\n"
    "v_mfma_f32_16x16x32_bf16 v[48:51], a[0:3], v[180:183], v[48:51]\n"
    "buffer_load_dwordx4 v1, s[16:19], s67 offen offset:3072 sc1 lds\n"
    "v_mfma_f32_16x16x32_bf16 v[32:35], a[136:139], v[180:183], v[32:35]\n"
    "ds_read_b128 a[92:95], v252 offset:35840\n"
    "ds_read_b128 a[88:91], v252 offset:33792\n"
    "v_mfma_f32_16x16x32_bf16 v[16:19], a[112:115], v[180:183], v[16:19]\n"
    "s_waitcnt lgkmcnt(6)\n"
    "v_accvgpr_write_b32 a180, v192\n"
    "v_accvgpr_write_b32 a181, v193\n"
    "v_mfma_f32_16x16x32_bf16 v[4:7], a[48:51], a[192:195], v[100:103]\n"
    "s_waitcnt lgkmcnt(4)\n"
    "ds_read_b128 a[60:63], v252 offset:30720\n"
    "ds_read_b128 a[56:59], v252 offset:28672\n"
    "v_mfma_f32_16x16x32_bf16 v[20:23], a[72:75], a[192:195], v[104:107]\n"
    "ds_read_b128 a[64:67], v252 offset:32768\n"
    "v_accvgpr_write_b32 a191, v203\n"
    "v_mfma_f32_16x16x32_bf16 v[28:31], a[72:75], a[240:243], v[104:107]\n"
    "s_add_u32 m0, s68, 4096\n"
    "v_accvgpr_write_b32 a175, v187\n"
    "v_accvgpr_write_b32 a182, v194\n"
    "v_mfma_f32_16x16x32_bf16 v[12:15], a[48:51], a[240:243], v[100:103]\n"
    "v_accvgpr_write_b32 a178, v190\n"
    "v_accvgpr_write_b32 a171, v183\n"
    "v_mfma_f32_16x16x32_bf16 v[8:11], a[68:71], a[236:239], v[100:103]\n"
    "buffer_load_dwordx4 v1, s[16:19], s83 offen offset:0 sc1 lds\n"
    "v_mfma_f32_16x16x32_bf16 v[64:67], a[24:27], v[180:183], v[64:67]\n"
    "s_waitcnt lgkmcnt(6)\n"
    "ds_read_b128 a[80:83], v252 offset:29696\n"
    "v_accvgpr_write_b32 a172, v184\n"
    "v_mfma_f32_16x16x32_bf16 v[4:7], a[52:55], a[196:199], v[4:7]\n"
    "s_waitcnt lgkmcnt(5)\n"
    "v_exp_f32 v16, v16\n"
    "v_mfma_f32_16x16x32_bf16 v[24:27], a[92:95], a[236:239], v[104:107]\n"
    "s_waitcnt lgkmcnt(4)\n"
    "v_accvgpr_write_b32 a174, v186\n"
    "v_accvgpr_write_b32 a169, v181\n"
    "v_mfma_f32_16x16x32_bf16 v[24:27], a[88:91], a[232:235], v[24:27]\n"
    "s_waitcnt lgkmcnt(2)\n"
    "ds_read_b128 a[84:87], v252 offset:31744\n"
    "v_mfma_f32_16x16x32_bf16 v[4:7], a[56:59], a[200:203], v[4:7]\n"
    "s_waitcnt lgkmcnt(2)\n"
    "v_accvgpr_write_b32 a189, v201\n"
    "v_accvgpr_write_b32 a179, v191\n"
    "v_mfma_f32_16x16x32_bf16 v[8:11], a[64:67], a[232:235], v[8:11]\n"
    "v_exp_f32 v17, v17\n"
    "v_mfma_f32_16x16x32_bf16 v[28:31], a[76:79], a[244:247], v[28:31]\n"
    "buffer_load_dwordx4 v1, s[16:19], s83 offen offset:1024 sc1 lds\n"
    "v_mfma_f32_16x16x32_bf16 v[20:23], a[76:79], a[196:199], v[20:23]\n"
    "v_accvgpr_write_b32 a184, v196\n"
    "v_accvgpr_write_b32 a170, v182\n"
    "v_mfma_f32_16x16x32_bf16 v[12:15], a[52:55], a[244:247], v[12:15]\n"
    "v_accvgpr_write_b32 a185, v197\n"
    "v_accvgpr_write_b32 a186, v198\n"
    "v_mfma_f32_16x16x32_bf16 v[12:15], a[56:59], a[248:251], v[12:15]\n"
    "v_cvt_pk_bf16_f32 v92, v16, v17\n"
    "v_mfma_f32_16x16x32_bf16 v[12:15], a[60:63], a[252:255], v[12:15]\n"
    "s_add_u32 m0, s70, 0\n"
    "s_add_u32 s83, s67, 24576\n"
    "v_accvgpr_write_b32 a168, v180\n"
    "v_mfma_f32_16x16x32_bf16 v[12:15], a[64:67], a[160:163], v[12:15]\n"
    "s_waitcnt lgkmcnt(1)\n"
    "v_accvgpr_write_b32 a183, v195\n"
    "v_pk_add_u16 v98, s89, v98\n"
    "v_mfma_f32_16x16x32_bf16 v[28:31], a[80:83], a[248:251], v[28:31]\n"
    "s_waitcnt lgkmcnt(0)\n"
    "v_accvgpr_write_b32 a173, v185\n"
    "v_accvgpr_write_b32 a187, v199\n"
    "v_mfma_f32_16x16x32_bf16 v[28:31], a[84:87], a[252:255], v[28:31]\n"
    "buffer_load_dwordx4 v1, s[16:19], s83 offen offset:0 sc1 lds\n"
    "v_mfma_f32_16x16x32_bf16 v[4:7], a[60:63], a[204:207], v[4:7]\n"
    "v_exp_f32 v18, v18\n"
    "v_mfma_f32_16x16x32_bf16 v[8:11], a[60:63], a[228:231], v[8:11]\n"
    "v_exp_f32 v19, v19\n"
    "v_mfma_f32_16x16x32_bf16 v[8:11], a[56:59], a[224:227], v[8:11]\n"
    "v_exp_f32 v32, v32\n"
    "v_mfma_f32_16x16x32_bf16 v[8:11], a[52:55], a[220:223], v[8:11]\n"
    "v_exp_f32 v35, v35\n"
    "v_mfma_f32_16x16x32_bf16 v[28:31], a[88:91], a[160:163], v[28:31]\n"
    "v_cvt_pk_bf16_f32 v93, v18, v19\n"
    "v_mfma_f32_16x16x32_bf16 v[24:27], a[84:87], a[228:231], v[24:27]\n"
    "v_exp_f32 v34, v34\n"
    "v_mfma_f32_16x16x32_bf16 v[20:23], a[80:83], a[200:203], v[20:23]\n"
    "v_exp_f32 v67, v67\n"
    "v_mfma_f32_16x16x32_bf16 v[16:19], a[68:71], a[188:191], v[100:103]\n"
    "buffer_load_dwordx4 v1, s[16:19], s83 offen offset:1024 sc1 lds\n"
    "v_mfma_f32_16x16x32_bf16 v[4:7], a[64:67], a[208:211], v[4:7]\n"
    "v_exp_f32 v33, v33\n"
    "v_mfma_f32_16x16x32_bf16 v[20:23], a[84:87], a[204:207], v[20:23]\n"
    "v_mfma_f32_16x16x32_bf16 v[20:23], a[88:91], a[208:211], v[20:23]\n"
    "v_mfma_f32_16x16x32_bf16 v[24:27], a[80:83], a[224:227], v[24:27]\n"
    "v_cvt_pk_bf16_f32 v95, v34, v35\n"
    "v_cvt_pk_bf16_f32 v94, v32, v33\n"
    "v_mfma_f32_16x16x32_bf16 v[32:35], a[92:95], a[188:191], v[104:107]\n"
    "s_waitcnt vmcnt(14)\n"
    "v_exp_f32 v66, v66\n"
    "v_mfma_f32_16x16x32_bf16 v[16:19], a[64:67], a[184:187], v[16:19]\n"
    "s_barrier\n"
    "ds_read_b128 a[112:115], v249 offset:0\n"
    "v_mfma_f32_16x16x32_bf16 v[32:35], a[88:91], a[184:187], v[32:35]\n"
    "v_exp_f32 v51, v51\n"
    "v_mfma_f32_16x16x32_bf16 v[32:35], a[84:87], a[180:183], v[32:35]\n"
    "buffer_load_dwordx4 v1, s[16:19], s83 offen offset:2048 sc1 lds\n"
    "v_mfma_f32_16x16x32_bf16 v[16:19], a[60:63], a[180:183], v[16:19]\n"
    "ds_read_b128 a[144:147], v249 offset:4096\n"
    "ds_read_b128 a[140:143], v249 offset:15360\n"
    "v_mfma_f32_16x16x32_bf16 v[16:19], a[56:59], a[176:179], v[16:19]\n"
    "ds_read_b128 a[152:155], v249 offset:5120\n"
    "ds_read_b128 a[132:135], v249 offset:14336\n"
    "v_mfma_f32_16x16x32_bf16 v[4:7], a[68:71], a[212:215], v[4:7]\n"
    "ds_read_b128 a[120:123], v249 offset:1024\n"
    "ds_read_b128 a[128:131], v249 offset:2048\n"
    "v_mfma_f32_16x16x32_bf16 v[8:11], a[48:51], a[216:219], v[8:11]\n"
    "ds_read_b128 a[124:127], v249 offset:13312\n"
    "ds_read_b128 a[136:139], v249 offset:3072\n"
    "v_mfma_f32_16x16x32_bf16 v[24:27], a[76:79], a[220:223], v[24:27]\n"
    "v_exp_f32 v65, v65\n"
    "v_mfma_f32_16x16x32_bf16 v[12:15], a[68:71], a[164:167], v[12:15]\n"
    "ds_read_b128 a[116:119], v249 offset:12288\n"
    "v_mfma_f32_16x16x32_bf16 v[32:35], a[80:83], a[176:179], v[32:35]\n"
    "buffer_load_dwordx4 v1, s[16:19], s83 offen offset:3072 sc1 lds\n"
    "v_mfma_f32_16x16x32_bf16 v[20:23], a[92:95], a[212:215], v[20:23]\n"
    "v_mfma_f32_16x16x32_bf16 v[16:19], a[52:55], a[172:175], v[16:19]\n"
    "s_waitcnt lgkmcnt(7)\n"
    "v_exp_f32 v50, v50\n"
    "v_mfma_f32_16x16x32_bf16 v[152:155], a[140:143], v[80:83], 0\n"
    "v_and_b32 v98, 0x80008000, v98\n"
    "v_mfma_f32_16x16x32_bf16 v[24:27], a[72:75], a[216:219], v[24:27]\n"
    "s_add_u32 m0, s70, 4096\n"
    "s_add_u32 s83, s67, 28672\n"
    "s_waitcnt lgkmcnt(2)\n"
    "v_mfma_f32_16x16x32_bf16 v[128:131], a[124:127], v[72:75], 0\n"
    "ds_read_b128 a[148:151], v249 offset:16384\n"
    "v_mfma_f32_16x16x32_bf16 v[148:151], a[132:135], v[80:83], 0\n"
    "s_add_u32 s76, s52, s60\n"
    "v_mfma_f32_16x16x32_bf16 v[28:31], a[92:95], a[164:167], v[28:31]\n"
    "buffer_load_dwordx4 v1, s[16:19], s83 offen offset:0 sc1 lds\n"
    "v_mfma_f32_16x16x32_bf16 v[160:163], a[124:127], v[88:91], 0\n"
    "s_waitcnt lgkmcnt(2)\n"
    "v_exp_f32 v48, v48\n"
    "v_mfma_f32_16x16x32_bf16 v[136:139], a[136:139], v[68:71], 0\n"
    "v_exp_f32 v49, v49\n"
    "v_mfma_f32_16x16x32_bf16 a[32:35], a[144:147], v[92:95], 0\n"
    "v_exp_f32 v27, v27\n"
    "v_mfma_f32_16x16x32_bf16 v[136:139], a[140:143], v[72:75], v[136:139]\n"
    "v_cmp_ne_u32 s[34:35], 0, v98\n"
    "v_mfma_f32_16x16x32_bf16 v[156:159], a[112:115], v[84:87], 0\n"
    "v_mfma_f32_16x16x32_bf16 v[132:135], a[128:131], v[68:71], 0\n"
    "v_cvt_pk_bf16_f32 v96, v48, v49\n"
    "v_mfma_f32_16x16x32_bf16 v[144:147], a[120:123], v[76:79], 0\n"
    "buffer_load_dwordx4 v1, s[16:19], s83 offen offset:1024 sc1 lds\n"
    "v_mfma_f32_16x16x32_bf16 v[16:19], a[48:51], a[168:171], v[16:19]\n"
    "s_waitcnt lgkmcnt(0)\n"
    "v_exp_f32 v20, v20\n"
    "v_mfma_f32_16x16x32_bf16 a[16:19], a[148:151], v[88:91], 0\n"
    "v_exp_f32 v64, v64\n"
    "v_mfma_f32_16x16x32_bf16 v[176:179], a[120:123], v[92:95], 0\n"
    "v_mfma_f32_16x16x32_bf16 v[124:127], a[112:115], v[68:71], 0\n"
    "v_exp_f32 v4, v4\n"
    "v_mfma_f32_16x16x32_bf16 v[156:159], a[116:119], v[88:91], v[156:159]\n"
    "v_exp_f32 v7, v7\n"
    "v_mfma_f32_16x16x32_bf16 v[188:191], a[148:151], v[72:75], 0\n"
    "v_exp_f32 v10, v10\n"
    "v_mfma_f32_16x16x32_bf16 v[32:35], a[76:79], a[172:175], v[32:35]\n"
    "v_mfma_f32_16x16x32_bf16 v[192:195], a[152:155], v[68:71], 0\n"
    "s_add_u32 s78, s52, s62\n"
    "v_exp_f32 v11, v11\n"
    "v_mfma_f32_16x16x32_bf16 v[124:127], a[116:119], v[72:75], v[124:127]\n"
    "s_waitcnt vmcnt(12)\n"
    "v_cvt_pk_bf16_f32 v97, v50, v51\n"
    "v_cvt_pk_bf16_f32 v99, v66, v67\n"
    "v_mfma_f32_16x16x32_bf16 v[164:167], a[128:131], v[84:87], 0\n"
    "v_exp_f32 v19, v19\n"
    "v_mfma_f32_16x16x32_bf16 v[160:163], a[120:123], v[84:87], v[160:163]\n"
    "v_cvt_pk_u16_u32 v204, v204, v208\n"
    "v_cvt_pk_bf16_f32 v98, v64, v65\n"
    "v_mfma_f32_16x16x32_bf16 a[20:23], a[152:155], v[84:87], 0\n"
    "v_cvt_pk_u16_u32 v214, v214, v218\n"
    "v_mfma_f32_16x16x32_bf16 v[148:151], a[128:131], v[76:79], v[148:151]\n"
    "v_cvt_pk_u16_u32 v207, v207, v211\n"
    "v_cvt_pk_u16_u32 v205, v205, v209\n"
    "v_mfma_f32_16x16x32_bf16 v[128:131], a[120:123], v[68:71], v[128:131]\n"
    "ds_read_b128 a[156:159], v249 offset:17408\n"
    "v_mfma_f32_16x16x32_bf16 v[172:175], a[116:119], v[96:99], 0\n"
    "s_add_u32 s77, s52, s61\n"
    "v_exp_f32 v25, v25\n"
    "v_mfma_f32_16x16x32_bf16 v[132:135], a[132:135], v[72:75], v[132:135]\n"
    "v_mfma_f32_16x16x32_bf16 v[144:147], a[124:127], v[80:83], v[144:147]\n"
    "v_mfma_f32_16x16x32_bf16 v[180:183], a[132:135], v[96:99], 0\n"
    "v_exp_f32 v17, v17\n"
    "v_mfma_f32_16x16x32_bf16 v[168:171], a[136:139], v[84:87], 0\n"
    "v_exp_f32 v9, v9\n"
    "v_mfma_f32_16x16x32_bf16 v[164:167], a[132:135], v[88:91], v[164:167]\n"
    "v_mfma_f32_16x16x32_bf16 v[152:155], a[136:139], v[76:79], v[152:155]\n"
    "v_exp_f32 v18, v18\n"
    "v_mfma_f32_16x16x32_bf16 v[176:179], a[124:127], v[96:99], v[176:179]\n"
    "ds_read_b128 a[132:135], v249 offset:20480\n"
    "v_mfma_f32_16x16x32_bf16 v[140:143], a[116:119], v[80:83], 0\n"
    "v_exp_f32 v31, v31\n"
    "v_mfma_f32_16x16x32_bf16 v[188:191], a[144:147], v[68:71], v[188:191]\n"
    "v_mfma_f32_16x16x32_bf16 v[168:171], a[140:143], v[88:91], v[168:171]\n"
    "v_exp_f32 v12, v12\n"
    "v_mfma_f32_16x16x32_bf16 v[180:183], a[128:131], v[92:95], v[180:183]\n"
    "v_cvt_pk_u16_u32 v212, v212, v216\n"
    "v_cvt_pk_u16_u32 v223, v223, v227\n"
    "v_mfma_f32_16x16x32_bf16 v[140:143], a[112:115], v[76:79], v[140:143]\n"
    "s_waitcnt lgkmcnt(1)\n"
    "v_exp_f32 v21, v21\n"
    "v_mfma_f32_16x16x32_bf16 a[36:39], a[156:159], v[96:99], 0\n"
    "ds_read_b128 a[128:131], v249 offset:8192\n"
    "v_cvt_pk_u16_u32 v222, v222, v226\n"
    "v_mfma_f32_16x16x32_bf16 v[172:175], a[112:115], v[92:95], v[172:175]\n"
    "v_or3_b32 v243, v204, v205, v243\n"
    "s_waitcnt lgkmcnt(1)\n"
    "v_mfma_f32_16x16x32_bf16 a[96:99], a[132:135], v[96:99], 0\n"
    "v_mfma_f32_16x16x32_bf16 a[20:23], a[156:159], v[88:91], a[20:23]\n"
    "ds_read_b128 a[116:119], v249 offset:18432\n"
    "v_mfma_f32_16x16x32_bf16 a[16:19], a[144:147], v[84:87], a[16:19]\n"
    "v_exp_f32 v15, v15\n"
    "v_mfma_f32_16x16x32_bf16 a[0:3], a[144:147], v[76:79], 0\n"
    "ds_read_b128 a[120:123], v249 offset:7168\n"
    "v_mfma_f32_16x16x32_bf16 v[184:187], a[140:143], v[96:99], 0\n"
    "v_exp_f32 v26, v26\n"
    "v_mfma_f32_16x16x32_bf16 a[64:67], a[132:135], v[80:83], 0\n"
    "v_cvt_pk_u16_u32 v206, v206, v210\n"
    "v_mfma_f32_16x16x32_bf16 a[36:39], a[152:155], v[92:95], a[36:39]\n"
    "ds_read_b128 a[124:127], v249 offset:19456\n"
    "ds_read_b128 a[140:143], v249 offset:21504\n"
    "v_mfma_f32_16x16x32_bf16 a[0:3], a[148:151], v[80:83], a[0:3]\n"
    "v_cvt_pk_u16_u32 v220, v220, v224\n"
    "v_cvt_pk_u16_u32 v221, v221, v225\n"
    "v_mfma_f32_16x16x32_bf16 v[184:187], a[136:139], v[92:95], v[184:187]\n"
    "s_waitcnt lgkmcnt(4)\n"
    "v_or3_b32 v243, v206, v207, v243\n"
    "v_cvt_pk_u16_u32 v213, v213, v217\n"
    "v_mfma_f32_16x16x32_bf16 a[80:83], a[128:131], v[84:87], 0\n"
    "s_add_u32 s67, s67, 49152\n"
    "ds_read_b128 a[136:139], v249 offset:9216\n"
    "ds_read_b128 a[144:147], v249 offset:10240\n"
    "v_mfma_f32_16x16x32_bf16 a[64:67], a[128:131], v[76:79], a[64:67]\n"
    "v_exp_f32 v23, v23\n"
    "v_mfma_f32_16x16x32_bf16 a[32:35], a[148:151], v[96:99], a[32:35]\n"
    "v_mfma_f32_16x16x32_bf16 a[4:7], a[156:159], v[80:83], 0\n"
    "v_mfma_f32_16x16x32_bf16 a[4:7], a[152:155], v[76:79], a[4:7]\n"
    "v_or3_b32 v243, v212, v213, v243\n"
    "v_cvt_pk_u16_u32 v215, v215, v219\n"
    "v_mfma_f32_16x16x32_bf16 v[192:195], a[156:159], v[72:75], v[192:195]\n"
    "v_exp_f32 v28, v28\n"
    "v_mfma_f32_16x16x32_bf16 a[96:99], a[128:131], v[92:95], a[96:99]\n"
    "s_waitcnt lgkmcnt(3)\n"
    "ds_read_b128 a[112:115], v249 offset:6144\n"
    "ds_read_b128 a[148:151], v249 offset:22528\n"
    "v_mfma_f32_16x16x32_bf16 v[200:203], a[124:127], v[72:75], 0\n"
    "v_exp_f32 v16, v16\n"
    "v_mfma_f32_16x16x32_bf16 a[12:15], a[120:123], v[76:79], 0\n"
    "buffer_load_dwordx2 v[236:237], v241, s[36:39], s81 offen offset:0 sc1\n"
    "v_mfma_f32_16x16x32_bf16 v[200:203], a[120:123], v[68:71], v[200:203]\n"
    "s_waitcnt lgkmcnt(4)\n"
    "v_add_u32 v251, 1536, v251\n"
    "v_mfma_f32_16x16x32_bf16 a[100:103], a[140:143], v[96:99], 0\n"
    "v_exp_f32 v29, v29\n"
    "v_mfma_f32_16x16x32_bf16 a[28:31], a[124:127], v[88:91], 0\n"
    "buffer_store_dword v204, v244, s[20:23], s76 offen offset:0 sc1\n"
    "v_exp_f32 v30, v30\n"
    "v_mfma_f32_16x16x32_bf16 v[32:35], a[72:75], a[168:171], v[32:35]\n"
    "v_mfma_f32_16x16x32_bf16 a[40:43], a[116:119], v[96:99], 0\n"
    "v_exp_f32 v13, v13\n"
    "v_mfma_f32_16x16x32_bf16 a[68:71], a[140:143], v[80:83], 0\n"
    "s_waitcnt lgkmcnt(3)\n"
    "v_mfma_f32_16x16x32_bf16 a[52:55], a[136:139], v[68:71], 0\n"
    "buffer_store_dword v205, v244, s[20:23], s76 offen offset:1024 sc1\n"
    "v_exp_f32 v22, v22\n"
    "v_mfma_f32_16x16x32_bf16 a[84:87], a[136:139], v[84:87], 0\n"
    "v_exp_f32 v24, v24\n"
    "v_mfma_f32_16x16x32_bf16 a[12:15], a[124:127], v[80:83], a[12:15]\n"
    "ds_read_b128 a[156:159], v249 offset:23552\n"
    "v_mfma_f32_16x16x32_bf16 a[44:47], a[120:123], v[92:95], 0\n"
    "v_exp_f32 v35, v35\n"
    "v_mfma_f32_16x16x32_bf16 a[80:83], a[132:135], v[88:91], a[80:83]\n"
    "buffer_store_dword v206, v244, s[20:23], s76 offen offset:2048 sc1\n"
    "ds_read_b128 v[100:103], v250 offset:1664\n"
    "v_mfma_f32_16x16x32_bf16 a[84:87], a[140:143], v[88:91], a[84:87]\n"
    "v_mfma_f32_16x16x32_bf16 a[8:11], a[116:119], v[80:83], 0\n"
    "s_waitcnt lgkmcnt(2)\n"
    "buffer_store_dword v207, v244, s[20:23], s76 offen offset:3072 sc1\n"
    "v_or3_b32 v243, v214, v215, v243\n"
    "v_or3_b32 v243, v220, v221, v243\n"
    "v_mfma_f32_16x16x32_bf16 a[88:91], a[148:151], v[88:91], 0\n"
    "v_mfma_f32_16x16x32_bf16 a[44:47], a[124:127], v[96:99], a[44:47]\n"
    "buffer_store_dword v212, v244, s[20:23], s77 offen offset:0 sc1\n"
    "v_mfma_f32_16x16x32_bf16 a[28:31], a[120:123], v[84:87], a[28:31]\n"
    "v_mfma_f32_16x16x32_bf16 a[24:27], a[112:115], v[84:87], 0\n"
    "ds_read_b128 a[152:155], v249 offset:11264\n"
    "v_mfma_f32_16x16x32_bf16 a[40:43], a[112:115], v[92:95], a[40:43]\n"
    "buffer_store_dword v213, v244, s[20:23], s77 offen offset:1024 sc1\n"
    "v_exp_f32 v6, v6\n"
    "v_mfma_f32_16x16x32_bf16 a[48:51], a[128:131], v[68:71], 0\n"
    "s_waitcnt lgkmcnt(2)\n"
    "v_exp_f32 v8, v8\n"
    "v_mfma_f32_16x16x32_bf16 a[76:79], a[156:159], v[80:83], 0\n"
    "buffer_store_dword v214, v244, s[20:23], s77 offen offset:2048 sc1\n"
    "v_mfma_f32_16x16x32_bf16 a[104:107], a[144:147], v[92:95], 0\n"
    "v_exp_f32 v34, v34\n"
    "v_mfma_f32_16x16x32_bf16 a[52:55], a[140:143], v[72:75], a[52:55]\n"
    "buffer_store_dword v215, v244, s[20:23], s77 offen offset:3072 sc1\n"
    "v_exp_f32 v14, v14\n"
    "v_mfma_f32_16x16x32_bf16 a[48:51], a[132:135], v[72:75], a[48:51]\n"
    "v_mfma_f32_16x16x32_bf16 a[24:27], a[116:119], v[88:91], a[24:27]\n"
    "buffer_store_dword v220, v244, s[20:23], s78 offen offset:0 sc1\n"
    "ds_read_b128 a[120:123], v3 offset:16384\n"
    "v_mfma_f32_16x16x32_bf16 v[196:199], a[112:115], v[68:71], 0\n"
    "v_mfma_f32_16x16x32_bf16 v[196:199], a[116:119], v[72:75], v[196:199]\n"
    "v_mfma_f32_16x16x32_bf16 a[8:11], a[112:115], v[76:79], a[8:11]\n"
    "s_waitcnt lgkmcnt(1)\n"
    "ds_read_b128 a[132:135], v3 offset:22528\n"
    "v_mfma_f32_16x16x32_bf16 a[76:79], a[152:155], v[76:79], a[76:79]\n"
    "buffer_store_dword v221, v244, s[20:23], s78 offen offset:1024 sc1\n"
    "v_mfma_f32_16x16x32_bf16 a[108:111], a[156:159], v[96:99], 0\n"
    "ds_read_b128 a[124:127], v3 offset:18432\n"
    "ds_read_b128 a[128:131], v3 offset:20480\n"
    "v_mfma_f32_16x16x32_bf16 a[92:95], a[152:155], v[84:87], 0\n"
    "v_or3_b32 v243, v222, v223, v243\n"
    "v_mfma_f32_16x16x32_bf16 a[92:95], a[156:159], v[88:91], a[92:95]\n"
    "ds_read_b128 a[112:115], v3 offset:12288\n"
    "ds_read_b128 a[116:119], v3 offset:14336\n"
    "v_mfma_f32_16x16x32_bf16 a[72:75], a[144:147], v[76:79], 0\n"
    "s_waitcnt vmcnt(11)\n"
    "ds_add_u32 v247, v246\n"
    "v_mfma_f32_16x16x32_bf16 a[56:59], a[148:151], v[72:75], 0\n"
    "v_exp_f32 v32, v32\n"
    "v_mfma_f32_16x16x32_bf16 a[60:63], a[152:155], v[68:71], 0\n"
    "v_and_b32 v208, 0xff00ff00, v243\n"
    "v_add_u32 v250, 1536, v250\n"
    "v_mfma_f32_16x16x32_bf16 a[68:71], a[136:139], v[76:79], a[68:71]\n"
    "v_exp_f32 v33, v33\n"
    "v_mfma_f32_16x16x32_bf16 a[88:91], a[144:147], v[84:87], a[88:91]\n"
    "buffer_store_dword v222, v244, s[20:23], s78 offen offset:2048 sc1\n"
    "v_exp_f32 v5, v5\n"
    "v_mfma_f32_16x16x32_bf16 a[72:75], a[148:151], v[80:83], a[72:75]\n"
    "v_cmp_ne_u32 vcc, 0, v208\n"
    "v_mfma_f32_16x16x32_bf16 a[104:107], a[148:151], v[96:99], a[104:107]\n"
    "v_mfma_f32_16x16x32_bf16 a[108:111], a[152:155], v[92:95], a[108:111]\n"
    "buffer_store_dword v223, v244, s[20:23], s78 offen offset:3072 sc1\n"
    "v_add_u32 v3, 49152, v252\n"
    "v_mfma_f32_16x16x32_bf16 a[56:59], a[144:147], v[68:71], a[56:59]\n"
    "s_waitcnt lgkmcnt(1)\n"
    "v_cndmask_b32 v208, 2.0, v241, vcc\n"
    "v_mfma_f32_16x16x32_bf16 a[60:63], a[156:159], v[72:75], a[60:63]\n"
    "v_mfma_f32_16x16x32_bf16 a[100:103], a[136:139], v[92:95], a[100:103]\n"
    "buffer_store_dwordx2 v[238:239], v208, s[36:39], 0 offen offset:0 sc1\n"
    "v_smfmac_f32_16x16x64_bf16 v[120:123], v[52:55], v[92:99], v253\n"
    "s_branch .LPV\n"
    ".fill ((0x20000 - (.LwKe - .LwK) - (. - _Z8attn_fwdPKtPKiS2_S0_S0_S0_S0_PcPt)) & 4095) / 4, 4, 0xBF800000\n"
    ".LwK:\n"
    "s_branch .LwKe\n"
    ".LPK:\n"
    "v_mfma_f32_16x16x32_bf16 v[36:39], a[112:115], a[192:195], v[100:103]\n"
    "s_add_u32 s71, s71, 98304\n"
    "ds_read_b128 a[136:139], v2 offset:13312\n"
    "v_cvt_pk_bf16_f32 v68, v4, v5\n"
    "v_mfma_f32_16x16x32_bf16 v[36:39], a[116:119], a[196:199], v[36:39]\n"
    "s_add_u32 s75, s71, s63\n"
    "ds_read_b128 a[140:143], v2 offset:15360\n"
    "v_cvt_pk_bf16_f32 v69, v6, v7\n"
    "v_mfma_f32_16x16x32_bf16 v[36:39], a[120:123], a[200:203], v[36:39]\n"
    "s_add_u32 s76, s75, 6144\n"
    "ds_read_b128 a[144:147], v2 offset:17408\n"
    "v_cvt_pk_bf16_f32 v76, v8, v9\n"
    "s_nop 0\n"
    "v_mfma_f32_16x16x32_bf16 v[36:39], a[124:127], a[204:207], v[36:39]\n"
    "s_add_u32 s77, s75, 24576\n"
    "ds_read_b128 a[148:151], v2 offset:19456\n"
    "v_cvt_pk_bf16_f32 v77, v10, v11\n"
    "v_mfma_f32_16x16x32_bf16 v[36:39], a[128:131], a[208:211], v[36:39]\n"
    "s_add_u32 s78, s75, 30720\n"
    "ds_read_b128 a[152:155], v2 offset:21504\n"
    "v_cvt_pk_bf16_f32 v84, v12, v13\n"
    "v_mfma_f32_16x16x32_bf16 v[36:39], a[132:135], a[212:215], v[36:39]\n"
    "ds_read_b128 a[156:159], v2 offset:23552\n"
    "v_cvt_pk_bf16_f32 v85, v14, v15\n"
    "v_mfma_f32_16x16x32_bf16 v[40:43], a[132:135], a[236:239], v[100:103]\n"
    "ds_read_b128 v[104:107], v250 offset:144\n"
    "v_cvt_pk_bf16_f32 v92, v16, v17\n"
    "v_mfma_f32_16x16x32_bf16 v[40:43], a[128:131], a[232:235], v[40:43]\n"
    "s_add_u32 s68, s68, 24576\n"
    "v_cvt_pk_bf16_f32 v93, v18, v19\n"
    "v_cvt_pk_bf16_f32 v70, v20, v21\n"
    "v_mfma_f32_16x16x32_bf16 v[40:43], a[124:127], a[228:231], v[40:43]\n"
    "s_cmp_ge_u32 s68, s69\n"
    "s_cselect_b32 s68, s65, s68\n"
    "v_cvt_pk_bf16_f32 v71, v22, v23\n"
    "v_mfma_f32_16x16x32_bf16 v[40:43], a[120:123], a[224:227], v[40:43]\n"
    "v_cvt_pk_bf16_f32 v78, v24, v25\n"
    "v_cvt_pk_bf16_f32 v79, v26, v27\n"
    "v_mfma_f32_16x16x32_bf16 v[40:43], a[116:119], a[220:223], v[40:43]\n"
    "v_cvt_pk_bf16_f32 v86, v28, v29\n"
    "v_cvt_pk_bf16_f32 v87, v30, v31\n"
    "v_mfma_f32_16x16x32_bf16 v[40:43], a[112:115], a[216:219], v[40:43]\n"
    "v_cvt_pk_bf16_f32 v94, v32, v33\n"
    "v_cvt_pk_bf16_f32 v95, v34, v35\n"
    "v_mfma_f32_16x16x32_bf16 v[44:47], a[112:115], a[240:243], v[100:103]\n"
    "v_exp_f32_e64 v36, v36\n"
    "v_mfma_f32_16x16x32_bf16 v[44:47], a[116:119], a[244:247], v[44:47]\n"
    "v_exp_f32_e64 v37, v37\n"
    "v_mfma_f32_16x16x32_bf16 v[44:47], a[120:123], a[248:251], v[44:47]\n"
    "v_exp_f32_e64 v38, v38\n"
    "v_mfma_f32_16x16x32_bf16 v[44:47], a[124:127], a[252:255], v[44:47]\n"
    "v_exp_f32_e64 v39, v39\n"
    "v_mfma_f32_16x16x32_bf16 v[44:47], a[128:131], a[160:163], v[44:47]\n"
    "v_cvt_pk_bf16_f32 v72, v36, v37\n"
    "v_cvt_pk_bf16_f32 v73, v38, v39\n"
    "v_mfma_f32_16x16x32_bf16 v[44:47], a[132:135], a[164:167], v[44:47]\n"
    "v_exp_f32_e64 v40, v40\n"
    "v_mfma_f32_16x16x32_bf16 v[48:51], a[132:135], a[188:191], v[100:103]\n"
    "ds_read_b32 v245, v247\n"
    "v_mfma_f32_16x16x32_bf16 v[48:51], a[128:131], a[184:187], v[48:51]\n"
    "s_add_u32 m0, s68, .Lend-.Lend2+(0)\n"
    "v_exp_f32_e64 v41, v41\n"
    "v_mfma_f32_16x16x32_bf16 v[48:51], a[124:127], a[180:183], v[48:51]\n"
    "v_exp_f32 v42, v42\n"
    "v_mfma_f32_16x16x32_bf16 v[48:51], a[120:123], a[176:179], v[48:51]\n"
    "v_exp_f32 v43, v43\n"
    "v_mfma_f32_16x16x32_bf16 v[48:51], a[116:119], a[172:175], v[48:51]\n"
    "s_waitcnt vmcnt(10)\n"
    "v_cmp_eq_u64 s[54:55], v[236:237], v[238:239]\n"
    "v_cvt_pk_bf16_f32 v80, v40, v41\n"
    "v_mfma_f32_16x16x32_bf16 v[48:51], a[112:115], a[168:171], v[48:51]\n"
    "s_waitcnt lgkmcnt(1)\n"
    "v_cvt_pk_bf16_f32 v81, v42, v43\n"
    "v_mfma_f32_16x16x32_bf16 v[52:55], a[136:139], a[192:195], v[104:107]\n"
    "s_waitcnt lgkmcnt(0)\n"
    "v_cmp_le_u32 s[56:57], s59, v245\n"
    "v_mfma_f32_16x16x32_bf16 v[52:55], a[140:143], a[196:199], v[52:55]\n"
    "v_exp_f32 v44, v44\n"
    "v_mfma_f32_16x16x32_bf16 v[52:55], a[144:147], a[200:203], v[52:55]\n"
    "s_and_b64 s[54:55], s[54:55], s[56:57]\n"
    "s_cmp_eq_u64 s[54:55], -1\n"
    "v_mfma_f32_16x16x32_bf16 v[52:55], a[148:151], a[204:207], v[52:55]\n"
    "s_cbranch_scc0 .Lpoll1\n"
    ".Lpoll1r:\n"
    "v_mfma_f32_16x16x32_bf16 v[52:55], a[152:155], a[208:211], v[52:55]\n"
    "buffer_load_dwordx4 v1, s[16:19], s67 offen offset:0 sc1 lds\n"
    "v_mfma_f32_16x16x32_bf16 v[52:55], a[156:159], a[212:215], v[52:55]\n"
    "ds_read_b128 a[112:115], v3 offset:0\n"
    "v_mfma_f32_16x16x32_bf16 v[56:59], a[156:159], a[236:239], v[104:107]\n"
    "ds_read_b128 a[116:119], v3 offset:2048\n"
    "v_mfma_f32_16x16x32_bf16 v[56:59], a[152:155], a[232:235], v[56:59]\n"
    "ds_read_b128 a[120:123], v3 offset:4096\n"
    "v_mfma_f32_16x16x32_bf16 v[56:59], a[148:151], a[228:231], v[56:59]\n"
    "buffer_load_dwordx4 v1, s[16:19], s67 offen offset:1024 sc1 lds\n"
    "v_mfma_f32_16x16x32_bf16 v[56:59], a[144:147], a[224:227], v[56:59]\n"
    "ds_read_b128 a[124:127], v3 offset:6144\n"
    "v_mfma_f32_16x16x32_bf16 v[56:59], a[140:143], a[220:223], v[56:59]\n"
    "ds_read_b128 a[128:131], v3 offset:8192\n"
    "v_mfma_f32_16x16x32_bf16 v[56:59], a[136:139], a[216:219], v[56:59]\n"
    "ds_read_b128 a[132:135], v3 offset:10240\n"
    "v_mfma_f32_16x16x32_bf16 v[60:63], a[136:139], a[240:243], v[104:107]\n"
    "buffer_load_dwordx4 v1, s[16:19], s67 offen offset:2048 sc1 lds\n"
    "v_mfma_f32_16x16x32_bf16 v[60:63], a[140:143], a[244:247], v[60:63]\n"
    "ds_read_b128 v[100:103], v250 offset:768\n"
    "v_mfma_f32_16x16x32_bf16 v[60:63], a[144:147], a[248:251], v[60:63]\n"
    "v_exp_f32 v52, v52\n"
    "v_mfma_f32_16x16x32_bf16 v[60:63], a[148:151], a[252:255], v[60:63]\n"
    "v_exp_f32 v53, v53\n"
    "v_mfma_f32_16x16x32_bf16 v[60:63], a[152:155], a[160:163], v[60:63]\n"
    "buffer_load_dwordx4 v1, s[16:19], s67 offen offset:3072 sc1 lds\n"
    "v_mfma_f32_16x16x32_bf16 v[60:63], a[156:159], a[164:167], v[60:63]\n"
    "s_add_u32 m0, s68, 4096\n"
    "v_exp_f32 v54, v54\n"
    "v_mfma_f32_16x16x32_bf16 v[64:67], a[156:159], a[188:191], v[104:107]\n"
    "s_add_u32 s83, s67, 4096\n"
    "v_exp_f32 v55, v55\n"
    "v_mfma_f32_16x16x32_bf16 v[64:67], a[152:155], a[184:187], v[64:67]\n"
    "v_cvt_pk_bf16_f32 v74, v52, v53\n"
    "v_cvt_pk_bf16_f32 v75, v54, v55\n"
    "v_mfma_f32_16x16x32_bf16 v[64:67], a[148:151], a[180:183], v[64:67]\n"
    "buffer_load_dwordx4 v1, s[16:19], s83 offen offset:0 sc1 lds\n"
    "v_mfma_f32_16x16x32_bf16 v[64:67], a[144:147], a[176:179], v[64:67]\n"
    "v_exp_f32 v56, v56\n"
    "v_mfma_f32_16x16x32_bf16 v[64:67], a[140:143], a[172:175], v[64:67]\n"
    "v_exp_f32 v57, v57\n"
    "v_mfma_f32_16x16x32_bf16 v[64:67], a[136:139], a[168:171], v[64:67]\n"
    "s_waitcnt lgkmcnt(0)\n"
    "v_exp_f32 v58, v58\n"
    "v_mfma_f32_16x16x32_bf16 v[4:7], a[112:115], a[192:195], v[100:103]\n"
    "buffer_load_dwordx4 v1, s[16:19], s83 offen offset:1024 sc1 lds\n"
    "v_mfma_f32_16x16x32_bf16 v[4:7], a[116:119], a[196:199], v[4:7]\n"
    "s_add_u32 m0, s64, 0\n"
    "ds_read_b128 a[136:139], v3 offset:1024\n"
    "v_cvt_pk_bf16_f32 v82, v56, v57\n"
    "v_mfma_f32_16x16x32_bf16 v[4:7], a[120:123], a[200:203], v[4:7]\n"
    "s_add_u32 s83, s67, 24576\n"
    "ds_read_b128 a[140:143], v3 offset:3072\n"
    "v_mfma_f32_16x16x32_bf16 v[4:7], a[124:127], a[204:207], v[4:7]\n"
    "ds_read_b128 a[144:147], v3 offset:5120\n"
    "v_mfma_f32_16x16x32_bf16 v[4:7], a[128:131], a[208:211], v[4:7]\n"
    "buffer_load_dwordx4 v1, s[16:19], s83 offen offset:0 sc1 lds\n"
    "v_mfma_f32_16x16x32_bf16 v[4:7], a[132:135], a[212:215], v[4:7]\n"
    "ds_read_b128 a[148:151], v3 offset:7168\n"
    "v_mfma_f32_16x16x32_bf16 v[8:11], a[132:135], a[236:239], v[100:103]\n"
    "ds_read_b128 a[152:155], v3 offset:9216\n"
    "v_mfma_f32_16x16x32_bf16 v[8:11], a[128:131], a[232:235], v[8:11]\n"
    "ds_read_b128 a[156:159], v3 offset:11264\n"
    "v_mfma_f32_16x16x32_bf16 v[8:11], a[124:127], a[228:231], v[8:11]\n"
    "buffer_load_dwordx4 v1, s[16:19], s83 offen offset:1024 sc1 lds\n"
    "v_mfma_f32_16x16x32_bf16 v[8:11], a[120:123], a[224:227], v[8:11]\n"
    "ds_read_b128 v[104:107], v250 offset:784\n"
    "v_mfma_f32_16x16x32_bf16 v[8:11], a[116:119], a[220:223], v[8:11]\n"
    "v_exp_f32 v59, v59\n"
    "v_mfma_f32_16x16x32_bf16 v[8:11], a[112:115], a[216:219], v[8:11]\n"
    "v_cvt_pk_bf16_f32 v83, v58, v59\n"
    "v_mfma_f32_16x16x32_bf16 v[12:15], a[112:115], a[240:243], v[100:103]\n"
    "buffer_load_dwordx4 v1, s[16:19], s83 offen offset:2048 sc1 lds\n"
    "v_mfma_f32_16x16x32_bf16 v[12:15], a[116:119], a[244:247], v[12:15]\n"
    "v_exp_f32 v45, v45\n"
    "v_mfma_f32_16x16x32_bf16 v[12:15], a[120:123], a[248:251], v[12:15]\n"
    "v_exp_f32 v46, v46\n"
    "v_mfma_f32_16x16x32_bf16 v[12:15], a[124:127], a[252:255], v[12:15]\n"
    "v_exp_f32 v47, v47\n"
    "v_mfma_f32_16x16x32_bf16 v[12:15], a[128:131], a[160:163], v[12:15]\n"
    "buffer_load_dwordx4 v1, s[16:19], s83 offen offset:3072 sc1 lds\n"
    "v_mfma_f32_16x16x32_bf16 v[12:15], a[132:135], a[164:167], v[12:15]\n"
    "s_add_u32 m0, s64, 4096\n"
    "v_exp_f32 v60, v60\n"
    "v_mfma_f32_16x16x32_bf16 v[16:19], a[132:135], a[188:191], v[100:103]\n"
    "s_add_u32 s83, s67, 28672\n"
    "v_exp_f32 v61, v61\n"
    "v_mfma_f32_16x16x32_bf16 v[16:19], a[128:131], a[184:187], v[16:19]\n"
    "v_exp_f32 v62, v62\n"
    "v_mfma_f32_16x16x32_bf16 v[16:19], a[124:127], a[180:183], v[16:19]\n"
    "buffer_load_dwordx4 v1, s[16:19], s83 offen offset:0 sc1 lds\n"
    "v_mfma_f32_16x16x32_bf16 v[16:19], a[120:123], a[176:179], v[16:19]\n"
    "v_exp_f32 v63, v63\n"
    "v_mfma_f32_16x16x32_bf16 v[16:19], a[116:119], a[172:175], v[16:19]\n"
    "v_cvt_pk_bf16_f32 v88, v44, v45\n"
    "v_cvt_pk_bf16_f32 v89, v46, v47\n"
    "v_mfma_f32_16x16x32_bf16 v[16:19], a[112:115], a[168:171], v[16:19]\n"
    "s_waitcnt lgkmcnt(0)\n"
    "v_cvt_pk_bf16_f32 v90, v60, v61\n"
    "v_cvt_pk_bf16_f32 v91, v62, v63\n"
    "v_mfma_f32_16x16x32_bf16 v[20:23], a[136:139], a[192:195], v[104:107]\n"
    "buffer_load_dwordx4 v1, s[16:19], s83 offen offset:1024 sc1 lds\n"
    "v_mfma_f32_16x16x32_bf16 v[20:23], a[140:143], a[196:199], v[20:23]\n"
    "s_add_u32 s67, s67, 49152\n"
    "ds_read_b128 a[112:115], v248 offset:0\n"
    "v_mfma_f32_16x16x32_bf16 v[20:23], a[144:147], a[200:203], v[20:23]\n"
    "s_add_u32 s59, s59, 4\n"
    "ds_read_b128 a[116:119], v248 offset:12288\n"
    "v_mfma_f32_16x16x32_bf16 v[20:23], a[148:151], a[204:207], v[20:23]\n"
    "s_waitcnt vmcnt(12)\n"
    "ds_read_b128 a[120:123], v248 offset:1024\n"
    "v_mfma_f32_16x16x32_bf16 v[20:23], a[152:155], a[208:211], v[20:23]\n"
    "buffer_store_dwordx2 v[238:239], v240, s[36:39], s80 offen offset:0 sc1\n"
    "v_mfma_f32_16x16x32_bf16 v[20:23], a[156:159], a[212:215], v[20:23]\n"
    "buffer_load_dwordx2 v[204:205], v242, s[28:31], s75 offen offset:0 nt\n"
    "v_mfma_f32_16x16x32_bf16 v[24:27], a[156:159], a[236:239], v[104:107]\n"
    "s_add_u32 s80, s80, 256\n"
    "ds_read_b128 a[124:127], v248 offset:13312\n"
    "v_mfma_f32_16x16x32_bf16 v[24:27], a[152:155], a[232:235], v[24:27]\n"
    "buffer_load_dwordx2 v[206:207], v242, s[28:31], s75 offen offset:768 nt\n"
    "v_mfma_f32_16x16x32_bf16 v[24:27], a[148:151], a[228:231], v[24:27]\n"
    "ds_read_b128 a[128:131], v248 offset:2048\n"
    "v_mfma_f32_16x16x32_bf16 v[24:27], a[144:147], a[224:227], v[24:27]\n"
    "buffer_load_dwordx2 v[208:209], v242, s[28:31], s75 offen offset:3072 nt\n"
    "v_mfma_f32_16x16x32_bf16 v[24:27], a[140:143], a[220:223], v[24:27]\n"
    "ds_read_b128 a[132:135], v248 offset:14336\n"
    "v_mfma_f32_16x16x32_bf16 v[24:27], a[136:139], a[216:219], v[24:27]\n"
    "buffer_load_dwordx2 v[210:211], v242, s[28:31], s75 offen offset:3840 nt\n"
    "v_mfma_f32_16x16x32_bf16 v[28:31], a[136:139], a[240:243], v[104:107]\n"
    "v_exp_f32 v48, v48\n"
    "v_mfma_f32_16x16x32_bf16 v[28:31], a[140:143], a[244:247], v[28:31]\n"
    "buffer_load_dwordx2 v[212:213], v242, s[28:31], s76 offen offset:0 nt\n"
    "v_mfma_f32_16x16x32_bf16 v[28:31], a[144:147], a[248:251], v[28:31]\n"
    "v_exp_f32 v49, v49\n"
    "v_mfma_f32_16x16x32_bf16 v[28:31], a[148:151], a[252:255], v[28:31]\n"
    "buffer_load_dwordx2 v[214:215], v242, s[28:31], s76 offen offset:768 nt\n"
    "v_mfma_f32_16x16x32_bf16 v[28:31], a[152:155], a[160:163], v[28:31]\n"
    "v_exp_f32 v50, v50\n"
    "v_mfma_f32_16x16x32_bf16 v[28:31], a[156:159], a[164:167], v[28:31]\n"
    "buffer_load_dwordx2 v[216:217], v242, s[28:31], s76 offen offset:3072 nt\n"
    "v_mfma_f32_16x16x32_bf16 v[32:35], a[156:159], a[188:191], v[104:107]\n"
    "v_exp_f32 v51, v51\n"
    "v_mfma_f32_16x16x32_bf16 v[32:35], a[152:155], a[184:187], v[32:35]\n"
    "buffer_load_dwordx2 v[218:219], v242, s[28:31], s76 offen offset:3840 nt\n"
    "v_mfma_f32_16x16x32_bf16 v[32:35], a[148:151], a[180:183], v[32:35]\n"
    "v_exp_f32 v64, v64\n"
    "v_mfma_f32_16x16x32_bf16 v[32:35], a[144:147], a[176:179], v[32:35]\n"
    "buffer_load_dwordx2 v[220:221], v242, s[28:31], s77 offen offset:0 nt\n"
    "v_mfma_f32_16x16x32_bf16 v[32:35], a[140:143], a[172:175], v[32:35]\n"
    "v_exp_f32 v65, v65\n"
    "v_mfma_f32_16x16x32_bf16 v[32:35], a[136:139], a[168:171], v[32:35]\n"
    "s_waitcnt lgkmcnt(0)\n"
    "v_exp_f32 v66, v66\n"
    "v_mfma_f32_16x16x32_bf16 v[124:127], a[112:115], v[68:71], v[124:127]\n"
    "buffer_load_dwordx2 v[222:223], v242, s[28:31], s77 offen offset:768 nt\n"
    "v_mfma_f32_16x16x32_bf16 v[124:127], a[116:119], v[72:75], v[124:127]\n"
    "ds_read_b128 a[136:139], v248 offset:3072\n"
    "v_cvt_pk_bf16_f32 v96, v48, v49\n"
    "v_mfma_f32_16x16x32_bf16 v[128:131], a[124:127], v[72:75], v[128:131]\n"
    "buffer_load_dwordx2 v[224:225], v242, s[28:31], s77 offen offset:3072 nt\n"
    "v_mfma_f32_16x16x32_bf16 v[128:131], a[120:123], v[68:71], v[128:131]\n"
    "ds_read_b128 a[140:143], v248 offset:15360\n"
    "v_cvt_pk_bf16_f32 v97, v50, v51\n"
    "v_mfma_f32_16x16x32_bf16 v[132:135], a[128:131], v[68:71], v[132:135]\n"
    "buffer_load_dwordx2 v[226:227], v242, s[28:31], s77 offen offset:3840 nt\n"
    "v_mfma_f32_16x16x32_bf16 v[132:135], a[132:135], v[72:75], v[132:135]\n"
    "ds_read_b128 a[144:147], v248 offset:4096\n"
    "v_cvt_pk_bf16_f32 v98, v64, v65\n"
    "v_mfma_f32_16x16x32_bf16 v[148:151], a[132:135], v[80:83], v[148:151]\n"
    "buffer_load_dwordx2 v[228:229], v242, s[28:31], s78 offen offset:0 nt\n"
    "v_mfma_f32_16x16x32_bf16 v[148:151], a[128:131], v[76:79], v[148:151]\n"
    "ds_read_b128 a[148:151], v248 offset:16384\n"
    "v_mfma_f32_16x16x32_bf16 v[144:147], a[120:123], v[76:79], v[144:147]\n"
    "buffer_load_dwordx2 v[230:231], v242, s[28:31], s78 offen offset:768 nt\n"
    "v_mfma_f32_16x16x32_bf16 v[144:147], a[124:127], v[80:83], v[144:147]\n"
    "ds_read_b128 a[152:155], v248 offset:5120\n"
    "v_mfma_f32_16x16x32_bf16 v[140:143], a[116:119], v[80:83], v[140:143]\n"
    "buffer_load_dwordx2 v[232:233], v242, s[28:31], s78 offen offset:3072 nt\n"
    "v_mfma_f32_16x16x32_bf16 v[140:143], a[112:115], v[76:79], v[140:143]\n"
    "ds_read_b128 a[156:159], v248 offset:17408\n"
    "v_mfma_f32_16x16x32_bf16 v[156:159], a[112:115], v[84:87], v[156:159]\n"
    "buffer_load_dwordx2 v[234:235], v242, s[28:31], s78 offen offset:3840 nt\n"
    "v_mfma_f32_16x16x32_bf16 v[156:159], a[116:119], v[88:91], v[156:159]\n"
    "ds_read_b128 v[104:107], v251 offset:0\n"
    "v_mfma_f32_16x16x32_bf16 v[160:163], a[124:127], v[88:91], v[160:163]\n"
    "v_exp_f32 v67, v67\n"
    "v_mfma_f32_16x16x32_bf16 v[160:163], a[120:123], v[84:87], v[160:163]\n"
    "v_cvt_pk_bf16_f32 v99, v66, v67\n"
    "v_mfma_f32_16x16x32_bf16 v[164:167], a[128:131], v[84:87], v[164:167]\n"
    "v_exp_f32 v4, v4\n"
    "v_mfma_f32_16x16x32_bf16 v[164:167], a[132:135], v[88:91], v[164:167]\n"
    "v_exp_f32 v5, v5\n"
    "v_mfma_f32_16x16x32_bf16 v[180:183], a[132:135], v[96:99], v[180:183]\n"
    "v_exp_f32 v6, v6\n"
    "v_mfma_f32_16x16x32_bf16 v[180:183], a[128:131], v[92:95], v[180:183]\n"
    "v_exp_f32 v7, v7\n"
    "v_mfma_f32_16x16x32_bf16 v[176:179], a[120:123], v[92:95], v[176:179]\n"
    "v_exp_f32 v8, v8\n"
    "v_mfma_f32_16x16x32_bf16 v[176:179], a[124:127], v[96:99], v[176:179]\n"
    "v_exp_f32 v9, v9\n"
    "v_mfma_f32_16x16x32_bf16 v[172:175], a[116:119], v[96:99], v[172:175]\n"
    "v_exp_f32 v10, v10\n"
    "v_mfma_f32_16x16x32_bf16 v[172:175], a[112:115], v[92:95], v[172:175]\n"
    "s_waitcnt lgkmcnt(0)\n"
    "v_exp_f32 v11, v11\n"
    "v_mfma_f32_16x16x32_bf16 v[136:139], a[136:139], v[68:71], v[136:139]\n"
    "ds_read_b128 a[112:115], v248 offset:6144\n"
    "v_mfma_f32_16x16x32_bf16 v[136:139], a[140:143], v[72:75], v[136:139]\n"
    "ds_read_b128 a[116:119], v248 offset:18432\n"
    "v_mfma_f32_16x16x32_bf16 v[188:191], a[148:151], v[72:75], v[188:191]\n"
    "ds_read_b128 a[120:123], v248 offset:7168\n"
    "v_mfma_f32_16x16x32_bf16 v[188:191], a[144:147], v[68:71], v[188:191]\n"
    "ds_read_b128 a[124:127], v248 offset:19456\n"
    "v_mfma_f32_16x16x32_bf16 v[192:195], a[152:155], v[68:71], v[192:195]\n"
    "ds_read_b128 a[128:131], v248 offset:8192\n"
    "v_mfma_f32_16x16x32_bf16 v[192:195], a[156:159], v[72:75], v[192:195]\n"
    "ds_read_b128 a[132:135], v248 offset:20480\n"
    "v_smfmac_f32_16x16x64_bf16 v[108:111], v[104:107], v[68:75], v253\n"
    "s_add_u32 s81, s81, 128\n"
    "s_cmp_gt_u32 s81, 4096\n"
    "s_cselect_b32 s81, 0, s81\n"
    "v_smfmac_f32_16x16x64_bf16 v[112:115], v[104:107], v[76:83], v253\n"
    "s_cmp_ge_u32 s75, 1474560\n"
    "s_cmov_b64 s[28:29], s[6:7]\n"
    "v_mfma_f32_16x16x32_bf16 a[4:7], a[156:159], v[80:83], a[4:7]\n"
    "v_exp_f32 v12, v12\n"
    "v_mfma_f32_16x16x32_bf16 a[4:7], a[152:155], v[76:79], a[4:7]\n"
    "v_exp_f32 v13, v13\n"
    "v_mfma_f32_16x16x32_bf16 a[0:3], a[144:147], v[76:79], a[0:3]\n"
    "v_exp_f32 v14, v14\n"
    "v_mfma_f32_16x16x32_bf16 a[0:3], a[148:151], v[80:83], a[0:3]\n"
    "v_exp_f32 v15, v15\n"
    "v_mfma_f32_16x16x32_bf16 v[152:155], a[140:143], v[80:83], v[152:155]\n"
    "v_exp_f32 v16, v16\n"
    "v_mfma_f32_16x16x32_bf16 v[152:155], a[136:139], v[76:79], v[152:155]\n"
    "v_mfma_f32_16x16x32_bf16 v[168:171], a[136:139], v[84:87], v[168:171]\n"
    "v_exp_f32 v17, v17\n"
    "v_mfma_f32_16x16x32_bf16 v[168:171], a[140:143], v[88:91], v[168:171]\n"
    "v_mfma_f32_16x16x32_bf16 a[16:19], a[148:151], v[88:91], a[16:19]\n"
    "v_mfma_f32_16x16x32_bf16 a[16:19], a[144:147], v[84:87], a[16:19]\n"
    "v_exp_f32 v18, v18\n"
    "v_mfma_f32_16x16x32_bf16 a[20:23], a[152:155], v[84:87], a[20:23]\n"
    "v_mfma_f32_16x16x32_bf16 a[20:23], a[156:159], v[88:91], a[20:23]\n"
    "v_smfmac_f32_16x16x64_bf16 v[116:119], v[104:107], v[84:91], v253\n"
    "v_smfmac_f32_16x16x64_bf16 v[120:123], v[104:107], v[92:99], v253\n"
    "v_mfma_f32_16x16x32_bf16 a[36:39], a[156:159], v[96:99], a[36:39]\n"
    "v_exp_f32 v19, v19\n"
    "v_mfma_f32_16x16x32_bf16 a[36:39], a[152:155], v[92:95], a[36:39]\n"
    "v_exp_f32 v20, v20\n"
    "v_mfma_f32_16x16x32_bf16 a[32:35], a[144:147], v[92:95], a[32:35]\n"
    "v_mfma_f32_16x16x32_bf16 a[32:35], a[148:151], v[96:99], a[32:35]\n"
    "v_exp_f32 v21, v21\n"
    "v_mfma_f32_16x16x32_bf16 v[184:187], a[140:143], v[96:99], v[184:187]\n"
    "v_mfma_f32_16x16x32_bf16 v[184:187], a[136:139], v[92:95], v[184:187]\n"
    "s_waitcnt lgkmcnt(0)\n"
    "v_mfma_f32_16x16x32_bf16 v[196:199], a[112:115], v[68:71], v[196:199]\n"
    "ds_read_b128 a[136:139], v248 offset:9216\n"
    "v_mfma_f32_16x16x32_bf16 v[196:199], a[116:119], v[72:75], v[196:199]\n"
    "ds_read_b128 a[140:143], v248 offset:21504\n"
    "v_mfma_f32_16x16x32_bf16 v[200:203], a[124:127], v[72:75], v[200:203]\n"
    "ds_read_b128 a[144:147], v248 offset:10240\n"
    "v_mfma_f32_16x16x32_bf16 v[200:203], a[120:123], v[68:71], v[200:203]\n"
    "ds_read_b128 a[148:151], v248 offset:22528\n"
    "v_mfma_f32_16x16x32_bf16 a[48:51], a[128:131], v[68:71], a[48:51]\n"
    "buffer_load_dwordx2 v[236:237], v241, s[36:39], s81 offen offset:0 sc1\n"
    "v_mfma_f32_16x16x32_bf16 a[48:51], a[132:135], v[72:75], a[48:51]\n"
    "ds_read_b128 a[152:155], v248 offset:11264\n"
    "v_mfma_f32_16x16x32_bf16 a[64:67], a[132:135], v[80:83], a[64:67]\n"
    "ds_read_b128 a[156:159], v248 offset:23552\n"
    "v_mfma_f32_16x16x32_bf16 a[64:67], a[128:131], v[76:79], a[64:67]\n"
    "v_exp_f32 v22, v22\n"
    "v_mfma_f32_16x16x32_bf16 a[12:15], a[120:123], v[76:79], a[12:15]\n"
    "v_exp_f32 v23, v23\n"
    "v_mfma_f32_16x16x32_bf16 a[12:15], a[124:127], v[80:83], a[12:15]\n"
    "v_exp_f32 v24, v24\n"
    "v_mfma_f32_16x16x32_bf16 a[8:11], a[116:119], v[80:83], a[8:11]\n"
    "v_exp_f32 v25, v25\n"
    "v_mfma_f32_16x16x32_bf16 a[8:11], a[112:115], v[76:79], a[8:11]\n"
    "v_exp_f32 v26, v26\n"
    "v_mfma_f32_16x16x32_bf16 a[24:27], a[112:115], v[84:87], a[24:27]\n"
    "v_mfma_f32_16x16x32_bf16 a[24:27], a[116:119], v[88:91], a[24:27]\n"
    "v_exp_f32 v27, v27\n"
    "v_mfma_f32_16x16x32_bf16 a[28:31], a[124:127], v[88:91], a[28:31]\n"
    "v_mfma_f32_16x16x32_bf16 a[28:31], a[120:123], v[84:87], a[28:31]\n"
    "v_mfma_f32_16x16x32_bf16 a[80:83], a[128:131], v[84:87], a[80:83]\n"
    "v_exp_f32 v28, v28\n"
    "v_mfma_f32_16x16x32_bf16 a[80:83], a[132:135], v[88:91], a[80:83]\n"
    "v_mfma_f32_16x16x32_bf16 a[96:99], a[132:135], v[96:99], a[96:99]\n"
    "v_mfma_f32_16x16x32_bf16 a[96:99], a[128:131], v[92:95], a[96:99]\n"
    "v_exp_f32 v29, v29\n"
    "v_mfma_f32_16x16x32_bf16 a[44:47], a[120:123], v[92:95], a[44:47]\n"
    "v_mfma_f32_16x16x32_bf16 a[44:47], a[124:127], v[96:99], a[44:47]\n"
    "v_exp_f32 v30, v30\n"
    "v_mfma_f32_16x16x32_bf16 a[40:43], a[116:119], v[96:99], a[40:43]\n"
    "v_mfma_f32_16x16x32_bf16 a[40:43], a[112:115], v[92:95], a[40:43]\n"
    "s_waitcnt lgkmcnt(0)\n"
    "v_mfma_f32_16x16x32_bf16 a[52:55], a[136:139], v[68:71], a[52:55]\n"
    "ds_read_b128 a[112:115], v3 offset:12288\n"
    "v_mfma_f32_16x16x32_bf16 a[52:55], a[140:143], v[72:75], a[52:55]\n"
    "ds_read_b128 a[116:119], v3 offset:14336\n"
    "v_mfma_f32_16x16x32_bf16 a[56:59], a[148:151], v[72:75], a[56:59]\n"
    "ds_read_b128 a[120:123], v3 offset:16384\n"
    "v_mfma_f32_16x16x32_bf16 a[56:59], a[144:147], v[68:71], a[56:59]\n"
    "ds_read_b128 a[124:127], v3 offset:18432\n"
    "v_mfma_f32_16x16x32_bf16 a[60:63], a[152:155], v[68:71], a[60:63]\n"
    "ds_read_b128 a[128:131], v3 offset:20480\n"
    "v_mfma_f32_16x16x32_bf16 a[60:63], a[156:159], v[72:75], a[60:63]\n"
    "ds_read_b128 a[132:135], v3 offset:22528\n"
    "v_mfma_f32_16x16x32_bf16 a[76:79], a[156:159], v[80:83], a[76:79]\n"
    "ds_read_b128 v[100:103], v250 offset:896\n"
    "v_mfma_f32_16x16x32_bf16 a[76:79], a[152:155], v[76:79], a[76:79]\n"
    "v_exp_f32 v31, v31\n"
    "v_mfma_f32_16x16x32_bf16 a[72:75], a[144:147], v[76:79], a[72:75]\n"
    "v_exp_f32 v32, v32\n"
    "v_mfma_f32_16x16x32_bf16 a[72:75], a[148:151], v[80:83], a[72:75]\n"
    "v_exp_f32 v33, v33\n"
    "v_mfma_f32_16x16x32_bf16 a[68:71], a[140:143], v[80:83], a[68:71]\n"
    "v_exp_f32 v34, v34\n"
    "v_mfma_f32_16x16x32_bf16 a[68:71], a[136:139], v[76:79], a[68:71]\n"
    "v_exp_f32 v35, v35\n"
    "v_mfma_f32_16x16x32_bf16 a[84:87], a[136:139], v[84:87], a[84:87]\n"
    "v_mfma_f32_16x16x32_bf16 a[84:87], a[140:143], v[88:91], a[84:87]\n"
    "v_mfma_f32_16x16x32_bf16 a[88:91], a[148:151], v[88:91], a[88:91]\n"
    "s_waitcnt vmcnt(18)\n"
    "ds_add_u32 v247, v246\n"
    "v_mfma_f32_16x16x32_bf16 a[88:91], a[144:147], v[84:87], a[88:91]\n"
    "v_mfma_f32_16x16x32_bf16 a[92:95], a[152:155], v[84:87], a[92:95]\n"
    "v_mfma_f32_16x16x32_bf16 a[92:95], a[156:159], v[88:91], a[92:95]\n"
    "v_mfma_f32_16x16x32_bf16 a[108:111], a[156:159], v[96:99], a[108:111]\n"
    "v_mfma_f32_16x16x32_bf16 a[108:111], a[152:155], v[92:95], a[108:111]\n"
    "v_mfma_f32_16x16x32_bf16 a[104:107], a[144:147], v[92:95], a[104:107]\n"
    "s_sub_u32 s74, s68, s65\n"
    "v_mfma_f32_16x16x32_bf16 a[104:107], a[148:151], v[96:99], a[104:107]\n"
    "s_add_u32 s66, s66, 1\n"
    "v_mov_b32 v2, v3\n"
    "v_add_u32 v3, s74, v252\n"
    "v_mfma_f32_16x16x32_bf16 a[100:103], a[140:143], v[96:99], a[100:103]\n"
    "s_waitcnt lgkmcnt(1)\n"
    "s_cmp_eq_u32 s66, 29\n"
    "v_mfma_f32_16x16x32_bf16 a[100:103], a[136:139], v[92:95], a[100:103]\n"
    ".LwKe:\n"
    "s_cbranch_scc1 .LtailP\n"
    ".LNK:\n"
    "v_mfma_f32_16x16x32_bf16 v[36:39], a[112:115], a[192:195], v[100:103]\n"
    "ds_read_b128 a[136:139], v2 offset:13312\n"
    "v_cvt_pk_bf16_f32 v68, v4, v5\n"
    "v_mfma_f32_16x16x32_bf16 v[36:39], a[116:119], a[196:199], v[36:39]\n"
    "ds_read_b128 a[140:143], v2 offset:15360\n"
    "v_mfma_f32_16x16x32_bf16 v[36:39], a[120:123], a[200:203], v[36:39]\n"
    "ds_read_b128 a[144:147], v2 offset:17408\n"
    "v_mfma_f32_16x16x32_bf16 v[36:39], a[124:127], a[204:207], v[36:39]\n"
    "ds_read_b128 a[148:151], v2 offset:19456\n"
    "v_cvt_pk_bf16_f32 v69, v6, v7\n"
    "v_mfma_f32_16x16x32_bf16 v[36:39], a[128:131], a[208:211], v[36:39]\n"
    "ds_read_b128 a[152:155], v2 offset:21504\n"
    "v_mfma_f32_16x16x32_bf16 v[36:39], a[132:135], a[212:215], v[36:39]\n"
    "ds_read_b128 a[156:159], v2 offset:23552\n"
    "v_mfma_f32_16x16x32_bf16 v[40:43], a[132:135], a[236:239], v[100:103]\n"
    "ds_read_b128 v[104:107], v250 offset:912\n"
    "v_mfma_f32_16x16x32_bf16 v[40:43], a[128:131], a[232:235], v[40:43]\n"
    "s_add_u32 s68, s68, 24576\n"
    "v_cvt_pk_bf16_f32 v76, v8, v9\n"
    "v_mfma_f32_16x16x32_bf16 v[40:43], a[124:127], a[228:231], v[40:43]\n"
    "s_cmp_ge_u32 s68, s69\n"
    "s_cselect_b32 s68, s65, s68\n"
    "v_mfma_f32_16x16x32_bf16 v[40:43], a[120:123], a[224:227], v[40:43]\n"
    "v_exp_f32 v36, v36\n"
    "v_mfma_f32_16x16x32_bf16 v[40:43], a[116:119], a[220:223], v[40:43]\n"
    "v_exp_f32 v37, v37\n"
    "v_mfma_f32_16x16x32_bf16 v[40:43], a[112:115], a[216:219], v[40:43]\n"
    "v_cvt_pk_bf16_f32 v77, v10, v11\n"
    "v_cvt_pk_bf16_f32 v72, v36, v37\n"
    "v_mfma_f32_16x16x32_bf16 v[44:47], a[112:115], a[240:243], v[100:103]\n"
    "v_mfma_f32_16x16x32_bf16 v[44:47], a[116:119], a[244:247], v[44:47]\n"
    "v_exp_f32 v38, v38\n"
    "v_mfma_f32_16x16x32_bf16 v[44:47], a[120:123], a[248:251], v[44:47]\n"
    "v_mfma_f32_16x16x32_bf16 v[44:47], a[124:127], a[252:255], v[44:47]\n"
    "v_cvt_pk_bf16_f32 v84, v12, v13\n"
    "v_mfma_f32_16x16x32_bf16 v[44:47], a[128:131], a[160:163], v[44:47]\n"
    "v_exp_f32 v39, v39\n"
    "v_mfma_f32_16x16x32_bf16 v[44:47], a[132:135], a[164:167], v[44:47]\n"
    "v_cvt_pk_bf16_f32 v73, v38, v39\n"
    "v_mfma_f32_16x16x32_bf16 v[48:51], a[132:135], a[188:191], v[100:103]\n"
    "ds_read_b32 v245, v247\n"
    "v_cvt_pk_bf16_f32 v85, v14, v15\n"
    "v_mfma_f32_16x16x32_bf16 v[48:51], a[128:131], a[184:187], v[48:51]\n"
    "s_add_u32 m0, s68, 0\n"
    "v_exp_f32 v40, v40\n"
    "v_mfma_f32_16x16x32_bf16 v[48:51], a[124:127], a[180:183], v[48:51]\n"
    "v_mfma_f32_16x16x32_bf16 v[48:51], a[120:123], a[176:179], v[48:51]\n"
    "v_mfma_f32_16x16x32_bf16 v[48:51], a[116:119], a[172:175], v[48:51]\n"
    "s_waitcnt vmcnt(0)\n"
    "v_cmp_eq_u64 s[54:55], v[236:237], v[238:239]\n"
    "v_cvt_pk_bf16_f32 v92, v16, v17\n"
    "v_mfma_f32_16x16x32_bf16 v[48:51], a[112:115], a[168:171], v[48:51]\n"
    "s_waitcnt lgkmcnt(1)\n"
    "v_exp_f32 v41, v41\n"
    "v_mfma_f32_16x16x32_bf16 v[52:55], a[136:139], a[192:195], v[104:107]\n"
    "s_waitcnt lgkmcnt(0)\n"
    "v_cmp_le_u32 s[56:57], s59, v245\n"
    "v_cvt_pk_bf16_f32 v80, v40, v41\n"
    "v_mfma_f32_16x16x32_bf16 v[52:55], a[140:143], a[196:199], v[52:55]\n"
    "s_waitcnt vmcnt(1)\n"
    "v_exp_f32 v42, v42\n"
    "v_mfma_f32_16x16x32_bf16 v[52:55], a[144:147], a[200:203], v[52:55]\n"
    "s_and_b64 s[54:55], s[54:55], s[56:57]\n"
    "s_cmp_eq_u64 s[54:55], -1\n"
    "v_mfma_f32_16x16x32_bf16 v[52:55], a[148:151], a[204:207], v[52:55]\n"
    "s_cbranch_scc0 .Lpoll2\n"
    ".Lpoll2r:\n"
    "v_mfma_f32_16x16x32_bf16 v[52:55], a[152:155], a[208:211], v[52:55]\n"
    "buffer_load_dwordx4 v1, s[16:19], s67 offen offset:0 sc1 lds\n"
    "v_mfma_f32_16x16x32_bf16 v[52:55], a[156:159], a[212:215], v[52:55]\n"
    "s_add_u32 s76, s71, s60\n"
    "ds_read_b128 a[112:115], v3 offset:0\n"
    "v_cvt_pk_bf16_f32 v93, v18, v19\n"
    "v_mfma_f32_16x16x32_bf16 v[56:59], a[156:159], a[236:239], v[104:107]\n"
    "s_add_u32 s77, s71, s61\n"
    "ds_read_b128 a[116:119], v3 offset:2048\n"
    "v_cvt_pk_bf16_f32 v70, v20, v21\n"
    "v_mfma_f32_16x16x32_bf16 v[56:59], a[152:155], a[232:235], v[56:59]\n"
    "ds_read_b128 a[120:123], v3 offset:4096\n"
    "v_mfma_f32_16x16x32_bf16 v[56:59], a[148:151], a[228:231], v[56:59]\n"
    "buffer_load_dwordx4 v1, s[16:19], s67 offen offset:1024 sc1 lds\n"
    "v_mfma_f32_16x16x32_bf16 v[56:59], a[144:147], a[224:227], v[56:59]\n"
    "ds_read_b128 a[124:127], v3 offset:6144\n"
    "v_cvt_pk_bf16_f32 v71, v22, v23\n"
    "v_mfma_f32_16x16x32_bf16 v[56:59], a[140:143], a[220:223], v[56:59]\n"
    "ds_read_b128 a[128:131], v3 offset:8192\n"
    "v_mfma_f32_16x16x32_bf16 v[56:59], a[136:139], a[216:219], v[56:59]\n"
    "ds_read_b128 a[132:135], v3 offset:10240\n"
    "v_mfma_f32_16x16x32_bf16 v[60:63], a[136:139], a[240:243], v[104:107]\n"
    "buffer_load_dwordx4 v1, s[16:19], s67 offen offset:2048 sc1 lds\n"
    "v_mfma_f32_16x16x32_bf16 v[60:63], a[140:143], a[244:247], v[60:63]\n"
    "ds_read_b128 v[100:103], v250 offset:1536\n"
    "v_cvt_pk_bf16_f32 v78, v24, v25\n"
    "v_mfma_f32_16x16x32_bf16 v[60:63], a[144:147], a[248:251], v[60:63]\n"
    "v_exp_f32 v43, v43\n"
    "v_mfma_f32_16x16x32_bf16 v[60:63], a[148:151], a[252:255], v[60:63]\n"
    "ds_read_b128 v[36:39], v243 offset:0\n"
    "v_cvt_pk_bf16_f32 v81, v42, v43\n"
    "v_mfma_f32_16x16x32_bf16 v[60:63], a[152:155], a[160:163], v[60:63]\n"
    "buffer_load_dwordx4 v1, s[16:19], s67 offen offset:3072 sc1 lds\n"
    "v_mfma_f32_16x16x32_bf16 v[60:63], a[156:159], a[164:167], v[60:63]\n"
    "s_add_u32 m0, s68, 4096\n"
    "v_cvt_pk_bf16_f32 v79, v26, v27\n"
    "v_add_u32 v243, 512, v243\n"
    "v_mfma_f32_16x16x32_bf16 v[64:67], a[156:159], a[188:191], v[104:107]\n"
    "s_add_u32 s83, s67, 4096\n"
    "v_exp_f32 v44, v44\n"
    "v_mfma_f32_16x16x32_bf16 v[64:67], a[152:155], a[184:187], v[64:67]\n"
    "v_exp_f32 v45, v45\n"
    "v_mfma_f32_16x16x32_bf16 v[64:67], a[148:151], a[180:183], v[64:67]\n"
    "buffer_load_dwordx4 v1, s[16:19], s83 offen offset:0 sc1 lds\n"
    "v_mfma_f32_16x16x32_bf16 v[64:67], a[144:147], a[176:179], v[64:67]\n"
    "v_cvt_pk_bf16_f32 v86, v28, v29\n"
    "v_cvt_pk_bf16_f32 v88, v44, v45\n"
    "v_mfma_f32_16x16x32_bf16 v[64:67], a[140:143], a[172:175], v[64:67]\n"
    "v_exp_f32 v46, v46\n"
    "v_mfma_f32_16x16x32_bf16 v[64:67], a[136:139], a[168:171], v[64:67]\n"
    "s_waitcnt lgkmcnt(1)\n"
    "v_exp_f32 v47, v47\n"
    "v_mfma_f32_16x16x32_bf16 v[4:7], a[112:115], a[192:195], v[100:103]\n"
    "buffer_load_dwordx4 v1, s[16:19], s83 offen offset:1024 sc1 lds\n"
    "v_mfma_f32_16x16x32_bf16 v[4:7], a[116:119], a[196:199], v[4:7]\n"
    "s_add_u32 m0, s70, .Lend-.Lend2+(0)\n"
    "ds_read_b128 a[136:139], v3 offset:1024\n"
    "v_cvt_pk_bf16_f32 v87, v30, v31\n"
    "v_mfma_f32_16x16x32_bf16 v[4:7], a[120:123], a[200:203], v[4:7]\n"
    "s_add_u32 s83, s67, 24576\n"
    "ds_read_b128 a[140:143], v3 offset:3072\n"
    "v_cvt_pk_bf16_f32 v89, v46, v47\n"
    "v_mfma_f32_16x16x32_bf16 v[4:7], a[124:127], a[204:207], v[4:7]\n"
    "s_waitcnt lgkmcnt(2)\n"
    "ds_read_b128 a[144:147], v3 offset:5120\n"
    "v_fma_f32 v204, v204, v36, -v38\n"
    "v_mfma_f32_16x16x32_bf16 v[4:7], a[128:131], a[208:211], v[4:7]\n"
    "buffer_load_dwordx4 v1, s[16:19], s83 offen offset:0 sc1 lds\n"
    "v_mfma_f32_16x16x32_bf16 v[4:7], a[132:135], a[212:215], v[4:7]\n"
    "ds_read_b128 a[148:151], v3 offset:7168\n"
    "v_cvt_pk_bf16_f32 v94, v32, v33\n"
    "v_mfma_f32_16x16x32_bf16 v[8:11], a[132:135], a[236:239], v[100:103]\n"
    "ds_read_b128 a[152:155], v3 offset:9216\n"
    "v_fma_f32 v205, v205, v37, -v39\n"
    "v_mfma_f32_16x16x32_bf16 v[8:11], a[128:131], a[232:235], v[8:11]\n"
    "ds_read_b128 a[156:159], v3 offset:11264\n"
    "v_fma_f32 v206, v206, v36, -v38\n"
    "v_mfma_f32_16x16x32_bf16 v[8:11], a[124:127], a[228:231], v[8:11]\n"
    "buffer_load_dwordx4 v1, s[16:19], s83 offen offset:1024 sc1 lds\n"
    "v_mfma_f32_16x16x32_bf16 v[8:11], a[120:123], a[224:227], v[8:11]\n"
    "ds_read_b128 v[104:107], v250 offset:1552\n"
    "v_cvt_pk_bf16_f32 v95, v34, v35\n"
    "v_mfma_f32_16x16x32_bf16 v[8:11], a[116:119], a[220:223], v[8:11]\n"
    "v_exp_f32 v52, v52\n"
    "v_mfma_f32_16x16x32_bf16 v[8:11], a[112:115], a[216:219], v[8:11]\n"
    "v_exp_f32 v53, v53\n"
    "v_mfma_f32_16x16x32_bf16 v[12:15], a[112:115], a[240:243], v[100:103]\n"
    "buffer_load_dwordx4 v1, s[16:19], s83 offen offset:2048 sc1 lds\n"
    "v_mfma_f32_16x16x32_bf16 v[12:15], a[116:119], a[244:247], v[12:15]\n"
    "v_exp_f32 v54, v54\n"
    "v_mfma_f32_16x16x32_bf16 v[12:15], a[120:123], a[248:251], v[12:15]\n"
    "v_exp_f32 v55, v55\n"
    "v_mfma_f32_16x16x32_bf16 v[12:15], a[124:127], a[252:255], v[12:15]\n"
    "v_cvt_pk_bf16_f32 v74, v52, v53\n"
    "v_cvt_pk_bf16_f32 v75, v54, v55\n"
    "v_mfma_f32_16x16x32_bf16 v[12:15], a[128:131], a[160:163], v[12:15]\n"
    "buffer_load_dwordx4 v1, s[16:19], s83 offen offset:3072 sc1 lds\n"
    "v_mfma_f32_16x16x32_bf16 v[12:15], a[132:135], a[164:167], v[12:15]\n"
    "s_add_u32 m0, s70, 4096\n"
    "v_exp_f32 v56, v56\n"
    "v_mfma_f32_16x16x32_bf16 v[16:19], a[132:135], a[188:191], v[100:103]\n"
    "s_add_u32 s83, s67, 28672\n"
    "v_exp_f32 v48, v48\n"
    "v_mfma_f32_16x16x32_bf16 v[16:19], a[128:131], a[184:187], v[16:19]\n"
    "v_exp_f32 v49, v49\n"
    "v_mfma_f32_16x16x32_bf16 v[16:19], a[124:127], a[180:183], v[16:19]\n"
    "buffer_load_dwordx4 v1, s[16:19], s83 offen offset:0 sc1 lds\n"
    "v_mfma_f32_16x16x32_bf16 v[16:19], a[120:123], a[176:179], v[16:19]\n"
    "v_exp_f32 v57, v57\n"
    "v_mfma_f32_16x16x32_bf16 v[16:19], a[116:119], a[172:175], v[16:19]\n"
    "v_cvt_pk_bf16_f32 v82, v56, v57\n"
    "v_cvt_pk_bf16_f32 v96, v48, v49\n"
    "v_mfma_f32_16x16x32_bf16 v[16:19], a[112:115], a[168:171], v[16:19]\n"
    "s_waitcnt lgkmcnt(0)\n"
    "v_exp_f32 v58, v58\n"
    "v_mfma_f32_16x16x32_bf16 v[20:23], a[136:139], a[192:195], v[104:107]\n"
    "buffer_load_dwordx4 v1, s[16:19], s83 offen offset:1024 sc1 lds\n"
    "v_mfma_f32_16x16x32_bf16 v[20:23], a[140:143], a[196:199], v[20:23]\n"
    "s_add_u32 s67, s67, 49152\n"
    "ds_read_b128 a[112:115], v249 offset:0\n"
    "v_fma_f32 v207, v207, v37, -v39\n"
    "v_mfma_f32_16x16x32_bf16 v[20:23], a[144:147], a[200:203], v[20:23]\n"
    "s_add_u32 s59, s59, 4\n"
    "ds_read_b128 a[116:119], v249 offset:12288\n"
    "v_cvt_pk_bf16_f32 v204, v204, v205\n"
    "v_mfma_f32_16x16x32_bf16 v[20:23], a[148:151], a[204:207], v[20:23]\n"
    "ds_read_b128 a[120:123], v249 offset:1024\n"
    "v_cvt_pk_bf16_f32 v206, v206, v207\n"
    "v_mfma_f32_16x16x32_bf16 v[20:23], a[152:155], a[208:211], v[20:23]\n"
    "ds_read_b128 a[124:127], v249 offset:13312\n"
    "v_fma_f32 v208, v208, v36, -v38\n"
    "v_mfma_f32_16x16x32_bf16 v[20:23], a[156:159], a[212:215], v[20:23]\n"
    "ds_read_b128 a[128:131], v249 offset:2048\n"
    "v_fma_f32 v209, v209, v37, -v39\n"
    "v_mfma_f32_16x16x32_bf16 v[24:27], a[156:159], a[236:239], v[104:107]\n"
    "ds_read_b128 a[132:135], v249 offset:14336\n"
    "v_fma_f32 v210, v210, v36, -v38\n"
    "v_mfma_f32_16x16x32_bf16 v[24:27], a[152:155], a[232:235], v[24:27]\n"
    "v_exp_f32 v59, v59\n"
    "v_mfma_f32_16x16x32_bf16 v[24:27], a[148:151], a[228:231], v[24:27]\n"
    "v_cvt_pk_bf16_f32 v83, v58, v59\n"
    "v_fma_f32 v211, v211, v37, -v39\n"
    "v_mfma_f32_16x16x32_bf16 v[24:27], a[144:147], a[224:227], v[24:27]\n"
    "v_exp_f32 v60, v60\n"
    "v_mfma_f32_16x16x32_bf16 v[24:27], a[140:143], a[220:223], v[24:27]\n"
    "v_exp_f32 v61, v61\n"
    "v_mfma_f32_16x16x32_bf16 v[24:27], a[136:139], a[216:219], v[24:27]\n"
    "v_exp_f32 v62, v62\n"
    "v_mfma_f32_16x16x32_bf16 v[28:31], a[136:139], a[240:243], v[104:107]\n"
    "v_cvt_pk_bf16_f32 v90, v60, v61\n"
    "v_cvt_pk_bf16_f32 v208, v208, v209\n"
    "v_mfma_f32_16x16x32_bf16 v[28:31], a[140:143], a[244:247], v[28:31]\n"
    "v_exp_f32 v63, v63\n"
    "v_mfma_f32_16x16x32_bf16 v[28:31], a[144:147], a[248:251], v[28:31]\n"
    "v_cvt_pk_bf16_f32 v91, v62, v63\n"
    "v_cvt_pk_bf16_f32 v210, v210, v211\n"
    "v_mfma_f32_16x16x32_bf16 v[28:31], a[148:151], a[252:255], v[28:31]\n"
    "v_exp_f32 v50, v50\n"
    "v_mfma_f32_16x16x32_bf16 v[28:31], a[152:155], a[160:163], v[28:31]\n"
    "v_exp_f32 v51, v51\n"
    "v_mfma_f32_16x16x32_bf16 v[28:31], a[156:159], a[164:167], v[28:31]\n"
    "v_exp_f32 v64, v64\n"
    "v_mfma_f32_16x16x32_bf16 v[32:35], a[156:159], a[188:191], v[104:107]\n"
    "v_cvt_pk_bf16_f32 v97, v50, v51\n"
    "v_fma_f32 v212, v212, v36, -v38\n"
    "v_mfma_f32_16x16x32_bf16 v[32:35], a[152:155], a[184:187], v[32:35]\n"
    "v_exp_f32 v65, v65\n"
    "v_mfma_f32_16x16x32_bf16 v[32:35], a[148:151], a[180:183], v[32:35]\n"
    "v_cvt_pk_bf16_f32 v98, v64, v65\n"
    "v_fma_f32 v213, v213, v37, -v39\n"
    "v_mfma_f32_16x16x32_bf16 v[32:35], a[144:147], a[176:179], v[32:35]\n"
    "buffer_store_dword v204, v244, s[16:19], s76 offen offset:0 sc1\n"
    "v_mfma_f32_16x16x32_bf16 v[32:35], a[140:143], a[172:175], v[32:35]\n"
    "v_exp_f32 v66, v66\n"
    "v_mfma_f32_16x16x32_bf16 v[32:35], a[136:139], a[168:171], v[32:35]\n"
    "s_waitcnt lgkmcnt(0)\n"
    "v_fma_f32 v214, v214, v36, -v38\n"
    "v_fma_f32 v215, v215, v37, -v39\n"
    "v_mfma_f32_16x16x32_bf16 v[124:127], a[112:115], v[68:71], v[124:127]\n"
    "ds_read_b128 a[136:139], v249 offset:3072\n"
    "v_cvt_pk_bf16_f32 v212, v212, v213\n"
    "v_mfma_f32_16x16x32_bf16 v[124:127], a[116:119], v[72:75], v[124:127]\n"
    "ds_read_b128 a[140:143], v249 offset:15360\n"
    "v_cvt_pk_bf16_f32 v214, v214, v215\n"
    "v_mfma_f32_16x16x32_bf16 v[128:131], a[124:127], v[72:75], v[128:131]\n"
    "ds_read_b128 a[144:147], v249 offset:4096\n"
    "v_fma_f32 v216, v216, v36, -v38\n"
    "v_mfma_f32_16x16x32_bf16 v[128:131], a[120:123], v[68:71], v[128:131]\n"
    "ds_read_b128 a[148:151], v249 offset:16384\n"
    "v_fma_f32 v217, v217, v37, -v39\n"
    "v_mfma_f32_16x16x32_bf16 v[132:135], a[128:131], v[68:71], v[132:135]\n"
    "ds_read_b128 a[152:155], v249 offset:5120\n"
    "v_fma_f32 v218, v218, v36, -v38\n"
    "v_mfma_f32_16x16x32_bf16 v[132:135], a[132:135], v[72:75], v[132:135]\n"
    "ds_read_b128 a[156:159], v249 offset:17408\n"
    "v_fma_f32 v219, v219, v37, -v39\n"
    "v_mfma_f32_16x16x32_bf16 v[148:151], a[132:135], v[80:83], v[148:151]\n"
    "ds_read_b128 v[104:107], v251 offset:768\n"
    "v_cvt_pk_bf16_f32 v216, v216, v217\n"
    "v_mfma_f32_16x16x32_bf16 v[148:151], a[128:131], v[76:79], v[148:151]\n"
    "v_exp_f32 v67, v67\n"
    "v_mfma_f32_16x16x32_bf16 v[144:147], a[120:123], v[76:79], v[144:147]\n"
    "v_cvt_pk_bf16_f32 v99, v66, v67\n"
    "v_cvt_pk_bf16_f32 v218, v218, v219\n"
    "v_mfma_f32_16x16x32_bf16 v[144:147], a[124:127], v[80:83], v[144:147]\n"
    "buffer_store_dword v206, v244, s[16:19], s76 offen offset:128 sc1\n"
    "v_mfma_f32_16x16x32_bf16 v[140:143], a[116:119], v[80:83], v[140:143]\n"
    "v_exp_f32 v4, v4\n"
    "v_mfma_f32_16x16x32_bf16 v[140:143], a[112:115], v[76:79], v[140:143]\n"
    "buffer_store_dword v208, v244, s[16:19], s76 offen offset:1024 sc1\n"
    "v_mfma_f32_16x16x32_bf16 v[156:159], a[112:115], v[84:87], v[156:159]\n"
    "v_fma_f32 v220, v220, v36, -v38\n"
    "v_fma_f32 v221, v221, v37, -v39\n"
    "v_mfma_f32_16x16x32_bf16 v[156:159], a[116:119], v[88:91], v[156:159]\n"
    "buffer_store_dword v210, v244, s[16:19], s76 offen offset:1152 sc1\n"
    "v_mfma_f32_16x16x32_bf16 v[160:163], a[124:127], v[88:91], v[160:163]\n"
    "v_exp_f32 v5, v5\n"
    "v_mfma_f32_16x16x32_bf16 v[160:163], a[120:123], v[84:87], v[160:163]\n"
    "buffer_store_dword v212, v244, s[16:19], s76 offen offset:256 sc1\n"
    "v_mfma_f32_16x16x32_bf16 v[164:167], a[128:131], v[84:87], v[164:167]\n"
    "v_exp_f32 v6, v6\n"
    "v_mfma_f32_16x16x32_bf16 v[164:167], a[132:135], v[88:91], v[164:167]\n"
    "buffer_store_dword v214, v244, s[16:19], s76 offen offset:384 sc1\n"
    "v_mfma_f32_16x16x32_bf16 v[180:183], a[132:135], v[96:99], v[180:183]\n"
    "v_fma_f32 v222, v222, v36, -v38\n"
    "v_fma_f32 v223, v223, v37, -v39\n"
    "v_mfma_f32_16x16x32_bf16 v[180:183], a[128:131], v[92:95], v[180:183]\n"
    "v_exp_f32 v7, v7\n"
    "v_mfma_f32_16x16x32_bf16 v[176:179], a[120:123], v[92:95], v[176:179]\n"
    "v_exp_f32 v8, v8\n"
    "v_mfma_f32_16x16x32_bf16 v[176:179], a[124:127], v[96:99], v[176:179]\n"
    "v_cvt_pk_bf16_f32 v220, v220, v221\n"
    "v_cvt_pk_bf16_f32 v222, v222, v223\n"
    "v_mfma_f32_16x16x32_bf16 v[172:175], a[116:119], v[96:99], v[172:175]\n"
    "v_exp_f32 v9, v9\n"
    "v_mfma_f32_16x16x32_bf16 v[172:175], a[112:115], v[92:95], v[172:175]\n"
    "s_waitcnt lgkmcnt(0)\n"
    "v_exp_f32 v10, v10\n"
    "v_mfma_f32_16x16x32_bf16 v[136:139], a[136:139], v[68:71], v[136:139]\n"
    "ds_read_b128 a[112:115], v249 offset:6144\n"
    "v_fma_f32 v224, v224, v36, -v38\n"
    "v_mfma_f32_16x16x32_bf16 v[136:139], a[140:143], v[72:75], v[136:139]\n"
    "ds_read_b128 a[116:119], v249 offset:18432\n"
    "v_fma_f32 v225, v225, v37, -v39\n"
    "v_mfma_f32_16x16x32_bf16 v[188:191], a[148:151], v[72:75], v[188:191]\n"
    "ds_read_b128 a[120:123], v249 offset:7168\n"
    "v_fma_f32 v226, v226, v36, -v38\n"
    "v_mfma_f32_16x16x32_bf16 v[188:191], a[144:147], v[68:71], v[188:191]\n"
    "ds_read_b128 a[124:127], v249 offset:19456\n"
    "v_fma_f32 v227, v227, v37, -v39\n"
    "v_mfma_f32_16x16x32_bf16 v[192:195], a[152:155], v[68:71], v[192:195]\n"
    "ds_read_b128 a[128:131], v249 offset:8192\n"
    "v_cvt_pk_bf16_f32 v224, v224, v225\n"
    "v_mfma_f32_16x16x32_bf16 v[192:195], a[156:159], v[72:75], v[192:195]\n"
    "ds_read_b128 a[132:135], v249 offset:20480\n"
    "v_smfmac_f32_16x16x64_bf16 v[108:111], v[104:107], v[68:75], v253\n"
    "s_add_u32 s81, s81, 128\n"
    "s_cmp_gt_u32 s81, 4096\n"
    "s_cselect_b32 s81, 0, s81\n"
    "v_smfmac_f32_16x16x64_bf16 v[112:115], v[104:107], v[76:83], v253\n"
    "v_mfma_f32_16x16x32_bf16 a[4:7], a[156:159], v[80:83], a[4:7]\n"
    "v_exp_f32 v11, v11\n"
    "v_mfma_f32_16x16x32_bf16 a[4:7], a[152:155], v[76:79], a[4:7]\n"
    "v_exp_f32 v12, v12\n"
    "v_mfma_f32_16x16x32_bf16 a[0:3], a[144:147], v[76:79], a[0:3]\n"
    "v_exp_f32 v13, v13\n"
    "v_mfma_f32_16x16x32_bf16 a[0:3], a[148:151], v[80:83], a[0:3]\n"
    "v_exp_f32 v14, v14\n"
    "v_mfma_f32_16x16x32_bf16 v[152:155], a[140:143], v[80:83], v[152:155]\n"
    "v_exp_f32 v15, v15\n"
    "v_mfma_f32_16x16x32_bf16 v[152:155], a[136:139], v[76:79], v[152:155]\n"
    "v_cvt_pk_bf16_f32 v226, v226, v227\n"
    "v_fma_f32 v228, v228, v36, -v38\n"
    "v_mfma_f32_16x16x32_bf16 v[168:171], a[136:139], v[84:87], v[168:171]\n"
    "v_exp_f32 v16, v16\n"
    "v_mfma_f32_16x16x32_bf16 v[168:171], a[140:143], v[88:91], v[168:171]\n"
    "v_exp_f32 v17, v17\n"
    "v_mfma_f32_16x16x32_bf16 a[16:19], a[148:151], v[88:91], a[16:19]\n"
    "v_fma_f32 v229, v229, v37, -v39\n"
    "v_fma_f32 v230, v230, v36, -v38\n"
    "v_mfma_f32_16x16x32_bf16 a[16:19], a[144:147], v[84:87], a[16:19]\n"
    "v_exp_f32 v18, v18\n"
    "v_mfma_f32_16x16x32_bf16 a[20:23], a[152:155], v[84:87], a[20:23]\n"
    "v_fma_f32 v231, v231, v37, -v39\n"
    "v_mfma_f32_16x16x32_bf16 a[20:23], a[156:159], v[88:91], a[20:23]\n"
    "v_smfmac_f32_16x16x64_bf16 v[116:119], v[104:107], v[84:91], v253\n"
    "v_smfmac_f32_16x16x64_bf16 v[120:123], v[104:107], v[92:99], v253\n"
    "v_mfma_f32_16x16x32_bf16 a[36:39], a[156:159], v[96:99], a[36:39]\n"
    "v_exp_f32 v19, v19\n"
    "v_mfma_f32_16x16x32_bf16 a[36:39], a[152:155], v[92:95], a[36:39]\n"
    "v_exp_f32 v20, v20\n"
    "v_mfma_f32_16x16x32_bf16 a[32:35], a[144:147], v[92:95], a[32:35]\n"
    "v_cvt_pk_bf16_f32 v228, v228, v229\n"
    "v_cvt_pk_bf16_f32 v230, v230, v231\n"
    "v_mfma_f32_16x16x32_bf16 a[32:35], a[148:151], v[96:99], a[32:35]\n"
    "v_exp_f32 v21, v21\n"
    "v_mfma_f32_16x16x32_bf16 v[184:187], a[140:143], v[96:99], v[184:187]\n"
    "v_fma_f32 v232, v232, v36, -v38\n"
    "v_mfma_f32_16x16x32_bf16 v[184:187], a[136:139], v[92:95], v[184:187]\n"
    "s_waitcnt lgkmcnt(0)\n"
    "v_mfma_f32_16x16x32_bf16 v[196:199], a[112:115], v[68:71], v[196:199]\n"
    "ds_read_b128 a[136:139], v249 offset:9216\n"
    "v_fma_f32 v233, v233, v37, -v39\n"
    "v_mfma_f32_16x16x32_bf16 v[196:199], a[116:119], v[72:75], v[196:199]\n"
    "ds_read_b128 a[140:143], v249 offset:21504\n"
    "v_mfma_f32_16x16x32_bf16 v[200:203], a[124:127], v[72:75], v[200:203]\n"
    "ds_read_b128 a[144:147], v249 offset:10240\n"
    "v_mfma_f32_16x16x32_bf16 v[200:203], a[120:123], v[68:71], v[200:203]\n"
    "ds_read_b128 a[148:151], v249 offset:22528\n"
    "v_fma_f32 v234, v234, v36, -v38\n"
    "v_mfma_f32_16x16x32_bf16 a[48:51], a[128:131], v[68:71], a[48:51]\n"
    "buffer_load_dwordx2 v[236:237], v241, s[36:39], s81 offen offset:0 sc1\n"
    "v_mfma_f32_16x16x32_bf16 a[48:51], a[132:135], v[72:75], a[48:51]\n"
    "ds_read_b128 a[152:155], v249 offset:11264\n"
    "v_fma_f32 v235, v235, v37, -v39\n"
    "v_mfma_f32_16x16x32_bf16 a[64:67], a[132:135], v[80:83], a[64:67]\n"
    "ds_read_b128 a[156:159], v249 offset:23552\n"
    "v_mfma_f32_16x16x32_bf16 a[64:67], a[128:131], v[76:79], a[64:67]\n"
    "buffer_store_dword v216, v244, s[16:19], s76 offen offset:1280 sc1\n"
    "v_mfma_f32_16x16x32_bf16 a[12:15], a[120:123], v[76:79], a[12:15]\n"
    "v_exp_f32 v22, v22\n"
    "v_mfma_f32_16x16x32_bf16 a[12:15], a[124:127], v[80:83], a[12:15]\n"
    "buffer_store_dword v218, v244, s[16:19], s76 offen offset:1408 sc1\n"
    "v_mfma_f32_16x16x32_bf16 a[8:11], a[116:119], v[80:83], a[8:11]\n"
    "v_exp_f32 v23, v23\n"
    "v_mfma_f32_16x16x32_bf16 a[8:11], a[112:115], v[76:79], a[8:11]\n"
    "buffer_store_dword v220, v244, s[16:19], s77 offen offset:0 sc1\n"
    "v_mfma_f32_16x16x32_bf16 a[24:27], a[112:115], v[84:87], a[24:27]\n"
    "v_exp_f32 v24, v24\n"
    "v_mfma_f32_16x16x32_bf16 a[24:27], a[116:119], v[88:91], a[24:27]\n"
    "buffer_store_dword v222, v244, s[16:19], s77 offen offset:128 sc1\n"
    "v_mfma_f32_16x16x32_bf16 a[28:31], a[124:127], v[88:91], a[28:31]\n"
    "v_exp_f32 v25, v25\n"
    "v_mfma_f32_16x16x32_bf16 a[28:31], a[120:123], v[84:87], a[28:31]\n"
    "buffer_store_dword v224, v244, s[16:19], s77 offen offset:1024 sc1\n"
    "v_mfma_f32_16x16x32_bf16 a[80:83], a[128:131], v[84:87], a[80:83]\n"
    "v_exp_f32 v26, v26\n"
    "v_mfma_f32_16x16x32_bf16 a[80:83], a[132:135], v[88:91], a[80:83]\n"
    "buffer_store_dword v226, v244, s[16:19], s77 offen offset:1152 sc1\n"
    "v_mfma_f32_16x16x32_bf16 a[96:99], a[132:135], v[96:99], a[96:99]\n"
    "v_cvt_pk_bf16_f32 v232, v232, v233\n"
    "v_cvt_pk_bf16_f32 v234, v234, v235\n"
    "v_mfma_f32_16x16x32_bf16 a[96:99], a[128:131], v[92:95], a[96:99]\n"
    "buffer_store_dword v228, v244, s[16:19], s77 offen offset:256 sc1\n"
    "v_mfma_f32_16x16x32_bf16 a[44:47], a[120:123], v[92:95], a[44:47]\n"
    "v_exp_f32 v27, v27\n"
    "v_mfma_f32_16x16x32_bf16 a[44:47], a[124:127], v[96:99], a[44:47]\n"
    "buffer_store_dword v230, v244, s[16:19], s77 offen offset:384 sc1\n"
    "v_mfma_f32_16x16x32_bf16 a[40:43], a[116:119], v[96:99], a[40:43]\n"
    "v_exp_f32 v28, v28\n"
    "v_mfma_f32_16x16x32_bf16 a[40:43], a[112:115], v[92:95], a[40:43]\n"
    "s_waitcnt lgkmcnt(0)\n"
    "v_exp_f32 v29, v29\n"
    "v_mfma_f32_16x16x32_bf16 a[52:55], a[136:139], v[68:71], a[52:55]\n"
    "ds_read_b128 a[112:115], v3 offset:12288\n"
    "v_mfma_f32_16x16x32_bf16 a[52:55], a[140:143], v[72:75], a[52:55]\n"
    "ds_read_b128 a[116:119], v3 offset:14336\n"
    "v_mfma_f32_16x16x32_bf16 a[56:59], a[148:151], v[72:75], a[56:59]\n"
    "ds_read_b128 a[120:123], v3 offset:16384\n"
    "v_mfma_f32_16x16x32_bf16 a[56:59], a[144:147], v[68:71], a[56:59]\n"
    "ds_read_b128 a[124:127], v3 offset:18432\n"
    "v_mfma_f32_16x16x32_bf16 a[60:63], a[152:155], v[68:71], a[60:63]\n"
    "ds_read_b128 a[128:131], v3 offset:20480\n"
    "v_mfma_f32_16x16x32_bf16 a[60:63], a[156:159], v[72:75], a[60:63]\n"
    "ds_read_b128 a[132:135], v3 offset:22528\n"
    "v_mfma_f32_16x16x32_bf16 a[76:79], a[156:159], v[80:83], a[76:79]\n"
    "ds_read_b128 v[100:103], v250 offset:1664\n"
    "v_mfma_f32_16x16x32_bf16 a[76:79], a[152:155], v[76:79], a[76:79]\n"
    "buffer_store_dword v232, v244, s[16:19], s77 offen offset:1280 sc1\n"
    "v_mfma_f32_16x16x32_bf16 a[72:75], a[144:147], v[76:79], a[72:75]\n"
    "v_exp_f32 v30, v30\n"
    "v_mfma_f32_16x16x32_bf16 a[72:75], a[148:151], v[80:83], a[72:75]\n"
    "buffer_store_dword v234, v244, s[16:19], s77 offen offset:1408 sc1\n"
    "v_mfma_f32_16x16x32_bf16 a[68:71], a[140:143], v[80:83], a[68:71]\n"
    "v_exp_f32 v31, v31\n"
    "v_mfma_f32_16x16x32_bf16 a[68:71], a[136:139], v[76:79], a[68:71]\n"
    "v_exp_f32 v32, v32\n"
    "v_mfma_f32_16x16x32_bf16 a[84:87], a[136:139], v[84:87], a[84:87]\n"
    "v_exp_f32 v33, v33\n"
    "v_mfma_f32_16x16x32_bf16 a[84:87], a[140:143], v[88:91], a[84:87]\n"
    "v_exp_f32 v34, v34\n"
    "v_mfma_f32_16x16x32_bf16 a[88:91], a[148:151], v[88:91], a[88:91]\n"
    "s_waitcnt vmcnt(17)\n"
    "ds_add_u32 v247, v246\n"
    "v_mfma_f32_16x16x32_bf16 a[88:91], a[144:147], v[84:87], a[88:91]\n"
    "v_exp_f32 v35, v35\n"
    "v_mfma_f32_16x16x32_bf16 a[92:95], a[152:155], v[84:87], a[92:95]\n"
    "v_mfma_f32_16x16x32_bf16 a[92:95], a[156:159], v[88:91], a[92:95]\n"
    "v_mfma_f32_16x16x32_bf16 a[108:111], a[156:159], v[96:99], a[108:111]\n"
    "v_mfma_f32_16x16x32_bf16 a[108:111], a[152:155], v[92:95], a[108:111]\n"
    "v_mfma_f32_16x16x32_bf16 a[104:107], a[144:147], v[92:95], a[104:107]\n"
    "s_sub_u32 s74, s68, s65\n"
    "v_add_u32 v250, 1536, v250\n"
    "v_add_u32 v251, 1536, v251\n"
    "v_mfma_f32_16x16x32_bf16 a[104:107], a[148:151], v[96:99], a[104:107]\n"
    "s_add_u32 s66, s66, 1\n"
    "v_mov_b32 v2, v3\n"
    "v_add_u32 v3, s74, v252\n"
    "v_mfma_f32_16x16x32_bf16 a[100:103], a[140:143], v[96:99], a[100:103]\n"
    "s_waitcnt lgkmcnt(1)\n"
    "s_cmp_lt_u32 s66, 29\n"
    "v_mfma_f32_16x16x32_bf16 a[100:103], a[136:139], v[92:95], a[100:103]\n"
    "s_cbranch_scc1 .LPK\n"
    "s_branch .Ltail\n"
    "s_nop 0\n"
    ".LpollAK:\n"
    "s_sleep 1\n"
    "buffer_load_dwordx2 v[236:237], v87, s[36:39], 0 offen offset:0 sc1\n"
    "s_waitcnt vmcnt(0)\n"
    "v_cmp_eq_u64 s[54:55], v[236:237], v[238:239]\n"
    "s_add_u32 s58, s58, 1\n"
    "s_cmp_eq_u64 s[54:55], -1\n"
    "s_cbranch_scc1 .LpollAKr\n"
    "s_cmp_lt_u32 s58, 0x80000\n"
    "s_cbranch_scc1 .LpollAK\n"
    "s_branch .LpollAKr\n"
    "s_nop 0\n"
    "s_nop 0\n"
    "s_nop 0\n"
    ".LpollAV:\n"
    "s_sleep 1\n"
    "buffer_load_dwordx2 v[236:237], v87, s[36:39], 0 offen offset:0 sc1\n"
    "s_waitcnt vmcnt(0)\n"
    "v_cmp_eq_u64 s[54:55], v[236:237], v[238:239]\n"
    "s_add_u32 s58, s58, 1\n"
    "s_cmp_eq_u64 s[54:55], -1\n"
    "s_cbranch_scc1 .LpollAVr\n"
    "s_cmp_lt_u32 s58, 0x80000\n"
    "s_cbranch_scc1 .LpollAV\n"
    "s_branch .LpollAVr\n"
    "s_nop 0\n"
    "s_nop 0\n"
    "s_nop 0\n"
    "s_nop 0\n"
    "s_nop 0\n"
    "s_nop 0\n"
    "s_nop 0\n"
    ".Lpoll1:\n"
    "s_sleep 1\n"
    "v_cmp_eq_u64 s[54:55], v[236:237], v[238:239]\n"
    "s_nop 1\n"
    "s_cmp_eq_u64 s[54:55], -1\n"
    "s_cbranch_scc1 .Lpoll1l\n"
    "buffer_load_dwordx2 v[236:237], v241, s[36:39], s81 offen offset:0 sc1\n"
    "s_waitcnt vmcnt(0)\n"
    "v_cmp_eq_u64 s[54:55], v[236:237], v[238:239]\n"
    ".Lpoll1l:\n"
    "ds_read_b32 v245, v247\n"
    "s_waitcnt lgkmcnt(0)\n"
    "v_cmp_le_u32 s[56:57], s59, v245\n"
    "s_add_u32 s58, s58, 1\n"
    "s_nop 1\n"
    "s_and_b64 s[54:55], s[54:55], s[56:57]\n"
    "s_cmp_eq_u64 s[54:55], -1\n"
    "s_cbranch_scc1 .Lpoll1r\n"
    "s_cmp_lt_u32 s58, 0x80000\n"
    "s_cbranch_scc1 .Lpoll1\n"
    "s_branch .Lpoll1r\n"
    "s_nop 0\n"
    "s_nop 0\n"
    "s_nop 0\n"
    "s_nop 0\n"
    "s_nop 0\n"
    "s_nop 0\n"
    "s_nop 0\n"
    ".Lpoll2:\n"
    "s_sleep 1\n"
    "v_cmp_eq_u64 s[54:55], v[236:237], v[238:239]\n"
    "s_nop 1\n"
    "s_cmp_eq_u64 s[54:55], -1\n"
    "s_cbranch_scc1 .Lpoll2l\n"
    "buffer_load_dwordx2 v[236:237], v241, s[36:39], s81 offen offset:0 sc1\n"
    "s_waitcnt vmcnt(0)\n"
    "v_cmp_eq_u64 s[54:55], v[236:237], v[238:239]\n"
    ".Lpoll2l:\n"
    "ds_read_b32 v245, v247\n"
    "s_waitcnt lgkmcnt(0)\n"
    "v_cmp_le_u32 s[56:57], s59, v245\n"
    "s_add_u32 s58, s58, 1\n"
    "s_nop 1\n"
    "s_and_b64 s[54:55], s[54:55], s[56:57]\n"
    "s_cmp_eq_u64 s[54:55], -1\n"
    "s_cbranch_scc1 .Lpoll2r\n"
    "s_cmp_lt_u32 s58, 0x80000\n"
    "s_cbranch_scc1 .Lpoll2\n"
    "s_branch .Lpoll2r\n"
    "s_nop 0\n"
    "s_nop 0\n"
    "s_nop 0\n"
    "s_nop 0\n"
    "s_nop 0\n"
    "s_nop 0\n"
    "s_nop 0\n"
    ".Lpoll3:\n"
    "s_sleep 1\n"
    "v_cmp_eq_u64 s[54:55], v[236:237], v[238:239]\n"
    "s_nop 1\n"
    "s_cmp_eq_u64 s[54:55], -1\n"
    "s_cbranch_scc1 .Lpoll3l\n"
    "buffer_load_dwordx2 v[236:237], v241, s[36:39], s81 offen offset:0 sc1\n"
    "s_waitcnt vmcnt(0)\n"
    "v_cmp_eq_u64 s[54:55], v[236:237], v[238:239]\n"
    ".Lpoll3l:\n"
    "ds_read_b32 v245, v247\n"
    "s_waitcnt lgkmcnt(0)\n"
    "v_cmp_le_u32 s[56:57], s59, v245\n"
    "s_add_u32 s58, s58, 1\n"
    "s_nop 1\n"
    "s_and_b64 s[54:55], s[54:55], s[56:57]\n"
    "s_cmp_eq_u64 s[54:55], -1\n"
    "s_cbranch_scc1 .Lpoll3r\n"
    "s_cmp_lt_u32 s58, 0x80000\n"
    "s_cbranch_scc1 .Lpoll3\n"
    "s_branch .Lpoll3r\n"
    "s_nop 0\n"
    "s_nop 0\n"
    "s_nop 0\n"
    "s_nop 0\n"
    "s_nop 0\n"
    "s_nop 0\n"
    "s_nop 0\n"
    ".Lpoll4:\n"
    "s_sleep 1\n"
    "v_cmp_eq_u64 s[54:55], v[236:237], v[238:239]\n"
    "s_nop 1\n"
    "s_cmp_eq_u64 s[54:55], -1\n"
    "s_cbranch_scc1 .Lpoll4l\n"
    "buffer_load_dwordx2 v[236:237], v241, s[36:39], s81 offen offset:0 sc1\n"
    "s_waitcnt vmcnt(0)\n"
    "v_cmp_eq_u64 s[54:55], v[236:237], v[238:239]\n"
    ".Lpoll4l:\n"
    "ds_read_b32 v245, v247\n"
    "s_waitcnt lgkmcnt(0)\n"
    "v_cmp_le_u32 s[56:57], s59, v245\n"
    "s_add_u32 s58, s58, 1\n"
    "s_nop 1\n"
    "s_and_b64 s[54:55], s[54:55], s[56:57]\n"
    "s_cmp_eq_u64 s[54:55], -1\n"
    "s_cbranch_scc1 .Lpoll4r\n"
    "s_cmp_lt_u32 s58, 0x80000\n"
    "s_cbranch_scc1 .Lpoll4\n"
    "s_branch .Lpoll4r\n"
    "s_nop 0\n"
    "s_nop 0\n"
    "s_nop 0\n"
    "s_nop 0\n"
    "s_nop 0\n"
    "s_nop 0\n"
    "s_nop 0\n"
    ".Lpt0:\n"
    "s_sleep 1\n"
    "v_cmp_eq_u64 s[54:55], v[236:237], v[238:239]\n"
    "s_nop 1\n"
    "s_cmp_eq_u64 s[54:55], -1\n"
    "s_cbranch_scc1 .Lpt0l\n"
    "buffer_load_dwordx2 v[236:237], v241, s[36:39], s81 offen offset:0 sc1\n"
    "s_waitcnt vmcnt(0)\n"
    "v_cmp_eq_u64 s[54:55], v[236:237], v[238:239]\n"
    ".Lpt0l:\n"
    "ds_read_b32 v245, v247\n"
    "s_waitcnt lgkmcnt(0)\n"
    "v_cmp_le_u32 s[56:57], s59, v245\n"
    "s_add_u32 s58, s58, 1\n"
    "s_nop 1\n"
    "s_and_b64 s[54:55], s[54:55], s[56:57]\n"
    "s_cmp_eq_u64 s[54:55], -1\n"
    "s_cbranch_scc1 .Lpt0r\n"
    "s_cmp_lt_u32 s58, 0x80000\n"
    "s_cbranch_scc1 .Lpt0\n"
    "s_branch .Lpt0r\n"
    "s_nop 0\n"
    "s_nop 0\n"
    "s_nop 0\n"
    ".Lpt1:\n"
    "s_sleep 1\n"
    "ds_read_b32 v245, v247\n"
    "s_waitcnt lgkmcnt(0)\n"
    "v_cmp_le_u32 s[56:57], s59, v245\n"
    "s_add_u32 s58, s58, 1\n"
    "s_nop 1\n"
    "s_cmp_eq_u64 s[56:57], -1\n"
    "s_cbranch_scc1 .Lpt1r\n"
    "s_cmp_lt_u32 s58, 0x80000\n"
    "s_cbranch_scc1 .Lpt1\n"
    "s_branch .Lpt1r\n"
    "s_nop 0\n"
    "s_nop 0\n"
    "s_nop 0\n"
    ".Lpt4:\n"
    "s_sleep 1\n"
    "ds_read_b32 v245, v247\n"
    "s_waitcnt lgkmcnt(0)\n"
    "v_cmp_le_u32 s[56:57], s59, v245\n"
    "s_add_u32 s58, s58, 1\n"
    "s_nop 1\n"
    "s_cmp_eq_u64 s[56:57], -1\n"
    "s_cbranch_scc1 .Lpt4r\n"
    "s_cmp_lt_u32 s58, 0x80000\n"
    "s_cbranch_scc1 .Lpt4\n"
    "s_branch .Lpt4r\n"
    "s_nop 0\n"
    ".Lpt3:\n"
    "s_sleep 1\n"
    "ds_read_b32 v245, v247\n"
    "s_waitcnt lgkmcnt(0)\n"
    "v_cmp_le_u32 s[56:57], s59, v245\n"
    "s_add_u32 s58, s58, 1\n"
    "s_nop 1\n"
    "s_cmp_eq_u64 s[56:57], -1\n"
    "s_cbranch_scc1 .Lpt3r\n"
    "s_cmp_lt_u32 s58, 0x80000\n"
    "s_cbranch_scc1 .Lpt3\n"
    "s_branch .Lpt3r\n"
    ".fill ((0x20000 - (.LwVe - .LwV) - (. - _Z8attn_fwdPKtPKiS2_S0_S0_S0_S0_PcPt)) & 4095) / 4, 4, 0xBF800000\n"
    ".LwV:\n"
    "s_branch .LwVe\n"
    ".LPV:\n"
    "v_mfma_f32_16x16x32_bf16 v[36:39], a[112:115], a[192:195], v[100:103]\n"
    "s_add_u32 s71, s71, s44\n"
    "ds_read_b128 a[136:139], v2 offset:13312\n"
    "v_cvt_pk_bf16_f32 v68, v4, v5\n"
    "v_mfma_f32_16x16x32_bf16 v[36:39], a[116:119], a[196:199], v[36:39]\n"
    "s_add_u32 s75, s71, s63\n"
    "ds_read_b128 a[140:143], v2 offset:15360\n"
    "v_cvt_pk_bf16_f32 v69, v6, v7\n"
    "v_mfma_f32_16x16x32_bf16 v[36:39], a[120:123], a[200:203], v[36:39]\n"
    "ds_read_b128 a[144:147], v2 offset:17408\n"
    "v_cvt_pk_bf16_f32 v76, v8, v9\n"
    "v_mfma_f32_16x16x32_bf16 v[36:39], a[124:127], a[204:207], v[36:39]\n"
    "ds_read_b128 a[148:151], v2 offset:19456\n"
    "v_cvt_pk_bf16_f32 v77, v10, v11\n"
    "v_mfma_f32_16x16x32_bf16 v[36:39], a[128:131], a[208:211], v[36:39]\n"
    "ds_read_b128 a[152:155], v2 offset:21504\n"
    "v_cvt_pk_bf16_f32 v84, v12, v13\n"
    "v_mfma_f32_16x16x32_bf16 v[36:39], a[132:135], a[212:215], v[36:39]\n"
    "ds_read_b128 a[156:159], v2 offset:23552\n"
    "v_cvt_pk_bf16_f32 v85, v14, v15\n"
    "v_mfma_f32_16x16x32_bf16 v[40:43], a[132:135], a[236:239], v[100:103]\n"
    "ds_read_b128 v[104:107], v250 offset:144\n"
    "v_cvt_pk_bf16_f32 v92, v16, v17\n"
    "v_mfma_f32_16x16x32_bf16 v[40:43], a[128:131], a[232:235], v[40:43]\n"
    "s_add_u32 s68, s68, 24576\n"
    "v_cvt_pk_bf16_f32 v93, v18, v19\n"
    "v_cvt_pk_bf16_f32 v70, v20, v21\n"
    "v_mfma_f32_16x16x32_bf16 v[40:43], a[124:127], a[228:231], v[40:43]\n"
    "s_cmp_ge_u32 s68, s69\n"
    "s_cselect_b32 s68, s65, s68\n"
    "v_cvt_pk_bf16_f32 v71, v22, v23\n"
    "v_mfma_f32_16x16x32_bf16 v[40:43], a[120:123], a[224:227], v[40:43]\n"
    "v_cvt_pk_bf16_f32 v78, v24, v25\n"
    "v_cvt_pk_bf16_f32 v79, v26, v27\n"
    "v_mfma_f32_16x16x32_bf16 v[40:43], a[116:119], a[220:223], v[40:43]\n"
    "v_cvt_pk_bf16_f32 v86, v28, v29\n"
    "v_cvt_pk_bf16_f32 v87, v30, v31\n"
    "v_mfma_f32_16x16x32_bf16 v[40:43], a[112:115], a[216:219], v[40:43]\n"
    "v_cvt_pk_bf16_f32 v94, v32, v33\n"
    "v_cvt_pk_bf16_f32 v95, v34, v35\n"
    "v_mfma_f32_16x16x32_bf16 v[44:47], a[112:115], a[240:243], v[100:103]\n"
    "v_exp_f32 v36, v36\n"
    "v_mfma_f32_16x16x32_bf16 v[44:47], a[116:119], a[244:247], v[44:47]\n"
    "v_exp_f32 v37, v37\n"
    "v_mfma_f32_16x16x32_bf16 v[44:47], a[120:123], a[248:251], v[44:47]\n"
    "v_exp_f32 v38, v38\n"
    "v_mfma_f32_16x16x32_bf16 v[44:47], a[124:127], a[252:255], v[44:47]\n"
    "v_exp_f32 v39, v39\n"
    "v_mfma_f32_16x16x32_bf16 v[44:47], a[128:131], a[160:163], v[44:47]\n"
    "v_cvt_pk_bf16_f32 v72, v36, v37\n"
    "v_cvt_pk_bf16_f32 v73, v38, v39\n"
    "v_mfma_f32_16x16x32_bf16 v[44:47], a[132:135], a[164:167], v[44:47]\n"
    "v_exp_f32 v40, v40\n"
    "v_mfma_f32_16x16x32_bf16 v[48:51], a[132:135], a[188:191], v[100:103]\n"
    "ds_read_b32 v245, v247\n"
    "v_mfma_f32_16x16x32_bf16 v[48:51], a[128:131], a[184:187], v[48:51]\n"
    "s_add_u32 m0, s68, 0\n"
    "v_exp_f32 v41, v41\n"
    "v_mfma_f32_16x16x32_bf16 v[48:51], a[124:127], a[180:183], v[48:51]\n"
    "v_exp_f32 v42, v42\n"
    "v_mfma_f32_16x16x32_bf16 v[48:51], a[120:123], a[176:179], v[48:51]\n"
    "v_exp_f32 v43, v43\n"
    "v_mfma_f32_16x16x32_bf16 v[48:51], a[116:119], a[172:175], v[48:51]\n"
    "s_waitcnt vmcnt(13)\n"
    "v_cmp_eq_u64 s[54:55], v[236:237], v[238:239]\n"
    "v_cvt_pk_bf16_f32 v80, v40, v41\n"
    "v_mfma_f32_16x16x32_bf16 v[48:51], a[112:115], a[168:171], v[48:51]\n"
    "s_waitcnt lgkmcnt(1)\n"
    "v_cvt_pk_bf16_f32 v81, v42, v43\n"
    "v_mfma_f32_16x16x32_bf16 v[52:55], a[136:139], a[192:195], v[104:107]\n"
    "s_waitcnt lgkmcnt(0)\n"
    "v_cmp_le_u32 s[56:57], s59, v245\n"
    "v_mfma_f32_16x16x32_bf16 v[52:55], a[140:143], a[196:199], v[52:55]\n"
    "v_exp_f32 v44, v44\n"
    "v_mfma_f32_16x16x32_bf16 v[52:55], a[144:147], a[200:203], v[52:55]\n"
    "s_and_b64 s[54:55], s[54:55], s[56:57]\n"
    "s_cmp_eq_u64 s[54:55], -1\n"
    "v_mfma_f32_16x16x32_bf16 v[52:55], a[148:151], a[204:207], v[52:55]\n"
    "s_cbranch_scc0 .Lpoll3\n"
    ".Lpoll3r:\n"
    "v_mfma_f32_16x16x32_bf16 v[52:55], a[152:155], a[208:211], v[52:55]\n"
    "buffer_load_dwordx4 v1, s[16:19], s67 offen offset:0 sc1 lds\n"
    "v_mfma_f32_16x16x32_bf16 v[52:55], a[156:159], a[212:215], v[52:55]\n"
    "ds_read_b128 a[112:115], v3 offset:0\n"
    "v_mfma_f32_16x16x32_bf16 v[56:59], a[156:159], a[236:239], v[104:107]\n"
    "ds_read_b128 a[116:119], v3 offset:2048\n"
    "v_mfma_f32_16x16x32_bf16 v[56:59], a[152:155], a[232:235], v[56:59]\n"
    "ds_read_b128 a[120:123], v3 offset:4096\n"
    "v_mfma_f32_16x16x32_bf16 v[56:59], a[148:151], a[228:231], v[56:59]\n"
    "buffer_load_dwordx4 v1, s[16:19], s67 offen offset:1024 sc1 lds\n"
    "v_mfma_f32_16x16x32_bf16 v[56:59], a[144:147], a[224:227], v[56:59]\n"
    "ds_read_b128 a[124:127], v3 offset:6144\n"
    "v_mfma_f32_16x16x32_bf16 v[56:59], a[140:143], a[220:223], v[56:59]\n"
    "ds_read_b128 a[128:131], v3 offset:8192\n"
    "v_mfma_f32_16x16x32_bf16 v[56:59], a[136:139], a[216:219], v[56:59]\n"
    "ds_read_b128 a[132:135], v3 offset:10240\n"
    "v_mfma_f32_16x16x32_bf16 v[60:63], a[136:139], a[240:243], v[104:107]\n"
    "buffer_load_dwordx4 v1, s[16:19], s67 offen offset:2048 sc1 lds\n"
    "v_mfma_f32_16x16x32_bf16 v[60:63], a[140:143], a[244:247], v[60:63]\n"
    "ds_read_b128 v[100:103], v250 offset:768\n"
    "v_mfma_f32_16x16x32_bf16 v[60:63], a[144:147], a[248:251], v[60:63]\n"
    "v_exp_f32 v52, v52\n"
    "v_mfma_f32_16x16x32_bf16 v[60:63], a[148:151], a[252:255], v[60:63]\n"
    "v_exp_f32 v53, v53\n"
    "v_mfma_f32_16x16x32_bf16 v[60:63], a[152:155], a[160:163], v[60:63]\n"
    "buffer_load_dwordx4 v1, s[16:19], s67 offen offset:3072 sc1 lds\n"
    "v_mfma_f32_16x16x32_bf16 v[60:63], a[156:159], a[164:167], v[60:63]\n"
    "s_add_u32 m0, s68, 4096\n"
    "v_exp_f32 v54, v54\n"
    "v_mfma_f32_16x16x32_bf16 v[64:67], a[156:159], a[188:191], v[104:107]\n"
    "s_add_u32 s83, s67, 4096\n"
    "v_exp_f32 v55, v55\n"
    "v_mfma_f32_16x16x32_bf16 v[64:67], a[152:155], a[184:187], v[64:67]\n"
    "v_cvt_pk_bf16_f32 v74, v52, v53\n"
    "v_cvt_pk_bf16_f32 v75, v54, v55\n"
    "v_mfma_f32_16x16x32_bf16 v[64:67], a[148:151], a[180:183], v[64:67]\n"
    "buffer_load_dwordx4 v1, s[16:19], s83 offen offset:0 sc1 lds\n"
    "v_mfma_f32_16x16x32_bf16 v[64:67], a[144:147], a[176:179], v[64:67]\n"
    "v_exp_f32 v56, v56\n"
    "v_mfma_f32_16x16x32_bf16 v[64:67], a[140:143], a[172:175], v[64:67]\n"
    "v_exp_f32 v57, v57\n"
    "v_mfma_f32_16x16x32_bf16 v[64:67], a[136:139], a[168:171], v[64:67]\n"
    "s_waitcnt lgkmcnt(0)\n"
    "v_exp_f32 v58, v58\n"
    "v_mfma_f32_16x16x32_bf16 v[4:7], a[112:115], a[192:195], v[100:103]\n"
    "buffer_load_dwordx4 v1, s[16:19], s83 offen offset:1024 sc1 lds\n"
    "v_mfma_f32_16x16x32_bf16 v[4:7], a[116:119], a[196:199], v[4:7]\n"
    "s_add_u32 m0, s64, 0\n"
    "ds_read_b128 a[136:139], v3 offset:1024\n"
    "v_cvt_pk_bf16_f32 v82, v56, v57\n"
    "v_mfma_f32_16x16x32_bf16 v[4:7], a[120:123], a[200:203], v[4:7]\n"
    "s_add_u32 s83, s67, 24576\n"
    "ds_read_b128 a[140:143], v3 offset:3072\n"
    "v_mfma_f32_16x16x32_bf16 v[4:7], a[124:127], a[204:207], v[4:7]\n"
    "ds_read_b128 a[144:147], v3 offset:5120\n"
    "v_mfma_f32_16x16x32_bf16 v[4:7], a[128:131], a[208:211], v[4:7]\n"
    "buffer_load_dwordx4 v1, s[16:19], s83 offen offset:0 sc1 lds\n"
    "v_mfma_f32_16x16x32_bf16 v[4:7], a[132:135], a[212:215], v[4:7]\n"
    "ds_read_b128 a[148:151], v3 offset:7168\n"
    "v_mfma_f32_16x16x32_bf16 v[8:11], a[132:135], a[236:239], v[100:103]\n"
    "ds_read_b128 a[152:155], v3 offset:9216\n"
    "v_mfma_f32_16x16x32_bf16 v[8:11], a[128:131], a[232:235], v[8:11]\n"
    "ds_read_b128 a[156:159], v3 offset:11264\n"
    "v_mfma_f32_16x16x32_bf16 v[8:11], a[124:127], a[228:231], v[8:11]\n"
    "buffer_load_dwordx4 v1, s[16:19], s83 offen offset:1024 sc1 lds\n"
    "v_mfma_f32_16x16x32_bf16 v[8:11], a[120:123], a[224:227], v[8:11]\n"
    "ds_read_b128 v[104:107], v250 offset:784\n"
    "v_mfma_f32_16x16x32_bf16 v[8:11], a[116:119], a[220:223], v[8:11]\n"
    "v_exp_f32 v59, v59\n"
    "v_mfma_f32_16x16x32_bf16 v[8:11], a[112:115], a[216:219], v[8:11]\n"
    "v_cvt_pk_bf16_f32 v83, v58, v59\n"
    "v_mfma_f32_16x16x32_bf16 v[12:15], a[112:115], a[240:243], v[100:103]\n"
    "buffer_load_dwordx4 v1, s[16:19], s83 offen offset:2048 sc1 lds\n"
    "v_mfma_f32_16x16x32_bf16 v[12:15], a[116:119], a[244:247], v[12:15]\n"
    "v_exp_f32 v45, v45\n"
    "v_mfma_f32_16x16x32_bf16 v[12:15], a[120:123], a[248:251], v[12:15]\n"
    "v_exp_f32 v46, v46\n"
    "v_mfma_f32_16x16x32_bf16 v[12:15], a[124:127], a[252:255], v[12:15]\n"
    "v_exp_f32 v47, v47\n"
    "v_mfma_f32_16x16x32_bf16 v[12:15], a[128:131], a[160:163], v[12:15]\n"
    "buffer_load_dwordx4 v1, s[16:19], s83 offen offset:3072 sc1 lds\n"
    "v_mfma_f32_16x16x32_bf16 v[12:15], a[132:135], a[164:167], v[12:15]\n"
    "s_add_u32 m0, s64, 4096\n"
    "v_exp_f32 v60, v60\n"
    "v_mfma_f32_16x16x32_bf16 v[16:19], a[132:135], a[188:191], v[100:103]\n"
    "s_add_u32 s83, s67, 28672\n"
    "v_exp_f32 v61, v61\n"
    "v_mfma_f32_16x16x32_bf16 v[16:19], a[128:131], a[184:187], v[16:19]\n"
    "v_exp_f32 v62, v62\n"
    "v_mfma_f32_16x16x32_bf16 v[16:19], a[124:127], a[180:183], v[16:19]\n"
    "buffer_load_dwordx4 v1, s[16:19], s83 offen offset:0 sc1 lds\n"
    "v_mfma_f32_16x16x32_bf16 v[16:19], a[120:123], a[176:179], v[16:19]\n"
    "v_exp_f32 v63, v63\n"
    "v_mfma_f32_16x16x32_bf16 v[16:19], a[116:119], a[172:175], v[16:19]\n"
    "v_cvt_pk_bf16_f32 v88, v44, v45\n"
    "v_cvt_pk_bf16_f32 v89, v46, v47\n"
    "v_mfma_f32_16x16x32_bf16 v[16:19], a[112:115], a[168:171], v[16:19]\n"
    "s_waitcnt lgkmcnt(0)\n"
    "v_cvt_pk_bf16_f32 v90, v60, v61\n"
    "v_cvt_pk_bf16_f32 v91, v62, v63\n"
    "v_mfma_f32_16x16x32_bf16 v[20:23], a[136:139], a[192:195], v[104:107]\n"
    "buffer_load_dwordx4 v1, s[16:19], s83 offen offset:1024 sc1 lds\n"
    "v_mfma_f32_16x16x32_bf16 v[20:23], a[140:143], a[196:199], v[20:23]\n"
    "s_add_u32 s67, s67, 49152\n"
    "ds_read_b128 a[112:115], v248 offset:0\n"
    "v_mfma_f32_16x16x32_bf16 v[20:23], a[144:147], a[200:203], v[20:23]\n"
    "s_add_u32 s59, s59, 4\n"
    "ds_read_b128 a[116:119], v248 offset:12288\n"
    "v_mfma_f32_16x16x32_bf16 v[20:23], a[148:151], a[204:207], v[20:23]\n"
    "s_waitcnt vmcnt(12)\n"
    "ds_read_b128 a[120:123], v248 offset:1024\n"
    "v_mfma_f32_16x16x32_bf16 v[20:23], a[152:155], a[208:211], v[20:23]\n"
    "buffer_store_dwordx2 v[238:239], v240, s[36:39], s80 offen offset:0 sc1\n"
    "v_mfma_f32_16x16x32_bf16 v[20:23], a[156:159], a[212:215], v[20:23]\n"
    "buffer_load_dwordx4 v[204:207], v242, s[40:43], s75 offen offset:0 nt\n"
    "v_mfma_f32_16x16x32_bf16 v[24:27], a[156:159], a[236:239], v[104:107]\n"
    "s_add_u32 s80, s80, 256\n"
    "ds_read_b128 a[124:127], v248 offset:13312\n"
    "v_mfma_f32_16x16x32_bf16 v[24:27], a[152:155], a[232:235], v[24:27]\n"
    "buffer_load_dwordx4 v[208:211], v242, s[40:43], s75 offen offset:768 nt\n"
    "v_mfma_f32_16x16x32_bf16 v[24:27], a[148:151], a[228:231], v[24:27]\n"
    "ds_read_b128 a[128:131], v248 offset:2048\n"
    "v_mfma_f32_16x16x32_bf16 v[24:27], a[144:147], a[224:227], v[24:27]\n"
    "buffer_load_dwordx4 v[212:215], v242, s[40:43], s75 offen offset:256 nt\n"
    "v_mfma_f32_16x16x32_bf16 v[24:27], a[140:143], a[220:223], v[24:27]\n"
    "ds_read_b128 a[132:135], v248 offset:14336\n"
    "v_mfma_f32_16x16x32_bf16 v[24:27], a[136:139], a[216:219], v[24:27]\n"
    "buffer_load_dwordx4 v[216:219], v242, s[40:43], s75 offen offset:1024 nt\n"
    "v_mfma_f32_16x16x32_bf16 v[28:31], a[136:139], a[240:243], v[104:107]\n"
    "v_exp_f32 v48, v48\n"
    "v_mfma_f32_16x16x32_bf16 v[28:31], a[140:143], a[244:247], v[28:31]\n"
    "buffer_load_dwordx4 v[220:223], v242, s[40:43], s75 offen offset:512 nt\n"
    "v_mfma_f32_16x16x32_bf16 v[28:31], a[144:147], a[248:251], v[28:31]\n"
    "v_exp_f32 v49, v49\n"
    "v_mfma_f32_16x16x32_bf16 v[28:31], a[148:151], a[252:255], v[28:31]\n"
    "buffer_load_dwordx4 v[224:227], v242, s[40:43], s75 offen offset:1280 nt\n"
    "v_mfma_f32_16x16x32_bf16 v[28:31], a[152:155], a[160:163], v[28:31]\n"
    "v_exp_f32 v50, v50\n"
    "v_mfma_f32_16x16x32_bf16 v[28:31], a[156:159], a[164:167], v[28:31]\n"
    "v_exp_f32 v51, v51\n"
    "v_mfma_f32_16x16x32_bf16 v[32:35], a[156:159], a[188:191], v[104:107]\n"
    "v_exp_f32 v64, v64\n"
    "v_mfma_f32_16x16x32_bf16 v[32:35], a[152:155], a[184:187], v[32:35]\n"
    "v_exp_f32 v65, v65\n"
    "v_mfma_f32_16x16x32_bf16 v[32:35], a[148:151], a[180:183], v[32:35]\n"
    "v_exp_f32 v66, v66\n"
    "v_mfma_f32_16x16x32_bf16 v[32:35], a[144:147], a[176:179], v[32:35]\n"
    "v_exp_f32 v67, v67\n"
    "v_mfma_f32_16x16x32_bf16 v[32:35], a[140:143], a[172:175], v[32:35]\n"
    "v_cvt_pk_bf16_f32 v96, v48, v49\n"
    "v_cvt_pk_bf16_f32 v97, v50, v51\n"
    "v_mfma_f32_16x16x32_bf16 v[32:35], a[136:139], a[168:171], v[32:35]\n"
    "s_waitcnt lgkmcnt(0)\n"
    "v_cvt_pk_bf16_f32 v98, v64, v65\n"
    "v_cvt_pk_bf16_f32 v99, v66, v67\n"
    "v_mfma_f32_16x16x32_bf16 v[124:127], a[112:115], v[68:71], v[124:127]\n"
    "ds_read_b128 a[136:139], v248 offset:3072\n"
    "v_mfma_f32_16x16x32_bf16 v[124:127], a[116:119], v[72:75], v[124:127]\n"
    "ds_read_b128 a[140:143], v248 offset:15360\n"
    "v_mfma_f32_16x16x32_bf16 v[128:131], a[124:127], v[72:75], v[128:131]\n"
    "ds_read_b128 a[144:147], v248 offset:4096\n"
    "v_mfma_f32_16x16x32_bf16 v[128:131], a[120:123], v[68:71], v[128:131]\n"
    "ds_read_b128 a[148:151], v248 offset:16384\n"
    "v_mfma_f32_16x16x32_bf16 v[132:135], a[128:131], v[68:71], v[132:135]\n"
    "ds_read_b128 a[152:155], v248 offset:5120\n"
    "v_mfma_f32_16x16x32_bf16 v[132:135], a[132:135], v[72:75], v[132:135]\n"
    "ds_read_b128 a[156:159], v248 offset:17408\n"
    "v_mfma_f32_16x16x32_bf16 v[148:151], a[132:135], v[80:83], v[148:151]\n"
    "ds_read_b128 v[104:107], v251 offset:0\n"
    "v_mfma_f32_16x16x32_bf16 v[148:151], a[128:131], v[76:79], v[148:151]\n"
    "v_exp_f32 v4, v4\n"
    "v_mfma_f32_16x16x32_bf16 v[144:147], a[120:123], v[76:79], v[144:147]\n"
    "v_exp_f32 v5, v5\n"
    "v_mfma_f32_16x16x32_bf16 v[144:147], a[124:127], v[80:83], v[144:147]\n"
    "v_exp_f32 v6, v6\n"
    "v_mfma_f32_16x16x32_bf16 v[140:143], a[116:119], v[80:83], v[140:143]\n"
    "v_mfma_f32_16x16x32_bf16 v[140:143], a[112:115], v[76:79], v[140:143]\n"
    "v_mfma_f32_16x16x32_bf16 v[156:159], a[112:115], v[84:87], v[156:159]\n"
    "v_exp_f32 v7, v7\n"
    "v_mfma_f32_16x16x32_bf16 v[156:159], a[116:119], v[88:91], v[156:159]\n"
    "v_mfma_f32_16x16x32_bf16 v[160:163], a[124:127], v[88:91], v[160:163]\n"
    "v_exp_f32 v8, v8\n"
    "v_mfma_f32_16x16x32_bf16 v[160:163], a[120:123], v[84:87], v[160:163]\n"
    "v_mfma_f32_16x16x32_bf16 v[164:167], a[128:131], v[84:87], v[164:167]\n"
    "v_mfma_f32_16x16x32_bf16 v[164:167], a[132:135], v[88:91], v[164:167]\n"
    "v_exp_f32 v9, v9\n"
    "v_mfma_f32_16x16x32_bf16 v[180:183], a[132:135], v[96:99], v[180:183]\n"
    "v_mfma_f32_16x16x32_bf16 v[180:183], a[128:131], v[92:95], v[180:183]\n"
    "v_mfma_f32_16x16x32_bf16 v[176:179], a[120:123], v[92:95], v[176:179]\n"
    "v_exp_f32 v10, v10\n"
    "v_mfma_f32_16x16x32_bf16 v[176:179], a[124:127], v[96:99], v[176:179]\n"
    "v_mfma_f32_16x16x32_bf16 v[172:175], a[116:119], v[96:99], v[172:175]\n"
    "v_exp_f32 v11, v11\n"
    "v_mfma_f32_16x16x32_bf16 v[172:175], a[112:115], v[92:95], v[172:175]\n"
    "s_waitcnt lgkmcnt(0)\n"
    "v_mfma_f32_16x16x32_bf16 v[136:139], a[136:139], v[68:71], v[136:139]\n"
    "ds_read_b128 a[112:115], v248 offset:6144\n"
    "v_mfma_f32_16x16x32_bf16 v[136:139], a[140:143], v[72:75], v[136:139]\n"
    "ds_read_b128 a[116:119], v248 offset:18432\n"
    "v_mfma_f32_16x16x32_bf16 v[188:191], a[148:151], v[72:75], v[188:191]\n"
    "ds_read_b128 a[120:123], v248 offset:7168\n"
    "v_mfma_f32_16x16x32_bf16 v[188:191], a[144:147], v[68:71], v[188:191]\n"
    "ds_read_b128 a[124:127], v248 offset:19456\n"
    "v_mfma_f32_16x16x32_bf16 v[192:195], a[152:155], v[68:71], v[192:195]\n"
    "ds_read_b128 a[128:131], v248 offset:8192\n"
    "v_mfma_f32_16x16x32_bf16 v[192:195], a[156:159], v[72:75], v[192:195]\n"
    "ds_read_b128 a[132:135], v248 offset:20480\n"
    "v_smfmac_f32_16x16x64_bf16 v[108:111], v[104:107], v[68:75], v253\n"
    "s_add_u32 s81, s81, 128\n"
    "s_cmp_gt_u32 s81, 4096\n"
    "s_cselect_b32 s81, 0, s81\n"
    "v_smfmac_f32_16x16x64_bf16 v[112:115], v[104:107], v[76:83], v253\n"
    "s_cmp_ge_u32 s75, 1474560\n"
    "s_cmov_b64 s[40:41], s[8:9]\n"
    "v_mfma_f32_16x16x32_bf16 a[4:7], a[156:159], v[80:83], a[4:7]\n"
    "v_exp_f32 v12, v12\n"
    "v_mfma_f32_16x16x32_bf16 a[4:7], a[152:155], v[76:79], a[4:7]\n"
    "v_exp_f32 v13, v13\n"
    "v_mfma_f32_16x16x32_bf16 a[0:3], a[144:147], v[76:79], a[0:3]\n"
    "v_exp_f32 v14, v14\n"
    "v_mfma_f32_16x16x32_bf16 a[0:3], a[148:151], v[80:83], a[0:3]\n"
    "v_exp_f32 v15, v15\n"
    "v_mfma_f32_16x16x32_bf16 v[152:155], a[140:143], v[80:83], v[152:155]\n"
    "v_exp_f32 v16, v16\n"
    "v_mfma_f32_16x16x32_bf16 v[152:155], a[136:139], v[76:79], v[152:155]\n"
    "v_mfma_f32_16x16x32_bf16 v[168:171], a[136:139], v[84:87], v[168:171]\n"
    "v_exp_f32 v17, v17\n"
    "v_mfma_f32_16x16x32_bf16 v[168:171], a[140:143], v[88:91], v[168:171]\n"
    "v_mfma_f32_16x16x32_bf16 a[16:19], a[148:151], v[88:91], a[16:19]\n"
    "v_mfma_f32_16x16x32_bf16 a[16:19], a[144:147], v[84:87], a[16:19]\n"
    "v_exp_f32 v18, v18\n"
    "v_mfma_f32_16x16x32_bf16 a[20:23], a[152:155], v[84:87], a[20:23]\n"
    "v_mfma_f32_16x16x32_bf16 a[20:23], a[156:159], v[88:91], a[20:23]\n"
    "v_smfmac_f32_16x16x64_bf16 v[116:119], v[104:107], v[84:91], v253\n"
    "s_add_u32 s52, s52, s45\n"
    "v_smfmac_f32_16x16x64_bf16 v[120:123], v[104:107], v[92:99], v253\n"
    "v_mfma_f32_16x16x32_bf16 a[36:39], a[156:159], v[96:99], a[36:39]\n"
    "v_exp_f32 v19, v19\n"
    "v_mfma_f32_16x16x32_bf16 a[36:39], a[152:155], v[92:95], a[36:39]\n"
    "v_exp_f32 v20, v20\n"
    "v_mfma_f32_16x16x32_bf16 a[32:35], a[144:147], v[92:95], a[32:35]\n"
    "v_mfma_f32_16x16x32_bf16 a[32:35], a[148:151], v[96:99], a[32:35]\n"
    "v_exp_f32 v21, v21\n"
    "v_mfma_f32_16x16x32_bf16 v[184:187], a[140:143], v[96:99], v[184:187]\n"
    "v_mfma_f32_16x16x32_bf16 v[184:187], a[136:139], v[92:95], v[184:187]\n"
    "s_waitcnt lgkmcnt(0)\n"
    "v_mfma_f32_16x16x32_bf16 v[196:199], a[112:115], v[68:71], v[196:199]\n"
    "ds_read_b128 a[136:139], v248 offset:9216\n"
    "v_mfma_f32_16x16x32_bf16 v[196:199], a[116:119], v[72:75], v[196:199]\n"
    "ds_read_b128 a[140:143], v248 offset:21504\n"
    "v_mfma_f32_16x16x32_bf16 v[200:203], a[124:127], v[72:75], v[200:203]\n"
    "ds_read_b128 a[144:147], v248 offset:10240\n"
    "v_mfma_f32_16x16x32_bf16 v[200:203], a[120:123], v[68:71], v[200:203]\n"
    "ds_read_b128 a[148:151], v248 offset:22528\n"
    "v_mfma_f32_16x16x32_bf16 a[48:51], a[128:131], v[68:71], a[48:51]\n"
    "buffer_load_dwordx2 v[236:237], v241, s[36:39], s81 offen offset:0 sc1\n"
    "v_mfma_f32_16x16x32_bf16 a[48:51], a[132:135], v[72:75], a[48:51]\n"
    "ds_read_b128 a[152:155], v248 offset:11264\n"
    "v_mfma_f32_16x16x32_bf16 a[64:67], a[132:135], v[80:83], a[64:67]\n"
    "ds_read_b128 a[156:159], v248 offset:23552\n"
    "v_mfma_f32_16x16x32_bf16 a[64:67], a[128:131], v[76:79], a[64:67]\n"
    "v_exp_f32 v22, v22\n"
    "v_mfma_f32_16x16x32_bf16 a[12:15], a[120:123], v[76:79], a[12:15]\n"
    "v_exp_f32 v23, v23\n"
    "v_mfma_f32_16x16x32_bf16 a[12:15], a[124:127], v[80:83], a[12:15]\n"
    "v_exp_f32 v24, v24\n"
    "v_mfma_f32_16x16x32_bf16 a[8:11], a[116:119], v[80:83], a[8:11]\n"
    "v_exp_f32 v25, v25\n"
    "v_mfma_f32_16x16x32_bf16 a[8:11], a[112:115], v[76:79], a[8:11]\n"
    "v_exp_f32 v26, v26\n"
    "v_mfma_f32_16x16x32_bf16 a[24:27], a[112:115], v[84:87], a[24:27]\n"
    "v_mfma_f32_16x16x32_bf16 a[24:27], a[116:119], v[88:91], a[24:27]\n"
    "v_exp_f32 v27, v27\n"
    "v_mfma_f32_16x16x32_bf16 a[28:31], a[124:127], v[88:91], a[28:31]\n"
    "v_mfma_f32_16x16x32_bf16 a[28:31], a[120:123], v[84:87], a[28:31]\n"
    "v_mfma_f32_16x16x32_bf16 a[80:83], a[128:131], v[84:87], a[80:83]\n"
    "v_exp_f32 v28, v28\n"
    "v_mfma_f32_16x16x32_bf16 a[80:83], a[132:135], v[88:91], a[80:83]\n"
    "v_mfma_f32_16x16x32_bf16 a[96:99], a[132:135], v[96:99], a[96:99]\n"
    "v_mfma_f32_16x16x32_bf16 a[96:99], a[128:131], v[92:95], a[96:99]\n"
    "v_exp_f32 v29, v29\n"
    "v_mfma_f32_16x16x32_bf16 a[44:47], a[120:123], v[92:95], a[44:47]\n"
    "v_mfma_f32_16x16x32_bf16 a[44:47], a[124:127], v[96:99], a[44:47]\n"
    "v_exp_f32 v30, v30\n"
    "v_mfma_f32_16x16x32_bf16 a[40:43], a[116:119], v[96:99], a[40:43]\n"
    "v_mfma_f32_16x16x32_bf16 a[40:43], a[112:115], v[92:95], a[40:43]\n"
    "s_waitcnt lgkmcnt(0)\n"
    "v_mfma_f32_16x16x32_bf16 a[52:55], a[136:139], v[68:71], a[52:55]\n"
    "ds_read_b128 a[112:115], v3 offset:12288\n"
    "v_mfma_f32_16x16x32_bf16 a[52:55], a[140:143], v[72:75], a[52:55]\n"
    "ds_read_b128 a[116:119], v3 offset:14336\n"
    "v_mfma_f32_16x16x32_bf16 a[56:59], a[148:151], v[72:75], a[56:59]\n"
    "ds_read_b128 a[120:123], v3 offset:16384\n"
    "v_mfma_f32_16x16x32_bf16 a[56:59], a[144:147], v[68:71], a[56:59]\n"
    "ds_read_b128 a[124:127], v3 offset:18432\n"
    "v_mfma_f32_16x16x32_bf16 a[60:63], a[152:155], v[68:71], a[60:63]\n"
    "ds_read_b128 a[128:131], v3 offset:20480\n"
    "v_mfma_f32_16x16x32_bf16 a[60:63], a[156:159], v[72:75], a[60:63]\n"
    "ds_read_b128 a[132:135], v3 offset:22528\n"
    "v_mfma_f32_16x16x32_bf16 a[76:79], a[156:159], v[80:83], a[76:79]\n"
    "ds_read_b128 v[100:103], v250 offset:896\n"
    "v_mfma_f32_16x16x32_bf16 a[76:79], a[152:155], v[76:79], a[76:79]\n"
    "v_exp_f32 v31, v31\n"
    "v_mfma_f32_16x16x32_bf16 a[72:75], a[144:147], v[76:79], a[72:75]\n"
    "v_exp_f32 v32, v32\n"
    "v_mfma_f32_16x16x32_bf16 a[72:75], a[148:151], v[80:83], a[72:75]\n"
    "v_exp_f32 v33, v33\n"
    "v_mfma_f32_16x16x32_bf16 a[68:71], a[140:143], v[80:83], a[68:71]\n"
    "v_exp_f32 v34, v34\n"
    "v_mfma_f32_16x16x32_bf16 a[68:71], a[136:139], v[76:79], a[68:71]\n"
    "v_exp_f32 v35, v35\n"
    "v_mfma_f32_16x16x32_bf16 a[84:87], a[136:139], v[84:87], a[84:87]\n"
    "v_mfma_f32_16x16x32_bf16 a[84:87], a[140:143], v[88:91], a[84:87]\n"
    "v_mfma_f32_16x16x32_bf16 a[88:91], a[148:151], v[88:91], a[88:91]\n"
    "s_waitcnt vmcnt(8)\n"
    "ds_add_u32 v247, v246\n"
    "v_mfma_f32_16x16x32_bf16 a[88:91], a[144:147], v[84:87], a[88:91]\n"
    "v_mfma_f32_16x16x32_bf16 a[92:95], a[152:155], v[84:87], a[92:95]\n"
    "v_mfma_f32_16x16x32_bf16 a[92:95], a[156:159], v[88:91], a[92:95]\n"
    "v_mfma_f32_16x16x32_bf16 a[108:111], a[156:159], v[96:99], a[108:111]\n"
    "v_mfma_f32_16x16x32_bf16 a[108:111], a[152:155], v[92:95], a[108:111]\n"
    "v_mfma_f32_16x16x32_bf16 a[104:107], a[144:147], v[92:95], a[104:107]\n"
    "s_sub_u32 s74, s68, s65\n"
    "v_mfma_f32_16x16x32_bf16 a[104:107], a[148:151], v[96:99], a[104:107]\n"
    "s_add_u32 s66, s66, 1\n"
    "v_mov_b32 v2, v3\n"
    "v_add_u32_e64 v3, s74, v252\n"
    "v_mfma_f32_16x16x32_bf16 a[100:103], a[140:143], v[96:99], a[100:103]\n"
    "s_waitcnt lgkmcnt(1)\n"
    "s_cmp_eq_u32 s66, .Lend-.Lend2+(29)\n"
    "v_mfma_f32_16x16x32_bf16 a[100:103], a[136:139], v[92:95], a[100:103]\n"
    ".LwVe:\n"
    "s_cbranch_scc1 .LtailP\n"
    ".LNV:\n"
    "v_mfma_f32_16x16x32_bf16 v[36:39], a[112:115], a[192:195], v[100:103]\n"
    "ds_read_b128 a[136:139], v2 offset:13312\n"
    "v_cvt_pk_bf16_f32 v68, v4, v5\n"
    "v_mfma_f32_16x16x32_bf16 v[36:39], a[116:119], a[196:199], v[36:39]\n"
    "ds_read_b128 a[140:143], v2 offset:15360\n"
    "v_mfma_f32_16x16x32_bf16 v[36:39], a[120:123], a[200:203], v[36:39]\n"
    "ds_read_b128 a[144:147], v2 offset:17408\n"
    "v_mfma_f32_16x16x32_bf16 v[36:39], a[124:127], a[204:207], v[36:39]\n"
    "ds_read_b128 a[148:151], v2 offset:19456\n"
    "v_cvt_pk_bf16_f32 v69, v6, v7\n"
    "v_mfma_f32_16x16x32_bf16 v[36:39], a[128:131], a[208:211], v[36:39]\n"
    "ds_read_b128 a[152:155], v2 offset:21504\n"
    "v_mfma_f32_16x16x32_bf16 v[36:39], a[132:135], a[212:215], v[36:39]\n"
    "ds_read_b128 a[156:159], v2 offset:23552\n"
    "v_mfma_f32_16x16x32_bf16 v[40:43], a[132:135], a[236:239], v[100:103]\n"
    "ds_read_b128 v[104:107], v250 offset:912\n"
    "v_mfma_f32_16x16x32_bf16 v[40:43], a[128:131], a[232:235], v[40:43]\n"
    "s_add_u32 s68, s68, 24576\n"
    "v_cvt_pk_bf16_f32 v76, v8, v9\n"
    "v_mfma_f32_16x16x32_bf16 v[40:43], a[124:127], a[228:231], v[40:43]\n"
    "s_cmp_ge_u32 s68, s69\n"
    "s_cselect_b32 s68, s65, s68\n"
    "v_mfma_f32_16x16x32_bf16 v[40:43], a[120:123], a[224:227], v[40:43]\n"
    "v_exp_f32 v36, v36\n"
    "v_mfma_f32_16x16x32_bf16 v[40:43], a[116:119], a[220:223], v[40:43]\n"
    "v_exp_f32 v37, v37\n"
    "v_mfma_f32_16x16x32_bf16 v[40:43], a[112:115], a[216:219], v[40:43]\n"
    "v_cvt_pk_bf16_f32 v77, v10, v11\n"
    "v_cvt_pk_bf16_f32 v72, v36, v37\n"
    "v_mfma_f32_16x16x32_bf16 v[44:47], a[112:115], a[240:243], v[100:103]\n"
    "v_mfma_f32_16x16x32_bf16 v[44:47], a[116:119], a[244:247], v[44:47]\n"
    "v_exp_f32 v38, v38\n"
    "v_mfma_f32_16x16x32_bf16 v[44:47], a[120:123], a[248:251], v[44:47]\n"
    "v_mfma_f32_16x16x32_bf16 v[44:47], a[124:127], a[252:255], v[44:47]\n"
    "v_cvt_pk_bf16_f32 v84, v12, v13\n"
    "v_mfma_f32_16x16x32_bf16 v[44:47], a[128:131], a[160:163], v[44:47]\n"
    "v_exp_f32 v39, v39\n"
    "v_mfma_f32_16x16x32_bf16 v[44:47], a[132:135], a[164:167], v[44:47]\n"
    "v_cvt_pk_bf16_f32 v73, v38, v39\n"
    "v_mfma_f32_16x16x32_bf16 v[48:51], a[132:135], a[188:191], v[100:103]\n"
    "ds_read_b32 v245, v247\n"
    "v_cvt_pk_bf16_f32 v85, v14, v15\n"
    "v_mfma_f32_16x16x32_bf16 v[48:51], a[128:131], a[184:187], v[48:51]\n"
    "s_add_u32 m0, s68, 0\n"
    "v_exp_f32 v40, v40\n"
    "v_mfma_f32_16x16x32_bf16 v[48:51], a[124:127], a[180:183], v[48:51]\n"
    "v_mfma_f32_16x16x32_bf16 v[48:51], a[120:123], a[176:179], v[48:51]\n"
    "v_mfma_f32_16x16x32_bf16 v[48:51], a[116:119], a[172:175], v[48:51]\n"
    "s_waitcnt vmcnt(0)\n"
    "v_cmp_eq_u64 s[54:55], v[236:237], v[238:239]\n"
    "v_cvt_pk_bf16_f32 v92, v16, v17\n"
    "v_mfma_f32_16x16x32_bf16 v[48:51], a[112:115], a[168:171], v[48:51]\n"
    "s_waitcnt lgkmcnt(1)\n"
    "v_exp_f32 v41, v41\n"
    "v_mfma_f32_16x16x32_bf16 v[52:55], a[136:139], a[192:195], v[104:107]\n"
    "s_waitcnt lgkmcnt(0)\n"
    "v_cmp_le_u32 s[56:57], s59, v245\n"
    "v_cvt_pk_bf16_f32 v80, v40, v41\n"
    "v_mfma_f32_16x16x32_bf16 v[52:55], a[140:143], a[196:199], v[52:55]\n"
    "s_waitcnt vmcnt(1)\n"
    "v_exp_f32 v42, v42\n"
    "v_mfma_f32_16x16x32_bf16 v[52:55], a[144:147], a[200:203], v[52:55]\n"
    "s_and_b64 s[54:55], s[54:55], s[56:57]\n"
    "s_cmp_eq_u64 s[54:55], -1\n"
    "v_mfma_f32_16x16x32_bf16 v[52:55], a[148:151], a[204:207], v[52:55]\n"
    "s_cbranch_scc0 .Lpoll4\n"
    ".Lpoll4r:\n"
    "v_mfma_f32_16x16x32_bf16 v[52:55], a[152:155], a[208:211], v[52:55]\n"
    "buffer_load_dwordx4 v1, s[16:19], s67 offen offset:0 sc1 lds\n"
    "v_mfma_f32_16x16x32_bf16 v[52:55], a[156:159], a[212:215], v[52:55]\n"
    "s_add_u32 s76, s52, s60\n"
    "ds_read_b128 a[112:115], v3 offset:0\n"
    "v_cvt_pk_bf16_f32 v93, v18, v19\n"
    "v_mfma_f32_16x16x32_bf16 v[56:59], a[156:159], a[236:239], v[104:107]\n"
    "s_add_u32 s77, s52, s61\n"
    "ds_read_b128 a[116:119], v3 offset:2048\n"
    "v_cvt_pk_bf16_f32 v70, v20, v21\n"
    "v_mfma_f32_16x16x32_bf16 v[56:59], a[152:155], a[232:235], v[56:59]\n"
    "ds_read_b128 a[120:123], v3 offset:4096\n"
    "v_mfma_f32_16x16x32_bf16 v[56:59], a[148:151], a[228:231], v[56:59]\n"
    "buffer_load_dwordx4 v1, s[16:19], s67 offen offset:1024 sc1 lds\n"
    "v_mfma_f32_16x16x32_bf16 v[56:59], a[144:147], a[224:227], v[56:59]\n"
    "ds_read_b128 a[124:127], v3 offset:6144\n"
    "v_cvt_pk_bf16_f32 v71, v22, v23\n"
    "v_mfma_f32_16x16x32_bf16 v[56:59], a[140:143], a[220:223], v[56:59]\n"
    "s_add_u32 s78, s52, s62\n"
    "ds_read_b128 a[128:131], v3 offset:8192\n"
    "v_mfma_f32_16x16x32_bf16 v[56:59], a[136:139], a[216:219], v[56:59]\n"
    "ds_read_b128 a[132:135], v3 offset:10240\n"
    "v_mfma_f32_16x16x32_bf16 v[60:63], a[136:139], a[240:243], v[104:107]\n"
    "buffer_load_dwordx4 v1, s[16:19], s67 offen offset:2048 sc1 lds\n"
    "v_mfma_f32_16x16x32_bf16 v[60:63], a[140:143], a[244:247], v[60:63]\n"
    "ds_read_b128 v[100:103], v250 offset:1536\n"
    "v_cvt_pk_bf16_f32 v78, v24, v25\n"
    "v_mfma_f32_16x16x32_bf16 v[60:63], a[144:147], a[248:251], v[60:63]\n"
    "v_exp_f32 v43, v43\n"
    "v_mfma_f32_16x16x32_bf16 v[60:63], a[148:151], a[252:255], v[60:63]\n"
    "v_cvt_pk_bf16_f32 v81, v42, v43\n"
    "v_cvt_pk_u16_u32 v204, v204, v208\n"
    "v_mfma_f32_16x16x32_bf16 v[60:63], a[152:155], a[160:163], v[60:63]\n"
    "buffer_load_dwordx4 v1, s[16:19], s67 offen offset:3072 sc1 lds\n"
    "v_mfma_f32_16x16x32_bf16 v[60:63], a[156:159], a[164:167], v[60:63]\n"
    "s_add_u32 m0, s68, 4096\n"
    "v_cvt_pk_bf16_f32 v79, v26, v27\n"
    "v_mfma_f32_16x16x32_bf16 v[64:67], a[156:159], a[188:191], v[104:107]\n"
    "s_add_u32 s83, s67, 4096\n"
    "v_exp_f32 v44, v44\n"
    "v_mfma_f32_16x16x32_bf16 v[64:67], a[152:155], a[184:187], v[64:67]\n"
    "v_exp_f32 v45, v45\n"
    "v_mfma_f32_16x16x32_bf16 v[64:67], a[148:151], a[180:183], v[64:67]\n"
    "buffer_load_dwordx4 v1, s[16:19], s83 offen offset:0 sc1 lds\n"
    "v_mfma_f32_16x16x32_bf16 v[64:67], a[144:147], a[176:179], v[64:67]\n"
    "v_cvt_pk_bf16_f32 v86, v28, v29\n"
    "v_cvt_pk_bf16_f32 v88, v44, v45\n"
    "v_mfma_f32_16x16x32_bf16 v[64:67], a[140:143], a[172:175], v[64:67]\n"
    "v_exp_f32 v46, v46\n"
    "v_mfma_f32_16x16x32_bf16 v[64:67], a[136:139], a[168:171], v[64:67]\n"
    "s_waitcnt lgkmcnt(0)\n"
    "v_exp_f32 v47, v47\n"
    "v_mfma_f32_16x16x32_bf16 v[4:7], a[112:115], a[192:195], v[100:103]\n"
    "buffer_load_dwordx4 v1, s[16:19], s83 offen offset:1024 sc1 lds\n"
    "v_mfma_f32_16x16x32_bf16 v[4:7], a[116:119], a[196:199], v[4:7]\n"
    "s_add_u32 m0, s70, 0\n"
    "ds_read_b128 a[136:139], v3 offset:1024\n"
    "v_cvt_pk_bf16_f32 v87, v30, v31\n"
    "v_mfma_f32_16x16x32_bf16 v[4:7], a[120:123], a[200:203], v[4:7]\n"
    "s_add_u32 s83, s67, 24576\n"
    "ds_read_b128 a[140:143], v3 offset:3072\n"
    "v_cvt_pk_bf16_f32 v89, v46, v47\n"
    "v_mfma_f32_16x16x32_bf16 v[4:7], a[124:127], a[204:207], v[4:7]\n"
    "ds_read_b128 a[144:147], v3 offset:5120\n"
    "v_cvt_pk_u16_u32 v205, v205, v209\n"
    "v_mfma_f32_16x16x32_bf16 v[4:7], a[128:131], a[208:211], v[4:7]\n"
    "buffer_load_dwordx4 v1, s[16:19], s83 offen offset:0 sc1 lds\n"
    "v_mfma_f32_16x16x32_bf16 v[4:7], a[132:135], a[212:215], v[4:7]\n"
    "ds_read_b128 a[148:151], v3 offset:7168\n"
    "v_cvt_pk_bf16_f32 v94, v32, v33\n"
    "v_mfma_f32_16x16x32_bf16 v[8:11], a[132:135], a[236:239], v[100:103]\n"
    "ds_read_b128 a[152:155], v3 offset:9216\n"
    "v_cvt_pk_u16_u32 v206, v206, v210\n"
    "v_mfma_f32_16x16x32_bf16 v[8:11], a[128:131], a[232:235], v[8:11]\n"
    "ds_read_b128 a[156:159], v3 offset:11264\n"
    "v_cvt_pk_u16_u32 v207, v207, v211\n"
    "v_mfma_f32_16x16x32_bf16 v[8:11], a[124:127], a[228:231], v[8:11]\n"
    "buffer_load_dwordx4 v1, s[16:19], s83 offen offset:1024 sc1 lds\n"
    "v_mfma_f32_16x16x32_bf16 v[8:11], a[120:123], a[224:227], v[8:11]\n"
    "ds_read_b128 v[104:107], v250 offset:1552\n"
    "v_cvt_pk_bf16_f32 v95, v34, v35\n"
    "v_mfma_f32_16x16x32_bf16 v[8:11], a[116:119], a[220:223], v[8:11]\n"
    "v_exp_f32 v52, v52\n"
    "v_mfma_f32_16x16x32_bf16 v[8:11], a[112:115], a[216:219], v[8:11]\n"
    "v_exp_f32 v53, v53\n"
    "v_mfma_f32_16x16x32_bf16 v[12:15], a[112:115], a[240:243], v[100:103]\n"
    "buffer_load_dwordx4 v1, s[16:19], s83 offen offset:2048 sc1 lds\n"
    "v_mfma_f32_16x16x32_bf16 v[12:15], a[116:119], a[244:247], v[12:15]\n"
    "v_exp_f32 v54, v54\n"
    "v_mfma_f32_16x16x32_bf16 v[12:15], a[120:123], a[248:251], v[12:15]\n"
    "v_exp_f32 v55, v55\n"
    "v_mfma_f32_16x16x32_bf16 v[12:15], a[124:127], a[252:255], v[12:15]\n"
    "v_cvt_pk_bf16_f32 v74, v52, v53\n"
    "v_cvt_pk_bf16_f32 v75, v54, v55\n"
    "v_mfma_f32_16x16x32_bf16 v[12:15], a[128:131], a[160:163], v[12:15]\n"
    "buffer_load_dwordx4 v1, s[16:19], s83 offen offset:3072 sc1 lds\n"
    "v_mfma_f32_16x16x32_bf16 v[12:15], a[132:135], a[164:167], v[12:15]\n"
    "s_add_u32 m0, s70, 4096\n"
    "v_exp_f32 v56, v56\n"
    "v_mfma_f32_16x16x32_bf16 v[16:19], a[132:135], a[188:191], v[100:103]\n"
    "s_add_u32 s83, s67, 28672\n"
    "v_exp_f32 v48, v48\n"
    "v_mfma_f32_16x16x32_bf16 v[16:19], a[128:131], a[184:187], v[16:19]\n"
    "v_exp_f32 v49, v49\n"
    "v_mfma_f32_16x16x32_bf16 v[16:19], a[124:127], a[180:183], v[16:19]\n"
    "buffer_load_dwordx4 v1, s[16:19], s83 offen offset:0 sc1 lds\n"
    "v_mfma_f32_16x16x32_bf16 v[16:19], a[120:123], a[176:179], v[16:19]\n"
    "v_exp_f32 v57, v57\n"
    "v_mfma_f32_16x16x32_bf16 v[16:19], a[116:119], a[172:175], v[16:19]\n"
    "v_cvt_pk_bf16_f32 v82, v56, v57\n"
    "v_cvt_pk_bf16_f32 v96, v48, v49\n"
    "v_mfma_f32_16x16x32_bf16 v[16:19], a[112:115], a[168:171], v[16:19]\n"
    "s_waitcnt lgkmcnt(0)\n"
    "v_exp_f32 v58, v58\n"
    "v_mfma_f32_16x16x32_bf16 v[20:23], a[136:139], a[192:195], v[104:107]\n"
    "buffer_load_dwordx4 v1, s[16:19], s83 offen offset:1024 sc1 lds\n"
    "v_mfma_f32_16x16x32_bf16 v[20:23], a[140:143], a[196:199], v[20:23]\n"
    "s_add_u32 s67, s67, 49152\n"
    "ds_read_b128 a[112:115], v249 offset:0\n"
    "v_or3_b32 v243, v204, v205, v243\n"
    "v_mfma_f32_16x16x32_bf16 v[20:23], a[144:147], a[200:203], v[20:23]\n"
    "s_add_u32 s59, s59, 4\n"
    "ds_read_b128 a[116:119], v249 offset:12288\n"
    "v_or3_b32 v243, v206, v207, v243\n"
    "v_mfma_f32_16x16x32_bf16 v[20:23], a[148:151], a[204:207], v[20:23]\n"
    "ds_read_b128 a[120:123], v249 offset:1024\n"
    "v_cvt_pk_u16_u32 v212, v212, v216\n"
    "v_mfma_f32_16x16x32_bf16 v[20:23], a[152:155], a[208:211], v[20:23]\n"
    "ds_read_b128 a[124:127], v249 offset:13312\n"
    "v_cvt_pk_u16_u32 v213, v213, v217\n"
    "v_mfma_f32_16x16x32_bf16 v[20:23], a[156:159], a[212:215], v[20:23]\n"
    "ds_read_b128 a[128:131], v249 offset:2048\n"
    "v_mfma_f32_16x16x32_bf16 v[24:27], a[156:159], a[236:239], v[104:107]\n"
    "ds_read_b128 a[132:135], v249 offset:14336\n"
    "v_mfma_f32_16x16x32_bf16 v[24:27], a[152:155], a[232:235], v[24:27]\n"
    "v_exp_f32 v59, v59\n"
    "v_mfma_f32_16x16x32_bf16 v[24:27], a[148:151], a[228:231], v[24:27]\n"
    "v_cvt_pk_bf16_f32 v83, v58, v59\n"
    "v_cvt_pk_u16_u32 v214, v214, v218\n"
    "v_mfma_f32_16x16x32_bf16 v[24:27], a[144:147], a[224:227], v[24:27]\n"
    "v_exp_f32 v60, v60\n"
    "v_mfma_f32_16x16x32_bf16 v[24:27], a[140:143], a[220:223], v[24:27]\n"
    "v_exp_f32 v61, v61\n"
    "v_mfma_f32_16x16x32_bf16 v[24:27], a[136:139], a[216:219], v[24:27]\n"
    "v_exp_f32 v62, v62\n"
    "v_mfma_f32_16x16x32_bf16 v[28:31], a[136:139], a[240:243], v[104:107]\n"
    "v_cvt_pk_bf16_f32 v90, v60, v61\n"
    "v_mfma_f32_16x16x32_bf16 v[28:31], a[140:143], a[244:247], v[28:31]\n"
    "v_exp_f32 v63, v63\n"
    "v_mfma_f32_16x16x32_bf16 v[28:31], a[144:147], a[248:251], v[28:31]\n"
    "v_cvt_pk_bf16_f32 v91, v62, v63\n"
    "v_cvt_pk_u16_u32 v215, v215, v219\n"
    "v_mfma_f32_16x16x32_bf16 v[28:31], a[148:151], a[252:255], v[28:31]\n"
    "v_exp_f32 v50, v50\n"
    "v_mfma_f32_16x16x32_bf16 v[28:31], a[152:155], a[160:163], v[28:31]\n"
    "v_exp_f32 v51, v51\n"
    "v_mfma_f32_16x16x32_bf16 v[28:31], a[156:159], a[164:167], v[28:31]\n"
    "v_exp_f32 v64, v64\n"
    "v_mfma_f32_16x16x32_bf16 v[32:35], a[156:159], a[188:191], v[104:107]\n"
    "v_cvt_pk_bf16_f32 v97, v50, v51\n"
    "v_mfma_f32_16x16x32_bf16 v[32:35], a[152:155], a[184:187], v[32:35]\n"
    "v_exp_f32 v65, v65\n"
    "v_mfma_f32_16x16x32_bf16 v[32:35], a[148:151], a[180:183], v[32:35]\n"
    "v_cvt_pk_bf16_f32 v98, v64, v65\n"
    "v_or3_b32 v243, v212, v213, v243\n"
    "v_mfma_f32_16x16x32_bf16 v[32:35], a[144:147], a[176:179], v[32:35]\n"
    "v_mfma_f32_16x16x32_bf16 v[32:35], a[140:143], a[172:175], v[32:35]\n"
    "v_exp_f32 v66, v66\n"
    "v_mfma_f32_16x16x32_bf16 v[32:35], a[136:139], a[168:171], v[32:35]\n"
    "s_waitcnt lgkmcnt(0)\n"
    "v_mfma_f32_16x16x32_bf16 v[124:127], a[112:115], v[68:71], v[124:127]\n"
    "ds_read_b128 a[136:139], v249 offset:3072\n"
    "v_or3_b32 v243, v214, v215, v243\n"
    "v_mfma_f32_16x16x32_bf16 v[124:127], a[116:119], v[72:75], v[124:127]\n"
    "ds_read_b128 a[140:143], v249 offset:15360\n"
    "v_mfma_f32_16x16x32_bf16 v[128:131], a[124:127], v[72:75], v[128:131]\n"
    "ds_read_b128 a[144:147], v249 offset:4096\n"
    "v_mfma_f32_16x16x32_bf16 v[128:131], a[120:123], v[68:71], v[128:131]\n"
    "ds_read_b128 a[148:151], v249 offset:16384\n"
    "v_mfma_f32_16x16x32_bf16 v[132:135], a[128:131], v[68:71], v[132:135]\n"
    "ds_read_b128 a[152:155], v249 offset:5120\n"
    "v_mfma_f32_16x16x32_bf16 v[132:135], a[132:135], v[72:75], v[132:135]\n"
    "ds_read_b128 a[156:159], v249 offset:17408\n"
    "v_cvt_pk_u16_u32 v220, v220, v224\n"
    "v_mfma_f32_16x16x32_bf16 v[148:151], a[132:135], v[80:83], v[148:151]\n"
    "ds_read_b128 v[104:107], v251 offset:768\n"
    "v_mfma_f32_16x16x32_bf16 v[148:151], a[128:131], v[76:79], v[148:151]\n"
    "v_exp_f32 v67, v67\n"
    "v_mfma_f32_16x16x32_bf16 v[144:147], a[120:123], v[76:79], v[144:147]\n"
    "v_cvt_pk_bf16_f32 v99, v66, v67\n"
    "v_mfma_f32_16x16x32_bf16 v[144:147], a[124:127], v[80:83], v[144:147]\n"
    "v_exp_f32 v4, v4\n"
    "v_mfma_f32_16x16x32_bf16 v[140:143], a[116:119], v[80:83], v[140:143]\n"
    "v_exp_f32 v5, v5\n"
    "v_mfma_f32_16x16x32_bf16 v[140:143], a[112:115], v[76:79], v[140:143]\n"
    "v_exp_f32 v6, v6\n"
    "v_mfma_f32_16x16x32_bf16 v[156:159], a[112:115], v[84:87], v[156:159]\n"
    "v_exp_f32 v7, v7\n"
    "v_mfma_f32_16x16x32_bf16 v[156:159], a[116:119], v[88:91], v[156:159]\n"
    "v_cvt_pk_u16_u32 v221, v221, v225\n"
    "v_mfma_f32_16x16x32_bf16 v[160:163], a[124:127], v[88:91], v[160:163]\n"
    "v_exp_f32 v8, v8\n"
    "v_mfma_f32_16x16x32_bf16 v[160:163], a[120:123], v[84:87], v[160:163]\n"
    "v_cvt_pk_u16_u32 v222, v222, v226\n"
    "v_mfma_f32_16x16x32_bf16 v[164:167], a[128:131], v[84:87], v[164:167]\n"
    "v_mfma_f32_16x16x32_bf16 v[164:167], a[132:135], v[88:91], v[164:167]\n"
    "v_exp_f32 v9, v9\n"
    "v_mfma_f32_16x16x32_bf16 v[180:183], a[132:135], v[96:99], v[180:183]\n"
    "v_mfma_f32_16x16x32_bf16 v[180:183], a[128:131], v[92:95], v[180:183]\n"
    "v_mfma_f32_16x16x32_bf16 v[176:179], a[120:123], v[92:95], v[176:179]\n"
    "v_exp_f32 v10, v10\n"
    "v_mfma_f32_16x16x32_bf16 v[176:179], a[124:127], v[96:99], v[176:179]\n"
    "v_cvt_pk_u16_u32 v223, v223, v227\n"
    "v_mfma_f32_16x16x32_bf16 v[172:175], a[116:119], v[96:99], v[172:175]\n"
    "v_exp_f32 v11, v11\n"
    "v_mfma_f32_16x16x32_bf16 v[172:175], a[112:115], v[92:95], v[172:175]\n"
    "s_waitcnt lgkmcnt(0)\n"
    "v_mfma_f32_16x16x32_bf16 v[136:139], a[136:139], v[68:71], v[136:139]\n"
    "ds_read_b128 a[112:115], v249 offset:6144\n"
    "v_mfma_f32_16x16x32_bf16 v[136:139], a[140:143], v[72:75], v[136:139]\n"
    "ds_read_b128 a[116:119], v249 offset:18432\n"
    "v_mfma_f32_16x16x32_bf16 v[188:191], a[148:151], v[72:75], v[188:191]\n"
    "ds_read_b128 a[120:123], v249 offset:7168\n"
    "v_or3_b32 v243, v220, v221, v243\n"
    "v_mfma_f32_16x16x32_bf16 v[188:191], a[144:147], v[68:71], v[188:191]\n"
    "ds_read_b128 a[124:127], v249 offset:19456\n"
    "v_mfma_f32_16x16x32_bf16 v[192:195], a[152:155], v[68:71], v[192:195]\n"
    "ds_read_b128 a[128:131], v249 offset:8192\n"
    "v_mfma_f32_16x16x32_bf16 v[192:195], a[156:159], v[72:75], v[192:195]\n"
    "ds_read_b128 a[132:135], v249 offset:20480\n"
    "v_smfmac_f32_16x16x64_bf16 v[108:111], v[104:107], v[68:75], v253\n"
    "s_add_u32 s81, s81, 128\n"
    "s_cmp_gt_u32 s81, 4096\n"
    "s_cselect_b32 s81, 0, s81\n"
    "v_smfmac_f32_16x16x64_bf16 v[112:115], v[104:107], v[76:83], v253\n"
    "v_mfma_f32_16x16x32_bf16 a[4:7], a[156:159], v[80:83], a[4:7]\n"
    "v_exp_f32 v12, v12\n"
    "v_mfma_f32_16x16x32_bf16 a[4:7], a[152:155], v[76:79], a[4:7]\n"
    "v_exp_f32 v13, v13\n"
    "v_mfma_f32_16x16x32_bf16 a[0:3], a[144:147], v[76:79], a[0:3]\n"
    "v_exp_f32 v14, v14\n"
    "v_mfma_f32_16x16x32_bf16 a[0:3], a[148:151], v[80:83], a[0:3]\n"
    "v_exp_f32 v15, v15\n"
    "v_mfma_f32_16x16x32_bf16 v[152:155], a[140:143], v[80:83], v[152:155]\n"
    "v_or3_b32 v243, v222, v223, v243\n"
    "v_and_b32 v208, 0xff00ff00, v243\n"
    "v_mfma_f32_16x16x32_bf16 v[152:155], a[136:139], v[76:79], v[152:155]\n"
    "v_exp_f32 v16, v16\n"
    "v_mfma_f32_16x16x32_bf16 v[168:171], a[136:139], v[84:87], v[168:171]\n"
    "v_exp_f32 v17, v17\n"
    "v_mfma_f32_16x16x32_bf16 v[168:171], a[140:143], v[88:91], v[168:171]\n"
    "v_mfma_f32_16x16x32_bf16 a[16:19], a[148:151], v[88:91], a[16:19]\n"
    "v_mfma_f32_16x16x32_bf16 a[16:19], a[144:147], v[84:87], a[16:19]\n"
    "v_exp_f32 v18, v18\n"
    "v_mfma_f32_16x16x32_bf16 a[20:23], a[152:155], v[84:87], a[20:23]\n"
    "v_cmp_ne_u32 vcc, 0, v208\n"
    "v_mfma_f32_16x16x32_bf16 a[20:23], a[156:159], v[88:91], a[20:23]\n"
    "v_smfmac_f32_16x16x64_bf16 v[116:119], v[104:107], v[84:91], v253\n"
    "v_smfmac_f32_16x16x64_bf16 v[120:123], v[104:107], v[92:99], v253\n"
    "v_mfma_f32_16x16x32_bf16 a[36:39], a[156:159], v[96:99], a[36:39]\n"
    "v_exp_f32 v19, v19\n"
    "v_mfma_f32_16x16x32_bf16 a[36:39], a[152:155], v[92:95], a[36:39]\n"
    "v_exp_f32 v20, v20\n"
    "v_mfma_f32_16x16x32_bf16 a[32:35], a[144:147], v[92:95], a[32:35]\n"
    "v_cndmask_b32 v208, 2.0, v241, vcc\n"
    "v_mfma_f32_16x16x32_bf16 a[32:35], a[148:151], v[96:99], a[32:35]\n"
    "v_exp_f32 v21, v21\n"
    "v_mfma_f32_16x16x32_bf16 v[184:187], a[140:143], v[96:99], v[184:187]\n"
    "v_mfma_f32_16x16x32_bf16 v[184:187], a[136:139], v[92:95], v[184:187]\n"
    "s_waitcnt lgkmcnt(0)\n"
    "v_mfma_f32_16x16x32_bf16 v[196:199], a[112:115], v[68:71], v[196:199]\n"
    "ds_read_b128 a[136:139], v249 offset:9216\n"
    "v_mfma_f32_16x16x32_bf16 v[196:199], a[116:119], v[72:75], v[196:199]\n"
    "ds_read_b128 a[140:143], v249 offset:21504\n"
    "v_mfma_f32_16x16x32_bf16 v[200:203], a[124:127], v[72:75], v[200:203]\n"
    "ds_read_b128 a[144:147], v249 offset:10240\n"
    "v_mfma_f32_16x16x32_bf16 v[200:203], a[120:123], v[68:71], v[200:203]\n"
    "ds_read_b128 a[148:151], v249 offset:22528\n"
    "v_mfma_f32_16x16x32_bf16 a[48:51], a[128:131], v[68:71], a[48:51]\n"
    "buffer_load_dwordx2 v[236:237], v241, s[36:39], s81 offen offset:0 sc1\n"
    "v_mfma_f32_16x16x32_bf16 a[48:51], a[132:135], v[72:75], a[48:51]\n"
    "ds_read_b128 a[152:155], v249 offset:11264\n"
    "v_mfma_f32_16x16x32_bf16 a[64:67], a[132:135], v[80:83], a[64:67]\n"
    "ds_read_b128 a[156:159], v249 offset:23552\n"
    "v_mfma_f32_16x16x32_bf16 a[64:67], a[128:131], v[76:79], a[64:67]\n"
    "buffer_store_dword v204, v244, s[20:23], s76 offen offset:0 sc1\n"
    "v_mfma_f32_16x16x32_bf16 a[12:15], a[120:123], v[76:79], a[12:15]\n"
    "v_exp_f32 v22, v22\n"
    "v_mfma_f32_16x16x32_bf16 a[12:15], a[124:127], v[80:83], a[12:15]\n"
    "buffer_store_dword v205, v244, s[20:23], s76 offen offset:1024 sc1\n"
    "v_mfma_f32_16x16x32_bf16 a[8:11], a[116:119], v[80:83], a[8:11]\n"
    "v_exp_f32 v23, v23\n"
    "v_mfma_f32_16x16x32_bf16 a[8:11], a[112:115], v[76:79], a[8:11]\n"
    "buffer_store_dword v206, v244, s[20:23], s76 offen offset:2048 sc1\n"
    "v_mfma_f32_16x16x32_bf16 a[24:27], a[112:115], v[84:87], a[24:27]\n"
    "v_exp_f32 v24, v24\n"
    "v_mfma_f32_16x16x32_bf16 a[24:27], a[116:119], v[88:91], a[24:27]\n"
    "buffer_store_dword v207, v244, s[20:23], s76 offen offset:3072 sc1\n"
    "v_mfma_f32_16x16x32_bf16 a[28:31], a[124:127], v[88:91], a[28:31]\n"
    "v_exp_f32 v25, v25\n"
    "v_mfma_f32_16x16x32_bf16 a[28:31], a[120:123], v[84:87], a[28:31]\n"
    "buffer_store_dword v212, v244, s[20:23], s77 offen offset:0 sc1\n"
    "v_mfma_f32_16x16x32_bf16 a[80:83], a[128:131], v[84:87], a[80:83]\n"
    "v_exp_f32 v26, v26\n"
    "v_mfma_f32_16x16x32_bf16 a[80:83], a[132:135], v[88:91], a[80:83]\n"
    "buffer_store_dword v213, v244, s[20:23], s77 offen offset:1024 sc1\n"
    "v_mfma_f32_16x16x32_bf16 a[96:99], a[132:135], v[96:99], a[96:99]\n"
    "v_exp_f32 v27, v27\n"
    "v_mfma_f32_16x16x32_bf16 a[96:99], a[128:131], v[92:95], a[96:99]\n"
    "buffer_store_dword v214, v244, s[20:23], s77 offen offset:2048 sc1\n"
    "v_mfma_f32_16x16x32_bf16 a[44:47], a[120:123], v[92:95], a[44:47]\n"
    "v_exp_f32 v28, v28\n"
    "v_mfma_f32_16x16x32_bf16 a[44:47], a[124:127], v[96:99], a[44:47]\n"
    "buffer_store_dword v215, v244, s[20:23], s77 offen offset:3072 sc1\n"
    "v_mfma_f32_16x16x32_bf16 a[40:43], a[116:119], v[96:99], a[40:43]\n"
    "v_exp_f32 v29, v29\n"
    "v_mfma_f32_16x16x32_bf16 a[40:43], a[112:115], v[92:95], a[40:43]\n"
    "s_waitcnt lgkmcnt(0)\n"
    "v_exp_f32 v30, v30\n"
    "v_mfma_f32_16x16x32_bf16 a[52:55], a[136:139], v[68:71], a[52:55]\n"
    "ds_read_b128 a[112:115], v3 offset:12288\n"
    "v_mfma_f32_16x16x32_bf16 a[52:55], a[140:143], v[72:75], a[52:55]\n"
    "ds_read_b128 a[116:119], v3 offset:14336\n"
    "v_mfma_f32_16x16x32_bf16 a[56:59], a[148:151], v[72:75], a[56:59]\n"
    "ds_read_b128 a[120:123], v3 offset:16384\n"
    "v_mfma_f32_16x16x32_bf16 a[56:59], a[144:147], v[68:71], a[56:59]\n"
    "ds_read_b128 a[124:127], v3 offset:18432\n"
    "v_mfma_f32_16x16x32_bf16 a[60:63], a[152:155], v[68:71], a[60:63]\n"
    "ds_read_b128 a[128:131], v3 offset:20480\n"
    "v_mfma_f32_16x16x32_bf16 a[60:63], a[156:159], v[72:75], a[60:63]\n"
    "ds_read_b128 a[132:135], v3 offset:22528\n"
    "v_mfma_f32_16x16x32_bf16 a[76:79], a[156:159], v[80:83], a[76:79]\n"
    "ds_read_b128 v[100:103], v250 offset:1664\n"
    "v_mfma_f32_16x16x32_bf16 a[76:79], a[152:155], v[76:79], a[76:79]\n"
    "buffer_store_dword v220, v244, s[20:23], s78 offen offset:0 sc1\n"
    "v_mfma_f32_16x16x32_bf16 a[72:75], a[144:147], v[76:79], a[72:75]\n"
    "v_exp_f32 v31, v31\n"
    "v_mfma_f32_16x16x32_bf16 a[72:75], a[148:151], v[80:83], a[72:75]\n"
    "buffer_store_dword v221, v244, s[20:23], s78 offen offset:1024 sc1\n"
    "v_mfma_f32_16x16x32_bf16 a[68:71], a[140:143], v[80:83], a[68:71]\n"
    "v_exp_f32 v32, v32\n"
    "v_mfma_f32_16x16x32_bf16 a[68:71], a[136:139], v[76:79], a[68:71]\n"
    "buffer_store_dword v222, v244, s[20:23], s78 offen offset:2048 sc1\n"
    "v_mfma_f32_16x16x32_bf16 a[84:87], a[136:139], v[84:87], a[84:87]\n"
    "v_exp_f32 v33, v33\n"
    "v_mfma_f32_16x16x32_bf16 a[84:87], a[140:143], v[88:91], a[84:87]\n"
    "buffer_store_dword v223, v244, s[20:23], s78 offen offset:3072 sc1\n"
    "v_mfma_f32_16x16x32_bf16 a[88:91], a[148:151], v[88:91], a[88:91]\n"
    "s_waitcnt vmcnt(13)\n"
    "ds_add_u32 v247, v246\n"
    "v_mfma_f32_16x16x32_bf16 a[88:91], a[144:147], v[84:87], a[88:91]\n"
    "buffer_store_dwordx2 v[238:239], v208, s[36:39], 0 offen offset:0 sc1\n"
    "v_mfma_f32_16x16x32_bf16 a[92:95], a[152:155], v[84:87], a[92:95]\n"
    "v_exp_f32 v34, v34\n"
    "v_mfma_f32_16x16x32_bf16 a[92:95], a[156:159], v[88:91], a[92:95]\n"
    "v_exp_f32 v35, v35\n"
    "v_mfma_f32_16x16x32_bf16 a[108:111], a[156:159], v[96:99], a[108:111]\n"
    "v_mfma_f32_16x16x32_bf16 a[108:111], a[152:155], v[92:95], a[108:111]\n"
    "v_mfma_f32_16x16x32_bf16 a[104:107], a[144:147], v[92:95], a[104:107]\n"
    "s_sub_u32 s74, s68, s65\n"
    "v_add_u32 v250, 1536, v250\n"
    "v_add_u32 v251, 1536, v251\n"
    "v_mfma_f32_16x16x32_bf16 a[104:107], a[148:151], v[96:99], a[104:107]\n"
    "s_add_u32 s66, s66, 1\n"
    "v_mov_b32 v2, v3\n"
    "v_add_u32 v3, s74, v252\n"
    "v_mfma_f32_16x16x32_bf16 a[100:103], a[140:143], v[96:99], a[100:103]\n"
    "s_waitcnt lgkmcnt(1)\n"
    "s_cmp_lt_u32 s66, 29\n"
    "v_mfma_f32_16x16x32_bf16 a[100:103], a[136:139], v[92:95], a[100:103]\n"
    "s_cbranch_scc1 .LPV\n"
    "s_branch .Ltail\n"
    "s_nop 0\n"
    "s_nop 0\n"
    "s_nop 0\n"
    "s_nop 0\n"
    "s_nop 0\n"
    "s_nop 0\n"
    ".LtailP:\n"
    "s_cbranch_execz .Lw2\n"
    "v_swap_b32 v248, v249\n"
    "s_mov_b32 s64, s70\n"
    "v_add_u32 v250, 768, v250\n"
    "v_add_u32 v251, 768, v251\n"
    ".Ltail:\n"
    "v_mfma_f32_16x16x32_bf16 v[36:39], a[112:115], a[192:195], v[100:103]\n"
    "v_cvt_pk_bf16_f32 v93, v18, v19\n"
    "v_cvt_pk_bf16_f32 v84, v12, v13\n"
    "v_mfma_f32_16x16x32_bf16 v[36:39], a[116:119], a[196:199], v[36:39]\n"
    "ds_read_b128 a[136:139], v2 offset:13312\n"
    "ds_read_b128 a[140:143], v2 offset:15360\n"
    "v_mfma_f32_16x16x32_bf16 v[36:39], a[120:123], a[200:203], v[36:39]\n"
    "s_mul_i32 s75, s84, 49152\n"
    "v_mfma_f32_16x16x32_bf16 v[36:39], a[124:127], a[204:207], v[36:39]\n"
    "v_cvt_pk_bf16_f32 v77, v10, v11\n"
    "v_subrev_u32 v249, 24576, v249\n"
    "v_mfma_f32_16x16x32_bf16 v[36:39], a[128:131], a[208:211], v[36:39]\n"
    "ds_read_b128 a[152:155], v2 offset:21504\n"
    "v_mfma_f32_16x16x32_bf16 v[40:43], a[132:135], a[236:239], v[100:103]\n"
    "v_cvt_pk_bf16_f32 v69, v6, v7\n"
    "v_lshlrev_b32 v240, 16, v1\n"
    "v_mfma_f32_16x16x32_bf16 v[36:39], a[132:135], a[212:215], v[36:39]\n"
    "ds_read_b128 a[156:159], v2 offset:23552\n"
    "ds_read_b128 v[104:107], v250 offset:144\n"
    "v_mfma_f32_16x16x32_bf16 v[48:51], a[132:135], a[188:191], v[100:103]\n"
    "s_add_u32 s76, s75, 196608\n"
    "ds_read_b128 a[144:147], v2 offset:17408\n"
    "v_and_b32 v3, 0xf0, v1\n"
    "v_mfma_f32_16x16x32_bf16 v[48:51], a[128:131], a[184:187], v[48:51]\n"
    "ds_read_b128 a[148:151], v2 offset:19456\n"
    "s_add_u32 s77, s76, 196608\n"
    "v_mfma_f32_16x16x32_bf16 v[40:43], a[128:131], a[232:235], v[40:43]\n"
    "v_exp_f32 v37, v37\n"
    "v_mfma_f32_16x16x32_bf16 v[48:51], a[124:127], a[180:183], v[48:51]\n"
    "v_exp_f32 v38, v38\n"
    "v_mfma_f32_16x16x32_bf16 v[48:51], a[120:123], a[176:179], v[48:51]\n"
    "s_add_u32 s78, s77, 196608\n"
    "v_cvt_pk_bf16_f32 v76, v8, v9\n"
    "v_cvt_pk_bf16_f32 v68, v4, v5\n"
    "v_mfma_f32_16x16x32_bf16 v[40:43], a[124:127], a[228:231], v[40:43]\n"
    "v_cvt_pk_bf16_f32 v85, v14, v15\n"
    "v_cvt_pk_bf16_f32 v71, v22, v23\n"
    "v_mfma_f32_16x16x32_bf16 v[48:51], a[116:119], a[172:175], v[48:51]\n"
    "v_and_b32 v2, 0x70, v1\n"
    "v_cvt_pk_bf16_f32 v94, v32, v33\n"
    "v_mfma_f32_16x16x32_bf16 v[44:47], a[112:115], a[240:243], v[100:103]\n"
    "v_cvt_pk_bf16_f32 v95, v34, v35\n"
    "v_cvt_pk_bf16_f32 v86, v28, v29\n"
    "v_mfma_f32_16x16x32_bf16 v[48:51], a[112:115], a[168:171], v[48:51]\n"
    "v_exp_f32 v39, v39\n"
    "v_mfma_f32_16x16x32_bf16 v[44:47], a[116:119], a[244:247], v[44:47]\n"
    "s_waitcnt lgkmcnt(2)\n"
    "v_cvt_pk_bf16_f32 v70, v20, v21\n"
    "v_cvt_pk_bf16_f32 v92, v16, v17\n"
    "v_mfma_f32_16x16x32_bf16 v[60:63], a[136:139], a[240:243], v[104:107]\n"
    "v_exp_f32 v36, v36\n"
    "v_mfma_f32_16x16x32_bf16 v[56:59], a[156:159], a[236:239], v[104:107]\n"
    "s_add_u32 s72, s65, 122880\n"
    "ds_read_b32 v245, v247\n"
    "v_mfma_f32_16x16x32_bf16 v[40:43], a[120:123], a[224:227], v[40:43]\n"
    "v_exp_f32 v51, v51\n"
    "v_mfma_f32_16x16x32_bf16 v[60:63], a[140:143], a[244:247], v[60:63]\n"
    "v_exp_f32 v48, v48\n"
    "v_mfma_f32_16x16x32_bf16 v[44:47], a[120:123], a[248:251], v[44:47]\n"
    "v_exp_f32 v50, v50\n"
    "v_mfma_f32_16x16x32_bf16 v[40:43], a[116:119], a[220:223], v[40:43]\n"
    "s_waitcnt vmcnt(0)\n"
    "v_exp_f32 v49, v49\n"
    "v_mfma_f32_16x16x32_bf16 v[44:47], a[124:127], a[252:255], v[44:47]\n"
    "ds_read_b128 v[232:235], v251 offset:0\n"
    "ds_read_b128 v[100:103], v250 offset:768\n"
    "v_mfma_f32_16x16x32_bf16 v[52:55], a[136:139], a[192:195], v[104:107]\n"
    "v_cvt_pk_bf16_f32 v79, v26, v27\n"
    "v_lshrrev_b32 v224, 4, v1\n"
    "v_mfma_f32_16x16x32_bf16 v[64:67], a[156:159], a[188:191], v[104:107]\n"
    "v_lshl_add_u32 v3, v3, 3, s72\n"
    "v_cmp_eq_u64 s[54:55], v[236:237], v[238:239]\n"
    "v_mfma_f32_16x16x32_bf16 v[52:55], a[140:143], a[196:199], v[52:55]\n"
    "s_waitcnt lgkmcnt(2)\n"
    "v_cvt_pk_bf16_f32 v78, v24, v25\n"
    "v_cmp_le_u32 s[56:57], s59, v245\n"
    "v_mfma_f32_16x16x32_bf16 v[40:43], a[112:115], a[216:219], v[40:43]\n"
    "s_and_b64 s[54:55], s[54:55], s[56:57]\n"
    "v_and_b32 v224, 48, v224\n"
    "s_cmp_eq_u64 s[54:55], -1\n"
    "v_mfma_f32_16x16x32_bf16 v[60:63], a[144:147], a[248:251], v[60:63]\n"
    "s_cbranch_scc0 .Lpt0\n"
    ".Lpt0r:\n"
    "v_mfma_f32_16x16x32_bf16 v[44:47], a[128:131], a[160:163], v[44:47]\n"
    "s_add_u32 s81, s81, 128\n"
    "ds_read_b128 a[116:119], v252 offset:2048\n"
    "s_add_u32 m0, s65, 24576\n"
    "v_mfma_f32_16x16x32_bf16 v[52:55], a[144:147], a[200:203], v[52:55]\n"
    "s_add_u32 s83, s67, .Lend-.Lend2+(0)\n"
    "buffer_load_dwordx4 v1, s[16:19], s83 offen offset:0 sc1 lds\n"
    "v_mfma_f32_16x16x32_bf16 v[60:63], a[148:151], a[252:255], v[60:63]\n"
    "ds_read_b128 a[120:123], v252 offset:4096\n"
    "ds_read_b128 a[128:131], v252 offset:8192\n"
    "v_mfma_f32_16x16x32_bf16 v[56:59], a[152:155], a[232:235], v[56:59]\n"
    "buffer_load_dwordx4 v1, s[16:19], s83 offen offset:1024 sc1 lds\n"
    "v_mfma_f32_16x16x32_bf16 v[60:63], a[152:155], a[160:163], v[60:63]\n"
    "ds_read_b128 v[104:107], v250 offset:784\n"
    "ds_read_b128 a[112:115], v252 offset:0\n"
    "v_mfma_f32_16x16x32_bf16 v[44:47], a[132:135], a[164:167], v[44:47]\n"
    "buffer_load_dwordx4 v1, s[16:19], s83 offen offset:2048 sc1 lds\n"
    "v_mfma_f32_16x16x32_bf16 v[52:55], a[148:151], a[204:207], v[52:55]\n"
    "ds_read_b128 a[132:135], v252 offset:10240\n"
    "v_mfma_f32_16x16x32_bf16 v[52:55], a[152:155], a[208:211], v[52:55]\n"
    "buffer_load_dwordx4 v1, s[16:19], s83 offen offset:3072 sc1 lds\n"
    "v_mfma_f32_16x16x32_bf16 v[60:63], a[156:159], a[164:167], v[60:63]\n"
    "s_add_u32 m0, s65, 28672\n"
    "v_exp_f32_e64 v40, v40\n"
    "v_mfma_f32_16x16x32_bf16 v[56:59], a[148:151], a[228:231], v[56:59]\n"
    "s_add_u32 s83, s67, 4096\n"
    "buffer_load_dwordx4 v1, s[16:19], s83 offen offset:0 sc1 lds\n"
    "v_mfma_f32_16x16x32_bf16 v[56:59], a[144:147], a[224:227], v[56:59]\n"
    "ds_read_b128 a[124:127], v252 offset:6144\n"
    "v_cvt_pk_bf16_f32 v72, v36, v37\n"
    "v_mfma_f32_16x16x32_bf16 v[56:59], a[140:143], a[220:223], v[56:59]\n"
    "buffer_load_dwordx4 v1, s[16:19], s83 offen offset:1024 sc1 lds\n"
    "v_mfma_f32_16x16x32_bf16 v[52:55], a[156:159], a[212:215], v[52:55]\n"
    "v_exp_f32 v45, v45\n"
    "v_mfma_f32_16x16x32_bf16 v[56:59], a[136:139], a[216:219], v[56:59]\n"
    "v_exp_f32 v62, v62\n"
    "v_mfma_f32_16x16x32_bf16 v[64:67], a[152:155], a[184:187], v[64:67]\n"
    "s_waitcnt lgkmcnt(2)\n"
    "v_xor_b32 v224, v224, v2\n"
    "v_cvt_pk_bf16_f32 v87, v30, v31\n"
    "v_mfma_f32_16x16x32_bf16 v[12:15], a[112:115], a[240:243], v[100:103]\n"
    "s_waitcnt lgkmcnt(1)\n"
    "ds_read_b128 a[156:159], v252 offset:11264\n"
    "ds_read_b128 a[152:155], v252 offset:9216\n"
    "v_mfma_f32_16x16x32_bf16 v[16:19], a[132:135], a[188:191], v[100:103]\n"
    ".Lwp0:\n"
    "s_cbranch_execz .Lw3\n"
    "s_add_u32 m0, s65, 73728\n"
    "v_exp_f32 v56, v56\n"
    "v_mfma_f32_16x16x32_bf16 v[64:67], a[148:151], a[180:183], v[64:67]\n"
    "v_exp_f32 v58, v58\n"
    "v_mfma_f32_16x16x32_bf16 v[4:7], a[112:115], a[192:195], v[100:103]\n"
    "s_add_u32 s83, s67, 24576\n"
    "v_exp_f32 v61, v61\n"
    "v_mfma_f32_16x16x32_bf16 v[16:19], a[128:131], a[184:187], v[16:19]\n"
    "v_exp_f32 v53, v53\n"
    "v_mfma_f32_16x16x32_bf16 v[64:67], a[144:147], a[176:179], v[64:67]\n"
    "v_exp_f32 v54, v54\n"
    "v_mfma_f32_16x16x32_bf16 v[64:67], a[140:143], a[172:175], v[64:67]\n"
    "v_exp_f32 v52, v52\n"
    "v_mfma_f32_16x16x32_bf16 v[8:11], a[132:135], a[236:239], v[100:103]\n"
    "s_waitcnt lgkmcnt(2)\n"
    "v_exp_f32 v63, v63\n"
    "v_mfma_f32_16x16x32_bf16 v[16:19], a[124:127], a[180:183], v[16:19]\n"
    "v_exp_f32 v41, v41\n"
    "v_mfma_f32_16x16x32_bf16 v[64:67], a[136:139], a[168:171], v[64:67]\n"
    "v_exp_f32 v43, v43\n"
    "v_mfma_f32_16x16x32_bf16 v[8:11], a[128:131], a[232:235], v[8:11]\n"
    "ds_read_b128 a[144:147], v252 offset:5120\n"
    "ds_read_b128 a[148:151], v252 offset:7168\n"
    "v_mfma_f32_16x16x32_bf16 v[12:15], a[116:119], a[244:247], v[12:15]\n"
    "v_exp_f32 v44, v44\n"
    "v_mfma_f32_16x16x32_bf16 v[4:7], a[116:119], a[196:199], v[4:7]\n"
    "v_exp_f32 v55, v55\n"
    "v_mfma_f32_16x16x32_bf16 v[16:19], a[120:123], a[176:179], v[16:19]\n"
    "ds_read_b128 a[136:139], v252 offset:1024\n"
    "v_cvt_pk_bf16_f32 v80, v40, v41\n"
    "v_mfma_f32_16x16x32_bf16 v[12:15], a[120:123], a[248:251], v[12:15]\n"
    "buffer_load_dwordx4 v1, s[16:19], s83 offen offset:0 sc1 lds\n"
    "v_mfma_f32_16x16x32_bf16 v[4:7], a[120:123], a[200:203], v[4:7]\n"
    "v_exp_f32 v42, v42\n"
    "v_mfma_f32_16x16x32_bf16 v[16:19], a[116:119], a[172:175], v[16:19]\n"
    "v_exp_f32 v66, v66\n"
    "v_mfma_f32_16x16x32_bf16 v[4:7], a[124:127], a[204:207], v[4:7]\n"
    "v_exp_f32 v59, v59\n"
    "v_mfma_f32_16x16x32_bf16 v[4:7], a[128:131], a[208:211], v[4:7]\n"
    "buffer_load_dwordx4 v1, s[16:19], s83 offen offset:1024 sc1 lds\n"
    "v_mfma_f32_16x16x32_bf16 v[4:7], a[132:135], a[212:215], v[4:7]\n"
    "v_exp_f32 v57, v57\n"
    "v_mfma_f32_16x16x32_bf16 v[12:15], a[124:127], a[252:255], v[12:15]\n"
    "ds_read_b128 v[100:103], v250 offset:896\n"
    "ds_read_b128 a[140:143], v252 offset:3072\n"
    "v_mfma_f32_16x16x32_bf16 v[12:15], a[128:131], a[160:163], v[12:15]\n"
    "v_exp_f32 v65, v65\n"
    "v_mfma_f32_16x16x32_bf16 v[8:11], a[124:127], a[228:231], v[8:11]\n"
    "v_exp_f32 v47, v47\n"
    "v_mfma_f32_16x16x32_bf16 v[12:15], a[132:135], a[164:167], v[12:15]\n"
    "s_waitcnt lgkmcnt(6)\n"
    "buffer_load_dwordx4 v1, s[16:19], s83 offen offset:2048 sc1 lds\n"
    "v_mfma_f32_16x16x32_bf16 v[32:35], a[156:159], a[188:191], v[104:107]\n"
    "v_exp_f32 v60, v60\n"
    "v_mfma_f32_16x16x32_bf16 v[24:27], a[156:159], a[236:239], v[104:107]\n"
    "s_waitcnt lgkmcnt(2)\n"
    "v_cvt_pk_bf16_f32 v81, v42, v43\n"
    "v_mfma_f32_16x16x32_bf16 v[28:31], a[136:139], a[240:243], v[104:107]\n"
    "v_cvt_pk_bf16_f32 v96, v48, v49\n"
    "v_cvt_pk_bf16_f32 v90, v60, v61\n"
    "v_mfma_f32_16x16x32_bf16 v[8:11], a[120:123], a[224:227], v[8:11]\n"
    "v_exp_f32 v7, v7\n"
    "v_mfma_f32_16x16x32_bf16 v[24:27], a[152:155], a[232:235], v[24:27]\n"
    "v_exp_f32 v67, v67\n"
    "v_mfma_f32_16x16x32_bf16 v[8:11], a[116:119], a[220:223], v[8:11]\n"
    "ds_read_b128 a[124:127], v252 offset:18432\n"
    "v_mfma_f32_16x16x32_bf16 v[16:19], a[112:115], a[168:171], v[16:19]\n"
    "buffer_load_dwordx4 v1, s[16:19], s83 offen offset:3072 sc1 lds\n"
    "v_mfma_f32_16x16x32_bf16 v[32:35], a[152:155], a[184:187], v[32:35]\n"
    "s_waitcnt lgkmcnt(1)\n"
    "ds_read_b128 a[132:135], v252 offset:22528\n"
    "v_cvt_pk_bf16_f32 v75, v54, v55\n"
    "v_mfma_f32_16x16x32_bf16 v[24:27], a[148:151], a[228:231], v[24:27]\n"
    "s_add_u32 m0, s65, 77824\n"
    "v_cvt_pk_bf16_f32 v74, v52, v53\n"
    "v_cvt_pk_bf16_f32 v97, v50, v51\n"
    "v_mfma_f32_16x16x32_bf16 v[20:23], a[136:139], a[192:195], v[104:107]\n"
    "s_add_u32 s83, s67, 28672\n"
    "v_exp_f32 v6, v6\n"
    "v_mfma_f32_16x16x32_bf16 v[20:23], a[140:143], a[196:199], v[20:23]\n"
    "v_cvt_pk_bf16_f32 v99, v66, v67\n"
    "v_exp_f32 v14, v14\n"
    "v_mfma_f32_16x16x32_bf16 v[24:27], a[144:147], a[224:227], v[24:27]\n"
    "ds_read_b128 a[128:131], v252 offset:20480\n"
    "ds_read_b128 v[104:107], v250 offset:912\n"
    "v_mfma_f32_16x16x32_bf16 v[8:11], a[112:115], a[216:219], v[8:11]\n"
    "v_exp_f32 v46, v46\n"
    "v_mfma_f32_16x16x32_bf16 v[28:31], a[140:143], a[244:247], v[28:31]\n"
    "v_exp_f32 v13, v13\n"
    "v_mfma_f32_16x16x32_bf16 v[32:35], a[148:151], a[180:183], v[32:35]\n"
    "s_waitcnt lgkmcnt(2)\n"
    "v_exp_f32 v64, v64\n"
    "v_mfma_f32_16x16x32_bf16 v[40:43], a[132:135], a[236:239], v[100:103]\n"
    "ds_read_b128 a[120:123], v252 offset:16384\n"
    "v_cvt_pk_bf16_f32 v91, v62, v63\n"
    "v_mfma_f32_16x16x32_bf16 v[32:35], a[144:147], a[176:179], v[32:35]\n"
    "ds_read_b128 a[112:115], v252 offset:12288\n"
    "v_cvt_pk_bf16_f32 v73, v38, v39\n"
    "v_mfma_f32_16x16x32_bf16 v[28:31], a[144:147], a[248:251], v[28:31]\n"
    "v_exp_f32 v11, v11\n"
    "v_mfma_f32_16x16x32_bf16 v[32:35], a[140:143], a[172:175], v[32:35]\n"
    "v_cvt_pk_bf16_f32 v88, v44, v45\n"
    "v_cvt_pk_bf16_f32 v89, v46, v47\n"
    "v_mfma_f32_16x16x32_bf16 v[32:35], a[136:139], a[168:171], v[32:35]\n"
    "buffer_load_dwordx4 v1, s[16:19], s83 offen offset:0 sc1 lds\n"
    "v_mfma_f32_16x16x32_bf16 v[24:27], a[140:143], a[220:223], v[24:27]\n"
    "v_exp_f32 v9, v9\n"
    "v_mfma_f32_16x16x32_bf16 v[20:23], a[144:147], a[200:203], v[20:23]\n"
    "buffer_load_dwordx4 v1, s[16:19], s83 offen offset:1024 sc1 lds\n"
    "v_mfma_f32_16x16x32_bf16 v[20:23], a[148:151], a[204:207], v[20:23]\n"
    "v_exp_f32 v15, v15\n"
    "v_mfma_f32_16x16x32_bf16 v[20:23], a[152:155], a[208:211], v[20:23]\n"
    "v_exp_f32 v34, v34\n"
    "v_mfma_f32_16x16x32_bf16 v[20:23], a[156:159], a[212:215], v[20:23]\n"
    "v_exp_f32 v10, v10\n"
    "v_mfma_f32_16x16x32_bf16 v[28:31], a[148:151], a[252:255], v[28:31]\n"
    "s_add_u32 s59, s59, 4\n"
    "ds_read_b128 a[140:143], v252 offset:15360\n"
    "ds_read_b128 a[116:119], v252 offset:14336\n"
    "v_mfma_f32_16x16x32_bf16 v[28:31], a[152:155], a[160:163], v[28:31]\n"
    "s_waitcnt lgkmcnt(5)\n"
    "v_exp_f32 v4, v4\n"
    "v_mfma_f32_16x16x32_bf16 v[40:43], a[128:131], a[232:235], v[40:43]\n"
    "v_exp_f32 v12, v12\n"
    "v_mfma_f32_16x16x32_bf16 v[24:27], a[136:139], a[216:219], v[24:27]\n"
    "ds_read_b128 a[136:139], v252 offset:13312\n"
    "v_add_u32 v3, v3, v224\n"
    "v_mfma_f32_16x16x32_bf16 v[48:51], a[132:135], a[188:191], v[100:103]\n"
    "v_exp_f32 v23, v23\n"
    "v_smfmac_f32_16x16x64_bf16 v[116:119], v[232:235], v[84:91], v253\n"
    "ds_read_b128 a[152:155], v252 offset:21504\n"
    "v_mfma_f32_16x16x32_bf16 v[28:31], a[156:159], a[164:167], v[28:31]\n"
    "s_waitcnt lgkmcnt(5)\n"
    "v_exp_f32 v5, v5\n"
    "v_mfma_f32_16x16x32_bf16 v[40:43], a[124:127], a[228:231], v[40:43]\n"
    "s_mov_b32 s74, 0x47000000\n"
    "v_exp_f32 v20, v20\n"
    "v_mfma_f32_16x16x32_bf16 v[40:43], a[120:123], a[224:227], v[40:43]\n"
    "s_waitcnt lgkmcnt(4)\n"
    "ds_read_b128 a[156:159], v252 offset:23552\n"
    "v_cvt_pk_bf16_f32 v204, v4, v5\n"
    "v_mfma_f32_16x16x32_bf16 v[36:39], a[112:115], a[192:195], v[100:103]\n"
    "s_waitcnt lgkmcnt(3)\n"
    "v_exp_f32 v32, v32\n"
    "v_mfma_f32_16x16x32_bf16 v[36:39], a[116:119], a[196:199], v[36:39]\n"
    "v_cvt_pk_bf16_f32 v220, v12, v13\n"
    "v_cvt_pk_bf16_f32 v82, v56, v57\n"
    "v_mfma_f32_16x16x32_bf16 v[40:43], a[116:119], a[220:223], v[40:43]\n"
    "v_exp_f32 v16, v16\n"
    "v_mfma_f32_16x16x32_bf16 v[40:43], a[112:115], a[216:219], v[40:43]\n"
    "s_waitcnt lgkmcnt(2)\n"
    "v_exp_f32 v28, v28\n"
    "v_mfma_f32_16x16x32_bf16 v[52:55], a[136:139], a[192:195], v[104:107]\n"
    "v_exp_f32 v33, v33\n"
    "v_mfma_f32_16x16x32_bf16 v[48:51], a[128:131], a[184:187], v[48:51]\n"
    "v_exp_f32 v8, v8\n"
    "v_mfma_f32_16x16x32_bf16 v[44:47], a[112:115], a[240:243], v[100:103]\n"
    "v_exp_f32 v17, v17\n"
    "v_mfma_f32_16x16x32_bf16 v[60:63], a[136:139], a[240:243], v[104:107]\n"
    "v_cvt_pk_bf16_f32 v98, v64, v65\n"
    "v_cvt_pk_bf16_f32 v83, v58, v59\n"
    "v_mfma_f32_16x16x32_bf16 v[48:51], a[124:127], a[180:183], v[48:51]\n"
    "v_exp_f32 v35, v35\n"
    "v_mfma_f32_16x16x32_bf16 v[44:47], a[116:119], a[244:247], v[44:47]\n"
    "v_exp_f32 v41, v41\n"
    "v_mfma_f32_16x16x32_bf16 v[44:47], a[120:123], a[248:251], v[44:47]\n"
    "v_exp_f32 v29, v29\n"
    "v_mfma_f32_16x16x32_bf16 v[60:63], a[140:143], a[244:247], v[60:63]\n"
    "v_exp_f32 v31, v31\n"
    "v_mfma_f32_16x16x32_bf16 v[36:39], a[120:123], a[200:203], v[36:39]\n"
    "v_lshrrev_b32 v224, 3, v1\n"
    "v_cvt_pk_bf16_f32 v205, v6, v7\n"
    "v_mfma_f32_16x16x32_bf16 v[48:51], a[120:123], a[176:179], v[48:51]\n"
    "s_waitcnt vmcnt(6)\n"
    "ds_add_u32 v247, v246\n"
    "v_mfma_f32_16x16x32_bf16 v[48:51], a[116:119], a[172:175], v[48:51]\n"
    "s_waitcnt lgkmcnt(1)\n"
    "ds_read_b128 a[144:147], v252 offset:17408\n"
    "ds_read_b128 a[148:151], v252 offset:19456\n"
    "v_mfma_f32_16x16x32_bf16 v[56:59], a[156:159], a[236:239], v[104:107]\n"
    "v_exp_f32 v25, v25\n"
    "v_cvt_pk_bf16_f32 v228, v16, v17\n"
    "v_mfma_f32_16x16x32_bf16 v[52:55], a[140:143], a[196:199], v[52:55]\n"
    "v_exp_f32 v18, v18\n"
    "v_mfma_f32_16x16x32_bf16 v[44:47], a[124:127], a[252:255], v[44:47]\n"
    "v_exp_f32 v19, v19\n"
    "v_mfma_f32_16x16x32_bf16 v[64:67], a[156:159], a[188:191], v[104:107]\n"
    "v_exp_f32 v43, v43\n"
    "v_mfma_f32_16x16x32_bf16 v[48:51], a[112:115], a[168:171], v[48:51]\n"
    "v_exp_f32 v27, v27\n"
    "v_mfma_f32_16x16x32_bf16 v[36:39], a[124:127], a[204:207], v[36:39]\n"
    "v_exp_f32 v30, v30\n"
    "v_mfma_f32_16x16x32_bf16 v[64:67], a[152:155], a[184:187], v[64:67]\n"
    "v_exp_f32 v22, v22\n"
    "v_mfma_f32_16x16x32_bf16 v[36:39], a[128:131], a[208:211], v[36:39]\n"
    "v_exp_f32 v42, v42\n"
    "v_mfma_f32_16x16x32_bf16 v[44:47], a[128:131], a[160:163], v[44:47]\n"
    "s_waitcnt lgkmcnt(1)\n"
    "v_exp_f32 v26, v26\n"
    "v_mfma_f32_16x16x32_bf16 v[60:63], a[144:147], a[248:251], v[60:63]\n"
    "s_waitcnt lgkmcnt(0)\n"
    "v_exp_f32 v40, v40\n"
    "v_mfma_f32_16x16x32_bf16 v[64:67], a[148:151], a[180:183], v[64:67]\n"
    "ds_read_b128 v[100:103], v250 offset:1536\n"
    "ds_read_b32 v245, v247\n"
    "v_mfma_f32_16x16x32_bf16 v[56:59], a[152:155], a[232:235], v[56:59]\n"
    "v_cvt_pk_bf16_f32 v216, v40, v41\n"
    "v_cvt_pk_bf16_f32 v231, v34, v35\n"
    "v_mfma_f32_16x16x32_bf16 v[64:67], a[144:147], a[176:179], v[64:67]\n"
    "v_exp_f32 v21, v21\n"
    "v_smfmac_f32_16x16x64_bf16 v[120:123], v[232:235], v[92:99], v253\n"
    "v_cvt_pk_bf16_f32 v221, v14, v15\n"
    "v_mfma_f32_16x16x32_bf16 v[44:47], a[132:135], a[164:167], v[44:47]\n"
    "v_cvt_pk_bf16_f32 v207, v22, v23\n"
    "v_cvt_pk_bf16_f32 v229, v18, v19\n"
    "v_mfma_f32_16x16x32_bf16 v[64:67], a[140:143], a[172:175], v[64:67]\n"
    "v_cvt_pk_bf16_f32 v206, v20, v21\n"
    "v_cvt_pk_bf16_f32 v212, v8, v9\n"
    "v_mfma_f32_16x16x32_bf16 v[52:55], a[144:147], a[200:203], v[52:55]\n"
    "v_exp_f32 v50, v50\n"
    "v_mfma_f32_16x16x32_bf16 v[60:63], a[148:151], a[252:255], v[60:63]\n"
    "v_exp_f32 v24, v24\n"
    "v_mfma_f32_16x16x32_bf16 v[36:39], a[132:135], a[212:215], v[36:39]\n"
    "s_waitcnt lgkmcnt(0)\n"
    "v_cmp_le_u32 s[56:57], s59, v245\n"
    "v_cvt_pk_bf16_f32 v213, v10, v11\n"
    "v_smfmac_f32_16x16x64_bf16 v[108:111], v[232:235], v[68:75], v253\n"
    "s_cmp_eq_u64 s[56:57], -1\n"
    "v_cvt_pk_bf16_f32 v214, v24, v25\n"
    "v_smfmac_f32_16x16x64_bf16 v[112:115], v[232:235], v[76:83], v253\n"
    "s_cbranch_scc0 .Lpt1\n"
    ".Lpt1r:\n"
    "v_mfma_f32_16x16x32_bf16 v[52:55], a[148:151], a[204:207], v[52:55]\n"
    "s_add_u32 m0, s65, 49152\n"
    "ds_read_b128 a[128:131], v252 offset:32768\n"
    "ds_read_b128 a[124:127], v252 offset:30720\n"
    "v_mfma_f32_16x16x32_bf16 v[60:63], a[152:155], a[160:163], v[60:63]\n"
    "ds_read_b128 a[112:115], v252 offset:24576\n"
    "v_cvt_pk_bf16_f32 v222, v28, v29\n"
    "v_mfma_f32_16x16x32_bf16 v[60:63], a[156:159], a[164:167], v[60:63]\n"
    "s_add_u32 s83, s67, 73728\n"
    "ds_read_b128 a[132:135], v252 offset:34816\n"
    "v_cvt_pk_bf16_f32 v215, v26, v27\n"
    "v_mfma_f32_16x16x32_bf16 v[56:59], a[148:151], a[228:231], v[56:59]\n"
    "buffer_load_dwordx4 v1, s[16:19], s83 offen offset:0 sc1 lds\n"
    "v_mfma_f32_16x16x32_bf16 v[56:59], a[144:147], a[224:227], v[56:59]\n"
    "ds_read_b128 a[116:119], v252 offset:26624\n"
    "v_cvt_pk_bf16_f32 v223, v30, v31\n"
    "v_mfma_f32_16x16x32_bf16 v[56:59], a[140:143], a[220:223], v[56:59]\n"
    "v_exp_f32 v38, v38\n"
    "v_exp_f32 v47, v47\n"
    "v_mfma_f32_16x16x32_bf16 v[52:55], a[152:155], a[208:211], v[52:55]\n"
    "ds_read_b128 a[120:123], v252 offset:28672\n"
    "ds_read_b128 v[104:107], v250 offset:1552\n"
    "v_mfma_f32_16x16x32_bf16 v[64:67], a[136:139], a[168:171], v[64:67]\n"
    "buffer_load_dwordx4 v1, s[16:19], s83 offen offset:1024 sc1 lds\n"
    "v_mfma_f32_16x16x32_bf16 v[56:59], a[136:139], a[216:219], v[56:59]\n"
    "v_exp_f32 v37, v37\n"
    "v_mfma_f32_16x16x32_bf16 v[52:55], a[156:159], a[212:215], v[52:55]\n"
    "s_waitcnt lgkmcnt(4)\n"
    "buffer_load_dwordx4 v1, s[16:19], s83 offen offset:2048 sc1 lds\n"
    "v_mfma_f32_16x16x32_bf16 v[4:7], a[112:115], a[192:195], v[100:103]\n"
    "ds_read_b128 a[140:143], v252 offset:27648\n"
    "v_cvt_pk_bf16_f32 v230, v32, v33\n"
    "v_mfma_f32_16x16x32_bf16 v[20:23], a[112:115], a[240:243], v[100:103]\n"
    "s_waitcnt lgkmcnt(3)\n"
    "buffer_load_dwordx4 v1, s[16:19], s83 offen offset:3072 sc1 lds\n"
    "v_mfma_f32_16x16x32_bf16 v[20:23], a[116:119], a[244:247], v[20:23]\n"
    "s_add_u32 m0, s65, 53248\n"
    "v_exp_f32 v58, v58\n"
    "v_mfma_f32_16x16x32_bf16 v[28:31], a[132:135], a[188:191], v[100:103]\n"
    "s_add_u32 s83, s67, 77824\n"
    "v_exp_f32 v44, v44\n"
    "v_mfma_f32_16x16x32_bf16 v[28:31], a[128:131], a[184:187], v[28:31]\n"
    "ds_read_b128 a[148:151], v252 offset:31744\n"
    "ds_read_b128 a[152:155], v252 offset:33792\n"
    "v_mfma_f32_16x16x32_bf16 v[12:15], a[132:135], a[236:239], v[100:103]\n"
    "ds_read_b128 a[156:159], v252 offset:35840\n"
    "v_and_b32 v224, 0x70, v224\n"
    "v_mfma_f32_16x16x32_bf16 v[12:15], a[128:131], a[232:235], v[12:15]\n"
    "buffer_load_dwordx4 v1, s[16:19], s83 offen offset:0 sc1 lds\n"
    "v_mfma_f32_16x16x32_bf16 v[28:31], a[124:127], a[180:183], v[28:31]\n"
    "v_exp_f32 v55, v55\n"
    "v_mfma_f32_16x16x32_bf16 v[4:7], a[116:119], a[196:199], v[4:7]\n"
    "s_waitcnt lgkmcnt(5)\n"
    "buffer_load_dwordx4 v1, s[16:19], s83 offen offset:1024 sc1 lds\n"
    "v_mfma_f32_16x16x32_bf16 v[28:31], a[120:123], a[176:179], v[28:31]\n"
    "v_exp_f32 v65, v65\n"
    "v_mfma_f32_16x16x32_bf16 v[28:31], a[116:119], a[172:175], v[28:31]\n"
    "v_exp_f32 v61, v61\n"
    "v_mfma_f32_16x16x32_bf16 v[12:15], a[124:127], a[228:231], v[12:15]\n"
    "v_exp_f32 v53, v53\n"
    "v_mfma_f32_16x16x32_bf16 v[4:7], a[120:123], a[200:203], v[4:7]\n"
    "buffer_load_dwordx2 v[236:237], v241, s[36:39], 0 offen offset:0 sc1\n"
    "v_mfma_f32_16x16x32_bf16 v[20:23], a[120:123], a[248:251], v[20:23]\n"
    "s_waitcnt lgkmcnt(0)\n"
    "v_exp_f32 v67, v67\n"
    "v_mfma_f32_16x16x32_bf16 v[16:19], a[156:159], a[236:239], v[104:107]\n"
    "ds_read_b128 a[136:139], v252 offset:25600\n"
    "ds_read_b128 v[100:103], v250 offset:1664\n"
    "v_mfma_f32_16x16x32_bf16 v[4:7], a[124:127], a[204:207], v[4:7]\n"
    "v_exp_f32 v36, v36\n"
    "v_mfma_f32_16x16x32_bf16 v[28:31], a[112:115], a[168:171], v[28:31]\n"
    "s_waitcnt vmcnt(7)\n"
    "buffer_store_dwordx2 v[238:239], v240, s[36:39], s82 offen offset:0 sc1\n"
    "v_exp_f32 v46, v46\n"
    "v_mfma_f32_16x16x32_bf16 v[32:35], a[156:159], a[188:191], v[104:107]\n"
    "v_exp_f32 v60, v60\n"
    "v_mfma_f32_16x16x32_bf16 v[12:15], a[120:123], a[224:227], v[12:15]\n"
    "v_exp_f32 v64, v64\n"
    "v_mfma_f32_16x16x32_bf16 v[4:7], a[128:131], a[208:211], v[4:7]\n"
    "v_exp_f32 v66, v66\n"
    "v_mfma_f32_16x16x32_bf16 v[12:15], a[116:119], a[220:223], v[12:15]\n"
    "v_exp_f32 v49, v49\n"
    "v_mfma_f32_16x16x32_bf16 v[20:23], a[124:127], a[252:255], v[20:23]\n"
    "ds_read_b128 a[116:119], v252 offset:38912\n"
    "v_cvt_pk_bf16_f32 v235, v66, v67\n"
    "v_mfma_f32_16x16x32_bf16 v[32:35], a[152:155], a[184:187], v[32:35]\n"
    "ds_read_b128 a[120:123], v252 offset:40960\n"
    "v_cvt_pk_bf16_f32 v208, v36, v37\n"
    "v_mfma_f32_16x16x32_bf16 v[12:15], a[112:115], a[216:219], v[12:15]\n"
    "s_mov_b32 s73, 0x7f800000\n"
    "ds_read_b128 a[144:147], v252 offset:29696\n"
    "ds_read_b128 a[112:115], v252 offset:36864\n"
    "v_mfma_f32_16x16x32_bf16 v[4:7], a[132:135], a[212:215], v[4:7]\n"
    "s_waitcnt lgkmcnt(5)\n"
    "ds_add_u32 v247, v246\n"
    "v_mfma_f32_16x16x32_bf16 v[24:27], a[136:139], a[240:243], v[104:107]\n"
    "v_exp_f32 v39, v39\n"
    "v_mfma_f32_16x16x32_bf16 v[16:19], a[152:155], a[232:235], v[16:19]\n"
    "v_cvt_pk_bf16_f32 v226, v60, v61\n"
    "v_cvt_pk_bf16_f32 v234, v64, v65\n"
    "v_mfma_f32_16x16x32_bf16 v[8:11], a[136:139], a[192:195], v[104:107]\n"
    "v_exp_f32 v48, v48\n"
    "v_mfma_f32_16x16x32_bf16 v[24:27], a[140:143], a[244:247], v[24:27]\n"
    "v_exp_f32 v56, v56\n"
    "v_mfma_f32_16x16x32_bf16 v[20:23], a[128:131], a[160:163], v[20:23]\n"
    "ds_read_b128 v[240:243], v251 offset:768\n"
    "v_cvt_pk_bf16_f32 v209, v38, v39\n"
    "v_mfma_f32_16x16x32_bf16 v[16:19], a[148:151], a[228:231], v[16:19]\n"
    "v_exp_f32 v62, v62\n"
    "v_mfma_f32_16x16x32_bf16 v[20:23], a[132:135], a[164:167], v[20:23]\n"
    "v_exp_f32 v63, v63\n"
    "v_mfma_f32_16x16x32_bf16 v[8:11], a[140:143], a[196:199], v[8:11]\n"
    "s_waitcnt lgkmcnt(3)\n"
    "ds_read_b128 a[128:131], v252 offset:45056\n"
    "ds_read_b128 a[132:135], v252 offset:47104\n"
    "v_mfma_f32_16x16x32_bf16 v[24:27], a[144:147], a[248:251], v[24:27]\n"
    "v_exp_f32 v12, v12\n"
    "v_mfma_f32_16x16x32_bf16 v[8:11], a[144:147], a[200:203], v[8:11]\n"
    "v_exp_f32 v13, v13\n"
    "v_mfma_f32_16x16x32_bf16 v[24:27], a[148:151], a[252:255], v[24:27]\n"
    "v_exp_f32 v4, v4\n"
    "v_mfma_f32_16x16x32_bf16 v[24:27], a[152:155], a[160:163], v[24:27]\n"
    "v_exp_f32 v59, v59\n"
    "v_mfma_f32_16x16x32_bf16 v[16:19], a[144:147], a[224:227], v[16:19]\n"
    "s_mov_b32 s79, 133\n"
    "v_exp_f32 v5, v5\n"
    "v_mfma_f32_16x16x32_bf16 v[16:19], a[140:143], a[220:223], v[16:19]\n"
    "v_exp_f32 v54, v54\n"
    "v_mfma_f32_16x16x32_bf16 v[8:11], a[148:151], a[204:207], v[8:11]\n"
    "v_exp_f32 v45, v45\n"
    "v_mfma_f32_16x16x32_bf16 v[16:19], a[136:139], a[216:219], v[16:19]\n"
    "ds_read_b128 a[124:127], v252 offset:43008\n"
    "ds_read_b32 v245, v247\n"
    "v_mfma_f32_16x16x32_bf16 v[32:35], a[148:151], a[180:183], v[32:35]\n"
    "v_exp_f32 v6, v6\n"
    "v_mfma_f32_16x16x32_bf16 v[8:11], a[152:155], a[208:211], v[8:11]\n"
    "ds_read_b128 a[152:155], v252 offset:46080\n"
    "v_cvt_pk_bf16_f32 v217, v42, v43\n"
    "v_mfma_f32_16x16x32_bf16 v[8:11], a[156:159], a[212:215], v[8:11]\n"
    "v_exp_f32 v19, v19\n"
    "v_mfma_f32_16x16x32_bf16 v[24:27], a[156:159], a[164:167], v[24:27]\n"
    "v_exp_f32 v51, v51\n"
    "v_mfma_f32_16x16x32_bf16 v[32:35], a[144:147], a[176:179], v[32:35]\n"
    "s_waitcnt lgkmcnt(7)\n"
    "v_cvt_pk_bf16_f32 v219, v58, v59\n"
    "v_cvt_pk_bf16_f32 v233, v50, v51\n"
    "v_mfma_f32_16x16x32_bf16 v[36:39], a[112:115], a[192:195], v[100:103]\n"
    "s_waitcnt lgkmcnt(3)\n"
    "v_exp_f32 v21, v21\n"
    "v_mfma_f32_16x16x32_bf16 v[40:43], a[132:135], a[236:239], v[100:103]\n"
    "ds_read_b128 a[156:159], v252 offset:48128\n"
    "v_cvt_pk_bf16_f32 v227, v62, v63\n"
    "v_mfma_f32_16x16x32_bf16 v[36:39], a[116:119], a[196:199], v[36:39]\n"
    "s_add_u32 s59, s59, 4\n"
    "v_exp_f32 v28, v28\n"
    "v_mfma_f32_16x16x32_bf16 v[32:35], a[140:143], a[172:175], v[32:35]\n"
    "v_exp_f32 v52, v52\n"
    "v_mfma_f32_16x16x32_bf16 v[32:35], a[136:139], a[168:171], v[32:35]\n"
    "v_cvt_pk_bf16_f32 v232, v48, v49\n"
    "v_mad_u32_u24 v244, v224, 24, v2\n"
    "v_mfma_f32_16x16x32_bf16 v[48:51], a[132:135], a[188:191], v[100:103]\n"
    "ds_read_b128 a[136:139], v252 offset:37888\n"
    "ds_read_b128 v[104:107], v250 offset:1680\n"
    "v_mfma_f32_16x16x32_bf16 v[36:39], a[120:123], a[200:203], v[36:39]\n"
    "s_waitcnt lgkmcnt(5)\n"
    "v_exp_f32 v57, v57\n"
    "v_mfma_f32_16x16x32_bf16 v[36:39], a[124:127], a[204:207], v[36:39]\n"
    "v_cvt_pk_bf16_f32 v225, v46, v47\n"
    "v_cvt_pk_bf16_f32 v210, v52, v53\n"
    "v_mfma_f32_16x16x32_bf16 v[36:39], a[128:131], a[208:211], v[36:39]\n"
    "v_cvt_pk_bf16_f32 v224, v44, v45\n"
    "v_cvt_pk_bf16_f32 v211, v54, v55\n"
    "v_mfma_f32_16x16x32_bf16 v[40:43], a[128:131], a[232:235], v[40:43]\n"
    "ds_read_b128 a[144:147], v252 offset:41984\n"
    "ds_read_b128 a[140:143], v252 offset:39936\n"
    "v_mfma_f32_16x16x32_bf16 v[44:47], a[112:115], a[240:243], v[100:103]\n"
    "v_exp_f32 v20, v20\n"
    "v_mfma_f32_16x16x32_bf16 v[40:43], a[124:127], a[228:231], v[40:43]\n"
    "v_cvt_pk_bf16_f32 v12, v12, v13\n"
    "v_cvt_pk_bf16_f32 v218, v56, v57\n"
    "v_mfma_f32_16x16x32_bf16 v[36:39], a[132:135], a[212:215], v[36:39]\n"
    "v_exp_f32 v32, v32\n"
    "v_mfma_f32_16x16x32_bf16 v[40:43], a[120:123], a[224:227], v[40:43]\n"
    "v_exp_f32 v30, v30\n"
    "v_smfmac_f32_16x16x64_bf16 v[120:123], v[240:243], v[228:235], v253\n"
    "ds_read_b128 a[148:151], v252 offset:44032\n"
    "v_mfma_f32_16x16x32_bf16 v[44:47], a[116:119], a[244:247], v[44:47]\n"
    "s_waitcnt lgkmcnt(3)\n"
    "v_exp_f32 v26, v26\n"
    "v_mfma_f32_16x16x32_bf16 v[52:55], a[136:139], a[192:195], v[104:107]\n"
    "v_exp_f32 v15, v15\n"
    "v_mfma_f32_16x16x32_bf16 v[56:59], a[156:159], a[236:239], v[104:107]\n"
    "s_waitcnt lgkmcnt(1)\n"
    "v_cvt_pk_bf16_f32 v4, v4, v5\n"
    "v_cmp_le_u32 s[56:57], s59, v245\n"
    "v_smfmac_f32_16x16x64_bf16 v[108:111], v[240:243], v[204:211], v253\n"
    "s_cmp_eq_u64 s[56:57], -1\n"
    "v_lshrrev_b32 v252, 3, v1\n"
    "v_mfma_f32_16x16x32_bf16 v[52:55], a[140:143], a[196:199], v[52:55]\n"
    "v_exp_f32 v7, v7\n"
    "v_mfma_f32_16x16x32_bf16 v[60:63], a[136:139], a[240:243], v[104:107]\n"
    "v_exp_f32 v17, v17\n"
    "v_mfma_f32_16x16x32_bf16 v[60:63], a[140:143], a[244:247], v[60:63]\n"
    "v_exp_f32 v14, v14\n"
    "v_mfma_f32_16x16x32_bf16 v[56:59], a[152:155], a[232:235], v[56:59]\n"
    "v_exp_f32 v39, v39\n"
    "v_mfma_f32_16x16x32_bf16 v[64:67], a[156:159], a[188:191], v[104:107]\n"
    "v_exp_f32 v11, v11\n"
    "v_mfma_f32_16x16x32_bf16 v[60:63], a[144:147], a[248:251], v[60:63]\n"
    "v_exp_f32 v22, v22\n"
    "v_mfma_f32_16x16x32_bf16 v[52:55], a[144:147], a[200:203], v[52:55]\n"
    "s_waitcnt lgkmcnt(0)\n"
    "v_exp_f32 v27, v27\n"
    "v_mfma_f32_16x16x32_bf16 v[52:55], a[148:151], a[204:207], v[52:55]\n"
    "v_exp_f32 v8, v8\n"
    "v_mfma_f32_16x16x32_bf16 v[52:55], a[152:155], a[208:211], v[52:55]\n"
    "v_exp_f32 v9, v9\n"
    "v_mfma_f32_16x16x32_bf16 v[56:59], a[148:151], a[228:231], v[56:59]\n"
    "ds_read_b128 a[204:207], v248 offset:13312\n"
    "v_cvt_pk_bf16_f32 v5, v6, v7\n"
    "v_mfma_f32_16x16x32_bf16 v[64:67], a[152:155], a[184:187], v[64:67]\n"
    "v_exp_f32 v16, v16\n"
    "v_mfma_f32_16x16x32_bf16 v[56:59], a[144:147], a[224:227], v[56:59]\n"
    "ds_read_b128 a[208:211], v248 offset:2048\n"
    "ds_read_b128 a[196:199], v248 offset:12288\n"
    "v_mfma_f32_16x16x32_bf16 v[40:43], a[116:119], a[220:223], v[40:43]\n"
    "v_exp_f32_e64 v38, v38\n"
    "v_mfma_f32_16x16x32_bf16 v[52:55], a[156:159], a[212:215], v[52:55]\n"
    "v_exp_f32_e64 v37, v37\n"
    "v_smfmac_f32_16x16x64_bf16 v[116:119], v[240:243], v[220:227], v253\n"
    "v_cvt_pk_bf16_f32 v6, v8, v9\n"
    "v_smfmac_f32_16x16x64_bf16 v[112:115], v[240:243], v[212:219], v253\n"
    "s_cbranch_scc0 .Lpt4\n"
    ".Lpt4r:\n"
    "v_mfma_f32_16x16x32_bf16 v[44:47], a[120:123], a[248:251], v[44:47]\n"
    "v_exp_f32 v10, v10\n"
    "v_mfma_f32_16x16x32_bf16 v[40:43], a[112:115], a[216:219], v[40:43]\n"
    "v_exp_f32 v25, v25\n"
    "v_mfma_f32_16x16x32_bf16 v[48:51], a[128:131], a[184:187], v[48:51]\n"
    "v_exp_f32 v54, v54\n"
    "v_mfma_f32_16x16x32_bf16 v[56:59], a[140:143], a[220:223], v[56:59]\n"
    "s_waitcnt lgkmcnt(1)\n"
    "ds_read_b128 a[212:215], v248 offset:14336\n"
    "v_cvt_pk_bf16_f32 v20, v20, v21\n"
    "v_mfma_f32_16x16x32_bf16 v[148:151], a[208:211], v[76:79], v[148:151]\n"
    "s_waitcnt vmcnt(0)\n"
    "ds_add_u32 v247, v246\n"
    "v_mfma_f32_16x16x32_bf16 v[64:67], a[148:151], a[180:183], v[64:67]\n"
    "ds_read_b128 a[192:195], v248 offset:0\n"
    "ds_read_b128 a[200:203], v248 offset:1024\n"
    "v_mfma_f32_16x16x32_bf16 v[48:51], a[124:127], a[180:183], v[48:51]\n"
    "v_exp_f32 v52, v52\n"
    "v_mfma_f32_16x16x32_bf16 v[128:131], a[204:207], v[72:75], v[128:131]\n"
    "v_exp_f32 v41, v41\n"
    "v_mfma_f32_16x16x32_bf16 v[144:147], a[204:207], v[80:83], v[144:147]\n"
    "v_exp_f32 v36, v36\n"
    "v_mfma_f32_16x16x32_bf16 v[44:47], a[124:127], a[252:255], v[44:47]\n"
    "v_exp_f32 v24, v24\n"
    "v_mfma_f32_16x16x32_bf16 v[60:63], a[148:151], a[252:255], v[60:63]\n"
    "v_exp_f32 v23, v23\n"
    "v_mfma_f32_16x16x32_bf16 v[44:47], a[128:131], a[160:163], v[44:47]\n"
    "v_exp_f32 v55, v55\n"
    "v_mfma_f32_16x16x32_bf16 v[48:51], a[120:123], a[176:179], v[48:51]\n"
    "v_exp_f32 v29, v29\n"
    "v_mfma_f32_16x16x32_bf16 v[64:67], a[144:147], a[176:179], v[64:67]\n"
    "ds_read_b128 a[236:239], v249 offset:37888\n"
    "v_cvt_pk_bf16_f32 v9, v38, v39\n"
    "v_mfma_f32_16x16x32_bf16 v[56:59], a[136:139], a[216:219], v[56:59]\n"
    "ds_read_b128 a[220:223], v248 offset:15360\n"
    "ds_read_b128 a[244:247], v249 offset:38912\n"
    "v_mfma_f32_16x16x32_bf16 v[48:51], a[116:119], a[172:175], v[48:51]\n"
    "s_waitcnt lgkmcnt(6)\n"
    "ds_read_b128 a[232:235], v249 offset:25600\n"
    "ds_read_b128 a[240:243], v249 offset:26624\n"
    "v_mfma_f32_16x16x32_bf16 v[64:67], a[140:143], a[172:175], v[64:67]\n"
    "s_add_u32 s59, s59, 4\n"
    "v_cvt_pk_bf16_f32 v7, v10, v11\n"
    "v_cvt_pk_bf16_f32 v13, v14, v15\n"
    "v_mfma_f32_16x16x32_bf16 v[148:151], a[212:215], v[80:83], v[148:151]\n"
    "v_exp_f32 v53, v53\n"
    "v_mfma_f32_16x16x32_bf16 v[44:47], a[132:135], a[164:167], v[44:47]\n"
    "ds_read_b32 v245, v247\n"
    "v_cvt_pk_bf16_f32 v11, v54, v55\n"
    "v_mfma_f32_16x16x32_bf16 v[60:63], a[152:155], a[160:163], v[60:63]\n"
    "ds_read_b128 v[240:243], v251 offset:1536\n"
    "ds_read_b128 a[216:219], v248 offset:3072\n"
    "v_mfma_f32_16x16x32_bf16 v[64:67], a[136:139], a[168:171], v[64:67]\n"
    "s_waitcnt lgkmcnt(8)\n"
    "v_cvt_pk_bf16_f32 v8, v36, v37\n"
    "v_cvt_pk_bf16_f32 v10, v52, v53\n"
    "v_mfma_f32_16x16x32_bf16 v[128:131], a[200:203], v[68:71], v[128:131]\n"
    "v_exp_f32 v43, v43\n"
    "v_mfma_f32_16x16x32_bf16 v[60:63], a[156:159], a[164:167], v[60:63]\n"
    "v_exp_f32 v45, v45\n"
    "v_mfma_f32_16x16x32_bf16 v[140:143], a[192:195], v[76:79], v[140:143]\n"
    "ds_read_b128 a[252:255], v249 offset:39936\n"
    "ds_read_b128 a[224:227], v249 offset:24576\n"
    "v_mfma_f32_16x16x32_bf16 v[124:127], a[192:195], v[68:71], v[124:127]\n"
    "s_waitcnt lgkmcnt(9)\n"
    "v_cvt_pk_bf16_f32 v14, v16, v17\n"
    "v_cvt_pk_bf16_f32 v21, v22, v23\n"
    "v_mfma_f32_16x16x32_bf16 v[128:131], a[236:239], v[208:211], v[128:131]\n"
    "v_exp_f32 v58, v58\n"
    "v_mfma_f32_16x16x32_bf16 v[140:143], a[196:199], v[80:83], v[140:143]\n"
    "s_waitcnt lgkmcnt(4)\n"
    "ds_read_b128 a[228:231], v249 offset:36864\n"
    "v_cmp_le_u32 s[56:57], s59, v245\n"
    "v_mfma_f32_16x16x32_bf16 v[124:127], a[196:199], v[72:75], v[124:127]\n"
    "v_exp_f32 v62, v62\n"
    "v_mfma_f32_16x16x32_bf16 v[148:151], a[240:243], v[212:215], v[148:151]\n"
    "s_cmp_eq_u64 s[56:57], -1\n"
    "v_exp_f32 v40, v40\n"
    "v_mfma_f32_16x16x32_bf16 v[132:135], a[208:211], v[68:71], v[132:135]\n"
    "s_waitcnt lgkmcnt(4)\n"
    "v_exp_f32 v42, v42\n"
    "v_smfmac_f32_16x16x64_bf16 v[108:111], v[240:243], v[4:11], v253\n"
    "s_cbranch_scc0 .Lpt3\n"
    ".Lpt3r:\n"
    "v_mfma_f32_16x16x32_bf16 v[48:51], a[112:115], a[168:171], v[48:51]\n"
    "s_waitcnt lgkmcnt(1)\n"
    "ds_read_b128 a[176:179], v249 offset:2048\n"
    "ds_read_b128 a[168:171], v249 offset:1024\n"
    "v_mfma_f32_16x16x32_bf16 v[124:127], a[224:227], v[204:207], v[124:127]\n"
    "v_exp_f32 v18, v18\n"
    "v_mfma_f32_16x16x32_bf16 v[136:139], a[220:223], v[72:75], v[136:139]\n"
    "v_cvt_pk_bf16_f32 v16, v40, v41\n"
    "v_cvt_pk_bf16_f32 v15, v18, v19\n"
    "v_mfma_f32_16x16x32_bf16 v[152:155], a[220:223], v[80:83], v[152:155]\n"
    "s_waitcnt lgkmcnt(2)\n"
    "v_exp_f32 v61, v61\n"
    "v_mfma_f32_16x16x32_bf16 v[124:127], a[228:231], v[208:211], v[124:127]\n"
    "v_exp_f32 v63, v63\n"
    "v_mov_b32 v111, v110\n"
    "v_mfma_f32_16x16x32_bf16 v[152:155], a[216:219], v[76:79], v[152:155]\n"
    "v_add_f32 v108, v108, v109\n"
    "v_cvt_pk_bf16_f32 v22, v24, v25\n"
    "v_mfma_f32_16x16x32_bf16 v[132:135], a[212:215], v[72:75], v[132:135]\n"
    "ds_read_b128 a[188:191], v249 offset:15360\n"
    "ds_read_b128 a[248:251], v249 offset:27648\n"
    "v_mfma_f32_16x16x32_bf16 v[136:139], a[216:219], v[68:71], v[136:139]\n"
    "ds_read_b128 a[172:175], v249 offset:13312\n"
    "ds_read_b128 a[180:183], v249 offset:14336\n"
    "v_mfma_f32_16x16x32_bf16 v[144:147], a[200:203], v[76:79], v[144:147]\n"
    "v_exp_f32 v35, v35\n"
    "v_mfma_f32_16x16x32_bf16 v[132:135], a[240:243], v[204:207], v[132:135]\n"
    "ds_read_b128 a[160:163], v249 offset:0\n"
    "v_mov_b32 v109, v108\n"
    "v_mfma_f32_16x16x32_bf16 v[136:139], a[252:255], v[208:211], v[136:139]\n"
    "ds_read_b128 a[164:167], v249 offset:12288\n"
    "ds_read_b128 a[184:187], v249 offset:3072\n"
    "v_mfma_f32_16x16x32_bf16 v[132:135], a[244:247], v[208:211], v[132:135]\n"
    "s_waitcnt lgkmcnt(8)\n"
    "v_exp_f32 v46, v46\n"
    "v_mfma_f32_16x16x32_bf16 v[132:135], a[176:179], v[4:7], v[132:135]\n"
    "v_exp_f32 v59, v59\n"
    "v_mfma_f32_16x16x32_bf16 v[144:147], a[236:239], v[216:219], v[144:147]\n"
    "v_exp_f32 v67, v67\n"
    "v_mfma_f32_16x16x32_bf16 v[144:147], a[232:235], v[212:215], v[144:147]\n"
    "v_permlane16_swap_b32 v108, v109\n"
    "v_mfma_f32_16x16x32_bf16 v[128:131], a[232:235], v[204:207], v[128:131]\n"
    "s_waitcnt lgkmcnt(3)\n"
    "v_exp_f32 v44, v44\n"
    "v_mfma_f32_16x16x32_bf16 v[132:135], a[180:183], v[8:11], v[132:135]\n"
    "v_cvt_pk_bf16_f32 v23, v26, v27\n"
    "v_cvt_pk_bf16_f32 v27, v62, v63\n"
    "v_mfma_f32_16x16x32_bf16 v[136:139], a[248:251], v[204:207], v[136:139]\n"
    "s_waitcnt lgkmcnt(2)\n"
    "v_exp_f32 v57, v57\n"
    "v_mfma_f32_16x16x32_bf16 v[124:127], a[160:163], v[4:7], v[124:127]\n"
    "v_exp_f32 v56, v56\n"
    "v_mfma_f32_16x16x32_bf16 v[140:143], a[224:227], v[212:215], v[140:143]\n"
    "s_waitcnt lgkmcnt(1)\n"
    "v_and_b32 v252, 0x70, v252\n"
    "v_add_f32 v108, v108, v109\n"
    "v_mfma_f32_16x16x32_bf16 v[124:127], a[164:167], v[8:11], v[124:127]\n"
    "v_permlane16_swap_b32 v110, v111\n"
    "v_mfma_f32_16x16x32_bf16 v[128:131], a[172:175], v[8:11], v[128:131]\n"
    "v_add_f32 v110, v110, v111\n"
    "v_cvt_pk_bf16_f32 v18, v56, v57\n"
    "v_mfma_f32_16x16x32_bf16 v[128:131], a[168:171], v[4:7], v[128:131]\n"
    ".Lwp1:\n"
    "s_cbranch_execz .Lwp2\n"
    "v_rcp_f32 v109, v108\n"
    "v_mfma_f32_16x16x32_bf16 v[136:139], a[188:191], v[8:11], v[136:139]\n"
    "s_waitcnt lgkmcnt(0)\n"
    "v_exp_f32 v60, v60\n"
    "v_mfma_f32_16x16x32_bf16 v[136:139], a[184:187], v[4:7], v[136:139]\n"
    "v_mul_f32 v111, -v110, v109\n"
    "v_cvt_pk_bf16_f32 v19, v58, v59\n"
    "v_cvt_pk_bf16_f32 v17, v42, v43\n"
    "v_xor_b32 v252, v252, v1\n"
    "v_ldexp_f32 v109, v109, s79\n"
    "v_pk_fma_f32 v[134:135], v[134:135], v[108:109], v[110:111] op_sel:[0,1,1] op_sel_hi:[1,1,1]\n"
    "v_pk_fma_f32 v[124:125], v[124:125], v[108:109], v[110:111] op_sel:[0,1,1] op_sel_hi:[1,1,1]\n"
    "v_pk_fma_f32 v[126:127], v[126:127], v[108:109], v[110:111] op_sel:[0,1,1] op_sel_hi:[1,1,1]\n"
    "v_pk_fma_f32 v[128:129], v[128:129], v[108:109], v[110:111] op_sel:[0,1,1] op_sel_hi:[1,1,1]\n"
    "v_pk_fma_f32 v[132:133], v[132:133], v[108:109], v[110:111] op_sel:[0,1,1] op_sel_hi:[1,1,1]\n"
    "v_pk_fma_f32 v[130:131], v[130:131], v[108:109], v[110:111] op_sel:[0,1,1] op_sel_hi:[1,1,1]\n"
    "v_pk_fma_f32 v[138:139], v[138:139], v[108:109], v[110:111] op_sel:[0,1,1] op_sel_hi:[1,1,1]\n"
    "v_pk_fma_f32 v[136:137], v[136:137], v[108:109], v[110:111] op_sel:[0,1,1] op_sel_hi:[1,1,1]\n"
    "v_cvt_pk_bf16_f32 v124, v124, v128\n"
    "v_cvt_pk_bf16_f32 v128, v126, v130\n"
    "v_mfma_f32_16x16x32_bf16 v[152:155], a[252:255], v[216:219], v[152:155]\n"
    "v_cvt_pk_bf16_f32 v130, v127, v131\n"
    "v_cvt_pk_bf16_f32 v126, v125, v129\n"
    "v_cvt_pk_bf16_f32 v127, v133, v137\n"
    "v_mfma_f32_16x16x32_bf16 v[148:151], a[244:247], v[216:219], v[148:151]\n"
    "v_add_u32 v252, s72, v252\n"
    "v_cvt_pk_bf16_f32 v125, v132, v136\n"
    "v_mfma_f32_16x16x32_bf16 v[140:143], a[228:231], v[216:219], v[140:143]\n"
    "ds_write_b128 v3, v[124:127] offset:0\n"
    "v_smfmac_f32_16x16x64_bf16 v[112:115], v[240:243], v[12:19], v253\n"
    "v_cvt_pk_bf16_f32 v129, v134, v138\n"
    "v_cvt_pk_bf16_f32 v26, v60, v61\n"
    "v_mfma_f32_16x16x32_bf16 v[148:151], a[176:179], v[12:15], v[148:151]\n"
    "v_exp_f32 v51, v51\n"
    "v_mfma_f32_16x16x32_bf16 v[140:143], a[160:163], v[12:15], v[140:143]\n"
    "v_cvt_pk_bf16_f32 v131, v135, v139\n"
    "v_xor_b32 v2, 64, v3\n"
    "v_mfma_f32_16x16x32_bf16 v[156:159], a[192:195], v[84:87], v[156:159]\n"
    "ds_write_b128 v2, v[128:131] offset:0\n"
    "v_mfma_f32_16x16x32_bf16 v[156:159], a[196:199], v[88:91], v[156:159]\n"
    "v_exp_f32 v47, v47\n"
    "v_mfma_f32_16x16x32_bf16 v[152:155], a[248:251], v[212:215], v[152:155]\n"
    "v_cvt_pk_bf16_f32 v24, v44, v45\n"
    "v_cvt_pk_bf16_f32 v25, v46, v47\n"
    "v_mfma_f32_16x16x32_bf16 v[156:159], a[224:227], v[220:223], v[156:159]\n"
    "ds_read_b128 v[132:135], v252 offset:0\n"
    "v_add_f32 v112, v112, v113\n"
    "v_mfma_f32_16x16x32_bf16 v[140:143], a[164:167], v[16:19], v[140:143]\n"
    "v_exp_f32 v34, v34\n"
    "v_mfma_f32_16x16x32_bf16 v[144:147], a[172:175], v[16:19], v[144:147]\n"
    "v_mov_b32 v113, v112\n"
    "v_mov_b32 v115, v114\n"
    "v_mfma_f32_16x16x32_bf16 v[144:147], a[168:171], v[12:15], v[144:147]\n"
    "ds_read_b128 v[136:139], v252 offset:1024\n"
    "v_permlane16_swap_b32 v112, v113\n"
    "v_mfma_f32_16x16x32_bf16 v[164:167], a[208:211], v[84:87], v[164:167]\n"
    "v_permlane16_swap_b32 v114, v115\n"
    "v_mfma_f32_16x16x32_bf16 v[152:155], a[188:191], v[16:19], v[152:155]\n"
    "v_exp_f32_e64 v48, v48\n"
    "v_mfma_f32_16x16x32_bf16 v[148:151], a[180:183], v[16:19], v[148:151]\n"
    "v_add_f32 v112, v112, v113\n"
    "v_cvt_pk_bf16_f32 v28, v28, v29\n"
    "v_mfma_f32_16x16x32_bf16 v[152:155], a[184:187], v[12:15], v[152:155]\n"
    "v_rcp_f32 v113, v112\n"
    "v_smfmac_f32_16x16x64_bf16 v[116:119], v[240:243], v[20:27], v253\n"
    "s_waitcnt lgkmcnt(1)\n"
    "buffer_store_dwordx4 v[132:135], v244, s[24:27], s75 offen offset:0 sc1\n"
    "v_add_f32 v114, v114, v115\n"
    "v_mul_f32 v115, -v114, v113\n"
    "v_ldexp_f32 v113, v113, s79\n"
    "v_pk_fma_f32 v[142:143], v[142:143], v[112:113], v[114:115] op_sel:[0,1,1] op_sel_hi:[1,1,1]\n"
    "v_pk_fma_f32 v[144:145], v[144:145], v[112:113], v[114:115] op_sel:[0,1,1] op_sel_hi:[1,1,1]\n"
    "v_pk_fma_f32 v[140:141], v[140:141], v[112:113], v[114:115] op_sel:[0,1,1] op_sel_hi:[1,1,1]\n"
    "v_pk_fma_f32 v[146:147], v[146:147], v[112:113], v[114:115] op_sel:[0,1,1] op_sel_hi:[1,1,1]\n"
    "v_pk_fma_f32 v[150:151], v[150:151], v[112:113], v[114:115] op_sel:[0,1,1] op_sel_hi:[1,1,1]\n"
    "v_add_f32_e64 v116, v116, v117\n"
    "v_pk_fma_f32 v[152:153], v[152:153], v[112:113], v[114:115] op_sel:[0,1,1] op_sel_hi:[1,1,1]\n"
    "v_pk_fma_f32 v[148:149], v[148:149], v[112:113], v[114:115] op_sel:[0,1,1] op_sel_hi:[1,1,1]\n"
    "v_pk_fma_f32 v[154:155], v[154:155], v[112:113], v[114:115] op_sel:[0,1,1] op_sel_hi:[1,1,1]\n"
    "v_cvt_pk_bf16_f32 v140, v140, v144\n"
    "v_cvt_pk_bf16_f32 v144, v142, v146\n"
    "v_mfma_f32_16x16x32_bf16 v[160:163], a[204:207], v[88:91], v[160:163]\n"
    "v_cvt_pk_bf16_f32 v146, v143, v147\n"
    "v_cvt_pk_bf16_f32 v142, v141, v145\n"
    "v_mfma_f32_16x16x32_bf16 v[156:159], a[228:231], v[224:227], v[156:159]\n"
    "v_cvt_pk_bf16_f32 v141, v148, v152\n"
    "v_cvt_pk_bf16_f32 v143, v149, v153\n"
    "v_mfma_f32_16x16x32_bf16 v[156:159], a[160:163], v[20:23], v[156:159]\n"
    "s_waitcnt lgkmcnt(0)\n"
    "buffer_store_dwordx4 v[136:139], v244, s[24:27], s75 offen offset:3072 sc1\n"
    "ds_write_b128 v3, v[140:143] offset:2048\n"
    "v_mfma_f32_16x16x32_bf16 v[172:175], a[192:195], v[92:95], v[172:175]\n"
    "v_mov_b32 v119, v118\n"
    "v_cvt_pk_bf16_f32 v145, v150, v154\n"
    "v_mfma_f32_16x16x32_bf16 v[156:159], a[164:167], v[24:27], v[156:159]\n"
    "v_cvt_pk_bf16_f32 v147, v151, v155\n"
    "v_mov_b32 v117, v116\n"
    "v_mfma_f32_16x16x32_bf16 v[180:183], a[208:211], v[92:95], v[180:183]\n"
    "ds_write_b128 v2, v[144:147] offset:2048\n"
    "v_mfma_f32_16x16x32_bf16 v[160:163], a[200:203], v[84:87], v[160:163]\n"
    "v_permlane16_swap_b32 v118, v119\n"
    "v_mfma_f32_16x16x32_bf16 v[168:171], a[220:223], v[88:91], v[168:171]\n"
    "v_exp_f32 v33, v33\n"
    "v_mfma_f32_16x16x32_bf16 v[172:175], a[196:199], v[96:99], v[172:175]\n"
    "ds_read_b128 v[148:151], v252 offset:2048\n"
    "ds_read_b128 v[152:155], v252 offset:3072\n"
    "v_mfma_f32_16x16x32_bf16 v[180:183], a[212:215], v[96:99], v[180:183]\n"
    "v_exp_f32 v49, v49\n"
    "v_mfma_f32_16x16x32_bf16 v[160:163], a[236:239], v[224:227], v[160:163]\n"
    "v_permlane16_swap_b32 v116, v117\n"
    "v_mfma_f32_16x16x32_bf16 v[168:171], a[216:219], v[84:87], v[168:171]\n"
    "v_exp_f32 v31, v31\n"
    "v_mfma_f32_16x16x32_bf16 v[176:179], a[204:207], v[96:99], v[176:179]\n"
    "v_exp_f32 v66, v66\n"
    "v_mfma_f32_16x16x32_bf16 v[164:167], a[212:215], v[88:91], v[164:167]\n"
    "ds_read_b128 a[204:207], v248 offset:17408\n"
    "v_add_f32 v118, v118, v119\n"
    "v_mfma_f32_16x16x32_bf16 v[160:163], a[232:235], v[220:223], v[160:163]\n"
    "v_cvt_pk_bf16_f32 v29, v30, v31\n"
    "v_add_f32 v116, v116, v117\n"
    "v_mfma_f32_16x16x32_bf16 v[168:171], a[252:255], v[224:227], v[168:171]\n"
    "v_rcp_f32 v117, v116\n"
    "v_mfma_f32_16x16x32_bf16 v[160:163], a[172:175], v[24:27], v[160:163]\n"
    "s_waitcnt lgkmcnt(1)\n"
    "buffer_store_dwordx4 v[152:155], v244, s[24:27], s76 offen offset:3072 sc1\n"
    "v_exp_f32 v50, v50\n"
    "v_exp_f32 v64, v64\n"
    "v_mfma_f32_16x16x32_bf16 v[176:179], a[200:203], v[92:95], v[176:179]\n"
    "ds_read_b128 a[196:199], v248 offset:16384\n"
    "v_cvt_pk_bf16_f32 v31, v34, v35\n"
    "v_mfma_f32_16x16x32_bf16 v[160:163], a[168:171], v[20:23], v[160:163]\n"
    "v_mul_f32 v119, -v118, v117\n"
    "v_cvt_pk_bf16_f32 v30, v32, v33\n"
    "v_mfma_f32_16x16x32_bf16 v[164:167], a[240:243], v[220:223], v[164:167]\n"
    "buffer_store_dwordx4 v[148:151], v244, s[24:27], s76 offen offset:0 sc1\n"
    "v_cvt_pk_bf16_f32 v35, v66, v67\n"
    "v_ldexp_f32 v117, v117, s79\n"
    "v_exp_f32 v65, v65\n"
    "v_mfma_f32_16x16x32_bf16 v[184:187], a[220:223], v[96:99], v[184:187]\n"
    "v_fma_f32 v156, v156, v117, v119\n"
    "v_fma_f32 v157, v157, v117, v119\n"
    "v_mfma_f32_16x16x32_bf16 v[168:171], a[248:251], v[220:223], v[168:171]\n"
    "v_fma_f32 v158, v158, v117, v119\n"
    "v_fma_f32 v159, v159, v117, v119\n"
    "v_mfma_f32_16x16x32_bf16 v[164:167], a[244:247], v[224:227], v[164:167]\n"
    "v_fma_f32 v160, v160, v117, v119\n"
    "v_fma_f32 v161, v161, v117, v119\n"
    "v_mfma_f32_16x16x32_bf16 v[168:171], a[188:191], v[24:27], v[168:171]\n"
    "v_fma_f32 v162, v162, v117, v119\n"
    "v_fma_f32 v163, v163, v117, v119\n"
    "v_mfma_f32_16x16x32_bf16 v[172:175], a[224:227], v[228:231], v[172:175]\n"
    "ds_read_b128 a[224:227], v249 offset:28672\n"
    "v_cvt_pk_bf16_f32 v34, v64, v65\n"
    "v_mfma_f32_16x16x32_bf16 v[168:171], a[184:187], v[20:23], v[168:171]\n"
    "v_cvt_pk_bf16_f32 v156, v156, v160\n"
    "v_cvt_pk_bf16_f32 v160, v158, v162\n"
    "v_mfma_f32_16x16x32_bf16 v[164:167], a[176:179], v[20:23], v[164:167]\n"
    "v_cvt_pk_bf16_f32 v162, v159, v163\n"
    "v_cvt_pk_bf16_f32 v32, v48, v49\n"
    "v_mfma_f32_16x16x32_bf16 v[164:167], a[180:183], v[24:27], v[164:167]\n"
    "ds_read_b128 a[192:195], v248 offset:4096\n"
    "v_cvt_pk_bf16_f32 v158, v157, v161\n"
    "v_mfma_f32_16x16x32_bf16 v[180:183], a[240:243], v[228:231], v[180:183]\n"
    "v_cvt_pk_bf16_f32 v33, v50, v51\n"
    "v_fma_f32 v168, v168, v117, v119\n"
    "v_fma_f32 v169, v169, v117, v119\n"
    "v_mfma_f32_16x16x32_bf16 v[184:187], a[216:219], v[92:95], v[184:187]\n"
    "v_fma_f32 v170, v170, v117, v119\n"
    "v_fma_f32 v171, v171, v117, v119\n"
    "v_smfmac_f32_16x16x64_bf16 v[120:123], v[240:243], v[28:35], v253\n"
    "ds_read_b128 a[220:223], v248 offset:19456\n"
    "v_mfma_f32_16x16x32_bf16 v[184:187], a[252:255], v[232:235], v[184:187]\n"
    "v_fma_f32 v164, v164, v117, v119\n"
    "v_fma_f32 v165, v165, v117, v119\n"
    "v_mfma_f32_16x16x32_bf16 v[172:175], a[228:231], v[232:235], v[172:175]\n"
    "v_cvt_pk_bf16_f32 v157, v164, v168\n"
    "v_cvt_pk_bf16_f32 v159, v165, v169\n"
    "v_mfma_f32_16x16x32_bf16 v[176:179], a[236:239], v[232:235], v[176:179]\n"
    "ds_write_b128 v3, v[156:159] offset:4096\n"
    "v_mfma_f32_16x16x32_bf16 v[180:183], a[244:247], v[232:235], v[180:183]\n"
    "s_waitcnt lgkmcnt(2)\n"
    "v_fma_f32 v166, v166, v117, v119\n"
    "v_fma_f32 v167, v167, v117, v119\n"
    "v_mfma_f32_16x16x32_bf16 v[188:191], a[192:195], v[68:71], v[188:191]\n"
    "v_cvt_pk_bf16_f32 v163, v167, v171\n"
    "v_cvt_pk_bf16_f32 v161, v166, v170\n"
    "v_mfma_f32_16x16x32_bf16 v[184:187], a[248:251], v[228:231], v[184:187]\n"
    "ds_write_b128 v2, v[160:163] offset:4096\n"
    "v_mfma_f32_16x16x32_bf16 v[192:195], a[204:207], v[72:75], v[192:195]\n"
    "v_add_f32_e64 v120, v120, v121\n"
    "v_mov_b32_e64 v121, v120\n"
    "v_mfma_f32_16x16x32_bf16 v[188:191], a[196:199], v[72:75], v[188:191]\n"
    "s_nop 0\n"
    "v_permlane16_swap_b32 v120, v121\n"
    "v_mfma_f32_16x16x32_bf16 v[176:179], a[232:235], v[228:231], v[176:179]\n"
    "s_waitcnt lgkmcnt(2)\n"
    "ds_read_b128 v[164:167], v252 offset:4096\n"
    "ds_read_b128 a[228:231], v249 offset:40960\n"
    "v_mfma_f32_16x16x32_bf16 v[200:203], a[220:223], v[72:75], v[200:203]\n"
    "ds_read_b128 a[212:215], v248 offset:18432\n"
    "ds_read_b128 v[168:171], v252 offset:5120\n"
    "v_mfma_f32_16x16x32_bf16 v[180:183], a[176:179], v[28:31], v[180:183]\n"
    "ds_read_b128 a[132:135], v249 offset:18432\n"
    "v_mov_b32 v123, v122\n"
    "v_mfma_f32_16x16x32_bf16 v[176:179], a[172:175], v[32:35], v[176:179]\n"
    "ds_read_b128 a[208:211], v248 offset:6144\n"
    "ds_read_b128 a[216:219], v248 offset:7168\n"
    "v_mfma_f32_16x16x32_bf16 v[188:191], a[224:227], v[204:207], v[188:191]\n"
    "ds_read_b128 a[248:251], v249 offset:31744\n"
    "ds_read_b128 a[236:239], v249 offset:41984\n"
    "v_mfma_f32_16x16x32_bf16 v[184:187], a[188:191], v[32:35], v[184:187]\n"
    "v_permlane16_swap_b32_e64 v122, v123\n"
    "v_mfma_f32_16x16x32_bf16 v[172:175], a[160:163], v[28:31], v[172:175]\n"
    "ds_read_b128 a[140:143], v249 offset:19456\n"
    "ds_read_b128 a[200:203], v248 offset:5120\n"
    "v_mfma_f32_16x16x32_bf16 v[180:183], a[180:183], v[32:35], v[180:183]\n"
    "ds_read_b128 a[136:139], v249 offset:7168\n"
    "ds_read_b128 a[124:127], v249 offset:17408\n"
    "v_mfma_f32_16x16x32_bf16 v[184:187], a[184:187], v[28:31], v[184:187]\n"
    "s_waitcnt lgkmcnt(12)\n"
    "buffer_store_dwordx4 v[164:167], v244, s[24:27], s77 offen offset:0 sc1\n"
    "ds_read_b128 a[240:243], v249 offset:30720\n"
    "ds_read_b128 a[252:255], v249 offset:44032\n"
    "v_add_f32 v122, v122, v123\n"
    "v_add_f32 v120, v120, v121\n"
    "v_mfma_f32_16x16x32_bf16 v[176:179], a[168:171], v[28:31], v[176:179]\n"
    "ds_read_b128 a[112:115], v249 offset:4096\n"
    "ds_read_b128 a[128:131], v249 offset:6144\n"
    "v_mfma_f32_16x16x32_bf16 v[172:175], a[164:167], v[32:35], v[172:175]\n"
    "s_waitcnt lgkmcnt(11)\n"
    "v_rcp_f32 v121, v120\n"
    "v_mfma_f32_16x16x32_bf16 v[196:199], a[208:211], v[68:71], v[196:199]\n"
    "ds_read_b128 a[244:247], v249 offset:43008\n"
    "v_mul_f32 v123, -v122, v121\n"
    "v_mfma_f32_16x16x32_bf16 v[188:191], a[228:231], v[208:211], v[188:191]\n"
    "buffer_store_dwordx4 v[168:171], v244, s[24:27], s77 offen offset:3072 sc1\n"
    "ds_read_b128 a[232:235], v249 offset:29696\n"
    "ds_read_b128 a[116:119], v249 offset:16384\n"
    "ds_read_b128 a[120:123], v249 offset:5120\n"
    "v_ldexp_f32 v121, v121, s79\n"
    "v_mfma_f32_16x16x32_bf16 v[196:199], a[212:215], v[72:75], v[196:199]\n"
    "s_waitcnt lgkmcnt(10)\n"
    "v_fma_f32 v174, v174, v121, v123\n"
    "v_fma_f32 v175, v175, v121, v123\n"
    "v_mfma_f32_16x16x32_bf16 v[192:195], a[200:203], v[68:71], v[192:195]\n"
    "v_fma_f32 v176, v176, v121, v123\n"
    "v_fma_f32 v177, v177, v121, v123\n"
    "v_mfma_f32_16x16x32_bf16 v[200:203], a[216:219], v[68:71], v[200:203]\n"
    "s_waitcnt lgkmcnt(7)\n"
    "v_fma_f32 v186, v186, v121, v123\n"
    "v_fma_f32 v187, v187, v121, v123\n"
    "v_mfma_f32_16x16x32_bf16 v[196:199], a[240:243], v[204:207], v[196:199]\n"
    "s_waitcnt lgkmcnt(6)\n"
    "v_fma_f32 v182, v182, v121, v123\n"
    "v_fma_f32 v183, v183, v121, v123\n"
    "v_mfma_f32_16x16x32_bf16 v[200:203], a[252:255], v[208:211], v[200:203]\n"
    "v_fma_f32 v178, v178, v121, v123\n"
    "v_fma_f32 v179, v179, v121, v123\n"
    "v_mfma_f32_16x16x32_bf16 v[200:203], a[248:251], v[204:207], v[200:203]\n"
    "s_waitcnt lgkmcnt(3)\n"
    "v_fma_f32 v172, v172, v121, v123\n"
    "v_fma_f32 v173, v173, v121, v123\n"
    "v_mfma_f32_16x16x32_bf16 v[196:199], a[244:247], v[208:211], v[196:199]\n"
    "v_cvt_pk_bf16_f32 v172, v172, v176\n"
    "v_cvt_pk_bf16_f32 v176, v174, v178\n"
    "v_mfma_f32_16x16x32_bf16 v[192:195], a[236:239], v[208:211], v[192:195]\n"
    "s_waitcnt lgkmcnt(2)\n"
    "v_cvt_pk_bf16_f32 v174, v173, v177\n"
    "v_cvt_pk_bf16_f32 v178, v175, v179\n"
    "v_mfma_f32_16x16x32_bf16 v[192:195], a[232:235], v[204:207], v[192:195]\n"
    "v_cvt_pk_bf16_f32 v177, v182, v186\n"
    "v_cvt_pk_bf16_f32 v179, v183, v187\n"
    "v_mfma_f32_16x16x32_bf16 v[196:199], a[128:131], v[4:7], v[196:199]\n"
    "v_fma_f32 v180, v180, v121, v123\n"
    "v_fma_f32 v181, v181, v121, v123\n"
    "v_mfma_f32_16x16x32_bf16 v[188:191], a[112:115], v[4:7], v[188:191]\n"
    "s_waitcnt lgkmcnt(1)\n"
    "ds_write_b128 v2, v[176:179] offset:0\n"
    "v_mfma_f32_16x16x32_bf16 v[188:191], a[116:119], v[8:11], v[188:191]\n"
    "v_fma_f32 v184, v184, v121, v123\n"
    "v_fma_f32 v185, v185, v121, v123\n"
    "v_mfma_f32_16x16x32_bf16 v[192:195], a[124:127], v[8:11], v[192:195]\n"
    "v_cvt_pk_bf16_f32 v175, v181, v185\n"
    "v_cvt_pk_bf16_f32 v173, v180, v184\n"
    "v_mfma_f32_16x16x32_bf16 v[196:199], a[132:135], v[8:11], v[196:199]\n"
    "ds_write_b128 v3, v[172:175] offset:0\n"
    "v_mfma_f32_16x16x32_bf16 a[4:7], a[204:207], v[80:83], a[4:7]\n"
    "v_fma_f32 v188, v188, v109, v111\n"
    "v_fma_f32 v189, v189, v109, v111\n"
    "v_mfma_f32_16x16x32_bf16 a[0:3], a[192:195], v[76:79], a[0:3]\n"
    "s_waitcnt lgkmcnt(2)\n"
    "v_fma_f32 v190, v190, v109, v111\n"
    "v_fma_f32 v191, v191, v109, v111\n"
    "v_mfma_f32_16x16x32_bf16 v[192:195], a[120:123], v[4:7], v[192:195]\n"
    "ds_read_b128 v[180:183], v252 offset:0\n"
    "ds_read_b128 v[184:187], v252 offset:1024\n"
    "v_mfma_f32_16x16x32_bf16 v[200:203], a[140:143], v[8:11], v[200:203]\n"
    "v_fma_f32 v198, v198, v109, v111\n"
    "v_fma_f32 v199, v199, v109, v111\n"
    "v_mfma_f32_16x16x32_bf16 a[0:3], a[196:199], v[80:83], a[0:3]\n"
    "v_fma_f32 v196, v196, v109, v111\n"
    "v_fma_f32 v197, v197, v109, v111\n"
    "v_mfma_f32_16x16x32_bf16 a[12:15], a[220:223], v[80:83], a[12:15]\n"
    "v_fma_f32 v192, v192, v109, v111\n"
    "v_fma_f32 v193, v193, v109, v111\n"
    "v_mfma_f32_16x16x32_bf16 a[4:7], a[200:203], v[76:79], a[4:7]\n"
    "ds_read_b128 a[164:167], v249 offset:20480\n"
    "ds_read_b128 a[180:183], v249 offset:22528\n"
    "v_mfma_f32_16x16x32_bf16 v[200:203], a[136:139], v[4:7], v[200:203]\n"
    "v_fma_f32 v194, v194, v109, v111\n"
    "v_fma_f32 v195, v195, v109, v111\n"
    "v_mfma_f32_16x16x32_bf16 a[0:3], a[224:227], v[212:215], a[0:3]\n"
    "ds_read_b128 a[172:175], v249 offset:21504\n"
    "v_mfma_f32_16x16x32_bf16 a[4:7], a[236:239], v[216:219], a[4:7]\n"
    "v_cvt_pk_bf16_f32 v188, v188, v192\n"
    "v_cvt_pk_bf16_f32 v192, v190, v194\n"
    "v_mfma_f32_16x16x32_bf16 a[0:3], a[228:231], v[216:219], a[0:3]\n"
    "s_waitcnt lgkmcnt(4)\n"
    "buffer_store_dwordx4 v[180:183], v244, s[24:27], s78 offen offset:0 sc1\n"
    "v_cvt_pk_bf16_f32 v190, v189, v193\n"
    "v_cvt_pk_bf16_f32 v194, v191, v195\n"
    "v_fma_f32 v200, v200, v109, v111\n"
    "v_fma_f32 v201, v201, v109, v111\n"
    "v_mfma_f32_16x16x32_bf16 a[0:3], a[112:115], v[12:15], a[0:3]\n"
    "v_cvt_pk_bf16_f32 v189, v196, v200\n"
    "v_cvt_pk_bf16_f32 v191, v197, v201\n"
    "v_mfma_f32_16x16x32_bf16 a[0:3], a[116:119], v[16:19], a[0:3]\n"
    "ds_read_b128 a[176:179], v249 offset:10240\n"
    "v_mfma_f32_16x16x32_bf16 a[4:7], a[232:235], v[212:215], a[4:7]\n"
    "s_waitcnt lgkmcnt(4)\n"
    "buffer_store_dwordx4 v[184:187], v244, s[24:27], s78 offen offset:3072 sc1\n"
    "ds_write_b128 v3, v[188:191] offset:2048\n"
    "v_mfma_f32_16x16x32_bf16 a[8:11], a[208:211], v[76:79], a[8:11]\n"
    "v_fma_f32 v202, v202, v109, v111\n"
    "v_fma_f32 v203, v203, v109, v111\n"
    "v_mfma_f32_16x16x32_bf16 a[12:15], a[216:219], v[76:79], a[12:15]\n"
    "v_cvt_pk_bf16_f32 v195, v199, v203\n"
    "v_cvt_pk_bf16_f32 v193, v198, v202\n"
    "v_mfma_f32_16x16x32_bf16 a[12:15], a[252:255], v[216:219], a[12:15]\n"
    "ds_write_b128 v2, v[192:195] offset:2048\n"
    "v_mfma_f32_16x16x32_bf16 a[4:7], a[124:127], v[16:19], a[4:7]\n"
    "v_accvgpr_read_b32 v40, a1\n"
    "v_mfma_f32_16x16x32_bf16 a[20:23], a[204:207], v[88:91], a[20:23]\n"
    "v_accvgpr_read_b32 v56, a3\n"
    "v_mov_b32_e64 v250, 0\n"
    "v_mfma_f32_16x16x32_bf16 a[4:7], a[120:123], v[12:15], a[4:7]\n"
    "ds_read_b128 v[200:203], v252 offset:3072\n"
    "ds_read_b128 v[196:199], v252 offset:2048\n"
    "v_mfma_f32_16x16x32_bf16 a[8:11], a[212:215], v[80:83], a[8:11]\n"
    "ds_read_b128 a[188:191], v249 offset:23552\n"
    "v_mfma_f32_16x16x32_bf16 a[8:11], a[240:243], v[212:215], a[8:11]\n"
    "v_accvgpr_read_b32 v36, a0\n"
    "v_accvgpr_read_b32 v52, a2\n"
    "v_mfma_f32_16x16x32_bf16 a[8:11], a[244:247], v[216:219], a[8:11]\n"
    "v_accvgpr_read_b32 v41, a5\n"
    "v_accvgpr_read_b32 v57, a7\n"
    "v_mfma_f32_16x16x32_bf16 a[8:11], a[128:131], v[12:15], a[8:11]\n"
    "v_accvgpr_read_b32 v37, a4\n"
    "v_accvgpr_read_b32 v53, a6\n"
    "v_mfma_f32_16x16x32_bf16 a[8:11], a[132:135], v[16:19], a[8:11]\n"
    "v_fma_f32 v40, v40, v113, v115\n"
    "v_fma_f32 v41, v41, v113, v115\n"
    "v_mfma_f32_16x16x32_bf16 a[12:15], a[248:251], v[212:215], a[12:15]\n"
    "ds_read_b128 a[160:163], v249 offset:8192\n"
    "v_mfma_f32_16x16x32_bf16 a[12:15], a[140:143], v[16:19], a[12:15]\n"
    "v_fma_f32 v52, v52, v113, v115\n"
    "v_fma_f32 v53, v53, v113, v115\n"
    "v_mfma_f32_16x16x32_bf16 a[12:15], a[136:139], v[12:15], a[12:15]\n"
    "s_waitcnt lgkmcnt(3)\n"
    "buffer_store_dwordx4 v[200:203], v244, s[24:27], s75 offen offset:3200 sc1\n"
    "v_cvt_pk_bf16_f32 v52, v52, v53\n"
    "v_fma_f32 v36, v36, v113, v115\n"
    "v_fma_f32 v37, v37, v113, v115\n"
    "v_accvgpr_read_b32 v38, a8\n"
    "v_mfma_f32_16x16x32_bf16 a[16:19], a[192:195], v[84:87], a[16:19]\n"
    "ds_read_b128 a[168:171], v249 offset:9216\n"
    "v_accvgpr_read_b32 v42, a9\n"
    "v_mfma_f32_16x16x32_bf16 a[16:19], a[196:199], v[88:91], a[16:19]\n"
    "v_accvgpr_read_b32 v54, a10\n"
    "v_accvgpr_read_b32 v39, a12\n"
    "v_mfma_f32_16x16x32_bf16 a[20:23], a[200:203], v[84:87], a[20:23]\n"
    "v_accvgpr_read_b32 v55, a14\n"
    "v_accvgpr_read_b32 v58, a11\n"
    "v_mfma_f32_16x16x32_bf16 a[16:19], a[224:227], v[220:223], a[16:19]\n"
    "v_accvgpr_read_b32 v59, a15\n"
    "v_cvt_pk_bf16_f32 v36, v36, v37\n"
    "v_mfma_f32_16x16x32_bf16 a[20:23], a[236:239], v[224:227], a[20:23]\n"
    "v_fma_f32 v38, v38, v113, v115\n"
    "v_fma_f32 v39, v39, v113, v115\n"
    "v_mfma_f32_16x16x32_bf16 a[24:27], a[208:211], v[84:87], a[24:27]\n"
    "v_fma_f32 v54, v54, v113, v115\n"
    "v_fma_f32 v55, v55, v113, v115\n"
    "v_mfma_f32_16x16x32_bf16 a[20:23], a[232:235], v[220:223], a[20:23]\n"
    "v_accvgpr_read_b32 v43, a13\n"
    "v_cvt_pk_bf16_f32 v53, v54, v55\n"
    "v_mfma_f32_16x16x32_bf16 a[20:23], a[124:127], v[24:27], a[20:23]\n"
    "v_fma_f32 v42, v42, v113, v115\n"
    "v_fma_f32 v43, v43, v113, v115\n"
    "v_mfma_f32_16x16x32_bf16 a[16:19], a[228:231], v[224:227], a[16:19]\n"
    "ds_read_b128 a[184:187], v249 offset:11264\n"
    "v_cvt_pk_bf16_f32 v37, v38, v39\n"
    "v_mfma_f32_16x16x32_bf16 a[20:23], a[120:123], v[20:23], a[20:23]\n"
    "v_cvt_pk_bf16_f32 v39, v42, v43\n"
    "v_cvt_pk_bf16_f32 v38, v40, v41\n"
    "v_mfma_f32_16x16x32_bf16 a[28:31], a[220:223], v[88:91], a[28:31]\n"
    "s_waitcnt lgkmcnt(4)\n"
    "buffer_store_dwordx4 v[196:199], v244, s[24:27], s75 offen offset:128 sc1\n"
    "ds_write_b128 v3, v[36:39] offset:4096\n"
    "v_mfma_f32_16x16x32_bf16 a[16:19], a[112:115], v[20:23], a[16:19]\n"
    "v_fma_f32 v58, v58, v113, v115\n"
    "v_fma_f32 v59, v59, v113, v115\n"
    "v_mfma_f32_16x16x32_bf16 a[16:19], a[116:119], v[24:27], a[16:19]\n"
    "v_fma_f32 v56, v56, v113, v115\n"
    "v_fma_f32 v57, v57, v113, v115\n"
    "v_cvt_pk_bf16_f32 v55, v58, v59\n"
    "v_mfma_f32_16x16x32_bf16 a[28:31], a[216:219], v[84:87], a[28:31]\n"
    "v_accvgpr_read_b32 v49, a21\n"
    "v_cvt_pk_bf16_f32 v54, v56, v57\n"
    "v_mfma_f32_16x16x32_bf16 a[24:27], a[212:215], v[88:91], a[24:27]\n"
    "ds_write_b128 v2, v[52:55] offset:4096\n"
    "v_mfma_f32_16x16x32_bf16 a[32:35], a[192:195], v[92:95], a[32:35]\n"
    "v_accvgpr_read_b32 v64, a19\n"
    "v_accvgpr_read_b32 v45, a20\n"
    "v_mfma_f32_16x16x32_bf16 a[24:27], a[240:243], v[220:223], a[24:27]\n"
    "v_accvgpr_read_b32 v44, a16\n"
    "v_accvgpr_read_b32 v65, a23\n"
    "v_mfma_f32_16x16x32_bf16 a[24:27], a[244:247], v[224:227], a[24:27]\n"
    "ds_read_b128 v[56:59], v252 offset:5120\n"
    "v_accvgpr_read_b32 v48, a17\n"
    "v_mfma_f32_16x16x32_bf16 a[28:31], a[252:255], v[224:227], a[28:31]\n"
    "v_fma_f32 v64, v64, v117, v119\n"
    "v_fma_f32 v65, v65, v117, v119\n"
    "v_mfma_f32_16x16x32_bf16 a[24:27], a[128:131], v[20:23], a[24:27]\n"
    "ds_read_b128 v[40:43], v252 offset:4096\n"
    "v_accvgpr_read_b32 v60, a18\n"
    "v_mfma_f32_16x16x32_bf16 a[24:27], a[132:135], v[24:27], a[24:27]\n"
    "v_fma_f32 v44, v44, v117, v119\n"
    "v_fma_f32 v45, v45, v117, v119\n"
    "v_mfma_f32_16x16x32_bf16 a[28:31], a[248:251], v[220:223], a[28:31]\n"
    "ds_read_b128 a[192:195], v248 offset:8192\n"
    "v_accvgpr_read_b32 v61, a22\n"
    "v_mfma_f32_16x16x32_bf16 a[28:31], a[140:143], v[24:27], a[28:31]\n"
    "v_fma_f32 v48, v48, v117, v119\n"
    "v_fma_f32 v49, v49, v117, v119\n"
    "v_mfma_f32_16x16x32_bf16 a[28:31], a[136:139], v[20:23], a[28:31]\n"
    "v_fma_f32 v60, v60, v117, v119\n"
    "v_fma_f32 v61, v61, v117, v119\n"
    "v_mfma_f32_16x16x32_bf16 a[40:43], a[208:211], v[92:95], a[40:43]\n"
    "v_accvgpr_read_b32 v62, a26\n"
    "v_accvgpr_read_b32 v66, a27\n"
    "v_mfma_f32_16x16x32_bf16 a[36:39], a[204:207], v[96:99], a[36:39]\n"
    "s_waitcnt lgkmcnt(2)\n"
    "buffer_store_dwordx4 v[56:59], v244, s[24:27], s76 offen offset:3200 sc1\n"
    "v_accvgpr_read_b32 v46, a24\n"
    "v_accvgpr_read_b32 v63, a30\n"
    "v_accvgpr_read_b32 v47, a28\n"
    "v_accvgpr_read_b32 v51, a29\n"
    "v_mfma_f32_16x16x32_bf16 a[40:43], a[212:215], v[96:99], a[40:43]\n"
    "v_fma_f32 v62, v62, v117, v119\n"
    "v_fma_f32 v63, v63, v117, v119\n"
    "v_mfma_f32_16x16x32_bf16 a[44:47], a[220:223], v[96:99], a[44:47]\n"
    "v_accvgpr_read_b32 v50, a25\n"
    "v_cvt_pk_bf16_f32 v44, v44, v45\n"
    "v_cvt_pk_bf16_f32 v60, v60, v61\n"
    "v_mfma_f32_16x16x32_bf16 a[36:39], a[200:203], v[92:95], a[36:39]\n"
    "v_fma_f32 v50, v50, v117, v119\n"
    "v_fma_f32 v51, v51, v117, v119\n"
    "v_mfma_f32_16x16x32_bf16 a[44:47], a[216:219], v[92:95], a[44:47]\n"
    "v_fma_f32 v46, v46, v117, v119\n"
    "v_fma_f32 v47, v47, v117, v119\n"
    "v_mfma_f32_16x16x32_bf16 a[36:39], a[236:239], v[232:235], a[36:39]\n"
    "v_cvt_pk_bf16_f32 v61, v62, v63\n"
    "v_cvt_pk_bf16_f32 v45, v46, v47\n"
    "v_mfma_f32_16x16x32_bf16 a[40:43], a[240:243], v[228:231], a[40:43]\n"
    "v_accvgpr_read_b32 v67, a31\n"
    "v_cvt_pk_bf16_f32 v47, v50, v51\n"
    "v_mfma_f32_16x16x32_bf16 a[36:39], a[232:235], v[228:231], a[36:39]\n"
    "ds_read_b128 a[208:211], v248 offset:10240\n"
    "v_cvt_pk_bf16_f32 v46, v48, v49\n"
    "v_mfma_f32_16x16x32_bf16 a[36:39], a[124:127], v[32:35], a[36:39]\n"
    "s_waitcnt lgkmcnt(2)\n"
    "buffer_store_dwordx4 v[40:43], v244, s[24:27], s76 offen offset:128 sc1\n"
    "ds_write_b128 v3, v[44:47] offset:0\n"
    "v_mfma_f32_16x16x32_bf16 a[36:39], a[120:123], v[28:31], a[36:39]\n"
    "v_fma_f32 v66, v66, v117, v119\n"
    "v_fma_f32 v67, v67, v117, v119\n"
    "v_mfma_f32_16x16x32_bf16 a[40:43], a[244:247], v[232:235], a[40:43]\n"
    "v_cvt_pk_bf16_f32 v62, v64, v65\n"
    "v_cvt_pk_bf16_f32 v63, v66, v67\n"
    "v_mfma_f32_16x16x32_bf16 a[44:47], a[252:255], v[232:235], a[44:47]\n"
    "ds_write_b128 v2, v[60:63] offset:0\n"
    "v_mfma_f32_16x16x32_bf16 a[32:35], a[196:199], v[96:99], a[32:35]\n"
    "v_accvgpr_read_b32 v125, a36\n"
    "v_fma_f32 v250, |v110|, 4.0, v250\n"
    "v_mfma_f32_16x16x32_bf16 a[44:47], a[248:251], v[228:231], a[44:47]\n"
    "v_fma_f32 v250, v108, 4.0, v250\n"
    "v_accvgpr_read_b32 v137, a39\n"
    "v_mfma_f32_16x16x32_bf16 a[32:35], a[224:227], v[228:231], a[32:35]\n"
    "ds_read_b128 a[236:239], v249 offset:46080\n"
    "ds_read_b128 v[48:51], v252 offset:0\n"
    "ds_read_b128 a[212:215], v248 offset:22528\n"
    "v_mfma_f32_16x16x32_bf16 a[32:35], a[228:231], v[232:235], a[32:35]\n"
    "ds_read_b128 a[224:227], v249 offset:32768\n"
    "v_accvgpr_read_b32 v133, a38\n"
    "v_mfma_f32_16x16x32_bf16 a[40:43], a[128:131], v[28:31], a[40:43]\n"
    "ds_read_b128 a[204:207], v248 offset:21504\n"
    "v_mfma_f32_16x16x32_bf16 a[44:47], a[140:143], v[32:35], a[44:47]\n"
    "ds_read_b128 v[64:67], v252 offset:1024\n"
    "ds_read_b128 a[240:243], v249 offset:34816\n"
    "v_mfma_f32_16x16x32_bf16 a[32:35], a[112:115], v[28:31], a[32:35]\n"
    "ds_read_b128 a[216:219], v248 offset:11264\n"
    "ds_read_b128 a[196:199], v248 offset:20480\n"
    "v_mfma_f32_16x16x32_bf16 a[32:35], a[116:119], v[32:35], a[32:35]\n"
    "s_waitcnt lgkmcnt(12)\n"
    "ds_read_b128 a[244:247], v249 offset:47104\n"
    "ds_read_b128 a[200:203], v248 offset:9216\n"
    "v_mfma_f32_16x16x32_bf16 a[48:51], a[192:195], v[68:71], a[48:51]\n"
    "ds_read_b128 a[232:235], v249 offset:33792\n"
    "ds_read_b128 a[252:255], v249 offset:48128\n"
    "v_mfma_f32_16x16x32_bf16 a[40:43], a[132:135], v[32:35], a[40:43]\n"
    "s_nop 0\n"
    "v_accvgpr_read_b32 v124, a32\n"
    "v_mfma_f32_16x16x32_bf16 a[44:47], a[136:139], v[28:31], a[44:47]\n"
    "s_waitcnt lgkmcnt(11)\n"
    "buffer_store_dwordx4 v[48:51], v244, s[24:27], s77 offen offset:128 sc1\n"
    "v_accvgpr_read_b32 v128, a33\n"
    "v_accvgpr_read_b32 v132, a34\n"
    "v_fma_f32 v132, v132, v121, v123\n"
    "v_fma_f32 v133, v133, v121, v123\n"
    "v_accvgpr_read_b32 v134, a42\n"
    "v_mfma_f32_16x16x32_bf16 a[56:59], a[208:211], v[68:71], a[56:59]\n"
    "s_waitcnt lgkmcnt(8)\n"
    "v_accvgpr_read_b32 v129, a37\n"
    "v_accvgpr_read_b32 v131, a45\n"
    "v_mfma_f32_16x16x32_bf16 a[52:55], a[204:207], v[72:75], a[52:55]\n"
    "v_accvgpr_read_b32 v130, a41\n"
    "v_accvgpr_read_b32 v138, a43\n"
    "v_mfma_f32_16x16x32_bf16 a[56:59], a[212:215], v[72:75], a[56:59]\n"
    "s_waitcnt lgkmcnt(4)\n"
    "v_accvgpr_read_b32 v135, a46\n"
    "v_fma_f32 v124, v124, v121, v123\n"
    "v_fma_f32 v125, v125, v121, v123\n"
    "v_mfma_f32_16x16x32_bf16 a[48:51], a[196:199], v[72:75], a[48:51]\n"
    "s_waitcnt lgkmcnt(2)\n"
    "ds_read_b128 a[228:231], v249 offset:45056\n"
    "ds_read_b128 a[248:251], v249 offset:35840\n"
    "v_mfma_f32_16x16x32_bf16 a[52:55], a[200:203], v[68:71], a[52:55]\n"
    ".Lwp2:\n"
    "s_cbranch_execz .Lw3\n"
    "v_fma_f32 v130, v130, v121, v123\n"
    "v_fma_f32 v131, v131, v121, v123\n"
    "v_mfma_f32_16x16x32_bf16 a[56:59], a[240:243], v[204:207], a[56:59]\n"
    "v_accvgpr_read_b32 v126, a40\n"
    "v_fma_f32 v134, v134, v121, v123\n"
    "v_fma_f32 v135, v135, v121, v123\n"
    "v_mfma_f32_16x16x32_bf16 a[48:51], a[224:227], v[204:207], a[48:51]\n"
    "ds_read_b128 a[220:223], v248 offset:23552\n"
    "v_accvgpr_read_b32 v139, a47\n"
    "v_mfma_f32_16x16x32_bf16 a[56:59], a[244:247], v[208:211], a[56:59]\n"
    "buffer_store_dwordx4 v[64:67], v244, s[24:27], s77 offen offset:3200 sc1\n"
    "v_accvgpr_read_b32 v136, a35\n"
    "v_accvgpr_read_b32 v127, a44\n"
    "v_fma_f32 v138, v138, v121, v123\n"
    "v_fma_f32 v139, v139, v121, v123\n"
    "v_mfma_f32_16x16x32_bf16 a[56:59], a[176:179], v[4:7], a[56:59]\n"
    "v_fma_f32 v126, v126, v121, v123\n"
    "v_fma_f32 v127, v127, v121, v123\n"
    "v_mfma_f32_16x16x32_bf16 a[56:59], a[180:183], v[8:11], a[56:59]\n"
    "v_cvt_pk_bf16_f32 v124, v124, v125\n"
    "v_cvt_pk_bf16_f32 v125, v126, v127\n"
    "v_mfma_f32_16x16x32_bf16 a[52:55], a[236:239], v[208:211], a[52:55]\n"
    "s_waitcnt lgkmcnt(2)\n"
    "v_fma_f32 v128, v128, v121, v123\n"
    "v_fma_f32 v129, v129, v121, v123\n"
    "v_mfma_f32_16x16x32_bf16 a[48:51], a[228:231], v[208:211], a[48:51]\n"
    "v_cvt_pk_bf16_f32 v132, v132, v133\n"
    "v_cvt_pk_bf16_f32 v126, v128, v129\n"
    "v_mfma_f32_16x16x32_bf16 a[52:55], a[232:235], v[204:207], a[52:55]\n"
    "v_cvt_pk_bf16_f32 v133, v134, v135\n"
    "v_cvt_pk_bf16_f32 v127, v130, v131\n"
    "v_mfma_f32_16x16x32_bf16 a[52:55], a[172:175], v[8:11], a[52:55]\n"
    "s_waitcnt lgkmcnt(0)\n"
    "ds_write_b128 v3, v[124:127] offset:2048\n"
    "v_mfma_f32_16x16x32_bf16 a[60:63], a[220:223], v[72:75], a[60:63]\n"
    "v_fma_f32 v136, v136, v121, v123\n"
    "v_fma_f32 v137, v137, v121, v123\n"
    "v_mfma_f32_16x16x32_bf16 a[52:55], a[168:171], v[4:7], a[52:55]\n"
    "v_cvt_pk_bf16_f32 v135, v138, v139\n"
    "v_cvt_pk_bf16_f32 v134, v136, v137\n"
    "v_mfma_f32_16x16x32_bf16 a[60:63], a[216:219], v[68:71], a[60:63]\n"
    "ds_write_b128 v2, v[132:135] offset:2048\n"
    "v_mfma_f32_16x16x32_bf16 a[48:51], a[160:163], v[4:7], a[48:51]\n"
    "v_accvgpr_read_b32 v146, a57\n"
    "v_mfma_f32_16x16x32_bf16 a[48:51], a[164:167], v[8:11], a[48:51]\n"
    "ds_read_b128 v[128:131], v252 offset:2048\n"
    "v_mfma_f32_16x16x32_bf16 a[60:63], a[252:255], v[208:211], a[60:63]\n"
    "v_accvgpr_read_b32 v141, a52\n"
    "v_accvgpr_read_b32 v153, a55\n"
    "v_mfma_f32_16x16x32_bf16 a[64:67], a[192:195], v[76:79], a[64:67]\n"
    "v_accvgpr_read_b32 v149, a54\n"
    "v_accvgpr_read_b32 v150, a58\n"
    "v_mfma_f32_16x16x32_bf16 a[60:63], a[248:251], v[204:207], a[60:63]\n"
    "ds_read_b128 v[136:139], v252 offset:3072\n"
    "v_accvgpr_read_b32 v142, a56\n"
    "v_mfma_f32_16x16x32_bf16 a[60:63], a[188:191], v[8:11], a[60:63]\n"
    "v_accvgpr_read_b32 v145, a53\n"
    "v_accvgpr_read_b32 v148, a50\n"
    "v_mfma_f32_16x16x32_bf16 a[60:63], a[184:187], v[4:7], a[60:63]\n"
    "v_accvgpr_read_b32 v154, a59\n"
    "v_accvgpr_read_b32 v140, a48\n"
    "v_mfma_f32_16x16x32_bf16 a[64:67], a[196:199], v[80:83], a[64:67]\n"
    "v_fma_f32 v140, v140, v109, v111\n"
    "v_fma_f32 v141, v141, v109, v111\n"
    "v_mfma_f32_16x16x32_bf16 a[68:71], a[204:207], v[80:83], a[68:71]\n"
    "s_nop 1\n"
    "v_accvgpr_read_b32 v143, a60\n"
    "v_mfma_f32_16x16x32_bf16 a[68:71], a[200:203], v[76:79], a[68:71]\n"
    "s_waitcnt lgkmcnt(1)\n"
    "buffer_store_dwordx4 v[128:131], v244, s[24:27], s78 offen offset:128 sc1\n"
    "v_fma_f32 v148, v148, v109, v111\n"
    "v_fma_f32 v149, v149, v109, v111\n"
    "v_accvgpr_read_b32 v144, a49\n"
    "v_cvt_pk_bf16_f32 v148, v148, v149\n"
    "v_mfma_f32_16x16x32_bf16 a[76:79], a[220:223], v[80:83], a[76:79]\n"
    "v_cvt_pk_bf16_f32 v140, v140, v141\n"
    "v_accvgpr_read_b32 v152, a51\n"
    "v_mfma_f32_16x16x32_bf16 a[76:79], a[216:219], v[76:79], a[76:79]\n"
    "v_fma_f32 v142, v142, v109, v111\n"
    "v_fma_f32 v143, v143, v109, v111\n"
    "v_mfma_f32_16x16x32_bf16 a[64:67], a[224:227], v[212:215], a[64:67]\n"
    "v_fma_f32 v144, v144, v109, v111\n"
    "v_fma_f32 v145, v145, v109, v111\n"
    "v_accvgpr_read_b32 v151, a62\n"
    "v_mfma_f32_16x16x32_bf16 a[68:71], a[236:239], v[216:219], a[68:71]\n"
    "v_accvgpr_read_b32 v155, a63\n"
    "s_nop 0\n"
    "v_mfma_f32_16x16x32_bf16 a[68:71], a[232:235], v[212:215], a[68:71]\n"
    "s_waitcnt lgkmcnt(0)\n"
    "buffer_store_dwordx4 v[136:139], v244, s[24:27], s78 offen offset:3200 sc1\n"
    "v_fma_f32 v150, v150, v109, v111\n"
    "v_fma_f32 v151, v151, v109, v111\n"
    "v_cvt_pk_bf16_f32 v141, v142, v143\n"
    "v_accvgpr_read_b32 v147, a61\n"
    "v_mfma_f32_16x16x32_bf16 a[72:75], a[208:211], v[76:79], a[72:75]\n"
    "v_fma_f32 v146, v146, v109, v111\n"
    "v_fma_f32 v147, v147, v109, v111\n"
    "v_mfma_f32_16x16x32_bf16 a[64:67], a[228:231], v[216:219], a[64:67]\n"
    "v_mfma_f32_16x16x32_bf16 a[68:71], a[172:175], v[16:19], a[68:71]\n"
    "v_fma_f32 v154, v154, v109, v111\n"
    "v_fma_f32 v155, v155, v109, v111\n"
    "v_mfma_f32_16x16x32_bf16 a[64:67], a[160:163], v[12:15], a[64:67]\n"
    "v_cvt_pk_bf16_f32 v149, v150, v151\n"
    "v_cvt_pk_bf16_f32 v142, v144, v145\n"
    "v_cvt_pk_bf16_f32 v143, v146, v147\n"
    "v_mfma_f32_16x16x32_bf16 a[68:71], a[168:171], v[12:15], a[68:71]\n"
    "ds_write_b128 v3, v[140:143] offset:4096\n"
    "v_mfma_f32_16x16x32_bf16 a[72:75], a[212:215], v[80:83], a[72:75]\n"
    "v_fma_f32 v152, v152, v109, v111\n"
    "v_fma_f32 v153, v153, v109, v111\n"
    "v_mfma_f32_16x16x32_bf16 a[72:75], a[240:243], v[212:215], a[72:75]\n"
    "v_cvt_pk_bf16_f32 v150, v152, v153\n"
    "v_cvt_pk_bf16_f32 v151, v154, v155\n"
    "v_mfma_f32_16x16x32_bf16 a[92:95], a[220:223], v[88:91], a[92:95]\n"
    "ds_write_b128 v2, v[148:151] offset:4096\n"
    "v_mfma_f32_16x16x32_bf16 a[92:95], a[216:219], v[84:87], a[92:95]\n"
    "v_accvgpr_read_b32 v161, a69\n"
    "v_fma_f32 v250, v109, s74, v250\n"
    "v_mfma_f32_16x16x32_bf16 a[64:67], a[164:167], v[16:19], a[64:67]\n"
    "v_fma_f32 v250, |v114|, 4.0, v250\n"
    "v_mfma_f32_16x16x32_bf16 a[72:75], a[244:247], v[216:219], a[72:75]\n"
    "ds_read_b128 v[144:147], v252 offset:4096\n"
    "v_mfma_f32_16x16x32_bf16 a[72:75], a[176:179], v[12:15], a[72:75]\n"
    "ds_read_b128 v[152:155], v252 offset:5120\n"
    "v_accvgpr_read_b32 v165, a70\n"
    "v_mfma_f32_16x16x32_bf16 a[72:75], a[180:183], v[16:19], a[72:75]\n"
    "s_nop 0\n"
    "v_accvgpr_read_b32 v156, a64\n"
    "v_accvgpr_read_b32 v160, a65\n"
    "v_mfma_f32_16x16x32_bf16 a[76:79], a[252:255], v[216:219], a[76:79]\n"
    "v_fma_f32 v160, v160, v113, v115\n"
    "v_fma_f32 v161, v161, v113, v115\n"
    "v_mfma_f32_16x16x32_bf16 a[84:87], a[204:207], v[88:91], a[84:87]\n"
    "s_nop 0\n"
    "v_accvgpr_read_b32 v162, a73\n"
    "v_accvgpr_read_b32 v169, a71\n"
    "v_mfma_f32_16x16x32_bf16 a[80:83], a[192:195], v[84:87], a[80:83]\n"
    "v_fma_f32 v250, v112, 4.0, v250\n"
    "s_nop 0\n"
    "v_mfma_f32_16x16x32_bf16 a[76:79], a[248:251], v[212:215], a[76:79]\n"
    "v_accvgpr_read_b32 v166, a74\n"
    "v_accvgpr_read_b32 v157, a68\n"
    "v_mfma_f32_16x16x32_bf16 a[76:79], a[188:191], v[16:19], a[76:79]\n"
    "v_accvgpr_read_b32 v164, a66\n"
    "v_accvgpr_read_b32 v168, a67\n"
    "v_mfma_f32_16x16x32_bf16 a[76:79], a[184:187], v[12:15], a[76:79]\n"
    "s_waitcnt lgkmcnt(1)\n"
    "buffer_store_dwordx4 v[144:147], v244, s[24:27], s75 offen offset:256 sc1\n"
    "v_fma_f32 v164, v164, v113, v115\n"
    "v_fma_f32 v165, v165, v113, v115\n"
    "v_accvgpr_read_b32 v158, a72\n"
    "v_fma_f32 v156, v156, v113, v115\n"
    "v_fma_f32 v157, v157, v113, v115\n"
    "v_mfma_f32_16x16x32_bf16 a[80:83], a[196:199], v[88:91], a[80:83]\n"
    "v_accvgpr_read_b32 v167, a78\n"
    "v_cvt_pk_bf16_f32 v156, v156, v157\n"
    "v_fma_f32 v250, v113, s74, v250\n"
    "v_mfma_f32_16x16x32_bf16 a[88:91], a[208:211], v[84:87], a[88:91]\n"
    "s_nop 0\n"
    "v_accvgpr_read_b32 v159, a76\n"
    "v_fma_f32 v166, v166, v113, v115\n"
    "v_fma_f32 v167, v167, v113, v115\n"
    "v_mfma_f32_16x16x32_bf16 a[84:87], a[200:203], v[84:87], a[84:87]\n"
    "s_waitcnt lgkmcnt(0)\n"
    "buffer_store_dwordx4 v[152:155], v244, s[24:27], s75 offen offset:3328 sc1\n"
    "v_fma_f32 v158, v158, v113, v115\n"
    "v_fma_f32 v159, v159, v113, v115\n"
    "v_accvgpr_read_b32 v170, a75\n"
    "v_accvgpr_read_b32 v171, a79\n"
    "v_mfma_f32_16x16x32_bf16 a[84:87], a[236:239], v[224:227], a[84:87]\n"
    "v_cvt_pk_bf16_f32 v157, v158, v159\n"
    "v_cvt_pk_bf16_f32 v158, v160, v161\n"
    "v_mfma_f32_16x16x32_bf16 a[84:87], a[232:235], v[220:223], a[84:87]\n"
    "v_cvt_pk_bf16_f32 v164, v164, v165\n"
    "v_accvgpr_read_b32 v163, a77\n"
    "v_mfma_f32_16x16x32_bf16 a[80:83], a[224:227], v[220:223], a[80:83]\n"
    "v_fma_f32 v162, v162, v113, v115\n"
    "v_fma_f32 v163, v163, v113, v115\n"
    "v_mfma_f32_16x16x32_bf16 a[88:91], a[212:215], v[88:91], a[88:91]\n"
    "v_cvt_pk_bf16_f32 v159, v162, v163\n"
    "v_cvt_pk_bf16_f32 v165, v166, v167\n"
    "v_mfma_f32_16x16x32_bf16 a[84:87], a[172:175], v[24:27], a[84:87]\n"
    "ds_write_b128 v3, v[156:159] offset:0\n"
    "v_mfma_f32_16x16x32_bf16 a[84:87], a[168:171], v[20:23], a[84:87]\n"
    "v_fma_f32 v168, v168, v113, v115\n"
    "v_fma_f32 v169, v169, v113, v115\n"
    "v_mfma_f32_16x16x32_bf16 a[92:95], a[252:255], v[224:227], a[92:95]\n"
    "v_fma_f32 v170, v170, v113, v115\n"
    "v_fma_f32 v171, v171, v113, v115\n"
    "v_fma_f32 v250, |v118|, 4.0, v250\n"
    "v_cvt_pk_bf16_f32 v167, v170, v171\n"
    "v_cvt_pk_bf16_f32 v166, v168, v169\n"
    "v_mfma_f32_16x16x32_bf16 a[80:83], a[228:231], v[224:227], a[80:83]\n"
    "ds_write_b128 v2, v[164:167] offset:0\n"
    "v_mfma_f32_16x16x32_bf16 a[80:83], a[160:163], v[20:23], a[80:83]\n"
    "v_mfma_f32_16x16x32_bf16 a[92:95], a[248:251], v[220:223], a[92:95]\n"
    "ds_read_b128 v[160:163], v252 offset:0\n"
    "v_mfma_f32_16x16x32_bf16 a[80:83], a[164:167], v[24:27], a[80:83]\n"
    "ds_read_b128 v[168:171], v252 offset:1024\n"
    "v_accvgpr_read_b32 v173, a84\n"
    "v_mfma_f32_16x16x32_bf16 a[92:95], a[188:191], v[24:27], a[92:95]\n"
    "v_accvgpr_read_b32 v177, a85\n"
    "v_mfma_f32_16x16x32_bf16 a[88:91], a[240:243], v[220:223], a[88:91]\n"
    "v_mfma_f32_16x16x32_bf16 a[88:91], a[244:247], v[224:227], a[88:91]\n"
    "s_nop 1\n"
    "v_accvgpr_read_b32 v176, a81\n"
    "v_mfma_f32_16x16x32_bf16 a[92:95], a[184:187], v[20:23], a[92:95]\n"
    "v_accvgpr_read_b32 v172, a80\n"
    "v_accvgpr_read_b32 v184, a83\n"
    "v_mfma_f32_16x16x32_bf16 a[88:91], a[176:179], v[20:23], a[88:91]\n"
    "v_fma_f32 v172, v172, v117, v119\n"
    "v_fma_f32 v173, v173, v117, v119\n"
    "v_mfma_f32_16x16x32_bf16 a[88:91], a[180:183], v[24:27], a[88:91]\n"
    "v_fma_f32 v176, v176, v117, v119\n"
    "v_fma_f32 v177, v177, v117, v119\n"
    "v_mfma_f32_16x16x32_bf16 a[96:99], a[192:195], v[92:95], a[96:99]\n"
    "s_waitcnt lgkmcnt(1)\n"
    "buffer_store_dwordx4 v[160:163], v244, s[24:27], s76 offen offset:256 sc1\n"
    "v_accvgpr_read_b32 v175, a92\n"
    "v_accvgpr_read_b32 v180, a82\n"
    "v_accvgpr_read_b32 v181, a86\n"
    "v_cvt_pk_bf16_f32 v172, v172, v173\n"
    "v_mfma_f32_16x16x32_bf16 a[108:111], a[220:223], v[96:99], a[108:111]\n"
    "v_accvgpr_read_b32 v179, a93\n"
    "v_accvgpr_read_b32 v174, a88\n"
    "v_mfma_f32_16x16x32_bf16 a[108:111], a[216:219], v[92:95], a[108:111]\n"
    "v_fma_f32 v250, v116, 4.0, v250\n"
    "v_accvgpr_read_b32 v183, a94\n"
    "v_mfma_f32_16x16x32_bf16 a[96:99], a[196:199], v[96:99], a[96:99]\n"
    "v_accvgpr_read_b32 v187, a95\n"
    "v_accvgpr_read_b32 v178, a89\n"
    "v_mfma_f32_16x16x32_bf16 a[100:103], a[204:207], v[96:99], a[100:103]\n"
    "v_mfma_f32_16x16x32_bf16 a[104:107], a[208:211], v[92:95], a[104:107]\n"
    "s_nop 0\n"
    "v_fma_f32 v174, v174, v117, v119\n"
    "v_fma_f32 v175, v175, v117, v119\n"
    "v_mfma_f32_16x16x32_bf16 a[108:111], a[252:255], v[232:235], a[108:111]\n"
    "s_waitcnt lgkmcnt(0)\n"
    "buffer_store_dwordx4 v[168:171], v244, s[24:27], s76 offen offset:3328 sc1\n"
    "v_cvt_pk_bf16_f32 v173, v174, v175\n"
    "v_accvgpr_read_b32 v182, a90\n"
    "v_accvgpr_read_b32 v185, a87\n"
    "v_cvt_pk_bf16_f32 v174, v176, v177\n"
    "v_pk_fma_f32 v[178:179], v[178:179], v[116:117], v[118:119] op_sel:[0,1,1] op_sel_hi:[1,1,1]\n"
    "v_accvgpr_read_b32 v186, a91\n"
    "v_cvt_pk_bf16_f32 v175, v178, v179\n"
    "v_fma_f32 v186, v186, v117, v119\n"
    "v_fma_f32 v187, v187, v117, v119\n"
    "v_pk_fma_f32 v[180:181], v[180:181], v[116:117], v[118:119] op_sel:[0,1,1] op_sel_hi:[1,1,1]\n"
    "v_cmp_eq_u64 s[56:57], v[236:237], v[238:239]\n"
    "v_fma_f32 v182, v182, v117, v119\n"
    "v_fma_f32 v183, v183, v117, v119\n"
    "v_mfma_f32_16x16x32_bf16 a[108:111], a[248:251], v[228:231], a[108:111]\n"
    "ds_write_b128 v3, v[172:175] offset:2048\n"
    "v_cvt_pk_bf16_f32 v180, v180, v181\n"
    "v_mfma_f32_16x16x32_bf16 a[96:99], a[224:227], v[228:231], a[96:99]\n"
    "v_fma_f32 v184, v184, v117, v119\n"
    "v_fma_f32 v185, v185, v117, v119\n"
    "v_cvt_pk_bf16_f32 v181, v182, v183\n"
    "v_mfma_f32_16x16x32_bf16 a[104:107], a[212:215], v[96:99], a[104:107]\n"
    "v_cvt_pk_bf16_f32 v182, v184, v185\n"
    "v_cvt_pk_bf16_f32 v183, v186, v187\n"
    "v_mfma_f32_16x16x32_bf16 a[96:99], a[228:231], v[232:235], a[96:99]\n"
    "ds_write_b128 v2, v[180:183] offset:2048\n"
    "v_mfma_f32_16x16x32_bf16 a[96:99], a[160:163], v[28:31], a[96:99]\n"
    "v_mfma_f32_16x16x32_bf16 a[104:107], a[240:243], v[228:231], a[104:107]\n"
    "ds_read_b128 v[176:179], v252 offset:2048\n"
    "v_mfma_f32_16x16x32_bf16 a[104:107], a[244:247], v[232:235], a[104:107]\n"
    "v_mfma_f32_16x16x32_bf16 a[100:103], a[200:203], v[92:95], a[100:103]\n"
    "v_mfma_f32_16x16x32_bf16 a[104:107], a[176:179], v[28:31], a[104:107]\n"
    "v_mfma_f32_16x16x32_bf16 a[104:107], a[180:183], v[32:35], a[104:107]\n"
    "v_mfma_f32_16x16x32_bf16 a[108:111], a[188:191], v[32:35], a[108:111]\n"
    "s_or_b64 s[56:57], s[56:57], s[34:35]\n"
    "ds_read_b128 v[184:187], v252 offset:3072\n"
    "v_mfma_f32_16x16x32_bf16 a[100:103], a[236:239], v[232:235], a[100:103]\n"
    "v_mfma_f32_16x16x32_bf16 a[96:99], a[164:167], v[32:35], a[96:99]\n"
    "v_mfma_f32_16x16x32_bf16 a[100:103], a[232:235], v[228:231], a[100:103]\n"
    "s_waitcnt lgkmcnt(1)\n"
    "buffer_store_dwordx4 v[176:179], v244, s[24:27], s77 offen offset:256 sc1\n"
    "v_accvgpr_read_b32 v198, a106\n"
    "v_fma_f32 v250, v117, s74, v250\n"
    "v_fma_f32 v250, |v122|, 4.0, v250\n"
    "v_accvgpr_read_b32 v194, a105\n"
    "s_nop 0\n"
    "v_accvgpr_read_b32 v196, a98\n"
    "v_mfma_f32_16x16x32_bf16 a[108:111], a[184:187], v[28:31], a[108:111]\n"
    "v_accvgpr_read_b32 v202, a107\n"
    "v_mfma_f32_16x16x32_bf16 a[100:103], a[172:175], v[32:35], a[100:103]\n"
    "s_nop 0\n"
    "v_mfma_f32_16x16x32_bf16 a[100:103], a[168:171], v[28:31], a[100:103]\n"
    "s_waitcnt lgkmcnt(0)\n"
    "buffer_store_dwordx4 v[184:187], v244, s[24:27], s77 offen offset:3328 sc1\n"
    "v_accvgpr_read_b32 v190, a104\n"
    "s_nop 1\n"
    "v_accvgpr_read_b32 v191, a108\n"
    "v_accvgpr_read_b32 v199, a110\n"
    "v_fma_f32 v250, v120, 4.0, v250\n"
    "v_accvgpr_read_b32 v197, a102\n"
    "v_accvgpr_read_b32 v200, a99\n"
    "v_accvgpr_read_b32 v195, a109\n"
    "v_accvgpr_read_b32 v188, a96\n"
    "v_pk_fma_f32 v[190:191], v[190:191], v[120:121], v[122:123] op_sel:[0,1,1] op_sel_hi:[1,1,1]\n"
    "v_accvgpr_read_b32 v201, a103\n"
    "v_accvgpr_read_b32 v203, a111\n"
    "v_accvgpr_read_b32 v189, a100\n"
    "v_pk_fma_f32 v[194:195], v[194:195], v[120:121], v[122:123] op_sel:[0,1,1] op_sel_hi:[1,1,1]\n"
    "v_fma_f32 v198, v198, v121, v123\n"
    "v_fma_f32 v199, v199, v121, v123\n"
    "v_fma_f32 v188, v188, v121, v123\n"
    "v_fma_f32 v189, v189, v121, v123\n"
    "v_accvgpr_read_b32 v192, a97\n"
    "v_cvt_pk_bf16_f32 v188, v188, v189\n"
    "v_accvgpr_read_b32 v193, a101\n"
    "v_cvt_pk_bf16_f32 v189, v190, v191\n"
    "v_cvt_pk_bf16_f32 v191, v194, v195\n"
    "v_fma_f32 v192, v192, v121, v123\n"
    "v_fma_f32 v193, v193, v121, v123\n"
    "v_cvt_pk_bf16_f32 v190, v192, v193\n"
    "ds_write_b128 v3, v[188:191] offset:4096\n"
    "v_pk_fma_f32 v[196:197], v[196:197], v[120:121], v[122:123] op_sel:[0,1,1] op_sel_hi:[1,1,1]\n"
    "v_cvt_pk_bf16_f32 v196, v196, v197\n"
    "v_cvt_pk_bf16_f32 v197, v198, v199\n"
    "v_pk_fma_f32 v[200:201], v[200:201], v[120:121], v[122:123] op_sel:[0,1,1] op_sel_hi:[1,1,1]\n"
    "v_fma_f32 v250, v121, s74, v250\n"
    "v_pk_fma_f32 v[202:203], v[202:203], v[120:121], v[122:123] op_sel:[0,1,1] op_sel_hi:[1,1,1]\n"
    "v_cvt_pk_bf16_f32 v199, v202, v203\n"
    "v_cvt_pk_bf16_f32 v198, v200, v201\n"
    "ds_write_b128 v2, v[196:199] offset:4096\n"
    "v_cmp_nlt_f32 s[54:55], |v250|, s73\n"
    "ds_read_b128 v[200:203], v252 offset:5120\n"
    "ds_read_b128 v[192:195], v252 offset:4096\n"
    "s_waitcnt lgkmcnt(1)\n"
    "buffer_store_dwordx4 v[200:203], v244, s[24:27], s78 offen offset:3328 sc1\n"
    "s_or_b64 s[54:55], s[54:55], s[56:57]\n"
    "s_waitcnt lgkmcnt(0)\n"
    "buffer_store_dwordx4 v[192:195], v244, s[24:27], s78 offen offset:256 sc1\n"
    "s_or_b32 %0, s54, s55\n"
    "s_cmp_eq_u32 %0, 0\n"
    "s_cbranch_scc1 .Lfin\n"
    "s_waitcnt vmcnt(0)\n"
    ".Lfin:\n"
    ".Lend:\n"
    ".Lend2:\n"
    : "=&s"(bad), "+s"(ka) : "s"(b), "v"(tid)
    : "memory", "vcc", "s4", "s5", "s6", "s7", "s8", "s9", "s10", "s11", "s12", "s13", "s14", "s15", "s16", "s17", "s18", "s19", "s20", "s21", "s22", "s23", "s24", "s25", "s26", "s27", "s28", "s29", "s30", "s31", "s34", "s35", "s36", "s37", "s38", "s39", "s40", "s41", "s42", "s43", "s44", "s45", "s46", "s47", "s48", "s49", "s50", "s51", "s52", "s53", "s54", "s55", "s56", "s57", "s58", "s59", "s60", "s61", "s62", "s63", "s64", "s65", "s66", "s67", "s68", "s69", "s70", "s71", "s72", "s73", "s74", "s75", "s76", "s77", "s78", "s79", "s80", "s81", "s82", "s83", "s84", "s85", "s86", "s87", "s88", "s89", "s90", "s91", "s92", "s93", "s94", "s95", "s96", "s97", "s98", "s99", "v1", "v2", "v3", "v4", "v5", "v6", "v7", "v8", "v9", "v10", "v11", "v12", "v13", "v14", "v15", "v16", "v17", "v18", "v19", "v20", "v21", "v22", "v23", "v24", "v25", "v26", "v27", "v28", "v29", "v30", "v31", "v32", "v33", "v34", "v35", "v36", "v37", "v38", "v39", "v40", "v41", "v42", "v43", "v44", "v45", "v46", "v47", "v48", "v49", "v50", "v51", "v52", "v53", "v54", "v55", "v56", "v57", "v58", "v59", "v60", "v61", "v62", "v63", "v64", "v65", "v66", "v67", "v68", "v69", "v70", "v71", "v72", "v73", "v74", "v75", "v76", "v77", "v78", "v79", "v80", "v81", "v82", "v83", "v84", "v85", "v86", "v87", "v88", "v89", "v90", "v91", "v92", "v93", "v94", "v95", "v96", "v97", "v98", "v99", "v100", "v101", "v102", "v103", "v104", "v105", "v106", "v107", "v108", "v109", "v110", "v111", "v112", "v113", "v114", "v115", "v116", "v117", "v118", "v119", "v120", "v121", "v122", "v123", "v124", "v125", "v126", "v127", "v128", "v129", "v130", "v131", "v132", "v133", "v134", "v135", "v136", "v137", "v138", "v139", "v140", "v141", "v142", "v143", "v144", "v145", "v146", "v147", "v148", "v149", "v150", "v151", "v152", "v153", "v154", "v155", "v156", "v157", "v158", "v159", "v160", "v161", "v162", "v163", "v164", "v165", "v166", "v167", "v168", "v169", "v170", "v171", "v172", "v173", "v174", "v175", "v176", "v177", "v178", "v179", "v180", "v181", "v182", "v183", "v184", "v185", "v186", "v187", "v188", "v189", "v190", "v191", "v192", "v193", "v194", "v195", "v196", "v197", "v198", "v199", "v200", "v201", "v202", "v203", "v204", "v205", "v206", "v207", "v208", "v209", "v210", "v211", "v212", "v213", "v214", "v215", "v216", "v217", "v218", "v219", "v220", "v221", "v222", "v223", "v224", "v225", "v226", "v227", "v228", "v229", "v230", "v231", "v232", "v233", "v234", "v235", "v236", "v237", "v238", "v239", "v240", "v241", "v242", "v243", "v244", "v245", "v246", "v247", "v248", "v249", "v250", "v251", "v252", "v253", "v254", "v255", "a0", "a1", "a2", "a3", "a4", "a5", "a6", "a7", "a8", "a9", "a10", "a11", "a12", "a13", "a14", "a15", "a16", "a17", "a18", "a19", "a20", "a21", "a22", "a23", "a24", "a25", "a26", "a27", "a28", "a29", "a30", "a31", "a32", "a33", "a34", "a35", "a36", "a37", "a38", "a39", "a40", "a41", "a42", "a43", "a44", "a45", "a46", "a47", "a48", "a49", "a50", "a51", "a52", "a53", "a54", "a55", "a56", "a57", "a58", "a59", "a60", "a61", "a62", "a63", "a64", "a65", "a66", "a67", "a68", "a69", "a70", "a71", "a72", "a73", "a74", "a75", "a76", "a77", "a78", "a79", "a80", "a81", "a82", "a83", "a84", "a85", "a86", "a87", "a88", "a89", "a90", "a91", "a92", "a93", "a94", "a95", "a96", "a97", "a98", "a99", "a100", "a101", "a102", "a103", "a104", "a105", "a106", "a107", "a108", "a109", "a110", "a111", "a112", "a113", "a114", "a115", "a116", "a117", "a118", "a119", "a120", "a121", "a122", "a123", "a124", "a125", "a126", "a127", "a128", "a129", "a130", "a131", "a132", "a133", "a134", "a135", "a136", "a137", "a138", "a139", "a140", "a141", "a142", "a143", "a144", "a145", "a146", "a147", "a148", "a149", "a150", "a151", "a152", "a153", "a154", "a155", "a156", "a157", "a158", "a159", "a160", "a161", "a162", "a163", "a164", "a165", "a166", "a167", "a168", "a169", "a170", "a171", "a172", "a173", "a174", "a175", "a176", "a177", "a178", "a179", "a180", "a181", "a182", "a183", "a184", "a185", "a186", "a187", "a188", "a189", "a190", "a191", "a192", "a193", "a194", "a195", "a196", "a197", "a198", "a199", "a200", "a201", "a202", "a203", "a204", "a205", "a206", "a207", "a208", "a209", "a210", "a211", "a212", "a213", "a214", "a215", "a216", "a217", "a218", "a219", "a220", "a221", "a222", "a223", "a224", "a225", "a226", "a227", "a228", "a229", "a230", "a231", "a232", "a233", "a234", "a235", "a236", "a237", "a238", "a239", "a240", "a241", "a242", "a243", "a244", "a245", "a246", "a247", "a248", "a249", "a250", "a251", "a252", "a253", "a254", "a255");
  if (bad) {
    slow_rows((const Args*)ka, (b & 7) * 4 + ((b >> 3) & 3), (b >> 5) * 16 + (tid >> 6) * 128, tid & 63);
  }
}

extern "C" void kernel_launch(void* const* d_in, const int* in_sizes, int n_in,
                              void* d_out, int out_size, void* d_ws, size_t ws_size,
                              hipStream_t stream) {
  (void)hipFuncSetAttribute((const void*)attn_fwd, hipFuncAttributeMaxDynamicSharedMemorySize, SHM);
  attn_fwd<<<256, 256, SHM, stream>>>((const unsigned short*)d_in[0], (const int*)d_in[1], (const int*)d_in[2], (const unsigned short*)d_in[3], (const unsigned short*)d_in[4],
                                      (const unsigned short*)d_in[5], (const unsigned short*)d_in[6], (char*)d_ws, (unsigned short*)d_out);
}
```

```cpp
#include <hip/hip_runtime.h>

constexpr int D = 192, NH = 32, S = 2048;
constexpr int SHM = 155968;

struct Args { const unsigned short* q; const int* qk; const int* qv; const unsigned short* ksc; const unsigned short* kzr;
              const unsigned short* vsc; const unsigned short* vzr; char* ws; unsigned short* o; };

__device__ __forceinline__ float bf2f(unsigned short x) { return __uint_as_float((unsigned)x << 16); }
__device__ __forceinline__ float rbf(float x) { return (float)(__bf16)x; }

__device__ __forceinline__ void slow_rows(const Args* a, unsigned h, unsigned row0, unsigned lane) {
  const unsigned short* q = a->q + (size_t)h * S * D;
  unsigned short* o = a->o + (size_t)h * S * D;
  const int* qk = a->qk + (size_t)h * S * D; const int* qv = a->qv + (size_t)h * S * D;
  const unsigned short* ks = a->ksc + h * 32 * D; const unsigned short* kz = a->kzr + h * 32 * D;
  const unsigned short* vs = a->vsc + h * S; const unsigned short* vz = a->vzr + h * S;
#pragma unroll 1
  for (int n = 0; n < 64; ++n) {
    const int r = (row0 + h * 0 + (n >> 4) * 512 + (n & 15)) % S;
    float m = -INFINITY, l = 0.f, a0 = 0.f, a1 = 0.f, a2 = 0.f;
#pragma unroll 1
    for (int t = 0; t < 32; ++t) {
      const int* kr = qk + (size_t)(t * 64 + lane) * D;
      float s = 0.f;
#pragma unroll 4
      for (int d = 0; d < D; ++d) s = fmaf(bf2f(q[r * D + d]), rbf(rbf(rbf((float)kr[d]) - bf2f(kz[t * D + d])) * bf2f(ks[t * D + d])), s);
      s *= 0.07216878364870322f;
      float mx = s;
      for (int x = 32; x; x >>= 1) mx = fmaxf(mx, __shfl_xor(mx, x));
      const float mn = fmaxf(m, mx), c = __expf(m - mn), p = __expf(s - mn);
      float ps = p;
      for (int x = 32; x; x >>= 1) ps += __shfl_xor(ps, x);
      l = l * c + ps; a0 *= c; a1 *= c; a2 *= c; m = mn;
#pragma unroll 2
      for (int k = 0; k < 64; ++k) {
        const float pk = __shfl(p, k), z = bf2f(vz[t * 64 + k]), sc = bf2f(vs[t * 64 + k]);
        const int* vr = qv + (size_t)(t * 64 + k) * D + lane;
        a0 = fmaf(pk, rbf(rbf(rbf((float)vr[0]) - z) * sc), a0); a1 = fmaf(pk, rbf(rbf(rbf((float)vr[64]) - z) * sc), a1); a2 = fmaf(pk, rbf(rbf(rbf((float)vr[128]) - z) * sc), a2);
      }
    }
    const float i = 1.f / l;
    o[r * D + lane] = __builtin_bit_cast(unsigned short, (__bf16)(a0 * i)); o[r * D + 64 + lane] = __builtin_bit_cast(unsigned short, (__bf16)(a1 * i));
    o[r * D + 128 + lane] = __builtin_bit_cast(unsigned short, (__bf16)(a2 * i));
  }
}

__global__ __launch_bounds__(256, 1) __attribute__((aligned(4096))) void attn_fwd(const unsigned short* q, const int* qk, const int* qv, const unsigned short* ksc, const unsigned short* kzr,
                                                   const unsigned short* vsc, const unsigned short* vzr, char* ws, unsigned short* o) {
  unsigned long long ka = (unsigned long long)__builtin_amdgcn_kernarg_segment_ptr();
  unsigned bad, b = blockIdx.x;
  const unsigned tid = threadIdx.x;
  asm volatile(
    "s_load_dwordx16 s[4:19], %1, 0\n"
    "s_load_dwordx2 s[24:25], %1, 64\n"
    "s_getpc_b64 s[54:55]\n"
    ".Lpc:\n"
    "s_and_b32 s72, %2, 24\n"
    "s_lshl_b32 s72, s72, 4\n"
    "v_lshl_add_u32 v246, %3, 9, s72\n"
    "v_min_u32 v246, .Lend-.Lpc-4, v246\n"
    "global_load_dword v246, v246, s[54:55]\n"
    "s_getreg_b32 s72, hwreg(HW_REG_XCC_ID, 0, 1)\n"
    "s_cmp_eq_u32 s72, 1\n"
    "s_cbranch_scc1 .Ldly\n"
    "s_sleep 20\n"
    ".Ldly:\n"
    "v_readfirstlane_b32 s84, %3\n"
    "v_and_b32 v156, 63, %3\n"
    "s_and_b32 s52, %2, 7\n"
    "s_bfe_u32 s72, %2, 0x20003\n"
    "s_lshr_b32 s53, %2, 5\n"
    "s_lshl_b32 s52, s52, 2\n"
    "s_lshr_b32 s84, s84, 6\n"
    "s_or_b32 s52, s52, s72\n"
    "v_lshlrev_b32 v1, 4, v156\n"
    "s_mov_b32 s30, 1572864\n"
    "s_mov_b32 s31, 0x20000\n"
    "s_mov_b32 s42, 1572864\n"
    "s_mov_b32 s43, 0x20000\n"
    "s_mov_b32 s46, 12288\n"
    "s_mov_b32 s47, 0x20000\n"
    "s_mov_b32 s90, 12288\n"
    "s_mov_b32 s91, 0x20000\n"
    "s_lshr_b32 s79, s84, 1\n"
    "s_and_b32 s59, s84, 1\n"
    "s_cmp_ge_u32 s53, 6\n"
    "s_cselect_b32 s83, 1, 0\n"
    "s_or_b32 s83, s83, s84\n"
    "s_sub_u32 s58, 2, s79\n"
    "s_mul_i32 s71, s58, 49152\n"
    "s_mul_i32 s72, s52, 1572864\n"
    "s_mul_i32 s73, s52, 12288\n"
    "s_mul_i32 s82, s52, 3\n"
    "s_and_b32 s82, s82, 31\n"
    "s_mul_i32 s82, s82, 23\n"
    "s_lshr_b32 s82, s82, 5\n"
    "s_add_u32 s82, s82, 1\n"
    "s_mul_i32 s67, s82, 49152\n"
    "s_add_u32 s30, s30, s67\n"
    "s_add_u32 s42, s42, s67\n"
    "v_lshrrev_b32 v157, 2, v156\n"
    "v_and_b32 v158, 3, v156\n"
    "s_mul_i32 s35, s84, 3072\n"
    "s_bitcmp1_b32 s83, 0\n"
    "s_cbranch_scc1 .Lf2b\n"
    "v_and_b32 v159, 32, v156\n"
    "v_mul_u32_u24 v242, 376, v159\n"
    "v_and_b32 v159, 16, v156\n"
    "v_mul_u32_u24 v159, 88, v159\n"
    "v_lshl_add_u32 v242, v156, 3, v242\n"
    "v_add_u32 v242, v159, v242\n"
    "s_lshl_b32 s63, s53, 7\n"
    "s_add_u32 s63, s63, s67\n"
    "s_and_b32 s81, s58, 1\n"
    "s_add_u32 s81, s81, s82\n"
    "v_lshl_add_u32 v160, v157, 1, s81\n"
    "v_and_b32 v160, 31, v160\n"
    "v_mul_u32_u24 v160, 384, v160\n"
    "v_lshl_add_u32 v160, v158, 4, v160\n"
    "s_lshl_b32 s81, s53, 6\n"
    "s_add_u32 s75, s71, s63\n"
    "s_add_u32 s76, s75, 6144\n"
    "s_add_u32 s77, s75, 24576\n"
    "s_add_u32 s78, s75, 30720\n"
    "s_waitcnt lgkmcnt(0)\n"
    "s_add_u32 s44, s10, s73\n"
    "s_addc_u32 s45, s11, 0\n"
    "s_add_u32 s88, s12, s73\n"
    "s_addc_u32 s89, s13, 0\n"
    "buffer_load_dwordx4 v[4:7], v1, s[44:47], s35 offen offset:0\n"
    "buffer_load_dwordx4 v[8:11], v1, s[44:47], s35 offen offset:1024\n"
    "buffer_load_dwordx4 v[12:15], v1, s[44:47], s35 offen offset:2048\n"
    "buffer_load_dwordx4 v[16:19], v1, s[88:91], 0 offen offset:0\n"
    "buffer_load_dwordx4 v[164:167], v160, s[44:47], s81 offen offset:0\n"
    "buffer_load_dwordx4 v[168:171], v160, s[88:91], s81 offen offset:0\n"
    "s_add_u32 s28, s6, s72\n"
    "s_addc_u32 s29, s7, 0\n"
    "buffer_load_dwordx2 v[204:205], v242, s[28:31], s75 offen offset:0 nt\n"
    "buffer_load_dwordx2 v[206:207], v242, s[28:31], s75 offen offset:768 nt\n"
    "buffer_load_dwordx2 v[208:209], v242, s[28:31], s75 offen offset:3072 nt\n"
    "buffer_load_dwordx2 v[210:211], v242, s[28:31], s75 offen offset:3840 nt\n"
    "buffer_load_dwordx2 v[212:213], v242, s[28:31], s76 offen offset:0 nt\n"
    "buffer_load_dwordx2 v[214:215], v242, s[28:31], s76 offen offset:768 nt\n"
    "buffer_load_dwordx2 v[216:217], v242, s[28:31], s76 offen offset:3072 nt\n"
    "buffer_load_dwordx2 v[218:219], v242, s[28:31], s76 offen offset:3840 nt\n"
    "buffer_load_dwordx2 v[220:221], v242, s[28:31], s77 offen offset:0 nt\n"
    "buffer_load_dwordx2 v[222:223], v242, s[28:31], s77 offen offset:768 nt\n"
    "buffer_load_dwordx2 v[224:225], v242, s[28:31], s77 offen offset:3072 nt\n"
    "buffer_load_dwordx2 v[226:227], v242, s[28:31], s77 offen offset:3840 nt\n"
    "buffer_load_dwordx2 v[228:229], v242, s[28:31], s78 offen offset:0 nt\n"
    "buffer_load_dwordx2 v[230:231], v242, s[28:31], s78 offen offset:768 nt\n"
    "buffer_load_dwordx2 v[232:233], v242, s[28:31], s78 offen offset:3072 nt\n"
    "buffer_load_dwordx2 v[234:235], v242, s[28:31], s78 offen offset:3840 nt\n"
    "s_add_u32 s75, s71, s63\n"
    "s_add_u32 s75, s75, 98304\n"
    "s_add_u32 s76, s75, 6144\n"
    "s_add_u32 s77, s75, 24576\n"
    "s_add_u32 s78, s75, 30720\n"
    "buffer_load_dwordx2 v[36:37], v242, s[28:31], s75 offen offset:0 nt\n"
    "buffer_load_dwordx2 v[38:39], v242, s[28:31], s75 offen offset:768 nt\n"
    "v_lshrrev_b32 v162, 1, v156\n"
    "v_and_b32 v162, 16, v162\n"
    "v_bfe_u32 v159, v156, 4, 1\n"
    "v_or_b32 v162, v162, v159\n"
    "v_and_or_b32 v162, v157, 2, v162\n"
    "v_mul_u32_u24 v162, 768, v162\n"
    "v_and_b32 v163, 0x70, v1\n"
    "v_add_u32 v162, v162, v163\n"
    "s_branch .Lf2e\n"
    ".Lf2b:\n"
    "v_and_b32 v159, 48, v156\n"
    "v_lshlrev_b32 v242, 2, v159\n"
    "v_lshl_add_u32 v243, v158, 4, v242\n"
    "v_and_b32 v159, 12, v156\n"
    "v_mul_u32_u24 v242, 384, v159\n"
    "v_add_u32 v242, v243, v242\n"
    "v_add_u32 v243, v243, v159\n"
    "s_mul_i32 s63, s53, 6144\n"
    "s_sub_u32 s63, s63, 49152\n"
    "s_add_u32 s63, s63, s67\n"
    "s_cmp_eq_u32 s59, 0\n"
    "s_cselect_b32 s63, 0x7f000000, s63\n"
    "s_add_u32 s75, s71, s63\n"
    "s_waitcnt lgkmcnt(0)\n"
    "s_add_u32 s44, s10, s73\n"
    "s_addc_u32 s45, s11, 0\n"
    "s_add_u32 s88, s12, s73\n"
    "s_addc_u32 s89, s13, 0\n"
    "buffer_load_dwordx4 v[4:7], v1, s[44:47], s35 offen offset:0\n"
    "buffer_load_dwordx4 v[8:11], v1, s[44:47], s35 offen offset:1024\n"
    "buffer_load_dwordx4 v[12:15], v1, s[44:47], s35 offen offset:2048\n"
    "buffer_load_dwordx4 v[16:19], v1, s[88:91], 0 offen offset:0\n"
    "s_add_u32 s40, s8, s72\n"
    "s_addc_u32 s41, s9, 0\n"
    "buffer_load_dwordx4 v[204:207], v242, s[40:43], s75 offen offset:0 nt\n"
    "buffer_load_dwordx4 v[208:211], v242, s[40:43], s75 offen offset:768 nt\n"
    "buffer_load_dwordx4 v[212:215], v242, s[40:43], s75 offen offset:256 nt\n"
    "buffer_load_dwordx4 v[216:219], v242, s[40:43], s75 offen offset:1024 nt\n"
    "buffer_load_dwordx4 v[220:223], v242, s[40:43], s75 offen offset:512 nt\n"
    "buffer_load_dwordx4 v[224:227], v242, s[40:43], s75 offen offset:1280 nt\n"
    "s_add_u32 s75, s71, s63\n"
    "s_add_u32 s75, s75, 98304\n"
    "buffer_load_dwordx4 v[36:39], v242, s[40:43], s75 offen offset:0 nt\n"
    "s_add_u32 s28, s6, s72\n"
    "s_addc_u32 s29, s7, 0\n"
    "v_lshrrev_b32 v162, 1, v156\n"
    "v_and_b32 v162, 16, v162\n"
    "v_bfe_u32 v159, v156, 4, 1\n"
    "v_or_b32 v162, v162, v159\n"
    "v_and_or_b32 v162, v157, 2, v162\n"
    "v_mul_u32_u24 v162, 768, v162\n"
    "v_and_b32 v163, 0x70, v1\n"
    "v_add_u32 v162, v162, v163\n"
    ".Lf2e:\n"
    "s_nop 4\n"
    "v_lshlrev_b32 v247, 2, v156\n"
    "v_add_u32 v247, 155648, v247\n"
    "v_mov_b32 v159, 0\n"
    "ds_write_b32 v247, v159\n"
    "v_mov_b32 v159, 0xff800000\n"
    "s_lshl2_add_u32 s74, s84, 155936\n"
    "v_mov_b32 v160, s74\n"
    "ds_write_b32 v160, v159\n"
    "s_mov_b32 s38, 5376\n"
    "s_mov_b32 s39, 0x20000\n"
    "s_mov_b32 s94, 4096\n"
    "s_mov_b32 s95, 0x20000\n"
    "s_mov_b32 s98, 4096\n"
    "s_mov_b32 s99, 0x20000\n"
    "s_mov_b32 s22, 743424\n"
    "s_mov_b32 s23, 0x20000\n"
    "s_mov_b32 s26, 743424\n"
    "s_mov_b32 s27, 0x20000\n"
    "s_lshl_b32 s80, s58, 7\n"
    "s_add_u32 s80, s80, 256\n"
    "s_mul_i32 s74, s52, 65536\n"
    "s_mul_i32 s69, s59, 384\n"
    "s_mul_i32 s57, s79, 6144\n"
    "s_add_u32 s57, s57, s69\n"
    "s_add_u32 s57, s57, s67\n"
    "s_mul_i32 s67, s82, 384\n"
    "s_add_u32 s66, s57, 24576\n"
    "s_add_u32 s36, s18, s74\n"
    "s_addc_u32 s37, s19, 0\n"
    "s_lshl_b32 s74, s52, 12\n"
    "s_add_u32 s92, s14, s74\n"
    "s_addc_u32 s93, s15, 0\n"
    "s_add_u32 s96, s16, s74\n"
    "s_addc_u32 s97, s17, 0\n"
    "v_lshlrev_b32 v128, 3, v156\n"
    "v_mov_b32 v129, 0\n"
    "v_lshlrev_b32 v130, 4, %3\n"
    "v_xor_b32 v130, 1024, v130\n"
    "buffer_load_dwordx2 v[124:125], v128, s[44:47], s67 offen offset:0\n"
    "buffer_load_dwordx2 v[126:127], v128, s[88:91], s67 offen offset:0\n"
    "buffer_load_dwordx2 v[238:239], v129, s[36:39], 0 offen offset:0 sc1\n"
    "s_mul_i32 s74, s52, 786432\n"
    "s_mul_i32 s73, s53, 6144\n"
    "s_add_u32 s74, s74, s73\n"
    "s_add_u32 s20, s4, s74\n"
    "s_addc_u32 s21, s5, 0\n"
    "s_add_u32 s24, s24, s74\n"
    "s_addc_u32 s25, s25, 0\n"
    "s_mul_i32 s72, s52, 1626112\n"
    "s_add_u32 s72, s72, 2097152\n"
    "s_add_u32 s16, s18, s72\n"
    "s_addc_u32 s17, s19, 0\n"
    "s_mov_b32 s18, 1622016\n"
    "s_mov_b32 s19, 0x20000\n"
    "s_mul_i32 s65, s84, 6144\n"
    "s_mul_i32 s85, s84, 49152\n"
    "s_getreg_b32 s10, hwreg(HW_REG_XCC_ID, 0, 1)\n"
    "s_lshl2_add_u32 s83, s10, s83\n"
    "s_cmp_eq_u32 s10, 1\n"
    "s_cselect_b32 s10, s85, 2.0\n"
    "s_waitcnt lgkmcnt(0)\n"
    "s_barrier\n"
    "s_add_u32 s73, s65, 49152\n"
    "v_add_u32 v131, s73, v1\n"
    "s_add_u32 s73, s73, s69\n"
    "v_add_u32 v132, s73, v163\n"
    "v_and_b32 v159, 32, v156\n"
    "v_lshlrev_b32 v244, 3, v156\n"
    "v_xor_b32 v244, v244, v159\n"
    "v_lshl_add_u32 v244, v159, 3, v244\n"
    "s_bitcmp1_b32 s83, 0\n"
    "s_cbranch_scc1 .Lf7b\n"
    "s_lshl_b32 s60, s53, 11\n"
    "s_add_u32 s61, s60, 12288\n"
    "s_lshl_b32 s73, s58, 8\n"
    "s_add_u32 s73, s73, 122880\n"
    "v_and_b32 v243, 0xf0, v1\n"
    "v_add_u32 v243, s73, v243\n"
    "s_and_b32 s73, s73, 0x1fd00\n"
    "v_lshlrev_b32 v160, 9, v157\n"
    "v_lshl_add_u32 v160, v158, 6, v160\n"
    "v_add_u32 v160, s73, v160\n"
    "s_lshl_b32 s73, s79, 8\n"
    "s_mul_i32 s74, s59, 6144\n"
    "s_add_u32 s73, s73, s74\n"
    "v_add_u32 v163, s73, v244\n"
    "v_and_b32 v159, 32, v156\n"
    "v_lshlrev_b32 v244, 2, v156\n"
    "v_xor_b32 v244, v244, v159\n"
    "v_mad_u32_u24 v244, v159, 12, v244\n"
    "s_sub_u32 s6, s28, 1572864\n"
    "s_subb_u32 s7, s29, 0\n"
    "buffer_load_dwordx2 v[40:41], v242, s[28:31], s75 offen offset:3072 nt\n"
    "buffer_load_dwordx2 v[42:43], v242, s[28:31], s75 offen offset:3840 nt\n"
    "v_add_u32 v159, 0, v156\n"
    "v_mul_u32_u24 v159, 2731, v159\n"
    "v_lshrrev_b32 v159, 16, v159\n"
    "v_xor_b32 v159, v159, v156\n"
    "v_and_b32 v159, 7, v159\n"
    "v_and_b32 v88, 0x380, v1\n"
    "v_lshl_add_u32 v88, v159, 4, v88\n"
    "v_add_u32 v159, 64, v156\n"
    "v_mul_u32_u24 v159, 2731, v159\n"
    "v_lshrrev_b32 v159, 16, v159\n"
    "v_xor_b32 v159, v159, v156\n"
    "v_and_b32 v159, 7, v159\n"
    "v_and_b32 v89, 0x380, v1\n"
    "v_lshl_add_u32 v89, v159, 4, v89\n"
    "v_add_u32 v159, 128, v156\n"
    "v_mul_u32_u24 v159, 2731, v159\n"
    "v_lshrrev_b32 v159, 16, v159\n"
    "v_xor_b32 v159, v159, v156\n"
    "v_and_b32 v159, 7, v159\n"
    "v_and_b32 v90, 0x380, v1\n"
    "v_lshl_add_u32 v90, v159, 4, v90\n"
    "v_and_b32 v2, 7, v156\n"
    "v_lshrrev_b32 v144, 7, v130\n"
    "v_mul_u32_u24 v144, 768, v144\n"
    "v_lshl_add_u32 v144, v2, 3, v144\n"
    "buffer_load_dwordx2 v[44:45], v242, s[28:31], s76 offen offset:0 nt\n"
    "buffer_load_dwordx2 v[46:47], v242, s[28:31], s76 offen offset:768 nt\n"
    "v_add_u32 v144, 131072, v144\n"
    "v_mad_u32_u24 v86, v2, 24, v144\n"
    "v_lshl_add_u32 v130, s82, 7, v130\n"
    "v_and_b32 v130, 4095, v130\n"
    "v_and_b32 v96, 15, v156\n"
    "v_lshrrev_b32 v159, 4, v156\n"
    "v_xor_b32 v159, v159, v2\n"
    "v_mul_u32_u24 v96, 384, v96\n"
    "v_lshl_add_u32 v96, v159, 4, v96\n"
    "s_add_u32 s73, s65, 49152\n"
    "v_add_u32 v96, s73, v96\n"
    "v_xor_b32 v97, 64, v96\n"
    "v_subrev_u32 v154, 24576, v96\n"
    "v_subrev_u32 v155, 24576, v97\n"
    "s_add_u32 s67, s65, 98304\n"
    "s_add_u32 s68, s65, 24576\n"
    "s_add_u32 s69, s65, 73728\n"
    "s_mov_b32 s81, 256\n"
    "s_mul_i32 s74, s79, 24576\n"
    "s_add_u32 s70, s74, s65\n"
    "s_add_u32 s70, s70, 73728\n"
    "s_sub_u32 s64, s65, s74\n"
    "s_add_u32 s64, s64, 98304\n"
    "v_and_b32 v249, 0x310, v1\n"
    "v_and_b32 v159, 12, v156\n"
    "v_lshl_add_u32 v249, v159, 3, v249\n"
    "buffer_load_dwordx2 v[48:49], v242, s[28:31], s76 offen offset:3072 nt\n"
    "buffer_load_dwordx2 v[50:51], v242, s[28:31], s76 offen offset:3840 nt\n"
    "v_and_b32 v159, 2, v156\n"
    "v_lshl_add_u32 v249, v159, 6, v249\n"
    "s_add_u32 s73, s74, 73728\n"
    "v_add_u32 v248, s73, v249\n"
    "s_sub_u32 s73, 98304, s74\n"
    "v_add_u32 v249, s73, v249\n"
    "s_or_b32 s73, s53, s84\n"
    "s_cmp_eq_u32 s73, 0\n"
    "s_cselect_b32 s82, 0, 0x7fff0000\n"
    "v_and_b32 v252, 12, v156\n"
    "v_bfe_u32 v159, v156, 1, 1\n"
    "v_or_b32 v252, v252, v159\n"
    "v_and_b32 v159, 1, v156\n"
    "v_lshl_or_b32 v252, v159, 1, v252\n"
    "v_lshlrev_b32 v252, 6, v252\n"
    "v_lshlrev_b32 v159, 2, v156\n"
    "v_and_b32 v159, 32, v159\n"
    "v_xor_b32 v159, v159, v156\n"
    "v_and_or_b32 v252, v159, 48, v252\n"
    "v_mov_b32 v2, v252\n"
    "v_add_u32 v3, 24576, v252\n"
    "v_lshrrev_b32 v91, 3, v156\n"
    "v_lshl_add_u32 v87, v91, 6, 64\n"
    "v_add_u32 v87, 64, v87\n"
    "v_and_b32 v241, 1, v156\n"
    "v_lshlrev_b32 v241, 3, v241\n"
    "buffer_load_dwordx2 v[52:53], v242, s[28:31], s77 offen offset:0 nt\n"
    "buffer_load_dwordx2 v[54:55], v242, s[28:31], s77 offen offset:768 nt\n"
    "v_lshl_add_u32 v241, v91, 4, v241\n"
    "v_add_u32 v241, 1152, v241\n"
    "v_lshlrev_b32 v91, 24, v91\n"
    "v_lshl_add_u32 v91, v156, 3, v91\n"
    "s_lshl_b32 s73, s53, 4\n"
    "s_lshl_b32 s74, s59, 3\n"
    "s_add_u32 s73, s73, s74\n"
    "s_add_u32 s73, s73, 1152\n"
    "v_mov_b32 v240, 0x7fff0000\n"
    "v_writelane_b32 v240, s73, 0\n"
    "v_and_b32 v159, 48, v156\n"
    "s_mul_i32 s73, s79, 768\n"
    "s_sub_u32 s73, 131072, s73\n"
    "v_lshlrev_b32 v250, 1, v159\n"
    "v_add_u32 v250, s73, v250\n"
    "v_and_b32 v251, 3, v156\n"
    "v_lshl_add_u32 v251, v251, 7, v159\n"
    "s_add_u32 s73, s73, 256\n"
    "v_add_u32 v251, s73, v251\n"
    "v_and_b32 v159, 64, v1\n"
    "v_add_u32 v251, v159, v251\n"
    "v_bfe_i32 v159, v156, 2, 1\n"
    "v_and_b32 v159, 0xaaaa, v159\n"
    "v_or_b32 v253, 0x4444, v159\n"
    "s_mov_b32 s58, 0\n"
    "s_mov_b32 s59, 8\n"
    "s_waitcnt vmcnt(31)\n"
    "v_pk_min_i16 v4, v4, v10\n"
    "v_pk_min_i16 v5, v5, v11\n"
    "v_pk_min_i16 v6, v6, v12\n"
    "v_pk_min_i16 v7, v7, v13\n"
    "v_pk_min_i16 v8, v8, v14\n"
    "v_pk_min_i16 v9, v9, v15\n"
    "v_pk_min_i16 v16, v16, v18\n"
    "v_pk_min_i16 v17, v17, v19\n"
    "v_pk_min_i16 v4, v4, v7\n"
    "v_pk_min_i16 v5, v5, v8\n"
    "v_pk_min_i16 v6, v6, v9\n"
    "v_pk_min_i16 v16, v16, v17\n"
    "v_pk_min_i16 v4, v4, v5\n"
    "v_lshlrev_b32 v17, 16, v16\n"
    "v_pk_min_i16 v4, v4, v6\n"
    "v_min_i32 v16, v16, v17\n"
    "v_lshlrev_b32 v5, 16, v4\n"
    "v_mov_b32 v7, 155920\n"
    "v_min_i32 v4, v4, v5\n"
    "v_lshl_add_u32 v6, s84, 2, v7\n"
    "v_min_i32_dpp v16, v16, v16 row_shr:1\n"
    "v_min_i32_dpp v4, v4, v4 row_shr:1\n"
    "s_nop 0\n"
    "v_min_i32_dpp v16, v16, v16 row_shr:2\n"
    "v_min_i32_dpp v4, v4, v4 row_shr:2\n"
    "s_nop 0\n"
    "v_min_i32_dpp v16, v16, v16 row_shr:4\n"
    "v_min_i32_dpp v4, v4, v4 row_shr:4\n"
    "s_nop 0\n"
    "v_min_i32_dpp v16, v16, v16 row_shr:8\n"
    "v_min_i32_dpp v4, v4, v4 row_shr:8\n"
    "s_nop 0\n"
    "v_min_i32_dpp v16, v16, v16 row_bcast:15 row_mask:0xa\n"
    "v_min_i32_dpp v4, v4, v4 row_bcast:15 row_mask:0xa\n"
    "s_nop 0\n"
    "v_min_i32_dpp v16, v16, v16 row_bcast:31 row_mask:0xc\n"
    "v_min_i32_dpp v4, v4, v4 row_bcast:31 row_mask:0xc\n"
    "v_readlane_b32 s35, v16, 63\n"
    "v_readlane_b32 s73, v4, 63\n"
    "s_max_i32 s35, s35, 0\n"
    "s_and_b32 s35, s35, 0xffff0000\n"
    "v_mov_b32 v4, s73\n"
    "ds_write_b32 v6, v4\n"
    "v_mov_b32 v14, -32\n"
    "v_mov_b32 v12, -126\n"
    "v_mov_b32 v13, 0x3dd53b95\n"
    "v_mov_b32 v98, 0\n"
    "s_waitcnt vmcnt(29)\n"
    "v_lshlrev_b32 v180, 16, v164\n"
    "v_and_b32 v181, 0xffff0000, v164\n"
    "v_lshlrev_b32 v182, 16, v168\n"
    "v_and_b32 v183, 0xffff0000, v168\n"
    "v_lshlrev_b32 v184, 16, v165\n"
    "v_and_b32 v185, 0xffff0000, v165\n"
    "v_lshlrev_b32 v186, 16, v169\n"
    "v_and_b32 v187, 0xffff0000, v169\n"
    "v_lshlrev_b32 v188, 16, v166\n"
    "v_and_b32 v189, 0xffff0000, v166\n"
    "v_lshlrev_b32 v190, 16, v170\n"
    "v_and_b32 v191, 0xffff0000, v170\n"
    "v_lshlrev_b32 v192, 16, v167\n"
    "v_and_b32 v193, 0xffff0000, v167\n"
    "v_lshlrev_b32 v194, 16, v171\n"
    "v_and_b32 v195, 0xffff0000, v171\n"
    "s_waitcnt lgkmcnt(0)\n"
    "s_barrier\n"
    "ds_read_b128 v[8:11], v7 offset:0\n"
    "buffer_load_dwordx2 v[56:57], v242, s[28:31], s77 offen offset:3072 nt\n"
    "buffer_load_dwordx2 v[58:59], v242, s[28:31], s77 offen offset:3840 nt\n"
    "buffer_load_dwordx4 v[136:139], v130, s[92:95], 0 offen offset:0\n"
    "buffer_load_dwordx4 v[140:143], v130, s[96:99], 0 offen offset:0\n"
    "s_nop 0\n"
    "s_nop 0\n"
    "s_nop 0\n"
    "s_nop 0\n"
    "s_nop 0\n"
    "s_nop 0\n"
    "s_nop 0\n"
    "s_nop 0\n"
    "s_nop 0\n"
    "s_nop 0\n"
    "s_nop 0\n"
    "s_nop 0\n"
    "s_nop 0\n"
    "s_nop 0\n"
    "s_nop 0\n"
    "s_nop 0\n"
    "s_nop 0\n"
    "s_nop 0\n"
    "s_nop 0\n"
    "s_nop 0\n"
    "s_nop 0\n"
    "s_nop 0\n"
    "s_nop 0\n"
    "s_nop 0\n"
    "s_nop 0\n"
    "s_nop 0\n"
    "s_nop 0\n"
    "s_nop 0\n"
    "s_nop 0\n"
    "s_nop 0\n"
    "s_nop 0\n"
    "s_nop 0\n"
    "s_nop 0\n"
    "s_nop 0\n"
    "s_nop 0\n"
    "s_nop 0\n"
    "s_nop 0\n"
    "s_nop 0\n"
    "s_nop 0\n"
    "s_nop 0\n"
    "s_nop 0\n"
    "s_nop 0\n"
    "s_nop 0\n"
    "s_nop 0\n"
    "s_nop 0\n"
    "s_nop 0\n"
    "s_nop 0\n"
    "s_nop 0\n"
    "s_nop 0\n"
    "s_nop 0\n"
    "s_nop 0\n"
    "s_nop 0\n"
    "s_nop 0\n"
    "s_nop 0\n"
    "s_nop 0\n"
    "s_nop 0\n"
    "s_nop 0\n"
    "s_nop 0\n"
    "s_nop 0\n"
    "s_nop 0\n"
    "s_waitcnt lgkmcnt(0)\n"
    "v_min3_i32 v4, v8, v9, v10\n"
    "v_min_i32 v4, v4, v11\n"
    "v_max_i32 v4, 0, v4\n"
    "v_and_b32 v4, 0xffff0000, v4\n"
    "v_mul_f32 v4, s35, v4\n"
    "v_mul_f32 v4, 0x3ed53b95, v4\n"
    "v_lshrrev_b32 v4, 23, v4\n"
    "v_sub_u32 v4, 2, v4\n"
    "v_med3_i32 v4, v4, v14, v12\n"
    "v_ldexp_f32 v13, v13, v4\n"
    "v_readfirstlane_b32 s89, v4\n"
    "v_readfirstlane_b32 s88, v13\n"
    "s_mul_i32 s89, s89, 0xff7fff80\n"
    "s_mov_b32 s90, 149\n"
    "v_mul_f32 v180, s88, v180\n"
    "v_mul_f32 v181, s88, v181\n"
    "v_mul_f32 v184, s88, v184\n"
    "v_mul_f32 v185, s88, v185\n"
    "v_mul_f32 v188, s88, v188\n"
    "v_mul_f32 v189, s88, v189\n"
    "v_mul_f32 v192, s88, v192\n"
    "v_mul_f32 v193, s88, v193\n"
    "v_fmaak_f32 v182, v182, v180, 0x80730000\n"
    "v_fmaak_f32 v183, v183, v181, 0x80730000\n"
    "v_fmaak_f32 v186, v186, v184, 0x80730000\n"
    "v_fmaak_f32 v187, v187, v185, 0x80730000\n"
    "v_fmaak_f32 v190, v190, v188, 0x80730000\n"
    "v_fmaak_f32 v191, v191, v189, 0x80730000\n"
    "v_fmaak_f32 v194, v194, v192, 0x80730000\n"
    "v_fmaak_f32 v195, v195, v193, 0x80730000\n"
    "v_ldexp_f32 v180, v180, s90\n"
    "v_ldexp_f32 v181, v181, s90\n"
    "v_ldexp_f32 v184, v184, s90\n"
    "v_ldexp_f32 v185, v185, s90\n"
    "v_ldexp_f32 v188, v188, s90\n"
    "v_ldexp_f32 v189, v189, s90\n"
    "v_ldexp_f32 v192, v192, s90\n"
    "v_ldexp_f32 v193, v193, s90\n"
    "v_fma_f32 v180, v180, 0, v180\n"
    "v_fma_f32 v181, v181, 0, v181\n"
    "v_fma_f32 v184, v184, 0, v184\n"
    "v_fma_f32 v185, v185, 0, v185\n"
    "v_fma_f32 v188, v188, 0, v188\n"
    "v_fma_f32 v189, v189, 0, v189\n"
    "v_fma_f32 v192, v192, 0, v192\n"
    "v_fma_f32 v193, v193, 0, v193\n"
    "ds_write_b128 v160, v[180:183] offset:0\n"
    "ds_write_b128 v160, v[184:187] offset:16\n"
    "ds_write_b128 v160, v[188:191] offset:32\n"
    "ds_write_b128 v160, v[192:195] offset:48\n"
    "s_waitcnt vmcnt(17)\n"
    "s_add_u32 s54, s71, s60\n"
    "s_add_u32 s55, s71, s61\n"
    "ds_read_b128 v[92:95], v243 offset:0\n"
    "s_waitcnt lgkmcnt(0)\n"
    "v_fma_f32 v204, v204, v92, -v94\n"
    "v_fma_f32 v205, v205, v93, -v95\n"
    "v_fma_f32 v206, v206, v92, -v94\n"
    "v_fma_f32 v207, v207, v93, -v95\n"
    "v_cvt_pk_bf16_f32 v204, v204, v205\n"
    "v_cvt_pk_bf16_f32 v206, v206, v207\n"
    "buffer_store_dword v204, v244, s[16:19], s54 offen offset:0 sc1\n"
    "buffer_store_dword v206, v244, s[16:19], s54 offen offset:128 sc1\n"
    "v_fma_f32 v208, v208, v92, -v94\n"
    "v_fma_f32 v209, v209, v93, -v95\n"
    "v_fma_f32 v210, v210, v92, -v94\n"
    "v_fma_f32 v211, v211, v93, -v95\n"
    "v_cvt_pk_bf16_f32 v208, v208, v209\n"
    "v_cvt_pk_bf16_f32 v210, v210, v211\n"
    "buffer_store_dword v208, v244, s[16:19], s54 offen offset:1024 sc1\n"
    "buffer_store_dword v210, v244, s[16:19], s54 offen offset:1152 sc1\n"
    "v_fma_f32 v212, v212, v92, -v94\n"
    "v_fma_f32 v213, v213, v93, -v95\n"
    "buffer_load_dwordx2 v[60:61], v242, s[28:31], s78 offen offset:0 nt\n"
    "buffer_load_dwordx2 v[62:63], v242, s[28:31], s78 offen offset:768 nt\n"
    "buffer_load_dwordx4 v[4:7], v162, s[28:31], s57 offen offset:0\n"
    "buffer_load_dwordx4 v[8:11], v162, s[28:31], s57 offen offset:3072\n"
    "v_fma_f32 v214, v214, v92, -v94\n"
    "v_fma_f32 v215, v215, v93, -v95\n"
    "v_cvt_pk_bf16_f32 v212, v212, v213\n"
    "v_cvt_pk_bf16_f32 v214, v214, v215\n"
    "buffer_store_dword v212, v244, s[16:19], s54 offen offset:256 sc1\n"
    "buffer_store_dword v214, v244, s[16:19], s54 offen offset:384 sc1\n"
    "v_fma_f32 v216, v216, v92, -v94\n"
    "v_fma_f32 v217, v217, v93, -v95\n"
    "v_fma_f32 v218, v218, v92, -v94\n"
    "v_fma_f32 v219, v219, v93, -v95\n"
    "v_cvt_pk_bf16_f32 v216, v216, v217\n"
    "v_cvt_pk_bf16_f32 v218, v218, v219\n"
    "buffer_store_dword v216, v244, s[16:19], s54 offen offset:1280 sc1\n"
    "buffer_store_dword v218, v244, s[16:19], s54 offen offset:1408 sc1\n"
    "v_fma_f32 v220, v220, v92, -v94\n"
    "v_fma_f32 v221, v221, v93, -v95\n"
    "v_fma_f32 v222, v222, v92, -v94\n"
    "v_fma_f32 v223, v223, v93, -v95\n"
    "v_cvt_pk_bf16_f32 v220, v220, v221\n"
    "v_cvt_pk_bf16_f32 v222, v222, v223\n"
    "buffer_store_dword v220, v244, s[16:19], s55 offen offset:0 sc1\n"
    "buffer_store_dword v222, v244, s[16:19], s55 offen offset:128 sc1\n"
    "v_fma_f32 v224, v224, v92, -v94\n"
    "buffer_load_dwordx2 v[64:65], v242, s[28:31], s78 offen offset:3072 nt\n"
    "buffer_load_dwordx2 v[66:67], v242, s[28:31], s78 offen offset:3840 nt\n"
    "buffer_load_dwordx4 v[12:15], v162, s[28:31], s57 offen offset:128\n"
    "v_fma_f32 v225, v225, v93, -v95\n"
    "v_fma_f32 v226, v226, v92, -v94\n"
    "v_fma_f32 v227, v227, v93, -v95\n"
    "v_cvt_pk_bf16_f32 v224, v224, v225\n"
    "v_cvt_pk_bf16_f32 v226, v226, v227\n"
    "buffer_store_dword v224, v244, s[16:19], s55 offen offset:1024 sc1\n"
    "buffer_store_dword v226, v244, s[16:19], s55 offen offset:1152 sc1\n"
    "v_fma_f32 v228, v228, v92, -v94\n"
    "v_fma_f32 v229, v229, v93, -v95\n"
    "v_fma_f32 v230, v230, v92, -v94\n"
    "v_fma_f32 v231, v231, v93, -v95\n"
    "v_cvt_pk_bf16_f32 v228, v228, v229\n"
    "v_cvt_pk_bf16_f32 v230, v230, v231\n"
    "buffer_store_dword v228, v244, s[16:19], s55 offen offset:256 sc1\n"
    "buffer_store_dword v230, v244, s[16:19], s55 offen offset:384 sc1\n"
    "v_fma_f32 v232, v232, v92, -v94\n"
    "v_fma_f32 v233, v233, v93, -v95\n"
    "v_fma_f32 v234, v234, v92, -v94\n"
    "v_fma_f32 v235, v235, v93, -v95\n"
    "v_cvt_pk_bf16_f32 v232, v232, v233\n"
    "v_cvt_pk_bf16_f32 v234, v234, v235\n"
    "buffer_store_dword v232, v244, s[16:19], s55 offen offset:1280 sc1\n"
    "buffer_store_dword v234, v244, s[16:19], s55 offen offset:1408 sc1\n"
    "v_add_u32 v243, 512, v243\n"
    "s_add_u32 s71, s71, 98304\n"
    "s_add_u32 m0, s65, 24576\n"
    "s_add_u32 s72, s85, 0\n"
    "buffer_load_dwordx4 v88, s[20:23], s72 offen offset:0 nt lds\n"
    "buffer_load_dwordx4 v[16:19], v162, s[28:31], s57 offen offset:3200\n"
    "buffer_load_dwordx4 v[20:23], v162, s[28:31], s57 offen offset:256\n"
    "s_waitcnt vmcnt(38)\n"
    "v_add_co_u32 v238, vcc, 0x7f4a7c15, v238\n"
    "v_mov_b32 v159, 0x9e3779b9\n"
    "s_nop 0\n"
    "v_addc_co_u32 v239, vcc, v159, v239, vcc\n"
    "v_mov_b32 v246, 1\n"
    "s_waitcnt vmcnt(39)\n"
    "v_lshlrev_b32 v180, 16, v124\n"
    "v_and_b32 v181, 0xffff0000, v124\n"
    "v_lshlrev_b32 v182, 16, v125\n"
    "v_and_b32 v183, 0xffff0000, v125\n"
    "v_lshlrev_b32 v184, 16, v126\n"
    "v_and_b32 v185, 0xffff0000, v126\n"
    "v_lshlrev_b32 v186, 16, v127\n"
    "v_and_b32 v187, 0xffff0000, v127\n"
    "v_mul_f32 v180, s88, v180\n"
    "v_mul_f32 v181, s88, v181\n"
    "v_mul_f32 v182, s88, v182\n"
    "v_mul_f32 v183, s88, v183\n"
    "v_fmaak_f32 v184, v184, v180, 0x80730000\n"
    "v_fmaak_f32 v185, v185, v181, 0x80730000\n"
    "v_fmaak_f32 v186, v186, v182, 0x80730000\n"
    "v_fmaak_f32 v187, v187, v183, 0x80730000\n"
    "s_add_u32 m0, s65, 25600\n"
    "s_add_u32 s72, s85, 1024\n"
    "buffer_load_dwordx4 v89, s[20:23], s72 offen offset:0 nt lds\n"
    "buffer_load_dwordx4 v[24:27], v162, s[28:31], s57 offen offset:3328\n"
    "v_ldexp_f32 v180, v180, s90\n"
    "v_ldexp_f32 v181, v181, s90\n"
    "v_ldexp_f32 v182, v182, s90\n"
    "v_ldexp_f32 v183, v183, s90\n"
    "v_fma_f32 v180, v180, 0, v180\n"
    "v_fma_f32 v181, v181, 0, v181\n"
    "v_fma_f32 v182, v182, 0, v182\n"
    "v_fma_f32 v183, v183, 0, v183\n"
    "ds_write_b128 v131, v[180:183] offset:0\n"
    "ds_write_b128 v131, v[184:187] offset:1024\n"
    "ds_read_b128 v[180:183], v132 offset:0\n"
    "ds_read_b128 v[184:187], v132 offset:1024\n"
    "ds_read_b128 v[188:191], v132 offset:128\n"
    "ds_read_b128 v[192:195], v132 offset:1152\n"
    "ds_read_b128 v[196:199], v132 offset:256\n"
    "ds_read_b128 v[200:203], v132 offset:1280\n"
    "s_nop 0\n"
    "s_nop 0\n"
    "s_nop 0\n"
    "s_nop 0\n"
    "s_nop 0\n"
    "s_nop 0\n"
    "s_nop 0\n"
    "s_add_u32 m0, s65, 26624\n"
    "s_add_u32 s72, s85, 2048\n"
    "buffer_load_dwordx4 v90, s[20:23], s72 offen offset:0 nt lds\n"
    "buffer_load_dwordx4 v[100:103], v162, s[28:31], s66 offen offset:0\n"
    "buffer_load_dwordx4 v[104:107], v162, s[28:31], s66 offen offset:3072\n"
    "s_nop 0\n"
    "s_nop 0\n"
    "s_nop 0\n"
    "s_nop 0\n"
    "s_nop 0\n"
    "s_nop 0\n"
    "s_nop 0\n"
    "s_nop 0\n"
    "s_nop 0\n"
    "s_nop 0\n"
    "s_nop 0\n"
    "s_nop 0\n"
    "s_nop 0\n"
    "s_nop 0\n"
    "s_nop 0\n"
    "s_nop 0\n"
    "s_nop 0\n"
    "s_nop 0\n"
    "s_nop 0\n"
    "s_nop 0\n"
    "s_nop 0\n"
    "s_nop 0\n"
    "s_add_u32 m0, s65, 27648\n"
    "s_add_u32 s72, s85, 3072\n"
    "buffer_load_dwordx4 v88, s[20:23], s72 offen offset:0 nt lds\n"
    "buffer_load_dwordx4 v[108:111], v162, s[28:31], s66 offen offset:128\n"
    "s_nop 0\n"
    "s_nop 0\n"
    "s_nop 0\n"
    "s_nop 0\n"
    "s_nop 0\n"
    "s_nop 0\n"
    "s_nop 0\n"
    "s_nop 0\n"
    "s_nop 0\n"
    "s_nop 0\n"
    "s_nop 0\n"
    "s_nop 0\n"
    "s_nop 0\n"
    "s_nop 0\n"
    "s_nop 0\n"
    "s_nop 0\n"
    "s_nop 0\n"
    "s_nop 0\n"
    "s_nop 0\n"
    "s_nop 0\n"
    "s_nop 0\n"
    "s_nop 0\n"
    "s_nop 0\n"
    "s_waitcnt lgkmcnt(0)\n"
    "s_waitcnt vmcnt(10)\n"
    "ds_add_rtn_u32 v245, v247, v246\n"
    "s_lshl_b32 s73, s53, 6\n"
    "s_add_u32 s73, s73, 128\n"
    "s_waitcnt lgkmcnt(0)\n"
    "v_readfirstlane_b32 s74, v245\n"
    "s_nop 0\n"
    "s_cmp_eq_u32 s74, 3\n"
    "s_cselect_b32 s73, s73, 0x7fff0000\n"
    "buffer_store_dwordx2 v[238:239], v91, s[36:39], s73 offen offset:0 sc1\n"
    "s_waitcnt vmcnt(18)\n"
    "s_add_u32 m0, s65, 28672\n"
    "s_add_u32 s72, s85, 4096\n"
    "buffer_load_dwordx4 v89, s[20:23], s72 offen offset:0 nt lds\n"
    "buffer_load_dwordx4 v[112:115], v162, s[28:31], s66 offen offset:3200\n"
    "buffer_load_dwordx4 v[116:119], v162, s[28:31], s66 offen offset:256\n"
    "s_add_u32 s54, s71, s60\n"
    "s_add_u32 s55, s71, s61\n"
    "ds_read_b128 v[92:95], v243 offset:0\n"
    "s_waitcnt lgkmcnt(0)\n"
    "v_fma_f32 v36, v36, v92, -v94\n"
    "v_fma_f32 v37, v37, v93, -v95\n"
    "v_fma_f32 v38, v38, v92, -v94\n"
    "v_fma_f32 v39, v39, v93, -v95\n"
    "v_cvt_pk_bf16_f32 v36, v36, v37\n"
    "v_cvt_pk_bf16_f32 v38, v38, v39\n"
    "buffer_store_dword v36, v244, s[16:19], s54 offen offset:0 sc1\n"
    "buffer_store_dword v38, v244, s[16:19], s54 offen offset:128 sc1\n"
    "v_fma_f32 v40, v40, v92, -v94\n"
    "v_fma_f32 v41, v41, v93, -v95\n"
    "v_fma_f32 v42, v42, v92, -v94\n"
    "v_fma_f32 v43, v43, v93, -v95\n"
    "v_cvt_pk_bf16_f32 v40, v40, v41\n"
    "v_cvt_pk_bf16_f32 v42, v42, v43\n"
    "buffer_store_dword v40, v244, s[16:19], s54 offen offset:1024 sc1\n"
    "buffer_store_dword v42, v244, s[16:19], s54 offen offset:1152 sc1\n"
    "v_fma_f32 v44, v44, v92, -v94\n"
    "v_fma_f32 v45, v45, v93, -v95\n"
    "v_fma_f32 v46, v46, v92, -v94\n"
    "v_fma_f32 v47, v47, v93, -v95\n"
    "v_cvt_pk_bf16_f32 v44, v44, v45\n"
    "v_cvt_pk_bf16_f32 v46, v46, v47\n"
    "buffer_store_dword v44, v244, s[16:19], s54 offen offset:256 sc1\n"
    "buffer_store_dword v46, v244, s[16:19], s54 offen offset:384 sc1\n"
    "v_fma_f32 v48, v48, v92, -v94\n"
    "v_fma_f32 v49, v49, v93, -v95\n"
    "v_fma_f32 v50, v50, v92, -v94\n"
    "v_fma_f32 v51, v51, v93, -v95\n"
    "v_cvt_pk_bf16_f32 v48, v48, v49\n"
    "v_cvt_pk_bf16_f32 v50, v50, v51\n"
    "s_add_u32 m0, s65, 29696\n"
    "s_add_u32 s72, s85, 5120\n"
    "buffer_load_dwordx4 v90, s[20:23], s72 offen offset:0 nt lds\n"
    "buffer_load_dwordx4 v[120:123], v162, s[28:31], s66 offen offset:3328\n"
    "buffer_store_dword v48, v244, s[16:19], s54 offen offset:1280 sc1\n"
    "buffer_store_dword v50, v244, s[16:19], s54 offen offset:1408 sc1\n"
    "v_fma_f32 v52, v52, v92, -v94\n"
    "v_fma_f32 v53, v53, v93, -v95\n"
    "v_fma_f32 v54, v54, v92, -v94\n"
    "v_fma_f32 v55, v55, v93, -v95\n"
    "v_cvt_pk_bf16_f32 v52, v52, v53\n"
    "v_cvt_pk_bf16_f32 v54, v54, v55\n"
    "buffer_store_dword v52, v244, s[16:19], s55 offen offset:0 sc1\n"
    "buffer_store_dword v54, v244, s[16:19], s55 offen offset:128 sc1\n"
    "v_fma_f32 v56, v56, v92, -v94\n"
    "v_fma_f32 v57, v57, v93, -v95\n"
    "v_fma_f32 v58, v58, v92, -v94\n"
    "v_fma_f32 v59, v59, v93, -v95\n"
    "v_cvt_pk_bf16_f32 v56, v56, v57\n"
    "v_cvt_pk_bf16_f32 v58, v58, v59\n"
    "buffer_store_dword v56, v244, s[16:19], s55 offen offset:1024 sc1\n"
    "buffer_store_dword v58, v244, s[16:19], s55 offen offset:1152 sc1\n"
    "v_fma_f32 v60, v60, v92, -v94\n"
    "v_fma_f32 v61, v61, v93, -v95\n"
    "v_fma_f32 v62, v62, v92, -v94\n"
    "v_fma_f32 v63, v63, v93, -v95\n"
    "v_cvt_pk_bf16_f32 v60, v60, v61\n"
    "v_cvt_pk_bf16_f32 v62, v62, v63\n"
    "buffer_store_dword v60, v244, s[16:19], s55 offen offset:256 sc1\n"
    "buffer_store_dword v62, v244, s[16:19], s55 offen offset:384 sc1\n"
    "v_fma_f32 v64, v64, v92, -v94\n"
    "v_fma_f32 v65, v65, v93, -v95\n"
    "v_fma_f32 v66, v66, v92, -v94\n"
    "v_fma_f32 v67, v67, v93, -v95\n"
    "v_cvt_pk_bf16_f32 v64, v64, v65\n"
    "v_cvt_pk_bf16_f32 v66, v66, v67\n"
    "buffer_store_dword v64, v244, s[16:19], s55 offen offset:1280 sc1\n"
    "buffer_store_dword v66, v244, s[16:19], s55 offen offset:1408 sc1\n"
    "v_add_u32 v243, 512, v243\n"
    "s_add_u32 m0, s65, 73728\n"
    "s_add_u32 s72, s10, 196608\n"
    "buffer_load_dwordx4 v88, s[20:23], s72 offen offset:0 nt lds\n"
    "s_waitcnt vmcnt(56)\n"
    "v_lshlrev_b32 v204, 16, v136\n"
    "v_and_b32 v205, 0xffff0000, v136\n"
    "v_lshlrev_b32 v206, 16, v137\n"
    "v_and_b32 v207, 0xffff0000, v137\n"
    "v_lshlrev_b32 v208, 16, v138\n"
    "v_and_b32 v209, 0xffff0000, v138\n"
    "v_lshlrev_b32 v210, 16, v139\n"
    "v_and_b32 v211, 0xffff0000, v139\n"
    "v_log_f32 v68, v204\n"
    "v_log_f32 v69, v205\n"
    "v_log_f32 v70, v206\n"
    "v_log_f32 v71, v207\n"
    "v_log_f32 v72, v208\n"
    "v_log_f32 v73, v209\n"
    "v_log_f32 v74, v210\n"
    "v_log_f32 v75, v211\n"
    "v_rcp_f32 v204, v204\n"
    "v_rcp_f32 v205, v205\n"
    "v_rcp_f32 v206, v206\n"
    "s_add_u32 m0, s65, 74752\n"
    "s_add_u32 s72, s10, 197632\n"
    "buffer_load_dwordx4 v89, s[20:23], s72 offen offset:0 nt lds\n"
    "v_rcp_f32 v207, v207\n"
    "v_rcp_f32 v208, v208\n"
    "v_rcp_f32 v209, v209\n"
    "v_rcp_f32 v210, v210\n"
    "v_rcp_f32 v211, v211\n"
    "s_mov_b32 s73, 0x41a00000\n"
    "v_mov_b32 v228, 0x7fc00000\n"
    "v_cmp_le_f32 vcc, |v68|, s73\n"
    "s_nop 1\n"
    "v_cndmask_b32 v68, v228, v68, vcc\n"
    "v_cmp_le_f32 vcc, |v69|, s73\n"
    "s_nop 1\n"
    "v_cndmask_b32 v69, v228, v69, vcc\n"
    "v_cmp_le_f32 vcc, |v70|, s73\n"
    "s_nop 1\n"
    "v_cndmask_b32 v70, v228, v70, vcc\n"
    "v_cmp_le_f32 vcc, |v71|, s73\n"
    "s_nop 1\n"
    "v_cndmask_b32 v71, v228, v71, vcc\n"
    "v_cmp_le_f32 vcc, |v72|, s73\n"
    "s_nop 1\n"
    "v_cndmask_b32 v72, v228, v72, vcc\n"
    "v_cmp_le_f32 vcc, |v73|, s73\n"
    "s_nop 1\n"
    "v_cndmask_b32 v73, v228, v73, vcc\n"
    "v_cmp_le_f32 vcc, |v74|, s73\n"
    "s_nop 1\n"
    "v_cndmask_b32 v74, v228, v74, vcc\n"
    "s_add_u32 m0, s65, 75776\n"
    "s_add_u32 s72, s10, 198656\n"
    "buffer_load_dwordx4 v90, s[20:23], s72 offen offset:0 nt lds\n"
    "v_cmp_le_f32 vcc, |v75|, s73\n"
    "s_nop 1\n"
    "v_cndmask_b32 v75, v228, v75, vcc\n"
    "v_cvt_pk_bf16_f32 v220, v204, v205\n"
    "v_cvt_pk_bf16_f32 v221, v206, v207\n"
    "v_cvt_pk_bf16_f32 v222, v208, v209\n"
    "v_cvt_pk_bf16_f32 v223, v210, v211\n"
    "v_lshlrev_b32 v212, 16, v220\n"
    "v_and_b32 v213, 0xffff0000, v220\n"
    "v_lshlrev_b32 v214, 16, v221\n"
    "v_and_b32 v215, 0xffff0000, v221\n"
    "v_lshlrev_b32 v216, 16, v222\n"
    "v_and_b32 v217, 0xffff0000, v222\n"
    "v_lshlrev_b32 v218, 16, v223\n"
    "v_and_b32 v219, 0xffff0000, v223\n"
    "v_sub_f32 v204, v204, v212\n"
    "v_sub_f32 v205, v205, v213\n"
    "v_sub_f32 v206, v206, v214\n"
    "v_sub_f32 v207, v207, v215\n"
    "v_sub_f32 v208, v208, v216\n"
    "v_sub_f32 v209, v209, v217\n"
    "s_add_u32 m0, s65, 76800\n"
    "s_add_u32 s72, s10, 199680\n"
    "buffer_load_dwordx4 v88, s[20:23], s72 offen offset:0 nt lds\n"
    "v_sub_f32 v210, v210, v218\n"
    "v_sub_f32 v211, v211, v219\n"
    "v_cvt_pk_bf16_f32 v224, v204, v205\n"
    "v_cvt_pk_bf16_f32 v225, v206, v207\n"
    "v_cvt_pk_bf16_f32 v226, v208, v209\n"
    "v_cvt_pk_bf16_f32 v227, v210, v211\n"
    "v_mov_b32 v204, 0\n"
    "v_mov_b32 v205, 0\n"
    "v_mov_b32 v206, 0\n"
    "v_mov_b32 v207, 0\n"
    "v_swap_b32 v221, v222\n"
    "v_swap_b32 v225, v226\n"
    "v_swap_b32 v141, v142\n"
    "ds_write2_b64 v144, v[220:221], v[222:223] offset0:32 offset1:40\n"
    "ds_write2_b64 v144, v[224:225], v[226:227] offset0:48 offset1:56\n"
    "ds_write2_b64 v144, v[140:141], v[142:143] offset0:64 offset1:72\n"
    "ds_write2_b64 v144, v[204:205], v[206:207] offset0:80 offset1:88\n"
    "s_waitcnt vmcnt(31)\n"
    "v_fma_f32 v4, v4, v180, -v184\n"
    "v_fma_f32 v5, v5, v181, -v185\n"
    "v_fma_f32 v6, v6, v182, -v186\n"
    "s_add_u32 m0, s65, 77824\n"
    "s_add_u32 s72, s10, 200704\n"
    "buffer_load_dwordx4 v89, s[20:23], s72 offen offset:0 nt lds\n"
    "v_fma_f32 v7, v7, v183, -v187\n"
    "v_fma_f32 v8, v8, v180, -v184\n"
    "v_fma_f32 v9, v9, v181, -v185\n"
    "v_fma_f32 v10, v10, v182, -v186\n"
    "v_fma_f32 v11, v11, v183, -v187\n"
    "v_cvt_pk_bf16_f32 v4, v4, v5\n"
    "v_cvt_pk_bf16_f32 v5, v6, v7\n"
    "v_cvt_pk_bf16_f32 v6, v8, v9\n"
    "v_cvt_pk_bf16_f32 v7, v10, v11\n"
    "ds_write_b64 v163, v[4:5] offset:0\n"
    "ds_write_b64 v163, v[6:7] offset:1024\n"
    "v_fma_f32 v12, v12, v188, -v192\n"
    "v_fma_f32 v13, v13, v189, -v193\n"
    "v_fma_f32 v14, v14, v190, -v194\n"
    "v_fma_f32 v15, v15, v191, -v195\n"
    "v_fma_f32 v16, v16, v188, -v192\n"
    "v_fma_f32 v17, v17, v189, -v193\n"
    "v_fma_f32 v18, v18, v190, -v194\n"
    "v_fma_f32 v19, v19, v191, -v195\n"
    "v_cvt_pk_bf16_f32 v12, v12, v13\n"
    "s_add_u32 m0, s65, 78848\n"
    "s_add_u32 s72, s10, 201728\n"
    "buffer_load_dwordx4 v90, s[20:23], s72 offen offset:0 nt lds\n"
    "v_cvt_pk_bf16_f32 v13, v14, v15\n"
    "v_cvt_pk_bf16_f32 v14, v16, v17\n"
    "v_cvt_pk_bf16_f32 v15, v18, v19\n"
    "ds_write_b64 v163, v[12:13] offset:2048\n"
    "ds_write_b64 v163, v[14:15] offset:3072\n"
    "v_fma_f32 v20, v20, v196, -v200\n"
    "v_fma_f32 v21, v21, v197, -v201\n"
    "v_fma_f32 v22, v22, v198, -v202\n"
    "v_fma_f32 v23, v23, v199, -v203\n"
    "v_fma_f32 v24, v24, v196, -v200\n"
    "v_fma_f32 v25, v25, v197, -v201\n"
    "v_fma_f32 v26, v26, v198, -v202\n"
    "v_fma_f32 v27, v27, v199, -v203\n"
    "v_cvt_pk_bf16_f32 v20, v20, v21\n"
    "v_cvt_pk_bf16_f32 v21, v22, v23\n"
    "v_cvt_pk_bf16_f32 v22, v24, v25\n"
    "v_cvt_pk_bf16_f32 v23, v26, v27\n"
    "ds_write_b64 v163, v[20:21] offset:4096\n"
    "ds_write_b64 v163, v[22:23] offset:5120\n"
    "s_waitcnt vmcnt(16)\n"
    "v_fma_f32 v100, v100, v180, -v184\n"
    "s_add_u32 m0, s65, 49152\n"
    "s_add_u32 s72, s10, 393216\n"
    "buffer_load_dwordx4 v88, s[20:23], s72 offen offset:0 nt lds\n"
    "v_fma_f32 v101, v101, v181, -v185\n"
    "v_fma_f32 v102, v102, v182, -v186\n"
    "v_fma_f32 v103, v103, v183, -v187\n"
    "v_fma_f32 v104, v104, v180, -v184\n"
    "v_fma_f32 v105, v105, v181, -v185\n"
    "v_fma_f32 v106, v106, v182, -v186\n"
    "v_fma_f32 v107, v107, v183, -v187\n"
    "v_cvt_pk_bf16_f32 v100, v100, v101\n"
    "v_cvt_pk_bf16_f32 v101, v102, v103\n"
    "v_cvt_pk_bf16_f32 v102, v104, v105\n"
    "v_cvt_pk_bf16_f32 v103, v106, v107\n"
    "ds_write_b64 v163, v[100:101] offset:12288\n"
    "ds_write_b64 v163, v[102:103] offset:13312\n"
    "v_fma_f32 v108, v108, v188, -v192\n"
    "v_fma_f32 v109, v109, v189, -v193\n"
    "v_fma_f32 v110, v110, v190, -v194\n"
    "v_fma_f32 v111, v111, v191, -v195\n"
    "v_fma_f32 v112, v112, v188, -v192\n"
    "v_fma_f32 v113, v113, v189, -v193\n"
    "v_fma_f32 v114, v114, v190, -v194\n"
    "s_add_u32 m0, s65, 50176\n"
    "s_add_u32 s72, s10, 394240\n"
    "buffer_load_dwordx4 v89, s[20:23], s72 offen offset:0 nt lds\n"
    "v_fma_f32 v115, v115, v191, -v195\n"
    "v_cvt_pk_bf16_f32 v108, v108, v109\n"
    "v_cvt_pk_bf16_f32 v109, v110, v111\n"
    "v_cvt_pk_bf16_f32 v110, v112, v113\n"
    "v_cvt_pk_bf16_f32 v111, v114, v115\n"
    "ds_write_b64 v163, v[108:109] offset:14336\n"
    "ds_write_b64 v163, v[110:111] offset:15360\n"
    "v_fma_f32 v116, v116, v196, -v200\n"
    "v_fma_f32 v117, v117, v197, -v201\n"
    "v_fma_f32 v118, v118, v198, -v202\n"
    "v_fma_f32 v119, v119, v199, -v203\n"
    "v_fma_f32 v120, v120, v196, -v200\n"
    "v_fma_f32 v121, v121, v197, -v201\n"
    "v_fma_f32 v122, v122, v198, -v202\n"
    "v_fma_f32 v123, v123, v199, -v203\n"
    "v_cvt_pk_bf16_f32 v116, v116, v117\n"
    "v_cvt_pk_bf16_f32 v117, v118, v119\n"
    "v_cvt_pk_bf16_f32 v118, v120, v121\n"
    "v_cvt_pk_bf16_f32 v119, v122, v123\n"
    "ds_write_b64 v163, v[116:117] offset:16384\n"
    "ds_write_b64 v163, v[118:119] offset:17408\n"
    "s_waitcnt vmcnt(19)\n"
    "ds_read_b128 v[36:39], v154 offset:0\n"
    "ds_read_b128 v[40:43], v155 offset:0\n"
    "ds_read_b128 v[44:47], v154 offset:128\n"
    "ds_read_b128 v[48:51], v155 offset:128\n"
    "ds_read_b128 v[52:55], v154 offset:256\n"
    "ds_read_b128 v[56:59], v155 offset:256\n"
    "s_waitcnt lgkmcnt(0)\n"
    "s_barrier\n"
    "ds_read_b128 a[112:115], v252 offset:0\n"
    "ds_read_b128 a[116:119], v252 offset:2048\n"
    "ds_read_b128 a[120:123], v252 offset:4096\n"
    "ds_read_b128 a[124:127], v252 offset:6144\n"
    "ds_read_b128 a[128:131], v252 offset:8192\n"
    "ds_read_b128 a[132:135], v252 offset:10240\n"
    "ds_read_b128 a[136:139], v252 offset:1024\n"
    "ds_read_b128 a[140:143], v252 offset:3072\n"
    "ds_read_b128 a[144:147], v252 offset:5120\n"
    "ds_read_b128 a[148:151], v252 offset:7168\n"
    "ds_read_b128 a[152:155], v252 offset:9216\n"
    "ds_read_b128 a[156:159], v252 offset:11264\n"
    "ds_read_b128 a[0:3], v252 offset:12288\n"
    "ds_read_b128 a[4:7], v252 offset:14336\n"
    "ds_read_b128 a[8:11], v252 offset:16384\n"
    "ds_read_b128 a[12:15], v252 offset:18432\n"
    "ds_read_b128 a[16:19], v252 offset:20480\n"
    "ds_read_b128 a[20:23], v252 offset:22528\n"
    "ds_read_b128 a[24:27], v252 offset:13312\n"
    "ds_read_b128 a[28:31], v252 offset:15360\n"
    "ds_read_b128 a[32:35], v252 offset:17408\n"
    "ds_read_b128 a[36:39], v252 offset:19456\n"
    "ds_read_b128 a[40:43], v252 offset:21504\n"
    "ds_read_b128 a[44:47], v252 offset:23552\n"
    "s_add_u32 m0, s65, 51200\n"
    "s_add_u32 s72, s10, 395264\n"
    "buffer_load_dwordx4 v90, s[20:23], s72 offen offset:0 nt lds\n"
    "s_add_u32 m0, s65, 52224\n"
    "s_add_u32 s72, s10, 396288\n"
    "buffer_load_dwordx4 v88, s[20:23], s72 offen offset:0 nt lds\n"
    "v_pk_maximum3_f16 v98, v36, v37, v98\n"
    "v_pk_maximum3_f16 v98, v38, v39, v98\n"
    "v_pk_add_u16 v36, s89, v36 clamp\n"
    "v_pk_add_u16 v37, s89, v37 clamp\n"
    "v_pk_add_u16 v38, s89, v38 clamp\n"
    "v_pk_add_u16 v39, s89, v39 clamp\n"
    "v_pk_maximum3_f16 v98, v40, v41, v98\n"
    "v_pk_maximum3_f16 v98, v42, v43, v98\n"
    "v_pk_add_u16 v40, s89, v40 clamp\n"
    "v_pk_add_u16 v41, s89, v41 clamp\n"
    "v_pk_add_u16 v42, s89, v42 clamp\n"
    "v_pk_add_u16 v43, s89, v43 clamp\n"
    "v_pk_maximum3_f16 v98, v44, v45, v98\n"
    "v_pk_maximum3_f16 v98, v46, v47, v98\n"
    "v_pk_add_u16 v44, s89, v44 clamp\n"
    "v_pk_add_u16 v45, s89, v45 clamp\n"
    "v_pk_add_u16 v46, s89, v46 clamp\n"
    "v_pk_add_u16 v47, s89, v47 clamp\n"
    "v_pk_maximum3_f16 v98, v48, v49, v98\n"
    "v_pk_maximum3_f16 v98, v50, v51, v98\n"
    "v_pk_add_u16 v48, s89, v48 clamp\n"
    "v_pk_add_u16 v49, s89, v49 clamp\n"
    "v_pk_add_u16 v50, s89, v50 clamp\n"
    "v_pk_add_u16 v51, s89, v51 clamp\n"
    "v_pk_maximum3_f16 v98, v52, v53, v98\n"
    "v_pk_maximum3_f16 v98, v54, v55, v98\n"
    "v_pk_add_u16 v52, s89, v52 clamp\n"
    "v_pk_add_u16 v53, s89, v53 clamp\n"
    "v_pk_add_u16 v54, s89, v54 clamp\n"
    "v_pk_add_u16 v55, s89, v55 clamp\n"
    "v_pk_maximum3_f16 v98, v56, v57, v98\n"
    "v_pk_maximum3_f16 v98, v58, v59, v98\n"
    "v_pk_add_u16 v56, s89, v56 clamp\n"
    "v_pk_add_u16 v57, s89, v57 clamp\n"
    "v_pk_add_u16 v58, s89, v58 clamp\n"
    "v_pk_add_u16 v59, s89, v59 clamp\n"
    "v_mov_b32 v108, 0\n"
    "v_mov_b32 v109, 0\n"
    "v_mov_b32 v110, 0\n"
    "v_mov_b32 v111, 0\n"
    "v_mov_b32 v112, 0\n"
    "v_mov_b32 v113, 0\n"
    "v_mov_b32 v114, 0\n"
    "v_mov_b32 v115, 0\n"
    "v_mov_b32 v116, 0\n"
    "v_mov_b32 v117, 0\n"
    "v_mov_b32 v118, 0\n"
    "v_mov_b32 v119, 0\n"
    "v_mov_b32 v120, 0\n"
    "v_mov_b32 v121, 0\n"
    "v_mov_b32 v122, 0\n"
    "v_mov_b32 v123, 0\n"
    "s_mul_i32 s73, s79, 768\n"
    "v_add_u32 v78, s73, v250\n"
    "v_mov_b32 v79, 155936\n"
    "s_waitcnt lgkmcnt(15)\n"
    "v_mfma_f32_16x16x32_bf16 v[4:7], a[112:115], v[36:39], 0\n"
    "s_waitcnt lgkmcnt(15)\n"
    "v_mfma_f32_16x16x32_bf16 v[4:7], a[116:119], v[40:43], v[4:7]\n"
    "s_waitcnt lgkmcnt(15)\n"
    "v_mfma_f32_16x16x32_bf16 v[4:7], a[120:123], v[44:47], v[4:7]\n"
    "s_waitcnt lgkmcnt(15)\n"
    "v_mfma_f32_16x16x32_bf16 v[4:7], a[124:127], v[48:51], v[4:7]\n"
    "s_waitcnt lgkmcnt(15)\n"
    "v_mfma_f32_16x16x32_bf16 v[4:7], a[128:131], v[52:55], v[4:7]\n"
    "s_waitcnt lgkmcnt(15)\n"
    "v_mfma_f32_16x16x32_bf16 v[4:7], a[132:135], v[56:59], v[4:7]\n"
    "s_waitcnt lgkmcnt(15)\n"
    "v_mfma_f32_16x16x32_bf16 v[20:23], a[136:139], v[36:39], 0\n"
    "v_accvgpr_write_b32 a192, v36\n"
    "v_accvgpr_write_b32 a193, v37\n"
    "v_accvgpr_write_b32 a194, v38\n"
    "v_accvgpr_write_b32 a195, v39\n"
    "s_waitcnt lgkmcnt(15)\n"
    "v_mfma_f32_16x16x32_bf16 v[20:23], a[140:143], v[40:43], v[20:23]\n"
    "v_accvgpr_write_b32 a196, v40\n"
    "v_accvgpr_write_b32 a197, v41\n"
    "v_accvgpr_write_b32 a198, v42\n"
    "v_accvgpr_write_b32 a199, v43\n"
    "s_waitcnt lgkmcnt(15)\n"
    "v_mfma_f32_16x16x32_bf16 v[20:23], a[144:147], v[44:47], v[20:23]\n"
    "v_accvgpr_write_b32 a200, v44\n"
    "v_accvgpr_write_b32 a201, v45\n"
    "v_accvgpr_write_b32 a202, v46\n"
    "v_accvgpr_write_b32 a203, v47\n"
    "s_waitcnt lgkmcnt(14)\n"
    "v_mfma_f32_16x16x32_bf16 v[20:23], a[148:151], v[48:51], v[20:23]\n"
    "v_accvgpr_write_b32 a204, v48\n"
    "v_accvgpr_write_b32 a205, v49\n"
    "v_accvgpr_write_b32 a206, v50\n"
    "v_accvgpr_write_b32 a207, v51\n"
    "s_waitcnt lgkmcnt(13)\n"
    "v_mfma_f32_16x16x32_bf16 v[20:23], a[152:155], v[52:55], v[20:23]\n"
    "v_accvgpr_write_b32 a208, v52\n"
    "v_accvgpr_write_b32 a209, v53\n"
    "v_accvgpr_write_b32 a210, v54\n"
    "v_accvgpr_write_b32 a211, v55\n"
    "s_waitcnt lgkmcnt(12)\n"
    "v_mfma_f32_16x16x32_bf16 v[20:23], a[156:159], v[56:59], v[20:23]\n"
    "v_accvgpr_write_b32 a212, v56\n"
    "v_accvgpr_write_b32 a213, v57\n"
    "v_accvgpr_write_b32 a214, v58\n"
    "v_accvgpr_write_b32 a215, v59\n"
    "s_add_u32 m0, s65, 53248\n"
    "s_add_u32 s72, s10, 397312\n"
    "buffer_load_dwordx4 v89, s[20:23], s72 offen offset:0 nt lds\n"
    "buffer_load_dwordx2 v[236:237], v87, s[36:39], 0 offen offset:0 sc1\n"
    "ds_read_b128 v[16:19], v78 offset:0\n"
    "ds_read_b128 v[32:35], v78 offset:16\n"
    "ds_read_b128 v[48:51], v78 offset:128\n"
    "ds_read_b128 v[64:67], v78 offset:144\n"
    "ds_read_b128 v[100:103], v78 offset:768\n"
    "ds_read_b128 v[104:107], v78 offset:784\n"
    "s_add_u32 m0, s65, 54272\n"
    "s_add_u32 s72, s10, 398336\n"
    "buffer_load_dwordx4 v90, s[20:23], s72 offen offset:0 nt lds\n"
    "s_add_u32 m0, s65, 98304\n"
    "s_add_u32 s72, s10, 589824\n"
    "buffer_load_dwordx4 v88, s[20:23], s72 offen offset:0 nt lds\n"
    "s_waitcnt lgkmcnt(0)\n"
    "s_barrier\n"
    "ds_read_b128 v[80:83], v79 offset:0\n"
    "s_waitcnt vmcnt(14)\n"
    "buffer_store_dwordx2 v[238:239], v240, s[36:39], s80 offen offset:0 sc1\n"
    "s_lshl_b32 s73, s79, 8\n"
    "s_add_u32 s80, s80, s73\n"
    "s_mul_i32 s73, s79, 98304\n"
    "s_add_u32 s71, s71, s73\n"
    "s_add_u32 m0, s65, 99328\n"
    "s_add_u32 s72, s10, 590848\n"
    "buffer_load_dwordx4 v89, s[20:23], s72 offen offset:0 nt lds\n"
    "s_waitcnt lgkmcnt(0)\n"
    "v_max3_f32 v76, v80, v81, v82\n"
    "v_max_f32 v76, v76, v83\n"
    "v_add_f32 v76, 0xc2b00000, v76\n"
    "s_add_u32 m0, s65, 100352\n"
    "s_add_u32 s72, s10, 591872\n"
    "buffer_load_dwordx4 v90, s[20:23], s72 offen offset:0 nt lds\n"
    "s_bitcmp1_b32 s83, 2\n"
    "s_cbranch_scc1 .Lf3b\n"
    "s_sleep 0\n"
    "s_add_u32 m0, s65, 73728\n"
    "s_add_u32 s72, s85, 196608\n"
    "buffer_load_dwordx4 v88, s[20:23], s72 offen offset:0 nt lds\n"
    "s_add_u32 m0, s65, 74752\n"
    "s_add_u32 s72, s85, 197632\n"
    "buffer_load_dwordx4 v89, s[20:23], s72 offen offset:0 nt lds\n"
    "s_add_u32 m0, s65, 75776\n"
    "s_add_u32 s72, s85, 198656\n"
    "buffer_load_dwordx4 v90, s[20:23], s72 offen offset:0 nt lds\n"
    "s_add_u32 m0, s65, 76800\n"
    "s_add_u32 s72, s85, 199680\n"
    "buffer_load_dwordx4 v88, s[20:23], s72 offen offset:0 nt lds\n"
    "s_add_u32 m0, s65, 77824\n"
    "s_add_u32 s72, s85, 200704\n"
    "buffer_load_dwordx4 v89, s[20:23], s72 offen offset:0 nt lds\n"
    "s_add_u32 m0, s65, 78848\n"
    "s_add_u32 s72, s85, 201728\n"
    "buffer_load_dwordx4 v90, s[20:23], s72 offen offset:0 nt lds\n"
    "s_add_u32 m0, s65, 49152\n"
    "s_add_u32 s72, s85, 393216\n"
    "buffer_load_dwordx4 v88, s[20:23], s72 offen offset:0 nt lds\n"
    "s_add_u32 m0, s65, 50176\n"
    "s_add_u32 s72, s85, 394240\n"
    "buffer_load_dwordx4 v89, s[20:23], s72 offen offset:0 nt lds\n"
    "s_add_u32 m0, s65, 51200\n"
    "s_add_u32 s72, s85, 395264\n"
    "buffer_load_dwordx4 v90, s[20:23], s72 offen offset:0 nt lds\n"
    "s_add_u32 m0, s65, 52224\n"
    "s_add_u32 s72, s85, 396288\n"
    "buffer_load_dwordx4 v88, s[20:23], s72 offen offset:0 nt lds\n"
    "s_add_u32 m0, s65, 53248\n"
    "s_add_u32 s72, s85, 397312\n"
    "buffer_load_dwordx4 v89, s[20:23], s72 offen offset:0 nt lds\n"
    "s_add_u32 m0, s65, 54272\n"
    "s_add_u32 s72, s85, 398336\n"
    "buffer_load_dwordx4 v90, s[20:23], s72 offen offset:0 nt lds\n"
    "s_add_u32 m0, s65, 98304\n"
    "s_add_u32 s72, s85, 589824\n"
    "buffer_load_dwordx4 v88, s[20:23], s72 offen offset:0 nt lds\n"
    "s_add_u32 m0, s65, 99328\n"
    "s_add_u32 s72, s85, 590848\n"
    "buffer_load_dwordx4 v89, s[20:23], s72 offen offset:0 nt lds\n"
    "s_add_u32 m0, s65, 100352\n"
    "s_add_u32 s72, s85, 591872\n"
    "buffer_load_dwordx4 v90, s[20:23], s72 offen offset:0 nt lds\n"
    "s_branch .Lf3e\n"
    ".Lf3b:\n"
    ".Lf3e:\n"
    "s_nop 4\n"
    "s_add_u32 m0, s65, 101376\n"
    "s_add_u32 s72, s85, 592896\n"
    "buffer_load_dwordx4 v88, s[20:23], s72 offen offset:0 nt lds\n"
    "s_add_u32 m0, s65, 102400\n"
    "s_add_u32 s72, s85, 593920\n"
    "buffer_load_dwordx4 v89, s[20:23], s72 offen offset:0 nt lds\n"
    "s_add_u32 m0, s65, 103424\n"
    "s_add_u32 s72, s85, 594944\n"
    "buffer_load_dwordx4 v90, s[20:23], s72 offen offset:0 nt lds\n"
    "v_sub_f32 v48, v48, v76\n"
    "v_sub_f32 v51, v51, v76\n"
    "v_sub_f32 v49, v49, v76\n"
    "v_sub_f32 v50, v50, v76\n"
    "v_sub_f32 v66, v66, v76\n"
    "v_sub_f32 v65, v65, v76\n"
    "v_mfma_f32_16x16x32_bf16 v[36:39], a[0:3], a[192:195], v[48:51]\n"
    "s_waitcnt vmcnt(12)\n"
    "ds_read_b128 v[200:203], v97 offset:24832\n"
    "v_sub_f32 v67, v67, v76\n"
    "v_mfma_f32_16x16x32_bf16 v[36:39], a[4:7], a[196:199], v[36:39]\n"
    "v_sub_f32 v33, v33, v76\n"
    "v_sub_f32 v32, v32, v76\n"
    "v_mfma_f32_16x16x32_bf16 v[36:39], a[8:11], a[200:203], v[36:39]\n"
    "ds_read_b128 v[184:187], v97 offset:24576\n"
    "ds_read_b128 v[196:199], v96 offset:24832\n"
    "ds_read_b128 v[192:195], v97 offset:24704\n"
    "v_mfma_f32_16x16x32_bf16 v[36:39], a[12:15], a[204:207], v[36:39]\n"
    "v_sub_f32 v64, v64, v76\n"
    "v_sub_f32 v35, v35, v76\n"
    "v_sub_f32 v19, v19, v76\n"
    "v_mfma_f32_16x16x32_bf16 v[52:55], a[24:27], a[192:195], v[64:67]\n"
    "v_add_f32 v21, v21, v33\n"
    "v_sub_f32 v16, v16, v76\n"
    "v_mfma_f32_16x16x32_bf16 v[36:39], a[16:19], a[208:211], v[36:39]\n"
    "v_sub_f32 v18, v18, v76\n"
    "v_add_f32 v20, v20, v32\n"
    "v_sub_f32 v17, v17, v76\n"
    "v_mfma_f32_16x16x32_bf16 v[52:55], a[28:31], a[196:199], v[52:55]\n"
    "ds_read_b128 v[188:191], v96 offset:24704\n"
    "ds_read_b128 v[180:183], v96 offset:24576\n"
    "v_add_f32 v6, v6, v18\n"
    "v_sub_f32 v34, v34, v76\n"
    "v_mfma_f32_16x16x32_bf16 v[36:39], a[20:23], a[212:215], v[36:39]\n"
    "s_waitcnt lgkmcnt(5)\n"
    "v_pk_maximum3_f16 v98, v200, v201, v98\n"
    "v_exp_f32 v20, v20\n"
    "v_pk_maximum3_f16 v98, v202, v203, v98\n"
    "v_pk_add_u16 v203, s89, v203 clamp\n"
    "v_pk_add_u16 v200, s89, v200 clamp\n"
    "v_mfma_f32_16x16x32_bf16 v[52:55], a[32:35], a[200:203], v[52:55]\n"
    "s_waitcnt lgkmcnt(3)\n"
    "v_pk_maximum3_f16 v98, v196, v197, v98\n"
    "v_pk_maximum3_f16 v98, v198, v199, v98\n"
    "v_mfma_f32_16x16x32_bf16 v[52:55], a[36:39], a[204:207], v[52:55]\n"
    "v_exp_f32 v21, v21\n"
    "v_pk_add_u16 v202, s89, v202 clamp\n"
    "v_pk_add_u16 v201, s89, v201 clamp\n"
    "v_mfma_f32_16x16x32_bf16 v[52:55], a[40:43], a[208:211], v[52:55]\n"
    "v_add_f32 v22, v22, v34\n"
    "v_add_f32 v7, v7, v19\n"
    "v_mfma_f32_16x16x32_bf16 v[24:27], a[156:159], v[200:203], v[32:35]\n"
    "s_waitcnt lgkmcnt(2)\n"
    "v_pk_maximum3_f16 v98, v192, v193, v98\n"
    "v_pk_maximum3_f16 v98, v194, v195, v98\n"
    "v_pk_add_u16 v193, s89, v193 clamp\n"
    "v_pk_add_u16 v198, s89, v198 clamp\n"
    "v_add_f32 v5, v5, v17\n"
    "v_mfma_f32_16x16x32_bf16 v[52:55], a[44:47], a[212:215], v[52:55]\n"
    "s_waitcnt lgkmcnt(1)\n"
    "v_pk_add_u16 v194, s89, v194 clamp\n"
    "v_sub_f32 v69, v69, v76\n"
    "v_sub_f32 v73, v73, v76\n"
    "v_pk_maximum3_f16 v98, v188, v189, v98\n"
    "v_pk_maximum3_f16 v98, v190, v191, v98\n"
    "v_pk_add_u16 v196, s89, v196 clamp\n"
    "v_pk_maximum3_f16 v98, v184, v185, v98\n"
    "v_pk_add_u16 v197, s89, v197 clamp\n"
    "v_pk_maximum3_f16 v98, v186, v187, v98\n"
    "v_mfma_f32_16x16x32_bf16 v[8:11], a[132:135], v[200:203], v[16:19]\n"
    "v_sub_f32 v75, v75, v76\n"
    "v_pk_add_u16 v199, s89, v199 clamp\n"
    "v_sub_f32 v101, v101, v76\n"
    "v_exp_f32 v54, v54\n"
    "v_mfma_f32_16x16x32_bf16 v[24:27], a[152:155], v[196:199], v[24:27]\n"
    "s_waitcnt lgkmcnt(0)\n"
    "v_pk_add_u16 v188, s89, v188 clamp\n"
    "v_pk_add_u16 v184, s89, v184 clamp\n"
    "v_sub_f32 v70, v70, v76\n"
    "v_pk_maximum3_f16 v98, v180, v181, v98\n"
    "v_pk_add_u16 v186, s89, v186 clamp\n"
    "v_mfma_f32_16x16x32_bf16 v[56:59], a[44:47], v[200:203], v[64:67]\n"
    "v_pk_add_u16 v189, s89, v189 clamp\n"
    "v_pk_add_u16 v195, s89, v195 clamp\n"
    "v_pk_add_u16 v192, s89, v192 clamp\n"
    "v_pk_add_u16 v185, s89, v185 clamp\n"
    "v_pk_maximum3_f16 v98, v182, v183, v98\n"
    "v_exp_f32 v55, v55\n"
    "v_sub_f32 v68, v68, v76\n"
    "v_mfma_f32_16x16x32_bf16 v[56:59], a[40:43], v[196:199], v[56:59]\n"
    "v_exp_f32 v52, v52\n"
    "v_pk_add_u16 v190, s89, v190 clamp\n"
    "v_mfma_f32_16x16x32_bf16 v[8:11], a[128:131], v[196:199], v[8:11]\n"
    "v_accvgpr_write_b32 a231, v195\n"
    "v_sub_f32 v102, v102, v76\n"
    "v_sub_f32 v71, v71, v76\n"
    "v_sub_f32 v107, v107, v76\n"
    "v_mfma_f32_16x16x32_bf16 v[40:43], a[20:23], v[200:203], v[48:51]\n"
    "v_sub_f32 v74, v74, v76\n"
    "v_add_f32 v23, v23, v35\n"
    "v_mfma_f32_16x16x32_bf16 v[40:43], a[16:19], v[196:199], v[40:43]\n"
    "v_pk_add_u16 v183, s89, v183 clamp\n"
    "v_accvgpr_write_b32 a228, v192\n"
    "v_mfma_f32_16x16x32_bf16 v[56:59], a[36:39], v[192:195], v[56:59]\n"
    "v_add_f32 v4, v4, v16\n"
    "v_exp_f32 v6, v6\n"
    "v_mfma_f32_16x16x32_bf16 v[24:27], a[148:151], v[192:195], v[24:27]\n"
    "v_sub_f32 v72, v72, v76\n"
    "v_pk_add_u16 v187, s89, v187 clamp\n"
    "v_pk_add_u16 v191, s89, v191 clamp\n"
    "v_mfma_f32_16x16x32_bf16 v[8:11], a[124:127], v[192:195], v[8:11]\n"
    "v_exp_f32 v53, v53\n"
    "v_accvgpr_write_b32 a226, v190\n"
    "v_mfma_f32_16x16x32_bf16 v[8:11], a[120:123], v[188:191], v[8:11]\n"
    "v_accvgpr_write_b32 a221, v185\n"
    "v_exp_f32 v39, v39\n"
    "v_exp_f32 v22, v22\n"
    "v_pk_add_u16 v181, s89, v181 clamp\n"
    "v_mfma_f32_16x16x32_bf16 v[56:59], a[32:35], v[188:191], v[56:59]\n"
    "v_exp_f32 v36, v36\n"
    "v_pk_add_u16 v182, s89, v182 clamp\n"
    "v_exp_f32 v23, v23\n"
    "v_mfma_f32_16x16x32_bf16 v[24:27], a[144:147], v[188:191], v[24:27]\n"
    "v_accvgpr_write_b32 a237, v201\n"
    "v_pk_add_u16 v180, s89, v180 clamp\n"
    "v_mfma_f32_16x16x32_bf16 v[40:43], a[12:15], v[192:195], v[40:43]\n"
    "v_accvgpr_write_b32 a239, v203\n"
    "v_accvgpr_write_b32 a223, v187\n"
    "v_exp_f32 v7, v7\n"
    "v_accvgpr_write_b32 a233, v197\n"
    "v_mfma_f32_16x16x32_bf16 v[8:11], a[116:119], v[184:187], v[8:11]\n"
    "v_accvgpr_write_b32 a232, v196\n"
    "v_accvgpr_write_b32 a235, v199\n"
    "v_mfma_f32_16x16x32_bf16 v[40:43], a[8:11], v[188:191], v[40:43]\n"
    "ds_write_b128 v86, v[68:71] offset:0\n"
    "v_mfma_f32_16x16x32_bf16 v[40:43], a[4:7], v[184:187], v[40:43]\n"
    "v_sub_f32 v106, v106, v76\n"
    "v_accvgpr_write_b32 a234, v198\n"
    "v_mfma_f32_16x16x32_bf16 v[8:11], a[112:115], v[180:183], v[8:11]\n"
    "v_cvt_pk_bf16_f32 v70, v20, v21\n"
    "v_exp_f32 v37, v37\n"
    "v_cvt_pk_bf16_f32 v69, v6, v7\n"
    "v_accvgpr_write_b32 a218, v182\n"
    "v_mfma_f32_16x16x32_bf16 v[40:43], a[0:3], v[180:183], v[40:43]\n"
    "ds_write_b128 v86, v[72:75] offset:16\n"
    "v_accvgpr_write_b32 a230, v194\n"
    "v_mfma_f32_16x16x32_bf16 v[24:27], a[140:143], v[184:187], v[24:27]\n"
    "v_exp_f32 v10, v10\n"
    "v_mfma_f32_16x16x32_bf16 v[56:59], a[28:31], v[184:187], v[56:59]\n"
    "v_cvt_pk_bf16_f32 v72, v36, v37\n"
    "v_sub_f32 v103, v103, v76\n"
    "v_mfma_f32_16x16x32_bf16 v[56:59], a[24:27], v[180:183], v[56:59]\n"
    "v_sub_f32 v104, v104, v76\n"
    "v_accvgpr_write_b32 a217, v181\n"
    "v_exp_f32 v4, v4\n"
    "v_sub_f32 v100, v100, v76\n"
    "v_accvgpr_write_b32 a227, v191\n"
    "v_exp_f32 v11, v11\n"
    "v_exp_f32 v5, v5\n"
    "v_exp_f32 v38, v38\n"
    "v_exp_f32 v41, v41\n"
    "v_exp_f32 v9, v9\n"
    "v_exp_f32 v59, v59\n"
    "v_accvgpr_write_b32 a220, v184\n"
    "v_accvgpr_write_b32 a238, v202\n"
    "v_accvgpr_write_b32 a236, v200\n"
    "v_exp_f32 v43, v43\n"
    "v_cvt_pk_bf16_f32 v77, v10, v11\n"
    "v_exp_f32 v8, v8\n"
    "v_cvt_pk_bf16_f32 v75, v54, v55\n"
    "v_mfma_f32_16x16x32_bf16 v[24:27], a[136:139], v[180:183], v[24:27]\n"
    "v_cvt_pk_bf16_f32 v71, v22, v23\n"
    "v_sub_f32 v105, v105, v76\n"
    "v_accvgpr_write_b32 a216, v180\n"
    "v_exp_f32 v58, v58\n"
    "v_cvt_pk_bf16_f32 v73, v38, v39\n"
    "v_exp_f32 v40, v40\n"
    "v_accvgpr_write_b32 a222, v186\n"
    "v_accvgpr_write_b32 a225, v189\n"
    "v_exp_f32 v24, v24\n"
    "v_exp_f32 v57, v57\n"
    "v_cvt_pk_bf16_f32 v68, v4, v5\n"
    "v_cvt_pk_bf16_f32 v80, v40, v41\n"
    "v_exp_f32 v27, v27\n"
    "v_exp_f32 v25, v25\n"
    "v_cvt_pk_bf16_f32 v76, v8, v9\n"
    "v_accvgpr_write_b32 a224, v188\n"
    "v_accvgpr_write_b32 a219, v183\n"
    "v_exp_f32 v26, v26\n"
    "v_exp_f32 v42, v42\n"
    "v_cvt_pk_bf16_f32 v78, v24, v25\n"
    "v_exp_f32 v56, v56\n"
    "v_cvt_pk_bf16_f32 v74, v52, v53\n"
    "v_accvgpr_write_b32 a229, v193\n"
    "v_cvt_pk_bf16_f32 v79, v26, v27\n"
    "v_cvt_pk_bf16_f32 v82, v56, v57\n"
    "v_cvt_pk_bf16_f32 v83, v58, v59\n"
    "v_cvt_pk_bf16_f32 v81, v42, v43\n"
    "s_waitcnt vmcnt(8)\n"
    "v_cmp_eq_u64 s[54:55], v[236:237], v[238:239]\n"
    "s_nop 0\n"
    "s_cmp_eq_u64 s[54:55], -1\n"
    "s_cbranch_scc0 .LpollAK\n"
    ".LpollAKr:\n"
    "s_add_u32 m0, s65, 24576\n"
    "s_add_u32 s72, s65, 49152\n"
    "buffer_load_dwordx4 v1, s[16:19], s72 offen offset:0 sc1 lds\n"
    "s_add_u32 m0, s65, 25600\n"
    "s_add_u32 s72, s65, 50176\n"
    "buffer_load_dwordx4 v1, s[16:19], s72 offen offset:0 sc1 lds\n"
    "s_add_u32 m0, s65, 26624\n"
    "s_add_u32 s72, s65, 51200\n"
    "buffer_load_dwordx4 v1, s[16:19], s72 offen offset:0 sc1 lds\n"
    "s_add_u32 m0, s65, 27648\n"
    "s_add_u32 s72, s65, 52224\n"
    "buffer_load_dwordx4 v1, s[16:19], s72 offen offset:0 sc1 lds\n"
    "s_add_u32 m0, s65, 28672\n"
    "s_add_u32 s72, s65, 53248\n"
    "buffer_load_dwordx4 v1, s[16:19], s72 offen offset:0 sc1 lds\n"
    "s_add_u32 m0, s65, 29696\n"
    "s_add_u32 s72, s65, 54272\n"
    "buffer_load_dwordx4 v1, s[16:19], s72 offen offset:0 sc1 lds\n"
    "s_waitcnt vmcnt(12)\n"
    "ds_read_b128 v[156:159], v96 offset:0\n"
    "ds_read_b128 v[172:175], v96 offset:256\n"
    "ds_read_b128 v[168:171], v97 offset:128\n"
    "ds_read_b128 v[176:179], v97 offset:256\n"
    "ds_read_b128 v[160:163], v97 offset:0\n"
    "ds_read_b128 v[164:167], v96 offset:128\n"
    "s_waitcnt lgkmcnt(5)\n"
    "v_pk_maximum3_f16 v98, v156, v157, v98\n"
    "v_pk_maximum3_f16 v98, v158, v159, v98\n"
    "v_pk_add_u16 v157, s89, v157 clamp\n"
    "v_pk_add_u16 v158, s89, v158 clamp\n"
    "v_pk_add_u16 v156, s89, v156 clamp\n"
    "v_pk_add_u16 v159, s89, v159 clamp\n"
    "s_waitcnt lgkmcnt(1)\n"
    "v_pk_maximum3_f16 v98, v160, v161, v98\n"
    "s_add_u32 m0, s65, 73728\n"
    "s_add_u32 s72, s65, 73728\n"
    "buffer_load_dwordx4 v1, s[16:19], s72 offen offset:0 sc1 lds\n"
    "v_mfma_f32_16x16x32_bf16 v[28:31], a[136:139], v[156:159], v[32:35]\n"
    "s_waitcnt lgkmcnt(0)\n"
    "v_pk_maximum3_f16 v98, v162, v163, v98\n"
    "v_pk_maximum3_f16 v98, v164, v165, v98\n"
    "v_mfma_f32_16x16x32_bf16 v[12:15], a[112:115], v[156:159], v[16:19]\n"
    "v_pk_add_u16 v161, s89, v161 clamp\n"
    "v_pk_maximum3_f16 v98, v166, v167, v98\n"
    "v_pk_add_u16 v160, s89, v160 clamp\n"
    "v_pk_add_u16 v162, s89, v162 clamp\n"
    "v_mfma_f32_16x16x32_bf16 v[60:63], a[24:27], v[156:159], v[64:67]\n"
    "v_pk_maximum3_f16 v98, v168, v169, v98\n"
    "v_pk_add_u16 v163, s89, v163 clamp\n"
    "v_mfma_f32_16x16x32_bf16 v[44:47], a[0:3], v[156:159], v[48:51]\n"
    "v_pk_maximum3_f16 v98, v170, v171, v98\n"
    "v_pk_add_u16 v164, s89, v164 clamp\n"
    "v_mfma_f32_16x16x32_bf16 v[12:15], a[116:119], v[160:163], v[12:15]\n"
    "v_pk_add_u16 v165, s89, v165 clamp\n"
    "s_add_u32 m0, s65, 74752\n"
    "s_add_u32 s72, s65, 74752\n"
    "buffer_load_dwordx4 v1, s[16:19], s72 offen offset:0 sc1 lds\n"
    "v_pk_add_u16 v169, s89, v169 clamp\n"
    "v_pk_add_u16 v168, s89, v168 clamp\n"
    "v_mfma_f32_16x16x32_bf16 v[60:63], a[28:31], v[160:163], v[60:63]\n"
    "v_pk_maximum3_f16 v98, v172, v173, v98\n"
    "v_pk_add_u16 v170, s89, v170 clamp\n"
    "v_mfma_f32_16x16x32_bf16 v[28:31], a[140:143], v[160:163], v[28:31]\n"
    "v_pk_maximum3_f16 v98, v174, v175, v98\n"
    "v_pk_add_u16 v166, s89, v166 clamp\n"
    "v_pk_add_u16 v167, s89, v167 clamp\n"
    "v_mfma_f32_16x16x32_bf16 v[44:47], a[4:7], v[160:163], v[44:47]\n"
    "v_pk_maximum3_f16 v98, v176, v177, v98\n"
    "v_accvgpr_write_b32 a251, v167\n"
    "v_mfma_f32_16x16x32_bf16 v[44:47], a[8:11], v[164:167], v[44:47]\n"
    "v_pk_maximum3_f16 v98, v178, v179, v98\n"
    "v_pk_add_u16 v176, s89, v176 clamp\n"
    "v_mfma_f32_16x16x32_bf16 v[60:63], a[32:35], v[164:167], v[60:63]\n"
    "v_pk_add_u16 v171, s89, v171 clamp\n"
    "s_add_u32 m0, s65, 75776\n"
    "s_add_u32 s72, s65, 75776\n"
    "buffer_load_dwordx4 v1, s[16:19], s72 offen offset:0 sc1 lds\n"
    "v_pk_add_u16 v174, s89, v174 clamp\n"
    "v_mfma_f32_16x16x32_bf16 v[28:31], a[144:147], v[164:167], v[28:31]\n"
    "v_pk_add_u16 v173, s89, v173 clamp\n"
    "v_pk_add_u16 v172, s89, v172 clamp\n"
    "v_mfma_f32_16x16x32_bf16 v[12:15], a[120:123], v[164:167], v[12:15]\n"
    "v_accvgpr_write_b32 a254, v170\n"
    "v_pk_add_u16 v178, s89, v178 clamp\n"
    "v_mfma_f32_16x16x32_bf16 v[28:31], a[148:151], v[168:171], v[28:31]\n"
    "v_accvgpr_write_b32 a243, v159\n"
    "v_accvgpr_write_b32 a162, v174\n"
    "v_mfma_f32_16x16x32_bf16 v[12:15], a[124:127], v[168:171], v[12:15]\n"
    "v_pk_add_u16 v179, s89, v179 clamp\n"
    "v_pk_add_u16 v175, s89, v175 clamp\n"
    "v_mfma_f32_16x16x32_bf16 v[44:47], a[12:15], v[168:171], v[44:47]\n"
    "v_accvgpr_write_b32 a166, v178\n"
    "v_accvgpr_write_b32 a161, v173\n"
    "v_accvgpr_write_b32 a245, v161\n"
    "s_add_u32 m0, s65, 76800\n"
    "s_add_u32 s72, s65, 76800\n"
    "buffer_load_dwordx4 v1, s[16:19], s72 offen offset:0 sc1 lds\n"
    "v_mfma_f32_16x16x32_bf16 v[28:31], a[152:155], v[172:175], v[28:31]\n"
    "v_accvgpr_write_b32 a163, v175\n"
    "v_pk_add_u16 v177, s89, v177 clamp\n"
    "v_mfma_f32_16x16x32_bf16 v[44:47], a[16:19], v[172:175], v[44:47]\n"
    "v_accvgpr_write_b32 a160, v172\n"
    "v_accvgpr_write_b32 a252, v168\n"
    "v_mfma_f32_16x16x32_bf16 v[28:31], a[156:159], v[176:179], v[28:31]\n"
    "v_accvgpr_write_b32 a249, v165\n"
    "v_accvgpr_write_b32 a255, v171\n"
    "v_mfma_f32_16x16x32_bf16 v[12:15], a[128:131], v[172:175], v[12:15]\n"
    "v_accvgpr_write_b32 a247, v163\n"
    "v_accvgpr_write_b32 a164, v176\n"
    "v_mfma_f32_16x16x32_bf16 v[60:63], a[36:39], v[168:171], v[60:63]\n"
    "v_accvgpr_write_b32 a242, v158\n"
    "v_accvgpr_write_b32 a250, v166\n"
    "v_mfma_f32_16x16x32_bf16 v[44:47], a[20:23], v[176:179], v[44:47]\n"
    "v_accvgpr_write_b32 a241, v157\n"
    "s_add_u32 m0, s65, 77824\n"
    "s_add_u32 s72, s65, 77824\n"
    "buffer_load_dwordx4 v1, s[16:19], s72 offen offset:0 sc1 lds\n"
    "v_accvgpr_write_b32 a248, v164\n"
    "v_mfma_f32_16x16x32_bf16 v[12:15], a[132:135], v[176:179], v[12:15]\n"
    "v_accvgpr_write_b32 a246, v162\n"
    "v_exp_f32 v29, v29\n"
    "v_mfma_f32_16x16x32_bf16 v[60:63], a[40:43], v[172:175], v[60:63]\n"
    "v_exp_f32 v28, v28\n"
    "v_mfma_f32_16x16x32_bf16 v[60:63], a[44:47], v[176:179], v[60:63]\n"
    "v_exp_f32 v31, v31\n"
    "v_exp_f32 v30, v30\n"
    "v_exp_f32 v45, v45\n"
    "v_cvt_pk_bf16_f32 v86, v28, v29\n"
    "v_exp_f32 v47, v47\n"
    "v_accvgpr_write_b32 a240, v156\n"
    "v_exp_f32 v12, v12\n"
    "v_exp_f32 v14, v14\n"
    "v_accvgpr_write_b32 a167, v179\n"
    "v_exp_f32 v15, v15\n"
    "s_add_u32 m0, s65, 78848\n"
    "s_add_u32 s72, s65, 78848\n"
    "buffer_load_dwordx4 v1, s[16:19], s72 offen offset:0 sc1 lds\n"
    "v_accvgpr_write_b32 a253, v169\n"
    "v_exp_f32 v63, v63\n"
    "v_exp_f32 v13, v13\n"
    "v_exp_f32 v44, v44\n"
    "v_cvt_pk_bf16_f32 v84, v12, v13\n"
    "v_cvt_pk_bf16_f32 v87, v30, v31\n"
    "v_exp_f32 v46, v46\n"
    "v_exp_f32 v61, v61\n"
    "v_accvgpr_write_b32 a165, v177\n"
    "v_exp_f32 v60, v60\n"
    "v_exp_f32 v62, v62\n"
    "v_cvt_pk_bf16_f32 v90, v60, v61\n"
    "v_accvgpr_write_b32 a244, v160\n"
    "v_cvt_pk_bf16_f32 v85, v14, v15\n"
    "v_cvt_pk_bf16_f32 v89, v46, v47\n"
    "v_cvt_pk_bf16_f32 v88, v44, v45\n"
    "v_cvt_pk_bf16_f32 v91, v62, v63\n"
    "s_bitcmp1_b32 s83, 1\n"
    "s_cbranch_scc1 .Lf4b\n"
    "s_mov_b32 s66, 1\n"
    "s_add_u32 s68, s68, 24576\n"
    "s_add_u32 s81, s81, 128\n"
    "s_add_u32 s80, s80, 256\n"
    "s_add_u32 s71, s71, 98304\n"
    "s_add_u32 s75, s71, s63\n"
    "s_add_u32 s76, s75, 6144\n"
    "s_add_u32 s77, s75, 24576\n"
    "s_add_u32 s78, s75, 30720\n"
    "s_branch .LhKP\n"
    "s_branch .Lf4e\n"
    ".Lf4b:\n"
    "s_add_u32 s75, s71, s63\n"
    "s_add_u32 s76, s75, 6144\n"
    "s_add_u32 s77, s75, 24576\n"
    "s_add_u32 s78, s75, 30720\n"
    "buffer_load_dwordx2 v[204:205], v242, s[28:31], s75 offen offset:0 nt\n"
    "buffer_load_dwordx2 v[206:207], v242, s[28:31], s75 offen offset:768 nt\n"
    "buffer_load_dwordx2 v[208:209], v242, s[28:31], s75 offen offset:3072 nt\n"
    "buffer_load_dwordx2 v[210:211], v242, s[28:31], s75 offen offset:3840 nt\n"
    "buffer_load_dwordx2 v[212:213], v242, s[28:31], s76 offen offset:0 nt\n"
    "buffer_load_dwordx2 v[214:215], v242, s[28:31], s76 offen offset:768 nt\n"
    "buffer_load_dwordx2 v[216:217], v242, s[28:31], s76 offen offset:3072 nt\n"
    "buffer_load_dwordx2 v[218:219], v242, s[28:31], s76 offen offset:3840 nt\n"
    "buffer_load_dwordx2 v[220:221], v242, s[28:31], s77 offen offset:0 nt\n"
    "buffer_load_dwordx2 v[222:223], v242, s[28:31], s77 offen offset:768 nt\n"
    "buffer_load_dwordx2 v[224:225], v242, s[28:31], s77 offen offset:3072 nt\n"
    "buffer_load_dwordx2 v[226:227], v242, s[28:31], s77 offen offset:3840 nt\n"
    "buffer_load_dwordx2 v[228:229], v242, s[28:31], s78 offen offset:0 nt\n"
    "buffer_load_dwordx2 v[230:231], v242, s[28:31], s78 offen offset:768 nt\n"
    "buffer_load_dwordx2 v[232:233], v242, s[28:31], s78 offen offset:3072 nt\n"
    "buffer_load_dwordx2 v[234:235], v242, s[28:31], s78 offen offset:3840 nt\n"
    "s_mov_b32 s66, 1\n"
    "s_add_u32 s68, s68, 24576\n"
    "s_add_u32 s81, s81, 128\n"
    "s_branch .LhKN\n"
    ".Lf4e:\n"
    "s_nop 4\n"
    "s_branch .Lf7e\n"
    ".Lf7b:\n"
    "s_lshl_b32 s73, s79, 8\n"
    "s_mul_i32 s74, s59, 6144\n"
    "s_add_u32 s73, s73, s74\n"
    "v_add_u32 v163, s73, v244\n"
    "s_lshr_b32 s73, s53, 2\n"
    "s_mul_i32 s73, s73, 12288\n"
    "s_and_b32 s74, s53, 3\n"
    "s_lshl_b32 s74, s74, 8\n"
    "s_add_u32 s73, s73, s74\n"
    "s_cmp_eq_u32 s59, 0\n"
    "s_cselect_b32 s73, 0x7f000000, s73\n"
    "s_add_u32 s60, s73, 24576\n"
    "s_add_u32 s61, s73, 28672\n"
    "s_add_u32 s62, s73, 32768\n"
    "v_mov_b32 v244, v243\n"
    "v_mov_b32 v243, 0\n"
    "s_sub_u32 s8, s40, 1572864\n"
    "s_subb_u32 s9, s41, 0\n"
    "buffer_load_dwordx4 v[40:43], v242, s[40:43], s75 offen offset:768 nt\n"
    "v_add_u32 v159, 0, v156\n"
    "v_mul_u32_u24 v159, 2731, v159\n"
    "v_lshrrev_b32 v159, 16, v159\n"
    "v_xor_b32 v159, v159, v156\n"
    "v_and_b32 v159, 7, v159\n"
    "v_and_b32 v88, 0x380, v1\n"
    "v_lshl_add_u32 v88, v159, 4, v88\n"
    "v_add_u32 v159, 64, v156\n"
    "v_mul_u32_u24 v159, 2731, v159\n"
    "v_lshrrev_b32 v159, 16, v159\n"
    "v_xor_b32 v159, v159, v156\n"
    "v_and_b32 v159, 7, v159\n"
    "v_and_b32 v89, 0x380, v1\n"
    "v_lshl_add_u32 v89, v159, 4, v89\n"
    "v_add_u32 v159, 128, v156\n"
    "v_mul_u32_u24 v159, 2731, v159\n"
    "v_lshrrev_b32 v159, 16, v159\n"
    "v_xor_b32 v159, v159, v156\n"
    "v_and_b32 v159, 7, v159\n"
    "v_and_b32 v90, 0x380, v1\n"
    "v_lshl_add_u32 v90, v159, 4, v90\n"
    "v_and_b32 v2, 7, v156\n"
    "v_lshrrev_b32 v144, 7, v130\n"
    "v_mul_u32_u24 v144, 768, v144\n"
    "v_lshl_add_u32 v144, v2, 3, v144\n"
    "buffer_load_dwordx4 v[44:47], v242, s[40:43], s75 offen offset:256 nt\n"
    "v_add_u32 v144, 131072, v144\n"
    "v_mad_u32_u24 v86, v2, 24, v144\n"
    "v_lshl_add_u32 v130, s82, 7, v130\n"
    "v_and_b32 v130, 4095, v130\n"
    "v_and_b32 v96, 15, v156\n"
    "v_lshrrev_b32 v159, 4, v156\n"
    "v_xor_b32 v159, v159, v2\n"
    "v_mul_u32_u24 v96, 384, v96\n"
    "v_lshl_add_u32 v96, v159, 4, v96\n"
    "s_add_u32 s73, s65, 49152\n"
    "v_add_u32 v96, s73, v96\n"
    "v_xor_b32 v97, 64, v96\n"
    "v_subrev_u32 v154, 24576, v96\n"
    "v_subrev_u32 v155, 24576, v97\n"
    "s_add_u32 s67, s65, 98304\n"
    "s_add_u32 s68, s65, 24576\n"
    "s_add_u32 s69, s65, 73728\n"
    "s_mov_b32 s81, 256\n"
    "s_mul_i32 s74, s79, 24576\n"
    "s_add_u32 s70, s74, s65\n"
    "s_add_u32 s70, s70, 73728\n"
    "s_sub_u32 s64, s65, s74\n"
    "s_add_u32 s64, s64, 98304\n"
    "v_and_b32 v249, 0x310, v1\n"
    "v_and_b32 v159, 12, v156\n"
    "v_lshl_add_u32 v249, v159, 3, v249\n"
    "buffer_load_dwordx4 v[48:51], v242, s[40:43], s75 offen offset:1024 nt\n"
    "v_and_b32 v159, 2, v156\n"
    "v_lshl_add_u32 v249, v159, 6, v249\n"
    "s_add_u32 s73, s74, 73728\n"
    "v_add_u32 v248, s73, v249\n"
    "s_sub_u32 s73, 98304, s74\n"
    "v_add_u32 v249, s73, v249\n"
    "s_or_b32 s73, s53, s84\n"
    "s_cmp_eq_u32 s73, 0\n"
    "s_cselect_b32 s82, 0, 0x7fff0000\n"
    "v_and_b32 v252, 12, v156\n"
    "v_bfe_u32 v159, v156, 1, 1\n"
    "v_or_b32 v252, v252, v159\n"
    "v_and_b32 v159, 1, v156\n"
    "v_lshl_or_b32 v252, v159, 1, v252\n"
    "v_lshlrev_b32 v252, 6, v252\n"
    "v_lshlrev_b32 v159, 2, v156\n"
    "v_and_b32 v159, 32, v159\n"
    "v_xor_b32 v159, v159, v156\n"
    "v_and_or_b32 v252, v159, 48, v252\n"
    "v_mov_b32 v2, v252\n"
    "v_add_u32 v3, 24576, v252\n"
    "v_lshrrev_b32 v91, 3, v156\n"
    "v_lshl_add_u32 v87, v91, 6, 64\n"
    "v_add_u32 v87, 64, v87\n"
    "v_and_b32 v241, 1, v156\n"
    "v_lshlrev_b32 v241, 3, v241\n"
    "buffer_load_dwordx4 v[52:55], v242, s[40:43], s75 offen offset:512 nt\n"
    "v_lshl_add_u32 v241, v91, 4, v241\n"
    "v_add_u32 v241, 1152, v241\n"
    "v_lshlrev_b32 v91, 24, v91\n"
    "v_lshl_add_u32 v91, v156, 3, v91\n"
    "s_lshl_b32 s73, s53, 4\n"
    "s_lshl_b32 s74, s59, 3\n"
    "s_add_u32 s73, s73, s74\n"
    "s_add_u32 s73, s73, 1152\n"
    "v_mov_b32 v240, 0x7fff0000\n"
    "v_writelane_b32 v240, s73, 0\n"
    "v_and_b32 v159, 48, v156\n"
    "s_mul_i32 s73, s79, 768\n"
    "s_sub_u32 s73, 131072, s73\n"
    "v_lshlrev_b32 v250, 1, v159\n"
    "v_add_u32 v250, s73, v250\n"
    "v_and_b32 v251, 3, v156\n"
    "v_lshl_add_u32 v251, v251, 7, v159\n"
    "s_add_u32 s73, s73, 256\n"
    "v_add_u32 v251, s73, v251\n"
    "v_and_b32 v159, 64, v1\n"
    "v_add_u32 v251, v159, v251\n"
    "v_bfe_i32 v159, v156, 2, 1\n"
    "v_and_b32 v159, 0xaaaa, v159\n"
    "v_or_b32 v253, 0x4444, v159\n"
    "s_mov_b32 s58, 0\n"
    "s_mov_b32 s59, 8\n"
    "s_waitcnt vmcnt(14)\n"
    "v_pk_min_i16 v4, v4, v10\n"
    "v_pk_min_i16 v5, v5, v11\n"
    "v_pk_min_i16 v6, v6, v12\n"
    "v_pk_min_i16 v7, v7, v13\n"
    "v_pk_min_i16 v8, v8, v14\n"
    "v_pk_min_i16 v9, v9, v15\n"
    "v_pk_min_i16 v16, v16, v18\n"
    "v_pk_min_i16 v17, v17, v19\n"
    "v_pk_min_i16 v4, v4, v7\n"
    "v_pk_min_i16 v5, v5, v8\n"
    "v_pk_min_i16 v6, v6, v9\n"
    "v_pk_min_i16 v16, v16, v17\n"
    "v_pk_min_i16 v4, v4, v5\n"
    "v_lshlrev_b32 v17, 16, v16\n"
    "v_pk_min_i16 v4, v4, v6\n"
    "v_min_i32 v16, v16, v17\n"
    "v_lshlrev_b32 v5, 16, v4\n"
    "v_mov_b32 v7, 155920\n"
    "v_min_i32 v4, v4, v5\n"
    "v_lshl_add_u32 v6, s84, 2, v7\n"
    "v_min_i32_dpp v16, v16, v16 row_shr:1\n"
    "v_min_i32_dpp v4, v4, v4 row_shr:1\n"
    "s_nop 0\n"
    "v_min_i32_dpp v16, v16, v16 row_shr:2\n"
    "v_min_i32_dpp v4, v4, v4 row_shr:2\n"
    "s_nop 0\n"
    "v_min_i32_dpp v16, v16, v16 row_shr:4\n"
    "v_min_i32_dpp v4, v4, v4 row_shr:4\n"
    "s_nop 0\n"
    "v_min_i32_dpp v16, v16, v16 row_shr:8\n"
    "v_min_i32_dpp v4, v4, v4 row_shr:8\n"
    "s_nop 0\n"
    "v_min_i32_dpp v16, v16, v16 row_bcast:15 row_mask:0xa\n"
    "v_min_i32_dpp v4, v4, v4 row_bcast:15 row_mask:0xa\n"
    "s_nop 0\n"
    "v_min_i32_dpp v16, v16, v16 row_bcast:31 row_mask:0xc\n"
    "v_min_i32_dpp v4, v4, v4 row_bcast:31 row_mask:0xc\n"
    "v_readlane_b32 s35, v16, 63\n"
    "v_readlane_b32 s73, v4, 63\n"
    "s_max_i32 s35, s35, 0\n"
    "s_and_b32 s35, s35, 0xffff0000\n"
    "v_mov_b32 v4, s73\n"
    "ds_write_b32 v6, v4\n"
    "v_mov_b32 v14, -32\n"
    "v_mov_b32 v12, -126\n"
    "v_mov_b32 v13, 0x3dd53b95\n"
    "v_mov_b32 v98, 0\n"
    "s_waitcnt lgkmcnt(0)\n"
    "s_barrier\n"
    "ds_read_b128 v[8:11], v7 offset:0\n"
    "buffer_load_dwordx4 v[56:59], v242, s[40:43], s75 offen offset:1280 nt\n"
    "buffer_load_dwordx4 v[136:139], v130, s[92:95], 0 offen offset:0\n"
    "buffer_load_dwordx4 v[140:143], v130, s[96:99], 0 offen offset:0\n"
    "s_nop 0\n"
    "s_nop 0\n"
    "s_nop 0\n"
    "s_nop 0\n"
    "s_nop 0\n"
    "s_nop 0\n"
    "s_nop 0\n"
    "s_nop 0\n"
    "s_nop 0\n"
    "s_nop 0\n"
    "s_nop 0\n"
    "s_nop 0\n"
    "s_nop 0\n"
    "s_nop 0\n"
    "s_nop 0\n"
    "s_nop 0\n"
    "s_nop 0\n"
    "s_nop 0\n"
    "s_nop 0\n"
    "s_nop 0\n"
    "s_nop 0\n"
    "s_nop 0\n"
    "s_nop 0\n"
    "s_nop 0\n"
    "s_nop 0\n"
    "s_nop 0\n"
    "s_nop 0\n"
    "s_nop 0\n"
    "s_nop 0\n"
    "s_nop 0\n"
    "s_nop 0\n"
    "s_nop 0\n"
    "s_nop 0\n"
    "s_nop 0\n"
    "s_nop 0\n"
    "s_nop 0\n"
    "s_nop 0\n"
    "s_nop 0\n"
    "s_nop 0\n"
    "s_nop 0\n"
    "s_nop 0\n"
    "s_nop 0\n"
    "s_nop 0\n"
    "s_nop 0\n"
    "s_nop 0\n"
    "s_nop 0\n"
    "s_nop 0\n"
    "s_nop 0\n"
    "s_nop 0\n"
    "s_nop 0\n"
    "s_nop 0\n"
    "s_nop 0\n"
    "s_nop 0\n"
    "s_nop 0\n"
    "s_nop 0\n"
    "s_nop 0\n"
    "s_nop 0\n"
    "s_nop 0\n"
    "s_nop 0\n"
    "s_nop 0\n"
    "s_waitcnt lgkmcnt(0)\n"
    "v_min3_i32 v4, v8, v9, v10\n"
    "v_min_i32 v4, v4, v11\n"
    "v_max_i32 v4, 0, v4\n"
    "v_and_b32 v4, 0xffff0000, v4\n"
    "v_mul_f32 v4, s35, v4\n"
    "v_mul_f32 v4, 0x3ed53b95, v4\n"
    "v_lshrrev_b32 v4, 23, v4\n"
    "v_sub_u32 v4, 2, v4\n"
    "v_med3_i32 v4, v4, v14, v12\n"
    "v_ldexp_f32 v13, v13, v4\n"
    "v_readfirstlane_b32 s89, v4\n"
    "v_readfirstlane_b32 s88, v13\n"
    "s_mul_i32 s89, s89, 0xff7fff80\n"
    "s_mov_b32 s90, 149\n"
    "s_waitcnt vmcnt(11)\n"
    "s_add_u32 s54, s71, s60\n"
    "s_add_u32 s55, s71, s61\n"
    "s_add_u32 s56, s71, s62\n"
    "v_cvt_pk_u16_u32 v204, v204, v208\n"
    "v_cvt_pk_u16_u32 v205, v205, v209\n"
    "v_cvt_pk_u16_u32 v206, v206, v210\n"
    "v_cvt_pk_u16_u32 v207, v207, v211\n"
    "v_or3_b32 v243, v204, v205, v243\n"
    "v_or3_b32 v243, v206, v207, v243\n"
    "buffer_store_dword v204, v244, s[16:19], s54 offen offset:0 sc1\n"
    "buffer_store_dword v205, v244, s[16:19], s54 offen offset:1024 sc1\n"
    "buffer_load_dwordx4 v[4:7], v162, s[28:31], s57 offen offset:0\n"
    "buffer_load_dwordx4 v[8:11], v162, s[28:31], s57 offen offset:3072\n"
    "buffer_store_dword v206, v244, s[16:19], s54 offen offset:2048 sc1\n"
    "buffer_store_dword v207, v244, s[16:19], s54 offen offset:3072 sc1\n"
    "v_cvt_pk_u16_u32 v212, v212, v216\n"
    "v_cvt_pk_u16_u32 v213, v213, v217\n"
    "v_cvt_pk_u16_u32 v214, v214, v218\n"
    "v_cvt_pk_u16_u32 v215, v215, v219\n"
    "v_or3_b32 v243, v212, v213, v243\n"
    "v_or3_b32 v243, v214, v215, v243\n"
    "buffer_store_dword v212, v244, s[16:19], s55 offen offset:0 sc1\n"
    "buffer_store_dword v213, v244, s[16:19], s55 offen offset:1024 sc1\n"
    "buffer_store_dword v214, v244, s[16:19], s55 offen offset:2048 sc1\n"
    "buffer_load_dwordx4 v[12:15], v162, s[28:31], s57 offen offset:128\n"
    "buffer_store_dword v215, v244, s[16:19], s55 offen offset:3072 sc1\n"
    "v_cvt_pk_u16_u32 v220, v220, v224\n"
    "v_cvt_pk_u16_u32 v221, v221, v225\n"
    "v_cvt_pk_u16_u32 v222, v222, v226\n"
    "v_cvt_pk_u16_u32 v223, v223, v227\n"
    "v_or3_b32 v243, v220, v221, v243\n"
    "v_or3_b32 v243, v222, v223, v243\n"
    "buffer_store_dword v220, v244, s[16:19], s56 offen offset:0 sc1\n"
    "buffer_store_dword v221, v244, s[16:19], s56 offen offset:1024 sc1\n"
    "buffer_store_dword v222, v244, s[16:19], s56 offen offset:2048 sc1\n"
    "buffer_store_dword v223, v244, s[16:19], s56 offen offset:3072 sc1\n"
    "s_add_u32 s71, s71, 98304\n"
    "s_add_u32 m0, s65, 24576\n"
    "s_add_u32 s72, s85, 0\n"
    "buffer_load_dwordx4 v88, s[20:23], s72 offen offset:0 nt lds\n"
    "buffer_load_dwordx4 v[16:19], v162, s[28:31], s57 offen offset:3200\n"
    "buffer_load_dwordx4 v[20:23], v162, s[28:31], s57 offen offset:256\n"
    "s_waitcnt vmcnt(25)\n"
    "v_add_co_u32 v238, vcc, 0x7f4a7c15, v238\n"
    "v_mov_b32 v159, 0x9e3779b9\n"
    "s_nop 0\n"
    "v_addc_co_u32 v239, vcc, v159, v239, vcc\n"
    "v_mov_b32 v246, 1\n"
    "s_waitcnt vmcnt(26)\n"
    "v_lshlrev_b32 v180, 16, v124\n"
    "v_and_b32 v181, 0xffff0000, v124\n"
    "v_lshlrev_b32 v182, 16, v125\n"
    "v_and_b32 v183, 0xffff0000, v125\n"
    "v_lshlrev_b32 v184, 16, v126\n"
    "v_and_b32 v185, 0xffff0000, v126\n"
    "v_lshlrev_b32 v186, 16, v127\n"
    "v_and_b32 v187, 0xffff0000, v127\n"
    "v_mul_f32 v180, s88, v180\n"
    "v_mul_f32 v181, s88, v181\n"
    "v_mul_f32 v182, s88, v182\n"
    "v_mul_f32 v183, s88, v183\n"
    "v_fmaak_f32 v184, v184, v180, 0x80730000\n"
    "v_fmaak_f32 v185, v185, v181, 0x80730000\n"
    "v_fmaak_f32 v186, v186, v182, 0x80730000\n"
    "v_fmaak_f32 v187, v187, v183, 0x80730000\n"
    "s_add_u32 m0, s65, 25600\n"
    "s_add_u32 s72, s85, 1024\n"
    "buffer_load_dwordx4 v89, s[20:23], s72 offen offset:0 nt lds\n"
    "buffer_load_dwordx4 v[24:27], v162, s[28:31], s57 offen offset:3328\n"
    "v_ldexp_f32 v180, v180, s90\n"
    "v_ldexp_f32 v181, v181, s90\n"
    "v_ldexp_f32 v182, v182, s90\n"
    "v_ldexp_f32 v183, v183, s90\n"
    "v_fma_f32 v180, v180, 0, v180\n"
    "v_fma_f32 v181, v181, 0, v181\n"
    "v_fma_f32 v182, v182, 0, v182\n"
    "v_fma_f32 v183, v183, 0, v183\n"
    "ds_write_b128 v131, v[180:183] offset:0\n"
    "ds_write_b128 v131, v[184:187] offset:1024\n"
    "ds_read_b128 v[180:183], v132 offset:0\n"
    "ds_read_b128 v[184:187], v132 offset:1024\n"
    "ds_read_b128 v[188:191], v132 offset:128\n"
    "ds_read_b128 v[192:195], v132 offset:1152\n"
    "ds_read_b128 v[196:199], v132 offset:256\n"
    "ds_read_b128 v[200:203], v132 offset:1280\n"
    "s_nop 0\n"
    "s_nop 0\n"
    "s_nop 0\n"
    "s_nop 0\n"
    "s_nop 0\n"
    "s_nop 0\n"
    "s_nop 0\n"
    "s_add_u32 m0, s65, 26624\n"
    "s_add_u32 s72, s85, 2048\n"
    "buffer_load_dwordx4 v90, s[20:23], s72 offen offset:0 nt lds\n"
    "buffer_load_dwordx4 v[100:103], v162, s[28:31], s66 offen offset:0\n"
    "buffer_load_dwordx4 v[104:107], v162, s[28:31], s66 offen offset:3072\n"
    "s_nop 0\n"
    "s_nop 0\n"
    "s_nop 0\n"
    "s_nop 0\n"
    "s_nop 0\n"
    "s_nop 0\n"
    "s_nop 0\n"
    "s_nop 0\n"
    "s_nop 0\n"
    "s_nop 0\n"
    "s_nop 0\n"
    "s_nop 0\n"
    "s_nop 0\n"
    "s_nop 0\n"
    "s_nop 0\n"
    "s_nop 0\n"
    "s_nop 0\n"
    "s_nop 0\n"
    "s_nop 0\n"
    "s_nop 0\n"
    "s_nop 0\n"
    "s_nop 0\n"
    "s_add_u32 m0, s65, 27648\n"
    "s_add_u32 s72, s85, 3072\n"
    "buffer_load_dwordx4 v88, s[20:23], s72 offen offset:0 nt lds\n"
    "buffer_load_dwordx4 v[108:111], v162, s[28:31], s66 offen offset:128\n"
    "s_nop 0\n"
    "s_nop 0\n"
    "s_nop 0\n"
    "s_nop 0\n"
    "s_nop 0\n"
    "s_nop 0\n"
    "s_nop 0\n"
    "s_nop 0\n"
    "s_nop 0\n"
    "s_nop 0\n"
    "s_nop 0\n"
    "s_nop 0\n"
    "s_nop 0\n"
    "s_nop 0\n"
    "s_nop 0\n"
    "s_nop 0\n"
    "s_nop 0\n"
    "s_nop 0\n"
    "s_nop 0\n"
    "s_nop 0\n"
    "s_nop 0\n"
    "s_nop 0\n"
    "s_nop 0\n"
    "s_waitcnt lgkmcnt(0)\n"
    "s_waitcnt vmcnt(10)\n"
    "ds_add_rtn_u32 v245, v247, v246\n"
    "s_lshl_b32 s73, s53, 6\n"
    "s_add_u32 s73, s73, 128\n"
    "s_waitcnt lgkmcnt(0)\n"
    "v_readfirstlane_b32 s74, v245\n"
    "s_nop 0\n"
    "s_cmp_eq_u32 s74, 3\n"
    "s_cselect_b32 s73, s73, 0x7fff0000\n"
    "buffer_store_dwordx2 v[238:239], v91, s[36:39], s73 offen offset:0 sc1\n"
    "s_waitcnt vmcnt(28)\n"
    "s_add_u32 m0, s65, 28672\n"
    "s_add_u32 s72, s85, 4096\n"
    "buffer_load_dwordx4 v89, s[20:23], s72 offen offset:0 nt lds\n"
    "buffer_load_dwordx4 v[112:115], v162, s[28:31], s66 offen offset:3200\n"
    "buffer_load_dwordx4 v[116:119], v162, s[28:31], s66 offen offset:256\n"
    "s_add_u32 s54, s71, s60\n"
    "s_add_u32 s55, s71, s61\n"
    "s_add_u32 s56, s71, s62\n"
    "v_cvt_pk_u16_u32 v36, v36, v40\n"
    "v_cvt_pk_u16_u32 v37, v37, v41\n"
    "v_cvt_pk_u16_u32 v38, v38, v42\n"
    "v_cvt_pk_u16_u32 v39, v39, v43\n"
    "v_or3_b32 v243, v36, v37, v243\n"
    "v_or3_b32 v243, v38, v39, v243\n"
    "buffer_store_dword v36, v244, s[16:19], s54 offen offset:0 sc1\n"
    "buffer_store_dword v37, v244, s[16:19], s54 offen offset:1024 sc1\n"
    "buffer_store_dword v38, v244, s[16:19], s54 offen offset:2048 sc1\n"
    "buffer_store_dword v39, v244, s[16:19], s54 offen offset:3072 sc1\n"
    "v_cvt_pk_u16_u32 v44, v44, v48\n"
    "v_cvt_pk_u16_u32 v45, v45, v49\n"
    "v_cvt_pk_u16_u32 v46, v46, v50\n"
    "s_add_u32 m0, s65, 29696\n"
    "s_add_u32 s72, s85, 5120\n"
    "buffer_load_dwordx4 v90, s[20:23], s72 offen offset:0 nt lds\n"
    "buffer_load_dwordx4 v[120:123], v162, s[28:31], s66 offen offset:3328\n"
    "v_cvt_pk_u16_u32 v47, v47, v51\n"
    "v_or3_b32 v243, v44, v45, v243\n"
    "v_or3_b32 v243, v46, v47, v243\n"
    "buffer_store_dword v44, v244, s[16:19], s55 offen offset:0 sc1\n"
    "buffer_store_dword v45, v244, s[16:19], s55 offen offset:1024 sc1\n"
    "buffer_store_dword v46, v244, s[16:19], s55 offen offset:2048 sc1\n"
    "buffer_store_dword v47, v244, s[16:19], s55 offen offset:3072 sc1\n"
    "v_cvt_pk_u16_u32 v52, v52, v56\n"
    "v_cvt_pk_u16_u32 v53, v53, v57\n"
    "v_cvt_pk_u16_u32 v54, v54, v58\n"
    "v_cvt_pk_u16_u32 v55, v55, v59\n"
    "v_or3_b32 v243, v52, v53, v243\n"
    "v_or3_b32 v243, v54, v55, v243\n"
    "buffer_store_dword v52, v244, s[16:19], s56 offen offset:0 sc1\n"
    "buffer_store_dword v53, v244, s[16:19], s56 offen offset:1024 sc1\n"
    "buffer_store_dword v54, v244, s[16:19], s56 offen offset:2048 sc1\n"
    "buffer_store_dword v55, v244, s[16:19], s56 offen offset:3072 sc1\n"
    "s_add_u32 m0, s65, 73728\n"
    "s_add_u32 s72, s10, 196608\n"
    "buffer_load_dwordx4 v88, s[20:23], s72 offen offset:0 nt lds\n"
    "s_waitcnt vmcnt(44)\n"
    "v_lshlrev_b32 v204, 16, v136\n"
    "v_and_b32 v205, 0xffff0000, v136\n"
    "v_lshlrev_b32 v206, 16, v137\n"
    "v_and_b32 v207, 0xffff0000, v137\n"
    "v_lshlrev_b32 v208, 16, v138\n"
    "v_and_b32 v209, 0xffff0000, v138\n"
    "v_lshlrev_b32 v210, 16, v139\n"
    "v_and_b32 v211, 0xffff0000, v139\n"
    "v_log_f32 v68, v204\n"
    "v_log_f32 v69, v205\n"
    "v_log_f32 v70, v206\n"
    "v_log_f32 v71, v207\n"
    "v_log_f32 v72, v208\n"
    "v_log_f32 v73, v209\n"
    "v_log_f32 v74, v210\n"
    "v_log_f32 v75, v211\n"
    "v_rcp_f32 v204, v204\n"
    "v_rcp_f32 v205, v205\n"
    "v_rcp_f32 v206, v206\n"
    "s_add_u32 m0, s65, 74752\n"
    "s_add_u32 s72, s10, 197632\n"
    "buffer_load_dwordx4 v89, s[20:23], s72 offen offset:0 nt lds\n"
    "v_rcp_f32 v207, v207\n"
    "v_rcp_f32 v208, v208\n"
    "v_rcp_f32 v209, v209\n"
    "v_rcp_f32 v210, v210\n"
    "v_rcp_f32 v211, v211\n"
    "s_mov_b32 s73, 0x41a00000\n"
    "v_mov_b32 v228, 0x7fc00000\n"
    "v_cmp_le_f32 vcc, |v68|, s73\n"
    "s_nop 1\n"
    "v_cndmask_b32 v68, v228, v68, vcc\n"
    "v_cmp_le_f32 vcc, |v69|, s73\n"
    "s_nop 1\n"
    "v_cndmask_b32 v69, v228, v69, vcc\n"
    "v_cmp_le_f32 vcc, |v70|, s73\n"
    "s_nop 1\n"
    "v_cndmask_b32 v70, v228, v70, vcc\n"
    "v_cmp_le_f32 vcc, |v71|, s73\n"
    "s_nop 1\n"
    "v_cndmask_b32 v71, v228, v71, vcc\n"
    "v_cmp_le_f32 vcc, |v72|, s73\n"
    "s_nop 1\n"
    "v_cndmask_b32 v72, v228, v72, vcc\n"
    "v_cmp_le_f32 vcc, |v73|, s73\n"
    "s_nop 1\n"
    "v_cndmask_b32 v73, v228, v73, vcc\n"
    "v_cmp_le_f32 vcc, |v74|, s73\n"
    "s_nop 1\n"
    "v_cndmask_b32 v74, v228, v74, vcc\n"
    "s_add_u32 m0, s65, 75776\n"
    "s_add_u32 s72, s10, 198656\n"
    "buffer_load_dwordx4 v90, s[20:23], s72 offen offset:0 nt lds\n"
    "v_cmp_le_f32 vcc, |v75|, s73\n"
    "s_nop 1\n"
    "v_cndmask_b32 v75, v228, v75, vcc\n"
    "ds_write_b128 v86, v[68:71] offset:0\n"
    "ds_write_b128 v86, v[72:75] offset:16\n"
    "v_cvt_pk_bf16_f32 v220, v204, v205\n"
    "v_cvt_pk_bf16_f32 v221, v206, v207\n"
    "v_cvt_pk_bf16_f32 v222, v208, v209\n"
    "v_cvt_pk_bf16_f32 v223, v210, v211\n"
    "v_lshlrev_b32 v212, 16, v220\n"
    "v_and_b32 v213, 0xffff0000, v220\n"
    "v_lshlrev_b32 v214, 16, v221\n"
    "v_and_b32 v215, 0xffff0000, v221\n"
    "v_lshlrev_b32 v216, 16, v222\n"
    "v_and_b32 v217, 0xffff0000, v222\n"
    "v_lshlrev_b32 v218, 16, v223\n"
    "v_and_b32 v219, 0xffff0000, v223\n"
    "v_sub_f32 v204, v204, v212\n"
    "v_sub_f32 v205, v205, v213\n"
    "v_sub_f32 v206, v206, v214\n"
    "v_sub_f32 v207, v207, v215\n"
    "v_sub_f32 v208, v208, v216\n"
    "s_add_u32 m0, s65, 76800\n"
    "s_add_u32 s72, s10, 199680\n"
    "buffer_load_dwordx4 v88, s[20:23], s72 offen offset:0 nt lds\n"
    "v_sub_f32 v209, v209, v217\n"
    "v_sub_f32 v210, v210, v218\n"
    "v_sub_f32 v211, v211, v219\n"
    "v_cvt_pk_bf16_f32 v224, v204, v205\n"
    "v_cvt_pk_bf16_f32 v225, v206, v207\n"
    "v_cvt_pk_bf16_f32 v226, v208, v209\n"
    "v_cvt_pk_bf16_f32 v227, v210, v211\n"
    "v_mov_b32 v204, 0\n"
    "v_mov_b32 v205, 0\n"
    "v_mov_b32 v206, 0\n"
    "v_mov_b32 v207, 0\n"
    "v_swap_b32 v221, v222\n"
    "v_swap_b32 v225, v226\n"
    "v_swap_b32 v141, v142\n"
    "ds_write2_b64 v144, v[220:221], v[222:223] offset0:32 offset1:40\n"
    "ds_write2_b64 v144, v[224:225], v[226:227] offset0:48 offset1:56\n"
    "ds_write2_b64 v144, v[140:141], v[142:143] offset0:64 offset1:72\n"
    "ds_write2_b64 v144, v[204:205], v[206:207] offset0:80 offset1:88\n"
    "s_waitcnt vmcnt(27)\n"
    "v_fma_f32 v4, v4, v180, -v184\n"
    "v_fma_f32 v5, v5, v181, -v185\n"
    "s_add_u32 m0, s65, 77824\n"
    "s_add_u32 s72, s10, 200704\n"
    "buffer_load_dwordx4 v89, s[20:23], s72 offen offset:0 nt lds\n"
    "v_fma_f32 v6, v6, v182, -v186\n"
    "v_fma_f32 v7, v7, v183, -v187\n"
    "v_fma_f32 v8, v8, v180, -v184\n"
    "v_fma_f32 v9, v9, v181, -v185\n"
    "v_fma_f32 v10, v10, v182, -v186\n"
    "v_fma_f32 v11, v11, v183, -v187\n"
    "v_cvt_pk_bf16_f32 v4, v4, v5\n"
    "v_cvt_pk_bf16_f32 v5, v6, v7\n"
    "v_cvt_pk_bf16_f32 v6, v8, v9\n"
    "v_cvt_pk_bf16_f32 v7, v10, v11\n"
    "ds_write_b64 v163, v[4:5] offset:0\n"
    "ds_write_b64 v163, v[6:7] offset:1024\n"
    "v_fma_f32 v12, v12, v188, -v192\n"
    "v_fma_f32 v13, v13, v189, -v193\n"
    "v_fma_f32 v14, v14, v190, -v194\n"
    "v_fma_f32 v15, v15, v191, -v195\n"
    "v_fma_f32 v16, v16, v188, -v192\n"
    "v_fma_f32 v17, v17, v189, -v193\n"
    "v_fma_f32 v18, v18, v190, -v194\n"
    "v_fma_f32 v19, v19, v191, -v195\n"
    "s_add_u32 m0, s65, 78848\n"
    "s_add_u32 s72, s10, 201728\n"
    "buffer_load_dwordx4 v90, s[20:23], s72 offen offset:0 nt lds\n"
    "v_cvt_pk_bf16_f32 v12, v12, v13\n"
    "v_cvt_pk_bf16_f32 v13, v14, v15\n"
    "v_cvt_pk_bf16_f32 v14, v16, v17\n"
    "v_cvt_pk_bf16_f32 v15, v18, v19\n"
    "ds_write_b64 v163, v[12:13] offset:2048\n"
    "ds_write_b64 v163, v[14:15] offset:3072\n"
    "v_fma_f32 v20, v20, v196, -v200\n"
    "v_fma_f32 v21, v21, v197, -v201\n"
    "v_fma_f32 v22, v22, v198, -v202\n"
    "v_fma_f32 v23, v23, v199, -v203\n"
    "v_fma_f32 v24, v24, v196, -v200\n"
    "v_fma_f32 v25, v25, v197, -v201\n"
    "v_fma_f32 v26, v26, v198, -v202\n"
    "v_fma_f32 v27, v27, v199, -v203\n"
    "v_cvt_pk_bf16_f32 v20, v20, v21\n"
    "v_cvt_pk_bf16_f32 v21, v22, v23\n"
    "v_cvt_pk_bf16_f32 v22, v24, v25\n"
    "v_cvt_pk_bf16_f32 v23, v26, v27\n"
    "ds_write_b64 v163, v[20:21] offset:4096\n"
    "ds_write_b64 v163, v[22:23] offset:5120\n"
    "s_waitcnt vmcnt(14)\n"
    "s_add_u32 m0, s65, 49152\n"
    "s_add_u32 s72, s10, 393216\n"
    "buffer_load_dwordx4 v88, s[20:23], s72 offen offset:0 nt lds\n"
    "v_fma_f32 v100, v100, v180, -v184\n"
    "v_fma_f32 v101, v101, v181, -v185\n"
    "v_fma_f32 v102, v102, v182, -v186\n"
    "v_fma_f32 v103, v103, v183, -v187\n"
    "v_fma_f32 v104, v104, v180, -v184\n"
    "v_fma_f32 v105, v105, v181, -v185\n"
    "v_fma_f32 v106, v106, v182, -v186\n"
    "v_fma_f32 v107, v107, v183, -v187\n"
    "v_cvt_pk_bf16_f32 v100, v100, v101\n"
    "v_cvt_pk_bf16_f32 v101, v102, v103\n"
    "v_cvt_pk_bf16_f32 v102, v104, v105\n"
    "v_cvt_pk_bf16_f32 v103, v106, v107\n"
    "ds_write_b64 v163, v[100:101] offset:12288\n"
    "ds_write_b64 v163, v[102:103] offset:13312\n"
    "v_fma_f32 v108, v108, v188, -v192\n"
    "v_fma_f32 v109, v109, v189, -v193\n"
    "v_fma_f32 v110, v110, v190, -v194\n"
    "v_fma_f32 v111, v111, v191, -v195\n"
    "v_fma_f32 v112, v112, v188, -v192\n"
    "v_fma_f32 v113, v113, v189, -v193\n"
    "v_fma_f32 v114, v114, v190, -v194\n"
    "s_add_u32 m0, s65, 50176\n"
    "s_add_u32 s72, s10, 394240\n"
    "buffer_load_dwordx4 v89, s[20:23], s72 offen offset:0 nt lds\n"
    "v_fma_f32 v115, v115, v191, -v195\n"
    "v_cvt_pk_bf16_f32 v108, v108, v109\n"
    "v_cvt_pk_bf16_f32 v109, v110, v111\n"
    "v_cvt_pk_bf16_f32 v110, v112, v113\n"
    "v_cvt_pk_bf16_f32 v111, v114, v115\n"
    "ds_write_b64 v163, v[108:109] offset:14336\n"
    "ds_write_b64 v163, v[110:111] offset:15360\n"
    "v_fma_f32 v116, v116, v196, -v200\n"
    "v_fma_f32 v117, v117, v197, -v201\n"
    "v_fma_f32 v118, v118, v198, -v202\n"
    "v_fma_f32 v119, v119, v199, -v203\n"
    "v_fma_f32 v120, v120, v196, -v200\n"
    "v_fma_f32 v121, v121, v197, -v201\n"
    "v_fma_f32 v122, v122, v198, -v202\n"
    "v_fma_f32 v123, v123, v199, -v203\n"
    "v_cvt_pk_bf16_f32 v116, v116, v117\n"
    "v_cvt_pk_bf16_f32 v117, v118, v119\n"
    "v_cvt_pk_bf16_f32 v118, v120, v121\n"
    "v_cvt_pk_bf16_f32 v119, v122, v123\n"
    "ds_write_b64 v163, v[116:117] offset:16384\n"
    "ds_write_b64 v163, v[118:119] offset:17408\n"
    "s_waitcnt vmcnt(17)\n"
    "ds_read_b128 v[36:39], v154 offset:0\n"
    "ds_read_b128 v[40:43], v155 offset:0\n"
    "ds_read_b128 v[44:47], v154 offset:128\n"
    "ds_read_b128 v[48:51], v155 offset:128\n"
    "ds_read_b128 v[52:55], v154 offset:256\n"
    "ds_read_b128 v[56:59], v155 offset:256\n"
    "s_mov_b64 exec, 0\n"
    "s_bitcmp1_b32 s83, 1\n"
    "s_cbranch_scc1 .LwV\n"
    "s_cmp_eq_u32 0, 0\n"
    "s_branch .LwK\n"
    ".Lw2:\n"
    "s_bitcmp1_b32 s83, 1\n"
    "s_cbranch_scc1 .Lwp1\n"
    "s_branch .Lwp0\n"
    ".Lw3:\n"
    "s_mov_b64 exec, -1\n"
    "s_waitcnt lgkmcnt(5)\n"
    "v_pk_maximum3_f16 v98, v36, v37, v98\n"
    "v_pk_maximum3_f16 v98, v38, v39, v98\n"
    "v_pk_add_u16 v36, s89, v36 clamp\n"
    "v_pk_add_u16 v37, s89, v37 clamp\n"
    "v_pk_add_u16 v38, s89, v38 clamp\n"
    "v_pk_add_u16 v39, s89, v39 clamp\n"
    "s_waitcnt lgkmcnt(4)\n"
    "v_pk_maximum3_f16 v98, v40, v41, v98\n"
    "v_pk_maximum3_f16 v98, v42, v43, v98\n"
    "v_pk_add_u16 v40, s89, v40 clamp\n"
    "v_pk_add_u16 v41, s89, v41 clamp\n"
    "v_pk_add_u16 v42, s89, v42 clamp\n"
    "v_pk_add_u16 v43, s89, v43 clamp\n"
    "s_waitcnt lgkmcnt(3)\n"
    "v_pk_maximum3_f16 v98, v44, v45, v98\n"
    "v_pk_maximum3_f16 v98, v46, v47, v98\n"
    "v_pk_add_u16 v44, s89, v44 clamp\n"
    "v_pk_add_u16 v45, s89, v45 clamp\n"
    "v_pk_add_u16 v46, s89, v46 clamp\n"
    "v_pk_add_u16 v47, s89, v47 clamp\n"
    "s_waitcnt lgkmcnt(2)\n"
    "v_pk_maximum3_f16 v98, v48, v49, v98\n"
    "v_pk_maximum3_f16 v98, v50, v51, v98\n"
    "v_pk_add_u16 v48, s89, v48 clamp\n"
    "v_pk_add_u16 v49, s89, v49 clamp\n"
    "v_pk_add_u16 v50, s89, v50 clamp\n"
    "v_pk_add_u16 v51, s89, v51 clamp\n"
    "s_waitcnt lgkmcnt(1)\n"
    "v_pk_maximum3_f16 v98, v52, v53, v98\n"
    "v_pk_maximum3_f16 v98, v54, v55, v98\n"
    "v_pk_add_u16 v52, s89, v52 clamp\n"
    "v_pk_add_u16 v53, s89, v53 clamp\n"
    "v_pk_add_u16 v54, s89, v54 clamp\n"
    "v_pk_add_u16 v55, s89, v55 clamp\n"
    "s_waitcnt lgkmcnt(0)\n"
    "v_pk_maximum3_f16 v98, v56, v57, v98\n"
    "v_pk_maximum3_f16 v98, v58, v59, v98\n"
    "v_pk_add_u16 v56, s89, v56 clamp\n"
    "v_pk_add_u16 v57, s89, v57 clamp\n"
    "v_pk_add_u16 v58, s89, v58 clamp\n"
    "v_pk_add_u16 v59, s89, v59 clamp\n"
    "v_mov_b32 v108, 0\n"
    "v_mov_b32 v109, 0\n"
    "v_mov_b32 v110, 0\n"
    "v_mov_b32 v111, 0\n"
    "v_mov_b32 v112, 0\n"
    "v_mov_b32 v113, 0\n"
    "v_mov_b32 v114, 0\n"
    "v_mov_b32 v115, 0\n"
    "v_mov_b32 v116, 0\n"
    "v_mov_b32 v117, 0\n"
    "v_mov_b32 v118, 0\n"
    "v_mov_b32 v119, 0\n"
    "v_mov_b32 v120, 0\n"
    "v_mov_b32 v121, 0\n"
    "v_mov_b32 v122, 0\n"
    "v_mov_b32 v123, 0\n"
    "s_mul_i32 s73, s79, 768\n"
    "v_add_u32 v78, s73, v250\n"
    "v_mov_b32 v79, 155936\n"
    "s_lshl2_add_u32 s74, s84, 155936\n"
    "v_mov_b32 v77, 155952\n"
    "v_writelane_b32 v77, s74, 63\n"
    "s_waitcnt lgkmcnt(0)\n"
    "s_barrier\n"
    "ds_read_b128 a[112:115], v252 offset:0\n"
    "ds_read_b128 a[116:119], v252 offset:2048\n"
    "ds_read_b128 a[120:123], v252 offset:4096\n"
    "ds_read_b128 a[124:127], v252 offset:6144\n"
    "ds_read_b128 a[128:131], v252 offset:8192\n"
    "ds_read_b128 a[132:135], v252 offset:10240\n"
    "ds_read_b128 a[136:139], v252 offset:1024\n"
    "ds_read_b128 a[140:143], v252 offset:3072\n"
    "ds_read_b128 a[144:147], v252 offset:5120\n"
    "ds_read_b128 a[148:151], v252 offset:7168\n"
    "ds_read_b128 a[152:155], v252 offset:9216\n"
    "ds_read_b128 a[156:159], v252 offset:11264\n"
    "s_add_u32 m0, s65, 51200\n"
    "s_add_u32 s72, s10, 395264\n"
    "buffer_load_dwordx4 v90, s[20:23], s72 offen offset:0 nt lds\n"
    "s_add_u32 m0, s65, 52224\n"
    "s_add_u32 s72, s10, 396288\n"
    "buffer_load_dwordx4 v88, s[20:23], s72 offen offset:0 nt lds\n"
    "s_waitcnt lgkmcnt(11)\n"
    "v_mfma_f32_16x16x32_bf16 v[4:7], a[112:115], v[36:39], 0\n"
    "s_waitcnt lgkmcnt(10)\n"
    "v_mfma_f32_16x16x32_bf16 v[4:7], a[116:119], v[40:43], v[4:7]\n"
    "s_waitcnt lgkmcnt(9)\n"
    "v_mfma_f32_16x16x32_bf16 v[4:7], a[120:123], v[44:47], v[4:7]\n"
    "s_waitcnt lgkmcnt(8)\n"
    "v_mfma_f32_16x16x32_bf16 v[4:7], a[124:127], v[48:51], v[4:7]\n"
    "s_waitcnt lgkmcnt(7)\n"
    "v_mfma_f32_16x16x32_bf16 v[4:7], a[128:131], v[52:55], v[4:7]\n"
    "s_waitcnt lgkmcnt(6)\n"
    "v_mfma_f32_16x16x32_bf16 v[4:7], a[132:135], v[56:59], v[4:7]\n"
    "s_waitcnt lgkmcnt(5)\n"
    "v_mfma_f32_16x16x32_bf16 v[20:23], a[136:139], v[36:39], 0\n"
    "v_accvgpr_write_b32 a192, v36\n"
    "v_accvgpr_write_b32 a193, v37\n"
    "v_accvgpr_write_b32 a194, v38\n"
    "v_accvgpr_write_b32 a195, v39\n"
    "s_waitcnt lgkmcnt(4)\n"
    "v_mfma_f32_16x16x32_bf16 v[20:23], a[140:143], v[40:43], v[20:23]\n"
    "v_accvgpr_write_b32 a196, v40\n"
    "v_accvgpr_write_b32 a197, v41\n"
    "v_accvgpr_write_b32 a198, v42\n"
    "v_accvgpr_write_b32 a199, v43\n"
    "s_waitcnt lgkmcnt(3)\n"
    "v_mfma_f32_16x16x32_bf16 v[20:23], a[144:147], v[44:47], v[20:23]\n"
    "v_accvgpr_write_b32 a200, v44\n"
    "v_accvgpr_write_b32 a201, v45\n"
    "v_accvgpr_write_b32 a202, v46\n"
    "v_accvgpr_write_b32 a203, v47\n"
    "s_waitcnt lgkmcnt(2)\n"
    "v_mfma_f32_16x16x32_bf16 v[20:23], a[148:151], v[48:51], v[20:23]\n"
    "v_accvgpr_write_b32 a204, v48\n"
    "v_accvgpr_write_b32 a205, v49\n"
    "v_accvgpr_write_b32 a206, v50\n"
    "v_accvgpr_write_b32 a207, v51\n"
    "s_waitcnt lgkmcnt(1)\n"
    "v_mfma_f32_16x16x32_bf16 v[20:23], a[152:155], v[52:55], v[20:23]\n"
    "v_accvgpr_write_b32 a208, v52\n"
    "v_accvgpr_write_b32 a209, v53\n"
    "v_accvgpr_write_b32 a210, v54\n"
    "v_accvgpr_write_b32 a211, v55\n"
    "s_waitcnt lgkmcnt(0)\n"
    "v_mfma_f32_16x16x32_bf16 v[20:23], a[156:159], v[56:59], v[20:23]\n"
    "v_accvgpr_write_b32 a212, v56\n"
    "v_accvgpr_write_b32 a213, v57\n"
    "v_accvgpr_write_b32 a214, v58\n"
    "v_accvgpr_write_b32 a215, v59\n"
    "s_add_u32 m0, s65, 53248\n"
    "s_add_u32 s72, s10, 397312\n"
    "buffer_load_dwordx4 v89, s[20:23], s72 offen offset:0 nt lds\n"
    "buffer_load_dwordx2 v[236:237], v87, s[36:39], 0 offen offset:0 sc1\n"
    "ds_read_b128 v[16:19], v78 offset:0\n"
    "ds_read_b128 v[32:35], v78 offset:16\n"
    "ds_read_b128 v[48:51], v78 offset:128\n"
    "ds_read_b128 v[64:67], v78 offset:144\n"
    "ds_read_b128 v[100:103], v78 offset:768\n"
    "ds_read_b128 v[104:107], v78 offset:784\n"
    "v_max3_f32 v76, v4, v5, v6\n"
    "ds_read_b128 a[0:3], v252 offset:12288\n"
    "v_max3_f32 v76, v76, v7, v20\n"
    "ds_read_b128 a[4:7], v252 offset:14336\n"
    "v_max3_f32 v76, v76, v21, v22\n"
    "ds_read_b128 a[8:11], v252 offset:16384\n"
    "v_max_f32 v76, v76, v23\n"
    "ds_read_b128 a[12:15], v252 offset:18432\n"
    "s_nop 0\n"
    "v_max_f32_dpp v76, v76, v76 row_shr:1\n"
    "ds_read_b128 a[16:19], v252 offset:20480\n"
    "ds_read_b128 a[20:23], v252 offset:22528\n"
    "v_max_f32_dpp v76, v76, v76 row_shr:2\n"
    "ds_read_b128 a[24:27], v252 offset:13312\n"
    "s_nop 0\n"
    "v_max_f32_dpp v76, v76, v76 row_shr:4\n"
    "ds_read_b128 a[28:31], v252 offset:15360\n"
    "s_nop 0\n"
    "v_max_f32_dpp v76, v76, v76 row_shr:8\n"
    "ds_read_b128 a[32:35], v252 offset:17408\n"
    "s_nop 0\n"
    "v_max_f32_dpp v76, v76, v76 row_bcast:15 row_mask:0xa\n"
    "ds_read_b128 a[36:39], v252 offset:19456\n"
    "s_nop 0\n"
    "v_max_f32_dpp v76, v76, v76 row_bcast:31 row_mask:0xc\n"
    "ds_read_b128 a[40:43], v252 offset:21504\n"
    "ds_read_b128 a[44:47], v252 offset:23552\n"
    "ds_write_b32 v77, v76\n"
    "s_add_u32 m0, s65, 54272\n"
    "s_add_u32 s72, s10, 398336\n"
    "buffer_load_dwordx4 v90, s[20:23], s72 offen offset:0 nt lds\n"
    "s_add_u32 m0, s65, 98304\n"
    "s_add_u32 s72, s10, 589824\n"
    "buffer_load_dwordx4 v88, s[20:23], s72 offen offset:0 nt lds\n"
    "s_waitcnt lgkmcnt(0)\n"
    "s_barrier\n"
    "ds_read_b128 v[80:83], v79 offset:0\n"
    "s_waitcnt vmcnt(14)\n"
    "buffer_store_dwordx2 v[238:239], v240, s[36:39], s80 offen offset:0 sc1\n"
    "s_lshl_b32 s73, s79, 8\n"
    "s_add_u32 s80, s80, s73\n"
    "s_mul_i32 s73, s79, 98304\n"
    "s_add_u32 s71, s71, s73\n"
    "s_add_u32 m0, s65, 99328\n"
    "s_add_u32 s72, s10, 590848\n"
    "buffer_load_dwordx4 v89, s[20:23], s72 offen offset:0 nt lds\n"
    "s_waitcnt lgkmcnt(0)\n"
    "v_max3_f32 v76, v80, v81, v82\n"
    "v_max_f32 v76, v76, v83\n"
    "v_add_f32 v76, 0xc2b00000, v76\n"
    "s_add_u32 m0, s65, 100352\n"
    "s_add_u32 s72, s10, 591872\n"
    "buffer_load_dwordx4 v90, s[20:23], s72 offen offset:0 nt lds\n"
    "s_bitcmp1_b32 s83, 2\n"
    "s_cbranch_scc1 .Lf5b\n"
    "s_sleep 0\n"
    "s_add_u32 m0, s65, 73728\n"
    "s_add_u32 s72, s85, 196608\n"
    "buffer_load_dwordx4 v88, s[20:23], s72 offen offset:0 nt lds\n"
    "s_add_u32 m0, s65, 74752\n"
    "s_add_u32 s72, s85, 197632\n"
    "buffer_load_dwordx4 v89, s[20:23], s72 offen offset:0 nt lds\n"
    "s_add_u32 m0, s65, 75776\n"
    "s_add_u32 s72, s85, 198656\n"
    "buffer_load_dwordx4 v90, s[20:23], s72 offen offset:0 nt lds\n"
    "s_add_u32 m0, s65, 76800\n"
    "s_add_u32 s72, s85, 199680\n"
    "buffer_load_dwordx4 v88, s[20:23], s72 offen offset:0 nt lds\n"
    "s_add_u32 m0, s65, 77824\n"
    "s_add_u32 s72, s85, 200704\n"
    "buffer_load_dwordx4 v89, s[20:23], s72 offen offset:0 nt lds\n"
    "s_add_u32 m0, s65, 78848\n"
    "s_add_u32 s72, s85, 201728\n"
    "buffer_load_dwordx4 v90, s[20:23], s72 offen offset:0 nt lds\n"
    "s_add_u32 m0, s65, 49152\n"
    "s_add_u32 s72, s85, 393216\n"
    "buffer_load_dwordx4 v88, s[20:23], s72 offen offset:0 nt lds\n"
    "s_add_u32 m0, s65, 50176\n"
    "s_add_u32 s72, s85, 394240\n"
    "buffer_load_dwordx4 v89, s[20:23], s72 offen offset:0 nt lds\n"
    "s_add_u32 m0, s65, 51200\n"
    "s_add_u32 s72, s85, 395264\n"
    "buffer_load_dwordx4 v90, s[20:23], s72 offen offset:0 nt lds\n"
    "s_add_u32 m0, s65, 52224\n"
    "s_add_u32 s72, s85, 396288\n"
    "buffer_load_dwordx4 v88, s[20:23], s72 offen offset:0 nt lds\n"
    "s_add_u32 m0, s65, 53248\n"
    "s_add_u32 s72, s85, 397312\n"
    "buffer_load_dwordx4 v89, s[20:23], s72 offen offset:0 nt lds\n"
    "s_add_u32 m0, s65, 54272\n"
    "s_add_u32 s72, s85, 398336\n"
    "buffer_load_dwordx4 v90, s[20:23], s72 offen offset:0 nt lds\n"
    "s_add_u32 m0, s65, 98304\n"
    "s_add_u32 s72, s85, 589824\n"
    "buffer_load_dwordx4 v88, s[20:23], s72 offen offset:0 nt lds\n"
    "s_add_u32 m0, s65, 99328\n"
    "s_add_u32 s72, s85, 590848\n"
    "buffer_load_dwordx4 v89, s[20:23], s72 offen offset:0 nt lds\n"
    "s_add_u32 m0, s65, 100352\n"
    "s_add_u32 s72, s85, 591872\n"
    "buffer_load_dwordx4 v90, s[20:23], s72 offen offset:0 nt lds\n"
    "s_branch .Lf5e\n"
    ".Lf5b:\n"
    ".Lf5e:\n"
    "s_nop 4\n"
    "s_add_u32 m0, s65, 101376\n"
    "s_add_u32 s72, s85, 592896\n"
    "buffer_load_dwordx4 v88, s[20:23], s72 offen offset:0 nt lds\n"
    "s_add_u32 m0, s65, 102400\n"
    "s_add_u32 s72, s85, 593920\n"
    "buffer_load_dwordx4 v89, s[20:23], s72 offen offset:0 nt lds\n"
    "s_add_u32 m0, s65, 103424\n"
    "s_add_u32 s72, s85, 594944\n"
    "buffer_load_dwordx4 v90, s[20:23], s72 offen offset:0 nt lds\n"
    "v_sub_f32 v48, v48, v76\n"
    "v_sub_f32 v51, v51, v76\n"
    "v_sub_f32 v49, v49, v76\n"
    "v_sub_f32 v50, v50, v76\n"
    "v_sub_f32 v66, v66, v76\n"
    "v_sub_f32 v65, v65, v76\n"
    "v_mfma_f32_16x16x32_bf16 v[36:39], a[0:3], a[192:195], v[48:51]\n"
    "s_waitcnt vmcnt(12)\n"
    "ds_read_b128 v[200:203], v97 offset:24832\n"
    "v_sub_f32 v67, v67, v76\n"
    "v_mfma_f32_16x16x32_bf16 v[36:39], a[4:7], a[196:199], v[36:39]\n"
    "v_sub_f32 v33, v33, v76\n"
    "v_sub_f32 v32, v32, v76\n"
    "v_mfma_f32_16x16x32_bf16 v[36:39], a[8:11], a[200:203], v[36:39]\n"
    "ds_read_b128 v[184:187], v97 offset:24576\n"
    "ds_read_b128 v[196:199], v96 offset:24832\n"
    "ds_read_b128 v[192:195], v97 offset:24704\n"
    "v_mfma_f32_16x16x32_bf16 v[36:39], a[12:15], a[204:207], v[36:39]\n"
    "v_sub_f32 v64, v64, v76\n"
    "v_sub_f32 v35, v35, v76\n"
    "v_sub_f32 v19, v19, v76\n"
    "v_mfma_f32_16x16x32_bf16 v[52:55], a[24:27], a[192:195], v[64:67]\n"
    "v_add_f32 v21, v21, v33\n"
    "v_sub_f32 v16, v16, v76\n"
    "v_mfma_f32_16x16x32_bf16 v[36:39], a[16:19], a[208:211], v[36:39]\n"
    "v_sub_f32 v18, v18, v76\n"
    "v_add_f32 v20, v20, v32\n"
    "v_sub_f32 v17, v17, v76\n"
    "v_mfma_f32_16x16x32_bf16 v[52:55], a[28:31], a[196:199], v[52:55]\n"
    "ds_read_b128 v[188:191], v96 offset:24704\n"
    "ds_read_b128 v[180:183], v96 offset:24576\n"
    "v_add_f32 v6, v6, v18\n"
    "v_sub_f32 v34, v34, v76\n"
    "v_mfma_f32_16x16x32_bf16 v[36:39], a[20:23], a[212:215], v[36:39]\n"
    "s_waitcnt lgkmcnt(5)\n"
    "v_pk_maximum3_f16 v98, v200, v201, v98\n"
    "v_exp_f32 v20, v20\n"
    "v_pk_maximum3_f16 v98, v202, v203, v98\n"
    "v_pk_add_u16 v203, s89, v203 clamp\n"
    "v_pk_add_u16 v200, s89, v200 clamp\n"
    "v_mfma_f32_16x16x32_bf16 v[52:55], a[32:35], a[200:203], v[52:55]\n"
    "s_waitcnt lgkmcnt(3)\n"
    "v_pk_maximum3_f16 v98, v196, v197, v98\n"
    "v_pk_maximum3_f16 v98, v198, v199, v98\n"
    "v_mfma_f32_16x16x32_bf16 v[52:55], a[36:39], a[204:207], v[52:55]\n"
    "v_exp_f32 v21, v21\n"
    "v_pk_add_u16 v202, s89, v202 clamp\n"
    "v_pk_add_u16 v201, s89, v201 clamp\n"
    "v_mfma_f32_16x16x32_bf16 v[52:55], a[40:43], a[208:211], v[52:55]\n"
    "v_add_f32 v22, v22, v34\n"
    "v_add_f32 v7, v7, v19\n"
    "v_mfma_f32_16x16x32_bf16 v[24:27], a[156:159], v[200:203], v[32:35]\n"
    "s_waitcnt lgkmcnt(2)\n"
    "v_pk_maximum3_f16 v98, v192, v193, v98\n"
    "v_pk_maximum3_f16 v98, v194, v195, v98\n"
    "v_pk_add_u16 v193, s89, v193 clamp\n"
    "v_pk_add_u16 v198, s89, v198 clamp\n"
    "v_add_f32 v5, v5, v17\n"
    "v_mfma_f32_16x16x32_bf16 v[52:55], a[44:47], a[212:215], v[52:55]\n"
    "s_waitcnt lgkmcnt(1)\n"
    "v_pk_add_u16 v194, s89, v194 clamp\n"
    "v_sub_f32 v69, v69, v76\n"
    "v_sub_f32 v73, v73, v76\n"
    "v_pk_maximum3_f16 v98, v188, v189, v98\n"
    "v_pk_maximum3_f16 v98, v190, v191, v98\n"
    "v_pk_add_u16 v196, s89, v196 clamp\n"
    "v_pk_maximum3_f16 v98, v184, v185, v98\n"
    "v_pk_add_u16 v197, s89, v197 clamp\n"
    "v_pk_maximum3_f16 v98, v186, v187, v98\n"
    "v_mfma_f32_16x16x32_bf16 v[8:11], a[132:135], v[200:203], v[16:19]\n"
    "v_sub_f32 v75, v75, v76\n"
    "v_pk_add_u16 v199, s89, v199 clamp\n"
    "v_sub_f32 v101, v101, v76\n"
    "v_exp_f32 v54, v54\n"
    "v_mfma_f32_16x16x32_bf16 v[24:27], a[152:155], v[196:199], v[24:27]\n"
    "s_waitcnt lgkmcnt(0)\n"
    "v_pk_add_u16 v188, s89, v188 clamp\n"
    "v_pk_add_u16 v184, s89, v184 clamp\n"
    "v_sub_f32 v70, v70, v76\n"
    "v_pk_maximum3_f16 v98, v180, v181, v98\n"
    "v_pk_add_u16 v186, s89, v186 clamp\n"
    "v_mfma_f32_16x16x32_bf16 v[56:59], a[44:47], v[200:203], v[64:67]\n"
    "v_pk_add_u16 v189, s89, v189 clamp\n"
    "v_pk_add_u16 v195, s89, v195 clamp\n"
    "v_pk_add_u16 v192, s89, v192 clamp\n"
    "v_pk_add_u16 v185, s89, v185 clamp\n"
    "v_pk_maximum3_f16 v98, v182, v183, v98\n"
    "v_exp_f32 v55, v55\n"
    "v_sub_f32 v68, v68, v76\n"
    "v_mfma_f32_16x16x32_bf16 v[56:59], a[40:43], v[196:199], v[56:59]\n"
    "v_exp_f32 v52, v52\n"
    "v_pk_add_u16 v190, s89, v190 clamp\n"
    "v_mfma_f32_16x16x32_bf16 v[8:11], a[128:131], v[196:199], v[8:11]\n"
    "v_accvgpr_write_b32 a231, v195\n"
    "v_sub_f32 v102, v102, v76\n"
    "v_sub_f32 v71, v71, v76\n"
    "v_sub_f32 v107, v107, v76\n"
    "v_mfma_f32_16x16x32_bf16 v[40:43], a[20:23], v[200:203], v[48:51]\n"
    "v_sub_f32 v74, v74, v76\n"
    "v_add_f32 v23, v23, v35\n"
    "v_mfma_f32_16x16x32_bf16 v[40:43], a[16:19], v[196:199], v[40:43]\n"
    "v_pk_add_u16 v183, s89, v183 clamp\n"
    "v_accvgpr_write_b32 a228, v192\n"
    "v_mfma_f32_16x16x32_bf16 v[56:59], a[36:39], v[192:195], v[56:59]\n"
    "v_add_f32 v4, v4, v16\n"
    "v_exp_f32 v6, v6\n"
    "v_mfma_f32_16x16x32_bf16 v[24:27], a[148:151], v[192:195], v[24:27]\n"
    "v_sub_f32 v72, v72, v76\n"
    "v_pk_add_u16 v187, s89, v187 clamp\n"
    "v_pk_add_u16 v191, s89, v191 clamp\n"
    "v_mfma_f32_16x16x32_bf16 v[8:11], a[124:127], v[192:195], v[8:11]\n"
    "v_exp_f32 v53, v53\n"
    "v_accvgpr_write_b32 a226, v190\n"
    "v_mfma_f32_16x16x32_bf16 v[8:11], a[120:123], v[188:191], v[8:11]\n"
    "v_accvgpr_write_b32 a221, v185\n"
    "v_exp_f32 v39, v39\n"
    "v_exp_f32 v22, v22\n"
    "v_pk_add_u16 v181, s89, v181 clamp\n"
    "v_mfma_f32_16x16x32_bf16 v[56:59], a[32:35], v[188:191], v[56:59]\n"
    "v_exp_f32 v36, v36\n"
    "v_pk_add_u16 v182, s89, v182 clamp\n"
    "v_exp_f32 v23, v23\n"
    "v_mfma_f32_16x16x32_bf16 v[24:27], a[144:147], v[188:191], v[24:27]\n"
    "v_accvgpr_write_b32 a237, v201\n"
    "v_pk_add_u16 v180, s89, v180 clamp\n"
    "v_mfma_f32_16x16x32_bf16 v[40:43], a[12:15], v[192:195], v[40:43]\n"
    "v_accvgpr_write_b32 a239, v203\n"
    "v_accvgpr_write_b32 a223, v187\n"
    "v_exp_f32 v7, v7\n"
    "v_accvgpr_write_b32 a233, v197\n"
    "v_mfma_f32_16x16x32_bf16 v[8:11], a[116:119], v[184:187], v[8:11]\n"
    "v_accvgpr_write_b32 a232, v196\n"
    "v_accvgpr_write_b32 a235, v199\n"
    "v_mfma_f32_16x16x32_bf16 v[40:43], a[8:11], v[188:191], v[40:43]\n"
    "ds_write_b128 v86, v[68:71] offset:0\n"
    "v_mfma_f32_16x16x32_bf16 v[40:43], a[4:7], v[184:187], v[40:43]\n"
    "v_sub_f32 v106, v106, v76\n"
    "v_accvgpr_write_b32 a234, v198\n"
    "v_mfma_f32_16x16x32_bf16 v[8:11], a[112:115], v[180:183], v[8:11]\n"
    "v_cvt_pk_bf16_f32 v70, v20, v21\n"
    "v_exp_f32 v37, v37\n"
    "v_cvt_pk_bf16_f32 v69, v6, v7\n"
    "v_accvgpr_write_b32 a218, v182\n"
    "v_mfma_f32_16x16x32_bf16 v[40:43], a[0:3], v[180:183], v[40:43]\n"
    "ds_write_b128 v86, v[72:75] offset:16\n"
    "v_accvgpr_write_b32 a230, v194\n"
    "v_mfma_f32_16x16x32_bf16 v[24:27], a[140:143], v[184:187], v[24:27]\n"
    "v_exp_f32 v10, v10\n"
    "v_mfma_f32_16x16x32_bf16 v[56:59], a[28:31], v[184:187], v[56:59]\n"
    "v_cvt_pk_bf16_f32 v72, v36, v37\n"
    "v_sub_f32 v103, v103, v76\n"
    "v_mfma_f32_16x16x32_bf16 v[56:59], a[24:27], v[180:183], v[56:59]\n"
    "v_sub_f32 v104, v104, v76\n"
    "v_accvgpr_write_b32 a217, v181\n"
    "v_exp_f32 v4, v4\n"
    "v_sub_f32 v100, v100, v76\n"
    "v_accvgpr_write_b32 a227, v191\n"
    "v_exp_f32 v11, v11\n"
    "v_exp_f32 v5, v5\n"
    "v_exp_f32 v38, v38\n"
    "v_exp_f32 v41, v41\n"
    "v_exp_f32 v9, v9\n"
    "v_exp_f32 v59, v59\n"
    "v_accvgpr_write_b32 a220, v184\n"
    "v_accvgpr_write_b32 a238, v202\n"
    "v_accvgpr_write_b32 a236, v200\n"
    "v_exp_f32 v43, v43\n"
    "v_cvt_pk_bf16_f32 v77, v10, v11\n"
    "v_exp_f32 v8, v8\n"
    "v_cvt_pk_bf16_f32 v75, v54, v55\n"
    "v_mfma_f32_16x16x32_bf16 v[24:27], a[136:139], v[180:183], v[24:27]\n"
    "v_cvt_pk_bf16_f32 v71, v22, v23\n"
    "v_sub_f32 v105, v105, v76\n"
    "v_accvgpr_write_b32 a216, v180\n"
    "v_exp_f32 v58, v58\n"
    "v_cvt_pk_bf16_f32 v73, v38, v39\n"
    "v_exp_f32 v40, v40\n"
    "v_accvgpr_write_b32 a222, v186\n"
    "v_accvgpr_write_b32 a225, v189\n"
    "v_exp_f32 v24, v24\n"
    "v_exp_f32 v57, v57\n"
    "v_cvt_pk_bf16_f32 v68, v4, v5\n"
    "v_cvt_pk_bf16_f32 v80, v40, v41\n"
    "v_exp_f32 v27, v27\n"
    "v_exp_f32 v25, v25\n"
    "v_cvt_pk_bf16_f32 v76, v8, v9\n"
    "v_accvgpr_write_b32 a224, v188\n"
    "v_accvgpr_write_b32 a219, v183\n"
    "v_exp_f32 v26, v26\n"
    "v_exp_f32 v42, v42\n"
    "v_cvt_pk_bf16_f32 v78, v24, v25\n"
    "v_exp_f32 v56, v56\n"
    "v_cvt_pk_bf16_f32 v74, v52, v53\n"
    "v_accvgpr_write_b32 a229, v193\n"
    "v_cvt_pk_bf16_f32 v79, v26, v27\n"
    "v_cvt_pk_bf16_f32 v82, v56, v57\n"
    "v_cvt_pk_bf16_f32 v83, v58, v59\n"
    "v_cvt_pk_bf16_f32 v81, v42, v43\n"
    "s_waitcnt vmcnt(8)\n"
    "v_cmp_eq_u64 s[54:55], v[236:237], v[238:239]\n"
    "s_nop 0\n"
    "s_cmp_eq_u64 s[54:55], -1\n"
    "s_cbranch_scc0 .LpollAV\n"
    ".LpollAVr:\n"
    "s_add_u32 m0, s65, 24576\n"
    "s_add_u32 s72, s65, 49152\n"
    "buffer_load_dwordx4 v1, s[16:19], s72 offen offset:0 sc1 lds\n"
    "s_add_u32 m0, s65, 25600\n"
    "s_add_u32 s72, s65, 50176\n"
    "buffer_load_dwordx4 v1, s[16:19], s72 offen offset:0 sc1 lds\n"
    "s_add_u32 m0, s65, 26624\n"
    "s_add_u32 s72, s65, 51200\n"
    "buffer_load_dwordx4 v1, s[16:19], s72 offen offset:0 sc1 lds\n"
    "s_add_u32 m0, s65, 27648\n"
    "s_add_u32 s72, s65, 52224\n"
    "buffer_load_dwordx4 v1, s[16:19], s72 offen offset:0 sc1 lds\n"
    "s_add_u32 m0, s65, 28672\n"
    "s_add_u32 s72, s65, 53248\n"
    "buffer_load_dwordx4 v1, s[16:19], s72 offen offset:0 sc1 lds\n"
    "s_add_u32 m0, s65, 29696\n"
    "s_add_u32 s72, s65, 54272\n"
    "buffer_load_dwordx4 v1, s[16:19], s72 offen offset:0 sc1 lds\n"
    "s_waitcnt vmcnt(12)\n"
    "ds_read_b128 v[156:159], v96 offset:0\n"
    "ds_read_b128 v[172:175], v96 offset:256\n"
    "ds_read_b128 v[168:171], v97 offset:128\n"
    "ds_read_b128 v[176:179], v97 offset:256\n"
    "ds_read_b128 v[160:163], v97 offset:0\n"
    "ds_read_b128 v[164:167], v96 offset:128\n"
    "s_waitcnt lgkmcnt(5)\n"
    "v_pk_maximum3_f16 v98, v156, v157, v98\n"
    "v_pk_maximum3_f16 v98, v158, v159, v98\n"
    "v_pk_add_u16 v157, s89, v157 clamp\n"
    "v_pk_add_u16 v158, s89, v158 clamp\n"
    "v_pk_add_u16 v156, s89, v156 clamp\n"
    "v_pk_add_u16 v159, s89, v159 clamp\n"
    "s_waitcnt lgkmcnt(1)\n"
    "v_pk_maximum3_f16 v98, v160, v161, v98\n"
    "s_add_u32 m0, s65, 73728\n"
    "s_add_u32 s72, s65, 73728\n"
    "buffer_load_dwordx4 v1, s[16:19], s72 offen offset:0 sc1 lds\n"
    "v_mfma_f32_16x16x32_bf16 v[28:31], a[136:139], v[156:159], v[32:35]\n"
    "s_waitcnt lgkmcnt(0)\n"
    "v_pk_maximum3_f16 v98, v162, v163, v98\n"
    "v_pk_maximum3_f16 v98, v164, v165, v98\n"
    "v_mfma_f32_16x16x32_bf16 v[12:15], a[112:115], v[156:159], v[16:19]\n"
    "v_pk_add_u16 v161, s89, v161 clamp\n"
    "v_pk_maximum3_f16 v98, v166, v167, v98\n"
    "v_pk_add_u16 v160, s89, v160 clamp\n"
    "v_pk_add_u16 v162, s89, v162 clamp\n"
    "v_mfma_f32_16x16x32_bf16 v[60:63], a[24:27], v[156:159], v[64:67]\n"
    "v_pk_maximum3_f16 v98, v168, v169, v98\n"
    "v_pk_add_u16 v163, s89, v163 clamp\n"
    "v_mfma_f32_16x16x32_bf16 v[44:47], a[0:3], v[156:159], v[48:51]\n"
    "v_pk_maximum3_f16 v98, v170, v171, v98\n"
    "v_pk_add_u16 v164, s89, v164 clamp\n"
    "v_mfma_f32_16x16x32_bf16 v[12:15], a[116:119], v[160:163], v[12:15]\n"
    "v_pk_add_u16 v165, s89, v165 clamp\n"
    "s_add_u32 m0, s65, 74752\n"
    "s_add_u32 s72, s65, 74752\n"
    "buffer_load_dwordx4 v1, s[16:19], s72 offen offset:0 sc1 lds\n"
    "v_pk_add_u16 v169, s89, v169 clamp\n"
    "v_pk_add_u16 v168, s89, v168 clamp\n"
    "v_mfma_f32_16x16x32_bf16 v[60:63], a[28:31], v[160:163], v[60:63]\n"
    "v_pk_maximum3_f16 v98, v172, v173, v98\n"
    "v_pk_add_u16 v170, s89, v170 clamp\n"
    "v_mfma_f32_16x16x32_bf16 v[28:31], a[140:143], v[160:163], v[28:31]\n"
    "v_pk_maximum3_f16 v98, v174, v175, v98\n"
    "v_pk_add_u16 v166, s89, v166 clamp\n"
    "v_pk_add_u16 v167, s89, v167 clamp\n"
    "v_mfma_f32_16x16x32_bf16 v[44:47], a[4:7], v[160:163], v[44:47]\n"
    "v_pk_maximum3_f16 v98, v176, v177, v98\n"
    "v_accvgpr_write_b32 a251, v167\n"
    "v_mfma_f32_16x16x32_bf16 v[44:47], a[8:11], v[164:167], v[44:47]\n"
    "v_pk_maximum3_f16 v98, v178, v179, v98\n"
    "v_pk_add_u16 v176, s89, v176 clamp\n"
    "v_mfma_f32_16x16x32_bf16 v[60:63], a[32:35], v[164:167], v[60:63]\n"
    "v_pk_add_u16 v171, s89, v171 clamp\n"
    "s_add_u32 m0, s65, 75776\n"
    "s_add_u32 s72, s65, 75776\n"
    "buffer_load_dwordx4 v1, s[16:19], s72 offen offset:0 sc1 lds\n"
    "v_pk_add_u16 v174, s89, v174 clamp\n"
    "v_mfma_f32_16x16x32_bf16 v[28:31], a[144:147], v[164:167], v[28:31]\n"
    "v_pk_add_u16 v173, s89, v173 clamp\n"
    "v_pk_add_u16 v172, s89, v172 clamp\n"
    "v_mfma_f32_16x16x32_bf16 v[12:15], a[120:123], v[164:167], v[12:15]\n"
    "v_accvgpr_write_b32 a254, v170\n"
    "v_pk_add_u16 v178, s89, v178 clamp\n"
    "v_mfma_f32_16x16x32_bf16 v[28:31], a[148:151], v[168:171], v[28:31]\n"
    "v_accvgpr_write_b32 a243, v159\n"
    "v_accvgpr_write_b32 a162, v174\n"
    "v_mfma_f32_16x16x32_bf16 v[12:15], a[124:127], v[168:171], v[12:15]\n"
    "v_pk_add_u16 v179, s89, v179 clamp\n"
    "v_pk_add_u16 v175, s89, v175 clamp\n"
    "v_mfma_f32_16x16x32_bf16 v[44:47], a[12:15], v[168:171], v[44:47]\n"
    "v_accvgpr_write_b32 a166, v178\n"
    "v_accvgpr_write_b32 a161, v173\n"
    "v_accvgpr_write_b32 a245, v161\n"
    "s_add_u32 m0, s65, 76800\n"
    "s_add_u32 s72, s65, 76800\n"
    "buffer_load_dwordx4 v1, s[16:19], s72 offen offset:0 sc1 lds\n"
    "v_mfma_f32_16x16x32_bf16 v[28:31], a[152:155], v[172:175], v[28:31]\n"
    "v_accvgpr_write_b32 a163, v175\n"
    "v_pk_add_u16 v177, s89, v177 clamp\n"
    "v_mfma_f32_16x16x32_bf16 v[44:47], a[16:19], v[172:175], v[44:47]\n"
    "v_accvgpr_write_b32 a160, v172\n"
    "v_accvgpr_write_b32 a252, v168\n"
    "v_mfma_f32_16x16x32_bf16 v[28:31], a[156:159], v[176:179], v[28:31]\n"
    "v_accvgpr_write_b32 a249, v165\n"
    "v_accvgpr_write_b32 a255, v171\n"
    "v_mfma_f32_16x16x32_bf16 v[12:15], a[128:131], v[172:175], v[12:15]\n"
    "v_accvgpr_write_b32 a247, v163\n"
    "v_accvgpr_write_b32 a164, v176\n"
    "v_mfma_f32_16x16x32_bf16 v[60:63], a[36:39], v[168:171], v[60:63]\n"
    "v_accvgpr_write_b32 a242, v158\n"
    "v_accvgpr_write_b32 a250, v166\n"
    "v_mfma_f32_16x16x32_bf16 v[44:47], a[20:23], v[176:179], v[44:47]\n"
    "v_accvgpr_write_b32 a241, v157\n"
    "s_add_u32 m0, s65, 77824\n"
    "s_add_u32 s72, s65, 77824\n"
    "buffer_load_dwordx4 v1, s[16:19], s72 offen offset:0 sc1 lds\n"
    "v_accvgpr_write_b32 a248, v164\n"
    "v_mfma_f32_16x16x32_bf16 v[12:15], a[132:135], v[176:179], v[12:15]\n"
    "v_accvgpr_write_b32 a246, v162\n"
    "v_exp_f32 v29, v29\n"
    "v_mfma_f32_16x16x32_bf16 v[60:63], a[40:43], v[172:175], v[60:63]\n"
    "v_exp_f32 v28, v28\n"
    "v_mfma_f32_16x16x32_bf16 v[60:63], a[44:47], v[176:179], v[60:63]\n"
    "v_exp_f32 v31, v31\n"
    "v_exp_f32 v30, v30\n"
    "v_exp_f32 v45, v45\n"
    "v_cvt_pk_bf16_f32 v86, v28, v29\n"
    "v_exp_f32 v47, v47\n"
    "v_accvgpr_write_b32 a240, v156\n"
    "v_exp_f32 v12, v12\n"
    "v_exp_f32 v14, v14\n"
    "v_accvgpr_write_b32 a167, v179\n"
    "v_exp_f32 v15, v15\n"
    "s_add_u32 m0, s65, 78848\n"
    "s_add_u32 s72, s65, 78848\n"
    "buffer_load_dwordx4 v1, s[16:19], s72 offen offset:0 sc1 lds\n"
    "v_accvgpr_write_b32 a253, v169\n"
    "v_exp_f32 v63, v63\n"
    "v_exp_f32 v13, v13\n"
    "v_exp_f32 v44, v44\n"
    "v_cvt_pk_bf16_f32 v84, v12, v13\n"
    "v_cvt_pk_bf16_f32 v87, v30, v31\n"
    "v_exp_f32 v46, v46\n"
    "v_exp_f32 v61, v61\n"
    "v_accvgpr_write_b32 a165, v177\n"
    "v_exp_f32 v60, v60\n"
    "v_exp_f32 v62, v62\n"
    "v_cvt_pk_bf16_f32 v90, v60, v61\n"
    "v_accvgpr_write_b32 a244, v160\n"
    "v_cvt_pk_bf16_f32 v85, v14, v15\n"
    "v_cvt_pk_bf16_f32 v89, v46, v47\n"
    "v_cvt_pk_bf16_f32 v88, v44, v45\n"
    "v_cvt_pk_bf16_f32 v91, v62, v63\n"
    "s_bitcmp1_b32 s83, 1\n"
    "s_cbranch_scc1 .Lf6b\n"
    "s_lshl_b32 s73, s53, 1\n"
    "s_add_u32 s73, s73, s79\n"
    "s_mul_i32 s74, s73, 6144\n"
    "s_add_u32 s74, s74, 1425408\n"
    "s_lshl_b32 s73, s73, 8\n"
    "s_add_u32 s73, s73, 1557504\n"
    "s_mov_b64 s[20:21], s[16:17]\n"
    "s_mov_b32 s44, 98304\n"
    "s_mov_b32 s22, 1572864\n"
    "s_sub_u32 s72, s42, 1572864\n"
    "s_sub_u32 s45, s42, 49152\n"
    "s_bitcmp1_b32 s84, 0\n"
    "s_cselect_b32 s52, s71, s73\n"
    "s_cselect_b32 s71, s71, s74\n"
    "s_cselect_b32 s63, s63, s72\n"
    "s_cselect_b32 s42, s45, s42\n"
    "s_cselect_b32 s45, s44, 12288\n"
    "s_cselect_b32 s44, s44, 24576\n"
    "s_cselect_b32 s22, s22, s18\n"
    "s_cselect_b64 s[40:41], s[40:41], s[8:9]\n"
    "s_cselect_b32 s60, s60, 24576\n"
    "s_cselect_b32 s61, s61, 28672\n"
    "s_cselect_b32 s62, s62, 32768\n"
    "s_mov_b32 s66, 1\n"
    "s_add_u32 s68, s68, 24576\n"
    "s_add_u32 s81, s81, 128\n"
    "s_add_u32 s80, s80, 256\n"
    "s_add_u32 s71, s71, s44\n"
    "s_add_u32 s52, s52, s45\n"
    "s_add_u32 s75, s71, s63\n"
    "s_branch .LhVP\n"
    "s_branch .Lf6e\n"
    ".Lf6b:\n"
    "s_add_u32 s75, s71, s63\n"
    "buffer_load_dwordx4 v[204:207], v242, s[40:43], s75 offen offset:0 nt\n"
    "buffer_load_dwordx4 v[208:211], v242, s[40:43], s75 offen offset:768 nt\n"
    "buffer_load_dwordx4 v[212:215], v242, s[40:43], s75 offen offset:256 nt\n"
    "buffer_load_dwordx4 v[216:219], v242, s[40:43], s75 offen offset:1024 nt\n"
    "buffer_load_dwordx4 v[220:223], v242, s[40:43], s75 offen offset:512 nt\n"
    "buffer_load_dwordx4 v[224:227], v242, s[40:43], s75 offen offset:1280 nt\n"
    "s_lshl_b32 s73, s53, 1\n"
    "s_add_u32 s73, s73, s79\n"
    "s_mul_i32 s74, s73, 6144\n"
    "s_add_u32 s74, s74, 1425408\n"
    "s_lshl_b32 s73, s73, 8\n"
    "s_add_u32 s73, s73, 1557504\n"
    "s_mov_b64 s[20:21], s[16:17]\n"
    "s_mov_b32 s44, 98304\n"
    "s_mov_b32 s22, 1572864\n"
    "s_sub_u32 s72, s42, 1572864\n"
    "s_sub_u32 s45, s42, 49152\n"
    "s_bitcmp1_b32 s84, 0\n"
    "s_cselect_b32 s52, s71, s73\n"
    "s_cselect_b32 s71, s71, s74\n"
    "s_cselect_b32 s63, s63, s72\n"
    "s_cselect_b32 s42, s45, s42\n"
    "s_cselect_b32 s45, s44, 12288\n"
    "s_cselect_b32 s44, s44, 24576\n"
    "s_cselect_b32 s22, s22, s18\n"
    "s_cselect_b64 s[40:41], s[40:41], s[8:9]\n"
    "s_cselect_b32 s60, s60, 24576\n"
    "s_cselect_b32 s61, s61, 28672\n"
    "s_cselect_b32 s62, s62, 32768\n"
    "s_mov_b32 s66, 1\n"
    "s_add_u32 s68, s68, 24576\n"
    "s_add_u32 s81, s81, 128\n"
    "s_branch .LhVN\n"
    ".Lf6e:\n"
    "s_nop 4\n"
    ".Lf7e:\n"
    "s_nop 4\n"
    ".fill ((0x20000 - (. - _Z8attn_fwdPKtPKiS2_S0_S0_S0_S0_PcPt)) & 4095) / 4, 4, 0xBF800000\n"
    ".LhKP:\n"
    "s_waitcnt vmcnt(12)\n"
    "ds_read_b128 v[52:55], v251 offset:0\n"
    "ds_read_b128 v[192:195], v97 offset:49280\n"
    "ds_read_b128 v[196:199], v96 offset:49408\n"
    "ds_read_b128 v[200:203], v97 offset:49408\n"
    "s_add_u32 s83, s67, 4096\n"
    "ds_read_b128 v[188:191], v96 offset:49280\n"
    "ds_read_b128 v[180:183], v96 offset:49152\n"
    "s_nop 0\n"
    "ds_read_b128 v[184:187], v97 offset:49152\n"
    "s_waitcnt lgkmcnt(6)\n"
    "v_smfmac_f32_16x16x64_bf16 v[108:111], v[52:55], v[68:75], v253\n"
    "s_waitcnt lgkmcnt(3)\n"
    "v_pk_maximum3_f16 v98, v200, v201, v98\n"
    "v_pk_maximum3_f16 v98, v202, v203, v98\n"
    "v_pk_maximum3_f16 v98, v196, v197, v98\n"
    "v_pk_add_u16 v203, s89, v203 clamp\n"
    "v_pk_maximum3_f16 v98, v198, v199, v98\n"
    "v_pk_add_u16 v200, s89, v200 clamp\n"
    "v_pk_add_u16 v202, s89, v202 clamp\n"
    "v_pk_add_u16 v201, s89, v201 clamp\n"
    "v_pk_maximum3_f16 v98, v192, v193, v98\n"
    "v_pk_maximum3_f16 v98, v194, v195, v98\n"
    "v_mfma_f32_16x16x32_bf16 v[16:19], a[132:135], v[200:203], v[16:19]\n"
    "s_add_u32 m0, s68, .Lend-.Lend2+(0)\n"
    "v_pk_add_u16 v199, s89, v199 clamp\n"
    "v_pk_add_u16 v197, s89, v197 clamp\n"
    "v_mfma_f32_16x16x32_bf16 v[64:67], a[44:47], v[200:203], v[64:67]\n"
    "buffer_load_dwordx4 v1, s[16:19], s67 offen offset:0 sc1 lds\n"
    "v_pk_add_u16 v198, s89, v198 clamp\n"
    "v_mfma_f32_16x16x32_bf16 v[32:35], a[156:159], v[200:203], v[32:35]\n"
    "v_pk_add_u16 v196, s89, v196 clamp\n"
    "v_accvgpr_write_b32 a191, v203\n"
    "v_mfma_f32_16x16x32_bf16 v[48:51], a[20:23], v[200:203], v[48:51]\n"
    "s_waitcnt lgkmcnt(2)\n"
    "v_pk_maximum3_f16 v98, v188, v189, v98\n"
    "v_pk_add_u16 v195, s89, v195 clamp\n"
    "v_mfma_f32_16x16x32_bf16 v[64:67], a[40:43], v[196:199], v[64:67]\n"
    "v_pk_add_u16 v192, s89, v192 clamp\n"
    "v_pk_add_u16 v193, s89, v193 clamp\n"
    "v_mfma_f32_16x16x32_bf16 v[32:35], a[152:155], v[196:199], v[32:35]\n"
    "v_pk_maximum3_f16 v98, v190, v191, v98\n"
    "v_pk_add_u16 v194, s89, v194 clamp\n"
    "v_smfmac_f32_16x16x64_bf16 v[116:119], v[52:55], v[84:91], v253\n"
    "v_pk_add_u16 v191, s89, v191 clamp\n"
    "v_mfma_f32_16x16x32_bf16 v[48:51], a[16:19], v[196:199], v[48:51]\n"
    "buffer_load_dwordx4 v1, s[16:19], s67 offen offset:1024 sc1 lds\n"
    "v_mfma_f32_16x16x32_bf16 v[16:19], a[128:131], v[196:199], v[16:19]\n"
    "s_waitcnt lgkmcnt(0)\n"
    "v_pk_maximum3_f16 v98, v184, v185, v98\n"
    "v_pk_add_u16 v189, s89, v189 clamp\n"
    "v_mfma_f32_16x16x32_bf16 v[48:51], a[12:15], v[192:195], v[48:51]\n"
    "v_pk_add_u16 v190, s89, v190 clamp\n"
    "v_pk_add_u16 v184, s89, v184 clamp\n"
    "v_accvgpr_write_b32 a188, v200\n"
    "v_mfma_f32_16x16x32_bf16 v[64:67], a[36:39], v[192:195], v[64:67]\n"
    "v_pk_maximum3_f16 v98, v186, v187, v98\n"
    "v_pk_add_u16 v188, s89, v188 clamp\n"
    "v_mfma_f32_16x16x32_bf16 v[32:35], a[148:151], v[192:195], v[32:35]\n"
    "v_pk_add_u16 v187, s89, v187 clamp\n"
    "v_pk_maximum3_f16 v98, v180, v181, v98\n"
    "v_mfma_f32_16x16x32_bf16 v[32:35], a[144:147], v[188:191], v[32:35]\n"
    "v_pk_maximum3_f16 v98, v182, v183, v98\n"
    "v_pk_add_u16 v185, s89, v185 clamp\n"
    "v_mfma_f32_16x16x32_bf16 v[16:19], a[124:127], v[192:195], v[16:19]\n"
    "buffer_load_dwordx4 v1, s[16:19], s67 offen offset:2048 sc1 lds\n"
    "v_mfma_f32_16x16x32_bf16 v[16:19], a[120:123], v[188:191], v[16:19]\n"
    "s_waitcnt vmcnt(9)\n"
    "v_pk_add_u16 v186, s89, v186 clamp\n"
    "v_pk_add_u16 v181, s89, v181 clamp\n"
    "v_mfma_f32_16x16x32_bf16 v[64:67], a[32:35], v[188:191], v[64:67]\n"
    "s_barrier\n"
    "ds_read_b128 a[48:51], v252 offset:24576\n"
    "v_mfma_f32_16x16x32_bf16 v[48:51], a[8:11], v[188:191], v[48:51]\n"
    "ds_read_b128 a[72:75], v252 offset:25600\n"
    "ds_read_b128 a[92:95], v252 offset:35840\n"
    "v_mfma_f32_16x16x32_bf16 v[32:35], a[140:143], v[184:187], v[32:35]\n"
    "v_pk_add_u16 v183, s89, v183 clamp\n"
    "v_pk_add_u16 v180, s89, v180 clamp\n"
    "v_pk_add_u16 v182, s89, v182 clamp\n"
    "v_mfma_f32_16x16x32_bf16 v[16:19], a[116:119], v[184:187], v[16:19]\n"
    "ds_read_b128 a[84:87], v252 offset:31744\n"
    "ds_read_b128 a[52:55], v252 offset:26624\n"
    "v_mfma_f32_16x16x32_bf16 v[32:35], a[136:139], v[180:183], v[32:35]\n"
    "buffer_load_dwordx4 v1, s[16:19], s67 offen offset:3072 sc1 lds\n"
    "v_mfma_f32_16x16x32_bf16 v[48:51], a[4:7], v[184:187], v[48:51]\n"
    "ds_read_b128 a[88:91], v252 offset:33792\n"
    "ds_read_b128 a[56:59], v252 offset:28672\n"
    "v_mfma_f32_16x16x32_bf16 v[64:67], a[28:31], v[184:187], v[64:67]\n"
    "v_accvgpr_write_b32 a170, v182\n"
    "v_accvgpr_write_b32 a177, v189\n"
    "v_mfma_f32_16x16x32_bf16 v[16:19], a[112:115], v[180:183], v[16:19]\n"
    "s_add_u32 m0, s68, 4096\n"
    "ds_read_b128 a[76:79], v252 offset:27648\n"
    "ds_read_b128 a[68:71], v252 offset:34816\n"
    "ds_read_b128 a[60:63], v252 offset:30720\n"
    "v_mfma_f32_16x16x32_bf16 v[48:51], a[0:3], v[180:183], v[48:51]\n"
    "s_waitcnt lgkmcnt(9)\n"
    "ds_read_b128 a[80:83], v252 offset:29696\n"
    "v_accvgpr_write_b32 a179, v191\n"
    "v_mfma_f32_16x16x32_bf16 v[4:7], a[48:51], a[192:195], v[100:103]\n"
    "s_waitcnt lgkmcnt(8)\n"
    "v_pk_add_u16 v98, s89, v98\n"
    "v_accvgpr_write_b32 a174, v186\n"
    "v_mfma_f32_16x16x32_bf16 v[24:27], a[92:95], a[236:239], v[104:107]\n"
    "buffer_load_dwordx4 v1, s[16:19], s83 offen offset:0 sc1 lds\n"
    "v_mfma_f32_16x16x32_bf16 v[12:15], a[48:51], a[240:243], v[100:103]\n"
    "v_accvgpr_write_b32 a186, v198\n"
    "v_accvgpr_write_b32 a180, v192\n"
    "v_mfma_f32_16x16x32_bf16 v[20:23], a[72:75], a[192:195], v[104:107]\n"
    "v_accvgpr_write_b32 a181, v193\n"
    "v_accvgpr_write_b32 a172, v184\n"
    "v_mfma_f32_16x16x32_bf16 v[28:31], a[72:75], a[240:243], v[104:107]\n"
    "s_waitcnt lgkmcnt(6)\n"
    "v_exp_f32 v33, v33\n"
    "v_mfma_f32_16x16x32_bf16 v[4:7], a[52:55], a[196:199], v[4:7]\n"
    "ds_read_b128 a[64:67], v252 offset:32768\n"
    "v_accvgpr_write_b32 a184, v196\n"
    "v_mfma_f32_16x16x32_bf16 v[12:15], a[52:55], a[244:247], v[12:15]\n"
    "s_waitcnt lgkmcnt(6)\n"
    "v_accvgpr_write_b32 a168, v180\n"
    "v_accvgpr_write_b32 a182, v194\n"
    "v_mfma_f32_16x16x32_bf16 v[24:27], a[88:91], a[232:235], v[24:27]\n"
    "s_waitcnt lgkmcnt(3)\n"
    "v_accvgpr_write_b32 a183, v195\n"
    "v_accvgpr_write_b32 a190, v202\n"
    "v_mfma_f32_16x16x32_bf16 v[8:11], a[68:71], a[236:239], v[100:103]\n"
    "buffer_load_dwordx4 v1, s[16:19], s83 offen offset:1024 sc1 lds\n"
    "v_mfma_f32_16x16x32_bf16 v[4:7], a[56:59], a[200:203], v[4:7]\n"
    "v_accvgpr_write_b32 a173, v185\n"
    "v_accvgpr_write_b32 a171, v183\n"
    "v_mfma_f32_16x16x32_bf16 v[12:15], a[56:59], a[248:251], v[12:15]\n"
    "s_add_u32 m0, s64, 0\n"
    "v_accvgpr_write_b32 a176, v188\n"
    "v_accvgpr_write_b32 a189, v201\n"
    "v_accvgpr_write_b32 a169, v181\n"
    "v_mfma_f32_16x16x32_bf16 v[64:67], a[24:27], v[180:183], v[64:67]\n"
    "v_accvgpr_write_b32 a175, v187\n"
    "v_and_b32 v98, 0x80008000, v98\n"
    "v_mfma_f32_16x16x32_bf16 v[20:23], a[76:79], a[196:199], v[20:23]\n"
    "s_waitcnt lgkmcnt(0)\n"
    "v_cmp_ne_u32 s[34:35], 0, v98\n"
    "v_accvgpr_write_b32 a187, v199\n"
    "v_mfma_f32_16x16x32_bf16 v[8:11], a[64:67], a[232:235], v[8:11]\n"
    "s_add_u32 s83, s67, 24576\n"
    "v_accvgpr_write_b32 a185, v197\n"
    "v_accvgpr_write_b32 a178, v190\n"
    "v_mfma_f32_16x16x32_bf16 v[24:27], a[84:87], a[228:231], v[24:27]\n"
    "buffer_load_dwordx4 v1, s[16:19], s83 offen offset:0 sc1 lds\n"
    "v_mfma_f32_16x16x32_bf16 v[12:15], a[60:63], a[252:255], v[12:15]\n"
    "v_exp_f32 v32, v32\n"
    "v_mfma_f32_16x16x32_bf16 v[4:7], a[60:63], a[204:207], v[4:7]\n"
    "v_exp_f32 v18, v18\n"
    "v_mfma_f32_16x16x32_bf16 v[12:15], a[64:67], a[160:163], v[12:15]\n"
    "v_exp_f32 v48, v48\n"
    "v_mfma_f32_16x16x32_bf16 v[4:7], a[64:67], a[208:211], v[4:7]\n"
    "v_exp_f32 v19, v19\n"
    "v_mfma_f32_16x16x32_bf16 v[4:7], a[68:71], a[212:215], v[4:7]\n"
    "v_cvt_pk_bf16_f32 v93, v18, v19\n"
    "v_exp_f32 v17, v17\n"
    "v_mfma_f32_16x16x32_bf16 v[28:31], a[76:79], a[244:247], v[28:31]\n"
    "buffer_load_dwordx4 v1, s[16:19], s83 offen offset:1024 sc1 lds\n"
    "v_mfma_f32_16x16x32_bf16 v[24:27], a[80:83], a[224:227], v[24:27]\n"
    "v_exp_f32 v35, v35\n"
    "v_mfma_f32_16x16x32_bf16 v[20:23], a[80:83], a[200:203], v[20:23]\n"
    "v_exp_f32 v16, v16\n"
    "v_mfma_f32_16x16x32_bf16 v[8:11], a[60:63], a[228:231], v[8:11]\n"
    "v_exp_f32 v34, v34\n"
    "v_mfma_f32_16x16x32_bf16 v[20:23], a[84:87], a[204:207], v[20:23]\n"
    "v_cvt_pk_bf16_f32 v95, v34, v35\n"
    "v_mfma_f32_16x16x32_bf16 v[8:11], a[56:59], a[224:227], v[8:11]\n"
    "v_exp_f32 v66, v66\n"
    "v_mfma_f32_16x16x32_bf16 v[24:27], a[76:79], a[220:223], v[24:27]\n"
    "v_cvt_pk_bf16_f32 v94, v32, v33\n"
    "v_mfma_f32_16x16x32_bf16 v[8:11], a[52:55], a[220:223], v[8:11]\n"
    "buffer_load_dwordx4 v1, s[16:19], s83 offen offset:2048 sc1 lds\n"
    "v_mfma_f32_16x16x32_bf16 v[32:35], a[92:95], a[188:191], v[104:107]\n"
    "v_exp_f32 v49, v49\n"
    "v_mfma_f32_16x16x32_bf16 v[12:15], a[68:71], a[164:167], v[12:15]\n"
    "v_exp_f32 v67, v67\n"
    "v_mfma_f32_16x16x32_bf16 v[32:35], a[88:91], a[184:187], v[32:35]\n"
    "s_waitcnt vmcnt(9)\n"
    "s_barrier\n"
    "ds_read_b128 a[116:119], v248 offset:12288\n"
    "v_mfma_f32_16x16x32_bf16 v[8:11], a[48:51], a[216:219], v[8:11]\n"
    "ds_read_b128 a[144:147], v248 offset:4096\n"
    "v_cvt_pk_bf16_f32 v99, v66, v67\n"
    "v_mfma_f32_16x16x32_bf16 v[32:35], a[84:87], a[180:183], v[32:35]\n"
    "ds_read_b128 a[124:127], v248 offset:13312\n"
    "v_cvt_pk_bf16_f32 v92, v16, v17\n"
    "v_mfma_f32_16x16x32_bf16 v[16:19], a[68:71], a[188:191], v[100:103]\n"
    "buffer_load_dwordx4 v1, s[16:19], s83 offen offset:3072 sc1 lds\n"
    "v_mfma_f32_16x16x32_bf16 v[20:23], a[88:91], a[208:211], v[20:23]\n"
    "s_add_u32 s83, s67, 28672\n"
    "ds_read_b128 a[136:139], v248 offset:3072\n"
    "ds_read_b128 a[140:143], v248 offset:15360\n"
    "v_mfma_f32_16x16x32_bf16 v[32:35], a[80:83], a[176:179], v[32:35]\n"
    "ds_read_b128 a[120:123], v248 offset:1024\n"
    "ds_read_b128 a[148:151], v248 offset:16384\n"
    "v_mfma_f32_16x16x32_bf16 v[28:31], a[80:83], a[248:251], v[28:31]\n"
    "v_exp_f32 v64, v64\n"
    "v_mfma_f32_16x16x32_bf16 v[28:31], a[84:87], a[252:255], v[28:31]\n"
    "s_add_u32 m0, s64, 4096\n"
    "v_exp_f32 v50, v50\n"
    "v_mfma_f32_16x16x32_bf16 v[16:19], a[64:67], a[184:187], v[16:19]\n"
    "v_cvt_pk_bf16_f32 v96, v48, v49\n"
    "v_mfma_f32_16x16x32_bf16 v[16:19], a[60:63], a[180:183], v[16:19]\n"
    "ds_read_b128 a[152:155], v248 offset:5120\n"
    "ds_read_b128 a[132:135], v248 offset:14336\n"
    "v_mfma_f32_16x16x32_bf16 v[32:35], a[76:79], a[172:175], v[32:35]\n"
    "s_waitcnt lgkmcnt(7)\n"
    "buffer_load_dwordx4 v1, s[16:19], s83 offen offset:0 sc1 lds\n"
    "v_mfma_f32_16x16x32_bf16 a[32:35], a[144:147], v[92:95], 0\n"
    "ds_read_b128 a[128:131], v248 offset:2048\n"
    "v_smfmac_f32_16x16x64_bf16 v[112:115], v[52:55], v[76:83], v253\n"
    "v_mfma_f32_16x16x32_bf16 v[24:27], a[72:75], a[216:219], v[24:27]\n"
    "s_waitcnt lgkmcnt(5)\n"
    "v_exp_f32 v65, v65\n"
    "v_mfma_f32_16x16x32_bf16 v[152:155], a[140:143], v[80:83], 0\n"
    "s_waitcnt lgkmcnt(4)\n"
    "v_mfma_f32_16x16x32_bf16 v[144:147], a[120:123], v[76:79], 0\n"
    "v_exp_f32 v51, v51\n"
    "v_mfma_f32_16x16x32_bf16 v[16:19], a[56:59], a[176:179], v[16:19]\n"
    "s_waitcnt lgkmcnt(3)\n"
    "buffer_load_dwordx4 v1, s[16:19], s83 offen offset:1024 sc1 lds\n"
    "v_mfma_f32_16x16x32_bf16 v[188:191], a[148:151], v[72:75], 0\n"
    "s_waitcnt lgkmcnt(1)\n"
    "v_cvt_pk_bf16_f32 v97, v50, v51\n"
    "v_cvt_pk_bf16_f32 v98, v64, v65\n"
    "v_mfma_f32_16x16x32_bf16 v[148:151], a[132:135], v[80:83], 0\n"
    "s_waitcnt lgkmcnt(0)\n"
    "ds_read_b128 a[112:115], v248 offset:0\n"
    "v_mfma_f32_16x16x32_bf16 v[148:151], a[128:131], v[76:79], v[148:151]\n"
    "v_exp_f32 v4, v4\n"
    "v_mfma_f32_16x16x32_bf16 v[180:183], a[132:135], v[96:99], 0\n"
    "v_mfma_f32_16x16x32_bf16 v[176:179], a[120:123], v[92:95], 0\n"
    "v_exp_f32 v14, v14\n"
    "v_mfma_f32_16x16x32_bf16 v[28:31], a[88:91], a[160:163], v[28:31]\n"
    "v_exp_f32 v10, v10\n"
    "v_mfma_f32_16x16x32_bf16 v[164:167], a[128:131], v[84:87], 0\n"
    "buffer_load_dwordx2 v[204:205], v242, s[28:31], s75 offen offset:0 nt\n"
    "v_mfma_f32_16x16x32_bf16 v[176:179], a[124:127], v[96:99], v[176:179]\n"
    "v_mfma_f32_16x16x32_bf16 v[20:23], a[92:95], a[212:215], v[20:23]\n"
    "buffer_load_dwordx2 v[206:207], v242, s[28:31], s75 offen offset:768 nt\n"
    "v_mfma_f32_16x16x32_bf16 v[16:19], a[52:55], a[172:175], v[16:19]\n"
    "v_mfma_f32_16x16x32_bf16 v[132:135], a[128:131], v[68:71], 0\n"
    "buffer_load_dwordx2 v[208:209], v242, s[28:31], s75 offen offset:3072 nt\n"
    "v_mfma_f32_16x16x32_bf16 v[136:139], a[136:139], v[68:71], 0\n"
    "v_mfma_f32_16x16x32_bf16 v[16:19], a[48:51], a[168:171], v[16:19]\n"
    "buffer_load_dwordx2 v[210:211], v242, s[28:31], s75 offen offset:3840 nt\n"
    "v_mfma_f32_16x16x32_bf16 v[180:183], a[128:131], v[92:95], v[180:183]\n"
    "v_mfma_f32_16x16x32_bf16 v[184:187], a[140:143], v[96:99], 0\n"
    "s_waitcnt lgkmcnt(0)\n"
    "buffer_load_dwordx2 v[212:213], v242, s[28:31], s76 offen offset:0 nt\n"
    "v_mfma_f32_16x16x32_bf16 v[156:159], a[112:115], v[84:87], 0\n"
    "v_mfma_f32_16x16x32_bf16 a[16:19], a[148:151], v[88:91], 0\n"
    "buffer_load_dwordx2 v[214:215], v242, s[28:31], s76 offen offset:768 nt\n"
    "v_mfma_f32_16x16x32_bf16 v[168:171], a[136:139], v[84:87], 0\n"
    "v_exp_f32 v22, v22\n"
    "v_mfma_f32_16x16x32_bf16 v[152:155], a[136:139], v[76:79], v[152:155]\n"
    "buffer_load_dwordx2 v[216:217], v242, s[28:31], s76 offen offset:3072 nt\n"
    "v_mfma_f32_16x16x32_bf16 v[160:163], a[124:127], v[88:91], 0\n"
    "v_mfma_f32_16x16x32_bf16 v[140:143], a[116:119], v[80:83], 0\n"
    "buffer_load_dwordx2 v[218:219], v242, s[28:31], s76 offen offset:3840 nt\n"
    "v_mfma_f32_16x16x32_bf16 v[132:135], a[132:135], v[72:75], v[132:135]\n"
    "ds_read_b128 v[100:103], v250 offset:896\n"
    "ds_read_b128 a[128:131], v248 offset:8192\n"
    "v_mfma_f32_16x16x32_bf16 a[16:19], a[144:147], v[84:87], a[16:19]\n"
    "buffer_load_dwordx2 v[220:221], v242, s[28:31], s77 offen offset:0 nt\n"
    "v_mfma_f32_16x16x32_bf16 v[140:143], a[112:115], v[76:79], v[140:143]\n"
    "v_mfma_f32_16x16x32_bf16 v[128:131], a[124:127], v[72:75], 0\n"
    "buffer_load_dwordx2 v[222:223], v242, s[28:31], s77 offen offset:768 nt\n"
    "v_mfma_f32_16x16x32_bf16 a[0:3], a[144:147], v[76:79], 0\n"
    "v_exp_f32 v8, v8\n"
    "v_mfma_f32_16x16x32_bf16 a[20:23], a[152:155], v[84:87], 0\n"
    "buffer_load_dwordx2 v[224:225], v242, s[28:31], s77 offen offset:3072 nt\n"
    "v_mfma_f32_16x16x32_bf16 v[188:191], a[144:147], v[68:71], v[188:191]\n"
    "v_mfma_f32_16x16x32_bf16 v[144:147], a[124:127], v[80:83], v[144:147]\n"
    "buffer_load_dwordx2 v[226:227], v242, s[28:31], s77 offen offset:3840 nt\n"
    "v_mfma_f32_16x16x32_bf16 a[0:3], a[148:151], v[80:83], a[0:3]\n"
    "v_exp_f32 v15, v15\n"
    "v_mfma_f32_16x16x32_bf16 v[172:175], a[116:119], v[96:99], 0\n"
    "s_waitcnt lgkmcnt(0)\n"
    "buffer_load_dwordx2 v[228:229], v242, s[28:31], s78 offen offset:0 nt\n"
    "v_mfma_f32_16x16x32_bf16 a[48:51], a[128:131], v[68:71], 0\n"
    "ds_read_b128 a[156:159], v248 offset:17408\n"
    "v_mfma_f32_16x16x32_bf16 v[164:167], a[132:135], v[88:91], v[164:167]\n"
    "buffer_load_dwordx2 v[230:231], v242, s[28:31], s78 offen offset:768 nt\n"
    "v_mfma_f32_16x16x32_bf16 v[124:127], a[112:115], v[68:71], 0\n"
    "v_mfma_f32_16x16x32_bf16 v[172:175], a[112:115], v[92:95], v[172:175]\n"
    "buffer_load_dwordx2 v[232:233], v242, s[28:31], s78 offen offset:3072 nt\n"
    "v_mfma_f32_16x16x32_bf16 a[32:35], a[148:151], v[96:99], a[32:35]\n"
    "v_mfma_f32_16x16x32_bf16 v[128:131], a[120:123], v[68:71], v[128:131]\n"
    "buffer_load_dwordx2 v[234:235], v242, s[28:31], s78 offen offset:3840 nt\n"
    "v_mfma_f32_16x16x32_bf16 v[192:195], a[152:155], v[68:71], 0\n"
    "ds_read_b128 a[132:135], v248 offset:20480\n"
    "ds_read_b128 a[112:115], v248 offset:6144\n"
    "v_mfma_f32_16x16x32_bf16 v[160:163], a[120:123], v[84:87], v[160:163]\n"
    "s_waitcnt lgkmcnt(2)\n"
    "v_mfma_f32_16x16x32_bf16 a[36:39], a[156:159], v[96:99], 0\n"
    "ds_read_b128 a[144:147], v248 offset:10240\n"
    "ds_read_b128 a[124:127], v248 offset:19456\n"
    "v_mfma_f32_16x16x32_bf16 v[32:35], a[72:75], a[168:171], v[32:35]\n"
    "v_exp_f32 v7, v7\n"
    "v_mfma_f32_16x16x32_bf16 v[192:195], a[156:159], v[72:75], v[192:195]\n"
    "v_exp_f32 v21, v21\n"
    "v_mfma_f32_16x16x32_bf16 v[184:187], a[136:139], v[92:95], v[184:187]\n"
    "ds_read_b128 a[120:123], v248 offset:7168\n"
    "v_mfma_f32_16x16x32_bf16 v[28:31], a[92:95], a[164:167], v[28:31]\n"
    "v_mfma_f32_16x16x32_bf16 v[168:171], a[140:143], v[88:91], v[168:171]\n"
    "ds_read_b128 a[148:151], v248 offset:22528\n"
    "ds_read_b128 a[136:139], v248 offset:9216\n"
    "v_mfma_f32_16x16x32_bf16 a[4:7], a[156:159], v[80:83], 0\n"
    "v_exp_f32 v33, v33\n"
    "v_mfma_f32_16x16x32_bf16 a[36:39], a[152:155], v[92:95], a[36:39]\n"
    "v_mfma_f32_16x16x32_bf16 v[124:127], a[116:119], v[72:75], v[124:127]\n"
    "v_exp_f32 v16, v16\n"
    "v_mfma_f32_16x16x32_bf16 a[4:7], a[152:155], v[76:79], a[4:7]\n"
    "v_exp_f32 v5, v5\n"
    "v_mfma_f32_16x16x32_bf16 v[136:139], a[140:143], v[72:75], v[136:139]\n"
    "ds_read_b128 a[140:143], v248 offset:21504\n"
    "ds_read_b128 a[152:155], v248 offset:11264\n"
    "v_mfma_f32_16x16x32_bf16 v[156:159], a[116:119], v[88:91], v[156:159]\n"
    "v_smfmac_f32_16x16x64_bf16 v[120:123], v[52:55], v[92:99], v253\n"
    "s_waitcnt lgkmcnt(8)\n"
    "v_mfma_f32_16x16x32_bf16 a[64:67], a[132:135], v[80:83], 0\n"
    "s_waitcnt lgkmcnt(6)\n"
    "v_mfma_f32_16x16x32_bf16 a[104:107], a[144:147], v[92:95], 0\n"
    "s_waitcnt lgkmcnt(2)\n"
    "v_exp_f32 v20, v20\n"
    "v_mfma_f32_16x16x32_bf16 a[52:55], a[136:139], v[68:71], 0\n"
    "ds_read_b128 a[116:119], v248 offset:18432\n"
    "v_mfma_f32_16x16x32_bf16 a[20:23], a[156:159], v[88:91], a[20:23]\n"
    "ds_read_b128 a[156:159], v248 offset:23552\n"
    "v_mfma_f32_16x16x32_bf16 v[196:199], a[112:115], v[68:71], 0\n"
    "buffer_load_dwordx2 v[236:237], v241, s[36:39], s81 offen offset:0 sc1\n"
    "v_mfma_f32_16x16x32_bf16 v[200:203], a[124:127], v[72:75], 0\n"
    "s_waitcnt lgkmcnt(2)\n"
    "v_exp_f32 v27, v27\n"
    "v_mfma_f32_16x16x32_bf16 a[60:63], a[152:155], v[68:71], 0\n"
    "v_mfma_f32_16x16x32_bf16 a[96:99], a[132:135], v[96:99], 0\n"
    "v_exp_f32 v17, v17\n"
    "v_mfma_f32_16x16x32_bf16 a[72:75], a[144:147], v[76:79], 0\n"
    "v_exp_f32 v24, v24\n"
    "v_mfma_f32_16x16x32_bf16 a[24:27], a[112:115], v[84:87], 0\n"
    "v_exp_f32 v35, v35\n"
    "v_mfma_f32_16x16x32_bf16 a[64:67], a[128:131], v[76:79], a[64:67]\n"
    "v_mfma_f32_16x16x32_bf16 a[80:83], a[128:131], v[84:87], 0\n"
    "s_add_u32 s67, s67, 49152\n"
    "v_exp_f32 v32, v32\n"
    "v_mfma_f32_16x16x32_bf16 a[12:15], a[120:123], v[76:79], 0\n"
    "v_mfma_f32_16x16x32_bf16 a[104:107], a[148:151], v[96:99], a[104:107]\n"
    "v_mov_b32 v2, v3\n"
    "v_mfma_f32_16x16x32_bf16 a[28:31], a[124:127], v[88:91], 0\n"
    "v_mfma_f32_16x16x32_bf16 a[28:31], a[120:123], v[84:87], a[28:31]\n"
    "v_mfma_f32_16x16x32_bf16 a[92:95], a[152:155], v[84:87], 0\n"
    "v_mfma_f32_16x16x32_bf16 v[200:203], a[120:123], v[68:71], v[200:203]\n"
    "s_waitcnt lgkmcnt(0)\n"
    "v_exp_f32 v12, v12\n"
    "v_mfma_f32_16x16x32_bf16 a[108:111], a[156:159], v[96:99], 0\n"
    "v_exp_f32 v18, v18\n"
    "v_mfma_f32_16x16x32_bf16 a[80:83], a[132:135], v[88:91], a[80:83]\n"
    "v_mfma_f32_16x16x32_bf16 a[44:47], a[120:123], v[92:95], 0\n"
    "v_exp_f32 v29, v29\n"
    "v_mfma_f32_16x16x32_bf16 a[12:15], a[124:127], v[80:83], a[12:15]\n"
    "ds_read_b128 a[120:123], v3 offset:16384\n"
    "v_mfma_f32_16x16x32_bf16 a[44:47], a[124:127], v[96:99], a[44:47]\n"
    "v_mfma_f32_16x16x32_bf16 a[56:59], a[148:151], v[72:75], 0\n"
    "v_exp_f32 v28, v28\n"
    "v_mfma_f32_16x16x32_bf16 a[96:99], a[128:131], v[92:95], a[96:99]\n"
    "v_exp_f32 v19, v19\n"
    "v_mfma_f32_16x16x32_bf16 a[40:43], a[116:119], v[96:99], 0\n"
    "v_exp_f32 v31, v31\n"
    "v_mfma_f32_16x16x32_bf16 a[60:63], a[156:159], v[72:75], a[60:63]\n"
    "v_exp_f32 v26, v26\n"
    "v_mfma_f32_16x16x32_bf16 a[72:75], a[148:151], v[80:83], a[72:75]\n"
    "v_mfma_f32_16x16x32_bf16 a[8:11], a[116:119], v[80:83], 0\n"
    "v_exp_f32 v6, v6\n"
    "v_mfma_f32_16x16x32_bf16 a[68:71], a[140:143], v[80:83], 0\n"
    "v_exp_f32 v25, v25\n"
    "v_mfma_f32_16x16x32_bf16 a[48:51], a[132:135], v[72:75], a[48:51]\n"
    "ds_read_b128 a[124:127], v3 offset:18432\n"
    "v_mfma_f32_16x16x32_bf16 a[40:43], a[112:115], v[92:95], a[40:43]\n"
    "ds_read_b128 a[132:135], v3 offset:22528\n"
    "v_mfma_f32_16x16x32_bf16 a[100:103], a[140:143], v[96:99], 0\n"
    "v_mfma_f32_16x16x32_bf16 v[196:199], a[116:119], v[72:75], v[196:199]\n"
    "v_exp_f32 v30, v30\n"
    "v_mfma_f32_16x16x32_bf16 a[108:111], a[152:155], v[92:95], a[108:111]\n"
    "v_mfma_f32_16x16x32_bf16 a[88:91], a[148:151], v[88:91], 0\n"
    "s_waitcnt vmcnt(17)\n"
    "v_exp_f32 v11, v11\n"
    "v_mfma_f32_16x16x32_bf16 a[8:11], a[112:115], v[76:79], a[8:11]\n"
    "ds_read_b128 a[128:131], v3 offset:20480\n"
    "ds_read_b128 a[112:115], v3 offset:12288\n"
    "v_mfma_f32_16x16x32_bf16 a[76:79], a[156:159], v[80:83], 0\n"
    "ds_add_u32 v247, v246\n"
    "v_mfma_f32_16x16x32_bf16 a[24:27], a[116:119], v[88:91], a[24:27]\n"
    "ds_read_b128 a[116:119], v3 offset:14336\n"
    "v_mfma_f32_16x16x32_bf16 a[56:59], a[144:147], v[68:71], a[56:59]\n"
    "v_exp_f32 v9, v9\n"
    "v_mfma_f32_16x16x32_bf16 a[84:87], a[136:139], v[84:87], 0\n"
    "v_exp_f32 v23, v23\n"
    "v_mfma_f32_16x16x32_bf16 a[88:91], a[144:147], v[84:87], a[88:91]\n"
    "v_add_u32 v3, 49152, v252\n"
    "v_exp_f32 v34, v34\n"
    "v_mfma_f32_16x16x32_bf16 a[84:87], a[140:143], v[88:91], a[84:87]\n"
    "v_mfma_f32_16x16x32_bf16 a[100:103], a[136:139], v[92:95], a[100:103]\n"
    "v_exp_f32 v13, v13\n"
    "v_mfma_f32_16x16x32_bf16 a[76:79], a[152:155], v[76:79], a[76:79]\n"
    "v_mfma_f32_16x16x32_bf16 a[68:71], a[136:139], v[76:79], a[68:71]\n"
    "s_waitcnt lgkmcnt(0)\n"
    "v_mfma_f32_16x16x32_bf16 a[92:95], a[156:159], v[88:91], a[92:95]\n"
    "v_mfma_f32_16x16x32_bf16 a[52:55], a[140:143], v[72:75], a[52:55]\n"
    "s_branch .LNK\n"
    ".fill ((0x20000 - (. - _Z8attn_fwdPKtPKiS2_S0_S0_S0_S0_PcPt)) & 4095) / 4, 4, 0xBF800000\n"
    ".LhKN:\n"
    "s_waitcnt vmcnt(28)\n"
    "ds_read_b128 v[52:55], v251 offset:768\n"
    "ds_read_b128 v[180:183], v96 offset:49152\n"
    "ds_read_b128 v[200:203], v97 offset:49408\n"
    "ds_read_b128 v[184:187], v97 offset:49152\n"
    "s_add_u32 m0, s68, 0\n"
    "ds_read_b128 v[196:199], v96 offset:49408\n"
    "ds_read_b128 v[188:191], v96 offset:49280\n"
    "ds_read_b128 v[192:195], v97 offset:49280\n"
    "s_waitcnt lgkmcnt(6)\n"
    "v_smfmac_f32_16x16x64_bf16 v[108:111], v[52:55], v[68:75], v253\n"
    "s_waitcnt lgkmcnt(4)\n"
    "v_pk_maximum3_f16 v98, v200, v201, v98\n"
    "v_pk_maximum3_f16 v98, v202, v203, v98\n"
    "v_pk_add_u16 v200, s89, v200 clamp\n"
    "v_pk_add_u16 v203, s89, v203 clamp\n"
    "v_smfmac_f32_16x16x64_bf16 v[116:119], v[52:55], v[84:91], v253\n"
    "s_waitcnt lgkmcnt(2)\n"
    "s_nop 0\n"
    "v_pk_add_u16 v202, s89, v202 clamp\n"
    "v_pk_add_u16 v201, s89, v201 clamp\n"
    "v_pk_maximum3_f16 v98, v196, v197, v98\n"
    "v_pk_maximum3_f16 v98, v198, v199, v98\n"
    "v_mfma_f32_16x16x32_bf16 v[64:67], a[44:47], v[200:203], v[64:67]\n"
    "v_pk_add_u16 v198, s89, v198 clamp\n"
    "v_pk_add_u16 v196, s89, v196 clamp\n"
    "v_mfma_f32_16x16x32_bf16 v[48:51], a[20:23], v[200:203], v[48:51]\n"
    "v_pk_add_u16 v199, s89, v199 clamp\n"
    "v_pk_add_u16 v197, s89, v197 clamp\n"
    "v_mfma_f32_16x16x32_bf16 v[32:35], a[156:159], v[200:203], v[32:35]\n"
    "s_waitcnt lgkmcnt(0)\n"
    "v_pk_maximum3_f16 v98, v192, v193, v98\n"
    "v_pk_maximum3_f16 v98, v194, v195, v98\n"
    "v_mfma_f32_16x16x32_bf16 v[64:67], a[40:43], v[196:199], v[64:67]\n"
    "v_pk_maximum3_f16 v98, v188, v189, v98\n"
    "v_pk_add_u16 v193, s89, v193 clamp\n"
    "v_mfma_f32_16x16x32_bf16 v[48:51], a[16:19], v[196:199], v[48:51]\n"
    "buffer_load_dwordx4 v1, s[16:19], s67 offen offset:0 sc1 lds\n"
    "v_mfma_f32_16x16x32_bf16 v[16:19], a[132:135], v[200:203], v[16:19]\n"
    "v_pk_add_u16 v194, s89, v194 clamp\n"
    "v_pk_maximum3_f16 v98, v190, v191, v98\n"
    "v_pk_add_u16 v192, s89, v192 clamp\n"
    "v_smfmac_f32_16x16x64_bf16 v[112:115], v[52:55], v[76:83], v253\n"
    "v_pk_add_u16 v191, s89, v191 clamp\n"
    "v_mfma_f32_16x16x32_bf16 v[16:19], a[128:131], v[196:199], v[16:19]\n"
    "v_pk_add_u16 v195, s89, v195 clamp\n"
    "v_pk_maximum3_f16 v98, v184, v185, v98\n"
    "v_mfma_f32_16x16x32_bf16 v[32:35], a[152:155], v[196:199], v[32:35]\n"
    "v_pk_add_u16 v185, s89, v185 clamp\n"
    "v_pk_add_u16 v188, s89, v188 clamp\n"
    "v_mfma_f32_16x16x32_bf16 v[64:67], a[36:39], v[192:195], v[64:67]\n"
    "v_pk_add_u16 v189, s89, v189 clamp\n"
    "v_pk_maximum3_f16 v98, v186, v187, v98\n"
    "v_mfma_f32_16x16x32_bf16 v[32:35], a[148:151], v[192:195], v[32:35]\n"
    "buffer_load_dwordx4 v1, s[16:19], s67 offen offset:1024 sc1 lds\n"
    "v_mfma_f32_16x16x32_bf16 v[16:19], a[124:127], v[192:195], v[16:19]\n"
    "v_pk_add_u16 v190, s89, v190 clamp\n"
    "v_pk_add_u16 v184, s89, v184 clamp\n"
    "v_pk_maximum3_f16 v98, v180, v181, v98\n"
    "v_mfma_f32_16x16x32_bf16 v[32:35], a[144:147], v[188:191], v[32:35]\n"
    "v_pk_add_u16 v187, s89, v187 clamp\n"
    "v_pk_add_u16 v186, s89, v186 clamp\n"
    "v_mfma_f32_16x16x32_bf16 v[48:51], a[12:15], v[192:195], v[48:51]\n"
    "s_waitcnt vmcnt(24)\n"
    "v_pk_maximum3_f16 v98, v182, v183, v98\n"
    "v_pk_add_u16 v182, s89, v182 clamp\n"
    "v_mfma_f32_16x16x32_bf16 v[48:51], a[8:11], v[188:191], v[48:51]\n"
    "s_barrier\n"
    "v_pk_add_u16 v180, s89, v180 clamp\n"
    "v_mfma_f32_16x16x32_bf16 v[64:67], a[32:35], v[188:191], v[64:67]\n"
    "ds_read_b128 a[72:75], v252 offset:25600\n"
    "v_pk_add_u16 v183, s89, v183 clamp\n"
    "v_mfma_f32_16x16x32_bf16 v[32:35], a[140:143], v[184:187], v[32:35]\n"
    "ds_read_b128 a[48:51], v252 offset:24576\n"
    "v_pk_add_u16 v181, s89, v181 clamp\n"
    "v_mfma_f32_16x16x32_bf16 v[16:19], a[120:123], v[188:191], v[16:19]\n"
    "buffer_load_dwordx4 v1, s[16:19], s67 offen offset:2048 sc1 lds\n"
    "v_mfma_f32_16x16x32_bf16 v[32:35], a[136:139], v[180:183], v[32:35]\n"
    "v_accvgpr_write_b32 a187, v199\n"
    "v_accvgpr_write_b32 a186, v198\n"
    "v_mfma_f32_16x16x32_bf16 v[64:67], a[28:31], v[184:187], v[64:67]\n"
    "ds_read_b128 a[92:95], v252 offset:35840\n"
    "ds_read_b128 a[64:67], v252 offset:32768\n"
    "v_mfma_f32_16x16x32_bf16 v[48:51], a[4:7], v[184:187], v[48:51]\n"
    "s_add_u32 s83, s67, 4096\n"
    "ds_read_b128 a[68:71], v252 offset:34816\n"
    "ds_read_b128 a[88:91], v252 offset:33792\n"
    "v_mfma_f32_16x16x32_bf16 v[16:19], a[116:119], v[184:187], v[16:19]\n"
    "v_accvgpr_write_b32 a178, v190\n"
    "v_accvgpr_write_b32 a173, v185\n"
    "v_mfma_f32_16x16x32_bf16 v[64:67], a[24:27], v[180:183], v[64:67]\n"
    "ds_read_b128 a[52:55], v252 offset:26624\n"
    "ds_read_b128 a[56:59], v252 offset:28672\n"
    "v_mfma_f32_16x16x32_bf16 v[48:51], a[0:3], v[180:183], v[48:51]\n"
    "ds_read_b128 a[84:87], v252 offset:31744\n"
    "v_accvgpr_write_b32 a177, v189\n"
    "v_mfma_f32_16x16x32_bf16 v[16:19], a[112:115], v[180:183], v[16:19]\n"
    "s_waitcnt lgkmcnt(8)\n"
    "buffer_load_dwordx4 v1, s[16:19], s67 offen offset:3072 sc1 lds\n"
    "v_mfma_f32_16x16x32_bf16 v[28:31], a[72:75], a[240:243], v[104:107]\n"
    "s_waitcnt lgkmcnt(7)\n"
    "ds_read_b128 a[60:63], v252 offset:30720\n"
    "v_accvgpr_write_b32 a170, v182\n"
    "v_mfma_f32_16x16x32_bf16 v[4:7], a[48:51], a[192:195], v[100:103]\n"
    "ds_read_b128 a[76:79], v252 offset:27648\n"
    "ds_read_b128 a[80:83], v252 offset:29696\n"
    "v_mfma_f32_16x16x32_bf16 v[12:15], a[48:51], a[240:243], v[100:103]\n"
    "s_waitcnt lgkmcnt(9)\n"
    "v_exp_f32 v16, v16\n"
    "v_mfma_f32_16x16x32_bf16 v[24:27], a[92:95], a[236:239], v[104:107]\n"
    "s_waitcnt lgkmcnt(6)\n"
    "v_accvgpr_write_b32 a183, v195\n"
    "v_accvgpr_write_b32 a190, v202\n"
    "v_mfma_f32_16x16x32_bf16 v[24:27], a[88:91], a[232:235], v[24:27]\n"
    "v_exp_f32 v17, v17\n"
    "v_mfma_f32_16x16x32_bf16 v[20:23], a[72:75], a[192:195], v[104:107]\n"
    "s_waitcnt lgkmcnt(5)\n"
    "v_accvgpr_write_b32 a172, v184\n"
    "v_accvgpr_write_b32 a168, v180\n"
    "v_mfma_f32_16x16x32_bf16 v[4:7], a[52:55], a[196:199], v[4:7]\n"
    "s_add_u32 m0, s68, 4096\n"
    "v_exp_f32 v34, v34\n"
    "v_mfma_f32_16x16x32_bf16 v[12:15], a[52:55], a[244:247], v[12:15]\n"
    "buffer_load_dwordx4 v1, s[16:19], s83 offen offset:0 sc1 lds\n"
    "v_mfma_f32_16x16x32_bf16 v[8:11], a[68:71], a[236:239], v[100:103]\n"
    "s_waitcnt lgkmcnt(4)\n"
    "v_accvgpr_write_b32 a182, v194\n"
    "v_accvgpr_write_b32 a189, v201\n"
    "v_mfma_f32_16x16x32_bf16 v[4:7], a[56:59], a[200:203], v[4:7]\n"
    "s_waitcnt lgkmcnt(2)\n"
    "v_accvgpr_write_b32 a175, v187\n"
    "v_accvgpr_write_b32 a180, v192\n"
    "v_mfma_f32_16x16x32_bf16 v[4:7], a[60:63], a[204:207], v[4:7]\n"
    "s_waitcnt lgkmcnt(1)\n"
    "v_accvgpr_write_b32 a176, v188\n"
    "v_pk_add_u16 v98, s89, v98\n"
    "v_mfma_f32_16x16x32_bf16 v[28:31], a[76:79], a[244:247], v[28:31]\n"
    "v_accvgpr_write_b32 a188, v200\n"
    "v_accvgpr_write_b32 a191, v203\n"
    "v_mfma_f32_16x16x32_bf16 v[20:23], a[76:79], a[196:199], v[20:23]\n"
    "s_waitcnt lgkmcnt(0)\n"
    "v_accvgpr_write_b32 a184, v196\n"
    "v_mfma_f32_16x16x32_bf16 v[20:23], a[80:83], a[200:203], v[20:23]\n"
    "v_accvgpr_write_b32 a181, v193\n"
    "v_mfma_f32_16x16x32_bf16 v[12:15], a[56:59], a[248:251], v[12:15]\n"
    "buffer_load_dwordx4 v1, s[16:19], s83 offen offset:1024 sc1 lds\n"
    "v_mfma_f32_16x16x32_bf16 v[20:23], a[84:87], a[204:207], v[20:23]\n"
    "v_accvgpr_write_b32 a171, v183\n"
    "v_accvgpr_write_b32 a185, v197\n"
    "v_mfma_f32_16x16x32_bf16 v[4:7], a[64:67], a[208:211], v[4:7]\n"
    "s_add_u32 m0, s70, 0\n"
    "v_accvgpr_write_b32 a179, v191\n"
    "v_accvgpr_write_b32 a169, v181\n"
    "v_mfma_f32_16x16x32_bf16 v[4:7], a[68:71], a[212:215], v[4:7]\n"
    "s_add_u32 s83, s67, 24576\n"
    "v_mfma_f32_16x16x32_bf16 v[28:31], a[80:83], a[248:251], v[28:31]\n"
    "v_exp_f32 v19, v19\n"
    "v_mfma_f32_16x16x32_bf16 v[8:11], a[64:67], a[232:235], v[8:11]\n"
    "v_accvgpr_write_b32 a174, v186\n"
    "v_mfma_f32_16x16x32_bf16 v[24:27], a[84:87], a[228:231], v[24:27]\n"
    "v_exp_f32 v33, v33\n"
    "v_mfma_f32_16x16x32_bf16 v[20:23], a[88:91], a[208:211], v[20:23]\n"
    "buffer_load_dwordx4 v1, s[16:19], s83 offen offset:0 sc1 lds\n"
    "v_mfma_f32_16x16x32_bf16 v[28:31], a[84:87], a[252:255], v[28:31]\n"
    "v_exp_f32 v32, v32\n"
    "v_mfma_f32_16x16x32_bf16 v[28:31], a[88:91], a[160:163], v[28:31]\n"
    "v_exp_f32 v18, v18\n"
    "v_mfma_f32_16x16x32_bf16 v[24:27], a[80:83], a[224:227], v[24:27]\n"
    "v_exp_f32 v49, v49\n"
    "v_mfma_f32_16x16x32_bf16 v[8:11], a[60:63], a[228:231], v[8:11]\n"
    "v_exp_f32 v35, v35\n"
    "v_mfma_f32_16x16x32_bf16 v[24:27], a[76:79], a[220:223], v[24:27]\n"
    "v_cvt_pk_bf16_f32 v92, v16, v17\n"
    "v_cvt_pk_bf16_f32 v93, v18, v19\n"
    "v_mfma_f32_16x16x32_bf16 v[12:15], a[60:63], a[252:255], v[12:15]\n"
    "v_and_b32 v98, 0x80008000, v98\n"
    "v_cvt_pk_bf16_f32 v94, v32, v33\n"
    "v_mfma_f32_16x16x32_bf16 v[24:27], a[72:75], a[216:219], v[24:27]\n"
    "buffer_load_dwordx4 v1, s[16:19], s83 offen offset:1024 sc1 lds\n"
    "v_mfma_f32_16x16x32_bf16 v[16:19], a[68:71], a[188:191], v[100:103]\n"
    "v_cvt_pk_bf16_f32 v95, v34, v35\n"
    "v_mfma_f32_16x16x32_bf16 v[16:19], a[64:67], a[184:187], v[16:19]\n"
    "s_waitcnt vmcnt(24)\n"
    "s_barrier\n"
    "v_mfma_f32_16x16x32_bf16 v[8:11], a[56:59], a[224:227], v[8:11]\n"
    "ds_read_b128 a[124:127], v249 offset:13312\n"
    "ds_read_b128 a[120:123], v249 offset:1024\n"
    "v_mfma_f32_16x16x32_bf16 v[32:35], a[92:95], a[188:191], v[104:107]\n"
    "ds_read_b128 a[116:119], v249 offset:12288\n"
    "ds_read_b128 a[112:115], v249 offset:0\n"
    "v_mfma_f32_16x16x32_bf16 v[32:35], a[88:91], a[184:187], v[32:35]\n"
    "ds_read_b128 a[132:135], v249 offset:14336\n"
    "ds_read_b128 a[144:147], v249 offset:4096\n"
    "v_mfma_f32_16x16x32_bf16 v[8:11], a[52:55], a[220:223], v[8:11]\n"
    "s_add_u32 s76, s71, s60\n"
    "v_exp_f32 v51, v51\n"
    "v_mfma_f32_16x16x32_bf16 v[12:15], a[64:67], a[160:163], v[12:15]\n"
    "buffer_load_dwordx4 v1, s[16:19], s83 offen offset:2048 sc1 lds\n"
    "v_mfma_f32_16x16x32_bf16 v[28:31], a[92:95], a[164:167], v[28:31]\n"
    "v_exp_f32 v66, v66\n"
    "v_mfma_f32_16x16x32_bf16 v[16:19], a[60:63], a[180:183], v[16:19]\n"
    "v_exp_f32 v50, v50\n"
    "v_mfma_f32_16x16x32_bf16 v[12:15], a[68:71], a[164:167], v[12:15]\n"
    "v_exp_f32 v67, v67\n"
    "v_mfma_f32_16x16x32_bf16 v[8:11], a[48:51], a[216:219], v[8:11]\n"
    "s_add_u32 s77, s71, s61\n"
    "ds_read_b128 a[156:159], v249 offset:17408\n"
    "s_waitcnt lgkmcnt(6)\n"
    "v_mfma_f32_16x16x32_bf16 v[128:131], a[124:127], v[72:75], 0\n"
    "ds_read_b128 a[136:139], v249 offset:3072\n"
    "v_mfma_f32_16x16x32_bf16 v[160:163], a[124:127], v[88:91], 0\n"
    "s_waitcnt lgkmcnt(4)\n"
    "ds_read_b128 a[148:151], v249 offset:16384\n"
    "ds_read_b128 a[128:131], v249 offset:2048\n"
    "v_mfma_f32_16x16x32_bf16 v[156:159], a[112:115], v[84:87], 0\n"
    "buffer_load_dwordx4 v1, s[16:19], s83 offen offset:3072 sc1 lds\n"
    "v_mfma_f32_16x16x32_bf16 v[16:19], a[56:59], a[176:179], v[16:19]\n"
    "s_add_u32 s83, s67, 28672\n"
    "ds_read_b128 a[152:155], v249 offset:5120\n"
    "v_mfma_f32_16x16x32_bf16 v[16:19], a[52:55], a[172:175], v[16:19]\n"
    "s_add_u32 m0, s70, 4096\n"
    "v_exp_f32 v7, v7\n"
    "v_mfma_f32_16x16x32_bf16 v[20:23], a[92:95], a[212:215], v[20:23]\n"
    "v_mfma_f32_16x16x32_bf16 v[156:159], a[116:119], v[88:91], v[156:159]\n"
    "v_exp_f32 v48, v48\n"
    "v_mfma_f32_16x16x32_bf16 v[32:35], a[84:87], a[180:183], v[32:35]\n"
    "s_waitcnt lgkmcnt(6)\n"
    "v_exp_f32 v65, v65\n"
    "v_mfma_f32_16x16x32_bf16 v[148:151], a[132:135], v[80:83], 0\n"
    "ds_read_b128 v[36:39], v243 offset:0\n"
    "v_cmp_ne_u32 s[34:35], 0, v98\n"
    "v_mfma_f32_16x16x32_bf16 v[124:127], a[112:115], v[68:71], 0\n"
    "buffer_load_dwordx4 v1, s[16:19], s83 offen offset:0 sc1 lds\n"
    "v_mfma_f32_16x16x32_bf16 v[176:179], a[120:123], v[92:95], 0\n"
    "s_waitcnt lgkmcnt(3)\n"
    "v_exp_f32 v10, v10\n"
    "v_mfma_f32_16x16x32_bf16 a[16:19], a[148:151], v[88:91], 0\n"
    "v_exp_f32 v64, v64\n"
    "v_mfma_f32_16x16x32_bf16 v[136:139], a[136:139], v[68:71], 0\n"
    "v_exp_f32 v4, v4\n"
    "v_mfma_f32_16x16x32_bf16 v[32:35], a[80:83], a[176:179], v[32:35]\n"
    "ds_read_b128 a[140:143], v249 offset:15360\n"
    "v_add_u32 v251, 1536, v251\n"
    "v_mfma_f32_16x16x32_bf16 v[140:143], a[116:119], v[80:83], 0\n"
    "v_cvt_pk_bf16_f32 v97, v50, v51\n"
    "v_mfma_f32_16x16x32_bf16 v[16:19], a[48:51], a[168:171], v[16:19]\n"
    "s_waitcnt lgkmcnt(3)\n"
    "v_cvt_pk_bf16_f32 v98, v64, v65\n"
    "v_cvt_pk_bf16_f32 v99, v66, v67\n"
    "v_mfma_f32_16x16x32_bf16 v[164:167], a[128:131], v[84:87], 0\n"
    "buffer_load_dwordx4 v1, s[16:19], s83 offen offset:1024 sc1 lds\n"
    "v_mfma_f32_16x16x32_bf16 a[4:7], a[156:159], v[80:83], 0\n"
    "v_exp_f32 v27, v27\n"
    "v_mfma_f32_16x16x32_bf16 v[128:131], a[120:123], v[68:71], v[128:131]\n"
    "s_waitcnt lgkmcnt(2)\n"
    "v_mfma_f32_16x16x32_bf16 v[192:195], a[152:155], v[68:71], 0\n"
    "v_exp_f32 v26, v26\n"
    "v_mfma_f32_16x16x32_bf16 a[32:35], a[144:147], v[92:95], 0\n"
    "ds_read_b128 v[100:103], v250 offset:1664\n"
    "s_waitcnt lgkmcnt(1)\n"
    "v_mfma_f32_16x16x32_bf16 v[152:155], a[140:143], v[80:83], 0\n"
    "v_exp_f32 v22, v22\n"
    "v_mfma_f32_16x16x32_bf16 v[32:35], a[76:79], a[172:175], v[32:35]\n"
    "v_cvt_pk_bf16_f32 v96, v48, v49\n"
    "v_mfma_f32_16x16x32_bf16 v[144:147], a[120:123], v[76:79], 0\n"
    "s_waitcnt vmcnt(12)\n"
    "v_fma_f32 v216, v216, v36, -v38\n"
    "v_fma_f32 v229, v229, v37, -v39\n"
    "v_mfma_f32_16x16x32_bf16 v[160:163], a[120:123], v[84:87], v[160:163]\n"
    "v_fma_f32 v206, v206, v36, -v38\n"
    "v_fma_f32 v213, v213, v37, -v39\n"
    "v_mfma_f32_16x16x32_bf16 v[172:175], a[116:119], v[96:99], 0\n"
    "v_fma_f32 v214, v214, v36, -v38\n"
    "v_mfma_f32_16x16x32_bf16 v[124:127], a[116:119], v[72:75], v[124:127]\n"
    "v_fma_f32 v207, v207, v37, -v39\n"
    "v_fma_f32 v205, v205, v37, -v39\n"
    "v_mfma_f32_16x16x32_bf16 v[176:179], a[124:127], v[96:99], v[176:179]\n"
    "ds_read_b128 a[116:119], v249 offset:18432\n"
    "v_fma_f32 v209, v209, v37, -v39\n"
    "v_mfma_f32_16x16x32_bf16 v[144:147], a[124:127], v[80:83], v[144:147]\n"
    "v_fma_f32 v215, v215, v37, -v39\n"
    "v_fma_f32 v217, v217, v37, -v39\n"
    "v_mfma_f32_16x16x32_bf16 a[4:7], a[152:155], v[76:79], a[4:7]\n"
    "v_exp_f32 v13, v13\n"
    "v_mfma_f32_16x16x32_bf16 v[140:143], a[112:115], v[76:79], v[140:143]\n"
    "v_fma_f32 v212, v212, v36, -v38\n"
    "v_mfma_f32_16x16x32_bf16 v[184:187], a[140:143], v[96:99], 0\n"
    "v_cvt_pk_bf16_f32 v216, v216, v217\n"
    "v_fma_f32 v223, v223, v37, -v39\n"
    "v_mfma_f32_16x16x32_bf16 v[164:167], a[132:135], v[88:91], v[164:167]\n"
    "v_fma_f32 v222, v222, v36, -v38\n"
    "v_fma_f32 v231, v231, v37, -v39\n"
    "v_mfma_f32_16x16x32_bf16 v[188:191], a[148:151], v[72:75], 0\n"
    "v_exp_f32 v21, v21\n"
    "v_mfma_f32_16x16x32_bf16 v[172:175], a[112:115], v[92:95], v[172:175]\n"
    "v_fma_f32 v235, v235, v37, -v39\n"
    "v_fma_f32 v211, v211, v37, -v39\n"
    "v_mfma_f32_16x16x32_bf16 v[132:135], a[128:131], v[68:71], 0\n"
    "ds_read_b128 a[112:115], v249 offset:6144\n"
    "v_mfma_f32_16x16x32_bf16 a[32:35], a[148:151], v[96:99], a[32:35]\n"
    "v_exp_f32 v5, v5\n"
    "v_mfma_f32_16x16x32_bf16 v[180:183], a[132:135], v[96:99], 0\n"
    "ds_read_b128 a[120:123], v249 offset:7168\n"
    "v_cvt_pk_bf16_f32 v206, v206, v207\n"
    "v_mfma_f32_16x16x32_bf16 v[132:135], a[132:135], v[72:75], v[132:135]\n"
    "ds_read_b128 a[124:127], v249 offset:19456\n"
    "v_fma_f32 v221, v221, v37, -v39\n"
    "v_mfma_f32_16x16x32_bf16 v[180:183], a[128:131], v[92:95], v[180:183]\n"
    "ds_read_b128 a[132:135], v249 offset:20480\n"
    "v_fma_f32 v233, v233, v37, -v39\n"
    "v_mfma_f32_16x16x32_bf16 a[0:3], a[144:147], v[76:79], 0\n"
    "v_exp_f32 v12, v12\n"
    "v_mfma_f32_16x16x32_bf16 a[0:3], a[148:151], v[80:83], a[0:3]\n"
    "s_waitcnt lgkmcnt(4)\n"
    "v_fma_f32 v204, v204, v36, -v38\n"
    "v_fma_f32 v218, v218, v36, -v38\n"
    "v_mfma_f32_16x16x32_bf16 a[8:11], a[116:119], v[80:83], 0\n"
    "v_cvt_pk_bf16_f32 v212, v212, v213\n"
    "v_fma_f32 v219, v219, v37, -v39\n"
    "v_mfma_f32_16x16x32_bf16 a[20:23], a[152:155], v[84:87], 0\n"
    "v_cvt_pk_bf16_f32 v204, v204, v205\n"
    "v_fma_f32 v220, v220, v36, -v38\n"
    "v_mfma_f32_16x16x32_bf16 a[36:39], a[156:159], v[96:99], 0\n"
    "v_mfma_f32_16x16x32_bf16 a[16:19], a[144:147], v[84:87], a[16:19]\n"
    "s_waitcnt lgkmcnt(2)\n"
    "buffer_store_dword v204, v244, s[16:19], s76 offen offset:0 sc1\n"
    "v_exp_f32 v16, v16\n"
    "v_mfma_f32_16x16x32_bf16 a[12:15], a[120:123], v[76:79], 0\n"
    "v_mfma_f32_16x16x32_bf16 v[32:35], a[72:75], a[168:171], v[32:35]\n"
    "v_fma_f32 v234, v234, v36, -v38\n"
    "v_fma_f32 v208, v208, v36, -v38\n"
    "v_mfma_f32_16x16x32_bf16 a[36:39], a[152:155], v[92:95], a[36:39]\n"
    "buffer_store_dword v206, v244, s[16:19], s76 offen offset:128 sc1\n"
    "v_fma_f32 v225, v225, v37, -v39\n"
    "v_fma_f32 v224, v224, v36, -v38\n"
    "v_mfma_f32_16x16x32_bf16 v[168:171], a[136:139], v[84:87], 0\n"
    "s_waitcnt lgkmcnt(1)\n"
    "v_cvt_pk_bf16_f32 v208, v208, v209\n"
    "v_fma_f32 v230, v230, v36, -v38\n"
    "v_mfma_f32_16x16x32_bf16 v[200:203], a[124:127], v[72:75], 0\n"
    "v_fma_f32 v226, v226, v36, -v38\n"
    "v_fma_f32 v210, v210, v36, -v38\n"
    "v_mfma_f32_16x16x32_bf16 a[44:47], a[120:123], v[92:95], 0\n"
    "buffer_store_dword v208, v244, s[16:19], s76 offen offset:1024 sc1\n"
    "v_smfmac_f32_16x16x64_bf16 v[120:123], v[52:55], v[92:99], v253\n"
    "v_cvt_pk_bf16_f32 v210, v210, v211\n"
    "v_mfma_f32_16x16x32_bf16 v[188:191], a[144:147], v[68:71], v[188:191]\n"
    "buffer_store_dword v210, v244, s[16:19], s76 offen offset:1152 sc1\n"
    "v_mfma_f32_16x16x32_bf16 v[148:151], a[128:131], v[76:79], v[148:151]\n"
    "v_cvt_pk_bf16_f32 v214, v214, v215\n"
    "v_fma_f32 v227, v227, v37, -v39\n"
    "v_mfma_f32_16x16x32_bf16 a[8:11], a[112:115], v[76:79], a[8:11]\n"
    "ds_read_b128 a[144:147], v249 offset:10240\n"
    "ds_read_b128 a[128:131], v249 offset:8192\n"
    "v_mfma_f32_16x16x32_bf16 a[28:31], a[124:127], v[88:91], 0\n"
    "v_exp_f32 v24, v24\n"
    "v_mfma_f32_16x16x32_bf16 a[24:27], a[112:115], v[84:87], 0\n"
    "v_mov_b32 v2, v3\n"
    "v_cvt_pk_bf16_f32 v222, v222, v223\n"
    "v_mfma_f32_16x16x32_bf16 a[40:43], a[116:119], v[96:99], 0\n"
    "ds_read_b128 a[148:151], v249 offset:22528\n"
    "v_fma_f32 v232, v232, v36, -v38\n"
    "v_mfma_f32_16x16x32_bf16 v[200:203], a[120:123], v[68:71], v[200:203]\n"
    "buffer_store_dword v212, v244, s[16:19], s76 offen offset:256 sc1\n"
    "v_cvt_pk_bf16_f32 v224, v224, v225\n"
    "v_mfma_f32_16x16x32_bf16 v[152:155], a[136:139], v[76:79], v[152:155]\n"
    "s_waitcnt lgkmcnt(3)\n"
    "v_exp_f32 v14, v14\n"
    "v_mfma_f32_16x16x32_bf16 a[96:99], a[132:135], v[96:99], 0\n"
    "v_exp_f32 v31, v31\n"
    "v_mfma_f32_16x16x32_bf16 v[196:199], a[112:115], v[68:71], 0\n"
    "s_waitcnt lgkmcnt(2)\n"
    "buffer_store_dword v214, v244, s[16:19], s76 offen offset:384 sc1\n"
    "v_exp_f32 v6, v6\n"
    "v_mfma_f32_16x16x32_bf16 a[104:107], a[144:147], v[92:95], 0\n"
    "v_exp_f32 v18, v18\n"
    "v_mfma_f32_16x16x32_bf16 a[28:31], a[120:123], v[84:87], a[28:31]\n"
    "v_exp_f32 v33, v33\n"
    "v_mfma_f32_16x16x32_bf16 a[64:67], a[132:135], v[80:83], 0\n"
    "v_cvt_pk_bf16_f32 v230, v230, v231\n"
    "v_mfma_f32_16x16x32_bf16 v[168:171], a[140:143], v[88:91], v[168:171]\n"
    "s_waitcnt lgkmcnt(1)\n"
    "buffer_load_dwordx2 v[236:237], v241, s[36:39], s81 offen offset:0 sc1\n"
    "v_mfma_f32_16x16x32_bf16 a[48:51], a[128:131], v[68:71], 0\n"
    "v_exp_f32 v19, v19\n"
    "v_mfma_f32_16x16x32_bf16 v[136:139], a[140:143], v[72:75], v[136:139]\n"
    "s_waitcnt lgkmcnt(0)\n"
    "buffer_store_dword v216, v244, s[16:19], s76 offen offset:1280 sc1\n"
    "v_mfma_f32_16x16x32_bf16 a[104:107], a[148:151], v[96:99], a[104:107]\n"
    "v_exp_f32 v29, v29\n"
    "v_mfma_f32_16x16x32_bf16 a[88:91], a[148:151], v[88:91], 0\n"
    "v_fma_f32 v228, v228, v36, -v38\n"
    "v_cvt_pk_bf16_f32 v226, v226, v227\n"
    "v_mfma_f32_16x16x32_bf16 v[196:199], a[116:119], v[72:75], v[196:199]\n"
    "v_cvt_pk_bf16_f32 v218, v218, v219\n"
    "v_cvt_pk_bf16_f32 v220, v220, v221\n"
    "v_mfma_f32_16x16x32_bf16 v[184:187], a[136:139], v[92:95], v[184:187]\n"
    "buffer_store_dword v218, v244, s[16:19], s76 offen offset:1408 sc1\n"
    "ds_read_b128 a[140:143], v249 offset:21504\n"
    "v_mfma_f32_16x16x32_bf16 a[24:27], a[116:119], v[88:91], a[24:27]\n"
    "v_exp_f32 v32, v32\n"
    "v_mfma_f32_16x16x32_bf16 v[192:195], a[156:159], v[72:75], v[192:195]\n"
    "ds_read_b128 a[152:155], v249 offset:11264\n"
    "v_cvt_pk_bf16_f32 v232, v232, v233\n"
    "v_mfma_f32_16x16x32_bf16 a[20:23], a[156:159], v[88:91], a[20:23]\n"
    "v_exp_f32 v9, v9\n"
    "v_mfma_f32_16x16x32_bf16 a[80:83], a[128:131], v[84:87], 0\n"
    "v_exp_f32 v11, v11\n"
    "v_mfma_f32_16x16x32_bf16 a[12:15], a[124:127], v[80:83], a[12:15]\n"
    "buffer_store_dword v220, v244, s[16:19], s77 offen offset:0 sc1\n"
    "ds_read_b128 a[156:159], v249 offset:23552\n"
    "v_mfma_f32_16x16x32_bf16 a[72:75], a[144:147], v[76:79], 0\n"
    "v_exp_f32 v28, v28\n"
    "v_mfma_f32_16x16x32_bf16 a[64:67], a[128:131], v[76:79], a[64:67]\n"
    "buffer_store_dword v222, v244, s[16:19], s77 offen offset:128 sc1\n"
    "v_mfma_f32_16x16x32_bf16 a[80:83], a[132:135], v[88:91], a[80:83]\n"
    "v_mfma_f32_16x16x32_bf16 a[88:91], a[144:147], v[84:87], a[88:91]\n"
    "v_exp_f32_e64 v30, v30\n"
    "v_mfma_f32_16x16x32_bf16 a[56:59], a[148:151], v[72:75], 0\n"
    "v_add_u32 v243, 512, v243\n"
    "v_cvt_pk_bf16_f32 v228, v228, v229\n"
    "v_mfma_f32_16x16x32_bf16 a[56:59], a[144:147], v[68:71], a[56:59]\n"
    "s_add_u32 s67, s67, 49152\n"
    "buffer_store_dword v224, v244, s[16:19], s77 offen offset:1024 sc1\n"
    "v_mfma_f32_16x16x32_bf16 a[48:51], a[132:135], v[72:75], a[48:51]\n"
    "v_exp_f32 v20, v20\n"
    "v_mfma_f32_16x16x32_bf16 a[44:47], a[124:127], v[96:99], a[44:47]\n"
    "s_waitcnt lgkmcnt(0)\n"
    "buffer_store_dword v226, v244, s[16:19], s77 offen offset:1152 sc1\n"
    "ds_read_b128 a[136:139], v249 offset:9216\n"
    "v_mfma_f32_16x16x32_bf16 a[108:111], a[156:159], v[96:99], 0\n"
    "v_mfma_f32_16x16x32_bf16 a[96:99], a[128:131], v[92:95], a[96:99]\n"
    "v_exp_f32 v15, v15\n"
    "v_mfma_f32_16x16x32_bf16 a[100:103], a[140:143], v[96:99], 0\n"
    "buffer_store_dword v228, v244, s[16:19], s77 offen offset:256 sc1\n"
    "v_exp_f32 v35, v35\n"
    "v_mfma_f32_16x16x32_bf16 a[68:71], a[140:143], v[80:83], 0\n"
    "ds_read_b128 a[120:123], v3 offset:16384\n"
    "ds_read_b128 a[116:119], v3 offset:14336\n"
    "v_mfma_f32_16x16x32_bf16 a[40:43], a[112:115], v[92:95], a[40:43]\n"
    "v_exp_f32 v8, v8\n"
    "v_mfma_f32_16x16x32_bf16 a[76:79], a[156:159], v[80:83], 0\n"
    "buffer_store_dword v230, v244, s[16:19], s77 offen offset:384 sc1\n"
    "v_mfma_f32_16x16x32_bf16 a[76:79], a[152:155], v[76:79], a[76:79]\n"
    "v_exp_f32 v34, v34\n"
    "v_mfma_f32_16x16x32_bf16 a[60:63], a[152:155], v[68:71], 0\n"
    "s_waitcnt lgkmcnt(2)\n"
    "ds_read_b128 a[124:127], v3 offset:18432\n"
    "ds_read_b128 a[128:131], v3 offset:20480\n"
    "v_mfma_f32_16x16x32_bf16 a[92:95], a[152:155], v[84:87], 0\n"
    "s_waitcnt vmcnt(15)\n"
    "ds_add_u32 v247, v246\n"
    "v_mfma_f32_16x16x32_bf16 a[108:111], a[152:155], v[92:95], a[108:111]\n"
    "ds_read_b128 a[132:135], v3 offset:22528\n"
    "v_add_u32 v250, 1536, v250\n"
    "v_mfma_f32_16x16x32_bf16 a[52:55], a[136:139], v[68:71], 0\n"
    "ds_read_b128 a[112:115], v3 offset:12288\n"
    "v_mfma_f32_16x16x32_bf16 a[84:87], a[136:139], v[84:87], 0\n"
    "v_add_u32 v3, 49152, v252\n"
    "v_mfma_f32_16x16x32_bf16 a[72:75], a[148:151], v[80:83], a[72:75]\n"
    "buffer_store_dword v232, v244, s[16:19], s77 offen offset:1280 sc1\n"
    "v_cvt_pk_bf16_f32 v234, v234, v235\n"
    "v_mfma_f32_16x16x32_bf16 a[100:103], a[136:139], v[92:95], a[100:103]\n"
    "v_exp_f32 v17, v17\n"
    "v_mfma_f32_16x16x32_bf16 a[84:87], a[140:143], v[88:91], a[84:87]\n"
    "v_exp_f32 v23, v23\n"
    "v_mfma_f32_16x16x32_bf16 a[60:63], a[156:159], v[72:75], a[60:63]\n"
    "buffer_store_dword v234, v244, s[16:19], s77 offen offset:1408 sc1\n"
    "v_mfma_f32_16x16x32_bf16 a[52:55], a[140:143], v[72:75], a[52:55]\n"
    "v_exp_f32 v25, v25\n"
    "v_mfma_f32_16x16x32_bf16 a[68:71], a[136:139], v[76:79], a[68:71]\n"
    "s_waitcnt lgkmcnt(0)\n"
    "v_mfma_f32_16x16x32_bf16 a[92:95], a[156:159], v[88:91], a[92:95]\n"
    "s_branch .LPK\n"
    ".fill ((0x20000 - (. - _Z8attn_fwdPKtPKiS2_S0_S0_S0_S0_PcPt)) & 4095) / 4, 4, 0xBF800000\n"
    ".LhVP:\n"
    "s_waitcnt vmcnt(12)\n"
    "ds_read_b128 v[52:55], v251 offset:0\n"
    "s_add_u32 s83, s67, 4096\n"
    "ds_read_b128 v[200:203], v97 offset:49408\n"
    "ds_read_b128 v[196:199], v96 offset:49408\n"
    "v_mov_b32 v2, v3\n"
    "ds_read_b128 v[192:195], v97 offset:49280\n"
    "ds_read_b128 v[188:191], v96 offset:49280\n"
    "ds_read_b128 v[184:187], v97 offset:49152\n"
    "s_add_u32 m0, s68, .Lend-.Lend2+(0)\n"
    "ds_read_b128 v[180:183], v96 offset:49152\n"
    "s_waitcnt lgkmcnt(6)\n"
    "v_smfmac_f32_16x16x64_bf16 v[108:111], v[52:55], v[68:75], v253\n"
    "s_waitcnt lgkmcnt(4)\n"
    "v_pk_maximum3_f16 v98, v200, v201, v98\n"
    "v_pk_maximum3_f16 v98, v202, v203, v98\n"
    "v_pk_add_u16 v203, s89, v203 clamp\n"
    "v_pk_add_u16 v201, s89, v201 clamp\n"
    "v_pk_add_u16 v200, s89, v200 clamp\n"
    "v_pk_add_u16 v202, s89, v202 clamp\n"
    "v_pk_maximum3_f16 v98, v196, v197, v98\n"
    "v_smfmac_f32_16x16x64_bf16 v[116:119], v[52:55], v[84:91], v253\n"
    "v_pk_maximum3_f16 v98, v198, v199, v98\n"
    "v_mfma_f32_16x16x32_bf16 v[64:67], a[44:47], v[200:203], v[64:67]\n"
    "s_waitcnt lgkmcnt(3)\n"
    "v_pk_add_u16 v197, s89, v197 clamp\n"
    "v_pk_maximum3_f16 v98, v192, v193, v98\n"
    "v_mfma_f32_16x16x32_bf16 v[48:51], a[20:23], v[200:203], v[48:51]\n"
    "buffer_load_dwordx4 v1, s[16:19], s67 offen offset:0 sc1 lds\n"
    "v_mfma_f32_16x16x32_bf16 v[16:19], a[132:135], v[200:203], v[16:19]\n"
    "v_pk_add_u16 v199, s89, v199 clamp\n"
    "v_pk_add_u16 v198, s89, v198 clamp\n"
    "v_smfmac_f32_16x16x64_bf16 v[112:115], v[52:55], v[76:83], v253\n"
    "v_pk_add_u16 v196, s89, v196 clamp\n"
    "v_mfma_f32_16x16x32_bf16 v[32:35], a[156:159], v[200:203], v[32:35]\n"
    "s_waitcnt lgkmcnt(2)\n"
    "v_pk_add_u16 v193, s89, v193 clamp\n"
    "v_pk_maximum3_f16 v98, v194, v195, v98\n"
    "v_pk_maximum3_f16 v98, v188, v189, v98\n"
    "v_mfma_f32_16x16x32_bf16 v[32:35], a[152:155], v[196:199], v[32:35]\n"
    "v_pk_maximum3_f16 v98, v190, v191, v98\n"
    "v_pk_add_u16 v192, s89, v192 clamp\n"
    "v_mfma_f32_16x16x32_bf16 v[16:19], a[128:131], v[196:199], v[16:19]\n"
    "v_pk_add_u16 v194, s89, v194 clamp\n"
    "v_pk_add_u16 v195, s89, v195 clamp\n"
    "v_mfma_f32_16x16x32_bf16 v[48:51], a[16:19], v[196:199], v[48:51]\n"
    "buffer_load_dwordx4 v1, s[16:19], s67 offen offset:1024 sc1 lds\n"
    "v_mfma_f32_16x16x32_bf16 v[48:51], a[12:15], v[192:195], v[48:51]\n"
    "s_waitcnt lgkmcnt(1)\n"
    "v_pk_maximum3_f16 v98, v184, v185, v98\n"
    "v_pk_add_u16 v188, s89, v188 clamp\n"
    "v_pk_maximum3_f16 v98, v186, v187, v98\n"
    "v_mfma_f32_16x16x32_bf16 v[32:35], a[148:151], v[192:195], v[32:35]\n"
    "v_pk_add_u16 v190, s89, v190 clamp\n"
    "v_accvgpr_write_b32 a184, v196\n"
    "v_mfma_f32_16x16x32_bf16 v[16:19], a[124:127], v[192:195], v[16:19]\n"
    "v_pk_add_u16 v189, s89, v189 clamp\n"
    "v_pk_add_u16 v191, s89, v191 clamp\n"
    "v_mfma_f32_16x16x32_bf16 v[64:67], a[40:43], v[196:199], v[64:67]\n"
    "v_pk_add_u16 v186, s89, v186 clamp\n"
    "v_pk_add_u16 v187, s89, v187 clamp\n"
    "v_mfma_f32_16x16x32_bf16 v[32:35], a[144:147], v[188:191], v[32:35]\n"
    "s_waitcnt lgkmcnt(0)\n"
    "v_pk_add_u16 v185, s89, v185 clamp\n"
    "v_pk_maximum3_f16 v98, v180, v181, v98\n"
    "v_mfma_f32_16x16x32_bf16 v[48:51], a[8:11], v[188:191], v[48:51]\n"
    "s_waitcnt vmcnt(8)\n"
    "buffer_load_dwordx4 v1, s[16:19], s67 offen offset:2048 sc1 lds\n"
    "v_mfma_f32_16x16x32_bf16 v[64:67], a[36:39], v[192:195], v[64:67]\n"
    "s_barrier\n"
    "v_pk_add_u16 v184, s89, v184 clamp\n"
    "v_mfma_f32_16x16x32_bf16 v[16:19], a[120:123], v[188:191], v[16:19]\n"
    "ds_read_b128 a[48:51], v252 offset:24576\n"
    "ds_read_b128 a[68:71], v252 offset:34816\n"
    "v_mfma_f32_16x16x32_bf16 v[32:35], a[140:143], v[184:187], v[32:35]\n"
    "ds_read_b128 a[76:79], v252 offset:27648\n"
    "ds_read_b128 a[72:75], v252 offset:25600\n"
    "v_mfma_f32_16x16x32_bf16 v[48:51], a[4:7], v[184:187], v[48:51]\n"
    "ds_read_b128 a[92:95], v252 offset:35840\n"
    "v_pk_maximum3_f16 v98, v182, v183, v98\n"
    "v_mfma_f32_16x16x32_bf16 v[64:67], a[32:35], v[188:191], v[64:67]\n"
    "v_pk_add_u16 v182, s89, v182 clamp\n"
    "v_pk_add_u16 v181, s89, v181 clamp\n"
    "v_mfma_f32_16x16x32_bf16 v[64:67], a[28:31], v[184:187], v[64:67]\n"
    "v_pk_add_u16 v183, s89, v183 clamp\n"
    "v_pk_add_u16 v180, s89, v180 clamp\n"
    "v_mfma_f32_16x16x32_bf16 v[16:19], a[116:119], v[184:187], v[16:19]\n"
    "buffer_load_dwordx4 v1, s[16:19], s67 offen offset:3072 sc1 lds\n"
    "v_mfma_f32_16x16x32_bf16 v[32:35], a[136:139], v[180:183], v[32:35]\n"
    "ds_read_b128 a[64:67], v252 offset:32768\n"
    "v_accvgpr_write_b32 a176, v188\n"
    "v_mfma_f32_16x16x32_bf16 v[16:19], a[112:115], v[180:183], v[16:19]\n"
    "v_accvgpr_write_b32 a175, v187\n"
    "s_waitcnt lgkmcnt(5)\n"
    "v_mfma_f32_16x16x32_bf16 v[12:15], a[48:51], a[240:243], v[100:103]\n"
    "s_waitcnt lgkmcnt(4)\n"
    "ds_read_b128 a[56:59], v252 offset:28672\n"
    "v_accvgpr_write_b32 a177, v189\n"
    "v_mfma_f32_16x16x32_bf16 v[48:51], a[0:3], v[180:183], v[48:51]\n"
    "s_add_u32 m0, s68, 4096\n"
    "ds_read_b128 a[80:83], v252 offset:29696\n"
    "ds_read_b128 a[88:91], v252 offset:33792\n"
    "v_mfma_f32_16x16x32_bf16 v[8:11], a[68:71], a[236:239], v[100:103]\n"
    "v_accvgpr_write_b32 a188, v200\n"
    "v_accvgpr_write_b32 a190, v202\n"
    "v_mfma_f32_16x16x32_bf16 v[4:7], a[48:51], a[192:195], v[100:103]\n"
    "s_waitcnt lgkmcnt(5)\n"
    "ds_read_b128 a[60:63], v252 offset:30720\n"
    "ds_read_b128 a[52:55], v252 offset:26624\n"
    "v_mfma_f32_16x16x32_bf16 v[20:23], a[72:75], a[192:195], v[104:107]\n"
    "buffer_load_dwordx4 v1, s[16:19], s83 offen offset:0 sc1 lds\n"
    "v_mfma_f32_16x16x32_bf16 v[28:31], a[72:75], a[240:243], v[104:107]\n"
    "s_waitcnt lgkmcnt(6)\n"
    "v_accvgpr_write_b32 a171, v183\n"
    "v_accvgpr_write_b32 a186, v198\n"
    "v_mfma_f32_16x16x32_bf16 v[24:27], a[92:95], a[236:239], v[104:107]\n"
    "v_exp_f32 v18, v18\n"
    "v_mfma_f32_16x16x32_bf16 v[64:67], a[24:27], v[180:183], v[64:67]\n"
    "v_accvgpr_write_b32 a174, v186\n"
    "v_accvgpr_write_b32 a178, v190\n"
    "v_mfma_f32_16x16x32_bf16 v[20:23], a[76:79], a[196:199], v[20:23]\n"
    "v_accvgpr_write_b32 a191, v203\n"
    "v_accvgpr_write_b32 a179, v191\n"
    "v_mfma_f32_16x16x32_bf16 v[28:31], a[76:79], a[244:247], v[28:31]\n"
    "s_waitcnt lgkmcnt(5)\n"
    "v_accvgpr_write_b32 a180, v192\n"
    "v_mfma_f32_16x16x32_bf16 v[8:11], a[64:67], a[232:235], v[8:11]\n"
    "s_waitcnt lgkmcnt(3)\n"
    "ds_read_b128 a[84:87], v252 offset:31744\n"
    "v_accvgpr_write_b32 a187, v199\n"
    "v_mfma_f32_16x16x32_bf16 v[28:31], a[80:83], a[248:251], v[28:31]\n"
    "v_accvgpr_write_b32 a169, v181\n"
    "v_accvgpr_write_b32 a173, v185\n"
    "v_mfma_f32_16x16x32_bf16 v[20:23], a[80:83], a[200:203], v[20:23]\n"
    "s_waitcnt lgkmcnt(1)\n"
    "buffer_load_dwordx4 v1, s[16:19], s83 offen offset:1024 sc1 lds\n"
    "v_mfma_f32_16x16x32_bf16 v[4:7], a[52:55], a[196:199], v[4:7]\n"
    "v_exp_f32 v16, v16\n"
    "v_mfma_f32_16x16x32_bf16 v[24:27], a[88:91], a[232:235], v[24:27]\n"
    "v_accvgpr_write_b32 a168, v180\n"
    "v_accvgpr_write_b32 a182, v194\n"
    "v_mfma_f32_16x16x32_bf16 v[8:11], a[60:63], a[228:231], v[8:11]\n"
    "v_accvgpr_write_b32 a181, v193\n"
    "v_accvgpr_write_b32 a189, v201\n"
    "v_mfma_f32_16x16x32_bf16 v[8:11], a[56:59], a[224:227], v[8:11]\n"
    "s_add_u32 m0, s64, 0\n"
    "v_accvgpr_write_b32 a170, v182\n"
    "v_accvgpr_write_b32 a172, v184\n"
    "v_mfma_f32_16x16x32_bf16 v[4:7], a[56:59], a[200:203], v[4:7]\n"
    "v_exp_f32 v32, v32\n"
    "v_mfma_f32_16x16x32_bf16 v[4:7], a[60:63], a[204:207], v[4:7]\n"
    "s_waitcnt lgkmcnt(0)\n"
    "v_accvgpr_write_b32 a183, v195\n"
    "v_accvgpr_write_b32 a185, v197\n"
    "v_mfma_f32_16x16x32_bf16 v[28:31], a[84:87], a[252:255], v[28:31]\n"
    "s_add_u32 s83, s67, 24576\n"
    "buffer_load_dwordx4 v1, s[16:19], s83 offen offset:0 sc1 lds\n"
    "v_mfma_f32_16x16x32_bf16 v[12:15], a[52:55], a[244:247], v[12:15]\n"
    "v_exp_f32 v17, v17\n"
    "v_mfma_f32_16x16x32_bf16 v[12:15], a[56:59], a[248:251], v[12:15]\n"
    "v_exp_f32 v19, v19\n"
    "v_mfma_f32_16x16x32_bf16 v[8:11], a[52:55], a[220:223], v[8:11]\n"
    "v_exp_f32 v35, v35\n"
    "v_mfma_f32_16x16x32_bf16 v[20:23], a[84:87], a[204:207], v[20:23]\n"
    "v_exp_f32 v51, v51\n"
    "v_mfma_f32_16x16x32_bf16 v[12:15], a[60:63], a[252:255], v[12:15]\n"
    "v_exp_f32 v65, v65\n"
    "v_mfma_f32_16x16x32_bf16 v[4:7], a[64:67], a[208:211], v[4:7]\n"
    "v_mfma_f32_16x16x32_bf16 v[12:15], a[64:67], a[160:163], v[12:15]\n"
    "buffer_load_dwordx4 v1, s[16:19], s83 offen offset:1024 sc1 lds\n"
    "v_mfma_f32_16x16x32_bf16 v[4:7], a[68:71], a[212:215], v[4:7]\n"
    "v_cvt_pk_bf16_f32 v93, v18, v19\n"
    "v_mfma_f32_16x16x32_bf16 v[24:27], a[84:87], a[228:231], v[24:27]\n"
    "v_exp_f32 v34, v34\n"
    "v_mfma_f32_16x16x32_bf16 v[8:11], a[48:51], a[216:219], v[8:11]\n"
    "v_exp_f32 v33, v33\n"
    "v_mfma_f32_16x16x32_bf16 v[12:15], a[68:71], a[164:167], v[12:15]\n"
    "v_cvt_pk_bf16_f32 v92, v16, v17\n"
    "v_mfma_f32_16x16x32_bf16 v[28:31], a[88:91], a[160:163], v[28:31]\n"
    "s_waitcnt vmcnt(8)\n"
    "v_exp_f32 v48, v48\n"
    "v_mfma_f32_16x16x32_bf16 v[16:19], a[68:71], a[188:191], v[100:103]\n"
    "s_barrier\n"
    "ds_read_b128 a[120:123], v248 offset:1024\n"
    "v_mfma_f32_16x16x32_bf16 v[24:27], a[80:83], a[224:227], v[24:27]\n"
    "buffer_load_dwordx4 v1, s[16:19], s83 offen offset:2048 sc1 lds\n"
    "v_mfma_f32_16x16x32_bf16 v[16:19], a[64:67], a[184:187], v[16:19]\n"
    "ds_read_b128 a[124:127], v248 offset:13312\n"
    "v_cvt_pk_bf16_f32 v94, v32, v33\n"
    "v_mfma_f32_16x16x32_bf16 v[16:19], a[60:63], a[180:183], v[16:19]\n"
    "ds_read_b128 a[136:139], v248 offset:3072\n"
    "ds_read_b128 a[128:131], v248 offset:2048\n"
    "v_mfma_f32_16x16x32_bf16 v[24:27], a[76:79], a[220:223], v[24:27]\n"
    "v_exp_f32 v50, v50\n"
    "v_mfma_f32_16x16x32_bf16 v[20:23], a[88:91], a[208:211], v[20:23]\n"
    "v_cvt_pk_bf16_f32 v97, v50, v51\n"
    "v_cvt_pk_bf16_f32 v95, v34, v35\n"
    "v_mfma_f32_16x16x32_bf16 v[32:35], a[92:95], a[188:191], v[104:107]\n"
    "v_exp_f32 v67, v67\n"
    "v_mfma_f32_16x16x32_bf16 v[16:19], a[56:59], a[176:179], v[16:19]\n"
    "v_mfma_f32_16x16x32_bf16 v[32:35], a[88:91], a[184:187], v[32:35]\n"
    "buffer_load_dwordx4 v1, s[16:19], s83 offen offset:3072 sc1 lds\n"
    "v_mfma_f32_16x16x32_bf16 v[20:23], a[92:95], a[212:215], v[20:23]\n"
    "v_pk_add_u16 v98, s89, v98\n"
    "s_waitcnt lgkmcnt(2)\n"
    "v_mfma_f32_16x16x32_bf16 v[160:163], a[124:127], v[88:91], 0\n"
    "ds_read_b128 a[132:135], v248 offset:14336\n"
    "s_waitcnt lgkmcnt(2)\n"
    "v_mfma_f32_16x16x32_bf16 v[136:139], a[136:139], v[68:71], 0\n"
    "ds_read_b128 a[144:147], v248 offset:4096\n"
    "v_mfma_f32_16x16x32_bf16 v[128:131], a[124:127], v[72:75], 0\n"
    "s_add_u32 s83, s67, 28672\n"
    "ds_read_b128 a[148:151], v248 offset:16384\n"
    "ds_read_b128 a[112:115], v248 offset:0\n"
    "v_mfma_f32_16x16x32_bf16 v[144:147], a[120:123], v[76:79], 0\n"
    "v_exp_f32 v64, v64\n"
    "v_mfma_f32_16x16x32_bf16 v[32:35], a[84:87], a[180:183], v[32:35]\n"
    "s_add_u32 m0, s64, 4096\n"
    "ds_read_b128 a[152:155], v248 offset:5120\n"
    "v_mfma_f32_16x16x32_bf16 v[16:19], a[52:55], a[172:175], v[16:19]\n"
    "buffer_load_dwordx4 v1, s[16:19], s83 offen offset:0 sc1 lds\n"
    "v_mfma_f32_16x16x32_bf16 v[128:131], a[120:123], v[68:71], v[128:131]\n"
    "v_exp_f32 v66, v66\n"
    "v_mfma_f32_16x16x32_bf16 v[176:179], a[120:123], v[92:95], 0\n"
    "s_waitcnt lgkmcnt(5)\n"
    "ds_read_b128 a[116:119], v248 offset:12288\n"
    "v_cvt_pk_bf16_f32 v99, v66, v67\n"
    "v_mfma_f32_16x16x32_bf16 v[132:135], a[128:131], v[68:71], 0\n"
    "v_exp_f32 v49, v49\n"
    "v_mfma_f32_16x16x32_bf16 v[28:31], a[92:95], a[164:167], v[28:31]\n"
    "s_waitcnt lgkmcnt(5)\n"
    "v_cvt_pk_bf16_f32 v96, v48, v49\n"
    "v_mfma_f32_16x16x32_bf16 v[148:151], a[132:135], v[80:83], 0\n"
    "v_and_b32 v98, 0x80008000, v98\n"
    "v_mfma_f32_16x16x32_bf16 v[164:167], a[128:131], v[84:87], 0\n"
    "v_exp_f32 v4, v4\n"
    "v_mfma_f32_16x16x32_bf16 v[24:27], a[72:75], a[216:219], v[24:27]\n"
    "buffer_load_dwordx4 v1, s[16:19], s83 offen offset:1024 sc1 lds\n"
    "v_mfma_f32_16x16x32_bf16 v[32:35], a[80:83], a[176:179], v[32:35]\n"
    "v_exp_f32 v12, v12\n"
    "v_mfma_f32_16x16x32_bf16 v[144:147], a[124:127], v[80:83], v[144:147]\n"
    "v_exp_f32 v31, v31\n"
    "v_mfma_f32_16x16x32_bf16 v[160:163], a[120:123], v[84:87], v[160:163]\n"
    "s_waitcnt lgkmcnt(2)\n"
    "v_mfma_f32_16x16x32_bf16 v[124:127], a[112:115], v[68:71], 0\n"
    "v_exp_f32 v27, v27\n"
    "v_mfma_f32_16x16x32_bf16 v[188:191], a[148:151], v[72:75], 0\n"
    "ds_read_b128 a[140:143], v248 offset:15360\n"
    "v_mfma_f32_16x16x32_bf16 v[16:19], a[48:51], a[168:171], v[16:19]\n"
    "s_waitcnt lgkmcnt(2)\n"
    "ds_read_b128 a[120:123], v248 offset:7168\n"
    "v_cmp_ne_u32 s[34:35], 0, v98\n"
    "v_mfma_f32_16x16x32_bf16 a[20:23], a[152:155], v[84:87], 0\n"
    "s_waitcnt lgkmcnt(2)\n"
    "v_exp_f32 v29, v29\n"
    "v_mfma_f32_16x16x32_bf16 v[140:143], a[116:119], v[80:83], 0\n"
    "buffer_load_dwordx4 v[204:207], v242, s[40:43], s75 offen offset:0 nt\n"
    "v_mfma_f32_16x16x32_bf16 v[188:191], a[144:147], v[68:71], v[188:191]\n"
    "v_cvt_pk_bf16_f32 v98, v64, v65\n"
    "v_mfma_f32_16x16x32_bf16 v[124:127], a[116:119], v[72:75], v[124:127]\n"
    "ds_read_b128 a[156:159], v248 offset:17408\n"
    "v_mfma_f32_16x16x32_bf16 v[132:135], a[132:135], v[72:75], v[132:135]\n"
    "v_exp_f32 v5, v5\n"
    "v_mfma_f32_16x16x32_bf16 v[192:195], a[152:155], v[68:71], 0\n"
    "buffer_load_dwordx4 v[208:211], v242, s[40:43], s75 offen offset:768 nt\n"
    "v_mfma_f32_16x16x32_bf16 v[176:179], a[124:127], v[96:99], v[176:179]\n"
    "ds_read_b128 a[124:127], v248 offset:19456\n"
    "v_mfma_f32_16x16x32_bf16 v[140:143], a[112:115], v[76:79], v[140:143]\n"
    "v_exp_f32 v26, v26\n"
    "v_mfma_f32_16x16x32_bf16 v[172:175], a[116:119], v[96:99], 0\n"
    "s_waitcnt lgkmcnt(2)\n"
    "buffer_load_dwordx4 v[212:215], v242, s[40:43], s75 offen offset:256 nt\n"
    "v_mfma_f32_16x16x32_bf16 a[12:15], a[120:123], v[76:79], 0\n"
    "v_exp_f32 v20, v20\n"
    "v_mfma_f32_16x16x32_bf16 a[0:3], a[144:147], v[76:79], 0\n"
    "v_mfma_f32_16x16x32_bf16 v[152:155], a[140:143], v[80:83], 0\n"
    "v_mfma_f32_16x16x32_bf16 v[172:175], a[112:115], v[92:95], v[172:175]\n"
    "v_exp_f32 v16, v16\n"
    "v_mfma_f32_16x16x32_bf16 a[16:19], a[148:151], v[88:91], 0\n"
    "v_exp_f32 v17, v17\n"
    "v_mfma_f32_16x16x32_bf16 v[164:167], a[132:135], v[88:91], v[164:167]\n"
    "v_exp_f32 v24, v24\n"
    "v_mfma_f32_16x16x32_bf16 v[152:155], a[136:139], v[76:79], v[152:155]\n"
    "buffer_load_dwordx4 v[216:219], v242, s[40:43], s75 offen offset:1024 nt\n"
    "v_mfma_f32_16x16x32_bf16 v[180:183], a[132:135], v[96:99], 0\n"
    "v_exp_f32 v19, v19\n"
    "v_mfma_f32_16x16x32_bf16 v[156:159], a[112:115], v[84:87], 0\n"
    "v_exp_f32 v14, v14\n"
    "v_mfma_f32_16x16x32_bf16 a[44:47], a[120:123], v[92:95], 0\n"
    "v_exp_f32 v9, v9\n"
    "v_mfma_f32_16x16x32_bf16 a[32:35], a[144:147], v[92:95], 0\n"
    "s_waitcnt lgkmcnt(1)\n"
    "buffer_load_dwordx4 v[220:223], v242, s[40:43], s75 offen offset:512 nt\n"
    "v_mfma_f32_16x16x32_bf16 a[36:39], a[156:159], v[96:99], 0\n"
    "v_mfma_f32_16x16x32_bf16 a[0:3], a[148:151], v[80:83], a[0:3]\n"
    "buffer_load_dwordx4 v[224:227], v242, s[40:43], s75 offen offset:1280 nt\n"
    "v_mfma_f32_16x16x32_bf16 a[32:35], a[148:151], v[96:99], a[32:35]\n"
    "ds_read_b128 a[132:135], v248 offset:20480\n"
    "v_mfma_f32_16x16x32_bf16 v[148:151], a[128:131], v[76:79], v[148:151]\n"
    "v_exp_f32 v25, v25\n"
    "v_mfma_f32_16x16x32_bf16 a[4:7], a[156:159], v[80:83], 0\n"
    "ds_read_b128 a[112:115], v248 offset:6144\n"
    "v_mfma_f32_16x16x32_bf16 v[180:183], a[128:131], v[92:95], v[180:183]\n"
    "ds_read_b128 a[148:151], v248 offset:22528\n"
    "v_mfma_f32_16x16x32_bf16 v[184:187], a[140:143], v[96:99], 0\n"
    "v_mfma_f32_16x16x32_bf16 v[156:159], a[116:119], v[88:91], v[156:159]\n"
    "v_exp_f32 v10, v10\n"
    "v_mfma_f32_16x16x32_bf16 v[136:139], a[140:143], v[72:75], v[136:139]\n"
    "s_waitcnt lgkmcnt(3)\n"
    "v_mfma_f32_16x16x32_bf16 v[200:203], a[124:127], v[72:75], 0\n"
    "ds_read_b128 a[116:119], v248 offset:18432\n"
    "ds_read_b128 a[128:131], v248 offset:8192\n"
    "v_mfma_f32_16x16x32_bf16 a[20:23], a[156:159], v[88:91], a[20:23]\n"
    "v_exp_f32 v7, v7\n"
    "v_mfma_f32_16x16x32_bf16 a[4:7], a[152:155], v[76:79], a[4:7]\n"
    "v_mfma_f32_16x16x32_bf16 v[192:195], a[156:159], v[72:75], v[192:195]\n"
    "v_exp_f32 v21, v21\n"
    "v_mfma_f32_16x16x32_bf16 a[36:39], a[152:155], v[92:95], a[36:39]\n"
    "ds_read_b128 a[156:159], v248 offset:23552\n"
    "v_mfma_f32_16x16x32_bf16 v[184:187], a[136:139], v[92:95], v[184:187]\n"
    "s_waitcnt lgkmcnt(5)\n"
    "v_mfma_f32_16x16x32_bf16 a[64:67], a[132:135], v[80:83], 0\n"
    "v_mfma_f32_16x16x32_bf16 v[168:171], a[136:139], v[84:87], 0\n"
    "s_waitcnt lgkmcnt(3)\n"
    "v_mfma_f32_16x16x32_bf16 a[88:91], a[148:151], v[88:91], 0\n"
    "v_mfma_f32_16x16x32_bf16 a[16:19], a[144:147], v[84:87], a[16:19]\n"
    "s_waitcnt lgkmcnt(2)\n"
    "ds_read_b128 a[152:155], v248 offset:11264\n"
    "v_mfma_f32_16x16x32_bf16 a[8:11], a[116:119], v[80:83], 0\n"
    "v_mfma_f32_16x16x32_bf16 v[168:171], a[140:143], v[88:91], v[168:171]\n"
    "v_exp_f32 v15, v15\n"
    "v_mfma_f32_16x16x32_bf16 a[28:31], a[124:127], v[88:91], 0\n"
    "s_waitcnt lgkmcnt(2)\n"
    "v_exp_f32 v11, v11\n"
    "v_mfma_f32_16x16x32_bf16 a[48:51], a[128:131], v[68:71], 0\n"
    "v_mfma_f32_16x16x32_bf16 a[56:59], a[148:151], v[72:75], 0\n"
    "v_mfma_f32_16x16x32_bf16 a[28:31], a[120:123], v[84:87], a[28:31]\n"
    "v_mfma_f32_16x16x32_bf16 a[8:11], a[112:115], v[76:79], a[8:11]\n"
    "buffer_load_dwordx2 v[236:237], v241, s[36:39], s81 offen offset:0 sc1\n"
    "v_mfma_f32_16x16x32_bf16 v[200:203], a[120:123], v[68:71], v[200:203]\n"
    "v_exp_f32 v28, v28\n"
    "v_mfma_f32_16x16x32_bf16 v[196:199], a[112:115], v[68:71], 0\n"
    "s_waitcnt lgkmcnt(1)\n"
    "v_mfma_f32_16x16x32_bf16 a[108:111], a[156:159], v[96:99], 0\n"
    "v_mfma_f32_16x16x32_bf16 v[32:35], a[76:79], a[172:175], v[32:35]\n"
    "ds_read_b128 a[136:139], v248 offset:9216\n"
    "ds_read_b128 a[140:143], v248 offset:21504\n"
    "v_mfma_f32_16x16x32_bf16 a[80:83], a[128:131], v[84:87], 0\n"
    "v_mfma_f32_16x16x32_bf16 a[24:27], a[112:115], v[84:87], 0\n"
    "v_mfma_f32_16x16x32_bf16 a[12:15], a[124:127], v[80:83], a[12:15]\n"
    "v_exp_f32 v23, v23\n"
    "v_mfma_f32_16x16x32_bf16 v[32:35], a[72:75], a[168:171], v[32:35]\n"
    "ds_read_b128 a[120:123], v3 offset:16384\n"
    "ds_read_b128 a[144:147], v248 offset:10240\n"
    "v_mfma_f32_16x16x32_bf16 a[40:43], a[116:119], v[96:99], 0\n"
    "v_exp_f32 v22, v22\n"
    "v_mfma_f32_16x16x32_bf16 a[80:83], a[132:135], v[88:91], a[80:83]\n"
    "v_mfma_f32_16x16x32_bf16 a[76:79], a[156:159], v[80:83], 0\n"
    "v_mfma_f32_16x16x32_bf16 a[40:43], a[112:115], v[92:95], a[40:43]\n"
    "s_waitcnt lgkmcnt(3)\n"
    "v_exp_f32 v32, v32\n"
    "v_mfma_f32_16x16x32_bf16 a[52:55], a[136:139], v[68:71], 0\n"
    "v_exp_f32 v33, v33\n"
    "v_mfma_f32_16x16x32_bf16 a[48:51], a[132:135], v[72:75], a[48:51]\n"
    "v_mfma_f32_16x16x32_bf16 a[92:95], a[152:155], v[84:87], 0\n"
    "s_waitcnt lgkmcnt(2)\n"
    "v_mfma_f32_16x16x32_bf16 a[68:71], a[140:143], v[80:83], 0\n"
    "v_mfma_f32_16x16x32_bf16 a[100:103], a[140:143], v[96:99], 0\n"
    "s_waitcnt lgkmcnt(0)\n"
    "ds_read_b128 v[100:103], v250 offset:896\n"
    "v_mfma_f32_16x16x32_bf16 a[88:91], a[144:147], v[84:87], a[88:91]\n"
    "v_mfma_f32_16x16x32_bf16 a[24:27], a[116:119], v[88:91], a[24:27]\n"
    "v_mfma_f32_16x16x32_bf16 a[84:87], a[136:139], v[84:87], 0\n"
    "ds_read_b128 a[112:115], v3 offset:12288\n"
    "v_mfma_f32_16x16x32_bf16 a[96:99], a[132:135], v[96:99], 0\n"
    "v_exp_f32 v34, v34\n"
    "v_mfma_f32_16x16x32_bf16 v[196:199], a[116:119], v[72:75], v[196:199]\n"
    "v_exp_f32 v6, v6\n"
    "v_mfma_f32_16x16x32_bf16 a[44:47], a[124:127], v[96:99], a[44:47]\n"
    "v_mfma_f32_16x16x32_bf16 a[108:111], a[152:155], v[92:95], a[108:111]\n"
    "v_exp_f32 v8, v8\n"
    "v_mfma_f32_16x16x32_bf16 a[76:79], a[152:155], v[76:79], a[76:79]\n"
    "v_mfma_f32_16x16x32_bf16 a[104:107], a[144:147], v[92:95], 0\n"
    "s_add_u32 s67, s67, 49152\n"
    "v_mfma_f32_16x16x32_bf16 a[56:59], a[144:147], v[68:71], a[56:59]\n"
    "v_exp_f32 v30, v30\n"
    "v_mfma_f32_16x16x32_bf16 a[96:99], a[128:131], v[92:95], a[96:99]\n"
    "ds_read_b128 a[124:127], v3 offset:18432\n"
    "v_mfma_f32_16x16x32_bf16 a[72:75], a[144:147], v[76:79], 0\n"
    "ds_read_b128 a[132:135], v3 offset:22528\n"
    "s_waitcnt vmcnt(7)\n"
    "v_mfma_f32_16x16x32_bf16 a[104:107], a[148:151], v[96:99], a[104:107]\n"
    "ds_add_u32 v247, v246\n"
    "v_mfma_f32_16x16x32_bf16 a[64:67], a[128:131], v[76:79], a[64:67]\n"
    "ds_read_b128 a[116:119], v3 offset:14336\n"
    "ds_read_b128 a[128:131], v3 offset:20480\n"
    "v_smfmac_f32_16x16x64_bf16 v[120:123], v[52:55], v[92:99], v253\n"
    "v_add_u32 v3, 49152, v252\n"
    "v_mfma_f32_16x16x32_bf16 a[60:63], a[152:155], v[68:71], 0\n"
    "v_mfma_f32_16x16x32_bf16 a[52:55], a[140:143], v[72:75], a[52:55]\n"
    "v_mfma_f32_16x16x32_bf16 a[84:87], a[140:143], v[88:91], a[84:87]\n"
    "v_exp_f32 v13, v13\n"
    "v_mfma_f32_16x16x32_bf16 a[60:63], a[156:159], v[72:75], a[60:63]\n"
    "v_exp_f32 v35, v35\n"
    "v_mfma_f32_16x16x32_bf16 a[92:95], a[156:159], v[88:91], a[92:95]\n"
    "v_exp_f32 v18, v18\n"
    "v_mfma_f32_16x16x32_bf16 a[72:75], a[148:151], v[80:83], a[72:75]\n"
    "v_mfma_f32_16x16x32_bf16 a[100:103], a[136:139], v[92:95], a[100:103]\n"
    "s_waitcnt lgkmcnt(0)\n"
    "v_mfma_f32_16x16x32_bf16 a[68:71], a[136:139], v[76:79], a[68:71]\n"
    "s_branch .LNV\n"
    ".fill ((0x20000 - (. - _Z8attn_fwdPKtPKiS2_S0_S0_S0_S0_PcPt)) & 4095) / 4, 4, 0xBF800000\n"
    ".LhVN:\n"
    "s_waitcnt vmcnt(18)\n"
    "ds_read_b128 v[52:55], v251 offset:768\n"
    "ds_read_b128 v[184:187], v97 offset:49152\n"
    "ds_read_b128 v[196:199], v96 offset:49408\n"
    "ds_read_b128 v[200:203], v97 offset:49408\n"
    "ds_read_b128 v[188:191], v96 offset:49280\n"
    "ds_read_b128 v[180:183], v96 offset:49152\n"
    "v_mov_b32 v2, v3\n"
    "ds_read_b128 v[192:195], v97 offset:49280\n"
    "s_add_u32 m0, s68, .Lend-.Lend2+(0)\n"
    "s_waitcnt lgkmcnt(6)\n"
    "v_smfmac_f32_16x16x64_bf16 v[108:111], v[52:55], v[68:75], v253\n"
    "s_waitcnt lgkmcnt(3)\n"
    "v_pk_maximum3_f16 v98, v200, v201, v98\n"
    "v_pk_maximum3_f16 v98, v202, v203, v98\n"
    "v_pk_add_u16 v200, s89, v200 clamp\n"
    "v_smfmac_f32_16x16x64_bf16 v[116:119], v[52:55], v[84:91], v253\n"
    "s_add_u32 s83, s67, 4096\n"
    "v_pk_add_u16 v201, s89, v201 clamp\n"
    "v_pk_add_u16 v202, s89, v202 clamp\n"
    "v_pk_add_u16 v203, s89, v203 clamp\n"
    "v_pk_maximum3_f16 v98, v196, v197, v98\n"
    "v_pk_maximum3_f16 v98, v198, v199, v98\n"
    "v_mfma_f32_16x16x32_bf16 v[48:51], a[20:23], v[200:203], v[48:51]\n"
    "v_pk_add_u16 v197, s89, v197 clamp\n"
    "v_pk_add_u16 v198, s89, v198 clamp\n"
    "v_mfma_f32_16x16x32_bf16 v[64:67], a[44:47], v[200:203], v[64:67]\n"
    "buffer_load_dwordx4 v1, s[16:19], s67 offen offset:0 sc1 lds\n"
    "v_mfma_f32_16x16x32_bf16 v[16:19], a[132:135], v[200:203], v[16:19]\n"
    "v_pk_add_u16 v199, s89, v199 clamp\n"
    "v_pk_add_u16 v196, s89, v196 clamp\n"
    "v_mfma_f32_16x16x32_bf16 v[32:35], a[156:159], v[200:203], v[32:35]\n"
    "s_waitcnt lgkmcnt(0)\n"
    "v_pk_maximum3_f16 v98, v192, v193, v98\n"
    "v_pk_maximum3_f16 v98, v194, v195, v98\n"
    "v_pk_add_u16 v194, s89, v194 clamp\n"
    "v_mfma_f32_16x16x32_bf16 v[32:35], a[152:155], v[196:199], v[32:35]\n"
    "v_pk_add_u16 v192, s89, v192 clamp\n"
    "v_pk_add_u16 v193, s89, v193 clamp\n"
    "v_mfma_f32_16x16x32_bf16 v[64:67], a[40:43], v[196:199], v[64:67]\n"
    "v_pk_maximum3_f16 v98, v188, v189, v98\n"
    "v_pk_add_u16 v195, s89, v195 clamp\n"
    "v_mfma_f32_16x16x32_bf16 v[16:19], a[128:131], v[196:199], v[16:19]\n"
    "v_pk_add_u16 v188, s89, v188 clamp\n"
    "v_pk_maximum3_f16 v98, v190, v191, v98\n"
    "v_mfma_f32_16x16x32_bf16 v[32:35], a[148:151], v[192:195], v[32:35]\n"
    "buffer_load_dwordx4 v1, s[16:19], s67 offen offset:1024 sc1 lds\n"
    "v_mfma_f32_16x16x32_bf16 v[64:67], a[36:39], v[192:195], v[64:67]\n"
    "v_pk_maximum3_f16 v98, v184, v185, v98\n"
    "v_pk_add_u16 v189, s89, v189 clamp\n"
    "v_mfma_f32_16x16x32_bf16 v[48:51], a[16:19], v[196:199], v[48:51]\n"
    "v_pk_add_u16 v185, s89, v185 clamp\n"
    "v_pk_add_u16 v191, s89, v191 clamp\n"
    "v_pk_add_u16 v190, s89, v190 clamp\n"
    "v_mfma_f32_16x16x32_bf16 v[48:51], a[12:15], v[192:195], v[48:51]\n"
    "v_pk_maximum3_f16 v98, v186, v187, v98\n"
    "v_pk_add_u16 v186, s89, v186 clamp\n"
    "v_mfma_f32_16x16x32_bf16 v[16:19], a[124:127], v[192:195], v[16:19]\n"
    "v_pk_maximum3_f16 v98, v180, v181, v98\n"
    "v_pk_add_u16 v187, s89, v187 clamp\n"
    "v_mfma_f32_16x16x32_bf16 v[16:19], a[120:123], v[188:191], v[16:19]\n"
    "v_pk_add_u16 v180, s89, v180 clamp\n"
    "v_pk_add_u16 v184, s89, v184 clamp\n"
    "v_mfma_f32_16x16x32_bf16 v[64:67], a[32:35], v[188:191], v[64:67]\n"
    "v_accvgpr_write_b32 a177, v189\n"
    "v_accvgpr_write_b32 a190, v202\n"
    "v_mfma_f32_16x16x32_bf16 v[32:35], a[144:147], v[188:191], v[32:35]\n"
    "s_waitcnt vmcnt(14)\n"
    "buffer_load_dwordx4 v1, s[16:19], s67 offen offset:2048 sc1 lds\n"
    "v_smfmac_f32_16x16x64_bf16 v[112:115], v[52:55], v[76:83], v253\n"
    "s_barrier\n"
    "ds_read_b128 a[48:51], v252 offset:24576\n"
    "v_mfma_f32_16x16x32_bf16 v[48:51], a[8:11], v[188:191], v[48:51]\n"
    "ds_read_b128 a[68:71], v252 offset:34816\n"
    "ds_read_b128 a[72:75], v252 offset:25600\n"
    "v_mfma_f32_16x16x32_bf16 v[32:35], a[140:143], v[184:187], v[32:35]\n"
    "v_pk_add_u16 v181, s89, v181 clamp\n"
    "v_accvgpr_write_b32 a188, v200\n"
    "v_mfma_f32_16x16x32_bf16 v[48:51], a[4:7], v[184:187], v[48:51]\n"
    "v_pk_maximum3_f16 v98, v182, v183, v98\n"
    "v_accvgpr_write_b32 a176, v188\n"
    "v_mfma_f32_16x16x32_bf16 v[64:67], a[28:31], v[184:187], v[64:67]\n"
    "v_pk_add_u16 v183, s89, v183 clamp\n"
    "v_pk_add_u16 v182, s89, v182 clamp\n"
    "v_mfma_f32_16x16x32_bf16 v[16:19], a[116:119], v[184:187], v[16:19]\n"
    "ds_read_b128 a[52:55], v252 offset:26624\n"
    "ds_read_b128 a[76:79], v252 offset:27648\n"
    "v_mfma_f32_16x16x32_bf16 v[48:51], a[0:3], v[180:183], v[48:51]\n"
    "buffer_load_dwordx4 v1, s[16:19], s67 offen offset:3072 sc1 lds\n"
    "v_mfma_f32_16x16x32_bf16 v[32:35], a[136:139], v[180:183], v[32:35]\n"
    "ds_read_b128 a[92:95], v252 offset:35840\n"
    "ds_read_b128 a[88:91], v252 offset:33792\n"
    "v_mfma_f32_16x16x32_bf16 v[16:19], a[112:115], v[180:183], v[16:19]\n"
    "s_waitcnt lgkmcnt(6)\n"
    "v_accvgpr_write_b32 a180, v192\n"
    "v_accvgpr_write_b32 a181, v193\n"
    "v_mfma_f32_16x16x32_bf16 v[4:7], a[48:51], a[192:195], v[100:103]\n"
    "s_waitcnt lgkmcnt(4)\n"
    "ds_read_b128 a[60:63], v252 offset:30720\n"
    "ds_read_b128 a[56:59], v252 offset:28672\n"
    "v_mfma_f32_16x16x32_bf16 v[20:23], a[72:75], a[192:195], v[104:107]\n"
    "ds_read_b128 a[64:67], v252 offset:32768\n"
    "v_accvgpr_write_b32 a191, v203\n"
    "v_mfma_f32_16x16x32_bf16 v[28:31], a[72:75], a[240:243], v[104:107]\n"
    "s_add_u32 m0, s68, 4096\n"
    "v_accvgpr_write_b32 a175, v187\n"
    "v_accvgpr_write_b32 a182, v194\n"
    "v_mfma_f32_16x16x32_bf16 v[12:15], a[48:51], a[240:243], v[100:103]\n"
    "v_accvgpr_write_b32 a178, v190\n"
    "v_accvgpr_write_b32 a171, v183\n"
    "v_mfma_f32_16x16x32_bf16 v[8:11], a[68:71], a[236:239], v[100:103]\n"
    "buffer_load_dwordx4 v1, s[16:19], s83 offen offset:0 sc1 lds\n"
    "v_mfma_f32_16x16x32_bf16 v[64:67], a[24:27], v[180:183], v[64:67]\n"
    "s_waitcnt lgkmcnt(6)\n"
    "ds_read_b128 a[80:83], v252 offset:29696\n"
    "v_accvgpr_write_b32 a172, v184\n"
    "v_mfma_f32_16x16x32_bf16 v[4:7], a[52:55], a[196:199], v[4:7]\n"
    "s_waitcnt lgkmcnt(5)\n"
    "v_exp_f32 v16, v16\n"
    "v_mfma_f32_16x16x32_bf16 v[24:27], a[92:95], a[236:239], v[104:107]\n"
    "s_waitcnt lgkmcnt(4)\n"
    "v_accvgpr_write_b32 a174, v186\n"
    "v_accvgpr_write_b32 a169, v181\n"
    "v_mfma_f32_16x16x32_bf16 v[24:27], a[88:91], a[232:235], v[24:27]\n"
    "s_waitcnt lgkmcnt(2)\n"
    "ds_read_b128 a[84:87], v252 offset:31744\n"
    "v_mfma_f32_16x16x32_bf16 v[4:7], a[56:59], a[200:203], v[4:7]\n"
    "s_waitcnt lgkmcnt(2)\n"
    "v_accvgpr_write_b32 a189, v201\n"
    "v_accvgpr_write_b32 a179, v191\n"
    "v_mfma_f32_16x16x32_bf16 v[8:11], a[64:67], a[232:235], v[8:11]\n"
    "v_exp_f32 v17, v17\n"
    "v_mfma_f32_16x16x32_bf16 v[28:31], a[76:79], a[244:247], v[28:31]\n"
    "buffer_load_dwordx4 v1, s[16:19], s83 offen offset:1024 sc1 lds\n"
    "v_mfma_f32_16x16x32_bf16 v[20:23], a[76:79], a[196:199], v[20:23]\n"
    "v_accvgpr_write_b32 a184, v196\n"
    "v_accvgpr_write_b32 a170, v182\n"
    "v_mfma_f32_16x16x32_bf16 v[12:15], a[52:55], a[244:247], v[12:15]\n"
    "v_accvgpr_write_b32 a185, v197\n"
    "v_accvgpr_write_b32 a186, v198\n"
    "v_mfma_f32_16x16x32_bf16 v[12:15], a[56:59], a[248:251], v[12:15]\n"
    "v_cvt_pk_bf16_f32 v92, v16, v17\n"
    "v_mfma_f32_16x16x32_bf16 v[12:15], a[60:63], a[252:255], v[12:15]\n"
    "s_add_u32 m0, s70, 0\n"
    "s_add_u32 s83, s67, 24576\n"
    "v_accvgpr_write_b32 a168, v180\n"
    "v_mfma_f32_16x16x32_bf16 v[12:15], a[64:67], a[160:163], v[12:15]\n"
    "s_waitcnt lgkmcnt(1)\n"
    "v_accvgpr_write_b32 a183, v195\n"
    "v_pk_add_u16 v98, s89, v98\n"
    "v_mfma_f32_16x16x32_bf16 v[28:31], a[80:83], a[248:251], v[28:31]\n"
    "s_waitcnt lgkmcnt(0)\n"
    "v_accvgpr_write_b32 a173, v185\n"
    "v_accvgpr_write_b32 a187, v199\n"
    "v_mfma_f32_16x16x32_bf16 v[28:31], a[84:87], a[252:255], v[28:31]\n"
    "buffer_load_dwordx4 v1, s[16:19], s83 offen offset:0 sc1 lds\n"
    "v_mfma_f32_16x16x32_bf16 v[4:7], a[60:63], a[204:207], v[4:7]\n"
    "v_exp_f32 v18, v18\n"
    "v_mfma_f32_16x16x32_bf16 v[8:11], a[60:63], a[228:231], v[8:11]\n"
    "v_exp_f32 v19, v19\n"
    "v_mfma_f32_16x16x32_bf16 v[8:11], a[56:59], a[224:227], v[8:11]\n"
    "v_exp_f32 v32, v32\n"
    "v_mfma_f32_16x16x32_bf16 v[8:11], a[52:55], a[220:223], v[8:11]\n"
    "v_exp_f32 v35, v35\n"
    "v_mfma_f32_16x16x32_bf16 v[28:31], a[88:91], a[160:163], v[28:31]\n"
    "v_cvt_pk_bf16_f32 v93, v18, v19\n"
    "v_mfma_f32_16x16x32_bf16 v[24:27], a[84:87], a[228:231], v[24:27]\n"
    "v_exp_f32 v34, v34\n"
    "v_mfma_f32_16x16x32_bf16 v[20:23], a[80:83], a[200:203], v[20:23]\n"
    "v_exp_f32 v67, v67\n"
    "v_mfma_f32_16x16x32_bf16 v[16:19], a[68:71], a[188:191], v[100:103]\n"
    "buffer_load_dwordx4 v1, s[16:19], s83 offen offset:1024 sc1 lds\n"
    "v_mfma_f32_16x16x32_bf16 v[4:7], a[64:67], a[208:211], v[4:7]\n"
    "v_exp_f32 v33, v33\n"
    "v_mfma_f32_16x16x32_bf16 v[20:23], a[84:87], a[204:207], v[20:23]\n"
    "v_mfma_f32_16x16x32_bf16 v[20:23], a[88:91], a[208:211], v[20:23]\n"
    "v_mfma_f32_16x16x32_bf16 v[24:27], a[80:83], a[224:227], v[24:27]\n"
    "v_cvt_pk_bf16_f32 v95, v34, v35\n"
    "v_cvt_pk_bf16_f32 v94, v32, v33\n"
    "v_mfma_f32_16x16x32_bf16 v[32:35], a[92:95], a[188:191], v[104:107]\n"
    "s_waitcnt vmcnt(14)\n"
    "v_exp_f32 v66, v66\n"
    "v_mfma_f32_16x16x32_bf16 v[16:19], a[64:67], a[184:187], v[16:19]\n"
    "s_barrier\n"
    "ds_read_b128 a[112:115], v249 offset:0\n"
    "v_mfma_f32_16x16x32_bf16 v[32:35], a[88:91], a[184:187], v[32:35]\n"
    "v_exp_f32 v51, v51\n"
    "v_mfma_f32_16x16x32_bf16 v[32:35], a[84:87], a[180:183], v[32:35]\n"
    "buffer_load_dwordx4 v1, s[16:19], s83 offen offset:2048 sc1 lds\n"
    "v_mfma_f32_16x16x32_bf16 v[16:19], a[60:63], a[180:183], v[16:19]\n"
    "ds_read_b128 a[144:147], v249 offset:4096\n"
    "ds_read_b128 a[140:143], v249 offset:15360\n"
    "v_mfma_f32_16x16x32_bf16 v[16:19], a[56:59], a[176:179], v[16:19]\n"
    "ds_read_b128 a[152:155], v249 offset:5120\n"
    "ds_read_b128 a[132:135], v249 offset:14336\n"
    "v_mfma_f32_16x16x32_bf16 v[4:7], a[68:71], a[212:215], v[4:7]\n"
    "ds_read_b128 a[120:123], v249 offset:1024\n"
    "ds_read_b128 a[128:131], v249 offset:2048\n"
    "v_mfma_f32_16x16x32_bf16 v[8:11], a[48:51], a[216:219], v[8:11]\n"
    "ds_read_b128 a[124:127], v249 offset:13312\n"
    "ds_read_b128 a[136:139], v249 offset:3072\n"
    "v_mfma_f32_16x16x32_bf16 v[24:27], a[76:79], a[220:223], v[24:27]\n"
    "v_exp_f32 v65, v65\n"
    "v_mfma_f32_16x16x32_bf16 v[12:15], a[68:71], a[164:167], v[12:15]\n"
    "ds_read_b128 a[116:119], v249 offset:12288\n"
    "v_mfma_f32_16x16x32_bf16 v[32:35], a[80:83], a[176:179], v[32:35]\n"
    "buffer_load_dwordx4 v1, s[16:19], s83 offen offset:3072 sc1 lds\n"
    "v_mfma_f32_16x16x32_bf16 v[20:23], a[92:95], a[212:215], v[20:23]\n"
    "v_mfma_f32_16x16x32_bf16 v[16:19], a[52:55], a[172:175], v[16:19]\n"
    "s_waitcnt lgkmcnt(7)\n"
    "v_exp_f32 v50, v50\n"
    "v_mfma_f32_16x16x32_bf16 v[152:155], a[140:143], v[80:83], 0\n"
    "v_and_b32 v98, 0x80008000, v98\n"
    "v_mfma_f32_16x16x32_bf16 v[24:27], a[72:75], a[216:219], v[24:27]\n"
    "s_add_u32 m0, s70, 4096\n"
    "s_add_u32 s83, s67, 28672\n"
    "s_waitcnt lgkmcnt(2)\n"
    "v_mfma_f32_16x16x32_bf16 v[128:131], a[124:127], v[72:75], 0\n"
    "ds_read_b128 a[148:151], v249 offset:16384\n"
    "v_mfma_f32_16x16x32_bf16 v[148:151], a[132:135], v[80:83], 0\n"
    "s_add_u32 s76, s52, s60\n"
    "v_mfma_f32_16x16x32_bf16 v[28:31], a[92:95], a[164:167], v[28:31]\n"
    "buffer_load_dwordx4 v1, s[16:19], s83 offen offset:0 sc1 lds\n"
    "v_mfma_f32_16x16x32_bf16 v[160:163], a[124:127], v[88:91], 0\n"
    "s_waitcnt lgkmcnt(2)\n"
    "v_exp_f32 v48, v48\n"
    "v_mfma_f32_16x16x32_bf16 v[136:139], a[136:139], v[68:71], 0\n"
    "v_exp_f32 v49, v49\n"
    "v_mfma_f32_16x16x32_bf16 a[32:35], a[144:147], v[92:95], 0\n"
    "v_exp_f32 v27, v27\n"
    "v_mfma_f32_16x16x32_bf16 v[136:139], a[140:143], v[72:75], v[136:139]\n"
    "v_cmp_ne_u32 s[34:35], 0, v98\n"
    "v_mfma_f32_16x16x32_bf16 v[156:159], a[112:115], v[84:87], 0\n"
    "v_mfma_f32_16x16x32_bf16 v[132:135], a[128:131], v[68:71], 0\n"
    "v_cvt_pk_bf16_f32 v96, v48, v49\n"
    "v_mfma_f32_16x16x32_bf16 v[144:147], a[120:123], v[76:79], 0\n"
    "buffer_load_dwordx4 v1, s[16:19], s83 offen offset:1024 sc1 lds\n"
    "v_mfma_f32_16x16x32_bf16 v[16:19], a[48:51], a[168:171], v[16:19]\n"
    "s_waitcnt lgkmcnt(0)\n"
    "v_exp_f32 v20, v20\n"
    "v_mfma_f32_16x16x32_bf16 a[16:19], a[148:151], v[88:91], 0\n"
    "v_exp_f32 v64, v64\n"
    "v_mfma_f32_16x16x32_bf16 v[176:179], a[120:123], v[92:95], 0\n"
    "v_mfma_f32_16x16x32_bf16 v[124:127], a[112:115], v[68:71], 0\n"
    "v_exp_f32 v4, v4\n"
    "v_mfma_f32_16x16x32_bf16 v[156:159], a[116:119], v[88:91], v[156:159]\n"
    "v_exp_f32 v7, v7\n"
    "v_mfma_f32_16x16x32_bf16 v[188:191], a[148:151], v[72:75], 0\n"
    "v_exp_f32 v10, v10\n"
    "v_mfma_f32_16x16x32_bf16 v[32:35], a[76:79], a[172:175], v[32:35]\n"
    "v_mfma_f32_16x16x32_bf16 v[192:195], a[152:155], v[68:71], 0\n"
    "s_add_u32 s78, s52, s62\n"
    "v_exp_f32 v11, v11\n"
    "v_mfma_f32_16x16x32_bf16 v[124:127], a[116:119], v[72:75], v[124:127]\n"
    "s_waitcnt vmcnt(12)\n"
    "v_cvt_pk_bf16_f32 v97, v50, v51\n"
    "v_cvt_pk_bf16_f32 v99, v66, v67\n"
    "v_mfma_f32_16x16x32_bf16 v[164:167], a[128:131], v[84:87], 0\n"
    "v_exp_f32 v19, v19\n"
    "v_mfma_f32_16x16x32_bf16 v[160:163], a[120:123], v[84:87], v[160:163]\n"
    "v_cvt_pk_u16_u32 v204, v204, v208\n"
    "v_cvt_pk_bf16_f32 v98, v64, v65\n"
    "v_mfma_f32_16x16x32_bf16 a[20:23], a[152:155], v[84:87], 0\n"
    "v_cvt_pk_u16_u32 v214, v214, v218\n"
    "v_mfma_f32_16x16x32_bf16 v[148:151], a[128:131], v[76:79], v[148:151]\n"
    "v_cvt_pk_u16_u32 v207, v207, v211\n"
    "v_cvt_pk_u16_u32 v205, v205, v209\n"
    "v_mfma_f32_16x16x32_bf16 v[128:131], a[120:123], v[68:71], v[128:131]\n"
    "ds_read_b128 a[156:159], v249 offset:17408\n"
    "v_mfma_f32_16x16x32_bf16 v[172:175], a[116:119], v[96:99], 0\n"
    "s_add_u32 s77, s52, s61\n"
    "v_exp_f32 v25, v25\n"
    "v_mfma_f32_16x16x32_bf16 v[132:135], a[132:135], v[72:75], v[132:135]\n"
    "v_mfma_f32_16x16x32_bf16 v[144:147], a[124:127], v[80:83], v[144:147]\n"
    "v_mfma_f32_16x16x32_bf16 v[180:183], a[132:135], v[96:99], 0\n"
    "v_exp_f32 v17, v17\n"
    "v_mfma_f32_16x16x32_bf16 v[168:171], a[136:139], v[84:87], 0\n"
    "v_exp_f32 v9, v9\n"
    "v_mfma_f32_16x16x32_bf16 v[164:167], a[132:135], v[88:91], v[164:167]\n"
    "v_mfma_f32_16x16x32_bf16 v[152:155], a[136:139], v[76:79], v[152:155]\n"
    "v_exp_f32 v18, v18\n"
    "v_mfma_f32_16x16x32_bf16 v[176:179], a[124:127], v[96:99], v[176:179]\n"
    "ds_read_b128 a[132:135], v249 offset:20480\n"
    "v_mfma_f32_16x16x32_bf16 v[140:143], a[116:119], v[80:83], 0\n"
    "v_exp_f32 v31, v31\n"
    "v_mfma_f32_16x16x32_bf16 v[188:191], a[144:147], v[68:71], v[188:191]\n"
    "v_mfma_f32_16x16x32_bf16 v[168:171], a[140:143], v[88:91], v[168:171]\n"
    "v_exp_f32 v12, v12\n"
    "v_mfma_f32_16x16x32_bf16 v[180:183], a[128:131], v[92:95], v[180:183]\n"
    "v_cvt_pk_u16_u32 v212, v212, v216\n"
    "v_cvt_pk_u16_u32 v223, v223, v227\n"
    "v_mfma_f32_16x16x32_bf16 v[140:143], a[112:115], v[76:79], v[140:143]\n"
    "s_waitcnt lgkmcnt(1)\n"
    "v_exp_f32 v21, v21\n"
    "v_mfma_f32_16x16x32_bf16 a[36:39], a[156:159], v[96:99], 0\n"
    "ds_read_b128 a[128:131], v249 offset:8192\n"
    "v_cvt_pk_u16_u32 v222, v222, v226\n"
    "v_mfma_f32_16x16x32_bf16 v[172:175], a[112:115], v[92:95], v[172:175]\n"
    "v_or3_b32 v243, v204, v205, v243\n"
    "s_waitcnt lgkmcnt(1)\n"
    "v_mfma_f32_16x16x32_bf16 a[96:99], a[132:135], v[96:99], 0\n"
    "v_mfma_f32_16x16x32_bf16 a[20:23], a[156:159], v[88:91], a[20:23]\n"
    "ds_read_b128 a[116:119], v249 offset:18432\n"
    "v_mfma_f32_16x16x32_bf16 a[16:19], a[144:147], v[84:87], a[16:19]\n"
    "v_exp_f32 v15, v15\n"
    "v_mfma_f32_16x16x32_bf16 a[0:3], a[144:147], v[76:79], 0\n"
    "ds_read_b128 a[120:123], v249 offset:7168\n"
    "v_mfma_f32_16x16x32_bf16 v[184:187], a[140:143], v[96:99], 0\n"
    "v_exp_f32 v26, v26\n"
    "v_mfma_f32_16x16x32_bf16 a[64:67], a[132:135], v[80:83], 0\n"
    "v_cvt_pk_u16_u32 v206, v206, v210\n"
    "v_mfma_f32_16x16x32_bf16 a[36:39], a[152:155], v[92:95], a[36:39]\n"
    "ds_read_b128 a[124:127], v249 offset:19456\n"
    "ds_read_b128 a[140:143], v249 offset:21504\n"
    "v_mfma_f32_16x16x32_bf16 a[0:3], a[148:151], v[80:83], a[0:3]\n"
    "v_cvt_pk_u16_u32 v220, v220, v224\n"
    "v_cvt_pk_u16_u32 v221, v221, v225\n"
    "v_mfma_f32_16x16x32_bf16 v[184:187], a[136:139], v[92:95], v[184:187]\n"
    "s_waitcnt lgkmcnt(4)\n"
    "v_or3_b32 v243, v206, v207, v243\n"
    "v_cvt_pk_u16_u32 v213, v213, v217\n"
    "v_mfma_f32_16x16x32_bf16 a[80:83], a[128:131], v[84:87], 0\n"
    "s_add_u32 s67, s67, 49152\n"
    "ds_read_b128 a[136:139], v249 offset:9216\n"
    "ds_read_b128 a[144:147], v249 offset:10240\n"
    "v_mfma_f32_16x16x32_bf16 a[64:67], a[128:131], v[76:79], a[64:67]\n"
    "v_exp_f32 v23, v23\n"
    "v_mfma_f32_16x16x32_bf16 a[32:35], a[148:151], v[96:99], a[32:35]\n"
    "v_mfma_f32_16x16x32_bf16 a[4:7], a[156:159], v[80:83], 0\n"
    "v_mfma_f32_16x16x32_bf16 a[4:7], a[152:155], v[76:79], a[4:7]\n"
    "v_or3_b32 v243, v212, v213, v243\n"
    "v_cvt_pk_u16_u32 v215, v215, v219\n"
    "v_mfma_f32_16x16x32_bf16 v[192:195], a[156:159], v[72:75], v[192:195]\n"
    "v_exp_f32 v28, v28\n"
    "v_mfma_f32_16x16x32_bf16 a[96:99], a[128:131], v[92:95], a[96:99]\n"
    "s_waitcnt lgkmcnt(3)\n"
    "ds_read_b128 a[112:115], v249 offset:6144\n"
    "ds_read_b128 a[148:151], v249 offset:22528\n"
    "v_mfma_f32_16x16x32_bf16 v[200:203], a[124:127], v[72:75], 0\n"
    "v_exp_f32 v16, v16\n"
    "v_mfma_f32_16x16x32_bf16 a[12:15], a[120:123], v[76:79], 0\n"
    "buffer_load_dwordx2 v[236:237], v241, s[36:39], s81 offen offset:0 sc1\n"
    "v_mfma_f32_16x16x32_bf16 v[200:203], a[120:123], v[68:71], v[200:203]\n"
    "s_waitcnt lgkmcnt(4)\n"
    "v_add_u32 v251, 1536, v251\n"
    "v_mfma_f32_16x16x32_bf16 a[100:103], a[140:143], v[96:99], 0\n"
    "v_exp_f32 v29, v29\n"
    "v_mfma_f32_16x16x32_bf16 a[28:31], a[124:127], v[88:91], 0\n"
    "buffer_store_dword v204, v244, s[20:23], s76 offen offset:0 sc1\n"
    "v_exp_f32 v30, v30\n"
    "v_mfma_f32_16x16x32_bf16 v[32:35], a[72:75], a[168:171], v[32:35]\n"
    "v_mfma_f32_16x16x32_bf16 a[40:43], a[116:119], v[96:99], 0\n"
    "v_exp_f32 v13, v13\n"
    "v_mfma_f32_16x16x32_bf16 a[68:71], a[140:143], v[80:83], 0\n"
    "s_waitcnt lgkmcnt(3)\n"
    "v_mfma_f32_16x16x32_bf16 a[52:55], a[136:139], v[68:71], 0\n"
    "buffer_store_dword v205, v244, s[20:23], s76 offen offset:1024 sc1\n"
    "v_exp_f32 v22, v22\n"
    "v_mfma_f32_16x16x32_bf16 a[84:87], a[136:139], v[84:87], 0\n"
    "v_exp_f32 v24, v24\n"
    "v_mfma_f32_16x16x32_bf16 a[12:15], a[124:127], v[80:83], a[12:15]\n"
    "ds_read_b128 a[156:159], v249 offset:23552\n"
    "v_mfma_f32_16x16x32_bf16 a[44:47], a[120:123], v[92:95], 0\n"
    "v_exp_f32 v35, v35\n"
    "v_mfma_f32_16x16x32_bf16 a[80:83], a[132:135], v[88:91], a[80:83]\n"
    "buffer_store_dword v206, v244, s[20:23], s76 offen offset:2048 sc1\n"
    "ds_read_b128 v[100:103], v250 offset:1664\n"
    "v_mfma_f32_16x16x32_bf16 a[84:87], a[140:143], v[88:91], a[84:87]\n"
    "v_mfma_f32_16x16x32_bf16 a[8:11], a[116:119], v[80:83], 0\n"
    "s_waitcnt lgkmcnt(2)\n"
    "buffer_store_dword v207, v244, s[20:23], s76 offen offset:3072 sc1\n"
    "v_or3_b32 v243, v214, v215, v243\n"
    "v_or3_b32 v243, v220, v221, v243\n"
    "v_mfma_f32_16x16x32_bf16 a[88:91], a[148:151], v[88:91], 0\n"
    "v_mfma_f32_16x16x32_bf16 a[44:47], a[124:127], v[96:99], a[44:47]\n"
    "buffer_store_dword v212, v244, s[20:23], s77 offen offset:0 sc1\n"
    "v_mfma_f32_16x16x32_bf16 a[28:31], a[120:123], v[84:87], a[28:31]\n"
    "v_mfma_f32_16x16x32_bf16 a[24:27], a[112:115], v[84:87], 0\n"
    "ds_read_b128 a[152:155], v249 offset:11264\n"
    "v_mfma_f32_16x16x32_bf16 a[40:43], a[112:115], v[92:95], a[40:43]\n"
    "buffer_store_dword v213, v244, s[20:23], s77 offen offset:1024 sc1\n"
    "v_exp_f32 v6, v6\n"
    "v_mfma_f32_16x16x32_bf16 a[48:51], a[128:131], v[68:71], 0\n"
    "s_waitcnt lgkmcnt(2)\n"
    "v_exp_f32 v8, v8\n"
    "v_mfma_f32_16x16x32_bf16 a[76:79], a[156:159], v[80:83], 0\n"
    "buffer_store_dword v214, v244, s[20:23], s77 offen offset:2048 sc1\n"
    "v_mfma_f32_16x16x32_bf16 a[104:107], a[144:147], v[92:95], 0\n"
    "v_exp_f32 v34, v34\n"
    "v_mfma_f32_16x16x32_bf16 a[52:55], a[140:143], v[72:75], a[52:55]\n"
    "buffer_store_dword v215, v244, s[20:23], s77 offen offset:3072 sc1\n"
    "v_exp_f32 v14, v14\n"
    "v_mfma_f32_16x16x32_bf16 a[48:51], a[132:135], v[72:75], a[48:51]\n"
    "v_mfma_f32_16x16x32_bf16 a[24:27], a[116:119], v[88:91], a[24:27]\n"
    "buffer_store_dword v220, v244, s[20:23], s78 offen offset:0 sc1\n"
    "ds_read_b128 a[120:123], v3 offset:16384\n"
    "v_mfma_f32_16x16x32_bf16 v[196:199], a[112:115], v[68:71], 0\n"
    "v_mfma_f32_16x16x32_bf16 v[196:199], a[116:119], v[72:75], v[196:199]\n"
    "v_mfma_f32_16x16x32_bf16 a[8:11], a[112:115], v[76:79], a[8:11]\n"
    "s_waitcnt lgkmcnt(1)\n"
    "ds_read_b128 a[132:135], v3 offset:22528\n"
    "v_mfma_f32_16x16x32_bf16 a[76:79], a[152:155], v[76:79], a[76:79]\n"
    "buffer_store_dword v221, v244, s[20:23], s78 offen offset:1024 sc1\n"
    "v_mfma_f32_16x16x32_bf16 a[108:111], a[156:159], v[96:99], 0\n"
    "ds_read_b128 a[124:127], v3 offset:18432\n"
    "ds_read_b128 a[128:131], v3 offset:20480\n"
    "v_mfma_f32_16x16x32_bf16 a[92:95], a[152:155], v[84:87], 0\n"
    "v_or3_b32 v243, v222, v223, v243\n"
    "v_mfma_f32_16x16x32_bf16 a[92:95], a[156:159], v[88:91], a[92:95]\n"
    "ds_read_b128 a[112:115], v3 offset:12288\n"
    "ds_read_b128 a[116:119], v3 offset:14336\n"
    "v_mfma_f32_16x16x32_bf16 a[72:75], a[144:147], v[76:79], 0\n"
    "s_waitcnt vmcnt(11)\n"
    "ds_add_u32 v247, v246\n"
    "v_mfma_f32_16x16x32_bf16 a[56:59], a[148:151], v[72:75], 0\n"
    "v_exp_f32 v32, v32\n"
    "v_mfma_f32_16x16x32_bf16 a[60:63], a[152:155], v[68:71], 0\n"
    "v_and_b32 v208, 0xff00ff00, v243\n"
    "v_add_u32 v250, 1536, v250\n"
    "v_mfma_f32_16x16x32_bf16 a[68:71], a[136:139], v[76:79], a[68:71]\n"
    "v_exp_f32 v33, v33\n"
    "v_mfma_f32_16x16x32_bf16 a[88:91], a[144:147], v[84:87], a[88:91]\n"
    "buffer_store_dword v222, v244, s[20:23], s78 offen offset:2048 sc1\n"
    "v_exp_f32 v5, v5\n"
    "v_mfma_f32_16x16x32_bf16 a[72:75], a[148:151], v[80:83], a[72:75]\n"
    "v_cmp_ne_u32 vcc, 0, v208\n"
    "v_mfma_f32_16x16x32_bf16 a[104:107], a[148:151], v[96:99], a[104:107]\n"
    "v_mfma_f32_16x16x32_bf16 a[108:111], a[152:155], v[92:95], a[108:111]\n"
    "buffer_store_dword v223, v244, s[20:23], s78 offen offset:3072 sc1\n"
    "v_add_u32 v3, 49152, v252\n"
    "v_mfma_f32_16x16x32_bf16 a[56:59], a[144:147], v[68:71], a[56:59]\n"
    "s_waitcnt lgkmcnt(1)\n"
    "v_cndmask_b32 v208, 2.0, v241, vcc\n"
    "v_mfma_f32_16x16x32_bf16 a[60:63], a[156:159], v[72:75], a[60:63]\n"
    "v_mfma_f32_16x16x32_bf16 a[100:103], a[136:139], v[92:95], a[100:103]\n"
    "buffer_store_dwordx2 v[238:239], v208, s[36:39], 0 offen offset:0 sc1\n"
    "v_smfmac_f32_16x16x64_bf16 v[120:123], v[52:55], v[92:99], v253\n"
    "s_branch .LPV\n"
    ".fill ((0x20000 - (.LwKe - .LwK) - (. - _Z8attn_fwdPKtPKiS2_S0_S0_S0_S0_PcPt)) & 4095) / 4, 4, 0xBF800000\n"
    ".LwK:\n"
    "s_branch .LwKe\n"
    ".LPK:\n"
    "v_mfma_f32_16x16x32_bf16 v[36:39], a[112:115], a[192:195], v[100:103]\n"
    "s_add_u32 s71, s71, 98304\n"
    "ds_read_b128 a[136:139], v2 offset:13312\n"
    "v_cvt_pk_bf16_f32 v68, v4, v5\n"
    "v_mfma_f32_16x16x32_bf16 v[36:39], a[116:119], a[196:199], v[36:39]\n"
    "s_add_u32 s75, s71, s63\n"
    "ds_read_b128 a[140:143], v2 offset:15360\n"
    "v_cvt_pk_bf16_f32 v69, v6, v7\n"
    "v_mfma_f32_16x16x32_bf16 v[36:39], a[120:123], a[200:203], v[36:39]\n"
    "s_add_u32 s76, s75, 6144\n"
    "ds_read_b128 a[144:147], v2 offset:17408\n"
    "v_cvt_pk_bf16_f32 v76, v8, v9\n"
    "s_nop 0\n"
    "v_mfma_f32_16x16x32_bf16 v[36:39], a[124:127], a[204:207], v[36:39]\n"
    "s_add_u32 s77, s75, 24576\n"
    "ds_read_b128 a[148:151], v2 offset:19456\n"
    "v_cvt_pk_bf16_f32 v77, v10, v11\n"
    "v_mfma_f32_16x16x32_bf16 v[36:39], a[128:131], a[208:211], v[36:39]\n"
    "s_add_u32 s78, s75, 30720\n"
    "ds_read_b128 a[152:155], v2 offset:21504\n"
    "v_cvt_pk_bf16_f32 v84, v12, v13\n"
    "v_mfma_f32_16x16x32_bf16 v[36:39], a[132:135], a[212:215], v[36:39]\n"
    "ds_read_b128 a[156:159], v2 offset:23552\n"
    "v_cvt_pk_bf16_f32 v85, v14, v15\n"
    "v_mfma_f32_16x16x32_bf16 v[40:43], a[132:135], a[236:239], v[100:103]\n"
    "ds_read_b128 v[104:107], v250 offset:144\n"
    "v_cvt_pk_bf16_f32 v92, v16, v17\n"
    "v_mfma_f32_16x16x32_bf16 v[40:43], a[128:131], a[232:235], v[40:43]\n"
    "s_add_u32 s68, s68, 24576\n"
    "v_cvt_pk_bf16_f32 v93, v18, v19\n"
    "v_cvt_pk_bf16_f32 v70, v20, v21\n"
    "v_mfma_f32_16x16x32_bf16 v[40:43], a[124:127], a[228:231], v[40:43]\n"
    "s_cmp_ge_u32 s68, s69\n"
    "s_cselect_b32 s68, s65, s68\n"
    "v_cvt_pk_bf16_f32 v71, v22, v23\n"
    "v_mfma_f32_16x16x32_bf16 v[40:43], a[120:123], a[224:227], v[40:43]\n"
    "v_cvt_pk_bf16_f32 v78, v24, v25\n"
    "v_cvt_pk_bf16_f32 v79, v26, v27\n"
    "v_mfma_f32_16x16x32_bf16 v[40:43], a[116:119], a[220:223], v[40:43]\n"
    "v_cvt_pk_bf16_f32 v86, v28, v29\n"
    "v_cvt_pk_bf16_f32 v87, v30, v31\n"
    "v_mfma_f32_16x16x32_bf16 v[40:43], a[112:115], a[216:219], v[40:43]\n"
    "v_cvt_pk_bf16_f32 v94, v32, v33\n"
    "v_cvt_pk_bf16_f32 v95, v34, v35\n"
    "v_mfma_f32_16x16x32_bf16 v[44:47], a[112:115], a[240:243], v[100:103]\n"
    "v_exp_f32_e64 v36, v36\n"
    "v_mfma_f32_16x16x32_bf16 v[44:47], a[116:119], a[244:247], v[44:47]\n"
    "v_exp_f32_e64 v37, v37\n"
    "v_mfma_f32_16x16x32_bf16 v[44:47], a[120:123], a[248:251], v[44:47]\n"
    "v_exp_f32_e64 v38, v38\n"
    "v_mfma_f32_16x16x32_bf16 v[44:47], a[124:127], a[252:255], v[44:47]\n"
    "v_exp_f32_e64 v39, v39\n"
    "v_mfma_f32_16x16x32_bf16 v[44:47], a[128:131], a[160:163], v[44:47]\n"
    "v_cvt_pk_bf16_f32 v72, v36, v37\n"
    "v_cvt_pk_bf16_f32 v73, v38, v39\n"
    "v_mfma_f32_16x16x32_bf16 v[44:47], a[132:135], a[164:167], v[44:47]\n"
    "v_exp_f32_e64 v40, v40\n"
    "v_mfma_f32_16x16x32_bf16 v[48:51], a[132:135], a[188:191], v[100:103]\n"
    "ds_read_b32 v245, v247\n"
    "v_mfma_f32_16x16x32_bf16 v[48:51], a[128:131], a[184:187], v[48:51]\n"
    "s_add_u32 m0, s68, .Lend-.Lend2+(0)\n"
    "v_exp_f32_e64 v41, v41\n"
    "v_mfma_f32_16x16x32_bf16 v[48:51], a[124:127], a[180:183], v[48:51]\n"
    "v_exp_f32 v42, v42\n"
    "v_mfma_f32_16x16x32_bf16 v[48:51], a[120:123], a[176:179], v[48:51]\n"
    "v_exp_f32 v43, v43\n"
    "v_mfma_f32_16x16x32_bf16 v[48:51], a[116:119], a[172:175], v[48:51]\n"
    "s_waitcnt vmcnt(10)\n"
    "v_cmp_eq_u64 s[54:55], v[236:237], v[238:239]\n"
    "v_cvt_pk_bf16_f32 v80, v40, v41\n"
    "v_mfma_f32_16x16x32_bf16 v[48:51], a[112:115], a[168:171], v[48:51]\n"
    "s_waitcnt lgkmcnt(1)\n"
    "v_cvt_pk_bf16_f32 v81, v42, v43\n"
    "v_mfma_f32_16x16x32_bf16 v[52:55], a[136:139], a[192:195], v[104:107]\n"
    "s_waitcnt lgkmcnt(0)\n"
    "v_cmp_le_u32 s[56:57], s59, v245\n"
    "v_mfma_f32_16x16x32_bf16 v[52:55], a[140:143], a[196:199], v[52:55]\n"
    "v_exp_f32 v44, v44\n"
    "v_mfma_f32_16x16x32_bf16 v[52:55], a[144:147], a[200:203], v[52:55]\n"
    "s_and_b64 s[54:55], s[54:55], s[56:57]\n"
    "s_cmp_eq_u64 s[54:55], -1\n"
    "v_mfma_f32_16x16x32_bf16 v[52:55], a[148:151], a[204:207], v[52:55]\n"
    "s_cbranch_scc0 .Lpoll1\n"
    ".Lpoll1r:\n"
    "v_mfma_f32_16x16x32_bf16 v[52:55], a[152:155], a[208:211], v[52:55]\n"
    "buffer_load_dwordx4 v1, s[16:19], s67 offen offset:0 sc1 lds\n"
    "v_mfma_f32_16x16x32_bf16 v[52:55], a[156:159], a[212:215], v[52:55]\n"
    "ds_read_b128 a[112:115], v3 offset:0\n"
    "v_mfma_f32_16x16x32_bf16 v[56:59], a[156:159], a[236:239], v[104:107]\n"
    "ds_read_b128 a[116:119], v3 offset:2048\n"
    "v_mfma_f32_16x16x32_bf16 v[56:59], a[152:155], a[232:235], v[56:59]\n"
    "ds_read_b128 a[120:123], v3 offset:4096\n"
    "v_mfma_f32_16x16x32_bf16 v[56:59], a[148:151], a[228:231], v[56:59]\n"
    "buffer_load_dwordx4 v1, s[16:19], s67 offen offset:1024 sc1 lds\n"
    "v_mfma_f32_16x16x32_bf16 v[56:59], a[144:147], a[224:227], v[56:59]\n"
    "ds_read_b128 a[124:127], v3 offset:6144\n"
    "v_mfma_f32_16x16x32_bf16 v[56:59], a[140:143], a[220:223], v[56:59]\n"
    "ds_read_b128 a[128:131], v3 offset:8192\n"
    "v_mfma_f32_16x16x32_bf16 v[56:59], a[136:139], a[216:219], v[56:59]\n"
    "ds_read_b128 a[132:135], v3 offset:10240\n"
    "v_mfma_f32_16x16x32_bf16 v[60:63], a[136:139], a[240:243], v[104:107]\n"
    "buffer_load_dwordx4 v1, s[16:19], s67 offen offset:2048 sc1 lds\n"
    "v_mfma_f32_16x16x32_bf16 v[60:63], a[140:143], a[244:247], v[60:63]\n"
    "ds_read_b128 v[100:103], v250 offset:768\n"
    "v_mfma_f32_16x16x32_bf16 v[60:63], a[144:147], a[248:251], v[60:63]\n"
    "v_exp_f32 v52, v52\n"
    "v_mfma_f32_16x16x32_bf16 v[60:63], a[148:151], a[252:255], v[60:63]\n"
    "v_exp_f32 v53, v53\n"
    "v_mfma_f32_16x16x32_bf16 v[60:63], a[152:155], a[160:163], v[60:63]\n"
    "buffer_load_dwordx4 v1, s[16:19], s67 offen offset:3072 sc1 lds\n"
    "v_mfma_f32_16x16x32_bf16 v[60:63], a[156:159], a[164:167], v[60:63]\n"
    "s_add_u32 m0, s68, 4096\n"
    "v_exp_f32 v54, v54\n"
    "v_mfma_f32_16x16x32_bf16 v[64:67], a[156:159], a[188:191], v[104:107]\n"
    "s_add_u32 s83, s67, 4096\n"
    "v_exp_f32 v55, v55\n"
    "v_mfma_f32_16x16x32_bf16 v[64:67], a[152:155], a[184:187], v[64:67]\n"
    "v_cvt_pk_bf16_f32 v74, v52, v53\n"
    "v_cvt_pk_bf16_f32 v75, v54, v55\n"
    "v_mfma_f32_16x16x32_bf16 v[64:67], a[148:151], a[180:183], v[64:67]\n"
    "buffer_load_dwordx4 v1, s[16:19], s83 offen offset:0 sc1 lds\n"
    "v_mfma_f32_16x16x32_bf16 v[64:67], a[144:147], a[176:179], v[64:67]\n"
    "v_exp_f32 v56, v56\n"
    "v_mfma_f32_16x16x32_bf16 v[64:67], a[140:143], a[172:175], v[64:67]\n"
    "v_exp_f32 v57, v57\n"
    "v_mfma_f32_16x16x32_bf16 v[64:67], a[136:139], a[168:171], v[64:67]\n"
    "s_waitcnt lgkmcnt(0)\n"
    "v_exp_f32 v58, v58\n"
    "v_mfma_f32_16x16x32_bf16 v[4:7], a[112:115], a[192:195], v[100:103]\n"
    "buffer_load_dwordx4 v1, s[16:19], s83 offen offset:1024 sc1 lds\n"
    "v_mfma_f32_16x16x32_bf16 v[4:7], a[116:119], a[196:199], v[4:7]\n"
    "s_add_u32 m0, s64, 0\n"
    "ds_read_b128 a[136:139], v3 offset:1024\n"
    "v_cvt_pk_bf16_f32 v82, v56, v57\n"
    "v_mfma_f32_16x16x32_bf16 v[4:7], a[120:123], a[200:203], v[4:7]\n"
    "s_add_u32 s83, s67, 24576\n"
    "ds_read_b128 a[140:143], v3 offset:3072\n"
    "v_mfma_f32_16x16x32_bf16 v[4:7], a[124:127], a[204:207], v[4:7]\n"
    "ds_read_b128 a[144:147], v3 offset:5120\n"
    "v_mfma_f32_16x16x32_bf16 v[4:7], a[128:131], a[208:211], v[4:7]\n"
    "buffer_load_dwordx4 v1, s[16:19], s83 offen offset:0 sc1 lds\n"
    "v_mfma_f32_16x16x32_bf16 v[4:7], a[132:135], a[212:215], v[4:7]\n"
    "ds_read_b128 a[148:151], v3 offset:7168\n"
    "v_mfma_f32_16x16x32_bf16 v[8:11], a[132:135], a[236:239], v[100:103]\n"
    "ds_read_b128 a[152:155], v3 offset:9216\n"
    "v_mfma_f32_16x16x32_bf16 v[8:11], a[128:131], a[232:235], v[8:11]\n"
    "ds_read_b128 a[156:159], v3 offset:11264\n"
    "v_mfma_f32_16x16x32_bf16 v[8:11], a[124:127], a[228:231], v[8:11]\n"
    "buffer_load_dwordx4 v1, s[16:19], s83 offen offset:1024 sc1 lds\n"
    "v_mfma_f32_16x16x32_bf16 v[8:11], a[120:123], a[224:227], v[8:11]\n"
    "ds_read_b128 v[104:107], v250 offset:784\n"
    "v_mfma_f32_16x16x32_bf16 v[8:11], a[116:119], a[220:223], v[8:11]\n"
    "v_exp_f32 v59, v59\n"
    "v_mfma_f32_16x16x32_bf16 v[8:11], a[112:115], a[216:219], v[8:11]\n"
    "v_cvt_pk_bf16_f32 v83, v58, v59\n"
    "v_mfma_f32_16x16x32_bf16 v[12:15], a[112:115], a[240:243], v[100:103]\n"
    "buffer_load_dwordx4 v1, s[16:19], s83 offen offset:2048 sc1 lds\n"
    "v_mfma_f32_16x16x32_bf16 v[12:15], a[116:119], a[244:247], v[12:15]\n"
    "v_exp_f32 v45, v45\n"
    "v_mfma_f32_16x16x32_bf16 v[12:15], a[120:123], a[248:251], v[12:15]\n"
    "v_exp_f32 v46, v46\n"
    "v_mfma_f32_16x16x32_bf16 v[12:15], a[124:127], a[252:255], v[12:15]\n"
    "v_exp_f32 v47, v47\n"
    "v_mfma_f32_16x16x32_bf16 v[12:15], a[128:131], a[160:163], v[12:15]\n"
    "buffer_load_dwordx4 v1, s[16:19], s83 offen offset:3072 sc1 lds\n"
    "v_mfma_f32_16x16x32_bf16 v[12:15], a[132:135], a[164:167], v[12:15]\n"
    "s_add_u32 m0, s64, 4096\n"
    "v_exp_f32 v60, v60\n"
    "v_mfma_f32_16x16x32_bf16 v[16:19], a[132:135], a[188:191], v[100:103]\n"
    "s_add_u32 s83, s67, 28672\n"
    "v_exp_f32 v61, v61\n"
    "v_mfma_f32_16x16x32_bf16 v[16:19], a[128:131], a[184:187], v[16:19]\n"
    "v_exp_f32 v62, v62\n"
    "v_mfma_f32_16x16x32_bf16 v[16:19], a[124:127], a[180:183], v[16:19]\n"
    "buffer_load_dwordx4 v1, s[16:19], s83 offen offset:0 sc1 lds\n"
    "v_mfma_f32_16x16x32_bf16 v[16:19], a[120:123], a[176:179], v[16:19]\n"
    "v_exp_f32 v63, v63\n"
    "v_mfma_f32_16x16x32_bf16 v[16:19], a[116:119], a[172:175], v[16:19]\n"
    "v_cvt_pk_bf16_f32 v88, v44, v45\n"
    "v_cvt_pk_bf16_f32 v89, v46, v47\n"
    "v_mfma_f32_16x16x32_bf16 v[16:19], a[112:115], a[168:171], v[16:19]\n"
    "s_waitcnt lgkmcnt(0)\n"
    "v_cvt_pk_bf16_f32 v90, v60, v61\n"
    "v_cvt_pk_bf16_f32 v91, v62, v63\n"
    "v_mfma_f32_16x16x32_bf16 v[20:23], a[136:139], a[192:195], v[104:107]\n"
    "buffer_load_dwordx4 v1, s[16:19], s83 offen offset:1024 sc1 lds\n"
    "v_mfma_f32_16x16x32_bf16 v[20:23], a[140:143], a[196:199], v[20:23]\n"
    "s_add_u32 s67, s67, 49152\n"
    "ds_read_b128 a[112:115], v248 offset:0\n"
    "v_mfma_f32_16x16x32_bf16 v[20:23], a[144:147], a[200:203], v[20:23]\n"
    "s_add_u32 s59, s59, 4\n"
    "ds_read_b128 a[116:119], v248 offset:12288\n"
    "v_mfma_f32_16x16x32_bf16 v[20:23], a[148:151], a[204:207], v[20:23]\n"
    "s_waitcnt vmcnt(12)\n"
    "ds_read_b128 a[120:123], v248 offset:1024\n"
    "v_mfma_f32_16x16x32_bf16 v[20:23], a[152:155], a[208:211], v[20:23]\n"
    "buffer_store_dwordx2 v[238:239], v240, s[36:39], s80 offen offset:0 sc1\n"
    "v_mfma_f32_16x16x32_bf16 v[20:23], a[156:159], a[212:215], v[20:23]\n"
    "buffer_load_dwordx2 v[204:205], v242, s[28:31], s75 offen offset:0 nt\n"
    "v_mfma_f32_16x16x32_bf16 v[24:27], a[156:159], a[236:239], v[104:107]\n"
    "s_add_u32 s80, s80, 256\n"
    "ds_read_b128 a[124:127], v248 offset:13312\n"
    "v_mfma_f32_16x16x32_bf16 v[24:27], a[152:155], a[232:235], v[24:27]\n"
    "buffer_load_dwordx2 v[206:207], v242, s[28:31], s75 offen offset:768 nt\n"
    "v_mfma_f32_16x16x32_bf16 v[24:27], a[148:151], a[228:231], v[24:27]\n"
    "ds_read_b128 a[128:131], v248 offset:2048\n"
    "v_mfma_f32_16x16x32_bf16 v[24:27], a[144:147], a[224:227], v[24:27]\n"
    "buffer_load_dwordx2 v[208:209], v242, s[28:31], s75 offen offset:3072 nt\n"
    "v_mfma_f32_16x16x32_bf16 v[24:27], a[140:143], a[220:223], v[24:27]\n"
    "ds_read_b128 a[132:135], v248 offset:14336\n"
    "v_mfma_f32_16x16x32_bf16 v[24:27], a[136:139], a[216:219], v[24:27]\n"
    "buffer_load_dwordx2 v[210:211], v242, s[28:31], s75 offen offset:3840 nt\n"
    "v_mfma_f32_16x16x32_bf16 v[28:31], a[136:139], a[240:243], v[104:107]\n"
    "v_exp_f32 v48, v48\n"
    "v_mfma_f32_16x16x32_bf16 v[28:31], a[140:143], a[244:247], v[28:31]\n"
    "buffer_load_dwordx2 v[212:213], v242, s[28:31], s76 offen offset:0 nt\n"
    "v_mfma_f32_16x16x32_bf16 v[28:31], a[144:147], a[248:251], v[28:31]\n"
    "v_exp_f32 v49, v49\n"
    "v_mfma_f32_16x16x32_bf16 v[28:31], a[148:151], a[252:255], v[28:31]\n"
    "buffer_load_dwordx2 v[214:215], v242, s[28:31], s76 offen offset:768 nt\n"
    "v_mfma_f32_16x16x32_bf16 v[28:31], a[152:155], a[160:163], v[28:31]\n"
    "v_exp_f32 v50, v50\n"
    "v_mfma_f32_16x16x32_bf16 v[28:31], a[156:159], a[164:167], v[28:31]\n"
    "buffer_load_dwordx2 v[216:217], v242, s[28:31], s76 offen offset:3072 nt\n"
    "v_mfma_f32_16x16x32_bf16 v[32:35], a[156:159], a[188:191], v[104:107]\n"
    "v_exp_f32 v51, v51\n"
    "v_mfma_f32_16x16x32_bf16 v[32:35], a[152:155], a[184:187], v[32:35]\n"
    "buffer_load_dwordx2 v[218:219], v242, s[28:31], s76 offen offset:3840 nt\n"
    "v_mfma_f32_16x16x32_bf16 v[32:35], a[148:151], a[180:183], v[32:35]\n"
    "v_exp_f32 v64, v64\n"
    "v_mfma_f32_16x16x32_bf16 v[32:35], a[144:147], a[176:179], v[32:35]\n"
    "buffer_load_dwordx2 v[220:221], v242, s[28:31], s77 offen offset:0 nt\n"
    "v_mfma_f32_16x16x32_bf16 v[32:35], a[140:143], a[172:175], v[32:35]\n"
    "v_exp_f32 v65, v65\n"
    "v_mfma_f32_16x16x32_bf16 v[32:35], a[136:139], a[168:171], v[32:35]\n"
    "s_waitcnt lgkmcnt(0)\n"
    "v_exp_f32 v66, v66\n"
    "v_mfma_f32_16x16x32_bf16 v[124:127], a[112:115], v[68:71], v[124:127]\n"
    "buffer_load_dwordx2 v[222:223], v242, s[28:31], s77 offen offset:768 nt\n"
    "v_mfma_f32_16x16x32_bf16 v[124:127], a[116:119], v[72:75], v[124:127]\n"
    "ds_read_b128 a[136:139], v248 offset:3072\n"
    "v_cvt_pk_bf16_f32 v96, v48, v49\n"
    "v_mfma_f32_16x16x32_bf16 v[128:131], a[124:127], v[72:75], v[128:131]\n"
    "buffer_load_dwordx2 v[224:225], v242, s[28:31], s77 offen offset:3072 nt\n"
    "v_mfma_f32_16x16x32_bf16 v[128:131], a[120:123], v[68:71], v[128:131]\n"
    "ds_read_b128 a[140:143], v248 offset:15360\n"
    "v_cvt_pk_bf16_f32 v97, v50, v51\n"
    "v_mfma_f32_16x16x32_bf16 v[132:135], a[128:131], v[68:71], v[132:135]\n"
    "buffer_load_dwordx2 v[226:227], v242, s[28:31], s77 offen offset:3840 nt\n"
    "v_mfma_f32_16x16x32_bf16 v[132:135], a[132:135], v[72:75], v[132:135]\n"
    "ds_read_b128 a[144:147], v248 offset:4096\n"
    "v_cvt_pk_bf16_f32 v98, v64, v65\n"
    "v_mfma_f32_16x16x32_bf16 v[148:151], a[132:135], v[80:83], v[148:151]\n"
    "buffer_load_dwordx2 v[228:229], v242, s[28:31], s78 offen offset:0 nt\n"
    "v_mfma_f32_16x16x32_bf16 v[148:151], a[128:131], v[76:79], v[148:151]\n"
    "ds_read_b128 a[148:151], v248 offset:16384\n"
    "v_mfma_f32_16x16x32_bf16 v[144:147], a[120:123], v[76:79], v[144:147]\n"
    "buffer_load_dwordx2 v[230:231], v242, s[28:31], s78 offen offset:768 nt\n"
    "v_mfma_f32_16x16x32_bf16 v[144:147], a[124:127], v[80:83], v[144:147]\n"
    "ds_read_b128 a[152:155], v248 offset:5120\n"
    "v_mfma_f32_16x16x32_bf16 v[140:143], a[116:119], v[80:83], v[140:143]\n"
    "buffer_load_dwordx2 v[232:233], v242, s[28:31], s78 offen offset:3072 nt\n"
    "v_mfma_f32_16x16x32_bf16 v[140:143], a[112:115], v[76:79], v[140:143]\n"
    "ds_read_b128 a[156:159], v248 offset:17408\n"
    "v_mfma_f32_16x16x32_bf16 v[156:159], a[112:115], v[84:87], v[156:159]\n"
    "buffer_load_dwordx2 v[234:235], v242, s[28:31], s78 offen offset:3840 nt\n"
    "v_mfma_f32_16x16x32_bf16 v[156:159], a[116:119], v[88:91], v[156:159]\n"
    "ds_read_b128 v[104:107], v251 offset:0\n"
    "v_mfma_f32_16x16x32_bf16 v[160:163], a[124:127], v[88:91], v[160:163]\n"
    "v_exp_f32 v67, v67\n"
    "v_mfma_f32_16x16x32_bf16 v[160:163], a[120:123], v[84:87], v[160:163]\n"
    "v_cvt_pk_bf16_f32 v99, v66, v67\n"
    "v_mfma_f32_16x16x32_bf16 v[164:167], a[128:131], v[84:87], v[164:167]\n"
    "v_exp_f32 v4, v4\n"
    "v_mfma_f32_16x16x32_bf16 v[164:167], a[132:135], v[88:91], v[164:167]\n"
    "v_exp_f32 v5, v5\n"
    "v_mfma_f32_16x16x32_bf16 v[180:183], a[132:135], v[96:99], v[180:183]\n"
    "v_exp_f32 v6, v6\n"
    "v_mfma_f32_16x16x32_bf16 v[180:183], a[128:131], v[92:95], v[180:183]\n"
    "v_exp_f32 v7, v7\n"
    "v_mfma_f32_16x16x32_bf16 v[176:179], a[120:123], v[92:95], v[176:179]\n"
    "v_exp_f32 v8, v8\n"
    "v_mfma_f32_16x16x32_bf16 v[176:179], a[124:127], v[96:99], v[176:179]\n"
    "v_exp_f32 v9, v9\n"
    "v_mfma_f32_16x16x32_bf16 v[172:175], a[116:119], v[96:99], v[172:175]\n"
    "v_exp_f32 v10, v10\n"
    "v_mfma_f32_16x16x32_bf16 v[172:175], a[112:115], v[92:95], v[172:175]\n"
    "s_waitcnt lgkmcnt(0)\n"
    "v_exp_f32 v11, v11\n"
    "v_mfma_f32_16x16x32_bf16 v[136:139], a[136:139], v[68:71], v[136:139]\n"
    "ds_read_b128 a[112:115], v248 offset:6144\n"
    "v_mfma_f32_16x16x32_bf16 v[136:139], a[140:143], v[72:75], v[136:139]\n"
    "ds_read_b128 a[116:119], v248 offset:18432\n"
    "v_mfma_f32_16x16x32_bf16 v[188:191], a[148:151], v[72:75], v[188:191]\n"
    "ds_read_b128 a[120:123], v248 offset:7168\n"
    "v_mfma_f32_16x16x32_bf16 v[188:191], a[144:147], v[68:71], v[188:191]\n"
    "ds_read_b128 a[124:127], v248 offset:19456\n"
    "v_mfma_f32_16x16x32_bf16 v[192:195], a[152:155], v[68:71], v[192:195]\n"
    "ds_read_b128 a[128:131], v248 offset:8192\n"
    "v_mfma_f32_16x16x32_bf16 v[192:195], a[156:159], v[72:75], v[192:195]\n"
    "ds_read_b128 a[132:135], v248 offset:20480\n"
    "v_smfmac_f32_16x16x64_bf16 v[108:111], v[104:107], v[68:75], v253\n"
    "s_add_u32 s81, s81, 128\n"
    "s_cmp_gt_u32 s81, 4096\n"
    "s_cselect_b32 s81, 0, s81\n"
    "v_smfmac_f32_16x16x64_bf16 v[112:115], v[104:107], v[76:83], v253\n"
    "s_cmp_ge_u32 s75, 1474560\n"
    "s_cmov_b64 s[28:29], s[6:7]\n"
    "v_mfma_f32_16x16x32_bf16 a[4:7], a[156:159], v[80:83], a[4:7]\n"
    "v_exp_f32 v12, v12\n"
    "v_mfma_f32_16x16x32_bf16 a[4:7], a[152:155], v[76:79], a[4:7]\n"
    "v_exp_f32 v13, v13\n"
    "v_mfma_f32_16x16x32_bf16 a[0:3], a[144:147], v[76:79], a[0:3]\n"
    "v_exp_f32 v14, v14\n"
    "v_mfma_f32_16x16x32_bf16 a[0:3], a[148:151], v[80:83], a[0:3]\n"
    "v_exp_f32 v15, v15\n"
    "v_mfma_f32_16x16x32_bf16 v[152:155], a[140:143], v[80:83], v[152:155]\n"
    "v_exp_f32 v16, v16\n"
    "v_mfma_f32_16x16x32_bf16 v[152:155], a[136:139], v[76:79], v[152:155]\n"
    "v_mfma_f32_16x16x32_bf16 v[168:171], a[136:139], v[84:87], v[168:171]\n"
    "v_exp_f32 v17, v17\n"
    "v_mfma_f32_16x16x32_bf16 v[168:171], a[140:143], v[88:91], v[168:171]\n"
    "v_mfma_f32_16x16x32_bf16 a[16:19], a[148:151], v[88:91], a[16:19]\n"
    "v_mfma_f32_16x16x32_bf16 a[16:19], a[144:147], v[84:87], a[16:19]\n"
    "v_exp_f32 v18, v18\n"
    "v_mfma_f32_16x16x32_bf16 a[20:23], a[152:155], v[84:87], a[20:23]\n"
    "v_mfma_f32_16x16x32_bf16 a[20:23], a[156:159], v[88:91], a[20:23]\n"
    "v_smfmac_f32_16x16x64_bf16 v[116:119], v[104:107], v[84:91], v253\n"
    "v_smfmac_f32_16x16x64_bf16 v[120:123], v[104:107], v[92:99], v253\n"
    "v_mfma_f32_16x16x32_bf16 a[36:39], a[156:159], v[96:99], a[36:39]\n"
    "v_exp_f32 v19, v19\n"
    "v_mfma_f32_16x16x32_bf16 a[36:39], a[152:155], v[92:95], a[36:39]\n"
    "v_exp_f32 v20, v20\n"
    "v_mfma_f32_16x16x32_bf16 a[32:35], a[144:147], v[92:95], a[32:35]\n"
    "v_mfma_f32_16x16x32_bf16 a[32:35], a[148:151], v[96:99], a[32:35]\n"
    "v_exp_f32 v21, v21\n"
    "v_mfma_f32_16x16x32_bf16 v[184:187], a[140:143], v[96:99], v[184:187]\n"
    "v_mfma_f32_16x16x32_bf16 v[184:187], a[136:139], v[92:95], v[184:187]\n"
    "s_waitcnt lgkmcnt(0)\n"
    "v_mfma_f32_16x16x32_bf16 v[196:199], a[112:115], v[68:71], v[196:199]\n"
    "ds_read_b128 a[136:139], v248 offset:9216\n"
    "v_mfma_f32_16x16x32_bf16 v[196:199], a[116:119], v[72:75], v[196:199]\n"
    "ds_read_b128 a[140:143], v248 offset:21504\n"
    "v_mfma_f32_16x16x32_bf16 v[200:203], a[124:127], v[72:75], v[200:203]\n"
    "ds_read_b128 a[144:147], v248 offset:10240\n"
    "v_mfma_f32_16x16x32_bf16 v[200:203], a[120:123], v[68:71], v[200:203]\n"
    "ds_read_b128 a[148:151], v248 offset:22528\n"
    "v_mfma_f32_16x16x32_bf16 a[48:51], a[128:131], v[68:71], a[48:51]\n"
    "buffer_load_dwordx2 v[236:237], v241, s[36:39], s81 offen offset:0 sc1\n"
    "v_mfma_f32_16x16x32_bf16 a[48:51], a[132:135], v[72:75], a[48:51]\n"
    "ds_read_b128 a[152:155], v248 offset:11264\n"
    "v_mfma_f32_16x16x32_bf16 a[64:67], a[132:135], v[80:83], a[64:67]\n"
    "ds_read_b128 a[156:159], v248 offset:23552\n"
    "v_mfma_f32_16x16x32_bf16 a[64:67], a[128:131], v[76:79], a[64:67]\n"
    "v_exp_f32 v22, v22\n"
    "v_mfma_f32_16x16x32_bf16 a[12:15], a[120:123], v[76:79], a[12:15]\n"
    "v_exp_f32 v23, v23\n"
    "v_mfma_f32_16x16x32_bf16 a[12:15], a[124:127], v[80:83], a[12:15]\n"
    "v_exp_f32 v24, v24\n"
    "v_mfma_f32_16x16x32_bf16 a[8:11], a[116:119], v[80:83], a[8:11]\n"
    "v_exp_f32 v25, v25\n"
    "v_mfma_f32_16x16x32_bf16 a[8:11], a[112:115], v[76:79], a[8:11]\n"
    "v_exp_f32 v26, v26\n"
    "v_mfma_f32_16x16x32_bf16 a[24:27], a[112:115], v[84:87], a[24:27]\n"
    "v_mfma_f32_16x16x32_bf16 a[24:27], a[116:119], v[88:91], a[24:27]\n"
    "v_exp_f32 v27, v27\n"
    "v_mfma_f32_16x16x32_bf16 a[28:31], a[124:127], v[88:91], a[28:31]\n"
    "v_mfma_f32_16x16x32_bf16 a[28:31], a[120:123], v[84:87], a[28:31]\n"
    "v_mfma_f32_16x16x32_bf16 a[80:83], a[128:131], v[84:87], a[80:83]\n"
    "v_exp_f32 v28, v28\n"
    "v_mfma_f32_16x16x32_bf16 a[80:83], a[132:135], v[88:91], a[80:83]\n"
    "v_mfma_f32_16x16x32_bf16 a[96:99], a[132:135], v[96:99], a[96:99]\n"
    "v_mfma_f32_16x16x32_bf16 a[96:99], a[128:131], v[92:95], a[96:99]\n"
    "v_exp_f32 v29, v29\n"
    "v_mfma_f32_16x16x32_bf16 a[44:47], a[120:123], v[92:95], a[44:47]\n"
    "v_mfma_f32_16x16x32_bf16 a[44:47], a[124:127], v[96:99], a[44:47]\n"
    "v_exp_f32 v30, v30\n"
    "v_mfma_f32_16x16x32_bf16 a[40:43], a[116:119], v[96:99], a[40:43]\n"
    "v_mfma_f32_16x16x32_bf16 a[40:43], a[112:115], v[92:95], a[40:43]\n"
    "s_waitcnt lgkmcnt(0)\n"
    "v_mfma_f32_16x16x32_bf16 a[52:55], a[136:139], v[68:71], a[52:55]\n"
    "ds_read_b128 a[112:115], v3 offset:12288\n"
    "v_mfma_f32_16x16x32_bf16 a[52:55], a[140:143], v[72:75], a[52:55]\n"
    "ds_read_b128 a[116:119], v3 offset:14336\n"
    "v_mfma_f32_16x16x32_bf16 a[56:59], a[148:151], v[72:75], a[56:59]\n"
    "ds_read_b128 a[120:123], v3 offset:16384\n"
    "v_mfma_f32_16x16x32_bf16 a[56:59], a[144:147], v[68:71], a[56:59]\n"
    "ds_read_b128 a[124:127], v3 offset:18432\n"
    "v_mfma_f32_16x16x32_bf16 a[60:63], a[152:155], v[68:71], a[60:63]\n"
    "ds_read_b128 a[128:131], v3 offset:20480\n"
    "v_mfma_f32_16x16x32_bf16 a[60:63], a[156:159], v[72:75], a[60:63]\n"
    "ds_read_b128 a[132:135], v3 offset:22528\n"
    "v_mfma_f32_16x16x32_bf16 a[76:79], a[156:159], v[80:83], a[76:79]\n"
    "ds_read_b128 v[100:103], v250 offset:896\n"
    "v_mfma_f32_16x16x32_bf16 a[76:79], a[152:155], v[76:79], a[76:79]\n"
    "v_exp_f32 v31, v31\n"
    "v_mfma_f32_16x16x32_bf16 a[72:75], a[144:147], v[76:79], a[72:75]\n"
    "v_exp_f32 v32, v32\n"
    "v_mfma_f32_16x16x32_bf16 a[72:75], a[148:151], v[80:83], a[72:75]\n"
    "v_exp_f32 v33, v33\n"
    "v_mfma_f32_16x16x32_bf16 a[68:71], a[140:143], v[80:83], a[68:71]\n"
    "v_exp_f32 v34, v34\n"
    "v_mfma_f32_16x16x32_bf16 a[68:71], a[136:139], v[76:79], a[68:71]\n"
    "v_exp_f32 v35, v35\n"
    "v_mfma_f32_16x16x32_bf16 a[84:87], a[136:139], v[84:87], a[84:87]\n"
    "v_mfma_f32_16x16x32_bf16 a[84:87], a[140:143], v[88:91], a[84:87]\n"
    "v_mfma_f32_16x16x32_bf16 a[88:91], a[148:151], v[88:91], a[88:91]\n"
    "s_waitcnt vmcnt(18)\n"
    "ds_add_u32 v247, v246\n"
    "v_mfma_f32_16x16x32_bf16 a[88:91], a[144:147], v[84:87], a[88:91]\n"
    "v_mfma_f32_16x16x32_bf16 a[92:95], a[152:155], v[84:87], a[92:95]\n"
    "v_mfma_f32_16x16x32_bf16 a[92:95], a[156:159], v[88:91], a[92:95]\n"
    "v_mfma_f32_16x16x32_bf16 a[108:111], a[156:159], v[96:99], a[108:111]\n"
    "v_mfma_f32_16x16x32_bf16 a[108:111], a[152:155], v[92:95], a[108:111]\n"
    "v_mfma_f32_16x16x32_bf16 a[104:107], a[144:147], v[92:95], a[104:107]\n"
    "s_sub_u32 s74, s68, s65\n"
    "v_mfma_f32_16x16x32_bf16 a[104:107], a[148:151], v[96:99], a[104:107]\n"
    "s_add_u32 s66, s66, 1\n"
    "v_mov_b32 v2, v3\n"
    "v_add_u32 v3, s74, v252\n"
    "v_mfma_f32_16x16x32_bf16 a[100:103], a[140:143], v[96:99], a[100:103]\n"
    "s_waitcnt lgkmcnt(1)\n"
    "s_cmp_eq_u32 s66, 28\n"
    "v_mfma_f32_16x16x32_bf16 a[100:103], a[136:139], v[92:95], a[100:103]\n"
    ".LwKe:\n"
    "s_cbranch_scc1 .LtailP\n"
    ".LNK:\n"
    "v_mfma_f32_16x16x32_bf16 v[36:39], a[112:115], a[192:195], v[100:103]\n"
    "ds_read_b128 a[136:139], v2 offset:13312\n"
    "v_cvt_pk_bf16_f32 v68, v4, v5\n"
    "v_mfma_f32_16x16x32_bf16 v[36:39], a[116:119], a[196:199], v[36:39]\n"
    "ds_read_b128 a[140:143], v2 offset:15360\n"
    "v_mfma_f32_16x16x32_bf16 v[36:39], a[120:123], a[200:203], v[36:39]\n"
    "ds_read_b128 a[144:147], v2 offset:17408\n"
    "v_mfma_f32_16x16x32_bf16 v[36:39], a[124:127], a[204:207], v[36:39]\n"
    "ds_read_b128 a[148:151], v2 offset:19456\n"
    "v_cvt_pk_bf16_f32 v69, v6, v7\n"
    "v_mfma_f32_16x16x32_bf16 v[36:39], a[128:131], a[208:211], v[36:39]\n"
    "ds_read_b128 a[152:155], v2 offset:21504\n"
    "v_mfma_f32_16x16x32_bf16 v[36:39], a[132:135], a[212:215], v[36:39]\n"
    "ds_read_b128 a[156:159], v2 offset:23552\n"
    "v_mfma_f32_16x16x32_bf16 v[40:43], a[132:135], a[236:239], v[100:103]\n"
    "ds_read_b128 v[104:107], v250 offset:912\n"
    "v_mfma_f32_16x16x32_bf16 v[40:43], a[128:131], a[232:235], v[40:43]\n"
    "s_add_u32 s68, s68, 24576\n"
    "v_cvt_pk_bf16_f32 v76, v8, v9\n"
    "v_mfma_f32_16x16x32_bf16 v[40:43], a[124:127], a[228:231], v[40:43]\n"
    "s_cmp_ge_u32 s68, s69\n"
    "s_cselect_b32 s68, s65, s68\n"
    "v_mfma_f32_16x16x32_bf16 v[40:43], a[120:123], a[224:227], v[40:43]\n"
    "v_exp_f32 v36, v36\n"
    "v_mfma_f32_16x16x32_bf16 v[40:43], a[116:119], a[220:223], v[40:43]\n"
    "v_exp_f32 v37, v37\n"
    "v_mfma_f32_16x16x32_bf16 v[40:43], a[112:115], a[216:219], v[40:43]\n"
    "v_cvt_pk_bf16_f32 v77, v10, v11\n"
    "v_cvt_pk_bf16_f32 v72, v36, v37\n"
    "v_mfma_f32_16x16x32_bf16 v[44:47], a[112:115], a[240:243], v[100:103]\n"
    "v_mfma_f32_16x16x32_bf16 v[44:47], a[116:119], a[244:247], v[44:47]\n"
    "v_exp_f32 v38, v38\n"
    "v_mfma_f32_16x16x32_bf16 v[44:47], a[120:123], a[248:251], v[44:47]\n"
    "v_mfma_f32_16x16x32_bf16 v[44:47], a[124:127], a[252:255], v[44:47]\n"
    "v_cvt_pk_bf16_f32 v84, v12, v13\n"
    "v_mfma_f32_16x16x32_bf16 v[44:47], a[128:131], a[160:163], v[44:47]\n"
    "v_exp_f32 v39, v39\n"
    "v_mfma_f32_16x16x32_bf16 v[44:47], a[132:135], a[164:167], v[44:47]\n"
    "v_cvt_pk_bf16_f32 v73, v38, v39\n"
    "v_mfma_f32_16x16x32_bf16 v[48:51], a[132:135], a[188:191], v[100:103]\n"
    "ds_read_b32 v245, v247\n"
    "v_cvt_pk_bf16_f32 v85, v14, v15\n"
    "v_mfma_f32_16x16x32_bf16 v[48:51], a[128:131], a[184:187], v[48:51]\n"
    "s_add_u32 m0, s68, 0\n"
    "v_exp_f32 v40, v40\n"
    "v_mfma_f32_16x16x32_bf16 v[48:51], a[124:127], a[180:183], v[48:51]\n"
    "v_mfma_f32_16x16x32_bf16 v[48:51], a[120:123], a[176:179], v[48:51]\n"
    "v_mfma_f32_16x16x32_bf16 v[48:51], a[116:119], a[172:175], v[48:51]\n"
    "s_waitcnt vmcnt(0)\n"
    "v_cmp_eq_u64 s[54:55], v[236:237], v[238:239]\n"
    "v_cvt_pk_bf16_f32 v92, v16, v17\n"
    "v_mfma_f32_16x16x32_bf16 v[48:51], a[112:115], a[168:171], v[48:51]\n"
    "s_waitcnt lgkmcnt(1)\n"
    "v_exp_f32 v41, v41\n"
    "v_mfma_f32_16x16x32_bf16 v[52:55], a[136:139], a[192:195], v[104:107]\n"
    "s_waitcnt lgkmcnt(0)\n"
    "v_cmp_le_u32 s[56:57], s59, v245\n"
    "v_cvt_pk_bf16_f32 v80, v40, v41\n"
    "v_mfma_f32_16x16x32_bf16 v[52:55], a[140:143], a[196:199], v[52:55]\n"
    "s_waitcnt vmcnt(1)\n"
    "v_exp_f32 v42, v42\n"
    "v_mfma_f32_16x16x32_bf16 v[52:55], a[144:147], a[200:203], v[52:55]\n"
    "s_and_b64 s[54:55], s[54:55], s[56:57]\n"
    "s_cmp_eq_u64 s[54:55], -1\n"
    "v_mfma_f32_16x16x32_bf16 v[52:55], a[148:151], a[204:207], v[52:55]\n"
    "s_cbranch_scc0 .Lpoll2\n"
    ".Lpoll2r:\n"
    "v_mfma_f32_16x16x32_bf16 v[52:55], a[152:155], a[208:211], v[52:55]\n"
    "buffer_load_dwordx4 v1, s[16:19], s67 offen offset:0 sc1 lds\n"
    "v_mfma_f32_16x16x32_bf16 v[52:55], a[156:159], a[212:215], v[52:55]\n"
    "s_add_u32 s76, s71, s60\n"
    "ds_read_b128 a[112:115], v3 offset:0\n"
    "v_cvt_pk_bf16_f32 v93, v18, v19\n"
    "v_mfma_f32_16x16x32_bf16 v[56:59], a[156:159], a[236:239], v[104:107]\n"
    "s_add_u32 s77, s71, s61\n"
    "ds_read_b128 a[116:119], v3 offset:2048\n"
    "v_cvt_pk_bf16_f32 v70, v20, v21\n"
    "v_mfma_f32_16x16x32_bf16 v[56:59], a[152:155], a[232:235], v[56:59]\n"
    "ds_read_b128 a[120:123], v3 offset:4096\n"
    "v_mfma_f32_16x16x32_bf16 v[56:59], a[148:151], a[228:231], v[56:59]\n"
    "buffer_load_dwordx4 v1, s[16:19], s67 offen offset:1024 sc1 lds\n"
    "v_mfma_f32_16x16x32_bf16 v[56:59], a[144:147], a[224:227], v[56:59]\n"
    "ds_read_b128 a[124:127], v3 offset:6144\n"
    "v_cvt_pk_bf16_f32 v71, v22, v23\n"
    "v_mfma_f32_16x16x32_bf16 v[56:59], a[140:143], a[220:223], v[56:59]\n"
    "ds_read_b128 a[128:131], v3 offset:8192\n"
    "v_mfma_f32_16x16x32_bf16 v[56:59], a[136:139], a[216:219], v[56:59]\n"
    "ds_read_b128 a[132:135], v3 offset:10240\n"
    "v_mfma_f32_16x16x32_bf16 v[60:63], a[136:139], a[240:243], v[104:107]\n"
    "buffer_load_dwordx4 v1, s[16:19], s67 offen offset:2048 sc1 lds\n"
    "v_mfma_f32_16x16x32_bf16 v[60:63], a[140:143], a[244:247], v[60:63]\n"
    "ds_read_b128 v[100:103], v250 offset:1536\n"
    "v_cvt_pk_bf16_f32 v78, v24, v25\n"
    "v_mfma_f32_16x16x32_bf16 v[60:63], a[144:147], a[248:251], v[60:63]\n"
    "v_exp_f32 v43, v43\n"
    "v_mfma_f32_16x16x32_bf16 v[60:63], a[148:151], a[252:255], v[60:63]\n"
    "ds_read_b128 v[36:39], v243 offset:0\n"
    "v_cvt_pk_bf16_f32 v81, v42, v43\n"
    "v_mfma_f32_16x16x32_bf16 v[60:63], a[152:155], a[160:163], v[60:63]\n"
    "buffer_load_dwordx4 v1, s[16:19], s67 offen offset:3072 sc1 lds\n"
    "v_mfma_f32_16x16x32_bf16 v[60:63], a[156:159], a[164:167], v[60:63]\n"
    "s_add_u32 m0, s68, 4096\n"
    "v_cvt_pk_bf16_f32 v79, v26, v27\n"
    "v_add_u32 v243, 512, v243\n"
    "v_mfma_f32_16x16x32_bf16 v[64:67], a[156:159], a[188:191], v[104:107]\n"
    "s_add_u32 s83, s67, 4096\n"
    "v_exp_f32 v44, v44\n"
    "v_mfma_f32_16x16x32_bf16 v[64:67], a[152:155], a[184:187], v[64:67]\n"
    "v_exp_f32 v45, v45\n"
    "v_mfma_f32_16x16x32_bf16 v[64:67], a[148:151], a[180:183], v[64:67]\n"
    "buffer_load_dwordx4 v1, s[16:19], s83 offen offset:0 sc1 lds\n"
    "v_mfma_f32_16x16x32_bf16 v[64:67], a[144:147], a[176:179], v[64:67]\n"
    "v_cvt_pk_bf16_f32 v86, v28, v29\n"
    "v_cvt_pk_bf16_f32 v88, v44, v45\n"
    "v_mfma_f32_16x16x32_bf16 v[64:67], a[140:143], a[172:175], v[64:67]\n"
    "v_exp_f32 v46, v46\n"
    "v_mfma_f32_16x16x32_bf16 v[64:67], a[136:139], a[168:171], v[64:67]\n"
    "s_waitcnt lgkmcnt(1)\n"
    "v_exp_f32 v47, v47\n"
    "v_mfma_f32_16x16x32_bf16 v[4:7], a[112:115], a[192:195], v[100:103]\n"
    "buffer_load_dwordx4 v1, s[16:19], s83 offen offset:1024 sc1 lds\n"
    "v_mfma_f32_16x16x32_bf16 v[4:7], a[116:119], a[196:199], v[4:7]\n"
    "s_add_u32 m0, s70, .Lend-.Lend2+(0)\n"
    "ds_read_b128 a[136:139], v3 offset:1024\n"
    "v_cvt_pk_bf16_f32 v87, v30, v31\n"
    "v_mfma_f32_16x16x32_bf16 v[4:7], a[120:123], a[200:203], v[4:7]\n"
    "s_add_u32 s83, s67, 24576\n"
    "ds_read_b128 a[140:143], v3 offset:3072\n"
    "v_cvt_pk_bf16_f32 v89, v46, v47\n"
    "v_mfma_f32_16x16x32_bf16 v[4:7], a[124:127], a[204:207], v[4:7]\n"
    "s_waitcnt lgkmcnt(2)\n"
    "ds_read_b128 a[144:147], v3 offset:5120\n"
    "v_fma_f32 v204, v204, v36, -v38\n"
    "v_mfma_f32_16x16x32_bf16 v[4:7], a[128:131], a[208:211], v[4:7]\n"
    "buffer_load_dwordx4 v1, s[16:19], s83 offen offset:0 sc1 lds\n"
    "v_mfma_f32_16x16x32_bf16 v[4:7], a[132:135], a[212:215], v[4:7]\n"
    "ds_read_b128 a[148:151], v3 offset:7168\n"
    "v_cvt_pk_bf16_f32 v94, v32, v33\n"
    "v_mfma_f32_16x16x32_bf16 v[8:11], a[132:135], a[236:239], v[100:103]\n"
    "ds_read_b128 a[152:155], v3 offset:9216\n"
    "v_fma_f32 v205, v205, v37, -v39\n"
    "v_mfma_f32_16x16x32_bf16 v[8:11], a[128:131], a[232:235], v[8:11]\n"
    "ds_read_b128 a[156:159], v3 offset:11264\n"
    "v_fma_f32 v206, v206, v36, -v38\n"
    "v_mfma_f32_16x16x32_bf16 v[8:11], a[124:127], a[228:231], v[8:11]\n"
    "buffer_load_dwordx4 v1, s[16:19], s83 offen offset:1024 sc1 lds\n"
    "v_mfma_f32_16x16x32_bf16 v[8:11], a[120:123], a[224:227], v[8:11]\n"
    "ds_read_b128 v[104:107], v250 offset:1552\n"
    "v_cvt_pk_bf16_f32 v95, v34, v35\n"
    "v_mfma_f32_16x16x32_bf16 v[8:11], a[116:119], a[220:223], v[8:11]\n"
    "v_exp_f32 v52, v52\n"
    "v_mfma_f32_16x16x32_bf16 v[8:11], a[112:115], a[216:219], v[8:11]\n"
    "v_exp_f32 v53, v53\n"
    "v_mfma_f32_16x16x32_bf16 v[12:15], a[112:115], a[240:243], v[100:103]\n"
    "buffer_load_dwordx4 v1, s[16:19], s83 offen offset:2048 sc1 lds\n"
    "v_mfma_f32_16x16x32_bf16 v[12:15], a[116:119], a[244:247], v[12:15]\n"
    "v_exp_f32 v54, v54\n"
    "v_mfma_f32_16x16x32_bf16 v[12:15], a[120:123], a[248:251], v[12:15]\n"
    "v_exp_f32 v55, v55\n"
    "v_mfma_f32_16x16x32_bf16 v[12:15], a[124:127], a[252:255], v[12:15]\n"
    "v_cvt_pk_bf16_f32 v74, v52, v53\n"
    "v_cvt_pk_bf16_f32 v75, v54, v55\n"
    "v_mfma_f32_16x16x32_bf16 v[12:15], a[128:131], a[160:163], v[12:15]\n"
    "buffer_load_dwordx4 v1, s[16:19], s83 offen offset:3072 sc1 lds\n"
    "v_mfma_f32_16x16x32_bf16 v[12:15], a[132:135], a[164:167], v[12:15]\n"
    "s_add_u32 m0, s70, 4096\n"
    "v_exp_f32 v56, v56\n"
    "v_mfma_f32_16x16x32_bf16 v[16:19], a[132:135], a[188:191], v[100:103]\n"
    "s_add_u32 s83, s67, 28672\n"
    "v_exp_f32 v48, v48\n"
    "v_mfma_f32_16x16x32_bf16 v[16:19], a[128:131], a[184:187], v[16:19]\n"
    "v_exp_f32 v49, v49\n"
    "v_mfma_f32_16x16x32_bf16 v[16:19], a[124:127], a[180:183], v[16:19]\n"
    "buffer_load_dwordx4 v1, s[16:19], s83 offen offset:0 sc1 lds\n"
    "v_mfma_f32_16x16x32_bf16 v[16:19], a[120:123], a[176:179], v[16:19]\n"
    "v_exp_f32 v57, v57\n"
    "v_mfma_f32_16x16x32_bf16 v[16:19], a[116:119], a[172:175], v[16:19]\n"
    "v_cvt_pk_bf16_f32 v82, v56, v57\n"
    "v_cvt_pk_bf16_f32 v96, v48, v49\n"
    "v_mfma_f32_16x16x32_bf16 v[16:19], a[112:115], a[168:171], v[16:19]\n"
    "s_waitcnt lgkmcnt(0)\n"
    "v_exp_f32 v58, v58\n"
    "v_mfma_f32_16x16x32_bf16 v[20:23], a[136:139], a[192:195], v[104:107]\n"
    "buffer_load_dwordx4 v1, s[16:19], s83 offen offset:1024 sc1 lds\n"
    "v_mfma_f32_16x16x32_bf16 v[20:23], a[140:143], a[196:199], v[20:23]\n"
    "s_add_u32 s67, s67, 49152\n"
    "ds_read_b128 a[112:115], v249 offset:0\n"
    "v_fma_f32 v207, v207, v37, -v39\n"
    "v_mfma_f32_16x16x32_bf16 v[20:23], a[144:147], a[200:203], v[20:23]\n"
    "s_add_u32 s59, s59, 4\n"
    "ds_read_b128 a[116:119], v249 offset:12288\n"
    "v_cvt_pk_bf16_f32 v204, v204, v205\n"
    "v_mfma_f32_16x16x32_bf16 v[20:23], a[148:151], a[204:207], v[20:23]\n"
    "ds_read_b128 a[120:123], v249 offset:1024\n"
    "v_cvt_pk_bf16_f32 v206, v206, v207\n"
    "v_mfma_f32_16x16x32_bf16 v[20:23], a[152:155], a[208:211], v[20:23]\n"
    "ds_read_b128 a[124:127], v249 offset:13312\n"
    "v_fma_f32 v208, v208, v36, -v38\n"
    "v_mfma_f32_16x16x32_bf16 v[20:23], a[156:159], a[212:215], v[20:23]\n"
    "ds_read_b128 a[128:131], v249 offset:2048\n"
    "v_fma_f32 v209, v209, v37, -v39\n"
    "v_mfma_f32_16x16x32_bf16 v[24:27], a[156:159], a[236:239], v[104:107]\n"
    "ds_read_b128 a[132:135], v249 offset:14336\n"
    "v_fma_f32 v210, v210, v36, -v38\n"
    "v_mfma_f32_16x16x32_bf16 v[24:27], a[152:155], a[232:235], v[24:27]\n"
    "v_exp_f32 v59, v59\n"
    "v_mfma_f32_16x16x32_bf16 v[24:27], a[148:151], a[228:231], v[24:27]\n"
    "v_cvt_pk_bf16_f32 v83, v58, v59\n"
    "v_fma_f32 v211, v211, v37, -v39\n"
    "v_mfma_f32_16x16x32_bf16 v[24:27], a[144:147], a[224:227], v[24:27]\n"
    "v_exp_f32 v60, v60\n"
    "v_mfma_f32_16x16x32_bf16 v[24:27], a[140:143], a[220:223], v[24:27]\n"
    "v_exp_f32 v61, v61\n"
    "v_mfma_f32_16x16x32_bf16 v[24:27], a[136:139], a[216:219], v[24:27]\n"
    "v_exp_f32 v62, v62\n"
    "v_mfma_f32_16x16x32_bf16 v[28:31], a[136:139], a[240:243], v[104:107]\n"
    "v_cvt_pk_bf16_f32 v90, v60, v61\n"
    "v_cvt_pk_bf16_f32 v208, v208, v209\n"
    "v_mfma_f32_16x16x32_bf16 v[28:31], a[140:143], a[244:247], v[28:31]\n"
    "v_exp_f32 v63, v63\n"
    "v_mfma_f32_16x16x32_bf16 v[28:31], a[144:147], a[248:251], v[28:31]\n"
    "v_cvt_pk_bf16_f32 v91, v62, v63\n"
    "v_cvt_pk_bf16_f32 v210, v210, v211\n"
    "v_mfma_f32_16x16x32_bf16 v[28:31], a[148:151], a[252:255], v[28:31]\n"
    "v_exp_f32 v50, v50\n"
    "v_mfma_f32_16x16x32_bf16 v[28:31], a[152:155], a[160:163], v[28:31]\n"
    "v_exp_f32 v51, v51\n"
    "v_mfma_f32_16x16x32_bf16 v[28:31], a[156:159], a[164:167], v[28:31]\n"
    "v_exp_f32 v64, v64\n"
    "v_mfma_f32_16x16x32_bf16 v[32:35], a[156:159], a[188:191], v[104:107]\n"
    "v_cvt_pk_bf16_f32 v97, v50, v51\n"
    "v_fma_f32 v212, v212, v36, -v38\n"
    "v_mfma_f32_16x16x32_bf16 v[32:35], a[152:155], a[184:187], v[32:35]\n"
    "v_exp_f32 v65, v65\n"
    "v_mfma_f32_16x16x32_bf16 v[32:35], a[148:151], a[180:183], v[32:35]\n"
    "v_cvt_pk_bf16_f32 v98, v64, v65\n"
    "v_fma_f32 v213, v213, v37, -v39\n"
    "v_mfma_f32_16x16x32_bf16 v[32:35], a[144:147], a[176:179], v[32:35]\n"
    "buffer_store_dword v204, v244, s[16:19], s76 offen offset:0 sc1\n"
    "v_mfma_f32_16x16x32_bf16 v[32:35], a[140:143], a[172:175], v[32:35]\n"
    "v_exp_f32 v66, v66\n"
    "v_mfma_f32_16x16x32_bf16 v[32:35], a[136:139], a[168:171], v[32:35]\n"
    "s_waitcnt lgkmcnt(0)\n"
    "v_fma_f32 v214, v214, v36, -v38\n"
    "v_fma_f32 v215, v215, v37, -v39\n"
    "v_mfma_f32_16x16x32_bf16 v[124:127], a[112:115], v[68:71], v[124:127]\n"
    "ds_read_b128 a[136:139], v249 offset:3072\n"
    "v_cvt_pk_bf16_f32 v212, v212, v213\n"
    "v_mfma_f32_16x16x32_bf16 v[124:127], a[116:119], v[72:75], v[124:127]\n"
    "ds_read_b128 a[140:143], v249 offset:15360\n"
    "v_cvt_pk_bf16_f32 v214, v214, v215\n"
    "v_mfma_f32_16x16x32_bf16 v[128:131], a[124:127], v[72:75], v[128:131]\n"
    "ds_read_b128 a[144:147], v249 offset:4096\n"
    "v_fma_f32 v216, v216, v36, -v38\n"
    "v_mfma_f32_16x16x32_bf16 v[128:131], a[120:123], v[68:71], v[128:131]\n"
    "ds_read_b128 a[148:151], v249 offset:16384\n"
    "v_fma_f32 v217, v217, v37, -v39\n"
    "v_mfma_f32_16x16x32_bf16 v[132:135], a[128:131], v[68:71], v[132:135]\n"
    "ds_read_b128 a[152:155], v249 offset:5120\n"
    "v_fma_f32 v218, v218, v36, -v38\n"
    "v_mfma_f32_16x16x32_bf16 v[132:135], a[132:135], v[72:75], v[132:135]\n"
    "ds_read_b128 a[156:159], v249 offset:17408\n"
    "v_fma_f32 v219, v219, v37, -v39\n"
    "v_mfma_f32_16x16x32_bf16 v[148:151], a[132:135], v[80:83], v[148:151]\n"
    "ds_read_b128 v[104:107], v251 offset:768\n"
    "v_cvt_pk_bf16_f32 v216, v216, v217\n"
    "v_mfma_f32_16x16x32_bf16 v[148:151], a[128:131], v[76:79], v[148:151]\n"
    "v_exp_f32 v67, v67\n"
    "v_mfma_f32_16x16x32_bf16 v[144:147], a[120:123], v[76:79], v[144:147]\n"
    "v_cvt_pk_bf16_f32 v99, v66, v67\n"
    "v_cvt_pk_bf16_f32 v218, v218, v219\n"
    "v_mfma_f32_16x16x32_bf16 v[144:147], a[124:127], v[80:83], v[144:147]\n"
    "buffer_store_dword v206, v244, s[16:19], s76 offen offset:128 sc1\n"
    "v_mfma_f32_16x16x32_bf16 v[140:143], a[116:119], v[80:83], v[140:143]\n"
    "v_exp_f32 v4, v4\n"
    "v_mfma_f32_16x16x32_bf16 v[140:143], a[112:115], v[76:79], v[140:143]\n"
    "buffer_store_dword v208, v244, s[16:19], s76 offen offset:1024 sc1\n"
    "v_mfma_f32_16x16x32_bf16 v[156:159], a[112:115], v[84:87], v[156:159]\n"
    "v_fma_f32 v220, v220, v36, -v38\n"
    "v_fma_f32 v221, v221, v37, -v39\n"
    "v_mfma_f32_16x16x32_bf16 v[156:159], a[116:119], v[88:91], v[156:159]\n"
    "buffer_store_dword v210, v244, s[16:19], s76 offen offset:1152 sc1\n"
    "v_mfma_f32_16x16x32_bf16 v[160:163], a[124:127], v[88:91], v[160:163]\n"
    "v_exp_f32 v5, v5\n"
    "v_mfma_f32_16x16x32_bf16 v[160:163], a[120:123], v[84:87], v[160:163]\n"
    "buffer_store_dword v212, v244, s[16:19], s76 offen offset:256 sc1\n"
    "v_mfma_f32_16x16x32_bf16 v[164:167], a[128:131], v[84:87], v[164:167]\n"
    "v_exp_f32 v6, v6\n"
    "v_mfma_f32_16x16x32_bf16 v[164:167], a[132:135], v[88:91], v[164:167]\n"
    "buffer_store_dword v214, v244, s[16:19], s76 offen offset:384 sc1\n"
    "v_mfma_f32_16x16x32_bf16 v[180:183], a[132:135], v[96:99], v[180:183]\n"
    "v_fma_f32 v222, v222, v36, -v38\n"
    "v_fma_f32 v223, v223, v37, -v39\n"
    "v_mfma_f32_16x16x32_bf16 v[180:183], a[128:131], v[92:95], v[180:183]\n"
    "v_exp_f32 v7, v7\n"
    "v_mfma_f32_16x16x32_bf16 v[176:179], a[120:123], v[92:95], v[176:179]\n"
    "v_exp_f32 v8, v8\n"
    "v_mfma_f32_16x16x32_bf16 v[176:179], a[124:127], v[96:99], v[176:179]\n"
    "v_cvt_pk_bf16_f32 v220, v220, v221\n"
    "v_cvt_pk_bf16_f32 v222, v222, v223\n"
    "v_mfma_f32_16x16x32_bf16 v[172:175], a[116:119], v[96:99], v[172:175]\n"
    "v_exp_f32 v9, v9\n"
    "v_mfma_f32_16x16x32_bf16 v[172:175], a[112:115], v[92:95], v[172:175]\n"
    "s_waitcnt lgkmcnt(0)\n"
    "v_exp_f32 v10, v10\n"
    "v_mfma_f32_16x16x32_bf16 v[136:139], a[136:139], v[68:71], v[136:139]\n"
    "ds_read_b128 a[112:115], v249 offset:6144\n"
    "v_fma_f32 v224, v224, v36, -v38\n"
    "v_mfma_f32_16x16x32_bf16 v[136:139], a[140:143], v[72:75], v[136:139]\n"
    "ds_read_b128 a[116:119], v249 offset:18432\n"
    "v_fma_f32 v225, v225, v37, -v39\n"
    "v_mfma_f32_16x16x32_bf16 v[188:191], a[148:151], v[72:75], v[188:191]\n"
    "ds_read_b128 a[120:123], v249 offset:7168\n"
    "v_fma_f32 v226, v226, v36, -v38\n"
    "v_mfma_f32_16x16x32_bf16 v[188:191], a[144:147], v[68:71], v[188:191]\n"
    "ds_read_b128 a[124:127], v249 offset:19456\n"
    "v_fma_f32 v227, v227, v37, -v39\n"
    "v_mfma_f32_16x16x32_bf16 v[192:195], a[152:155], v[68:71], v[192:195]\n"
    "ds_read_b128 a[128:131], v249 offset:8192\n"
    "v_cvt_pk_bf16_f32 v224, v224, v225\n"
    "v_mfma_f32_16x16x32_bf16 v[192:195], a[156:159], v[72:75], v[192:195]\n"
    "ds_read_b128 a[132:135], v249 offset:20480\n"
    "v_smfmac_f32_16x16x64_bf16 v[108:111], v[104:107], v[68:75], v253\n"
    "s_add_u32 s81, s81, 128\n"
    "s_cmp_gt_u32 s81, 4096\n"
    "s_cselect_b32 s81, 0, s81\n"
    "v_smfmac_f32_16x16x64_bf16 v[112:115], v[104:107], v[76:83], v253\n"
    "v_mfma_f32_16x16x32_bf16 a[4:7], a[156:159], v[80:83], a[4:7]\n"
    "v_exp_f32 v11, v11\n"
    "v_mfma_f32_16x16x32_bf16 a[4:7], a[152:155], v[76:79], a[4:7]\n"
    "v_exp_f32 v12, v12\n"
    "v_mfma_f32_16x16x32_bf16 a[0:3], a[144:147], v[76:79], a[0:3]\n"
    "v_exp_f32 v13, v13\n"
    "v_mfma_f32_16x16x32_bf16 a[0:3], a[148:151], v[80:83], a[0:3]\n"
    "v_exp_f32 v14, v14\n"
    "v_mfma_f32_16x16x32_bf16 v[152:155], a[140:143], v[80:83], v[152:155]\n"
    "v_exp_f32 v15, v15\n"
    "v_mfma_f32_16x16x32_bf16 v[152:155], a[136:139], v[76:79], v[152:155]\n"
    "v_cvt_pk_bf16_f32 v226, v226, v227\n"
    "v_fma_f32 v228, v228, v36, -v38\n"
    "v_mfma_f32_16x16x32_bf16 v[168:171], a[136:139], v[84:87], v[168:171]\n"
    "v_exp_f32 v16, v16\n"
    "v_mfma_f32_16x16x32_bf16 v[168:171], a[140:143], v[88:91], v[168:171]\n"
    "v_exp_f32 v17, v17\n"
    "v_mfma_f32_16x16x32_bf16 a[16:19], a[148:151], v[88:91], a[16:19]\n"
    "v_fma_f32 v229, v229, v37, -v39\n"
    "v_fma_f32 v230, v230, v36, -v38\n"
    "v_mfma_f32_16x16x32_bf16 a[16:19], a[144:147], v[84:87], a[16:19]\n"
    "v_exp_f32 v18, v18\n"
    "v_mfma_f32_16x16x32_bf16 a[20:23], a[152:155], v[84:87], a[20:23]\n"
    "v_fma_f32 v231, v231, v37, -v39\n"
    "v_mfma_f32_16x16x32_bf16 a[20:23], a[156:159], v[88:91], a[20:23]\n"
    "v_smfmac_f32_16x16x64_bf16 v[116:119], v[104:107], v[84:91], v253\n"
    "v_smfmac_f32_16x16x64_bf16 v[120:123], v[104:107], v[92:99], v253\n"
    "v_mfma_f32_16x16x32_bf16 a[36:39], a[156:159], v[96:99], a[36:39]\n"
    "v_exp_f32 v19, v19\n"
    "v_mfma_f32_16x16x32_bf16 a[36:39], a[152:155], v[92:95], a[36:39]\n"
    "v_exp_f32 v20, v20\n"
    "v_mfma_f32_16x16x32_bf16 a[32:35], a[144:147], v[92:95], a[32:35]\n"
    "v_cvt_pk_bf16_f32 v228, v228, v229\n"
    "v_cvt_pk_bf16_f32 v230, v230, v231\n"
    "v_mfma_f32_16x16x32_bf16 a[32:35], a[148:151], v[96:99], a[32:35]\n"
    "v_exp_f32 v21, v21\n"
    "v_mfma_f32_16x16x32_bf16 v[184:187], a[140:143], v[96:99], v[184:187]\n"
    "v_fma_f32 v232, v232, v36, -v38\n"
    "v_mfma_f32_16x16x32_bf16 v[184:187], a[136:139], v[92:95], v[184:187]\n"
    "s_waitcnt lgkmcnt(0)\n"
    "v_mfma_f32_16x16x32_bf16 v[196:199], a[112:115], v[68:71], v[196:199]\n"
    "ds_read_b128 a[136:139], v249 offset:9216\n"
    "v_fma_f32 v233, v233, v37, -v39\n"
    "v_mfma_f32_16x16x32_bf16 v[196:199], a[116:119], v[72:75], v[196:199]\n"
    "ds_read_b128 a[140:143], v249 offset:21504\n"
    "v_mfma_f32_16x16x32_bf16 v[200:203], a[124:127], v[72:75], v[200:203]\n"
    "ds_read_b128 a[144:147], v249 offset:10240\n"
    "v_mfma_f32_16x16x32_bf16 v[200:203], a[120:123], v[68:71], v[200:203]\n"
    "ds_read_b128 a[148:151], v249 offset:22528\n"
    "v_fma_f32 v234, v234, v36, -v38\n"
    "v_mfma_f32_16x16x32_bf16 a[48:51], a[128:131], v[68:71], a[48:51]\n"
    "buffer_load_dwordx2 v[236:237], v241, s[36:39], s81 offen offset:0 sc1\n"
    "v_mfma_f32_16x16x32_bf16 a[48:51], a[132:135], v[72:75], a[48:51]\n"
    "ds_read_b128 a[152:155], v249 offset:11264\n"
    "v_fma_f32 v235, v235, v37, -v39\n"
    "v_mfma_f32_16x16x32_bf16 a[64:67], a[132:135], v[80:83], a[64:67]\n"
    "ds_read_b128 a[156:159], v249 offset:23552\n"
    "v_mfma_f32_16x16x32_bf16 a[64:67], a[128:131], v[76:79], a[64:67]\n"
    "buffer_store_dword v216, v244, s[16:19], s76 offen offset:1280 sc1\n"
    "v_mfma_f32_16x16x32_bf16 a[12:15], a[120:123], v[76:79], a[12:15]\n"
    "v_exp_f32 v22, v22\n"
    "v_mfma_f32_16x16x32_bf16 a[12:15], a[124:127], v[80:83], a[12:15]\n"
    "buffer_store_dword v218, v244, s[16:19], s76 offen offset:1408 sc1\n"
    "v_mfma_f32_16x16x32_bf16 a[8:11], a[116:119], v[80:83], a[8:11]\n"
    "v_exp_f32 v23, v23\n"
    "v_mfma_f32_16x16x32_bf16 a[8:11], a[112:115], v[76:79], a[8:11]\n"
    "buffer_store_dword v220, v244, s[16:19], s77 offen offset:0 sc1\n"
    "v_mfma_f32_16x16x32_bf16 a[24:27], a[112:115], v[84:87], a[24:27]\n"
    "v_exp_f32 v24, v24\n"
    "v_mfma_f32_16x16x32_bf16 a[24:27], a[116:119], v[88:91], a[24:27]\n"
    "buffer_store_dword v222, v244, s[16:19], s77 offen offset:128 sc1\n"
    "v_mfma_f32_16x16x32_bf16 a[28:31], a[124:127], v[88:91], a[28:31]\n"
    "v_exp_f32 v25, v25\n"
    "v_mfma_f32_16x16x32_bf16 a[28:31], a[120:123], v[84:87], a[28:31]\n"
    "buffer_store_dword v224, v244, s[16:19], s77 offen offset:1024 sc1\n"
    "v_mfma_f32_16x16x32_bf16 a[80:83], a[128:131], v[84:87], a[80:83]\n"
    "v_exp_f32 v26, v26\n"
    "v_mfma_f32_16x16x32_bf16 a[80:83], a[132:135], v[88:91], a[80:83]\n"
    "buffer_store_dword v226, v244, s[16:19], s77 offen offset:1152 sc1\n"
    "v_mfma_f32_16x16x32_bf16 a[96:99], a[132:135], v[96:99], a[96:99]\n"
    "v_cvt_pk_bf16_f32 v232, v232, v233\n"
    "v_cvt_pk_bf16_f32 v234, v234, v235\n"
    "v_mfma_f32_16x16x32_bf16 a[96:99], a[128:131], v[92:95], a[96:99]\n"
    "buffer_store_dword v228, v244, s[16:19], s77 offen offset:256 sc1\n"
    "v_mfma_f32_16x16x32_bf16 a[44:47], a[120:123], v[92:95], a[44:47]\n"
    "v_exp_f32 v27, v27\n"
    "v_mfma_f32_16x16x32_bf16 a[44:47], a[124:127], v[96:99], a[44:47]\n"
    "buffer_store_dword v230, v244, s[16:19], s77 offen offset:384 sc1\n"
    "v_mfma_f32_16x16x32_bf16 a[40:43], a[116:119], v[96:99], a[40:43]\n"
    "v_exp_f32 v28, v28\n"
    "v_mfma_f32_16x16x32_bf16 a[40:43], a[112:115], v[92:95], a[40:43]\n"
    "s_waitcnt lgkmcnt(0)\n"
    "v_exp_f32 v29, v29\n"
    "v_mfma_f32_16x16x32_bf16 a[52:55], a[136:139], v[68:71], a[52:55]\n"
    "ds_read_b128 a[112:115], v3 offset:12288\n"
    "v_mfma_f32_16x16x32_bf16 a[52:55], a[140:143], v[72:75], a[52:55]\n"
    "ds_read_b128 a[116:119], v3 offset:14336\n"
    "v_mfma_f32_16x16x32_bf16 a[56:59], a[148:151], v[72:75], a[56:59]\n"
    "ds_read_b128 a[120:123], v3 offset:16384\n"
    "v_mfma_f32_16x16x32_bf16 a[56:59], a[144:147], v[68:71], a[56:59]\n"
    "ds_read_b128 a[124:127], v3 offset:18432\n"
    "v_mfma_f32_16x16x32_bf16 a[60:63], a[152:155], v[68:71], a[60:63]\n"
    "ds_read_b128 a[128:131], v3 offset:20480\n"
    "v_mfma_f32_16x16x32_bf16 a[60:63], a[156:159], v[72:75], a[60:63]\n"
    "ds_read_b128 a[132:135], v3 offset:22528\n"
    "v_mfma_f32_16x16x32_bf16 a[76:79], a[156:159], v[80:83], a[76:79]\n"
    "ds_read_b128 v[100:103], v250 offset:1664\n"
    "v_mfma_f32_16x16x32_bf16 a[76:79], a[152:155], v[76:79], a[76:79]\n"
    "buffer_store_dword v232, v244, s[16:19], s77 offen offset:1280 sc1\n"
    "v_mfma_f32_16x16x32_bf16 a[72:75], a[144:147], v[76:79], a[72:75]\n"
    "v_exp_f32 v30, v30\n"
    "v_mfma_f32_16x16x32_bf16 a[72:75], a[148:151], v[80:83], a[72:75]\n"
    "buffer_store_dword v234, v244, s[16:19], s77 offen offset:1408 sc1\n"
    "v_mfma_f32_16x16x32_bf16 a[68:71], a[140:143], v[80:83], a[68:71]\n"
    "v_exp_f32 v31, v31\n"
    "v_mfma_f32_16x16x32_bf16 a[68:71], a[136:139], v[76:79], a[68:71]\n"
    "v_exp_f32 v32, v32\n"
    "v_mfma_f32_16x16x32_bf16 a[84:87], a[136:139], v[84:87], a[84:87]\n"
    "v_exp_f32 v33, v33\n"
    "v_mfma_f32_16x16x32_bf16 a[84:87], a[140:143], v[88:91], a[84:87]\n"
    "v_exp_f32 v34, v34\n"
    "v_mfma_f32_16x16x32_bf16 a[88:91], a[148:151], v[88:91], a[88:91]\n"
    "s_waitcnt vmcnt(17)\n"
    "ds_add_u32 v247, v246\n"
    "v_mfma_f32_16x16x32_bf16 a[88:91], a[144:147], v[84:87], a[88:91]\n"
    "v_exp_f32 v35, v35\n"
    "v_mfma_f32_16x16x32_bf16 a[92:95], a[152:155], v[84:87], a[92:95]\n"
    "v_mfma_f32_16x16x32_bf16 a[92:95], a[156:159], v[88:91], a[92:95]\n"
    "v_mfma_f32_16x16x32_bf16 a[108:111], a[156:159], v[96:99], a[108:111]\n"
    "v_mfma_f32_16x16x32_bf16 a[108:111], a[152:155], v[92:95], a[108:111]\n"
    "v_mfma_f32_16x16x32_bf16 a[104:107], a[144:147], v[92:95], a[104:107]\n"
    "s_sub_u32 s74, s68, s65\n"
    "v_add_u32 v250, 1536, v250\n"
    "v_add_u32 v251, 1536, v251\n"
    "v_mfma_f32_16x16x32_bf16 a[104:107], a[148:151], v[96:99], a[104:107]\n"
    "s_add_u32 s66, s66, 1\n"
    "v_mov_b32 v2, v3\n"
    "v_add_u32 v3, s74, v252\n"
    "v_mfma_f32_16x16x32_bf16 a[100:103], a[140:143], v[96:99], a[100:103]\n"
    "s_waitcnt lgkmcnt(1)\n"
    "s_cmp_lt_u32 s66, 28\n"
    "v_mfma_f32_16x16x32_bf16 a[100:103], a[136:139], v[92:95], a[100:103]\n"
    "s_cbranch_scc1 .LPK\n"
    "s_branch .Ltail\n"
    "s_nop 0\n"
    ".LpollAK:\n"
    "s_sleep 1\n"
    "buffer_load_dwordx2 v[236:237], v87, s[36:39], 0 offen offset:0 sc1\n"
    "s_waitcnt vmcnt(0)\n"
    "v_cmp_eq_u64 s[54:55], v[236:237], v[238:239]\n"
    "s_add_u32 s58, s58, 1\n"
    "s_cmp_eq_u64 s[54:55], -1\n"
    "s_cbranch_scc1 .LpollAKr\n"
    "s_cmp_lt_u32 s58, 0x80000\n"
    "s_cbranch_scc1 .LpollAK\n"
    "s_branch .LpollAKr\n"
    "s_nop 0\n"
    "s_nop 0\n"
    "s_nop 0\n"
    ".LpollAV:\n"
    "s_sleep 1\n"
    "buffer_load_dwordx2 v[236:237], v87, s[36:39], 0 offen offset:0 sc1\n"
    "s_waitcnt vmcnt(0)\n"
    "v_cmp_eq_u64 s[54:55], v[236:237], v[238:239]\n"
    "s_add_u32 s58, s58, 1\n"
    "s_cmp_eq_u64 s[54:55], -1\n"
    "s_cbranch_scc1 .LpollAVr\n"
    "s_cmp_lt_u32 s58, 0x80000\n"
    "s_cbranch_scc1 .LpollAV\n"
    "s_branch .LpollAVr\n"
    "s_nop 0\n"
    "s_nop 0\n"
    "s_nop 0\n"
    "s_nop 0\n"
    "s_nop 0\n"
    "s_nop 0\n"
    "s_nop 0\n"
    ".Lpoll1:\n"
    "s_sleep 1\n"
    "v_cmp_eq_u64 s[54:55], v[236:237], v[238:239]\n"
    "s_nop 1\n"
    "s_cmp_eq_u64 s[54:55], -1\n"
    "s_cbranch_scc1 .Lpoll1l\n"
    "buffer_load_dwordx2 v[236:237], v241, s[36:39], s81 offen offset:0 sc1\n"
    "s_waitcnt vmcnt(0)\n"
    "v_cmp_eq_u64 s[54:55], v[236:237], v[238:239]\n"
    ".Lpoll1l:\n"
    "ds_read_b32 v245, v247\n"
    "s_waitcnt lgkmcnt(0)\n"
    "v_cmp_le_u32 s[56:57], s59, v245\n"
    "s_add_u32 s58, s58, 1\n"
    "s_nop 1\n"
    "s_and_b64 s[54:55], s[54:55], s[56:57]\n"
    "s_cmp_eq_u64 s[54:55], -1\n"
    "s_cbranch_scc1 .Lpoll1r\n"
    "s_cmp_lt_u32 s58, 0x80000\n"
    "s_cbranch_scc1 .Lpoll1\n"
    "s_branch .Lpoll1r\n"
    "s_nop 0\n"
    "s_nop 0\n"
    "s_nop 0\n"
    "s_nop 0\n"
    "s_nop 0\n"
    "s_nop 0\n"
    "s_nop 0\n"
    ".Lpoll2:\n"
    "s_sleep 1\n"
    "v_cmp_eq_u64 s[54:55], v[236:237], v[238:239]\n"
    "s_nop 1\n"
    "s_cmp_eq_u64 s[54:55], -1\n"
    "s_cbranch_scc1 .Lpoll2l\n"
    "buffer_load_dwordx2 v[236:237], v241, s[36:39], s81 offen offset:0 sc1\n"
    "s_waitcnt vmcnt(0)\n"
    "v_cmp_eq_u64 s[54:55], v[236:237], v[238:239]\n"
    ".Lpoll2l:\n"
    "ds_read_b32 v245, v247\n"
    "s_waitcnt lgkmcnt(0)\n"
    "v_cmp_le_u32 s[56:57], s59, v245\n"
    "s_add_u32 s58, s58, 1\n"
    "s_nop 1\n"
    "s_and_b64 s[54:55], s[54:55], s[56:57]\n"
    "s_cmp_eq_u64 s[54:55], -1\n"
    "s_cbranch_scc1 .Lpoll2r\n"
    "s_cmp_lt_u32 s58, 0x80000\n"
    "s_cbranch_scc1 .Lpoll2\n"
    "s_branch .Lpoll2r\n"
    "s_nop 0\n"
    "s_nop 0\n"
    "s_nop 0\n"
    "s_nop 0\n"
    "s_nop 0\n"
    "s_nop 0\n"
    "s_nop 0\n"
    ".Lpoll3:\n"
    "s_sleep 1\n"
    "v_cmp_eq_u64 s[54:55], v[236:237], v[238:239]\n"
    "s_nop 1\n"
    "s_cmp_eq_u64 s[54:55], -1\n"
    "s_cbranch_scc1 .Lpoll3l\n"
    "buffer_load_dwordx2 v[236:237], v241, s[36:39], s81 offen offset:0 sc1\n"
    "s_waitcnt vmcnt(0)\n"
    "v_cmp_eq_u64 s[54:55], v[236:237], v[238:239]\n"
    ".Lpoll3l:\n"
    "ds_read_b32 v245, v247\n"
    "s_waitcnt lgkmcnt(0)\n"
    "v_cmp_le_u32 s[56:57], s59, v245\n"
    "s_add_u32 s58, s58, 1\n"
    "s_nop 1\n"
    "s_and_b64 s[54:55], s[54:55], s[56:57]\n"
    "s_cmp_eq_u64 s[54:55], -1\n"
    "s_cbranch_scc1 .Lpoll3r\n"
    "s_cmp_lt_u32 s58, 0x80000\n"
    "s_cbranch_scc1 .Lpoll3\n"
    "s_branch .Lpoll3r\n"
    "s_nop 0\n"
    "s_nop 0\n"
    "s_nop 0\n"
    "s_nop 0\n"
    "s_nop 0\n"
    "s_nop 0\n"
    "s_nop 0\n"
    ".Lpoll4:\n"
    "s_sleep 1\n"
    "v_cmp_eq_u64 s[54:55], v[236:237], v[238:239]\n"
    "s_nop 1\n"
    "s_cmp_eq_u64 s[54:55], -1\n"
    "s_cbranch_scc1 .Lpoll4l\n"
    "buffer_load_dwordx2 v[236:237], v241, s[36:39], s81 offen offset:0 sc1\n"
    "s_waitcnt vmcnt(0)\n"
    "v_cmp_eq_u64 s[54:55], v[236:237], v[238:239]\n"
    ".Lpoll4l:\n"
    "ds_read_b32 v245, v247\n"
    "s_waitcnt lgkmcnt(0)\n"
    "v_cmp_le_u32 s[56:57], s59, v245\n"
    "s_add_u32 s58, s58, 1\n"
    "s_nop 1\n"
    "s_and_b64 s[54:55], s[54:55], s[56:57]\n"
    "s_cmp_eq_u64 s[54:55], -1\n"
    "s_cbranch_scc1 .Lpoll4r\n"
    "s_cmp_lt_u32 s58, 0x80000\n"
    "s_cbranch_scc1 .Lpoll4\n"
    "s_branch .Lpoll4r\n"
    "s_nop 0\n"
    "s_nop 0\n"
    "s_nop 0\n"
    "s_nop 0\n"
    "s_nop 0\n"
    "s_nop 0\n"
    "s_nop 0\n"
    ".Lpt0:\n"
    "s_sleep 1\n"
    "v_cmp_eq_u64 s[54:55], v[236:237], v[238:239]\n"
    "s_nop 1\n"
    "s_cmp_eq_u64 s[54:55], -1\n"
    "s_cbranch_scc1 .Lpt0l\n"
    "buffer_load_dwordx2 v[236:237], v241, s[36:39], s81 offen offset:0 sc1\n"
    "s_waitcnt vmcnt(0)\n"
    "v_cmp_eq_u64 s[54:55], v[236:237], v[238:239]\n"
    ".Lpt0l:\n"
    "ds_read_b32 v245, v247\n"
    "s_waitcnt lgkmcnt(0)\n"
    "v_cmp_le_u32 s[56:57], s59, v245\n"
    "s_add_u32 s58, s58, 1\n"
    "s_nop 1\n"
    "s_and_b64 s[54:55], s[54:55], s[56:57]\n"
    "s_cmp_eq_u64 s[54:55], -1\n"
    "s_cbranch_scc1 .Lpt0r\n"
    "s_cmp_lt_u32 s58, 0x80000\n"
    "s_cbranch_scc1 .Lpt0\n"
    "s_branch .Lpt0r\n"
    "s_nop 0\n"
    "s_nop 0\n"
    "s_nop 0\n"
    "s_nop 0\n"
    "s_nop 0\n"
    "s_nop 0\n"
    "s_nop 0\n"
    ".Lpt1:\n"
    "s_sleep 1\n"
    "v_cmp_eq_u64 s[54:55], v[236:237], v[238:239]\n"
    "s_nop 1\n"
    "s_cmp_eq_u64 s[54:55], -1\n"
    "s_cbranch_scc1 .Lpt1l\n"
    "buffer_load_dwordx2 v[236:237], v241, s[36:39], s81 offen offset:0 sc1\n"
    "s_waitcnt vmcnt(0)\n"
    "v_cmp_eq_u64 s[54:55], v[236:237], v[238:239]\n"
    ".Lpt1l:\n"
    "ds_read_b32 v245, v247\n"
    "s_waitcnt lgkmcnt(0)\n"
    "v_cmp_le_u32 s[56:57], s59, v245\n"
    "s_add_u32 s58, s58, 1\n"
    "s_nop 1\n"
    "s_and_b64 s[54:55], s[54:55], s[56:57]\n"
    "s_cmp_eq_u64 s[54:55], -1\n"
    "s_cbranch_scc1 .Lpt1r\n"
    "s_cmp_lt_u32 s58, 0x80000\n"
    "s_cbranch_scc1 .Lpt1\n"
    "s_branch .Lpt1r\n"
    "s_nop 0\n"
    "s_nop 0\n"
    "s_nop 0\n"
    ".Lpt2:\n"
    "s_sleep 1\n"
    "ds_read_b32 v245, v247\n"
    "s_waitcnt lgkmcnt(0)\n"
    "v_cmp_le_u32 s[56:57], s59, v245\n"
    "s_add_u32 s58, s58, 1\n"
    "s_nop 1\n"
    "s_cmp_eq_u64 s[56:57], -1\n"
    "s_cbranch_scc1 .Lpt2r\n"
    "s_cmp_lt_u32 s58, 0x80000\n"
    "s_cbranch_scc1 .Lpt2\n"
    "s_branch .Lpt2r\n"
    "s_nop 0\n"
    "s_nop 0\n"
    ".Lpt3:\n"
    "s_sleep 1\n"
    "ds_read_b32 v245, v247\n"
    "s_waitcnt lgkmcnt(0)\n"
    "v_cmp_le_u32 s[56:57], s59, v245\n"
    "s_add_u32 s58, s58, 1\n"
    "s_nop 1\n"
    "s_cmp_eq_u64 s[56:57], -1\n"
    "s_cbranch_scc1 .Lpt3r\n"
    "s_cmp_lt_u32 s58, 0x80000\n"
    "s_cbranch_scc1 .Lpt3\n"
    "s_branch .Lpt3r\n"
    ".fill ((0x20000 - (.LwVe - .LwV) - (. - _Z8attn_fwdPKtPKiS2_S0_S0_S0_S0_PcPt)) & 4095) / 4, 4, 0xBF800000\n"
    ".LwV:\n"
    "s_branch .LwVe\n"
    ".LPV:\n"
    "v_mfma_f32_16x16x32_bf16 v[36:39], a[112:115], a[192:195], v[100:103]\n"
    "s_add_u32 s71, s71, s44\n"
    "ds_read_b128 a[136:139], v2 offset:13312\n"
    "v_cvt_pk_bf16_f32 v68, v4, v5\n"
    "v_mfma_f32_16x16x32_bf16 v[36:39], a[116:119], a[196:199], v[36:39]\n"
    "s_add_u32 s75, s71, s63\n"
    "ds_read_b128 a[140:143], v2 offset:15360\n"
    "v_cvt_pk_bf16_f32 v69, v6, v7\n"
    "v_mfma_f32_16x16x32_bf16 v[36:39], a[120:123], a[200:203], v[36:39]\n"
    "ds_read_b128 a[144:147], v2 offset:17408\n"
    "v_cvt_pk_bf16_f32 v76, v8, v9\n"
    "v_mfma_f32_16x16x32_bf16 v[36:39], a[124:127], a[204:207], v[36:39]\n"
    "ds_read_b128 a[148:151], v2 offset:19456\n"
    "v_cvt_pk_bf16_f32 v77, v10, v11\n"
    "v_mfma_f32_16x16x32_bf16 v[36:39], a[128:131], a[208:211], v[36:39]\n"
    "ds_read_b128 a[152:155], v2 offset:21504\n"
    "v_cvt_pk_bf16_f32 v84, v12, v13\n"
    "v_mfma_f32_16x16x32_bf16 v[36:39], a[132:135], a[212:215], v[36:39]\n"
    "ds_read_b128 a[156:159], v2 offset:23552\n"
    "v_cvt_pk_bf16_f32 v85, v14, v15\n"
    "v_mfma_f32_16x16x32_bf16 v[40:43], a[132:135], a[236:239], v[100:103]\n"
    "ds_read_b128 v[104:107], v250 offset:144\n"
    "v_cvt_pk_bf16_f32 v92, v16, v17\n"
    "v_mfma_f32_16x16x32_bf16 v[40:43], a[128:131], a[232:235], v[40:43]\n"
    "s_add_u32 s68, s68, 24576\n"
    "v_cvt_pk_bf16_f32 v93, v18, v19\n"
    "v_cvt_pk_bf16_f32 v70, v20, v21\n"
    "v_mfma_f32_16x16x32_bf16 v[40:43], a[124:127], a[228:231], v[40:43]\n"
    "s_cmp_ge_u32 s68, s69\n"
    "s_cselect_b32 s68, s65, s68\n"
    "v_cvt_pk_bf16_f32 v71, v22, v23\n"
    "v_mfma_f32_16x16x32_bf16 v[40:43], a[120:123], a[224:227], v[40:43]\n"
    "v_cvt_pk_bf16_f32 v78, v24, v25\n"
    "v_cvt_pk_bf16_f32 v79, v26, v27\n"
    "v_mfma_f32_16x16x32_bf16 v[40:43], a[116:119], a[220:223], v[40:43]\n"
    "v_cvt_pk_bf16_f32 v86, v28, v29\n"
    "v_cvt_pk_bf16_f32 v87, v30, v31\n"
    "v_mfma_f32_16x16x32_bf16 v[40:43], a[112:115], a[216:219], v[40:43]\n"
    "v_cvt_pk_bf16_f32 v94, v32, v33\n"
    "v_cvt_pk_bf16_f32 v95, v34, v35\n"
    "v_mfma_f32_16x16x32_bf16 v[44:47], a[112:115], a[240:243], v[100:103]\n"
    "v_exp_f32 v36, v36\n"
    "v_mfma_f32_16x16x32_bf16 v[44:47], a[116:119], a[244:247], v[44:47]\n"
    "v_exp_f32 v37, v37\n"
    "v_mfma_f32_16x16x32_bf16 v[44:47], a[120:123], a[248:251], v[44:47]\n"
    "v_exp_f32 v38, v38\n"
    "v_mfma_f32_16x16x32_bf16 v[44:47], a[124:127], a[252:255], v[44:47]\n"
    "v_exp_f32 v39, v39\n"
    "v_mfma_f32_16x16x32_bf16 v[44:47], a[128:131], a[160:163], v[44:47]\n"
    "v_cvt_pk_bf16_f32 v72, v36, v37\n"
    "v_cvt_pk_bf16_f32 v73, v38, v39\n"
    "v_mfma_f32_16x16x32_bf16 v[44:47], a[132:135], a[164:167], v[44:47]\n"
    "v_exp_f32 v40, v40\n"
    "v_mfma_f32_16x16x32_bf16 v[48:51], a[132:135], a[188:191], v[100:103]\n"
    "ds_read_b32 v245, v247\n"
    "v_mfma_f32_16x16x32_bf16 v[48:51], a[128:131], a[184:187], v[48:51]\n"
    "s_add_u32 m0, s68, 0\n"
    "v_exp_f32 v41, v41\n"
    "v_mfma_f32_16x16x32_bf16 v[48:51], a[124:127], a[180:183], v[48:51]\n"
    "v_exp_f32 v42, v42\n"
    "v_mfma_f32_16x16x32_bf16 v[48:51], a[120:123], a[176:179], v[48:51]\n"
    "v_exp_f32 v43, v43\n"
    "v_mfma_f32_16x16x32_bf16 v[48:51], a[116:119], a[172:175], v[48:51]\n"
    "s_waitcnt vmcnt(13)\n"
    "v_cmp_eq_u64 s[54:55], v[236:237], v[238:239]\n"
    "v_cvt_pk_bf16_f32 v80, v40, v41\n"
    "v_mfma_f32_16x16x32_bf16 v[48:51], a[112:115], a[168:171], v[48:51]\n"
    "s_waitcnt lgkmcnt(1)\n"
    "v_cvt_pk_bf16_f32 v81, v42, v43\n"
    "v_mfma_f32_16x16x32_bf16 v[52:55], a[136:139], a[192:195], v[104:107]\n"
    "s_waitcnt lgkmcnt(0)\n"
    "v_cmp_le_u32 s[56:57], s59, v245\n"
    "v_mfma_f32_16x16x32_bf16 v[52:55], a[140:143], a[196:199], v[52:55]\n"
    "v_exp_f32 v44, v44\n"
    "v_mfma_f32_16x16x32_bf16 v[52:55], a[144:147], a[200:203], v[52:55]\n"
    "s_and_b64 s[54:55], s[54:55], s[56:57]\n"
    "s_cmp_eq_u64 s[54:55], -1\n"
    "v_mfma_f32_16x16x32_bf16 v[52:55], a[148:151], a[204:207], v[52:55]\n"
    "s_cbranch_scc0 .Lpoll3\n"
    ".Lpoll3r:\n"
    "v_mfma_f32_16x16x32_bf16 v[52:55], a[152:155], a[208:211], v[52:55]\n"
    "buffer_load_dwordx4 v1, s[16:19], s67 offen offset:0 sc1 lds\n"
    "v_mfma_f32_16x16x32_bf16 v[52:55], a[156:159], a[212:215], v[52:55]\n"
    "ds_read_b128 a[112:115], v3 offset:0\n"
    "v_mfma_f32_16x16x32_bf16 v[56:59], a[156:159], a[236:239], v[104:107]\n"
    "ds_read_b128 a[116:119], v3 offset:2048\n"
    "v_mfma_f32_16x16x32_bf16 v[56:59], a[152:155], a[232:235], v[56:59]\n"
    "ds_read_b128 a[120:123], v3 offset:4096\n"
    "v_mfma_f32_16x16x32_bf16 v[56:59], a[148:151], a[228:231], v[56:59]\n"
    "buffer_load_dwordx4 v1, s[16:19], s67 offen offset:1024 sc1 lds\n"
    "v_mfma_f32_16x16x32_bf16 v[56:59], a[144:147], a[224:227], v[56:59]\n"
    "ds_read_b128 a[124:127], v3 offset:6144\n"
    "v_mfma_f32_16x16x32_bf16 v[56:59], a[140:143], a[220:223], v[56:59]\n"
    "ds_read_b128 a[128:131], v3 offset:8192\n"
    "v_mfma_f32_16x16x32_bf16 v[56:59], a[136:139], a[216:219], v[56:59]\n"
    "ds_read_b128 a[132:135], v3 offset:10240\n"
    "v_mfma_f32_16x16x32_bf16 v[60:63], a[136:139], a[240:243], v[104:107]\n"
    "buffer_load_dwordx4 v1, s[16:19], s67 offen offset:2048 sc1 lds\n"
    "v_mfma_f32_16x16x32_bf16 v[60:63], a[140:143], a[244:247], v[60:63]\n"
    "ds_read_b128 v[100:103], v250 offset:768\n"
    "v_mfma_f32_16x16x32_bf16 v[60:63], a[144:147], a[248:251], v[60:63]\n"
    "v_exp_f32 v52, v52\n"
    "v_mfma_f32_16x16x32_bf16 v[60:63], a[148:151], a[252:255], v[60:63]\n"
    "v_exp_f32 v53, v53\n"
    "v_mfma_f32_16x16x32_bf16 v[60:63], a[152:155], a[160:163], v[60:63]\n"
    "buffer_load_dwordx4 v1, s[16:19], s67 offen offset:3072 sc1 lds\n"
    "v_mfma_f32_16x16x32_bf16 v[60:63], a[156:159], a[164:167], v[60:63]\n"
    "s_add_u32 m0, s68, 4096\n"
    "v_exp_f32 v54, v54\n"
    "v_mfma_f32_16x16x32_bf16 v[64:67], a[156:159], a[188:191], v[104:107]\n"
    "s_add_u32 s83, s67, 4096\n"
    "v_exp_f32 v55, v55\n"
    "v_mfma_f32_16x16x32_bf16 v[64:67], a[152:155], a[184:187], v[64:67]\n"
    "v_cvt_pk_bf16_f32 v74, v52, v53\n"
    "v_cvt_pk_bf16_f32 v75, v54, v55\n"
    "v_mfma_f32_16x16x32_bf16 v[64:67], a[148:151], a[180:183], v[64:67]\n"
    "buffer_load_dwordx4 v1, s[16:19], s83 offen offset:0 sc1 lds\n"
    "v_mfma_f32_16x16x32_bf16 v[64:67], a[144:147], a[176:179], v[64:67]\n"
    "v_exp_f32 v56, v56\n"
    "v_mfma_f32_16x16x32_bf16 v[64:67], a[140:143], a[172:175], v[64:67]\n"
    "v_exp_f32 v57, v57\n"
    "v_mfma_f32_16x16x32_bf16 v[64:67], a[136:139], a[168:171], v[64:67]\n"
    "s_waitcnt lgkmcnt(0)\n"
    "v_exp_f32 v58, v58\n"
    "v_mfma_f32_16x16x32_bf16 v[4:7], a[112:115], a[192:195], v[100:103]\n"
    "buffer_load_dwordx4 v1, s[16:19], s83 offen offset:1024 sc1 lds\n"
    "v_mfma_f32_16x16x32_bf16 v[4:7], a[116:119], a[196:199], v[4:7]\n"
    "s_add_u32 m0, s64, 0\n"
    "ds_read_b128 a[136:139], v3 offset:1024\n"
    "v_cvt_pk_bf16_f32 v82, v56, v57\n"
    "v_mfma_f32_16x16x32_bf16 v[4:7], a[120:123], a[200:203], v[4:7]\n"
    "s_add_u32 s83, s67, 24576\n"
    "ds_read_b128 a[140:143], v3 offset:3072\n"
    "v_mfma_f32_16x16x32_bf16 v[4:7], a[124:127], a[204:207], v[4:7]\n"
    "ds_read_b128 a[144:147], v3 offset:5120\n"
    "v_mfma_f32_16x16x32_bf16 v[4:7], a[128:131], a[208:211], v[4:7]\n"
    "buffer_load_dwordx4 v1, s[16:19], s83 offen offset:0 sc1 lds\n"
    "v_mfma_f32_16x16x32_bf16 v[4:7], a[132:135], a[212:215], v[4:7]\n"
    "ds_read_b128 a[148:151], v3 offset:7168\n"
    "v_mfma_f32_16x16x32_bf16 v[8:11], a[132:135], a[236:239], v[100:103]\n"
    "ds_read_b128 a[152:155], v3 offset:9216\n"
    "v_mfma_f32_16x16x32_bf16 v[8:11], a[128:131], a[232:235], v[8:11]\n"
    "ds_read_b128 a[156:159], v3 offset:11264\n"
    "v_mfma_f32_16x16x32_bf16 v[8:11], a[124:127], a[228:231], v[8:11]\n"
    "buffer_load_dwordx4 v1, s[16:19], s83 offen offset:1024 sc1 lds\n"
    "v_mfma_f32_16x16x32_bf16 v[8:11], a[120:123], a[224:227], v[8:11]\n"
    "ds_read_b128 v[104:107], v250 offset:784\n"
    "v_mfma_f32_16x16x32_bf16 v[8:11], a[116:119], a[220:223], v[8:11]\n"
    "v_exp_f32 v59, v59\n"
    "v_mfma_f32_16x16x32_bf16 v[8:11], a[112:115], a[216:219], v[8:11]\n"
    "v_cvt_pk_bf16_f32 v83, v58, v59\n"
    "v_mfma_f32_16x16x32_bf16 v[12:15], a[112:115], a[240:243], v[100:103]\n"
    "buffer_load_dwordx4 v1, s[16:19], s83 offen offset:2048 sc1 lds\n"
    "v_mfma_f32_16x16x32_bf16 v[12:15], a[116:119], a[244:247], v[12:15]\n"
    "v_exp_f32 v45, v45\n"
    "v_mfma_f32_16x16x32_bf16 v[12:15], a[120:123], a[248:251], v[12:15]\n"
    "v_exp_f32 v46, v46\n"
    "v_mfma_f32_16x16x32_bf16 v[12:15], a[124:127], a[252:255], v[12:15]\n"
    "v_exp_f32 v47, v47\n"
    "v_mfma_f32_16x16x32_bf16 v[12:15], a[128:131], a[160:163], v[12:15]\n"
    "buffer_load_dwordx4 v1, s[16:19], s83 offen offset:3072 sc1 lds\n"
    "v_mfma_f32_16x16x32_bf16 v[12:15], a[132:135], a[164:167], v[12:15]\n"
    "s_add_u32 m0, s64, 4096\n"
    "v_exp_f32 v60, v60\n"
    "v_mfma_f32_16x16x32_bf16 v[16:19], a[132:135], a[188:191], v[100:103]\n"
    "s_add_u32 s83, s67, 28672\n"
    "v_exp_f32 v61, v61\n"
    "v_mfma_f32_16x16x32_bf16 v[16:19], a[128:131], a[184:187], v[16:19]\n"
    "v_exp_f32 v62, v62\n"
    "v_mfma_f32_16x16x32_bf16 v[16:19], a[124:127], a[180:183], v[16:19]\n"
    "buffer_load_dwordx4 v1, s[16:19], s83 offen offset:0 sc1 lds\n"
    "v_mfma_f32_16x16x32_bf16 v[16:19], a[120:123], a[176:179], v[16:19]\n"
    "v_exp_f32 v63, v63\n"
    "v_mfma_f32_16x16x32_bf16 v[16:19], a[116:119], a[172:175], v[16:19]\n"
    "v_cvt_pk_bf16_f32 v88, v44, v45\n"
    "v_cvt_pk_bf16_f32 v89, v46, v47\n"
    "v_mfma_f32_16x16x32_bf16 v[16:19], a[112:115], a[168:171], v[16:19]\n"
    "s_waitcnt lgkmcnt(0)\n"
    "v_cvt_pk_bf16_f32 v90, v60, v61\n"
    "v_cvt_pk_bf16_f32 v91, v62, v63\n"
    "v_mfma_f32_16x16x32_bf16 v[20:23], a[136:139], a[192:195], v[104:107]\n"
    "buffer_load_dwordx4 v1, s[16:19], s83 offen offset:1024 sc1 lds\n"
    "v_mfma_f32_16x16x32_bf16 v[20:23], a[140:143], a[196:199], v[20:23]\n"
    "s_add_u32 s67, s67, 49152\n"
    "ds_read_b128 a[112:115], v248 offset:0\n"
    "v_mfma_f32_16x16x32_bf16 v[20:23], a[144:147], a[200:203], v[20:23]\n"
    "s_add_u32 s59, s59, 4\n"
    "ds_read_b128 a[116:119], v248 offset:12288\n"
    "v_mfma_f32_16x16x32_bf16 v[20:23], a[148:151], a[204:207], v[20:23]\n"
    "s_waitcnt vmcnt(12)\n"
    "ds_read_b128 a[120:123], v248 offset:1024\n"
    "v_mfma_f32_16x16x32_bf16 v[20:23], a[152:155], a[208:211], v[20:23]\n"
    "buffer_store_dwordx2 v[238:239], v240, s[36:39], s80 offen offset:0 sc1\n"
    "v_mfma_f32_16x16x32_bf16 v[20:23], a[156:159], a[212:215], v[20:23]\n"
    "buffer_load_dwordx4 v[204:207], v242, s[40:43], s75 offen offset:0 nt\n"
    "v_mfma_f32_16x16x32_bf16 v[24:27], a[156:159], a[236:239], v[104:107]\n"
    "s_add_u32 s80, s80, 256\n"
    "ds_read_b128 a[124:127], v248 offset:13312\n"
    "v_mfma_f32_16x16x32_bf16 v[24:27], a[152:155], a[232:235], v[24:27]\n"
    "buffer_load_dwordx4 v[208:211], v242, s[40:43], s75 offen offset:768 nt\n"
    "v_mfma_f32_16x16x32_bf16 v[24:27], a[148:151], a[228:231], v[24:27]\n"
    "ds_read_b128 a[128:131], v248 offset:2048\n"
    "v_mfma_f32_16x16x32_bf16 v[24:27], a[144:147], a[224:227], v[24:27]\n"
    "buffer_load_dwordx4 v[212:215], v242, s[40:43], s75 offen offset:256 nt\n"
    "v_mfma_f32_16x16x32_bf16 v[24:27], a[140:143], a[220:223], v[24:27]\n"
    "ds_read_b128 a[132:135], v248 offset:14336\n"
    "v_mfma_f32_16x16x32_bf16 v[24:27], a[136:139], a[216:219], v[24:27]\n"
    "buffer_load_dwordx4 v[216:219], v242, s[40:43], s75 offen offset:1024 nt\n"
    "v_mfma_f32_16x16x32_bf16 v[28:31], a[136:139], a[240:243], v[104:107]\n"
    "v_exp_f32 v48, v48\n"
    "v_mfma_f32_16x16x32_bf16 v[28:31], a[140:143], a[244:247], v[28:31]\n"
    "buffer_load_dwordx4 v[220:223], v242, s[40:43], s75 offen offset:512 nt\n"
    "v_mfma_f32_16x16x32_bf16 v[28:31], a[144:147], a[248:251], v[28:31]\n"
    "v_exp_f32 v49, v49\n"
    "v_mfma_f32_16x16x32_bf16 v[28:31], a[148:151], a[252:255], v[28:31]\n"
    "buffer_load_dwordx4 v[224:227], v242, s[40:43], s75 offen offset:1280 nt\n"
    "v_mfma_f32_16x16x32_bf16 v[28:31], a[152:155], a[160:163], v[28:31]\n"
    "v_exp_f32 v50, v50\n"
    "v_mfma_f32_16x16x32_bf16 v[28:31], a[156:159], a[164:167], v[28:31]\n"
    "v_exp_f32 v51, v51\n"
    "v_mfma_f32_16x16x32_bf16 v[32:35], a[156:159], a[188:191], v[104:107]\n"
    "v_exp_f32 v64, v64\n"
    "v_mfma_f32_16x16x32_bf16 v[32:35], a[152:155], a[184:187], v[32:35]\n"
    "v_exp_f32 v65, v65\n"
    "v_mfma_f32_16x16x32_bf16 v[32:35], a[148:151], a[180:183], v[32:35]\n"
    "v_exp_f32 v66, v66\n"
    "v_mfma_f32_16x16x32_bf16 v[32:35], a[144:147], a[176:179], v[32:35]\n"
    "v_exp_f32 v67, v67\n"
    "v_mfma_f32_16x16x32_bf16 v[32:35], a[140:143], a[172:175], v[32:35]\n"
    "v_cvt_pk_bf16_f32 v96, v48, v49\n"
    "v_cvt_pk_bf16_f32 v97, v50, v51\n"
    "v_mfma_f32_16x16x32_bf16 v[32:35], a[136:139], a[168:171], v[32:35]\n"
    "s_waitcnt lgkmcnt(0)\n"
    "v_cvt_pk_bf16_f32 v98, v64, v65\n"
    "v_cvt_pk_bf16_f32 v99, v66, v67\n"
    "v_mfma_f32_16x16x32_bf16 v[124:127], a[112:115], v[68:71], v[124:127]\n"
    "ds_read_b128 a[136:139], v248 offset:3072\n"
    "v_mfma_f32_16x16x32_bf16 v[124:127], a[116:119], v[72:75], v[124:127]\n"
    "ds_read_b128 a[140:143], v248 offset:15360\n"
    "v_mfma_f32_16x16x32_bf16 v[128:131], a[124:127], v[72:75], v[128:131]\n"
    "ds_read_b128 a[144:147], v248 offset:4096\n"
    "v_mfma_f32_16x16x32_bf16 v[128:131], a[120:123], v[68:71], v[128:131]\n"
    "ds_read_b128 a[148:151], v248 offset:16384\n"
    "v_mfma_f32_16x16x32_bf16 v[132:135], a[128:131], v[68:71], v[132:135]\n"
    "ds_read_b128 a[152:155], v248 offset:5120\n"
    "v_mfma_f32_16x16x32_bf16 v[132:135], a[132:135], v[72:75], v[132:135]\n"
    "ds_read_b128 a[156:159], v248 offset:17408\n"
    "v_mfma_f32_16x16x32_bf16 v[148:151], a[132:135], v[80:83], v[148:151]\n"
    "ds_read_b128 v[104:107], v251 offset:0\n"
    "v_mfma_f32_16x16x32_bf16 v[148:151], a[128:131], v[76:79], v[148:151]\n"
    "v_exp_f32 v4, v4\n"
    "v_mfma_f32_16x16x32_bf16 v[144:147], a[120:123], v[76:79], v[144:147]\n"
    "v_exp_f32 v5, v5\n"
    "v_mfma_f32_16x16x32_bf16 v[144:147], a[124:127], v[80:83], v[144:147]\n"
    "v_exp_f32 v6, v6\n"
    "v_mfma_f32_16x16x32_bf16 v[140:143], a[116:119], v[80:83], v[140:143]\n"
    "v_mfma_f32_16x16x32_bf16 v[140:143], a[112:115], v[76:79], v[140:143]\n"
    "v_mfma_f32_16x16x32_bf16 v[156:159], a[112:115], v[84:87], v[156:159]\n"
    "v_exp_f32 v7, v7\n"
    "v_mfma_f32_16x16x32_bf16 v[156:159], a[116:119], v[88:91], v[156:159]\n"
    "v_mfma_f32_16x16x32_bf16 v[160:163], a[124:127], v[88:91], v[160:163]\n"
    "v_exp_f32 v8, v8\n"
    "v_mfma_f32_16x16x32_bf16 v[160:163], a[120:123], v[84:87], v[160:163]\n"
    "v_mfma_f32_16x16x32_bf16 v[164:167], a[128:131], v[84:87], v[164:167]\n"
    "v_mfma_f32_16x16x32_bf16 v[164:167], a[132:135], v[88:91], v[164:167]\n"
    "v_exp_f32 v9, v9\n"
    "v_mfma_f32_16x16x32_bf16 v[180:183], a[132:135], v[96:99], v[180:183]\n"
    "v_mfma_f32_16x16x32_bf16 v[180:183], a[128:131], v[92:95], v[180:183]\n"
    "v_mfma_f32_16x16x32_bf16 v[176:179], a[120:123], v[92:95], v[176:179]\n"
    "v_exp_f32 v10, v10\n"
    "v_mfma_f32_16x16x32_bf16 v[176:179], a[124:127], v[96:99], v[176:179]\n"
    "v_mfma_f32_16x16x32_bf16 v[172:175], a[116:119], v[96:99], v[172:175]\n"
    "v_exp_f32 v11, v11\n"
    "v_mfma_f32_16x16x32_bf16 v[172:175], a[112:115], v[92:95], v[172:175]\n"
    "s_waitcnt lgkmcnt(0)\n"
    "v_mfma_f32_16x16x32_bf16 v[136:139], a[136:139], v[68:71], v[136:139]\n"
    "ds_read_b128 a[112:115], v248 offset:6144\n"
    "v_mfma_f32_16x16x32_bf16 v[136:139], a[140:143], v[72:75], v[136:139]\n"
    "ds_read_b128 a[116:119], v248 offset:18432\n"
    "v_mfma_f32_16x16x32_bf16 v[188:191], a[148:151], v[72:75], v[188:191]\n"
    "ds_read_b128 a[120:123], v248 offset:7168\n"
    "v_mfma_f32_16x16x32_bf16 v[188:191], a[144:147], v[68:71], v[188:191]\n"
    "ds_read_b128 a[124:127], v248 offset:19456\n"
    "v_mfma_f32_16x16x32_bf16 v[192:195], a[152:155], v[68:71], v[192:195]\n"
    "ds_read_b128 a[128:131], v248 offset:8192\n"
    "v_mfma_f32_16x16x32_bf16 v[192:195], a[156:159], v[72:75], v[192:195]\n"
    "ds_read_b128 a[132:135], v248 offset:20480\n"
    "v_smfmac_f32_16x16x64_bf16 v[108:111], v[104:107], v[68:75], v253\n"
    "s_add_u32 s81, s81, 128\n"
    "s_cmp_gt_u32 s81, 4096\n"
    "s_cselect_b32 s81, 0, s81\n"
    "v_smfmac_f32_16x16x64_bf16 v[112:115], v[104:107], v[76:83], v253\n"
    "s_cmp_ge_u32 s75, 1474560\n"
    "s_cmov_b64 s[40:41], s[8:9]\n"
    "v_mfma_f32_16x16x32_bf16 a[4:7], a[156:159], v[80:83], a[4:7]\n"
    "v_exp_f32 v12, v12\n"
    "v_mfma_f32_16x16x32_bf16 a[4:7], a[152:155], v[76:79], a[4:7]\n"
    "v_exp_f32 v13, v13\n"
    "v_mfma_f32_16x16x32_bf16 a[0:3], a[144:147], v[76:79], a[0:3]\n"
    "v_exp_f32 v14, v14\n"
    "v_mfma_f32_16x16x32_bf16 a[0:3], a[148:151], v[80:83], a[0:3]\n"
    "v_exp_f32 v15, v15\n"
    "v_mfma_f32_16x16x32_bf16 v[152:155], a[140:143], v[80:83], v[152:155]\n"
    "v_exp_f32 v16, v16\n"
    "v_mfma_f32_16x16x32_bf16 v[152:155], a[136:139], v[76:79], v[152:155]\n"
    "v_mfma_f32_16x16x32_bf16 v[168:171], a[136:139], v[84:87], v[168:171]\n"
    "v_exp_f32 v17, v17\n"
    "v_mfma_f32_16x16x32_bf16 v[168:171], a[140:143], v[88:91], v[168:171]\n"
    "v_mfma_f32_16x16x32_bf16 a[16:19], a[148:151], v[88:91], a[16:19]\n"
    "v_mfma_f32_16x16x32_bf16 a[16:19], a[144:147], v[84:87], a[16:19]\n"
    "v_exp_f32 v18, v18\n"
    "v_mfma_f32_16x16x32_bf16 a[20:23], a[152:155], v[84:87], a[20:23]\n"
    "v_mfma_f32_16x16x32_bf16 a[20:23], a[156:159], v[88:91], a[20:23]\n"
    "v_smfmac_f32_16x16x64_bf16 v[116:119], v[104:107], v[84:91], v253\n"
    "s_add_u32 s52, s52, s45\n"
    "v_smfmac_f32_16x16x64_bf16 v[120:123], v[104:107], v[92:99], v253\n"
    "v_mfma_f32_16x16x32_bf16 a[36:39], a[156:159], v[96:99], a[36:39]\n"
    "v_exp_f32 v19, v19\n"
    "v_mfma_f32_16x16x32_bf16 a[36:39], a[152:155], v[92:95], a[36:39]\n"
    "v_exp_f32 v20, v20\n"
    "v_mfma_f32_16x16x32_bf16 a[32:35], a[144:147], v[92:95], a[32:35]\n"
    "v_mfma_f32_16x16x32_bf16 a[32:35], a[148:151], v[96:99], a[32:35]\n"
    "v_exp_f32 v21, v21\n"
    "v_mfma_f32_16x16x32_bf16 v[184:187], a[140:143], v[96:99], v[184:187]\n"
    "v_mfma_f32_16x16x32_bf16 v[184:187], a[136:139], v[92:95], v[184:187]\n"
    "s_waitcnt lgkmcnt(0)\n"
    "v_mfma_f32_16x16x32_bf16 v[196:199], a[112:115], v[68:71], v[196:199]\n"
    "ds_read_b128 a[136:139], v248 offset:9216\n"
    "v_mfma_f32_16x16x32_bf16 v[196:199], a[116:119], v[72:75], v[196:199]\n"
    "ds_read_b128 a[140:143], v248 offset:21504\n"
    "v_mfma_f32_16x16x32_bf16 v[200:203], a[124:127], v[72:75], v[200:203]\n"
    "ds_read_b128 a[144:147], v248 offset:10240\n"
    "v_mfma_f32_16x16x32_bf16 v[200:203], a[120:123], v[68:71], v[200:203]\n"
    "ds_read_b128 a[148:151], v248 offset:22528\n"
    "v_mfma_f32_16x16x32_bf16 a[48:51], a[128:131], v[68:71], a[48:51]\n"
    "buffer_load_dwordx2 v[236:237], v241, s[36:39], s81 offen offset:0 sc1\n"
    "v_mfma_f32_16x16x32_bf16 a[48:51], a[132:135], v[72:75], a[48:51]\n"
    "ds_read_b128 a[152:155], v248 offset:11264\n"
    "v_mfma_f32_16x16x32_bf16 a[64:67], a[132:135], v[80:83], a[64:67]\n"
    "ds_read_b128 a[156:159], v248 offset:23552\n"
    "v_mfma_f32_16x16x32_bf16 a[64:67], a[128:131], v[76:79], a[64:67]\n"
    "v_exp_f32 v22, v22\n"
    "v_mfma_f32_16x16x32_bf16 a[12:15], a[120:123], v[76:79], a[12:15]\n"
    "v_exp_f32 v23, v23\n"
    "v_mfma_f32_16x16x32_bf16 a[12:15], a[124:127], v[80:83], a[12:15]\n"
    "v_exp_f32 v24, v24\n"
    "v_mfma_f32_16x16x32_bf16 a[8:11], a[116:119], v[80:83], a[8:11]\n"
    "v_exp_f32 v25, v25\n"
    "v_mfma_f32_16x16x32_bf16 a[8:11], a[112:115], v[76:79], a[8:11]\n"
    "v_exp_f32 v26, v26\n"
    "v_mfma_f32_16x16x32_bf16 a[24:27], a[112:115], v[84:87], a[24:27]\n"
    "v_mfma_f32_16x16x32_bf16 a[24:27], a[116:119], v[88:91], a[24:27]\n"
    "v_exp_f32 v27, v27\n"
    "v_mfma_f32_16x16x32_bf16 a[28:31], a[124:127], v[88:91], a[28:31]\n"
    "v_mfma_f32_16x16x32_bf16 a[28:31], a[120:123], v[84:87], a[28:31]\n"
    "v_mfma_f32_16x16x32_bf16 a[80:83], a[128:131], v[84:87], a[80:83]\n"
    "v_exp_f32 v28, v28\n"
    "v_mfma_f32_16x16x32_bf16 a[80:83], a[132:135], v[88:91], a[80:83]\n"
    "v_mfma_f32_16x16x32_bf16 a[96:99], a[132:135], v[96:99], a[96:99]\n"
    "v_mfma_f32_16x16x32_bf16 a[96:99], a[128:131], v[92:95], a[96:99]\n"
    "v_exp_f32 v29, v29\n"
    "v_mfma_f32_16x16x32_bf16 a[44:47], a[120:123], v[92:95], a[44:47]\n"
    "v_mfma_f32_16x16x32_bf16 a[44:47], a[124:127], v[96:99], a[44:47]\n"
    "v_exp_f32 v30, v30\n"
    "v_mfma_f32_16x16x32_bf16 a[40:43], a[116:119], v[96:99], a[40:43]\n"
    "v_mfma_f32_16x16x32_bf16 a[40:43], a[112:115], v[92:95], a[40:43]\n"
    "s_waitcnt lgkmcnt(0)\n"
    "v_mfma_f32_16x16x32_bf16 a[52:55], a[136:139], v[68:71], a[52:55]\n"
    "ds_read_b128 a[112:115], v3 offset:12288\n"
    "v_mfma_f32_16x16x32_bf16 a[52:55], a[140:143], v[72:75], a[52:55]\n"
    "ds_read_b128 a[116:119], v3 offset:14336\n"
    "v_mfma_f32_16x16x32_bf16 a[56:59], a[148:151], v[72:75], a[56:59]\n"
    "ds_read_b128 a[120:123], v3 offset:16384\n"
    "v_mfma_f32_16x16x32_bf16 a[56:59], a[144:147], v[68:71], a[56:59]\n"
    "ds_read_b128 a[124:127], v3 offset:18432\n"
    "v_mfma_f32_16x16x32_bf16 a[60:63], a[152:155], v[68:71], a[60:63]\n"
    "ds_read_b128 a[128:131], v3 offset:20480\n"
    "v_mfma_f32_16x16x32_bf16 a[60:63], a[156:159], v[72:75], a[60:63]\n"
    "ds_read_b128 a[132:135], v3 offset:22528\n"
    "v_mfma_f32_16x16x32_bf16 a[76:79], a[156:159], v[80:83], a[76:79]\n"
    "ds_read_b128 v[100:103], v250 offset:896\n"
    "v_mfma_f32_16x16x32_bf16 a[76:79], a[152:155], v[76:79], a[76:79]\n"
    "v_exp_f32 v31, v31\n"
    "v_mfma_f32_16x16x32_bf16 a[72:75], a[144:147], v[76:79], a[72:75]\n"
    "v_exp_f32 v32, v32\n"
    "v_mfma_f32_16x16x32_bf16 a[72:75], a[148:151], v[80:83], a[72:75]\n"
    "v_exp_f32 v33, v33\n"
    "v_mfma_f32_16x16x32_bf16 a[68:71], a[140:143], v[80:83], a[68:71]\n"
    "v_exp_f32 v34, v34\n"
    "v_mfma_f32_16x16x32_bf16 a[68:71], a[136:139], v[76:79], a[68:71]\n"
    "v_exp_f32 v35, v35\n"
    "v_mfma_f32_16x16x32_bf16 a[84:87], a[136:139], v[84:87], a[84:87]\n"
    "v_mfma_f32_16x16x32_bf16 a[84:87], a[140:143], v[88:91], a[84:87]\n"
    "v_mfma_f32_16x16x32_bf16 a[88:91], a[148:151], v[88:91], a[88:91]\n"
    "s_waitcnt vmcnt(8)\n"
    "ds_add_u32 v247, v246\n"
    "v_mfma_f32_16x16x32_bf16 a[88:91], a[144:147], v[84:87], a[88:91]\n"
    "v_mfma_f32_16x16x32_bf16 a[92:95], a[152:155], v[84:87], a[92:95]\n"
    "v_mfma_f32_16x16x32_bf16 a[92:95], a[156:159], v[88:91], a[92:95]\n"
    "v_mfma_f32_16x16x32_bf16 a[108:111], a[156:159], v[96:99], a[108:111]\n"
    "v_mfma_f32_16x16x32_bf16 a[108:111], a[152:155], v[92:95], a[108:111]\n"
    "v_mfma_f32_16x16x32_bf16 a[104:107], a[144:147], v[92:95], a[104:107]\n"
    "s_sub_u32 s74, s68, s65\n"
    "v_mfma_f32_16x16x32_bf16 a[104:107], a[148:151], v[96:99], a[104:107]\n"
    "s_add_u32 s66, s66, 1\n"
    "v_mov_b32 v2, v3\n"
    "v_add_u32_e64 v3, s74, v252\n"
    "v_mfma_f32_16x16x32_bf16 a[100:103], a[140:143], v[96:99], a[100:103]\n"
    "s_waitcnt lgkmcnt(1)\n"
    "s_cmp_eq_u32 s66, .Lend-.Lend2+(28)\n"
    "v_mfma_f32_16x16x32_bf16 a[100:103], a[136:139], v[92:95], a[100:103]\n"
    ".LwVe:\n"
    "s_cbranch_scc1 .LtailP\n"
    ".LNV:\n"
    "v_mfma_f32_16x16x32_bf16 v[36:39], a[112:115], a[192:195], v[100:103]\n"
    "ds_read_b128 a[136:139], v2 offset:13312\n"
    "v_cvt_pk_bf16_f32 v68, v4, v5\n"
    "v_mfma_f32_16x16x32_bf16 v[36:39], a[116:119], a[196:199], v[36:39]\n"
    "ds_read_b128 a[140:143], v2 offset:15360\n"
    "v_mfma_f32_16x16x32_bf16 v[36:39], a[120:123], a[200:203], v[36:39]\n"
    "ds_read_b128 a[144:147], v2 offset:17408\n"
    "v_mfma_f32_16x16x32_bf16 v[36:39], a[124:127], a[204:207], v[36:39]\n"
    "ds_read_b128 a[148:151], v2 offset:19456\n"
    "v_cvt_pk_bf16_f32 v69, v6, v7\n"
    "v_mfma_f32_16x16x32_bf16 v[36:39], a[128:131], a[208:211], v[36:39]\n"
    "ds_read_b128 a[152:155], v2 offset:21504\n"
    "v_mfma_f32_16x16x32_bf16 v[36:39], a[132:135], a[212:215], v[36:39]\n"
    "ds_read_b128 a[156:159], v2 offset:23552\n"
    "v_mfma_f32_16x16x32_bf16 v[40:43], a[132:135], a[236:239], v[100:103]\n"
    "ds_read_b128 v[104:107], v250 offset:912\n"
    "v_mfma_f32_16x16x32_bf16 v[40:43], a[128:131], a[232:235], v[40:43]\n"
    "s_add_u32 s68, s68, 24576\n"
    "v_cvt_pk_bf16_f32 v76, v8, v9\n"
    "v_mfma_f32_16x16x32_bf16 v[40:43], a[124:127], a[228:231], v[40:43]\n"
    "s_cmp_ge_u32 s68, s69\n"
    "s_cselect_b32 s68, s65, s68\n"
    "v_mfma_f32_16x16x32_bf16 v[40:43], a[120:123], a[224:227], v[40:43]\n"
    "v_exp_f32 v36, v36\n"
    "v_mfma_f32_16x16x32_bf16 v[40:43], a[116:119], a[220:223], v[40:43]\n"
    "v_exp_f32 v37, v37\n"
    "v_mfma_f32_16x16x32_bf16 v[40:43], a[112:115], a[216:219], v[40:43]\n"
    "v_cvt_pk_bf16_f32 v77, v10, v11\n"
    "v_cvt_pk_bf16_f32 v72, v36, v37\n"
    "v_mfma_f32_16x16x32_bf16 v[44:47], a[112:115], a[240:243], v[100:103]\n"
    "v_mfma_f32_16x16x32_bf16 v[44:47], a[116:119], a[244:247], v[44:47]\n"
    "v_exp_f32 v38, v38\n"
    "v_mfma_f32_16x16x32_bf16 v[44:47], a[120:123], a[248:251], v[44:47]\n"
    "v_mfma_f32_16x16x32_bf16 v[44:47], a[124:127], a[252:255], v[44:47]\n"
    "v_cvt_pk_bf16_f32 v84, v12, v13\n"
    "v_mfma_f32_16x16x32_bf16 v[44:47], a[128:131], a[160:163], v[44:47]\n"
    "v_exp_f32 v39, v39\n"
    "v_mfma_f32_16x16x32_bf16 v[44:47], a[132:135], a[164:167], v[44:47]\n"
    "v_cvt_pk_bf16_f32 v73, v38, v39\n"
    "v_mfma_f32_16x16x32_bf16 v[48:51], a[132:135], a[188:191], v[100:103]\n"
    "ds_read_b32 v245, v247\n"
    "v_cvt_pk_bf16_f32 v85, v14, v15\n"
    "v_mfma_f32_16x16x32_bf16 v[48:51], a[128:131], a[184:187], v[48:51]\n"
    "s_add_u32 m0, s68, 0\n"
    "v_exp_f32 v40, v40\n"
    "v_mfma_f32_16x16x32_bf16 v[48:51], a[124:127], a[180:183], v[48:51]\n"
    "v_mfma_f32_16x16x32_bf16 v[48:51], a[120:123], a[176:179], v[48:51]\n"
    "v_mfma_f32_16x16x32_bf16 v[48:51], a[116:119], a[172:175], v[48:51]\n"
    "s_waitcnt vmcnt(0)\n"
    "v_cmp_eq_u64 s[54:55], v[236:237], v[238:239]\n"
    "v_cvt_pk_bf16_f32 v92, v16, v17\n"
    "v_mfma_f32_16x16x32_bf16 v[48:51], a[112:115], a[168:171], v[48:51]\n"
    "s_waitcnt lgkmcnt(1)\n"
    "v_exp_f32 v41, v41\n"
    "v_mfma_f32_16x16x32_bf16 v[52:55], a[136:139], a[192:195], v[104:107]\n"
    "s_waitcnt lgkmcnt(0)\n"
    "v_cmp_le_u32 s[56:57], s59, v245\n"
    "v_cvt_pk_bf16_f32 v80, v40, v41\n"
    "v_mfma_f32_16x16x32_bf16 v[52:55], a[140:143], a[196:199], v[52:55]\n"
    "s_waitcnt vmcnt(1)\n"
    "v_exp_f32 v42, v42\n"
    "v_mfma_f32_16x16x32_bf16 v[52:55], a[144:147], a[200:203], v[52:55]\n"
    "s_and_b64 s[54:55], s[54:55], s[56:57]\n"
    "s_cmp_eq_u64 s[54:55], -1\n"
    "v_mfma_f32_16x16x32_bf16 v[52:55], a[148:151], a[204:207], v[52:55]\n"
    "s_cbranch_scc0 .Lpoll4\n"
    ".Lpoll4r:\n"
    "v_mfma_f32_16x16x32_bf16 v[52:55], a[152:155], a[208:211], v[52:55]\n"
    "buffer_load_dwordx4 v1, s[16:19], s67 offen offset:0 sc1 lds\n"
    "v_mfma_f32_16x16x32_bf16 v[52:55], a[156:159], a[212:215], v[52:55]\n"
    "s_add_u32 s76, s52, s60\n"
    "ds_read_b128 a[112:115], v3 offset:0\n"
    "v_cvt_pk_bf16_f32 v93, v18, v19\n"
    "v_mfma_f32_16x16x32_bf16 v[56:59], a[156:159], a[236:239], v[104:107]\n"
    "s_add_u32 s77, s52, s61\n"
    "ds_read_b128 a[116:119], v3 offset:2048\n"
    "v_cvt_pk_bf16_f32 v70, v20, v21\n"
    "v_mfma_f32_16x16x32_bf16 v[56:59], a[152:155], a[232:235], v[56:59]\n"
    "ds_read_b128 a[120:123], v3 offset:4096\n"
    "v_mfma_f32_16x16x32_bf16 v[56:59], a[148:151], a[228:231], v[56:59]\n"
    "buffer_load_dwordx4 v1, s[16:19], s67 offen offset:1024 sc1 lds\n"
    "v_mfma_f32_16x16x32_bf16 v[56:59], a[144:147], a[224:227], v[56:59]\n"
    "ds_read_b128 a[124:127], v3 offset:6144\n"
    "v_cvt_pk_bf16_f32 v71, v22, v23\n"
    "v_mfma_f32_16x16x32_bf16 v[56:59], a[140:143], a[220:223], v[56:59]\n"
    "s_add_u32 s78, s52, s62\n"
    "ds_read_b128 a[128:131], v3 offset:8192\n"
    "v_mfma_f32_16x16x32_bf16 v[56:59], a[136:139], a[216:219], v[56:59]\n"
    "ds_read_b128 a[132:135], v3 offset:10240\n"
    "v_mfma_f32_16x16x32_bf16 v[60:63], a[136:139], a[240:243], v[104:107]\n"
    "buffer_load_dwordx4 v1, s[16:19], s67 offen offset:2048 sc1 lds\n"
    "v_mfma_f32_16x16x32_bf16 v[60:63], a[140:143], a[244:247], v[60:63]\n"
    "ds_read_b128 v[100:103], v250 offset:1536\n"
    "v_cvt_pk_bf16_f32 v78, v24, v25\n"
    "v_mfma_f32_16x16x32_bf16 v[60:63], a[144:147], a[248:251], v[60:63]\n"
    "v_exp_f32 v43, v43\n"
    "v_mfma_f32_16x16x32_bf16 v[60:63], a[148:151], a[252:255], v[60:63]\n"
    "v_cvt_pk_bf16_f32 v81, v42, v43\n"
    "v_cvt_pk_u16_u32 v204, v204, v208\n"
    "v_mfma_f32_16x16x32_bf16 v[60:63], a[152:155], a[160:163], v[60:63]\n"
    "buffer_load_dwordx4 v1, s[16:19], s67 offen offset:3072 sc1 lds\n"
    "v_mfma_f32_16x16x32_bf16 v[60:63], a[156:159], a[164:167], v[60:63]\n"
    "s_add_u32 m0, s68, 4096\n"
    "v_cvt_pk_bf16_f32 v79, v26, v27\n"
    "v_mfma_f32_16x16x32_bf16 v[64:67], a[156:159], a[188:191], v[104:107]\n"
    "s_add_u32 s83, s67, 4096\n"
    "v_exp_f32 v44, v44\n"
    "v_mfma_f32_16x16x32_bf16 v[64:67], a[152:155], a[184:187], v[64:67]\n"
    "v_exp_f32 v45, v45\n"
    "v_mfma_f32_16x16x32_bf16 v[64:67], a[148:151], a[180:183], v[64:67]\n"
    "buffer_load_dwordx4 v1, s[16:19], s83 offen offset:0 sc1 lds\n"
    "v_mfma_f32_16x16x32_bf16 v[64:67], a[144:147], a[176:179], v[64:67]\n"
    "v_cvt_pk_bf16_f32 v86, v28, v29\n"
    "v_cvt_pk_bf16_f32 v88, v44, v45\n"
    "v_mfma_f32_16x16x32_bf16 v[64:67], a[140:143], a[172:175], v[64:67]\n"
    "v_exp_f32 v46, v46\n"
    "v_mfma_f32_16x16x32_bf16 v[64:67], a[136:139], a[168:171], v[64:67]\n"
    "s_waitcnt lgkmcnt(0)\n"
    "v_exp_f32 v47, v47\n"
    "v_mfma_f32_16x16x32_bf16 v[4:7], a[112:115], a[192:195], v[100:103]\n"
    "buffer_load_dwordx4 v1, s[16:19], s83 offen offset:1024 sc1 lds\n"
    "v_mfma_f32_16x16x32_bf16 v[4:7], a[116:119], a[196:199], v[4:7]\n"
    "s_add_u32 m0, s70, 0\n"
    "ds_read_b128 a[136:139], v3 offset:1024\n"
    "v_cvt_pk_bf16_f32 v87, v30, v31\n"
    "v_mfma_f32_16x16x32_bf16 v[4:7], a[120:123], a[200:203], v[4:7]\n"
    "s_add_u32 s83, s67, 24576\n"
    "ds_read_b128 a[140:143], v3 offset:3072\n"
    "v_cvt_pk_bf16_f32 v89, v46, v47\n"
    "v_mfma_f32_16x16x32_bf16 v[4:7], a[124:127], a[204:207], v[4:7]\n"
    "ds_read_b128 a[144:147], v3 offset:5120\n"
    "v_cvt_pk_u16_u32 v205, v205, v209\n"
    "v_mfma_f32_16x16x32_bf16 v[4:7], a[128:131], a[208:211], v[4:7]\n"
    "buffer_load_dwordx4 v1, s[16:19], s83 offen offset:0 sc1 lds\n"
    "v_mfma_f32_16x16x32_bf16 v[4:7], a[132:135], a[212:215], v[4:7]\n"
    "ds_read_b128 a[148:151], v3 offset:7168\n"
    "v_cvt_pk_bf16_f32 v94, v32, v33\n"
    "v_mfma_f32_16x16x32_bf16 v[8:11], a[132:135], a[236:239], v[100:103]\n"
    "ds_read_b128 a[152:155], v3 offset:9216\n"
    "v_cvt_pk_u16_u32 v206, v206, v210\n"
    "v_mfma_f32_16x16x32_bf16 v[8:11], a[128:131], a[232:235], v[8:11]\n"
    "ds_read_b128 a[156:159], v3 offset:11264\n"
    "v_cvt_pk_u16_u32 v207, v207, v211\n"
    "v_mfma_f32_16x16x32_bf16 v[8:11], a[124:127], a[228:231], v[8:11]\n"
    "buffer_load_dwordx4 v1, s[16:19], s83 offen offset:1024 sc1 lds\n"
    "v_mfma_f32_16x16x32_bf16 v[8:11], a[120:123], a[224:227], v[8:11]\n"
    "ds_read_b128 v[104:107], v250 offset:1552\n"
    "v_cvt_pk_bf16_f32 v95, v34, v35\n"
    "v_mfma_f32_16x16x32_bf16 v[8:11], a[116:119], a[220:223], v[8:11]\n"
    "v_exp_f32 v52, v52\n"
    "v_mfma_f32_16x16x32_bf16 v[8:11], a[112:115], a[216:219], v[8:11]\n"
    "v_exp_f32 v53, v53\n"
    "v_mfma_f32_16x16x32_bf16 v[12:15], a[112:115], a[240:243], v[100:103]\n"
    "buffer_load_dwordx4 v1, s[16:19], s83 offen offset:2048 sc1 lds\n"
    "v_mfma_f32_16x16x32_bf16 v[12:15], a[116:119], a[244:247], v[12:15]\n"
    "v_exp_f32 v54, v54\n"
    "v_mfma_f32_16x16x32_bf16 v[12:15], a[120:123], a[248:251], v[12:15]\n"
    "v_exp_f32 v55, v55\n"
    "v_mfma_f32_16x16x32_bf16 v[12:15], a[124:127], a[252:255], v[12:15]\n"
    "v_cvt_pk_bf16_f32 v74, v52, v53\n"
    "v_cvt_pk_bf16_f32 v75, v54, v55\n"
    "v_mfma_f32_16x16x32_bf16 v[12:15], a[128:131], a[160:163], v[12:15]\n"
    "buffer_load_dwordx4 v1, s[16:19], s83 offen offset:3072 sc1 lds\n"
    "v_mfma_f32_16x16x32_bf16 v[12:15], a[132:135], a[164:167], v[12:15]\n"
    "s_add_u32 m0, s70, 4096\n"
    "v_exp_f32 v56, v56\n"
    "v_mfma_f32_16x16x32_bf16 v[16:19], a[132:135], a[188:191], v[100:103]\n"
    "s_add_u32 s83, s67, 28672\n"
    "v_exp_f32 v48, v48\n"
    "v_mfma_f32_16x16x32_bf16 v[16:19], a[128:131], a[184:187], v[16:19]\n"
    "v_exp_f32 v49, v49\n"
    "v_mfma_f32_16x16x32_bf16 v[16:19], a[124:127], a[180:183], v[16:19]\n"
    "buffer_load_dwordx4 v1, s[16:19], s83 offen offset:0 sc1 lds\n"
    "v_mfma_f32_16x16x32_bf16 v[16:19], a[120:123], a[176:179], v[16:19]\n"
    "v_exp_f32 v57, v57\n"
    "v_mfma_f32_16x16x32_bf16 v[16:19], a[116:119], a[172:175], v[16:19]\n"
    "v_cvt_pk_bf16_f32 v82, v56, v57\n"
    "v_cvt_pk_bf16_f32 v96, v48, v49\n"
    "v_mfma_f32_16x16x32_bf16 v[16:19], a[112:115], a[168:171], v[16:19]\n"
    "s_waitcnt lgkmcnt(0)\n"
    "v_exp_f32 v58, v58\n"
    "v_mfma_f32_16x16x32_bf16 v[20:23], a[136:139], a[192:195], v[104:107]\n"
    "buffer_load_dwordx4 v1, s[16:19], s83 offen offset:1024 sc1 lds\n"
    "v_mfma_f32_16x16x32_bf16 v[20:23], a[140:143], a[196:199], v[20:23]\n"
    "s_add_u32 s67, s67, 49152\n"
    "ds_read_b128 a[112:115], v249 offset:0\n"
    "v_or3_b32 v243, v204, v205, v243\n"
    "v_mfma_f32_16x16x32_bf16 v[20:23], a[144:147], a[200:203], v[20:23]\n"
    "s_add_u32 s59, s59, 4\n"
    "ds_read_b128 a[116:119], v249 offset:12288\n"
    "v_or3_b32 v243, v206, v207, v243\n"
    "v_mfma_f32_16x16x32_bf16 v[20:23], a[148:151], a[204:207], v[20:23]\n"
    "ds_read_b128 a[120:123], v249 offset:1024\n"
    "v_cvt_pk_u16_u32 v212, v212, v216\n"
    "v_mfma_f32_16x16x32_bf16 v[20:23], a[152:155], a[208:211], v[20:23]\n"
    "ds_read_b128 a[124:127], v249 offset:13312\n"
    "v_cvt_pk_u16_u32 v213, v213, v217\n"
    "v_mfma_f32_16x16x32_bf16 v[20:23], a[156:159], a[212:215], v[20:23]\n"
    "ds_read_b128 a[128:131], v249 offset:2048\n"
    "v_mfma_f32_16x16x32_bf16 v[24:27], a[156:159], a[236:239], v[104:107]\n"
    "ds_read_b128 a[132:135], v249 offset:14336\n"
    "v_mfma_f32_16x16x32_bf16 v[24:27], a[152:155], a[232:235], v[24:27]\n"
    "v_exp_f32 v59, v59\n"
    "v_mfma_f32_16x16x32_bf16 v[24:27], a[148:151], a[228:231], v[24:27]\n"
    "v_cvt_pk_bf16_f32 v83, v58, v59\n"
    "v_cvt_pk_u16_u32 v214, v214, v218\n"
    "v_mfma_f32_16x16x32_bf16 v[24:27], a[144:147], a[224:227], v[24:27]\n"
    "v_exp_f32 v60, v60\n"
    "v_mfma_f32_16x16x32_bf16 v[24:27], a[140:143], a[220:223], v[24:27]\n"
    "v_exp_f32 v61, v61\n"
    "v_mfma_f32_16x16x32_bf16 v[24:27], a[136:139], a[216:219], v[24:27]\n"
    "v_exp_f32 v62, v62\n"
    "v_mfma_f32_16x16x32_bf16 v[28:31], a[136:139], a[240:243], v[104:107]\n"
    "v_cvt_pk_bf16_f32 v90, v60, v61\n"
    "v_mfma_f32_16x16x32_bf16 v[28:31], a[140:143], a[244:247], v[28:31]\n"
    "v_exp_f32 v63, v63\n"
    "v_mfma_f32_16x16x32_bf16 v[28:31], a[144:147], a[248:251], v[28:31]\n"
    "v_cvt_pk_bf16_f32 v91, v62, v63\n"
    "v_cvt_pk_u16_u32 v215, v215, v219\n"
    "v_mfma_f32_16x16x32_bf16 v[28:31], a[148:151], a[252:255], v[28:31]\n"
    "v_exp_f32 v50, v50\n"
    "v_mfma_f32_16x16x32_bf16 v[28:31], a[152:155], a[160:163], v[28:31]\n"
    "v_exp_f32 v51, v51\n"
    "v_mfma_f32_16x16x32_bf16 v[28:31], a[156:159], a[164:167], v[28:31]\n"
    "v_exp_f32 v64, v64\n"
    "v_mfma_f32_16x16x32_bf16 v[32:35], a[156:159], a[188:191], v[104:107]\n"
    "v_cvt_pk_bf16_f32 v97, v50, v51\n"
    "v_mfma_f32_16x16x32_bf16 v[32:35], a[152:155], a[184:187], v[32:35]\n"
    "v_exp_f32 v65, v65\n"
    "v_mfma_f32_16x16x32_bf16 v[32:35], a[148:151], a[180:183], v[32:35]\n"
    "v_cvt_pk_bf16_f32 v98, v64, v65\n"
    "v_or3_b32 v243, v212, v213, v243\n"
    "v_mfma_f32_16x16x32_bf16 v[32:35], a[144:147], a[176:179], v[32:35]\n"
    "v_mfma_f32_16x16x32_bf16 v[32:35], a[140:143], a[172:175], v[32:35]\n"
    "v_exp_f32 v66, v66\n"
    "v_mfma_f32_16x16x32_bf16 v[32:35], a[136:139], a[168:171], v[32:35]\n"
    "s_waitcnt lgkmcnt(0)\n"
    "v_mfma_f32_16x16x32_bf16 v[124:127], a[112:115], v[68:71], v[124:127]\n"
    "ds_read_b128 a[136:139], v249 offset:3072\n"
    "v_or3_b32 v243, v214, v215, v243\n"
    "v_mfma_f32_16x16x32_bf16 v[124:127], a[116:119], v[72:75], v[124:127]\n"
    "ds_read_b128 a[140:143], v249 offset:15360\n"
    "v_mfma_f32_16x16x32_bf16 v[128:131], a[124:127], v[72:75], v[128:131]\n"
    "ds_read_b128 a[144:147], v249 offset:4096\n"
    "v_mfma_f32_16x16x32_bf16 v[128:131], a[120:123], v[68:71], v[128:131]\n"
    "ds_read_b128 a[148:151], v249 offset:16384\n"
    "v_mfma_f32_16x16x32_bf16 v[132:135], a[128:131], v[68:71], v[132:135]\n"
    "ds_read_b128 a[152:155], v249 offset:5120\n"
    "v_mfma_f32_16x16x32_bf16 v[132:135], a[132:135], v[72:75], v[132:135]\n"
    "ds_read_b128 a[156:159], v249 offset:17408\n"
    "v_cvt_pk_u16_u32 v220, v220, v224\n"
    "v_mfma_f32_16x16x32_bf16 v[148:151], a[132:135], v[80:83], v[148:151]\n"
    "ds_read_b128 v[104:107], v251 offset:768\n"
    "v_mfma_f32_16x16x32_bf16 v[148:151], a[128:131], v[76:79], v[148:151]\n"
    "v_exp_f32 v67, v67\n"
    "v_mfma_f32_16x16x32_bf16 v[144:147], a[120:123], v[76:79], v[144:147]\n"
    "v_cvt_pk_bf16_f32 v99, v66, v67\n"
    "v_mfma_f32_16x16x32_bf16 v[144:147], a[124:127], v[80:83], v[144:147]\n"
    "v_exp_f32 v4, v4\n"
    "v_mfma_f32_16x16x32_bf16 v[140:143], a[116:119], v[80:83], v[140:143]\n"
    "v_exp_f32 v5, v5\n"
    "v_mfma_f32_16x16x32_bf16 v[140:143], a[112:115], v[76:79], v[140:143]\n"
    "v_exp_f32 v6, v6\n"
    "v_mfma_f32_16x16x32_bf16 v[156:159], a[112:115], v[84:87], v[156:159]\n"
    "v_exp_f32 v7, v7\n"
    "v_mfma_f32_16x16x32_bf16 v[156:159], a[116:119], v[88:91], v[156:159]\n"
    "v_cvt_pk_u16_u32 v221, v221, v225\n"
    "v_mfma_f32_16x16x32_bf16 v[160:163], a[124:127], v[88:91], v[160:163]\n"
    "v_exp_f32 v8, v8\n"
    "v_mfma_f32_16x16x32_bf16 v[160:163], a[120:123], v[84:87], v[160:163]\n"
    "v_cvt_pk_u16_u32 v222, v222, v226\n"
    "v_mfma_f32_16x16x32_bf16 v[164:167], a[128:131], v[84:87], v[164:167]\n"
    "v_mfma_f32_16x16x32_bf16 v[164:167], a[132:135], v[88:91], v[164:167]\n"
    "v_exp_f32 v9, v9\n"
    "v_mfma_f32_16x16x32_bf16 v[180:183], a[132:135], v[96:99], v[180:183]\n"
    "v_mfma_f32_16x16x32_bf16 v[180:183], a[128:131], v[92:95], v[180:183]\n"
    "v_mfma_f32_16x16x32_bf16 v[176:179], a[120:123], v[92:95], v[176:179]\n"
    "v_exp_f32 v10, v10\n"
    "v_mfma_f32_16x16x32_bf16 v[176:179], a[124:127], v[96:99], v[176:179]\n"
    "v_cvt_pk_u16_u32 v223, v223, v227\n"
    "v_mfma_f32_16x16x32_bf16 v[172:175], a[116:119], v[96:99], v[172:175]\n"
    "v_exp_f32 v11, v11\n"
    "v_mfma_f32_16x16x32_bf16 v[172:175], a[112:115], v[92:95], v[172:175]\n"
    "s_waitcnt lgkmcnt(0)\n"
    "v_mfma_f32_16x16x32_bf16 v[136:139], a[136:139], v[68:71], v[136:139]\n"
    "ds_read_b128 a[112:115], v249 offset:6144\n"
    "v_mfma_f32_16x16x32_bf16 v[136:139], a[140:143], v[72:75], v[136:139]\n"
    "ds_read_b128 a[116:119], v249 offset:18432\n"
    "v_mfma_f32_16x16x32_bf16 v[188:191], a[148:151], v[72:75], v[188:191]\n"
    "ds_read_b128 a[120:123], v249 offset:7168\n"
    "v_or3_b32 v243, v220, v221, v243\n"
    "v_mfma_f32_16x16x32_bf16 v[188:191], a[144:147], v[68:71], v[188:191]\n"
    "ds_read_b128 a[124:127], v249 offset:19456\n"
    "v_mfma_f32_16x16x32_bf16 v[192:195], a[152:155], v[68:71], v[192:195]\n"
    "ds_read_b128 a[128:131], v249 offset:8192\n"
    "v_mfma_f32_16x16x32_bf16 v[192:195], a[156:159], v[72:75], v[192:195]\n"
    "ds_read_b128 a[132:135], v249 offset:20480\n"
    "v_smfmac_f32_16x16x64_bf16 v[108:111], v[104:107], v[68:75], v253\n"
    "s_add_u32 s81, s81, 128\n"
    "s_cmp_gt_u32 s81, 4096\n"
    "s_cselect_b32 s81, 0, s81\n"
    "v_smfmac_f32_16x16x64_bf16 v[112:115], v[104:107], v[76:83], v253\n"
    "v_mfma_f32_16x16x32_bf16 a[4:7], a[156:159], v[80:83], a[4:7]\n"
    "v_exp_f32 v12, v12\n"
    "v_mfma_f32_16x16x32_bf16 a[4:7], a[152:155], v[76:79], a[4:7]\n"
    "v_exp_f32 v13, v13\n"
    "v_mfma_f32_16x16x32_bf16 a[0:3], a[144:147], v[76:79], a[0:3]\n"
    "v_exp_f32 v14, v14\n"
    "v_mfma_f32_16x16x32_bf16 a[0:3], a[148:151], v[80:83], a[0:3]\n"
    "v_exp_f32 v15, v15\n"
    "v_mfma_f32_16x16x32_bf16 v[152:155], a[140:143], v[80:83], v[152:155]\n"
    "v_or3_b32 v243, v222, v223, v243\n"
    "v_and_b32 v208, 0xff00ff00, v243\n"
    "v_mfma_f32_16x16x32_bf16 v[152:155], a[136:139], v[76:79], v[152:155]\n"
    "v_exp_f32 v16, v16\n"
    "v_mfma_f32_16x16x32_bf16 v[168:171], a[136:139], v[84:87], v[168:171]\n"
    "v_exp_f32 v17, v17\n"
    "v_mfma_f32_16x16x32_bf16 v[168:171], a[140:143], v[88:91], v[168:171]\n"
    "v_mfma_f32_16x16x32_bf16 a[16:19], a[148:151], v[88:91], a[16:19]\n"
    "v_mfma_f32_16x16x32_bf16 a[16:19], a[144:147], v[84:87], a[16:19]\n"
    "v_exp_f32 v18, v18\n"
    "v_mfma_f32_16x16x32_bf16 a[20:23], a[152:155], v[84:87], a[20:23]\n"
    "v_cmp_ne_u32 vcc, 0, v208\n"
    "v_mfma_f32_16x16x32_bf16 a[20:23], a[156:159], v[88:91], a[20:23]\n"
    "v_smfmac_f32_16x16x64_bf16 v[116:119], v[104:107], v[84:91], v253\n"
    "v_smfmac_f32_16x16x64_bf16 v[120:123], v[104:107], v[92:99], v253\n"
    "v_mfma_f32_16x16x32_bf16 a[36:39], a[156:159], v[96:99], a[36:39]\n"
    "v_exp_f32 v19, v19\n"
    "v_mfma_f32_16x16x32_bf16 a[36:39], a[152:155], v[92:95], a[36:39]\n"
    "v_exp_f32 v20, v20\n"
    "v_mfma_f32_16x16x32_bf16 a[32:35], a[144:147], v[92:95], a[32:35]\n"
    "v_cndmask_b32 v208, 2.0, v241, vcc\n"
    "v_mfma_f32_16x16x32_bf16 a[32:35], a[148:151], v[96:99], a[32:35]\n"
    "v_exp_f32 v21, v21\n"
    "v_mfma_f32_16x16x32_bf16 v[184:187], a[140:143], v[96:99], v[184:187]\n"
    "v_mfma_f32_16x16x32_bf16 v[184:187], a[136:139], v[92:95], v[184:187]\n"
    "s_waitcnt lgkmcnt(0)\n"
    "v_mfma_f32_16x16x32_bf16 v[196:199], a[112:115], v[68:71], v[196:199]\n"
    "ds_read_b128 a[136:139], v249 offset:9216\n"
    "v_mfma_f32_16x16x32_bf16 v[196:199], a[116:119], v[72:75], v[196:199]\n"
    "ds_read_b128 a[140:143], v249 offset:21504\n"
    "v_mfma_f32_16x16x32_bf16 v[200:203], a[124:127], v[72:75], v[200:203]\n"
    "ds_read_b128 a[144:147], v249 offset:10240\n"
    "v_mfma_f32_16x16x32_bf16 v[200:203], a[120:123], v[68:71], v[200:203]\n"
    "ds_read_b128 a[148:151], v249 offset:22528\n"
    "v_mfma_f32_16x16x32_bf16 a[48:51], a[128:131], v[68:71], a[48:51]\n"
    "buffer_load_dwordx2 v[236:237], v241, s[36:39], s81 offen offset:0 sc1\n"
    "v_mfma_f32_16x16x32_bf16 a[48:51], a[132:135], v[72:75], a[48:51]\n"
    "ds_read_b128 a[152:155], v249 offset:11264\n"
    "v_mfma_f32_16x16x32_bf16 a[64:67], a[132:135], v[80:83], a[64:67]\n"
    "ds_read_b128 a[156:159], v249 offset:23552\n"
    "v_mfma_f32_16x16x32_bf16 a[64:67], a[128:131], v[76:79], a[64:67]\n"
    "buffer_store_dword v204, v244, s[20:23], s76 offen offset:0 sc1\n"
    "v_mfma_f32_16x16x32_bf16 a[12:15], a[120:123], v[76:79], a[12:15]\n"
    "v_exp_f32 v22, v22\n"
    "v_mfma_f32_16x16x32_bf16 a[12:15], a[124:127], v[80:83], a[12:15]\n"
    "buffer_store_dword v205, v244, s[20:23], s76 offen offset:1024 sc1\n"
    "v_mfma_f32_16x16x32_bf16 a[8:11], a[116:119], v[80:83], a[8:11]\n"
    "v_exp_f32 v23, v23\n"
    "v_mfma_f32_16x16x32_bf16 a[8:11], a[112:115], v[76:79], a[8:11]\n"
    "buffer_store_dword v206, v244, s[20:23], s76 offen offset:2048 sc1\n"
    "v_mfma_f32_16x16x32_bf16 a[24:27], a[112:115], v[84:87], a[24:27]\n"
    "v_exp_f32 v24, v24\n"
    "v_mfma_f32_16x16x32_bf16 a[24:27], a[116:119], v[88:91], a[24:27]\n"
    "buffer_store_dword v207, v244, s[20:23], s76 offen offset:3072 sc1\n"
    "v_mfma_f32_16x16x32_bf16 a[28:31], a[124:127], v[88:91], a[28:31]\n"
    "v_exp_f32 v25, v25\n"
    "v_mfma_f32_16x16x32_bf16 a[28:31], a[120:123], v[84:87], a[28:31]\n"
    "buffer_store_dword v212, v244, s[20:23], s77 offen offset:0 sc1\n"
    "v_mfma_f32_16x16x32_bf16 a[80:83], a[128:131], v[84:87], a[80:83]\n"
    "v_exp_f32 v26, v26\n"
    "v_mfma_f32_16x16x32_bf16 a[80:83], a[132:135], v[88:91], a[80:83]\n"
    "buffer_store_dword v213, v244, s[20:23], s77 offen offset:1024 sc1\n"
    "v_mfma_f32_16x16x32_bf16 a[96:99], a[132:135], v[96:99], a[96:99]\n"
    "v_exp_f32 v27, v27\n"
    "v_mfma_f32_16x16x32_bf16 a[96:99], a[128:131], v[92:95], a[96:99]\n"
    "buffer_store_dword v214, v244, s[20:23], s77 offen offset:2048 sc1\n"
    "v_mfma_f32_16x16x32_bf16 a[44:47], a[120:123], v[92:95], a[44:47]\n"
    "v_exp_f32 v28, v28\n"
    "v_mfma_f32_16x16x32_bf16 a[44:47], a[124:127], v[96:99], a[44:47]\n"
    "buffer_store_dword v215, v244, s[20:23], s77 offen offset:3072 sc1\n"
    "v_mfma_f32_16x16x32_bf16 a[40:43], a[116:119], v[96:99], a[40:43]\n"
    "v_exp_f32 v29, v29\n"
    "v_mfma_f32_16x16x32_bf16 a[40:43], a[112:115], v[92:95], a[40:43]\n"
    "s_waitcnt lgkmcnt(0)\n"
    "v_exp_f32 v30, v30\n"
    "v_mfma_f32_16x16x32_bf16 a[52:55], a[136:139], v[68:71], a[52:55]\n"
    "ds_read_b128 a[112:115], v3 offset:12288\n"
    "v_mfma_f32_16x16x32_bf16 a[52:55], a[140:143], v[72:75], a[52:55]\n"
    "ds_read_b128 a[116:119], v3 offset:14336\n"
    "v_mfma_f32_16x16x32_bf16 a[56:59], a[148:151], v[72:75], a[56:59]\n"
    "ds_read_b128 a[120:123], v3 offset:16384\n"
    "v_mfma_f32_16x16x32_bf16 a[56:59], a[144:147], v[68:71], a[56:59]\n"
    "ds_read_b128 a[124:127], v3 offset:18432\n"
    "v_mfma_f32_16x16x32_bf16 a[60:63], a[152:155], v[68:71], a[60:63]\n"
    "ds_read_b128 a[128:131], v3 offset:20480\n"
    "v_mfma_f32_16x16x32_bf16 a[60:63], a[156:159], v[72:75], a[60:63]\n"
    "ds_read_b128 a[132:135], v3 offset:22528\n"
    "v_mfma_f32_16x16x32_bf16 a[76:79], a[156:159], v[80:83], a[76:79]\n"
    "ds_read_b128 v[100:103], v250 offset:1664\n"
    "v_mfma_f32_16x16x32_bf16 a[76:79], a[152:155], v[76:79], a[76:79]\n"
    "buffer_store_dword v220, v244, s[20:23], s78 offen offset:0 sc1\n"
    "v_mfma_f32_16x16x32_bf16 a[72:75], a[144:147], v[76:79], a[72:75]\n"
    "v_exp_f32 v31, v31\n"
    "v_mfma_f32_16x16x32_bf16 a[72:75], a[148:151], v[80:83], a[72:75]\n"
    "buffer_store_dword v221, v244, s[20:23], s78 offen offset:1024 sc1\n"
    "v_mfma_f32_16x16x32_bf16 a[68:71], a[140:143], v[80:83], a[68:71]\n"
    "v_exp_f32 v32, v32\n"
    "v_mfma_f32_16x16x32_bf16 a[68:71], a[136:139], v[76:79], a[68:71]\n"
    "buffer_store_dword v222, v244, s[20:23], s78 offen offset:2048 sc1\n"
    "v_mfma_f32_16x16x32_bf16 a[84:87], a[136:139], v[84:87], a[84:87]\n"
    "v_exp_f32 v33, v33\n"
    "v_mfma_f32_16x16x32_bf16 a[84:87], a[140:143], v[88:91], a[84:87]\n"
    "buffer_store_dword v223, v244, s[20:23], s78 offen offset:3072 sc1\n"
    "v_mfma_f32_16x16x32_bf16 a[88:91], a[148:151], v[88:91], a[88:91]\n"
    "s_waitcnt vmcnt(13)\n"
    "ds_add_u32 v247, v246\n"
    "v_mfma_f32_16x16x32_bf16 a[88:91], a[144:147], v[84:87], a[88:91]\n"
    "buffer_store_dwordx2 v[238:239], v208, s[36:39], 0 offen offset:0 sc1\n"
    "v_mfma_f32_16x16x32_bf16 a[92:95], a[152:155], v[84:87], a[92:95]\n"
    "v_exp_f32 v34, v34\n"
    "v_mfma_f32_16x16x32_bf16 a[92:95], a[156:159], v[88:91], a[92:95]\n"
    "v_exp_f32 v35, v35\n"
    "v_mfma_f32_16x16x32_bf16 a[108:111], a[156:159], v[96:99], a[108:111]\n"
    "v_mfma_f32_16x16x32_bf16 a[108:111], a[152:155], v[92:95], a[108:111]\n"
    "v_mfma_f32_16x16x32_bf16 a[104:107], a[144:147], v[92:95], a[104:107]\n"
    "s_sub_u32 s74, s68, s65\n"
    "v_add_u32 v250, 1536, v250\n"
    "v_add_u32 v251, 1536, v251\n"
    "v_mfma_f32_16x16x32_bf16 a[104:107], a[148:151], v[96:99], a[104:107]\n"
    "s_add_u32 s66, s66, 1\n"
    "v_mov_b32 v2, v3\n"
    "v_add_u32 v3, s74, v252\n"
    "v_mfma_f32_16x16x32_bf16 a[100:103], a[140:143], v[96:99], a[100:103]\n"
    "s_waitcnt lgkmcnt(1)\n"
    "s_cmp_lt_u32 s66, 28\n"
    "v_mfma_f32_16x16x32_bf16 a[100:103], a[136:139], v[92:95], a[100:103]\n"
    "s_cbranch_scc1 .LPV\n"
    "s_branch .Ltail\n"
    "s_nop 0\n"
    "s_nop 0\n"
    "s_nop 0\n"
    "s_nop 0\n"
    "s_nop 0\n"
    "s_nop 0\n"
    "s_nop 0\n"
    ".LtailP:\n"
    "s_cbranch_execz .Lw2\n"
    "v_swap_b32 v248, v249\n"
    "s_mov_b32 s64, s70\n"
    "v_add_u32 v250, 768, v250\n"
    "v_add_u32 v251, 768, v251\n"
    ".Ltail:\n"
    "v_mfma_f32_16x16x32_bf16 v[36:39], a[112:115], a[192:195], v[100:103]\n"
    "v_cvt_pk_bf16_f32 v92, v16, v17\n"
    "v_cvt_pk_bf16_f32 v77, v10, v11\n"
    "v_mfma_f32_16x16x32_bf16 v[48:51], a[132:135], a[188:191], v[100:103]\n"
    "v_cvt_pk_bf16_f32 v95, v34, v35\n"
    "v_mfma_f32_16x16x32_bf16 v[36:39], a[116:119], a[196:199], v[36:39]\n"
    "v_lshlrev_b32_e64 v240, 16, v1\n"
    "v_lshrrev_b32 v242, 4, v1\n"
    "v_mfma_f32_16x16x32_bf16 v[40:43], a[132:135], a[236:239], v[100:103]\n"
    "ds_read_b128 a[136:139], v252 offset:37888\n"
    "ds_read_b128 a[152:155], v252 offset:46080\n"
    "v_mfma_f32_16x16x32_bf16 v[44:47], a[112:115], a[240:243], v[100:103]\n"
    "ds_read_b128 v[104:107], v250 offset:144\n"
    "v_cvt_pk_bf16_f32 v84, v12, v13\n"
    "v_mfma_f32_16x16x32_bf16 v[36:39], a[120:123], a[200:203], v[36:39]\n"
    "v_cvt_pk_bf16_f32 v76, v8, v9\n"
    "v_mfma_f32_16x16x32_bf16 v[40:43], a[128:131], a[232:235], v[40:43]\n"
    "s_mul_i32 s75, s84, 49152\n"
    "v_and_b32 v243, 0xf0, v1\n"
    "v_mfma_f32_16x16x32_bf16 v[36:39], a[124:127], a[204:207], v[36:39]\n"
    "ds_read_b128 a[140:143], v252 offset:39936\n"
    "ds_read_b128 a[144:147], v252 offset:41984\n"
    "v_mfma_f32_16x16x32_bf16 v[36:39], a[128:131], a[208:211], v[36:39]\n"
    "v_cvt_pk_bf16_f32 v86, v28, v29\n"
    "v_mfma_f32_16x16x32_bf16 v[36:39], a[132:135], a[212:215], v[36:39]\n"
    "v_cvt_pk_bf16_f32 v71, v22, v23\n"
    "v_cvt_pk_bf16_f32 v94, v32, v33\n"
    "v_mfma_f32_16x16x32_bf16 v[48:51], a[128:131], a[184:187], v[48:51]\n"
    "ds_read_b128 a[156:159], v252 offset:48128\n"
    "ds_read_b128 a[148:151], v252 offset:44032\n"
    "v_mfma_f32_16x16x32_bf16 v[40:43], a[124:127], a[228:231], v[40:43]\n"
    "s_add_u32 s76, s75, 196608\n"
    "v_cvt_pk_bf16_f32 v70, v20, v21\n"
    "v_subrev_u32 v248, 24576, v248\n"
    "v_mfma_f32_16x16x32_bf16 v[44:47], a[116:119], a[244:247], v[44:47]\n"
    "v_cvt_pk_bf16_f32 v87, v30, v31\n"
    "v_and_b32 v242, 48, v242\n"
    "v_mfma_f32_16x16x32_bf16 v[48:51], a[124:127], a[180:183], v[48:51]\n"
    "v_exp_f32 v38, v38\n"
    "v_mfma_f32_16x16x32_bf16 v[48:51], a[120:123], a[176:179], v[48:51]\n"
    "v_exp_f32 v36, v36\n"
    "v_mfma_f32_16x16x32_bf16 v[48:51], a[116:119], a[172:175], v[48:51]\n"
    "s_waitcnt lgkmcnt(4)\n"
    "v_cvt_pk_bf16_f32 v69, v6, v7\n"
    "v_mfma_f32_16x16x32_bf16 v[52:55], a[136:139], a[192:195], v[104:107]\n"
    "v_exp_f32 v39, v39\n"
    "v_mfma_f32_16x16x32_bf16 v[48:51], a[112:115], a[168:171], v[48:51]\n"
    "s_add_u32 s77, s76, 196608\n"
    "v_cvt_pk_bf16_f32 v85, v14, v15\n"
    "s_waitcnt lgkmcnt(1)\n"
    "v_mfma_f32_16x16x32_bf16 v[64:67], a[156:159], a[188:191], v[104:107]\n"
    "ds_read_b32 v245, v247\n"
    "v_cvt_pk_bf16_f32 v79, v26, v27\n"
    "v_mfma_f32_16x16x32_bf16 v[40:43], a[120:123], a[224:227], v[40:43]\n"
    "s_nop 0\n"
    "v_exp_f32 v50, v50\n"
    "v_mfma_f32_16x16x32_bf16 v[44:47], a[120:123], a[248:251], v[44:47]\n"
    "ds_read_b128 v[100:103], v250 offset:768\n"
    "v_and_b32 v2, 0x70, v1\n"
    "v_mfma_f32_16x16x32_bf16 v[64:67], a[152:155], a[184:187], v[64:67]\n"
    "s_add_u32 s78, s77, 196608\n"
    "v_exp_f32 v49, v49\n"
    "v_mfma_f32_16x16x32_bf16 v[44:47], a[124:127], a[252:255], v[44:47]\n"
    "s_add_u32 s72, s65, 122880\n"
    "v_exp_f32 v48, v48\n"
    "v_mfma_f32_16x16x32_bf16 v[40:43], a[116:119], a[220:223], v[40:43]\n"
    "v_exp_f32 v51, v51\n"
    "v_mfma_f32_16x16x32_bf16 v[56:59], a[156:159], a[236:239], v[104:107]\n"
    "s_waitcnt vmcnt(0)\n"
    "v_xor_b32 v242, v242, v2\n"
    "v_mfma_f32_16x16x32_bf16 v[40:43], a[112:115], a[216:219], v[40:43]\n"
    "s_waitcnt lgkmcnt(2)\n"
    "v_lshl_add_u32 v243, v243, 3, s72\n"
    "v_add_u32 v243, v243, v242\n"
    "v_mfma_f32_16x16x32_bf16 v[64:67], a[148:151], a[180:183], v[64:67]\n"
    "v_lshrrev_b32 v242, 3, v1\n"
    "v_cmp_eq_u64 s[54:55], v[236:237], v[238:239]\n"
    "v_mfma_f32_16x16x32_bf16 v[60:63], a[136:139], a[240:243], v[104:107]\n"
    "s_waitcnt lgkmcnt(1)\n"
    "v_cmp_le_u32 s[56:57], s59, v245\n"
    "v_mfma_f32_16x16x32_bf16 v[60:63], a[140:143], a[244:247], v[60:63]\n"
    "s_and_b64 s[54:55], s[54:55], s[56:57]\n"
    "v_cvt_pk_bf16_f32 v93, v18, v19\n"
    "v_and_b32 v242, 0x70, v242\n"
    "v_mfma_f32_16x16x32_bf16 v[60:63], a[144:147], a[248:251], v[60:63]\n"
    "s_cmp_eq_u64 s[54:55], -1\n"
    "ds_read_b128 v[104:107], v250 offset:784\n"
    "v_cvt_pk_bf16_f32 v96, v48, v49\n"
    "v_mfma_f32_16x16x32_bf16 v[60:63], a[148:151], a[252:255], v[60:63]\n"
    "s_cbranch_scc0 .Lpt0\n"
    ".Lpt0r:\n"
    "v_mfma_f32_16x16x32_bf16 v[44:47], a[128:131], a[160:163], v[44:47]\n"
    "ds_read_b128 a[120:123], v3 offset:4096\n"
    "v_cvt_pk_bf16_f32 v78, v24, v25\n"
    "v_mfma_f32_16x16x32_bf16 v[60:63], a[152:155], a[160:163], v[60:63]\n"
    "v_exp_f32 v42, v42\n"
    "v_mfma_f32_16x16x32_bf16 v[52:55], a[140:143], a[196:199], v[52:55]\n"
    "s_add_u32 s81, s81, 128\n"
    "ds_read_b128 a[112:115], v3 offset:0\n"
    "s_add_u32 m0, s65, 0\n"
    "v_mfma_f32_16x16x32_bf16 v[44:47], a[132:135], a[164:167], v[44:47]\n"
    "s_add_u32 s83, s67, 0\n"
    "buffer_load_dwordx4 v1, s[16:19], s83 offen offset:0 sc1 lds\n"
    "v_mfma_f32_16x16x32_bf16 v[56:59], a[152:155], a[232:235], v[56:59]\n"
    "ds_read_b128 a[128:131], v3 offset:8192\n"
    "ds_read_b128 a[132:135], v3 offset:10240\n"
    "v_mfma_f32_16x16x32_bf16 v[52:55], a[144:147], a[200:203], v[52:55]\n"
    "buffer_load_dwordx4 v1, s[16:19], s83 offen offset:1024 sc1 lds\n"
    "v_mfma_f32_16x16x32_bf16 v[64:67], a[144:147], a[176:179], v[64:67]\n"
    "v_exp_f32 v46, v46\n"
    "v_mfma_f32_16x16x32_bf16 v[56:59], a[148:151], a[228:231], v[56:59]\n"
    "v_exp_f32 v47, v47\n"
    "v_mfma_f32_16x16x32_bf16 v[60:63], a[156:159], a[164:167], v[60:63]\n"
    "v_exp_f32 v40, v40\n"
    "v_mfma_f32_16x16x32_bf16 v[52:55], a[148:151], a[204:207], v[52:55]\n"
    "buffer_load_dwordx4 v1, s[16:19], s83 offen offset:2048 sc1 lds\n"
    "v_mfma_f32_16x16x32_bf16 v[52:55], a[152:155], a[208:211], v[52:55]\n"
    "s_waitcnt lgkmcnt(2)\n"
    "v_exp_f32_e64 v44, v44\n"
    "v_mfma_f32_16x16x32_bf16 v[12:15], a[112:115], a[240:243], v[100:103]\n"
    "buffer_load_dwordx4 v1, s[16:19], s83 offen offset:3072 sc1 lds\n"
    "v_mfma_f32_16x16x32_bf16 v[64:67], a[140:143], a[172:175], v[64:67]\n"
    "ds_read_b128 a[124:127], v3 offset:6144\n"
    "ds_read_b128 a[116:119], v3 offset:2048\n"
    "v_mfma_f32_16x16x32_bf16 v[52:55], a[156:159], a[212:215], v[52:55]\n"
    "s_waitcnt lgkmcnt(2)\n"
    "v_exp_f32_e64 v62, v62\n"
    "v_mfma_f32_16x16x32_bf16 v[8:11], a[132:135], a[236:239], v[100:103]\n"
    "s_add_u32 m0, s65, 4096\n"
    "v_cvt_pk_bf16_f32 v73, v38, v39\n"
    "v_cvt_pk_bf16_f32 v68, v4, v5\n"
    "v_mfma_f32_16x16x32_bf16 v[56:59], a[144:147], a[224:227], v[56:59]\n"
    "v_cvt_pk_bf16_f32 v89, v46, v47\n"
    "v_mad_u32_u24 v244, v242, 24, v2\n"
    "v_mfma_f32_16x16x32_bf16 v[4:7], a[112:115], a[192:195], v[100:103]\n"
    ".Lwp0:\n"
    "s_cbranch_execz .Lwp2\n"
    "s_add_u32 s83, s67, 4096\n"
    "buffer_load_dwordx4 v1, s[16:19], s83 offen offset:0 sc1 lds\n"
    "v_mfma_f32_16x16x32_bf16 v[64:67], a[136:139], a[168:171], v[64:67]\n"
    "ds_read_b128 a[156:159], v3 offset:11264\n"
    "ds_read_b128 v[232:235], v251 offset:0\n"
    "v_mfma_f32_16x16x32_bf16 v[8:11], a[128:131], a[232:235], v[8:11]\n"
    "buffer_load_dwordx4 v1, s[16:19], s83 offen offset:1024 sc1 lds\n"
    "v_mfma_f32_16x16x32_bf16 v[16:19], a[132:135], a[188:191], v[100:103]\n"
    "v_exp_f32_e64 v55, v55\n"
    "v_mfma_f32_16x16x32_bf16 v[56:59], a[140:143], a[220:223], v[56:59]\n"
    "s_waitcnt lgkmcnt(2)\n"
    "v_exp_f32_e64 v54, v54\n"
    "v_mfma_f32_16x16x32_bf16 v[16:19], a[128:131], a[184:187], v[16:19]\n"
    "s_add_u32 m0, s65, 98304\n"
    "ds_read_b128 a[144:147], v3 offset:5120\n"
    "v_xor_b32_e64 v2, 64, v243\n"
    "v_mfma_f32_16x16x32_bf16 v[4:7], a[116:119], a[196:199], v[4:7]\n"
    "s_add_u32 s83, s67, 24576\n"
    "v_exp_f32_e64 v52, v52\n"
    "v_mfma_f32_16x16x32_bf16 v[4:7], a[120:123], a[200:203], v[4:7]\n"
    "v_exp_f32_e64 v53, v53\n"
    "v_mfma_f32_16x16x32_bf16 v[16:19], a[124:127], a[180:183], v[16:19]\n"
    "buffer_load_dwordx4 v1, s[16:19], s83 offen offset:0 sc1 lds\n"
    "v_mfma_f32_16x16x32_bf16 v[56:59], a[136:139], a[216:219], v[56:59]\n"
    "s_waitcnt lgkmcnt(2)\n"
    "ds_read_b128 a[152:155], v3 offset:9216\n"
    "ds_read_b128 a[148:151], v3 offset:7168\n"
    "v_mfma_f32_16x16x32_bf16 v[24:27], a[156:159], a[236:239], v[104:107]\n"
    "buffer_load_dwordx4 v1, s[16:19], s83 offen offset:1024 sc1 lds\n"
    "v_mfma_f32_16x16x32_bf16 v[4:7], a[124:127], a[204:207], v[4:7]\n"
    "v_exp_f32_e64 v43, v43\n"
    "v_mfma_f32_16x16x32_bf16 v[16:19], a[120:123], a[176:179], v[16:19]\n"
    "buffer_load_dwordx4 v1, s[16:19], s83 offen offset:2048 sc1 lds\n"
    "v_mfma_f32_16x16x32_bf16 v[8:11], a[124:127], a[228:231], v[8:11]\n"
    "v_exp_f32 v37, v37\n"
    "v_mfma_f32_16x16x32_bf16 v[12:15], a[116:119], a[244:247], v[12:15]\n"
    "v_exp_f32 v58, v58\n"
    "v_mfma_f32_16x16x32_bf16 v[12:15], a[120:123], a[248:251], v[12:15]\n"
    "ds_read_b128 a[136:139], v3 offset:1024\n"
    "ds_read_b128 a[140:143], v3 offset:3072\n"
    "v_mfma_f32_16x16x32_bf16 v[4:7], a[128:131], a[208:211], v[4:7]\n"
    "v_exp_f32 v45, v45\n"
    "v_mfma_f32_16x16x32_bf16 v[8:11], a[120:123], a[224:227], v[8:11]\n"
    "v_exp_f32 v59, v59\n"
    "v_mfma_f32_16x16x32_bf16 v[32:35], a[156:159], a[188:191], v[104:107]\n"
    "v_exp_f32 v61, v61\n"
    "v_mfma_f32_16x16x32_bf16 v[16:19], a[116:119], a[172:175], v[16:19]\n"
    "s_waitcnt lgkmcnt(3)\n"
    "v_exp_f32 v60, v60\n"
    "v_mfma_f32_16x16x32_bf16 v[32:35], a[152:155], a[184:187], v[32:35]\n"
    "v_exp_f32 v67, v67\n"
    "v_mfma_f32_16x16x32_bf16 v[24:27], a[152:155], a[232:235], v[24:27]\n"
    "v_exp_f32 v66, v66\n"
    "v_mfma_f32_16x16x32_bf16 v[16:19], a[112:115], a[168:171], v[16:19]\n"
    "s_mov_b32 s74, 0x47000000\n"
    "ds_read_b128 v[100:103], v250 offset:896\n"
    "v_cvt_pk_bf16_f32 v90, v60, v61\n"
    "v_mfma_f32_16x16x32_bf16 v[8:11], a[116:119], a[220:223], v[8:11]\n"
    "v_exp_f32 v41, v41\n"
    "v_mfma_f32_16x16x32_bf16 v[12:15], a[124:127], a[252:255], v[12:15]\n"
    "buffer_load_dwordx4 v1, s[16:19], s83 offen offset:3072 sc1 lds\n"
    "v_mfma_f32_16x16x32_bf16 v[8:11], a[112:115], a[216:219], v[8:11]\n"
    "s_waitcnt lgkmcnt(2)\n"
    "ds_read_b128 a[112:115], v3 offset:12288\n"
    "v_cvt_pk_bf16_f32 v80, v40, v41\n"
    "v_mfma_f32_16x16x32_bf16 v[20:23], a[136:139], a[192:195], v[104:107]\n"
    "v_exp_f32 v16, v16\n"
    "v_mfma_f32_16x16x32_bf16 v[24:27], a[148:151], a[228:231], v[24:27]\n"
    "s_add_u32 m0, s65, 102400\n"
    "ds_read_b128 a[116:119], v3 offset:14336\n"
    "v_cvt_pk_bf16_f32 v88, v44, v45\n"
    "v_mfma_f32_16x16x32_bf16 v[12:15], a[128:131], a[160:163], v[12:15]\n"
    "s_add_u32 s83, s67, 28672\n"
    "v_exp_f32 v57, v57\n"
    "v_mfma_f32_16x16x32_bf16 v[12:15], a[132:135], a[164:167], v[12:15]\n"
    "v_exp_f32 v64, v64\n"
    "v_mfma_f32_16x16x32_bf16 v[4:7], a[132:135], a[212:215], v[4:7]\n"
    "ds_read_b128 a[120:123], v3 offset:16384\n"
    "ds_read_b128 a[124:127], v3 offset:18432\n"
    "v_mfma_f32_16x16x32_bf16 v[28:31], a[136:139], a[240:243], v[104:107]\n"
    "s_waitcnt lgkmcnt(5)\n"
    "v_exp_f32 v19, v19\n"
    "v_mfma_f32_16x16x32_bf16 v[28:31], a[140:143], a[244:247], v[28:31]\n"
    "buffer_load_dwordx4 v1, s[16:19], s83 offen offset:0 sc1 lds\n"
    "v_mfma_f32_16x16x32_bf16 v[28:31], a[144:147], a[248:251], v[28:31]\n"
    "s_waitcnt lgkmcnt(3)\n"
    "v_exp_f32 v56, v56\n"
    "v_mfma_f32_16x16x32_bf16 v[44:47], a[112:115], a[240:243], v[100:103]\n"
    "buffer_load_dwordx2 v[236:237], v241, s[36:39], s81 offen offset:0 sc1\n"
    "v_mfma_f32_16x16x32_bf16 v[32:35], a[148:151], a[180:183], v[32:35]\n"
    "v_cvt_pk_bf16_f32 v72, v36, v37\n"
    "v_cvt_pk_bf16_f32 v82, v56, v57\n"
    "v_mfma_f32_16x16x32_bf16 v[28:31], a[148:151], a[252:255], v[28:31]\n"
    "ds_read_b128 a[132:135], v3 offset:22528\n"
    "ds_read_b128 a[128:131], v3 offset:20480\n"
    "v_mfma_f32_16x16x32_bf16 v[20:23], a[140:143], a[196:199], v[20:23]\n"
    "v_exp_f32 v14, v14\n"
    "v_mfma_f32_16x16x32_bf16 v[20:23], a[144:147], a[200:203], v[20:23]\n"
    "v_exp_f32 v65, v65\n"
    "v_mfma_f32_16x16x32_bf16 v[20:23], a[148:151], a[204:207], v[20:23]\n"
    "v_exp_f32 v18, v18\n"
    "v_mfma_f32_16x16x32_bf16 v[32:35], a[144:147], a[176:179], v[32:35]\n"
    "v_exp_f32 v5, v5\n"
    "v_mfma_f32_16x16x32_bf16 v[20:23], a[152:155], a[208:211], v[20:23]\n"
    "v_exp_f32 v15, v15\n"
    "v_mfma_f32_16x16x32_bf16 v[28:31], a[152:155], a[160:163], v[28:31]\n"
    "v_exp_f32 v12, v12\n"
    "v_mfma_f32_16x16x32_bf16 v[24:27], a[144:147], a[224:227], v[24:27]\n"
    "s_waitcnt lgkmcnt(4)\n"
    "v_exp_f32 v6, v6\n"
    "v_mfma_f32_16x16x32_bf16 v[44:47], a[116:119], a[244:247], v[44:47]\n"
    "v_exp_f32 v17, v17\n"
    "v_mfma_f32_16x16x32_bf16 v[20:23], a[156:159], a[212:215], v[20:23]\n"
    "s_waitcnt lgkmcnt(1)\n"
    "ds_read_b128 v[104:107], v250 offset:912\n"
    "v_cvt_pk_bf16_f32 v81, v42, v43\n"
    "v_mfma_f32_16x16x32_bf16 v[40:43], a[132:135], a[236:239], v[100:103]\n"
    "v_cvt_pk_bf16_f32 v97, v50, v51\n"
    "v_cvt_pk_bf16_f32 v228, v16, v17\n"
    "v_mfma_f32_16x16x32_bf16 v[28:31], a[156:159], a[164:167], v[28:31]\n"
    "v_cvt_pk_bf16_f32 v74, v52, v53\n"
    "v_cvt_pk_bf16_f32 v83, v58, v59\n"
    "v_mfma_f32_16x16x32_bf16 v[24:27], a[140:143], a[220:223], v[24:27]\n"
    "ds_read_b128 a[156:159], v3 offset:23552\n"
    "ds_read_b128 a[148:151], v3 offset:19456\n"
    "v_mfma_f32_16x16x32_bf16 v[36:39], a[112:115], a[192:195], v[100:103]\n"
    "ds_read_b128 a[152:155], v3 offset:21504\n"
    "v_cvt_pk_bf16_f32 v221, v14, v15\n"
    "v_mfma_f32_16x16x32_bf16 v[24:27], a[136:139], a[216:219], v[24:27]\n"
    "v_exp_f32 v22, v22\n"
    "v_mfma_f32_16x16x32_bf16 v[44:47], a[120:123], a[248:251], v[44:47]\n"
    "s_waitcnt lgkmcnt(4)\n"
    "v_exp_f32 v21, v21\n"
    "v_mfma_f32_16x16x32_bf16 v[40:43], a[128:131], a[232:235], v[40:43]\n"
    "v_exp_f32 v13, v13\n"
    "v_mfma_f32_16x16x32_bf16 v[48:51], a[132:135], a[188:191], v[100:103]\n"
    "v_cvt_pk_bf16_f32 v98, v64, v65\n"
    "v_cvt_pk_bf16_f32 v75, v54, v55\n"
    "v_mfma_f32_16x16x32_bf16 v[32:35], a[140:143], a[172:175], v[32:35]\n"
    "v_exp_f32 v20, v20\n"
    "v_mfma_f32_16x16x32_bf16 v[32:35], a[136:139], a[168:171], v[32:35]\n"
    "ds_read_b128 a[136:139], v3 offset:13312\n"
    "v_cvt_pk_bf16_f32 v99, v66, v67\n"
    "v_mfma_f32_16x16x32_bf16 v[40:43], a[124:127], a[228:231], v[40:43]\n"
    "v_exp_f32 v31, v31\n"
    "v_mfma_f32_16x16x32_bf16 v[40:43], a[120:123], a[224:227], v[40:43]\n"
    "v_exp_f32 v11, v11\n"
    "v_mfma_f32_16x16x32_bf16 v[40:43], a[116:119], a[220:223], v[40:43]\n"
    "v_exp_f32 v7, v7\n"
    "v_mfma_f32_16x16x32_bf16 v[36:39], a[116:119], a[196:199], v[36:39]\n"
    "v_exp_f32 v63, v63\n"
    "v_mfma_f32_16x16x32_bf16 v[36:39], a[120:123], a[200:203], v[36:39]\n"
    "v_exp_f32 v4, v4\n"
    "v_mfma_f32_16x16x32_bf16 v[48:51], a[128:131], a[184:187], v[48:51]\n"
    "v_exp_f32 v35, v35\n"
    "v_mfma_f32_16x16x32_bf16 v[40:43], a[112:115], a[216:219], v[40:43]\n"
    "s_waitcnt lgkmcnt(3)\n"
    "v_exp_f32 v23, v23\n"
    "v_mfma_f32_16x16x32_bf16 v[56:59], a[156:159], a[236:239], v[104:107]\n"
    "v_exp_f32 v29, v29\n"
    "v_mfma_f32_16x16x32_bf16 v[36:39], a[124:127], a[204:207], v[36:39]\n"
    "s_mov_b32 s79, 133\n"
    "v_exp_f32 v26, v26\n"
    "v_mfma_f32_16x16x32_bf16 v[48:51], a[124:127], a[180:183], v[48:51]\n"
    "ds_read_b128 a[140:143], v3 offset:15360\n"
    "v_cvt_pk_bf16_f32 v91, v62, v63\n"
    "v_mfma_f32_16x16x32_bf16 v[48:51], a[120:123], a[176:179], v[48:51]\n"
    "ds_read_b128 a[120:123], v248 offset:25600\n"
    "ds_read_b128 a[144:147], v3 offset:17408\n"
    "v_mfma_f32_16x16x32_bf16 v[48:51], a[116:119], a[172:175], v[48:51]\n"
    "s_waitcnt lgkmcnt(3)\n"
    "v_exp_f32 v24, v24\n"
    "v_mfma_f32_16x16x32_bf16 v[52:55], a[136:139], a[192:195], v[104:107]\n"
    "v_exp_f32 v41, v41\n"
    "v_mfma_f32_16x16x32_bf16 v[48:51], a[112:115], a[168:171], v[48:51]\n"
    "v_exp_f32 v25, v25\n"
    "v_mfma_f32_16x16x32_bf16 v[44:47], a[124:127], a[252:255], v[44:47]\n"
    "v_exp_f32 v40, v40\n"
    "v_mfma_f32_16x16x32_bf16 v[36:39], a[128:131], a[208:211], v[36:39]\n"
    "s_waitcnt vmcnt(6)\n"
    "v_exp_f32 v27, v27\n"
    "v_mfma_f32_16x16x32_bf16 v[64:67], a[156:159], a[188:191], v[104:107]\n"
    "v_exp_f32 v9, v9\n"
    "v_mfma_f32_16x16x32_bf16 v[36:39], a[132:135], a[212:215], v[36:39]\n"
    "v_exp_f32 v42, v42\n"
    "v_mfma_f32_16x16x32_bf16 v[44:47], a[128:131], a[160:163], v[44:47]\n"
    "v_exp_f32 v49, v49\n"
    "v_mfma_f32_16x16x32_bf16 v[60:63], a[136:139], a[240:243], v[104:107]\n"
    "s_waitcnt lgkmcnt(0)\n"
    "ds_add_u32 v247, v246\n"
    "v_mfma_f32_16x16x32_bf16 v[52:55], a[140:143], a[196:199], v[52:55]\n"
    "v_exp_f32 v28, v28\n"
    "v_mfma_f32_16x16x32_bf16 v[52:55], a[144:147], a[200:203], v[52:55]\n"
    "ds_read_b128 a[124:127], v248 offset:37888\n"
    "ds_read_b128 a[116:119], v248 offset:36864\n"
    "v_mfma_f32_16x16x32_bf16 v[64:67], a[152:155], a[184:187], v[64:67]\n"
    "v_exp_f32 v34, v34\n"
    "v_mfma_f32_16x16x32_bf16 v[44:47], a[132:135], a[164:167], v[44:47]\n"
    "v_exp_f32 v50, v50\n"
    "v_mfma_f32_16x16x32_bf16 v[56:59], a[152:155], a[232:235], v[56:59]\n"
    "s_mov_b32 s73, 0x7f800000\n"
    "v_exp_f32 v37, v37\n"
    "v_mfma_f32_16x16x32_bf16 v[56:59], a[148:151], a[228:231], v[56:59]\n"
    "v_cvt_pk_bf16_f32 v205, v6, v7\n"
    "v_cvt_pk_bf16_f32 v207, v22, v23\n"
    "v_mfma_f32_16x16x32_bf16 v[64:67], a[148:151], a[180:183], v[64:67]\n"
    "ds_read_b128 a[112:115], v248 offset:24576\n"
    "v_cvt_pk_bf16_f32 v216, v40, v41\n"
    "v_mfma_f32_16x16x32_bf16 v[56:59], a[144:147], a[224:227], v[56:59]\n"
    "v_exp_f32 v33, v33\n"
    "v_mfma_f32_16x16x32_bf16 v[52:55], a[148:151], a[204:207], v[52:55]\n"
    "ds_read_b128 a[128:131], v248 offset:26624\n"
    "ds_read_b128 a[132:135], v248 offset:38912\n"
    "v_mfma_f32_16x16x32_bf16 v[64:67], a[144:147], a[176:179], v[64:67]\n"
    "v_exp_f32 v51, v51\n"
    "v_mfma_f32_16x16x32_bf16 v[176:179], a[120:123], v[92:95], v[176:179]\n"
    "buffer_load_dwordx4 v1, s[16:19], s83 offen offset:1024 sc1 lds\n"
    "v_mfma_f32_16x16x32_bf16 v[56:59], a[140:143], a[220:223], v[56:59]\n"
    "s_waitcnt lgkmcnt(4)\n"
    "v_exp_f32 v36, v36\n"
    "v_mfma_f32_16x16x32_bf16 v[128:131], a[124:127], v[72:75], v[128:131]\n"
    "ds_read_b32 v245, v247\n"
    "v_cvt_pk_bf16_f32 v208, v36, v37\n"
    "v_mfma_f32_16x16x32_bf16 v[60:63], a[140:143], a[244:247], v[60:63]\n"
    "s_waitcnt lgkmcnt(4)\n"
    "v_exp_f32 v30, v30\n"
    "v_mfma_f32_16x16x32_bf16 v[140:143], a[116:119], v[80:83], v[140:143]\n"
    "v_exp_f32 v44, v44\n"
    "v_mfma_f32_16x16x32_bf16 v[64:67], a[140:143], a[172:175], v[64:67]\n"
    "v_exp_f32 v39, v39\n"
    "v_mfma_f32_16x16x32_bf16 v[60:63], a[144:147], a[248:251], v[60:63]\n"
    "s_waitcnt lgkmcnt(2)\n"
    "v_exp_f32 v8, v8\n"
    "v_mfma_f32_16x16x32_bf16 v[132:135], a[128:131], v[68:71], v[132:135]\n"
    "s_add_u32 s59, s59, 4\n"
    "v_exp_f32 v48, v48\n"
    "v_mfma_f32_16x16x32_bf16 v[52:55], a[152:155], a[208:211], v[52:55]\n"
    "v_exp_f32 v46, v46\n"
    "v_mfma_f32_16x16x32_bf16 v[164:167], a[128:131], v[84:87], v[164:167]\n"
    "v_exp_f32 v10, v10\n"
    "v_mfma_f32_16x16x32_bf16 v[56:59], a[136:139], a[216:219], v[56:59]\n"
    "v_exp_f32 v38, v38\n"
    "v_smfmac_f32_16x16x64_bf16 v[120:123], v[232:235], v[92:99], v253\n"
    "v_cvt_pk_bf16_f32 v212, v8, v9\n"
    "v_mfma_f32_16x16x32_bf16 v[128:131], a[120:123], v[68:71], v[128:131]\n"
    "s_waitcnt lgkmcnt(0)\n"
    "v_exp_f32 v43, v43\n"
    "v_mfma_f32_16x16x32_bf16 v[132:135], a[132:135], v[72:75], v[132:135]\n"
    "s_waitcnt vmcnt(1)\n"
    "v_cmp_eq_u64 s[54:55], v[236:237], v[238:239]\n"
    "v_cmp_le_u32 s[56:57], s59, v245\n"
    "v_smfmac_f32_16x16x64_bf16 v[108:111], v[232:235], v[68:75], v253\n"
    "s_and_b64 s[54:55], s[54:55], s[56:57]\n"
    "s_cmp_eq_u64 s[54:55], -1\n"
    "v_cvt_pk_bf16_f32 v206, v20, v21\n"
    "v_smfmac_f32_16x16x64_bf16 v[112:115], v[232:235], v[76:83], v253\n"
    "s_cbranch_scc0 .Lpt1\n"
    ".Lpt1r:\n"
    "v_mfma_f32_16x16x32_bf16 v[60:63], a[148:151], a[252:255], v[60:63]\n"
    "v_exp_f32 v45, v45\n"
    "v_mfma_f32_16x16x32_bf16 v[144:147], a[120:123], v[76:79], v[144:147]\n"
    "s_add_u32 m0, s65, 24576\n"
    "v_exp_f32 v57, v57\n"
    "v_mfma_f32_16x16x32_bf16 v[160:163], a[124:127], v[88:91], v[160:163]\n"
    "v_cvt_pk_bf16_f32 v213, v10, v11\n"
    "v_cvt_pk_bf16_f32 v223, v30, v31\n"
    "v_mfma_f32_16x16x32_bf16 v[124:127], a[112:115], v[68:71], v[124:127]\n"
    "s_add_u32 s83, s67, 49152\n"
    "buffer_load_dwordx4 v1, s[16:19], s83 offen offset:0 sc1 lds\n"
    "v_mfma_f32_16x16x32_bf16 v[64:67], a[136:139], a[168:171], v[64:67]\n"
    "ds_read_b128 a[136:139], v252 offset:0\n"
    "ds_read_b128 v[104:107], v250 offset:1536\n"
    "v_mfma_f32_16x16x32_bf16 v[164:167], a[132:135], v[88:91], v[164:167]\n"
    "buffer_load_dwordx4 v1, s[16:19], s83 offen offset:1024 sc1 lds\n"
    "v_mfma_f32_16x16x32_bf16 v[156:159], a[112:115], v[84:87], v[156:159]\n"
    "ds_read_b128 a[140:143], v252 offset:2048\n"
    "v_cvt_pk_bf16_f32 v214, v24, v25\n"
    "v_mfma_f32_16x16x32_bf16 v[148:151], a[132:135], v[80:83], v[148:151]\n"
    "buffer_load_dwordx4 v1, s[16:19], s83 offen offset:2048 sc1 lds\n"
    "v_mfma_f32_16x16x32_bf16 v[172:175], a[116:119], v[96:99], v[172:175]\n"
    "v_exp_f32 v58, v58\n"
    "v_mfma_f32_16x16x32_bf16 v[172:175], a[112:115], v[92:95], v[172:175]\n"
    "buffer_load_dwordx4 v1, s[16:19], s83 offen offset:3072 sc1 lds\n"
    "v_mfma_f32_16x16x32_bf16 v[60:63], a[152:155], a[160:163], v[60:63]\n"
    "ds_read_b128 a[148:151], v252 offset:6144\n"
    "ds_read_b128 v[100:103], v250 offset:1552\n"
    "v_mfma_f32_16x16x32_bf16 v[124:127], a[116:119], v[72:75], v[124:127]\n"
    "v_cvt_pk_bf16_f32 v209, v38, v39\n"
    "v_cvt_pk_bf16_f32 v220, v12, v13\n"
    "v_mfma_f32_16x16x32_bf16 v[52:55], a[156:159], a[212:215], v[52:55]\n"
    "s_waitcnt lgkmcnt(3)\n"
    "ds_read_b128 a[144:147], v252 offset:4096\n"
    "ds_read_b128 a[152:155], v252 offset:8192\n"
    "v_mfma_f32_16x16x32_bf16 v[60:63], a[156:159], a[164:167], v[60:63]\n"
    "s_add_u32 m0, s65, 28672\n"
    "v_exp_f32 v32, v32\n"
    "v_mfma_f32_16x16x32_bf16 v[20:23], a[136:139], a[240:243], v[104:107]\n"
    "s_add_u32 s83, s67, 53248\n"
    "v_exp_f32 v66, v66\n"
    "v_mfma_f32_16x16x32_bf16 v[156:159], a[116:119], v[88:91], v[156:159]\n"
    "buffer_load_dwordx4 v1, s[16:19], s83 offen offset:0 sc1 lds\n"
    "v_mfma_f32_16x16x32_bf16 v[148:151], a[128:131], v[76:79], v[148:151]\n"
    "ds_read_b128 a[156:159], v252 offset:10240\n"
    "v_cvt_pk_bf16_f32 v204, v4, v5\n"
    "v_mfma_f32_16x16x32_bf16 v[4:7], a[136:139], a[192:195], v[104:107]\n"
    "buffer_load_dwordx4 v1, s[16:19], s83 offen offset:1024 sc1 lds\n"
    "v_mfma_f32_16x16x32_bf16 v[140:143], a[112:115], v[76:79], v[140:143]\n"
    "s_waitcnt lgkmcnt(5)\n"
    "v_exp_f32 v63, v63\n"
    "v_mfma_f32_16x16x32_bf16 v[4:7], a[140:143], a[196:199], v[4:7]\n"
    "buffer_load_dwordx2 v[236:237], v241, s[36:39], 0 offen offset:0 sc1\n"
    "v_mfma_f32_16x16x32_bf16 v[20:23], a[140:143], a[244:247], v[20:23]\n"
    "s_waitcnt lgkmcnt(2)\n"
    "v_exp_f32 v67, v67\n"
    "v_mfma_f32_16x16x32_bf16 v[20:23], a[144:147], a[248:251], v[20:23]\n"
    "v_exp_f32 v53, v53\n"
    "v_mfma_f32_16x16x32_bf16 v[180:183], a[132:135], v[96:99], v[180:183]\n"
    "ds_read_b128 a[112:115], v252 offset:1024\n"
    "ds_read_b128 a[116:119], v252 offset:3072\n"
    "v_mfma_f32_16x16x32_bf16 v[160:163], a[120:123], v[84:87], v[160:163]\n"
    "s_add_u32 m0, s65, 49152\n"
    "buffer_store_dwordx2 v[238:239], v240, s[36:39], s82 offen offset:0 sc1\n"
    "v_exp_f32 v54, v54\n"
    "v_smfmac_f32_16x16x64_bf16 v[116:119], v[232:235], v[84:91], v253\n"
    "ds_read_b128 a[120:123], v252 offset:5120\n"
    "v_mfma_f32_16x16x32_bf16 v[144:147], a[124:127], v[80:83], v[144:147]\n"
    "v_cvt_pk_bf16_f32 v222, v28, v29\n"
    "v_cvt_pk_bf16_f32 v231, v34, v35\n"
    "v_mfma_f32_16x16x32_bf16 v[4:7], a[144:147], a[200:203], v[4:7]\n"
    "s_waitcnt lgkmcnt(3)\n"
    "v_exp_f32 v60, v60\n"
    "v_mfma_f32_16x16x32_bf16 v[28:31], a[156:159], a[188:191], v[104:107]\n"
    "v_exp_f32 v56, v56\n"
    "v_mfma_f32_16x16x32_bf16 v[176:179], a[124:127], v[96:99], v[176:179]\n"
    "v_exp_f32 v47, v47\n"
    "v_mfma_f32_16x16x32_bf16 v[12:15], a[156:159], a[236:239], v[104:107]\n"
    "v_exp_f32 v55, v55\n"
    "v_mfma_f32_16x16x32_bf16 v[28:31], a[152:155], a[184:187], v[28:31]\n"
    "ds_read_b128 a[132:135], v252 offset:11264\n"
    "v_cvt_pk_bf16_f32 v233, v50, v51\n"
    "v_mfma_f32_16x16x32_bf16 v[180:183], a[128:131], v[92:95], v[180:183]\n"
    "s_add_u32 s83, s67, 73728\n"
    "v_exp_f32 v62, v62\n"
    "v_mfma_f32_16x16x32_bf16 v[4:7], a[148:151], a[204:207], v[4:7]\n"
    "ds_read_b128 a[128:131], v252 offset:9216\n"
    "v_cvt_pk_bf16_f32 v230, v32, v33\n"
    "v_mfma_f32_16x16x32_bf16 v[20:23], a[148:151], a[252:255], v[20:23]\n"
    "v_exp_f32 v65, v65\n"
    "v_mfma_f32_16x16x32_bf16 v[12:15], a[152:155], a[232:235], v[12:15]\n"
    "buffer_load_dwordx4 v1, s[16:19], s83 offen offset:0 sc1 lds\n"
    "v_mfma_f32_16x16x32_bf16 v[12:15], a[148:151], a[228:231], v[12:15]\n"
    "ds_read_b128 v[240:243], v251 offset:768\n"
    "v_cvt_pk_bf16_f32 v224, v44, v45\n"
    "v_mfma_f32_16x16x32_bf16 v[28:31], a[148:151], a[180:183], v[28:31]\n"
    "v_cvt_pk_bf16_f32 v211, v54, v55\n"
    "v_cvt_pk_bf16_f32 v229, v18, v19\n"
    "v_mfma_f32_16x16x32_bf16 v[4:7], a[152:155], a[208:211], v[4:7]\n"
    "v_exp_f32 v64, v64\n"
    "v_mfma_f32_16x16x32_bf16 v[4:7], a[156:159], a[212:215], v[4:7]\n"
    "ds_read_b128 a[124:127], v252 offset:7168\n"
    "v_cvt_pk_bf16_f32 v232, v48, v49\n"
    "v_mfma_f32_16x16x32_bf16 v[20:23], a[152:155], a[160:163], v[20:23]\n"
    "s_waitcnt lgkmcnt(3)\n"
    "buffer_load_dwordx4 v1, s[16:19], s83 offen offset:1024 sc1 lds\n"
    "v_mfma_f32_16x16x32_bf16 v[16:19], a[132:135], a[236:239], v[100:103]\n"
    "v_exp_f32 v52, v52\n"
    "v_mfma_f32_16x16x32_bf16 v[12:15], a[144:147], a[224:227], v[12:15]\n"
    "buffer_load_dwordx4 v1, s[16:19], s83 offen offset:2048 sc1 lds\n"
    "v_mfma_f32_16x16x32_bf16 v[28:31], a[144:147], a[176:179], v[28:31]\n"
    "s_waitcnt lgkmcnt(2)\n"
    "v_exp_f32 v4, v4\n"
    "v_mfma_f32_16x16x32_bf16 v[16:19], a[128:131], a[232:235], v[16:19]\n"
    "v_exp_f32 v59, v59\n"
    "v_mfma_f32_16x16x32_bf16 v[20:23], a[156:159], a[164:167], v[20:23]\n"
    "ds_read_b128 a[156:159], v252 offset:22528\n"
    "v_mfma_f32_16x16x32_bf16 v[8:11], a[112:115], a[192:195], v[100:103]\n"
    "v_cvt_pk_bf16_f32 v215, v26, v27\n"
    "v_cvt_pk_bf16_f32 v235, v66, v67\n"
    "v_mfma_f32_16x16x32_bf16 v[8:11], a[116:119], a[196:199], v[8:11]\n"
    "v_exp_f32 v61, v61\n"
    "v_mfma_f32_16x16x32_bf16 v[12:15], a[140:143], a[220:223], v[12:15]\n"
    "v_exp_f32 v5, v5\n"
    "v_mfma_f32_16x16x32_bf16 v[28:31], a[140:143], a[172:175], v[28:31]\n"
    "buffer_load_dwordx4 v1, s[16:19], s83 offen offset:3072 sc1 lds\n"
    "v_mfma_f32_16x16x32_bf16 v[12:15], a[136:139], a[216:219], v[12:15]\n"
    "s_add_u32 m0, s65, 53248\n"
    "v_exp_f32 v21, v21\n"
    "v_mfma_f32_16x16x32_bf16 v[32:35], a[132:135], a[188:191], v[100:103]\n"
    "v_exp_f32 v6, v6\n"
    "v_mfma_f32_16x16x32_bf16 v[32:35], a[128:131], a[184:187], v[32:35]\n"
    "v_cvt_pk_bf16_f32 v226, v60, v61\n"
    "v_cvt_pk_bf16_f32 v234, v64, v65\n"
    "v_mfma_f32_16x16x32_bf16 v[24:27], a[112:115], a[240:243], v[100:103]\n"
    "v_exp_f32 v15, v15\n"
    "v_mfma_f32_16x16x32_bf16 v[28:31], a[136:139], a[168:171], v[28:31]\n"
    "s_waitcnt lgkmcnt(1)\n"
    "ds_read_b128 a[136:139], v252 offset:12288\n"
    "v_cvt_pk_bf16_f32 v210, v52, v53\n"
    "v_mfma_f32_16x16x32_bf16 v[16:19], a[124:127], a[228:231], v[16:19]\n"
    "s_add_u32 s83, s67, 77824\n"
    "ds_read_b128 v[104:107], v250 offset:1664\n"
    "v_cvt_pk_bf16_f32 v217, v42, v43\n"
    "v_mfma_f32_16x16x32_bf16 v[24:27], a[116:119], a[244:247], v[24:27]\n"
    "ds_read_b128 a[148:151], v252 offset:18432\n"
    "v_cvt_pk_bf16_f32 v225, v46, v47\n"
    "v_mfma_f32_16x16x32_bf16 v[32:35], a[124:127], a[180:183], v[32:35]\n"
    "v_exp_f32 v31, v31\n"
    "v_mfma_f32_16x16x32_bf16 v[8:11], a[120:123], a[200:203], v[8:11]\n"
    "buffer_load_dwordx4 v1, s[16:19], s83 offen offset:0 sc1 lds\n"
    "v_mfma_f32_16x16x32_bf16 v[16:19], a[120:123], a[224:227], v[16:19]\n"
    "v_exp_f32 v23, v23\n"
    "v_mfma_f32_16x16x32_bf16 v[8:11], a[124:127], a[204:207], v[8:11]\n"
    "v_exp_f32 v13, v13\n"
    "v_mfma_f32_16x16x32_bf16 v[32:35], a[120:123], a[176:179], v[32:35]\n"
    "ds_read_b128 a[152:155], v252 offset:20480\n"
    "ds_read_b128 a[144:147], v252 offset:16384\n"
    "v_mfma_f32_16x16x32_bf16 v[16:19], a[116:119], a[220:223], v[16:19]\n"
    "v_exp_f32 v30, v30\n"
    "v_mfma_f32_16x16x32_bf16 v[24:27], a[120:123], a[248:251], v[24:27]\n"
    "s_waitcnt lgkmcnt(3)\n"
    "v_exp_f32 v12, v12\n"
    "v_mfma_f32_16x16x32_bf16 v[44:47], a[136:139], a[240:243], v[104:107]\n"
    "ds_read_b128 a[140:143], v252 offset:14336\n"
    "v_cvt_pk_bf16_f32 v12, v12, v13\n"
    "v_mfma_f32_16x16x32_bf16 v[24:27], a[124:127], a[252:255], v[24:27]\n"
    "v_cvt_pk_bf16_f32 v4, v4, v5\n"
    "v_cvt_pk_bf16_f32 v218, v56, v57\n"
    "v_mfma_f32_16x16x32_bf16 v[32:35], a[116:119], a[172:175], v[32:35]\n"
    "v_exp_f32 v14, v14\n"
    "v_mfma_f32_16x16x32_bf16 v[16:19], a[112:115], a[216:219], v[16:19]\n"
    "v_exp_f32 v28, v28\n"
    "v_mfma_f32_16x16x32_bf16 v[8:11], a[128:131], a[208:211], v[8:11]\n"
    "v_exp_f32 v7, v7\n"
    "v_mfma_f32_16x16x32_bf16 v[32:35], a[112:115], a[168:171], v[32:35]\n"
    "v_exp_f32 v20, v20\n"
    "v_mfma_f32_16x16x32_bf16 v[24:27], a[128:131], a[160:163], v[24:27]\n"
    "ds_read_b128 a[112:115], v252 offset:13312\n"
    "ds_read_b128 a[128:131], v252 offset:21504\n"
    "v_mfma_f32_16x16x32_bf16 v[24:27], a[132:135], a[164:167], v[24:27]\n"
    "ds_read_b128 a[116:119], v252 offset:15360\n"
    "v_cvt_pk_bf16_f32 v227, v62, v63\n"
    "v_mfma_f32_16x16x32_bf16 v[36:39], a[136:139], a[192:195], v[104:107]\n"
    "v_exp_f32 v32, v32\n"
    "v_mfma_f32_16x16x32_bf16 v[8:11], a[132:135], a[212:215], v[8:11]\n"
    "ds_read_b128 a[132:135], v252 offset:23552\n"
    "ds_read_b128 a[124:127], v252 offset:19456\n"
    "v_mfma_f32_16x16x32_bf16 v[40:43], a[156:159], a[236:239], v[104:107]\n"
    "s_waitcnt lgkmcnt(5)\n"
    "v_exp_f32 v26, v26\n"
    "v_mfma_f32_16x16x32_bf16 v[44:47], a[140:143], a[244:247], v[44:47]\n"
    "v_exp_f32 v35, v35\n"
    "v_mfma_f32_16x16x32_bf16 v[44:47], a[144:147], a[248:251], v[44:47]\n"
    "v_cvt_pk_bf16_f32 v13, v14, v15\n"
    "v_cvt_pk_bf16_f32 v20, v20, v21\n"
    "v_mfma_f32_16x16x32_bf16 v[44:47], a[148:151], a[252:255], v[44:47]\n"
    "s_add_u32 s59, s59, 4\n"
    "ds_read_b128 a[120:123], v252 offset:17408\n"
    "ds_read_b128 v[100:103], v250 offset:1680\n"
    "v_mfma_f32_16x16x32_bf16 v[36:39], a[140:143], a[196:199], v[36:39]\n"
    "v_exp_f32 v33, v33\n"
    "v_mfma_f32_16x16x32_bf16 v[44:47], a[152:155], a[160:163], v[44:47]\n"
    "v_cvt_pk_bf16_f32 v5, v6, v7\n"
    "v_cvt_pk_bf16_f32 v219, v58, v59\n"
    "v_mfma_f32_16x16x32_bf16 v[36:39], a[144:147], a[200:203], v[36:39]\n"
    "v_exp_f32 v11, v11\n"
    "v_mfma_f32_16x16x32_bf16 v[48:51], a[156:159], a[188:191], v[104:107]\n"
    "v_exp_f32 v18, v18\n"
    "v_mfma_f32_16x16x32_bf16 v[40:43], a[152:155], a[232:235], v[40:43]\n"
    "v_exp_f32 v17, v17\n"
    "v_mfma_f32_16x16x32_bf16 v[36:39], a[148:151], a[204:207], v[36:39]\n"
    "v_exp_f32 v25, v25\n"
    "v_mfma_f32_16x16x32_bf16 v[48:51], a[152:155], a[184:187], v[48:51]\n"
    "v_exp_f32 v27, v27\n"
    "v_mfma_f32_16x16x32_bf16 v[48:51], a[148:151], a[180:183], v[48:51]\n"
    "s_waitcnt lgkmcnt(0)\n"
    "v_exp_f32 v22, v22\n"
    "v_mfma_f32_16x16x32_bf16 v[60:63], a[112:115], a[240:243], v[100:103]\n"
    "v_cvt_pk_bf16_f32 v21, v22, v23\n"
    "v_cvt_pk_bf16_f32 v23, v26, v27\n"
    "v_mfma_f32_16x16x32_bf16 v[56:59], a[132:135], a[236:239], v[100:103]\n"
    "s_waitcnt vmcnt(5)\n"
    "ds_add_u32 v247, v246\n"
    "v_mfma_f32_16x16x32_bf16 v[60:63], a[116:119], a[244:247], v[60:63]\n"
    "v_exp_f32 v29, v29\n"
    "v_mfma_f32_16x16x32_bf16 v[36:39], a[152:155], a[208:211], v[36:39]\n"
    "buffer_load_dwordx4 v1, s[16:19], s83 offen offset:1024 sc1 lds\n"
    "v_mfma_f32_16x16x32_bf16 v[56:59], a[128:131], a[232:235], v[56:59]\n"
    "v_exp_f32 v16, v16\n"
    "v_mfma_f32_16x16x32_bf16 v[36:39], a[156:159], a[212:215], v[36:39]\n"
    "v_exp_f32 v8, v8\n"
    "v_mfma_f32_16x16x32_bf16 v[44:47], a[156:159], a[164:167], v[44:47]\n"
    "v_exp_f32 v10, v10\n"
    "v_mfma_f32_16x16x32_bf16 v[60:63], a[120:123], a[248:251], v[60:63]\n"
    "v_exp_f32 v24, v24\n"
    "v_mfma_f32_16x16x32_bf16 v[60:63], a[124:127], a[252:255], v[60:63]\n"
    "v_cvt_pk_bf16_f32 v28, v28, v29\n"
    "v_cvt_pk_bf16_f32 v22, v24, v25\n"
    "v_mfma_f32_16x16x32_bf16 v[60:63], a[128:131], a[160:163], v[60:63]\n"
    "v_exp_f32 v34, v34\n"
    "v_mfma_f32_16x16x32_bf16 v[52:55], a[112:115], a[192:195], v[100:103]\n"
    "v_exp_f32 v9, v9\n"
    "v_mfma_f32_16x16x32_bf16 v[60:63], a[132:135], a[164:167], v[60:63]\n"
    "v_exp_f32 v38, v38\n"
    "v_mfma_f32_16x16x32_bf16 v[48:51], a[144:147], a[176:179], v[48:51]\n"
    "v_exp_f32 v37, v37\n"
    "v_mfma_f32_16x16x32_bf16 v[64:67], a[132:135], a[188:191], v[100:103]\n"
    "v_exp_f32 v45, v45\n"
    "v_mfma_f32_16x16x32_bf16 v[48:51], a[140:143], a[172:175], v[48:51]\n"
    "ds_read_b32 v245, v247\n"
    "ds_read_b128 v[104:107], v250 offset:2304\n"
    "v_mfma_f32_16x16x32_bf16 v[56:59], a[124:127], a[228:231], v[56:59]\n"
    "v_exp_f32 v39, v39\n"
    "v_mfma_f32_16x16x32_bf16 v[40:43], a[148:151], a[228:231], v[40:43]\n"
    "v_exp_f32 v63, v63\n"
    "v_mfma_f32_16x16x32_bf16 v[40:43], a[144:147], a[224:227], v[40:43]\n"
    "v_exp_f32 v47, v47\n"
    "v_smfmac_f32_16x16x64_bf16 v[120:123], v[240:243], v[228:235], v253\n"
    "v_cvt_pk_bf16_f32 v6, v8, v9\n"
    "v_mfma_f32_16x16x32_bf16 v[52:55], a[116:119], a[196:199], v[52:55]\n"
    "v_exp_f32 v46, v46\n"
    "v_mfma_f32_16x16x32_bf16 v[64:67], a[128:131], a[184:187], v[64:67]\n"
    "v_cvt_pk_bf16_f32 v25, v46, v47\n"
    "v_cvt_pk_bf16_f32 v29, v30, v31\n"
    "v_mfma_f32_16x16x32_bf16 v[64:67], a[124:127], a[180:183], v[64:67]\n"
    "v_exp_f32 v62, v62\n"
    "v_mfma_f32_16x16x32_bf16 v[40:43], a[140:143], a[220:223], v[40:43]\n"
    "s_waitcnt lgkmcnt(1)\n"
    "v_cmp_le_u32 s[56:57], s59, v245\n"
    "v_cvt_pk_bf16_f32 v9, v38, v39\n"
    "v_smfmac_f32_16x16x64_bf16 v[112:115], v[240:243], v[212:219], v253\n"
    "s_cmp_eq_u64 s[56:57], -1\n"
    "v_cvt_pk_bf16_f32 v27, v62, v63\n"
    "v_mfma_f32_16x16x32_bf16 v[52:55], a[120:123], a[200:203], v[52:55]\n"
    "v_exp_f32 v36, v36\n"
    "v_smfmac_f32_16x16x64_bf16 v[116:119], v[240:243], v[220:227], v253\n"
    "s_cbranch_scc0 .Lpt2\n"
    ".Lpt2r:\n"
    "v_mfma_f32_16x16x32_bf16 v[56:59], a[120:123], a[224:227], v[56:59]\n"
    "s_add_u32 m0, s65, 0\n"
    "ds_read_b128 a[152:155], v252 offset:32768\n"
    "ds_read_b128 a[156:159], v252 offset:34816\n"
    "v_mfma_f32_16x16x32_bf16 v[56:59], a[116:119], a[220:223], v[56:59]\n"
    "v_exp_f32 v60, v60\n"
    "v_mfma_f32_16x16x32_bf16 v[52:55], a[124:127], a[204:207], v[52:55]\n"
    "v_cvt_pk_bf16_f32 v8, v36, v37\n"
    "v_cvt_pk_bf16_f32 v31, v34, v35\n"
    "v_mfma_f32_16x16x32_bf16 v[64:67], a[120:123], a[176:179], v[64:67]\n"
    "s_add_u32 s83, s67, 122880\n"
    "ds_read_b128 a[148:151], v252 offset:30720\n"
    "ds_read_b128 a[140:143], v252 offset:26624\n"
    "v_mfma_f32_16x16x32_bf16 v[40:43], a[136:139], a[216:219], v[40:43]\n"
    "v_exp_f32 v61, v61\n"
    "v_mfma_f32_16x16x32_bf16 v[52:55], a[128:131], a[208:211], v[52:55]\n"
    "buffer_load_dwordx4 v1, s[16:19], s83 offen offset:0 sc1 lds\n"
    "v_mfma_f32_16x16x32_bf16 v[52:55], a[132:135], a[212:215], v[52:55]\n"
    "v_exp_f32 v44, v44\n"
    "v_mfma_f32_16x16x32_bf16 v[48:51], a[136:139], a[168:171], v[48:51]\n"
    "ds_read_b128 v[100:103], v250 offset:2320\n"
    "ds_read_b128 a[144:147], v252 offset:28672\n"
    "v_mfma_f32_16x16x32_bf16 v[64:67], a[116:119], a[172:175], v[64:67]\n"
    "s_waitcnt lgkmcnt(4)\n"
    "ds_read_b128 a[136:139], v252 offset:24576\n"
    "v_cvt_pk_bf16_f32 v24, v44, v45\n"
    "v_mfma_f32_16x16x32_bf16 v[44:47], a[156:159], a[236:239], v[104:107]\n"
    "buffer_load_dwordx4 v1, s[16:19], s83 offen offset:1024 sc1 lds\n"
    "v_mfma_f32_16x16x32_bf16 v[56:59], a[112:115], a[216:219], v[56:59]\n"
    "ds_read_b128 a[120:123], v252 offset:29696\n"
    "v_cvt_pk_bf16_f32 v26, v60, v61\n"
    "v_mfma_f32_16x16x32_bf16 v[60:63], a[156:159], a[188:191], v[104:107]\n"
    "buffer_load_dwordx4 v1, s[16:19], s83 offen offset:2048 sc1 lds\n"
    "v_mfma_f32_16x16x32_bf16 v[44:47], a[152:155], a[232:235], v[44:47]\n"
    "v_exp_f32 v54, v54\n"
    "v_smfmac_f32_16x16x64_bf16 v[108:111], v[240:243], v[204:211], v253\n"
    "s_waitcnt lgkmcnt(5)\n"
    "ds_read_b128 a[132:135], v252 offset:35840\n"
    "v_mfma_f32_16x16x32_bf16 v[44:47], a[148:151], a[228:231], v[44:47]\n"
    "ds_read_b128 a[124:127], v252 offset:31744\n"
    "ds_read_b128 a[116:119], v252 offset:27648\n"
    "v_mfma_f32_16x16x32_bf16 v[64:67], a[112:115], a[168:171], v[64:67]\n"
    "v_exp_f32 v53, v53\n"
    "v_mfma_f32_16x16x32_bf16 v[60:63], a[152:155], a[184:187], v[60:63]\n"
    "buffer_load_dwordx4 v1, s[16:19], s83 offen offset:3072 sc1 lds\n"
    "v_mfma_f32_16x16x32_bf16 v[60:63], a[148:151], a[180:183], v[60:63]\n"
    "s_waitcnt lgkmcnt(4)\n"
    "v_exp_f32 v42, v42\n"
    "v_mfma_f32_16x16x32_bf16 v[44:47], a[144:147], a[224:227], v[44:47]\n"
    "s_add_u32 m0, s65, 4096\n"
    "v_exp_f32 v52, v52\n"
    "v_mfma_f32_16x16x32_bf16 v[36:39], a[136:139], a[192:195], v[104:107]\n"
    "ds_read_b128 v[240:243], v251 offset:1536\n"
    "v_cvt_pk_bf16_f32 v14, v16, v17\n"
    "v_mfma_f32_16x16x32_bf16 v[60:63], a[144:147], a[176:179], v[60:63]\n"
    "v_exp_f32 v43, v43\n"
    "v_mfma_f32_16x16x32_bf16 v[36:39], a[140:143], a[196:199], v[36:39]\n"
    "s_add_u32 s83, s67, 126976\n"
    "buffer_load_dwordx4 v1, s[16:19], s83 offen offset:0 sc1 lds\n"
    "v_mfma_f32_16x16x32_bf16 v[44:47], a[140:143], a[220:223], v[44:47]\n"
    ".Lwp1:\n"
    "s_cbranch_execz .Lwp3\n"
    "v_exp_f32 v55, v55\n"
    "v_mfma_f32_16x16x32_bf16 v[60:63], a[140:143], a[172:175], v[60:63]\n"
    "ds_read_b128 a[112:115], v252 offset:25600\n"
    "v_cvt_pk_bf16_f32 v7, v10, v11\n"
    "v_mfma_f32_16x16x32_bf16 v[60:63], a[136:139], a[168:171], v[60:63]\n"
    "v_cvt_pk_bf16_f32 v11, v54, v55\n"
    "v_cvt_pk_bf16_f32 v10, v52, v53\n"
    "v_mfma_f32_16x16x32_bf16 v[52:55], a[136:139], a[240:243], v[104:107]\n"
    "buffer_load_dwordx4 v1, s[16:19], s83 offen offset:1024 sc1 lds\n"
    "v_mfma_f32_16x16x32_bf16 v[52:55], a[140:143], a[244:247], v[52:55]\n"
    "ds_read_b128 a[128:131], v252 offset:33792\n"
    "ds_read_b128 a[140:143], v249 offset:12288\n"
    "v_mfma_f32_16x16x32_bf16 v[36:39], a[144:147], a[200:203], v[36:39]\n"
    "v_exp_f32 v19, v19\n"
    "v_mfma_f32_16x16x32_bf16 v[36:39], a[148:151], a[204:207], v[36:39]\n"
    "v_exp_f32 v58, v58\n"
    "v_mfma_f32_16x16x32_bf16 v[44:47], a[136:139], a[216:219], v[44:47]\n"
    "v_exp_f32 v41, v41\n"
    "v_mfma_f32_16x16x32_bf16 v[52:55], a[144:147], a[248:251], v[52:55]\n"
    "v_exp_f32 v61, v61\n"
    "v_mfma_f32_16x16x32_bf16 v[36:39], a[152:155], a[208:211], v[36:39]\n"
    "s_waitcnt lgkmcnt(3)\n"
    "v_exp_f32 v40, v40\n"
    "v_smfmac_f32_16x16x64_bf16 v[116:119], v[240:243], v[20:27], v253\n"
    "v_cvt_pk_bf16_f32 v17, v42, v43\n"
    "v_mfma_f32_16x16x32_bf16 v[52:55], a[148:151], a[252:255], v[52:55]\n"
    "v_exp_f32 v59, v59\n"
    "v_mfma_f32_16x16x32_bf16 v[52:55], a[152:155], a[160:163], v[52:55]\n"
    "ds_read_b128 a[136:139], v249 offset:0\n"
    "v_cvt_pk_bf16_f32 v16, v40, v41\n"
    "v_mfma_f32_16x16x32_bf16 v[52:55], a[156:159], a[164:167], v[52:55]\n"
    "s_waitcnt lgkmcnt(3)\n"
    "ds_read_b128 a[144:147], v249 offset:1024\n"
    "v_cvt_pk_bf16_f32 v30, v32, v33\n"
    "v_mfma_f32_16x16x32_bf16 v[40:43], a[112:115], a[192:195], v[100:103]\n"
    "v_exp_f32 v65, v65\n"
    "v_mfma_f32_16x16x32_bf16 v[36:39], a[156:159], a[212:215], v[36:39]\n"
    "v_exp_f32 v47, v47\n"
    "v_mfma_f32_16x16x32_bf16 v[40:43], a[116:119], a[196:199], v[40:43]\n"
    "s_waitcnt lgkmcnt(2)\n"
    "ds_read_b128 a[156:159], v249 offset:14336\n"
    "ds_read_b128 a[152:155], v249 offset:2048\n"
    "v_mfma_f32_16x16x32_bf16 v[140:143], a[140:143], v[216:219], v[140:143]\n"
    "ds_read_b128 a[148:151], v249 offset:13312\n"
    "v_cvt_pk_bf16_f32 v15, v18, v19\n"
    "v_mfma_f32_16x16x32_bf16 v[40:43], a[120:123], a[200:203], v[40:43]\n"
    "v_exp_f32 v57, v57\n"
    "v_mfma_f32_16x16x32_bf16 v[40:43], a[124:127], a[204:207], v[40:43]\n"
    "v_exp_f32 v56, v56\n"
    "v_mfma_f32_16x16x32_bf16 v[40:43], a[128:131], a[208:211], v[40:43]\n"
    "v_exp_f32 v64, v64\n"
    "v_mfma_f32_16x16x32_bf16 v[172:175], a[140:143], v[232:235], v[172:175]\n"
    "s_waitcnt lgkmcnt(4)\n"
    "v_exp_f32 v51, v51\n"
    "v_mfma_f32_16x16x32_bf16 v[140:143], a[136:139], v[212:215], v[140:143]\n"
    "v_exp_f32 v39, v39\n"
    "v_mfma_f32_16x16x32_bf16 v[172:175], a[136:139], v[228:231], v[172:175]\n"
    "v_exp_f32 v67, v67\n"
    "v_mfma_f32_16x16x32_bf16 v[40:43], a[132:135], a[212:215], v[40:43]\n"
    "s_waitcnt lgkmcnt(2)\n"
    "v_exp_f32 v60, v60\n"
    "v_mfma_f32_16x16x32_bf16 v[148:151], a[156:159], v[216:219], v[148:151]\n"
    "v_exp_f32 v66, v66\n"
    "v_mfma_f32_16x16x32_bf16 v[124:127], a[136:139], v[204:207], v[124:127]\n"
    "v_cvt_pk_bf16_f32 v19, v58, v59\n"
    "v_cvt_pk_bf16_f32 v60, v60, v61\n"
    "v_mfma_f32_16x16x32_bf16 v[180:183], a[156:159], v[232:235], v[180:183]\n"
    "s_waitcnt lgkmcnt(0)\n"
    "v_cvt_pk_bf16_f32 v35, v66, v67\n"
    "v_cvt_pk_bf16_f32 v18, v56, v57\n"
    "v_mfma_f32_16x16x32_bf16 v[128:131], a[148:151], v[208:211], v[128:131]\n"
    "v_exp_f32 v46, v46\n"
    "v_mfma_f32_16x16x32_bf16 v[56:59], a[112:115], a[240:243], v[100:103]\n"
    "v_exp_f32 v45, v45\n"
    "v_mfma_f32_16x16x32_bf16 v[132:135], a[152:155], v[204:207], v[132:135]\n"
    "v_exp_f32 v48, v48\n"
    "v_mfma_f32_16x16x32_bf16 v[148:151], a[152:155], v[212:215], v[148:151]\n"
    "v_exp_f32 v49, v49\n"
    "v_mfma_f32_16x16x32_bf16 v[164:167], a[152:155], v[220:223], v[164:167]\n"
    "v_exp_f32 v37, v37\n"
    "v_mfma_f32_16x16x32_bf16 v[156:159], a[136:139], v[220:223], v[156:159]\n"
    "v_cvt_pk_bf16_f32 v34, v64, v65\n"
    "v_cmp_eq_u64 s[54:55], v[236:237], v[238:239]\n"
    "v_mfma_f32_16x16x32_bf16 v[56:59], a[116:119], a[244:247], v[56:59]\n"
    "v_exp_f32 v50, v50\n"
    "v_mfma_f32_16x16x32_bf16 v[56:59], a[120:123], a[248:251], v[56:59]\n"
    "v_exp_f32 v55, v55\n"
    "v_mfma_f32_16x16x32_bf16 v[124:127], a[140:143], v[208:211], v[124:127]\n"
    "ds_read_b128 v[104:107], v250 offset:2448\n"
    "ds_read_b128 a[136:139], v252 offset:37888\n"
    "v_mfma_f32_16x16x32_bf16 v[64:67], a[132:135], a[188:191], v[100:103]\n"
    "v_exp_f32 v42, v42\n"
    "v_mfma_f32_16x16x32_bf16 v[56:59], a[124:127], a[252:255], v[56:59]\n"
    "v_cvt_pk_bf16_f32 v32, v48, v49\n"
    "v_cvt_pk_bf16_f32 v33, v50, v51\n"
    "v_mfma_f32_16x16x32_bf16 v[64:67], a[128:131], a[184:187], v[64:67]\n"
    "v_exp_f32 v36, v36\n"
    "v_mfma_f32_16x16x32_bf16 v[160:163], a[148:151], v[224:227], v[160:163]\n"
    "v_exp_f32 v38, v38\n"
    "v_mfma_f32_16x16x32_bf16 v[48:51], a[132:135], a[236:239], v[100:103]\n"
    "v_exp_f32 v43, v43\n"
    "v_mfma_f32_16x16x32_bf16 v[56:59], a[128:131], a[160:163], v[56:59]\n"
    "v_exp_f32 v52, v52\n"
    "v_mfma_f32_16x16x32_bf16 v[64:67], a[124:127], a[180:183], v[64:67]\n"
    "v_exp_f32 v44, v44\n"
    "v_mfma_f32_16x16x32_bf16 v[48:51], a[128:131], a[232:235], v[48:51]\n"
    "v_exp_f32 v63, v63\n"
    "v_mfma_f32_16x16x32_bf16 v[160:163], a[144:147], v[220:223], v[160:163]\n"
    "v_exp_f32 v53, v53\n"
    "v_mfma_f32_16x16x32_bf16 v[56:59], a[132:135], a[164:167], v[56:59]\n"
    "v_exp_f32 v40, v40\n"
    "v_mfma_f32_16x16x32_bf16 v[48:51], a[124:127], a[228:231], v[48:51]\n"
    "ds_read_b128 a[132:135], v252 offset:47104\n"
    "v_cvt_pk_bf16_f32 v44, v44, v45\n"
    "v_mfma_f32_16x16x32_bf16 v[64:67], a[120:123], a[176:179], v[64:67]\n"
    "v_cvt_pk_bf16_f32 v52, v52, v53\n"
    "v_cvt_pk_bf16_f32 v36, v36, v37\n"
    "v_mfma_f32_16x16x32_bf16 v[48:51], a[120:123], a[224:227], v[48:51]\n"
    "v_exp_f32 v41, v41\n"
    "v_smfmac_f32_16x16x64_bf16 v[120:123], v[240:243], v[28:35], v253\n"
    "ds_read_b128 a[120:123], v252 offset:40960\n"
    "v_mfma_f32_16x16x32_bf16 v[180:183], a[152:155], v[228:231], v[180:183]\n"
    "ds_read_b128 a[124:127], v252 offset:43008\n"
    "v_cvt_pk_bf16_f32 v37, v38, v39\n"
    "v_mfma_f32_16x16x32_bf16 v[48:51], a[116:119], a[220:223], v[48:51]\n"
    "v_exp_f32 v54, v54\n"
    "v_mfma_f32_16x16x32_bf16 v[64:67], a[116:119], a[172:175], v[64:67]\n"
    "v_exp_f32 v58, v58\n"
    "v_mfma_f32_16x16x32_bf16 v[48:51], a[112:115], a[216:219], v[48:51]\n"
    "v_exp_f32 v56, v56\n"
    "v_mfma_f32_16x16x32_bf16 v[64:67], a[112:115], a[168:171], v[64:67]\n"
    "ds_read_b128 v[100:103], v250 offset:2432\n"
    "ds_read_b128 a[128:131], v252 offset:45056\n"
    "v_smfmac_f32_16x16x64_bf16 v[108:111], v[240:243], v[4:11], v253\n"
    "v_cvt_pk_bf16_f32 v39, v42, v43\n"
    "v_mfma_f32_16x16x32_bf16 v[128:131], a[144:147], v[204:207], v[128:131]\n"
    "ds_read_b128 a[112:115], v252 offset:36864\n"
    "v_cvt_pk_bf16_f32 v45, v46, v47\n"
    "v_mfma_f32_16x16x32_bf16 v[156:159], a[140:143], v[224:227], v[156:159]\n"
    "ds_read_b128 a[152:155], v252 offset:46080\n"
    "ds_read_b128 a[116:119], v252 offset:38912\n"
    "v_mfma_f32_16x16x32_bf16 v[176:179], a[144:147], v[228:231], v[176:179]\n"
    "v_exp_f32 v50, v50\n"
    "v_mfma_f32_16x16x32_bf16 v[144:147], a[144:147], v[212:215], v[144:147]\n"
    "ds_read_b128 a[140:143], v252 offset:39936\n"
    "ds_read_b128 a[144:147], v252 offset:41984\n"
    "v_mfma_f32_16x16x32_bf16 v[176:179], a[148:151], v[232:235], v[176:179]\n"
    "v_exp_f32 v51, v51\n"
    "v_mfma_f32_16x16x32_bf16 v[132:135], a[156:159], v[208:211], v[132:135]\n"
    "v_exp_f32 v67, v67\n"
    "v_mfma_f32_16x16x32_bf16 v[144:147], a[148:151], v[216:219], v[144:147]\n"
    "s_waitcnt lgkmcnt(10)\n"
    "v_exp_f32 v62, v62\n"
    "v_mfma_f32_16x16x32_bf16 v[236:239], a[136:139], a[192:195], v[104:107]\n"
    "v_exp_f32 v49, v49\n"
    "v_mfma_f32_16x16x32_bf16 v[164:167], a[156:159], v[224:227], v[164:167]\n"
    "ds_read_b128 a[148:151], v252 offset:44032\n"
    "ds_read_b128 a[156:159], v252 offset:48128\n"
    "v_smfmac_f32_16x16x64_bf16 v[112:115], v[240:243], v[12:19], v253\n"
    "s_waitcnt lgkmcnt(6)\n"
    "v_cvt_pk_bf16_f32 v38, v40, v41\n"
    "v_mfma_f32_16x16x32_bf16 v[40:43], a[112:115], a[192:195], v[100:103]\n"
    "s_waitcnt lgkmcnt(4)\n"
    "v_exp_f32 v59, v59\n"
    "v_mfma_f32_16x16x32_bf16 v[40:43], a[116:119], a[196:199], v[40:43]\n"
    "v_exp_f32 v64, v64\n"
    "v_mfma_f32_16x16x32_bf16 v[40:43], a[120:123], a[200:203], v[40:43]\n"
    "v_exp_f32 v66, v66\n"
    "v_mfma_f32_16x16x32_bf16 v[40:43], a[124:127], a[204:207], v[40:43]\n"
    "s_waitcnt lgkmcnt(3)\n"
    "v_exp_f32 v48, v48\n"
    "v_mfma_f32_16x16x32_bf16 v[236:239], a[140:143], a[196:199], v[236:239]\n"
    "s_or_b64 s[34:35], s[34:35], s[54:55]\n"
    "v_cvt_pk_bf16_f32 v46, v48, v49\n"
    "v_cvt_pk_bf16_f32 v53, v54, v55\n"
    "v_mfma_f32_16x16x32_bf16 v[40:43], a[128:131], a[208:211], v[40:43]\n"
    "s_waitcnt vmcnt(0)\n"
    "ds_add_u32 v247, v246\n"
    "v_mfma_f32_16x16x32_bf16 v[40:43], a[132:135], a[212:215], v[40:43]\n"
    "s_waitcnt lgkmcnt(3)\n"
    "v_cvt_pk_bf16_f32 v55, v58, v59\n"
    "v_cvt_pk_bf16_f32 v61, v62, v63\n"
    "v_mfma_f32_16x16x32_bf16 v[236:239], a[144:147], a[200:203], v[236:239]\n"
    "s_waitcnt lgkmcnt(2)\n"
    "v_exp_f32 v57, v57\n"
    "v_mfma_f32_16x16x32_bf16 v[236:239], a[148:151], a[204:207], v[236:239]\n"
    "v_cvt_pk_bf16_f32 v54, v56, v57\n"
    "v_cvt_pk_bf16_f32 v47, v50, v51\n"
    "v_mfma_f32_16x16x32_bf16 v[48:51], a[132:135], a[236:239], v[100:103]\n"
    "v_exp_f32 v41, v41\n"
    "v_mfma_f32_16x16x32_bf16 v[48:51], a[128:131], a[232:235], v[48:51]\n"
    "v_exp_f32 v42, v42\n"
    "v_mfma_f32_16x16x32_bf16 v[48:51], a[124:127], a[228:231], v[48:51]\n"
    "v_exp_f32 v43, v43\n"
    "v_mfma_f32_16x16x32_bf16 v[56:59], a[112:115], a[240:243], v[100:103]\n"
    "ds_read_b128 a[204:207], v249 offset:3072\n"
    "ds_read_b128 a[192:195], v248 offset:39936\n"
    "v_mfma_f32_16x16x32_bf16 v[236:239], a[152:155], a[208:211], v[236:239]\n"
    "v_subrev_u32 v246, 49152, v248\n"
    "v_cvt_pk_bf16_f32 v63, v66, v67\n"
    "v_mfma_f32_16x16x32_bf16 v[48:51], a[120:123], a[224:227], v[48:51]\n"
    "v_exp_f32 v40, v40\n"
    "v_mfma_f32_16x16x32_bf16 v[48:51], a[116:119], a[220:223], v[48:51]\n"
    "v_exp_f32 v65, v65\n"
    "v_mfma_f32_16x16x32_bf16 v[48:51], a[112:115], a[216:219], v[48:51]\n"
    "s_waitcnt lgkmcnt(3)\n"
    "ds_read_b128 v[240:243], v251 offset:2304\n"
    "ds_read_b128 a[196:199], v248 offset:27648\n"
    "v_mfma_f32_16x16x32_bf16 v[236:239], a[156:159], a[212:215], v[236:239]\n"
    "v_cvt_pk_bf16_f32 v40, v40, v41\n"
    "v_cvt_pk_bf16_f32 v62, v64, v65\n"
    "v_mfma_f32_16x16x32_bf16 v[64:67], a[156:159], a[236:239], v[104:107]\n"
    "s_add_u32 s59, s59, 4\n"
    "ds_read_b32 v245, v247\n"
    "v_cvt_pk_bf16_f32 v41, v42, v43\n"
    "v_mfma_f32_16x16x32_bf16 v[64:67], a[152:155], a[232:235], v[64:67]\n"
    "s_nop 0\n"
    "v_exp_f32 v238, v238\n"
    "v_mfma_f32_16x16x32_bf16 v[64:67], a[148:151], a[228:231], v[64:67]\n"
    "v_exp_f32 v51, v51\n"
    "v_mfma_f32_16x16x32_bf16 v[64:67], a[144:147], a[224:227], v[64:67]\n"
    "v_exp_f32 v49, v49\n"
    "v_mfma_f32_16x16x32_bf16 v[64:67], a[140:143], a[220:223], v[64:67]\n"
    "v_exp_f32 v236, v236\n"
    "v_mfma_f32_16x16x32_bf16 v[64:67], a[136:139], a[216:219], v[64:67]\n"
    "v_exp_f32 v48, v48\n"
    "v_mfma_f32_16x16x32_bf16 v[56:59], a[116:119], a[244:247], v[56:59]\n"
    "s_waitcnt lgkmcnt(3)\n"
    "v_exp_f32 v239, v239\n"
    "v_mfma_f32_16x16x32_bf16 v[152:155], a[192:195], v[80:83], v[152:155]\n"
    "v_exp_f32 v237, v237\n"
    "v_mfma_f32_16x16x32_bf16 v[136:139], a[192:195], v[72:75], v[136:139]\n"
    "s_waitcnt lgkmcnt(0)\n"
    "v_cmp_le_u32 s[56:57], s59, v245\n"
    "v_cvt_pk_bf16_f32 v42, v236, v237\n"
    "v_mfma_f32_16x16x32_bf16 v[56:59], a[120:123], a[248:251], v[56:59]\n"
    "s_cmp_eq_u64 s[56:57], -1\n"
    "v_cvt_pk_bf16_f32 v48, v48, v49\n"
    "v_cvt_pk_bf16_f32 v43, v238, v239\n"
    "v_mfma_f32_16x16x32_bf16 v[236:239], a[136:139], a[240:243], v[104:107]\n"
    "v_exp_f32 v50, v50\n"
    "v_mfma_f32_16x16x32_bf16 v[236:239], a[140:143], a[244:247], v[236:239]\n"
    "v_exp_f32 v65, v65\n"
    "v_mfma_f32_16x16x32_bf16 v[56:59], a[124:127], a[252:255], v[56:59]\n"
    "v_exp_f32 v67, v67\n"
    "v_mfma_f32_16x16x32_bf16 v[56:59], a[128:131], a[160:163], v[56:59]\n"
    "v_cvt_pk_bf16_f32 v49, v50, v51\n"
    "v_mov_b32 v250, 0\n"
    "v_mfma_f32_16x16x32_bf16 v[152:155], a[196:199], v[76:79], v[152:155]\n"
    "v_exp_f32 v66, v66\n"
    "v_smfmac_f32_16x16x64_bf16 v[108:111], v[240:243], v[36:43], v253\n"
    "s_cbranch_scc0 .Lpt3\n"
    ".Lpt3r:\n"
    "v_mfma_f32_16x16x32_bf16 v[236:239], a[144:147], a[248:251], v[236:239]\n"
    "v_exp_f32 v64, v64\n"
    "v_mfma_f32_16x16x32_bf16 v[236:239], a[148:151], a[252:255], v[236:239]\n"
    "ds_read_b128 a[200:203], v249 offset:15360\n"
    "ds_read_b128 a[228:231], v248 offset:2048\n"
    "v_mfma_f32_16x16x32_bf16 v[136:139], a[196:199], v[68:71], v[136:139]\n"
    "v_cvt_pk_bf16_f32 v51, v66, v67\n"
    "v_add_f32 v108, v108, v109\n"
    "v_mfma_f32_16x16x32_bf16 v[236:239], a[152:155], a[160:163], v[236:239]\n"
    "v_cvt_pk_bf16_f32 v50, v64, v65\n"
    "v_mov_b32 v109, v108\n"
    "v_mfma_f32_16x16x32_bf16 v[104:107], a[156:159], a[188:191], v[104:107]\n"
    "ds_read_b128 a[208:211], v248 offset:0\n"
    "ds_read_b128 a[236:239], v248 offset:13312\n"
    "v_smfmac_f32_16x16x64_bf16 v[112:115], v[240:243], v[44:51], v253\n"
    "ds_read_b128 a[212:215], v248 offset:12288\n"
    "v_mfma_f32_16x16x32_bf16 v[56:59], a[132:135], a[164:167], v[56:59]\n"
    "v_permlane16_swap_b32 v108, v109\n"
    "v_mfma_f32_16x16x32_bf16 v[236:239], a[156:159], a[164:167], v[236:239]\n"
    "ds_read_b128 a[216:219], v248 offset:3072\n"
    "ds_read_b128 a[244:247], v246 offset:12288\n"
    "v_mfma_f32_16x16x32_bf16 v[64:67], a[132:135], a[188:191], v[100:103]\n"
    "s_nop 0\n"
    "v_add_f32 v112, v112, v113\n"
    "v_lshrrev_b32 v252, 3, v1\n"
    "v_mfma_f32_16x16x32_bf16 v[64:67], a[128:131], a[184:187], v[64:67]\n"
    "v_exp_f32 v56, v56\n"
    "v_mfma_f32_16x16x32_bf16 v[64:67], a[124:127], a[180:183], v[64:67]\n"
    "s_waitcnt lgkmcnt(5)\n"
    "ds_read_b128 a[232:235], v248 offset:1024\n"
    "ds_read_b128 a[128:131], v248 offset:31744\n"
    "v_mfma_f32_16x16x32_bf16 v[132:135], a[228:231], v[4:7], v[132:135]\n"
    "ds_read_b128 a[252:255], v246 offset:1024\n"
    "v_add_f32 v108, v108, v109\n"
    "v_mfma_f32_16x16x32_bf16 v[64:67], a[120:123], a[176:179], v[64:67]\n"
    "s_waitcnt lgkmcnt(7)\n"
    "v_exp_f32 v58, v58\n"
    "v_mfma_f32_16x16x32_bf16 v[140:143], a[208:211], v[12:15], v[140:143]\n"
    "ds_read_b128 a[220:223], v248 offset:15360\n"
    "v_mov_b32 v115, v114\n"
    "v_mfma_f32_16x16x32_bf16 v[104:107], a[152:155], a[184:187], v[104:107]\n"
    "ds_read_b128 a[248:251], v246 offset:13312\n"
    "ds_read_b128 a[156:159], v246 offset:15360\n"
    "v_mfma_f32_16x16x32_bf16 v[136:139], a[200:203], v[208:211], v[136:139]\n"
    "v_exp_f32 v57, v57\n"
    "v_mfma_f32_16x16x32_bf16 v[136:139], a[204:207], v[204:207], v[136:139]\n"
    "s_waitcnt lgkmcnt(9)\n"
    "v_exp_f32 v238, v238\n"
    "v_mfma_f32_16x16x32_bf16 v[144:147], a[236:239], v[16:19], v[144:147]\n"
    "ds_read_b128 a[240:243], v246 offset:0\n"
    "ds_read_b128 a[224:227], v248 offset:14336\n"
    "v_mfma_f32_16x16x32_bf16 v[124:127], a[208:211], v[4:7], v[124:127]\n"
    "v_permlane16_swap_b32 v114, v115\n"
    "v_mfma_f32_16x16x32_bf16 v[128:131], a[236:239], v[8:11], v[128:131]\n"
    "v_exp_f32 v236, v236\n"
    "v_mfma_f32_16x16x32_bf16 v[152:155], a[200:203], v[216:219], v[152:155]\n"
    "v_exp_f32 v59, v59\n"
    "v_mfma_f32_16x16x32_bf16 v[148:151], a[228:231], v[12:15], v[148:151]\n"
    "ds_read_b128 a[160:163], v246 offset:2048\n"
    "ds_read_b128 a[152:155], v246 offset:3072\n"
    "v_mfma_f32_16x16x32_bf16 v[104:107], a[148:151], a[180:183], v[104:107]\n"
    "s_waitcnt lgkmcnt(6)\n"
    "v_exp_f32 v239, v239\n"
    "v_mfma_f32_16x16x32_bf16 v[136:139], a[220:223], v[8:11], v[136:139]\n"
    "v_and_b32 v252, 0x70, v252\n"
    "v_cvt_pk_bf16_f32 v56, v56, v57\n"
    "v_mfma_f32_16x16x32_bf16 v[136:139], a[216:219], v[4:7], v[136:139]\n"
    "v_mov_b32 v111, v110\n"
    "v_mov_b32 v113, v112\n"
    "v_mfma_f32_16x16x32_bf16 v[64:67], a[116:119], a[172:175], v[64:67]\n"
    "s_nop 0\n"
    "v_permlane16_swap_b32 v112, v113\n"
    "v_mfma_f32_16x16x32_bf16 v[128:131], a[232:235], v[4:7], v[128:131]\n"
    "v_rcp_f32 v109, v108\n"
    "v_mfma_f32_16x16x32_bf16 v[124:127], a[212:215], v[8:11], v[124:127]\n"
    "ds_read_b128 a[132:135], v248 offset:43008\n"
    "ds_read_b128 a[164:167], v246 offset:14336\n"
    "v_mfma_f32_16x16x32_bf16 v[64:67], a[112:115], a[168:171], v[64:67]\n"
    "s_waitcnt lgkmcnt(6)\n"
    "ds_read_b128 a[120:123], v249 offset:5120\n"
    "v_cvt_pk_bf16_f32 v57, v58, v59\n"
    "v_mfma_f32_16x16x32_bf16 v[136:139], a[156:159], v[40:43], v[136:139]\n"
    "v_add_f32 v112, v112, v113\n"
    "v_xor_b32 v3, 64, v2\n"
    "v_mfma_f32_16x16x32_bf16 v[104:107], a[144:147], a[176:179], v[104:107]\n"
    "v_exp_f32 v237, v237\n"
    "v_mfma_f32_16x16x32_bf16 v[128:131], a[248:251], v[40:43], v[128:131]\n"
    "s_waitcnt lgkmcnt(6)\n"
    "v_cvt_pk_bf16_f32 v59, v238, v239\n"
    "v_cvt_pk_bf16_f32 v58, v236, v237\n"
    "v_mfma_f32_16x16x32_bf16 v[124:127], a[240:243], v[36:39], v[124:127]\n"
    "v_permlane16_swap_b32 v110, v111\n"
    "v_smfmac_f32_16x16x64_bf16 v[116:119], v[240:243], v[52:59], v253\n"
    "s_waitcnt lgkmcnt(3)\n"
    "v_add_f32 v114, v114, v115\n"
    "v_mfma_f32_16x16x32_bf16 v[136:139], a[152:155], v[36:39], v[136:139]\n"
    "v_rcp_f32 v113, v112\n"
    "v_mfma_f32_16x16x32_bf16 v[128:131], a[252:255], v[36:39], v[128:131]\n"
    "v_xor_b32 v252, v252, v1\n"
    "v_mul_f32 v115, -v114, v113\n"
    "v_mfma_f32_16x16x32_bf16 v[140:143], a[212:215], v[16:19], v[140:143]\n"
    "v_mov_b32 v119, v118\n"
    "v_add_f32 v116, v116, v117\n"
    "v_mfma_f32_16x16x32_bf16 v[132:135], a[224:227], v[8:11], v[132:135]\n"
    "v_exp_f32 v66, v66\n"
    "v_mfma_f32_16x16x32_bf16 v[132:135], a[160:163], v[36:39], v[132:135]\n"
    "s_waitcnt lgkmcnt(1)\n"
    "v_add_f32 v110, v110, v111\n"
    "v_mul_f32 v111, -v110, v109\n"
    "v_mfma_f32_16x16x32_bf16 v[132:135], a[164:167], v[40:43], v[132:135]\n"
    "v_ldexp_f32 v109, v109, s79\n"
    "v_ldexp_f32 v113, v113, s79\n"
    "v_mfma_f32_16x16x32_bf16 v[124:127], a[244:247], v[40:43], v[124:127]\n"
    "ds_read_b128 a[144:147], v248 offset:40960\n"
    "v_mov_b32 v117, v116\n"
    "v_mfma_f32_16x16x32_bf16 v[140:143], a[240:243], v[44:47], v[140:143]\n"
    "v_permlane16_swap_b32 v118, v119\n"
    "v_mfma_f32_16x16x32_bf16 v[140:143], a[244:247], v[48:51], v[140:143]\n"
    "v_permlane16_swap_b32 v116, v117\n"
    "v_mfma_f32_16x16x32_bf16 v[144:147], a[232:235], v[12:15], v[144:147]\n"
    "v_fma_f32 v130, v130, v109, v111\n"
    "v_fma_f32 v131, v131, v109, v111\n"
    "v_mfma_f32_16x16x32_bf16 v[148:151], a[224:227], v[16:19], v[148:151]\n"
    "v_add_u32 v252, s72, v252\n"
    "v_fma_f32 v124, v124, v109, v111\n"
    "v_fma_f32 v125, v125, v109, v111\n"
    "v_fma_f32 v132, v132, v109, v111\n"
    "v_fma_f32 v133, v133, v109, v111\n"
    "v_pk_fma_f32 v[126:127], v[126:127], v[108:109], v[110:111] op_sel:[0,1,1] op_sel_hi:[1,1,1]\n"
    "v_pk_fma_f32 v[128:129], v[128:129], v[108:109], v[110:111] op_sel:[0,1,1] op_sel_hi:[1,1,1]\n"
    "v_pk_fma_f32 v[136:137], v[136:137], v[108:109], v[110:111] op_sel:[0,1,1] op_sel_hi:[1,1,1]\n"
    "v_pk_fma_f32 v[140:141], v[140:141], v[112:113], v[114:115] op_sel:[0,1,1] op_sel_hi:[1,1,1]\n"
    "v_pk_fma_f32 v[134:135], v[134:135], v[108:109], v[110:111] op_sel:[0,1,1] op_sel_hi:[1,1,1]\n"
    "v_cvt_pk_bf16_f32 v124, v124, v128\n"
    "v_cvt_pk_bf16_f32 v128, v126, v130\n"
    "v_mfma_f32_16x16x32_bf16 v[104:107], a[140:143], a[172:175], v[104:107]\n"
    "v_cvt_pk_bf16_f32 v130, v127, v131\n"
    "v_cvt_pk_bf16_f32 v127, v133, v137\n"
    "v_mfma_f32_16x16x32_bf16 v[152:155], a[204:207], v[212:215], v[152:155]\n"
    "v_cvt_pk_bf16_f32 v126, v125, v129\n"
    "v_cvt_pk_bf16_f32 v125, v132, v136\n"
    "v_mfma_f32_16x16x32_bf16 v[152:155], a[220:223], v[16:19], v[152:155]\n"
    "ds_write_b128 v3, v[124:127] offset:0\n"
    "v_mfma_f32_16x16x32_bf16 v[148:151], a[160:163], v[44:47], v[148:151]\n"
    "v_fma_f32 v138, v138, v109, v111\n"
    "v_fma_f32 v139, v139, v109, v111\n"
    "v_mfma_f32_16x16x32_bf16 v[144:147], a[248:251], v[48:51], v[144:147]\n"
    "v_cvt_pk_bf16_f32 v129, v134, v138\n"
    "v_cvt_pk_bf16_f32 v131, v135, v139\n"
    "v_mfma_f32_16x16x32_bf16 v[144:147], a[252:255], v[44:47], v[144:147]\n"
    "ds_write_b128 v2, v[128:131] offset:0\n"
    "v_mfma_f32_16x16x32_bf16 v[160:163], a[236:239], v[24:27], v[160:163]\n"
    "v_add_f32 v116, v116, v117\n"
    "v_add_f32 v118, v118, v119\n"
    "v_mfma_f32_16x16x32_bf16 v[148:151], a[164:167], v[48:51], v[148:151]\n"
    "v_exp_f32 v67, v67\n"
    "v_mfma_f32_16x16x32_bf16 v[168:171], a[192:195], v[88:91], v[168:171]\n"
    "v_fma_f32 v142, v142, v113, v115\n"
    "v_fma_f32 v143, v143, v113, v115\n"
    "v_mfma_f32_16x16x32_bf16 v[104:107], a[136:139], a[168:171], v[104:107]\n"
    "ds_read_b128 a[172:175], v249 offset:6144\n"
    "ds_read_b128 a[176:179], v249 offset:17408\n"
    "v_mfma_f32_16x16x32_bf16 v[152:155], a[216:219], v[12:15], v[152:155]\n"
    "v_fma_f32 v146, v146, v113, v115\n"
    "v_fma_f32 v147, v147, v113, v115\n"
    "v_mfma_f32_16x16x32_bf16 v[168:171], a[196:199], v[84:87], v[168:171]\n"
    "v_fma_f32 v148, v148, v113, v115\n"
    "v_fma_f32 v149, v149, v113, v115\n"
    "v_mfma_f32_16x16x32_bf16 v[152:155], a[156:159], v[48:51], v[152:155]\n"
    "ds_read_b128 v[136:139], v252 offset:1024\n"
    "ds_read_b128 a[116:119], v249 offset:18432\n"
    "v_mfma_f32_16x16x32_bf16 v[152:155], a[152:155], v[44:47], v[152:155]\n"
    "v_fma_f32 v144, v144, v113, v115\n"
    "v_fma_f32 v145, v145, v113, v115\n"
    "v_mfma_f32_16x16x32_bf16 v[160:163], a[232:235], v[20:23], v[160:163]\n"
    "v_cvt_pk_bf16_f32 v140, v140, v144\n"
    "v_cvt_pk_bf16_f32 v144, v142, v146\n"
    "v_mfma_f32_16x16x32_bf16 v[160:163], a[248:251], v[56:59], v[160:163]\n"
    "v_cvt_pk_bf16_f32 v142, v141, v145\n"
    "v_cvt_pk_bf16_f32 v146, v143, v147\n"
    "v_mfma_f32_16x16x32_bf16 v[156:159], a[208:211], v[20:23], v[156:159]\n"
    "v_fma_f32 v152, v152, v113, v115\n"
    "v_fma_f32 v153, v153, v113, v115\n"
    "v_mfma_f32_16x16x32_bf16 v[156:159], a[212:215], v[24:27], v[156:159]\n"
    "ds_read_b128 v[132:135], v252 offset:0\n"
    "v_cvt_pk_bf16_f32 v141, v148, v152\n"
    "v_mfma_f32_16x16x32_bf16 v[164:167], a[228:231], v[20:23], v[164:167]\n"
    "v_fma_f32 v150, v150, v113, v115\n"
    "v_fma_f32 v151, v151, v113, v115\n"
    "v_mfma_f32_16x16x32_bf16 v[164:167], a[224:227], v[24:27], v[164:167]\n"
    "ds_read_b128 a[184:187], v248 offset:44032\n"
    "v_cvt_pk_bf16_f32 v143, v149, v153\n"
    "v_mfma_f32_16x16x32_bf16 v[168:171], a[200:203], v[224:227], v[168:171]\n"
    "v_exp_f32 v105, v105\n"
    "v_mfma_f32_16x16x32_bf16 v[168:171], a[204:207], v[220:223], v[168:171]\n"
    "s_waitcnt lgkmcnt(3)\n"
    "buffer_store_dwordx4 v[136:139], v244, s[24:27], s75 offen offset:3072 sc1\n"
    "ds_write_b128 v3, v[140:143] offset:2048\n"
    "v_mfma_f32_16x16x32_bf16 v[172:175], a[208:211], v[28:31], v[172:175]\n"
    "v_fma_f32 v154, v154, v113, v115\n"
    "v_fma_f32 v155, v155, v113, v115\n"
    "v_mfma_f32_16x16x32_bf16 v[168:171], a[220:223], v[24:27], v[168:171]\n"
    "v_cvt_pk_bf16_f32 v145, v150, v154\n"
    "v_cvt_pk_bf16_f32 v147, v151, v155\n"
    "v_mfma_f32_16x16x32_bf16 v[184:187], a[192:195], v[96:99], v[184:187]\n"
    "v_exp_f32 v64, v64\n"
    "v_mfma_f32_16x16x32_bf16 v[156:159], a[240:243], v[52:55], v[156:159]\n"
    "s_waitcnt lgkmcnt(2)\n"
    "buffer_store_dwordx4 v[132:135], v244, s[24:27], s75 offen offset:0 sc1\n"
    "ds_write_b128 v2, v[144:147] offset:2048\n"
    "v_mfma_f32_16x16x32_bf16 v[180:183], a[228:231], v[28:31], v[180:183]\n"
    "v_exp_f32 v107, v107\n"
    "v_mfma_f32_16x16x32_bf16 v[168:171], a[216:219], v[20:23], v[168:171]\n"
    "v_exp_f32 v106, v106\n"
    "v_mfma_f32_16x16x32_bf16 v[156:159], a[244:247], v[56:59], v[156:159]\n"
    "v_rcp_f32 v117, v116\n"
    "v_mfma_f32_16x16x32_bf16 v[184:187], a[196:199], v[92:95], v[184:187]\n"
    "ds_read_b128 a[140:143], v248 offset:41984\n"
    "v_mul_f32 v119, -v118, v117\n"
    "v_mfma_f32_16x16x32_bf16 v[168:171], a[156:159], v[56:59], v[168:171]\n"
    "ds_read_b128 a[188:191], v248 offset:30720\n"
    "ds_read_b128 a[196:199], v248 offset:16384\n"
    "v_mfma_f32_16x16x32_bf16 v[160:163], a[252:255], v[52:55], v[160:163]\n"
    "ds_read_b128 v[148:151], v252 offset:2048\n"
    "v_ldexp_f32 v117, v117, s79\n"
    "v_mfma_f32_16x16x32_bf16 v[184:187], a[200:203], v[232:235], v[184:187]\n"
    "v_fma_f32 v156, v156, v117, v119\n"
    "v_fma_f32 v157, v157, v117, v119\n"
    "v_mfma_f32_16x16x32_bf16 v[180:183], a[224:227], v[32:35], v[180:183]\n"
    "v_exp_f32 v104, v104\n"
    "v_mfma_f32_16x16x32_bf16 v[180:183], a[160:163], v[60:63], v[180:183]\n"
    "ds_read_b128 a[136:139], v248 offset:29696\n"
    "ds_read_b128 v[152:155], v252 offset:3072\n"
    "v_mfma_f32_16x16x32_bf16 v[172:175], a[212:215], v[32:35], v[172:175]\n"
    "v_exp_f32_e64 v65, v65\n"
    "v_mfma_f32_16x16x32_bf16 v[184:187], a[204:207], v[228:231], v[184:187]\n"
    "v_fma_f32 v162, v162, v117, v119\n"
    "v_fma_f32 v163, v163, v117, v119\n"
    "v_mfma_f32_16x16x32_bf16 v[172:175], a[240:243], v[60:63], v[172:175]\n"
    "v_fma_f32 v158, v158, v117, v119\n"
    "v_fma_f32 v159, v159, v117, v119\n"
    "v_mfma_f32_16x16x32_bf16 v[164:167], a[160:163], v[52:55], v[164:167]\n"
    "v_cvt_pk_bf16_f32 v64, v64, v65\n"
    "v_cvt_pk_bf16_f32 v65, v66, v67\n"
    "v_mfma_f32_16x16x32_bf16 v[176:179], a[236:239], v[32:35], v[176:179]\n"
    "v_fma_f32 v160, v160, v117, v119\n"
    "v_fma_f32 v161, v161, v117, v119\n"
    "v_mfma_f32_16x16x32_bf16 v[164:167], a[164:167], v[56:59], v[164:167]\n"
    "ds_read_b128 a[228:231], v246 offset:4096\n"
    "ds_read_b128 a[124:127], v249 offset:16384\n"
    "v_mfma_f32_16x16x32_bf16 v[176:179], a[232:235], v[28:31], v[176:179]\n"
    "v_cvt_pk_bf16_f32 v66, v104, v105\n"
    "v_cvt_pk_bf16_f32 v67, v106, v107\n"
    "v_mfma_f32_16x16x32_bf16 v[184:187], a[220:223], v[32:35], v[184:187]\n"
    "v_cvt_pk_bf16_f32 v156, v156, v160\n"
    "v_cvt_pk_bf16_f32 v160, v158, v162\n"
    "v_mfma_f32_16x16x32_bf16 v[168:171], a[152:155], v[52:55], v[168:171]\n"
    "v_cvt_pk_bf16_f32 v158, v157, v161\n"
    "v_cvt_pk_bf16_f32 v162, v159, v163\n"
    "v_smfmac_f32_16x16x64_bf16 v[120:123], v[240:243], v[60:67], v253\n"
    "ds_read_b128 a[180:183], v249 offset:4096\n"
    "v_mfma_f32_16x16x32_bf16 v[180:183], a[164:167], v[64:67], v[180:183]\n"
    "ds_read_b128 a[208:211], v248 offset:6144\n"
    "ds_read_b128 a[200:203], v248 offset:17408\n"
    "v_mfma_f32_16x16x32_bf16 v[176:179], a[248:251], v[64:67], v[176:179]\n"
    "v_fma_f32 v168, v168, v117, v119\n"
    "v_fma_f32 v169, v169, v117, v119\n"
    "v_mfma_f32_16x16x32_bf16 v[176:179], a[252:255], v[60:63], v[176:179]\n"
    "ds_read_b128 a[148:151], v248 offset:28672\n"
    "ds_read_b128 a[232:235], v248 offset:7168\n"
    "v_mfma_f32_16x16x32_bf16 v[184:187], a[216:219], v[28:31], v[184:187]\n"
    "v_fma_f32 v164, v164, v117, v119\n"
    "v_fma_f32 v165, v165, v117, v119\n"
    "v_mfma_f32_16x16x32_bf16 v[172:175], a[244:247], v[64:67], v[172:175]\n"
    "v_cvt_pk_bf16_f32 v157, v164, v168\n"
    "v_cvt_pk_bf16_f32 v159, v165, v169\n"
    "v_mfma_f32_16x16x32_bf16 v[184:187], a[156:159], v[64:67], v[184:187]\n"
    "s_waitcnt lgkmcnt(7)\n"
    "buffer_store_dwordx4 v[152:155], v244, s[24:27], s76 offen offset:3072 sc1\n"
    "ds_write_b128 v3, v[156:159] offset:4096\n"
    "v_mfma_f32_16x16x32_bf16 v[184:187], a[152:155], v[60:63], v[184:187]\n"
    "v_fma_f32 v166, v166, v117, v119\n"
    "v_fma_f32 v167, v167, v117, v119\n"
    "v_mfma_f32_16x16x32_bf16 v[192:195], a[140:143], v[72:75], v[192:195]\n"
    "v_fma_f32 v170, v170, v117, v119\n"
    "v_fma_f32 v171, v171, v117, v119\n"
    "v_mfma_f32_16x16x32_bf16 v[196:199], a[188:191], v[68:71], v[196:199]\n"
    "ds_read_b128 a[240:243], v246 offset:6144\n"
    "v_add_f32_e64 v120, v120, v121\n"
    "v_mfma_f32_16x16x32_bf16 v[200:203], a[184:187], v[72:75], v[200:203]\n"
    "v_mov_b32 v123, v122\n"
    "v_mov_b32_e64 v121, v120\n"
    "v_mfma_f32_16x16x32_bf16 v[192:195], a[136:139], v[68:71], v[192:195]\n"
    "s_waitcnt lgkmcnt(3)\n"
    "ds_read_b128 a[224:227], v246 offset:16384\n"
    "ds_read_b128 a[204:207], v248 offset:5120\n"
    "v_mfma_f32_16x16x32_bf16 v[188:191], a[148:151], v[68:71], v[188:191]\n"
    "v_cvt_pk_bf16_f32 v161, v166, v170\n"
    "v_cvt_pk_bf16_f32 v163, v167, v171\n"
    "v_mfma_f32_16x16x32_bf16 v[196:199], a[132:135], v[72:75], v[196:199]\n"
    "ds_read_b128 a[192:195], v248 offset:4096\n"
    "ds_read_b128 a[168:171], v249 offset:19456\n"
    "v_mfma_f32_16x16x32_bf16 v[188:191], a[144:147], v[72:75], v[188:191]\n"
    "v_permlane16_swap_b32_e64 v122, v123\n"
    "v_mfma_f32_16x16x32_bf16 v[200:203], a[128:131], v[68:71], v[200:203]\n"
    "ds_read_b128 a[220:223], v246 offset:17408\n"
    "ds_read_b128 a[212:215], v248 offset:18432\n"
    "v_mfma_f32_16x16x32_bf16 v[188:191], a[180:183], v[204:207], v[188:191]\n"
    "buffer_store_dwordx4 v[148:151], v244, s[24:27], s76 offen offset:0 sc1\n"
    "ds_write_b128 v2, v[160:163] offset:4096\n"
    "v_mfma_f32_16x16x32_bf16 v[192:195], a[176:179], v[208:211], v[192:195]\n"
    "v_permlane16_swap_b32_e64 v120, v121\n"
    "v_mfma_f32_16x16x32_bf16 v[188:191], a[124:127], v[208:211], v[188:191]\n"
    "v_add_f32_e64 v120, v120, v121\n"
    "v_fma_f32 v250, |v110|, 4.0, v250\n"
    "v_mfma_f32_16x16x32_bf16 v[196:199], a[172:175], v[204:207], v[196:199]\n"
    "v_rcp_f32_e64 v121, v120\n"
    "v_mfma_f32_16x16x32_bf16 v[192:195], a[120:123], v[204:207], v[192:195]\n"
    "ds_read_b128 a[216:219], v246 offset:5120\n"
    "ds_read_b128 v[168:171], v252 offset:5120\n"
    "v_mfma_f32_16x16x32_bf16 v[196:199], a[116:119], v[208:211], v[196:199]\n"
    "s_waitcnt lgkmcnt(6)\n"
    "v_add_f32 v122, v122, v123\n"
    "v_mul_f32 v123, -v122, v121\n"
    "v_mfma_f32_16x16x32_bf16 v[188:191], a[192:195], v[4:7], v[188:191]\n"
    "s_waitcnt lgkmcnt(5)\n"
    "ds_read_b128 a[112:115], v249 offset:7168\n"
    "v_ldexp_f32 v121, v121, s79\n"
    "v_mfma_f32_16x16x32_bf16 v[200:203], a[168:171], v[208:211], v[200:203]\n"
    ".Lwp2:\n"
    "s_cbranch_execz .Lw3\n"
    "ds_read_b128 a[244:247], v246 offset:18432\n"
    "ds_read_b128 a[236:239], v248 offset:19456\n"
    "v_mfma_f32_16x16x32_bf16 v[196:199], a[208:211], v[4:7], v[196:199]\n"
    "v_fma_f32 v178, v178, v121, v123\n"
    "v_fma_f32 v179, v179, v121, v123\n"
    "v_mfma_f32_16x16x32_bf16 v[188:191], a[196:199], v[8:11], v[188:191]\n"
    "s_nop 0\n"
    "v_fma_f32 v180, v180, v121, v123\n"
    "v_fma_f32 v181, v181, v121, v123\n"
    "v_mfma_f32_16x16x32_bf16 v[188:191], a[228:231], v[36:39], v[188:191]\n"
    "v_fma_f32 v172, v172, v121, v123\n"
    "v_fma_f32 v173, v173, v121, v123\n"
    "v_mfma_f32_16x16x32_bf16 v[192:195], a[200:203], v[8:11], v[192:195]\n"
    "v_fma_f32 v184, v184, v121, v123\n"
    "v_fma_f32 v185, v185, v121, v123\n"
    "v_mfma_f32_16x16x32_bf16 v[192:195], a[204:207], v[4:7], v[192:195]\n"
    "s_waitcnt lgkmcnt(6)\n"
    "v_fma_f32 v174, v174, v121, v123\n"
    "v_fma_f32 v175, v175, v121, v123\n"
    "v_mfma_f32_16x16x32_bf16 v[196:199], a[212:215], v[8:11], v[196:199]\n"
    "v_fma_f32 v176, v176, v121, v123\n"
    "v_fma_f32 v177, v177, v121, v123\n"
    "v_mfma_f32_16x16x32_bf16 v[196:199], a[240:243], v[36:39], v[196:199]\n"
    "ds_read_b128 v[164:167], v252 offset:4096\n"
    "ds_read_b128 a[248:251], v246 offset:19456\n"
    "v_mfma_f32_16x16x32_bf16 v[192:195], a[220:223], v[40:43], v[192:195]\n"
    "s_waitcnt lgkmcnt(6)\n"
    "v_cvt_pk_bf16_f32 v172, v172, v176\n"
    "v_cvt_pk_bf16_f32 v176, v174, v178\n"
    "v_mfma_f32_16x16x32_bf16 v[192:195], a[216:219], v[36:39], v[192:195]\n"
    "v_cvt_pk_bf16_f32 v174, v173, v177\n"
    "v_cvt_pk_bf16_f32 v178, v175, v179\n"
    "v_mfma_f32_16x16x32_bf16 v[188:191], a[224:227], v[40:43], v[188:191]\n"
    "s_waitcnt lgkmcnt(4)\n"
    "v_cvt_pk_bf16_f32 v173, v180, v184\n"
    "v_cvt_pk_bf16_f32 v175, v181, v185\n"
    "v_mfma_f32_16x16x32_bf16 v[200:203], a[112:115], v[204:207], v[200:203]\n"
    "buffer_store_dwordx4 v[168:171], v244, s[24:27], s77 offen offset:3072 sc1\n"
    "ds_write_b128 v3, v[172:175] offset:0\n"
    "v_mfma_f32_16x16x32_bf16 a[12:15], a[184:187], v[80:83], a[12:15]\n"
    "v_fma_f32 v186, v186, v121, v123\n"
    "v_fma_f32 v187, v187, v121, v123\n"
    "v_mfma_f32_16x16x32_bf16 a[4:7], a[140:143], v[80:83], a[4:7]\n"
    "v_fma_f32 v192, v192, v109, v111\n"
    "v_fma_f32 v193, v193, v109, v111\n"
    "v_mfma_f32_16x16x32_bf16 a[4:7], a[136:139], v[76:79], a[4:7]\n"
    "s_waitcnt lgkmcnt(3)\n"
    "v_fma_f32 v182, v182, v121, v123\n"
    "v_fma_f32 v183, v183, v121, v123\n"
    "v_mfma_f32_16x16x32_bf16 v[200:203], a[236:239], v[8:11], v[200:203]\n"
    "ds_read_b128 a[252:255], v246 offset:7168\n"
    "v_cvt_pk_bf16_f32 v177, v182, v186\n"
    "v_mfma_f32_16x16x32_bf16 v[200:203], a[232:235], v[4:7], v[200:203]\n"
    "v_cvt_pk_bf16_f32 v179, v183, v187\n"
    "v_mfma_f32_16x16x32_bf16 v[196:199], a[244:247], v[40:43], v[196:199]\n"
    "s_waitcnt lgkmcnt(3)\n"
    "buffer_store_dwordx4 v[164:167], v244, s[24:27], s77 offen offset:0 sc1\n"
    "ds_write_b128 v2, v[176:179] offset:0\n"
    "v_mfma_f32_16x16x32_bf16 a[4:7], a[176:179], v[216:219], a[4:7]\n"
    "v_fma_f32 v188, v188, v109, v111\n"
    "v_fma_f32 v189, v189, v109, v111\n"
    "v_mfma_f32_16x16x32_bf16 a[4:7], a[120:123], v[212:215], a[4:7]\n"
    "s_waitcnt lgkmcnt(3)\n"
    "v_fma_f32 v194, v194, v109, v111\n"
    "v_fma_f32 v195, v195, v109, v111\n"
    "v_mfma_f32_16x16x32_bf16 v[200:203], a[248:251], v[40:43], v[200:203]\n"
    "v_fma_f32 v198, v198, v109, v111\n"
    "v_fma_f32 v199, v199, v109, v111\n"
    "v_mfma_f32_16x16x32_bf16 a[4:7], a[200:203], v[16:19], a[4:7]\n"
    "ds_read_b128 v[180:183], v252 offset:0\n"
    "v_cvt_pk_bf16_f32 v188, v188, v192\n"
    "v_mfma_f32_16x16x32_bf16 a[0:3], a[148:151], v[76:79], a[0:3]\n"
    "s_waitcnt lgkmcnt(2)\n"
    "v_fma_f32 v190, v190, v109, v111\n"
    "v_fma_f32 v191, v191, v109, v111\n"
    "v_mfma_f32_16x16x32_bf16 v[200:203], a[252:255], v[36:39], v[200:203]\n"
    "ds_read_b128 v[184:187], v252 offset:1024\n"
    "v_cvt_pk_bf16_f32 v192, v190, v194\n"
    "v_mfma_f32_16x16x32_bf16 a[4:7], a[204:207], v[12:15], a[4:7]\n"
    "ds_read_b128 a[152:155], v248 offset:33792\n"
    "v_cvt_pk_bf16_f32 v190, v189, v193\n"
    "v_mfma_f32_16x16x32_bf16 a[8:11], a[188:191], v[76:79], a[8:11]\n"
    "v_fma_f32 v196, v196, v109, v111\n"
    "v_fma_f32 v197, v197, v109, v111\n"
    "v_mfma_f32_16x16x32_bf16 a[8:11], a[132:135], v[80:83], a[8:11]\n"
    "v_cvt_pk_bf16_f32 v194, v191, v195\n"
    "v_mfma_f32_16x16x32_bf16 a[8:11], a[172:175], v[212:215], a[8:11]\n"
    "v_fma_f32 v202, v202, v109, v111\n"
    "v_fma_f32 v203, v203, v109, v111\n"
    "v_mfma_f32_16x16x32_bf16 a[4:7], a[220:223], v[48:51], a[4:7]\n"
    "v_cvt_pk_bf16_f32 v193, v198, v202\n"
    "v_cvt_pk_bf16_f32 v195, v199, v203\n"
    "v_mfma_f32_16x16x32_bf16 a[0:3], a[144:147], v[80:83], a[0:3]\n"
    "v_fma_f32 v200, v200, v109, v111\n"
    "v_fma_f32 v201, v201, v109, v111\n"
    "v_mfma_f32_16x16x32_bf16 a[12:15], a[128:131], v[76:79], a[12:15]\n"
    "v_mfma_f32_16x16x32_bf16 a[0:3], a[180:183], v[212:215], a[0:3]\n"
    "s_waitcnt lgkmcnt(1)\n"
    "buffer_store_dwordx4 v[184:187], v244, s[24:27], s78 offen offset:3072 sc1\n"
    "ds_write_b128 v2, v[192:195] offset:2048\n"
    "v_mfma_f32_16x16x32_bf16 a[4:7], a[216:219], v[44:47], a[4:7]\n"
    "v_cvt_pk_bf16_f32 v191, v197, v201\n"
    "v_cvt_pk_bf16_f32 v189, v196, v200\n"
    "v_mfma_f32_16x16x32_bf16 a[0:3], a[124:127], v[216:219], a[0:3]\n"
    "v_fma_f32 v250, v108, 4.0, v250\n"
    "v_mfma_f32_16x16x32_bf16 a[8:11], a[116:119], v[216:219], a[8:11]\n"
    "buffer_store_dwordx4 v[180:183], v244, s[24:27], s78 offen offset:0 sc1\n"
    "ds_write_b128 v3, v[188:191] offset:2048\n"
    "v_mfma_f32_16x16x32_bf16 a[0:3], a[192:195], v[12:15], a[0:3]\n"
    "v_mfma_f32_16x16x32_bf16 a[0:3], a[196:199], v[16:19], a[0:3]\n"
    "v_mfma_f32_16x16x32_bf16 a[12:15], a[168:171], v[216:219], a[12:15]\n"
    "v_fma_f32 v250, v109, s74, v250\n"
    "v_mfma_f32_16x16x32_bf16 a[0:3], a[228:231], v[44:47], a[0:3]\n"
    "v_mfma_f32_16x16x32_bf16 a[8:11], a[208:211], v[12:15], a[8:11]\n"
    "v_accvgpr_read_b32 v133, a6\n"
    "v_mfma_f32_16x16x32_bf16 a[0:3], a[224:227], v[48:51], a[0:3]\n"
    "v_accvgpr_read_b32 v125, a4\n"
    "v_mfma_f32_16x16x32_bf16 a[16:19], a[148:151], v[84:87], a[16:19]\n"
    "v_accvgpr_read_b32 v137, a7\n"
    "v_mfma_f32_16x16x32_bf16 a[8:11], a[212:215], v[16:19], a[8:11]\n"
    "v_mfma_f32_16x16x32_bf16 a[12:15], a[112:115], v[212:215], a[12:15]\n"
    "v_accvgpr_read_b32 v129, a5\n"
    "v_mfma_f32_16x16x32_bf16 a[8:11], a[240:243], v[44:47], a[8:11]\n"
    "v_fma_f32 v250, |v114|, 4.0, v250\n"
    "v_accvgpr_read_b32 v124, a0\n"
    "v_mfma_f32_16x16x32_bf16 a[12:15], a[236:239], v[16:19], a[12:15]\n"
    "v_accvgpr_read_b32 v132, a2\n"
    "v_accvgpr_read_b32 v136, a3\n"
    "v_mfma_f32_16x16x32_bf16 a[12:15], a[232:235], v[12:15], a[12:15]\n"
    "v_fma_f32 v124, v124, v113, v115\n"
    "v_fma_f32 v125, v125, v113, v115\n"
    "v_mfma_f32_16x16x32_bf16 a[12:15], a[248:251], v[48:51], a[12:15]\n"
    "v_accvgpr_read_b32 v128, a1\n"
    "v_fma_f32 v250, v112, 4.0, v250\n"
    "v_mfma_f32_16x16x32_bf16 a[16:19], a[144:147], v[88:91], a[16:19]\n"
    "v_fma_f32 v128, v128, v113, v115\n"
    "v_fma_f32 v129, v129, v113, v115\n"
    "v_mfma_f32_16x16x32_bf16 a[20:23], a[140:143], v[88:91], a[20:23]\n"
    "v_fma_f32 v132, v132, v113, v115\n"
    "v_fma_f32 v133, v133, v113, v115\n"
    "v_mfma_f32_16x16x32_bf16 a[12:15], a[252:255], v[44:47], a[12:15]\n"
    "ds_read_b128 v[200:203], v252 offset:3072\n"
    "ds_read_b128 v[196:199], v252 offset:2048\n"
    "v_mfma_f32_16x16x32_bf16 a[8:11], a[244:247], v[48:51], a[8:11]\n"
    "v_fma_f32 v136, v136, v113, v115\n"
    "v_fma_f32 v137, v137, v113, v115\n"
    "v_mfma_f32_16x16x32_bf16 a[20:23], a[136:139], v[84:87], a[20:23]\n"
    "s_nop 1\n"
    "v_accvgpr_read_b32 v135, a14\n"
    "v_mfma_f32_16x16x32_bf16 a[20:23], a[176:179], v[224:227], a[20:23]\n"
    "v_accvgpr_read_b32 v127, a12\n"
    "v_accvgpr_read_b32 v134, a10\n"
    "v_mfma_f32_16x16x32_bf16 a[20:23], a[120:123], v[220:223], a[20:23]\n"
    "v_accvgpr_read_b32 v131, a13\n"
    "v_accvgpr_read_b32 v138, a11\n"
    "v_mfma_f32_16x16x32_bf16 a[20:23], a[200:203], v[24:27], a[20:23]\n"
    "v_fma_f32 v134, v134, v113, v115\n"
    "v_fma_f32 v135, v135, v113, v115\n"
    "v_mfma_f32_16x16x32_bf16 a[20:23], a[204:207], v[20:23], a[20:23]\n"
    "v_accvgpr_read_b32 v139, a15\n"
    "v_accvgpr_read_b32 v130, a9\n"
    "v_mfma_f32_16x16x32_bf16 a[16:19], a[180:183], v[220:223], a[16:19]\n"
    "v_accvgpr_read_b32 v126, a8\n"
    "v_cvt_pk_bf16_f32 v124, v124, v125\n"
    "v_mfma_f32_16x16x32_bf16 a[20:23], a[220:223], v[56:59], a[20:23]\n"
    "v_fma_f32 v130, v130, v113, v115\n"
    "v_fma_f32 v131, v131, v113, v115\n"
    "v_mfma_f32_16x16x32_bf16 a[16:19], a[124:127], v[224:227], a[16:19]\n"
    "v_fma_f32 v126, v126, v113, v115\n"
    "v_fma_f32 v127, v127, v113, v115\n"
    "v_mfma_f32_16x16x32_bf16 a[24:27], a[188:191], v[84:87], a[24:27]\n"
    "v_cvt_pk_bf16_f32 v132, v132, v133\n"
    "v_mfma_f32_16x16x32_bf16 a[16:19], a[192:195], v[20:23], a[16:19]\n"
    "v_cvt_pk_bf16_f32 v133, v134, v135\n"
    "v_cvt_pk_bf16_f32 v125, v126, v127\n"
    "v_mfma_f32_16x16x32_bf16 a[24:27], a[132:135], v[88:91], a[24:27]\n"
    "ds_read_b128 a[160:163], v248 offset:32768\n"
    "v_cvt_pk_bf16_f32 v126, v128, v129\n"
    "v_mfma_f32_16x16x32_bf16 a[20:23], a[216:219], v[52:55], a[20:23]\n"
    "v_cvt_pk_bf16_f32 v134, v136, v137\n"
    "v_cvt_pk_bf16_f32 v127, v130, v131\n"
    "v_mfma_f32_16x16x32_bf16 a[28:31], a[184:187], v[88:91], a[28:31]\n"
    "s_waitcnt lgkmcnt(2)\n"
    "buffer_store_dwordx4 v[200:203], v244, s[24:27], s75 offen offset:3200 sc1\n"
    "ds_write_b128 v3, v[124:127] offset:4096\n"
    "v_mfma_f32_16x16x32_bf16 a[16:19], a[196:199], v[24:27], a[16:19]\n"
    "v_fma_f32 v138, v138, v113, v115\n"
    "v_fma_f32 v139, v139, v113, v115\n"
    "v_mfma_f32_16x16x32_bf16 a[16:19], a[228:231], v[52:55], a[16:19]\n"
    "v_accvgpr_read_b32 v145, a21\n"
    "v_cvt_pk_bf16_f32 v135, v138, v139\n"
    "v_mfma_f32_16x16x32_bf16 a[24:27], a[172:175], v[220:223], a[24:27]\n"
    "s_waitcnt lgkmcnt(2)\n"
    "buffer_store_dwordx4 v[196:199], v244, s[24:27], s75 offen offset:128 sc1\n"
    "ds_write_b128 v2, v[132:135] offset:4096\n"
    "v_mfma_f32_16x16x32_bf16 a[16:19], a[224:227], v[56:59], a[16:19]\n"
    "v_mfma_f32_16x16x32_bf16 a[28:31], a[128:131], v[84:87], a[28:31]\n"
    "v_mfma_f32_16x16x32_bf16 a[24:27], a[116:119], v[224:227], a[24:27]\n"
    "v_accvgpr_read_b32 v141, a20\n"
    "v_fma_f32 v250, v113, s74, v250\n"
    "v_mfma_f32_16x16x32_bf16 a[24:27], a[208:211], v[20:23], a[24:27]\n"
    "ds_read_b128 v[128:131], v252 offset:4096\n"
    "v_mfma_f32_16x16x32_bf16 a[24:27], a[212:215], v[24:27], a[24:27]\n"
    "ds_read_b128 a[164:167], v248 offset:45056\n"
    "v_mfma_f32_16x16x32_bf16 a[24:27], a[240:243], v[52:55], a[24:27]\n"
    "v_accvgpr_read_b32 v144, a17\n"
    "v_accvgpr_read_b32 v140, a16\n"
    "v_mfma_f32_16x16x32_bf16 a[24:27], a[244:247], v[56:59], a[24:27]\n"
    "ds_read_b128 v[136:139], v252 offset:5120\n"
    "ds_read_b128 a[156:159], v248 offset:46080\n"
    "v_mfma_f32_16x16x32_bf16 a[28:31], a[168:171], v[224:227], a[28:31]\n"
    "v_accvgpr_read_b32 v152, a19\n"
    "v_mfma_f32_16x16x32_bf16 a[28:31], a[112:115], v[220:223], a[28:31]\n"
    "v_fma_f32 v144, v144, v117, v119\n"
    "v_fma_f32 v145, v145, v117, v119\n"
    "v_mfma_f32_16x16x32_bf16 a[28:31], a[236:239], v[24:27], a[28:31]\n"
    "v_fma_f32 v140, v140, v117, v119\n"
    "v_fma_f32 v141, v141, v117, v119\n"
    "v_mfma_f32_16x16x32_bf16 a[28:31], a[232:235], v[20:23], a[28:31]\n"
    "v_accvgpr_read_b32 v149, a22\n"
    "v_accvgpr_read_b32 v154, a27\n"
    "v_mfma_f32_16x16x32_bf16 a[28:31], a[248:251], v[56:59], a[28:31]\n"
    "v_accvgpr_read_b32 v148, a18\n"
    "v_accvgpr_read_b32 v153, a23\n"
    "v_mfma_f32_16x16x32_bf16 a[28:31], a[252:255], v[52:55], a[28:31]\n"
    "s_waitcnt lgkmcnt(3)\n"
    "buffer_store_dwordx4 v[128:131], v244, s[24:27], s76 offen offset:128 sc1\n"
    "v_accvgpr_read_b32 v146, a25\n"
    "v_cvt_pk_bf16_f32 v140, v140, v141\n"
    "v_mfma_f32_16x16x32_bf16 a[32:35], a[148:151], v[92:95], a[32:35]\n"
    "v_fma_f32 v148, v148, v117, v119\n"
    "v_fma_f32 v149, v149, v117, v119\n"
    "v_mfma_f32_16x16x32_bf16 a[40:43], a[188:191], v[92:95], a[40:43]\n"
    "v_accvgpr_read_b32 v147, a29\n"
    "v_accvgpr_read_b32 v142, a24\n"
    "v_mfma_f32_16x16x32_bf16 a[36:39], a[140:143], v[96:99], a[36:39]\n"
    "v_accvgpr_read_b32 v143, a28\n"
    "v_mfma_f32_16x16x32_bf16 a[32:35], a[144:147], v[96:99], a[32:35]\n"
    "v_fma_f32 v142, v142, v117, v119\n"
    "v_fma_f32 v143, v143, v117, v119\n"
    "v_mfma_f32_16x16x32_bf16 a[40:43], a[132:135], v[96:99], a[40:43]\n"
    "v_fma_f32 v146, v146, v117, v119\n"
    "v_fma_f32 v147, v147, v117, v119\n"
    "v_mfma_f32_16x16x32_bf16 a[44:47], a[184:187], v[96:99], a[44:47]\n"
    "v_cvt_pk_bf16_f32 v141, v142, v143\n"
    "v_mfma_f32_16x16x32_bf16 a[36:39], a[136:139], v[92:95], a[36:39]\n"
    "ds_read_b128 a[140:143], v248 offset:48128\n"
    "v_mfma_f32_16x16x32_bf16 a[32:35], a[180:183], v[228:231], a[32:35]\n"
    "v_cvt_pk_bf16_f32 v142, v144, v145\n"
    "v_cvt_pk_bf16_f32 v143, v146, v147\n"
    "v_mfma_f32_16x16x32_bf16 a[44:47], a[128:131], v[92:95], a[44:47]\n"
    "s_waitcnt lgkmcnt(2)\n"
    "buffer_store_dwordx4 v[136:139], v244, s[24:27], s76 offen offset:3200 sc1\n"
    "ds_write_b128 v3, v[140:143] offset:0\n"
    "v_mfma_f32_16x16x32_bf16 a[32:35], a[124:127], v[232:235], a[32:35]\n"
    "v_fma_f32 v152, v152, v117, v119\n"
    "v_fma_f32 v153, v153, v117, v119\n"
    "v_mfma_f32_16x16x32_bf16 a[36:39], a[176:179], v[232:235], a[36:39]\n"
    "v_accvgpr_read_b32 v151, a30\n"
    "v_mfma_f32_16x16x32_bf16 a[32:35], a[192:195], v[28:31], a[32:35]\n"
    "ds_read_b128 a[148:151], v248 offset:34816\n"
    "v_accvgpr_read_b32 v155, a31\n"
    "v_mfma_f32_16x16x32_bf16 a[32:35], a[196:199], v[32:35], a[32:35]\n"
    "v_accvgpr_read_b32 v150, a26\n"
    "v_cvt_pk_bf16_f32 v148, v148, v149\n"
    "v_mfma_f32_16x16x32_bf16 a[36:39], a[120:123], v[228:231], a[36:39]\n"
    "v_fma_f32 v150, v150, v117, v119\n"
    "v_fma_f32 v151, v151, v117, v119\n"
    "v_mfma_f32_16x16x32_bf16 a[36:39], a[200:203], v[32:35], a[36:39]\n"
    "ds_read_b128 a[184:187], v249 offset:21504\n"
    "ds_read_b128 a[196:199], v248 offset:22528\n"
    "v_mfma_f32_16x16x32_bf16 a[32:35], a[228:231], v[60:63], a[32:35]\n"
    "ds_read_b128 a[136:139], v248 offset:35840\n"
    "v_mfma_f32_16x16x32_bf16 a[32:35], a[224:227], v[64:67], a[32:35]\n"
    "v_cvt_pk_bf16_f32 v149, v150, v151\n"
    "v_cvt_pk_bf16_f32 v150, v152, v153\n"
    "v_mfma_f32_16x16x32_bf16 a[36:39], a[204:207], v[28:31], a[36:39]\n"
    "v_fma_f32 v154, v154, v117, v119\n"
    "v_fma_f32 v155, v155, v117, v119\n"
    "v_mfma_f32_16x16x32_bf16 a[36:39], a[220:223], v[64:67], a[36:39]\n"
    "ds_read_b128 a[176:179], v249 offset:23552\n"
    "v_cvt_pk_bf16_f32 v151, v154, v155\n"
    "v_mfma_f32_16x16x32_bf16 a[36:39], a[216:219], v[60:63], a[36:39]\n"
    "ds_read_b128 a[128:131], v249 offset:9216\n"
    "ds_read_b128 a[228:231], v246 offset:10240\n"
    "v_mfma_f32_16x16x32_bf16 a[40:43], a[172:175], v[228:231], a[40:43]\n"
    "ds_read_b128 a[144:147], v248 offset:47104\n"
    "v_mfma_f32_16x16x32_bf16 a[40:43], a[116:119], v[232:235], a[40:43]\n"
    "ds_write_b128 v2, v[148:151] offset:0\n"
    "v_mfma_f32_16x16x32_bf16 a[40:43], a[208:211], v[28:31], a[40:43]\n"
    "v_mfma_f32_16x16x32_bf16 a[40:43], a[212:215], v[32:35], a[40:43]\n"
    "v_accvgpr_read_b32 v156, a32\n"
    "v_accvgpr_read_b32 v161, a37\n"
    "v_mfma_f32_16x16x32_bf16 a[44:47], a[168:171], v[232:235], a[44:47]\n"
    "ds_read_b128 v[144:147], v252 offset:0\n"
    "v_mfma_f32_16x16x32_bf16 a[40:43], a[240:243], v[60:63], a[40:43]\n"
    "ds_read_b128 a[220:223], v246 offset:23552\n"
    "ds_read_b128 a[188:191], v249 offset:8192\n"
    "v_mfma_f32_16x16x32_bf16 a[44:47], a[112:115], v[228:231], a[44:47]\n"
    "ds_read_b128 a[116:119], v248 offset:20480\n"
    "v_accvgpr_read_b32 v169, a39\n"
    "v_mfma_f32_16x16x32_bf16 a[44:47], a[236:239], v[32:35], a[44:47]\n"
    "ds_read_b128 v[152:155], v252 offset:1024\n"
    "v_accvgpr_read_b32 v160, a33\n"
    "v_mfma_f32_16x16x32_bf16 a[44:47], a[232:235], v[28:31], a[44:47]\n"
    "v_accvgpr_read_b32 v157, a36\n"
    "v_mfma_f32_16x16x32_bf16 a[44:47], a[248:251], v[64:67], a[44:47]\n"
    "ds_read_b128 a[172:175], v248 offset:8192\n"
    "ds_read_b128 a[132:135], v249 offset:20480\n"
    "v_mfma_f32_16x16x32_bf16 a[40:43], a[244:247], v[64:67], a[40:43]\n"
    "v_fma_f32 v156, v156, v121, v123\n"
    "v_fma_f32 v157, v157, v121, v123\n"
    "v_mfma_f32_16x16x32_bf16 a[48:51], a[160:163], v[68:71], a[48:51]\n"
    "ds_read_b128 a[180:183], v249 offset:10240\n"
    "ds_read_b128 a[168:171], v248 offset:21504\n"
    "v_mfma_f32_16x16x32_bf16 a[44:47], a[252:255], v[60:63], a[44:47]\n"
    "s_waitcnt lgkmcnt(8)\n"
    "buffer_store_dwordx4 v[144:147], v244, s[24:27], s77 offen offset:128 sc1\n"
    "v_accvgpr_read_b32 v168, a35\n"
    "ds_read_b128 a[112:115], v248 offset:9216\n"
    "v_mfma_f32_16x16x32_bf16 a[60:63], a[140:143], v[72:75], a[60:63]\n"
    "ds_read_b128 a[212:215], v246 offset:20480\n"
    "v_cvt_pk_bf16_f32 v156, v156, v157\n"
    "v_mfma_f32_16x16x32_bf16 a[48:51], a[164:167], v[72:75], a[48:51]\n"
    "ds_read_b128 a[124:127], v249 offset:22528\n"
    "v_accvgpr_read_b32 v158, a40\n"
    "v_mfma_f32_16x16x32_bf16 a[52:55], a[156:159], v[72:75], a[52:55]\n"
    "ds_read_b128 a[120:123], v249 offset:11264\n"
    "ds_read_b128 a[208:211], v246 offset:8192\n"
    "v_mfma_f32_16x16x32_bf16 a[52:55], a[152:155], v[68:71], a[52:55]\n"
    "s_waitcnt lgkmcnt(11)\n"
    "v_fma_f32 v160, v160, v121, v123\n"
    "v_fma_f32 v161, v161, v121, v123\n"
    "v_mfma_f32_16x16x32_bf16 a[48:51], a[188:191], v[204:207], a[48:51]\n"
    "v_accvgpr_read_b32 v163, a45\n"
    "v_accvgpr_read_b32 v159, a44\n"
    "v_mfma_f32_16x16x32_bf16 a[52:55], a[184:187], v[208:211], a[52:55]\n"
    "v_accvgpr_read_b32 v170, a43\n"
    "v_accvgpr_read_b32 v164, a34\n"
    "v_mfma_f32_16x16x32_bf16 a[56:59], a[148:151], v[68:71], a[56:59]\n"
    "ds_read_b128 a[224:227], v246 offset:22528\n"
    "v_accvgpr_read_b32 v165, a38\n"
    "v_mfma_f32_16x16x32_bf16 a[60:63], a[136:139], v[68:71], a[60:63]\n"
    "s_waitcnt lgkmcnt(8)\n"
    "ds_read_b128 a[192:195], v248 offset:10240\n"
    "v_accvgpr_read_b32 v166, a42\n"
    "v_mfma_f32_16x16x32_bf16 a[48:51], a[132:135], v[208:211], a[48:51]\n"
    "v_fma_f32 v164, v164, v121, v123\n"
    "v_fma_f32 v165, v165, v121, v123\n"
    "v_mfma_f32_16x16x32_bf16 a[48:51], a[172:175], v[4:7], a[48:51]\n"
    "ds_read_b128 a[200:203], v248 offset:23552\n"
    "v_accvgpr_read_b32 v162, a41\n"
    "v_mfma_f32_16x16x32_bf16 a[60:63], a[176:179], v[208:211], a[60:63]\n"
    "s_waitcnt lgkmcnt(4)\n"
    "ds_read_b128 a[204:207], v248 offset:11264\n"
    "v_accvgpr_read_b32 v167, a46\n"
    "v_mfma_f32_16x16x32_bf16 a[60:63], a[120:123], v[204:207], a[60:63]\n"
    "ds_read_b128 a[236:239], v246 offset:21504\n"
    "ds_read_b128 a[216:219], v246 offset:11264\n"
    "v_mfma_f32_16x16x32_bf16 a[56:59], a[144:147], v[72:75], a[56:59]\n"
    "v_fma_f32 v162, v162, v121, v123\n"
    "v_fma_f32 v163, v163, v121, v123\n"
    "v_mfma_f32_16x16x32_bf16 a[48:51], a[116:119], v[8:11], a[48:51]\n"
    "v_fma_f32 v158, v158, v121, v123\n"
    "v_fma_f32 v159, v159, v121, v123\n"
    "v_mfma_f32_16x16x32_bf16 a[52:55], a[128:131], v[204:207], a[52:55]\n"
    "s_waitcnt lgkmcnt(6)\n"
    "ds_read_b128 a[232:235], v246 offset:9216\n"
    "v_cvt_pk_bf16_f32 v157, v158, v159\n"
    "v_mfma_f32_16x16x32_bf16 a[48:51], a[208:211], v[36:39], a[48:51]\n"
    "v_cvt_pk_bf16_f32 v164, v164, v165\n"
    "v_cvt_pk_bf16_f32 v159, v162, v163\n"
    "v_mfma_f32_16x16x32_bf16 a[52:55], a[168:171], v[8:11], a[52:55]\n"
    "v_accvgpr_read_b32 v171, a47\n"
    "v_cvt_pk_bf16_f32 v158, v160, v161\n"
    "v_mfma_f32_16x16x32_bf16 a[52:55], a[112:115], v[4:7], a[52:55]\n"
    "buffer_store_dwordx4 v[152:155], v244, s[24:27], s77 offen offset:3200 sc1\n"
    "ds_write_b128 v3, v[156:159] offset:2048\n"
    "v_mfma_f32_16x16x32_bf16 a[68:71], a[156:159], v[80:83], a[68:71]\n"
    "v_fma_f32 v168, v168, v121, v123\n"
    "v_fma_f32 v169, v169, v121, v123\n"
    "v_mfma_f32_16x16x32_bf16 a[48:51], a[212:215], v[40:43], a[48:51]\n"
    "s_waitcnt lgkmcnt(3)\n"
    "v_fma_f32 v170, v170, v121, v123\n"
    "v_fma_f32 v171, v171, v121, v123\n"
    "v_mfma_f32_16x16x32_bf16 a[52:55], a[236:239], v[40:43], a[52:55]\n"
    "v_fma_f32 v166, v166, v121, v123\n"
    "v_fma_f32 v167, v167, v121, v123\n"
    "v_mfma_f32_16x16x32_bf16 a[56:59], a[180:183], v[204:207], a[56:59]\n"
    "v_cvt_pk_bf16_f32 v165, v166, v167\n"
    "v_cvt_pk_bf16_f32 v167, v170, v171\n"
    "v_mfma_f32_16x16x32_bf16 a[56:59], a[124:127], v[208:211], a[56:59]\n"
    "s_waitcnt lgkmcnt(1)\n"
    "v_fma_f32 v250, |v118|, 4.0, v250\n"
    "v_fma_f32 v250, v116, 4.0, v250\n"
    "v_mfma_f32_16x16x32_bf16 a[52:55], a[232:235], v[36:39], a[52:55]\n"
    "v_accvgpr_read_b32 v172, a48\n"
    "v_cvt_pk_bf16_f32 v166, v168, v169\n"
    "v_mfma_f32_16x16x32_bf16 a[56:59], a[192:195], v[4:7], a[56:59]\n"
    "ds_write_b128 v2, v[164:167] offset:2048\n"
    "v_mfma_f32_16x16x32_bf16 a[60:63], a[200:203], v[8:11], a[60:63]\n"
    "v_accvgpr_read_b32 v176, a49\n"
    "v_mfma_f32_16x16x32_bf16 a[56:59], a[196:199], v[8:11], a[56:59]\n"
    "v_mfma_f32_16x16x32_bf16 a[76:79], a[140:143], v[80:83], a[76:79]\n"
    "ds_read_b128 v[168:171], v252 offset:3072\n"
    "v_accvgpr_read_b32 v180, a50\n"
    "v_mfma_f32_16x16x32_bf16 a[60:63], a[204:207], v[4:7], a[60:63]\n"
    "v_accvgpr_read_b32 v173, a52\n"
    "v_accvgpr_read_b32 v184, a51\n"
    "v_mfma_f32_16x16x32_bf16 a[56:59], a[228:231], v[36:39], a[56:59]\n"
    "v_mfma_f32_16x16x32_bf16 a[60:63], a[220:223], v[40:43], a[60:63]\n"
    "v_accvgpr_read_b32 v181, a54\n"
    "v_accvgpr_read_b32 v185, a55\n"
    "v_mfma_f32_16x16x32_bf16 a[56:59], a[224:227], v[40:43], a[56:59]\n"
    "v_fma_f32 v172, v172, v109, v111\n"
    "v_fma_f32 v173, v173, v109, v111\n"
    "v_mfma_f32_16x16x32_bf16 a[60:63], a[216:219], v[36:39], a[60:63]\n"
    "v_fma_f32 v180, v180, v109, v111\n"
    "v_fma_f32 v181, v181, v109, v111\n"
    "v_mfma_f32_16x16x32_bf16 a[68:71], a[152:155], v[76:79], a[68:71]\n"
    "v_fma_f32 v184, v184, v109, v111\n"
    "v_fma_f32 v185, v185, v109, v111\n"
    "v_mfma_f32_16x16x32_bf16 a[68:71], a[184:187], v[216:219], a[68:71]\n"
    "ds_read_b128 v[160:163], v252 offset:2048\n"
    "v_accvgpr_read_b32 v182, a58\n"
    "v_mfma_f32_16x16x32_bf16 a[64:67], a[160:163], v[76:79], a[64:67]\n"
    "s_waitcnt lgkmcnt(1)\n"
    "buffer_store_dwordx4 v[168:171], v244, s[24:27], s78 offen offset:3200 sc1\n"
    "v_accvgpr_read_b32 v177, a53\n"
    "v_accvgpr_read_b32 v187, a63\n"
    "v_accvgpr_read_b32 v186, a59\n"
    "v_accvgpr_read_b32 v183, a62\n"
    "v_mfma_f32_16x16x32_bf16 a[68:71], a[128:131], v[212:215], a[68:71]\n"
    "v_fma_f32 v182, v182, v109, v111\n"
    "v_fma_f32 v183, v183, v109, v111\n"
    "v_mfma_f32_16x16x32_bf16 a[64:67], a[164:167], v[80:83], a[64:67]\n"
    "v_fma_f32 v186, v186, v109, v111\n"
    "v_fma_f32 v187, v187, v109, v111\n"
    "v_mfma_f32_16x16x32_bf16 a[72:75], a[148:151], v[76:79], a[72:75]\n"
    "v_accvgpr_read_b32 v178, a57\n"
    "v_mfma_f32_16x16x32_bf16 a[68:71], a[168:171], v[16:19], a[68:71]\n"
    "v_cvt_pk_bf16_f32 v180, v180, v181\n"
    "v_accvgpr_read_b32 v179, a61\n"
    "v_mfma_f32_16x16x32_bf16 a[64:67], a[188:191], v[212:215], a[64:67]\n"
    "v_cvt_pk_bf16_f32 v181, v182, v183\n"
    "v_cvt_pk_bf16_f32 v182, v184, v185\n"
    "v_mfma_f32_16x16x32_bf16 a[72:75], a[144:147], v[80:83], a[72:75]\n"
    "v_accvgpr_read_b32 v175, a60\n"
    "v_cvt_pk_bf16_f32 v183, v186, v187\n"
    "v_mfma_f32_16x16x32_bf16 a[64:67], a[132:135], v[216:219], a[64:67]\n"
    "v_accvgpr_read_b32 v174, a56\n"
    "v_mfma_f32_16x16x32_bf16 a[68:71], a[112:115], v[12:15], a[68:71]\n"
    "s_waitcnt lgkmcnt(0)\n"
    "buffer_store_dwordx4 v[160:163], v244, s[24:27], s78 offen offset:128 sc1\n"
    "ds_write_b128 v2, v[180:183] offset:4096\n"
    "v_mfma_f32_16x16x32_bf16 a[64:67], a[172:175], v[12:15], a[64:67]\n"
    "v_fma_f32 v174, v174, v109, v111\n"
    "v_fma_f32 v175, v175, v109, v111\n"
    "v_mfma_f32_16x16x32_bf16 a[76:79], a[136:139], v[76:79], a[76:79]\n"
    "v_cvt_pk_bf16_f32 v172, v172, v173\n"
    "v_cvt_pk_bf16_f32 v173, v174, v175\n"
    "v_mfma_f32_16x16x32_bf16 a[64:67], a[116:119], v[16:19], a[64:67]\n"
    "v_fma_f32 v178, v178, v109, v111\n"
    "v_fma_f32 v179, v179, v109, v111\n"
    "v_mfma_f32_16x16x32_bf16 a[64:67], a[208:211], v[44:47], a[64:67]\n"
    "v_fma_f32 v176, v176, v109, v111\n"
    "v_fma_f32 v177, v177, v109, v111\n"
    "v_mfma_f32_16x16x32_bf16 a[64:67], a[212:215], v[48:51], a[64:67]\n"
    "v_fma_f32 v250, v117, s74, v250\n"
    "v_mfma_f32_16x16x32_bf16 a[68:71], a[236:239], v[48:51], a[68:71]\n"
    "v_cvt_pk_bf16_f32 v174, v176, v177\n"
    "v_cvt_pk_bf16_f32 v175, v178, v179\n"
    "v_mfma_f32_16x16x32_bf16 a[68:71], a[232:235], v[44:47], a[68:71]\n"
    "ds_write_b128 v3, v[172:175] offset:4096\n"
    "v_mfma_f32_16x16x32_bf16 a[72:75], a[180:183], v[212:215], a[72:75]\n"
    "v_mfma_f32_16x16x32_bf16 a[76:79], a[176:179], v[216:219], a[76:79]\n"
    "v_accvgpr_read_b32 v200, a67\n"
    "v_mfma_f32_16x16x32_bf16 a[72:75], a[124:127], v[216:219], a[72:75]\n"
    "ds_read_b128 v[176:179], v252 offset:4096\n"
    "v_accvgpr_read_b32 v196, a66\n"
    "v_mfma_f32_16x16x32_bf16 a[76:79], a[120:123], v[212:215], a[76:79]\n"
    "v_accvgpr_read_b32 v197, a70\n"
    "v_accvgpr_read_b32 v188, a64\n"
    "v_mfma_f32_16x16x32_bf16 a[72:75], a[192:195], v[12:15], a[72:75]\n"
    "v_accvgpr_read_b32 v192, a65\n"
    "v_mfma_f32_16x16x32_bf16 a[76:79], a[200:203], v[16:19], a[76:79]\n"
    "ds_read_b128 v[184:187], v252 offset:5120\n"
    "v_accvgpr_read_b32 v201, a71\n"
    "v_mfma_f32_16x16x32_bf16 a[72:75], a[196:199], v[16:19], a[72:75]\n"
    "v_accvgpr_read_b32 v189, a68\n"
    "v_fma_f32 v250, |v122|, 4.0, v250\n"
    "v_mfma_f32_16x16x32_bf16 a[72:75], a[228:231], v[44:47], a[72:75]\n"
    "v_fma_f32 v188, v188, v113, v115\n"
    "v_fma_f32 v189, v189, v113, v115\n"
    "v_mfma_f32_16x16x32_bf16 a[72:75], a[224:227], v[48:51], a[72:75]\n"
    "v_fma_f32 v196, v196, v113, v115\n"
    "v_fma_f32 v197, v197, v113, v115\n"
    "v_mfma_f32_16x16x32_bf16 a[76:79], a[204:207], v[12:15], a[76:79]\n"
    "v_cvt_pk_bf16_f32 v188, v188, v189\n"
    "v_cvt_pk_bf16_f32 v196, v196, v197\n"
    "v_mfma_f32_16x16x32_bf16 a[76:79], a[220:223], v[48:51], a[76:79]\n"
    "s_waitcnt lgkmcnt(1)\n"
    "buffer_store_dwordx4 v[176:179], v244, s[24:27], s75 offen offset:256 sc1\n"
    "v_accvgpr_read_b32 v193, a69\n"
    "v_accvgpr_read_b32 v202, a75\n"
    "v_accvgpr_read_b32 v198, a74\n"
    "v_accvgpr_read_b32 v190, a72\n"
    "v_mfma_f32_16x16x32_bf16 a[76:79], a[216:219], v[44:47], a[76:79]\n"
    "v_fma_f32 v192, v192, v113, v115\n"
    "v_fma_f32 v193, v193, v113, v115\n"
    "v_mfma_f32_16x16x32_bf16 a[80:83], a[160:163], v[84:87], a[80:83]\n"
    "s_nop 0\n"
    "v_fma_f32 v200, v200, v113, v115\n"
    "v_fma_f32 v201, v201, v113, v115\n"
    "v_mfma_f32_16x16x32_bf16 a[88:91], a[148:151], v[84:87], a[88:91]\n"
    "s_nop 0\n"
    "v_accvgpr_read_b32 v194, a73\n"
    "v_fma_f32 v250, v120, 4.0, v250\n"
    "v_mfma_f32_16x16x32_bf16 a[84:87], a[156:159], v[88:91], a[84:87]\n"
    "v_accvgpr_read_b32 v191, a76\n"
    "v_accvgpr_read_b32 v195, a77\n"
    "v_mfma_f32_16x16x32_bf16 a[84:87], a[152:155], v[84:87], a[84:87]\n"
    "v_fma_f32 v194, v194, v113, v115\n"
    "v_fma_f32 v195, v195, v113, v115\n"
    "v_mfma_f32_16x16x32_bf16 a[84:87], a[184:187], v[224:227], a[84:87]\n"
    "v_fma_f32 v190, v190, v113, v115\n"
    "v_fma_f32 v191, v191, v113, v115\n"
    "v_mfma_f32_16x16x32_bf16 a[84:87], a[128:131], v[220:223], a[84:87]\n"
    ".Lwp3:\n"
    "s_cbranch_execz .Lw3\n"
    "v_cvt_pk_bf16_f32 v189, v190, v191\n"
    "v_cvt_pk_bf16_f32 v190, v192, v193\n"
    "v_mfma_f32_16x16x32_bf16 a[84:87], a[168:171], v[24:27], a[84:87]\n"
    "v_cvt_pk_bf16_f32 v191, v194, v195\n"
    "v_accvgpr_read_b32 v199, a78\n"
    "v_mfma_f32_16x16x32_bf16 a[84:87], a[112:115], v[20:23], a[84:87]\n"
    "s_waitcnt lgkmcnt(0)\n"
    "buffer_store_dwordx4 v[184:187], v244, s[24:27], s75 offen offset:3328 sc1\n"
    "ds_write_b128 v3, v[188:191] offset:0\n"
    "v_mfma_f32_16x16x32_bf16 a[80:83], a[164:167], v[88:91], a[80:83]\n"
    "v_fma_f32 v198, v198, v113, v115\n"
    "v_fma_f32 v199, v199, v113, v115\n"
    "v_mfma_f32_16x16x32_bf16 a[80:83], a[188:191], v[220:223], a[80:83]\n"
    "v_accvgpr_read_b32 v203, a79\n"
    "v_cvt_pk_bf16_f32 v197, v198, v199\n"
    "v_mfma_f32_16x16x32_bf16 a[84:87], a[236:239], v[56:59], a[84:87]\n"
    "v_fma_f32 v202, v202, v113, v115\n"
    "v_fma_f32 v203, v203, v113, v115\n"
    "v_mfma_f32_16x16x32_bf16 a[80:83], a[132:135], v[224:227], a[80:83]\n"
    "v_cvt_pk_bf16_f32 v198, v200, v201\n"
    "v_cvt_pk_bf16_f32 v199, v202, v203\n"
    "v_mfma_f32_16x16x32_bf16 a[92:95], a[140:143], v[88:91], a[92:95]\n"
    "ds_write_b128 v2, v[196:199] offset:0\n"
    "v_mfma_f32_16x16x32_bf16 a[80:83], a[172:175], v[20:23], a[80:83]\n"
    "v_fma_f32 v250, v121, s74, v250\n"
    "v_mfma_f32_16x16x32_bf16 a[84:87], a[232:235], v[52:55], a[84:87]\n"
    "v_mfma_f32_16x16x32_bf16 a[80:83], a[116:119], v[24:27], a[80:83]\n"
    "ds_read_b128 v[192:195], v252 offset:0\n"
    "v_mfma_f32_16x16x32_bf16 a[80:83], a[208:211], v[52:55], a[80:83]\n"
    "v_mfma_f32_16x16x32_bf16 a[88:91], a[144:147], v[88:91], a[88:91]\n"
    "v_mfma_f32_16x16x32_bf16 a[80:83], a[212:215], v[56:59], a[80:83]\n"
    "v_mfma_f32_16x16x32_bf16 a[92:95], a[136:139], v[84:87], a[92:95]\n"
    "s_nop 1\n"
    "v_accvgpr_read_b32 v133, a86\n"
    "v_mfma_f32_16x16x32_bf16 a[88:91], a[180:183], v[220:223], a[88:91]\n"
    "ds_read_b128 v[200:203], v252 offset:1024\n"
    "v_accvgpr_read_b32 v137, a87\n"
    "v_mfma_f32_16x16x32_bf16 a[92:95], a[176:179], v[224:227], a[92:95]\n"
    "v_accvgpr_read_b32 v136, a83\n"
    "v_accvgpr_read_b32 v132, a82\n"
    "v_mfma_f32_16x16x32_bf16 a[92:95], a[120:123], v[220:223], a[92:95]\n"
    "v_accvgpr_read_b32 v125, a84\n"
    "v_accvgpr_read_b32 v129, a85\n"
    "v_mfma_f32_16x16x32_bf16 a[92:95], a[200:203], v[24:27], a[92:95]\n"
    "s_waitcnt lgkmcnt(1)\n"
    "buffer_store_dwordx4 v[192:195], v244, s[24:27], s76 offen offset:256 sc1\n"
    "v_fma_f32 v136, v136, v117, v119\n"
    "v_fma_f32 v137, v137, v117, v119\n"
    "v_accvgpr_read_b32 v124, a80\n"
    "v_mfma_f32_16x16x32_bf16 a[92:95], a[204:207], v[20:23], a[92:95]\n"
    "v_fma_f32 v124, v124, v117, v119\n"
    "v_fma_f32 v125, v125, v117, v119\n"
    "v_mfma_f32_16x16x32_bf16 a[92:95], a[220:223], v[56:59], a[92:95]\n"
    "v_accvgpr_read_b32 v128, a81\n"
    "v_cmp_nlt_f32 s[54:55], |v250|, s73\n"
    "v_mfma_f32_16x16x32_bf16 a[88:91], a[124:127], v[224:227], a[88:91]\n"
    "v_mfma_f32_16x16x32_bf16 a[92:95], a[216:219], v[52:55], a[92:95]\n"
    "v_cvt_pk_bf16_f32 v124, v124, v125\n"
    "v_mfma_f32_16x16x32_bf16 a[88:91], a[192:195], v[20:23], a[88:91]\n"
    "v_fma_f32 v132, v132, v117, v119\n"
    "v_fma_f32 v133, v133, v117, v119\n"
    "v_mfma_f32_16x16x32_bf16 a[88:91], a[196:199], v[24:27], a[88:91]\n"
    "v_cvt_pk_bf16_f32 v132, v132, v133\n"
    "v_mfma_f32_16x16x32_bf16 a[88:91], a[228:231], v[52:55], a[88:91]\n"
    "s_waitcnt lgkmcnt(0)\n"
    "v_accvgpr_read_b32 v127, a92\n"
    "v_accvgpr_read_b32 v135, a94\n"
    "v_mfma_f32_16x16x32_bf16 a[88:91], a[224:227], v[56:59], a[88:91]\n"
    "s_or_b64 s[54:55], s[54:55], s[34:35]\n"
    "buffer_store_dwordx4 v[200:203], v244, s[24:27], s76 offen offset:3328 sc1\n"
    "v_fma_f32 v128, v128, v117, v119\n"
    "v_fma_f32 v129, v129, v117, v119\n"
    "v_accvgpr_read_b32 v139, a95\n"
    "v_accvgpr_read_b32 v131, a93\n"
    "v_mfma_f32_16x16x32_bf16 a[104:107], a[148:151], v[92:95], a[104:107]\n"
    "s_nop 0\n"
    "v_accvgpr_read_b32 v126, a88\n"
    "v_accvgpr_read_b32 v138, a91\n"
    "v_mfma_f32_16x16x32_bf16 a[100:103], a[156:159], v[96:99], a[100:103]\n"
    "v_accvgpr_read_b32 v134, a90\n"
    "v_accvgpr_read_b32 v130, a89\n"
    "v_mfma_f32_16x16x32_bf16 a[108:111], a[140:143], v[96:99], a[108:111]\n"
    "v_fma_f32 v130, v130, v117, v119\n"
    "v_fma_f32 v131, v131, v117, v119\n"
    "v_mfma_f32_16x16x32_bf16 a[96:99], a[160:163], v[92:95], a[96:99]\n"
    "v_fma_f32 v134, v134, v117, v119\n"
    "v_fma_f32 v135, v135, v117, v119\n"
    "v_mfma_f32_16x16x32_bf16 a[96:99], a[164:167], v[96:99], a[96:99]\n"
    "v_fma_f32 v126, v126, v117, v119\n"
    "v_fma_f32 v127, v127, v117, v119\n"
    "v_mfma_f32_16x16x32_bf16 a[100:103], a[152:155], v[92:95], a[100:103]\n"
    "v_cvt_pk_bf16_f32 v125, v126, v127\n"
    "v_cvt_pk_bf16_f32 v133, v134, v135\n"
    "v_mfma_f32_16x16x32_bf16 a[100:103], a[184:187], v[232:235], a[100:103]\n"
    "v_cvt_pk_bf16_f32 v127, v130, v131\n"
    "v_cvt_pk_bf16_f32 v126, v128, v129\n"
    "v_mfma_f32_16x16x32_bf16 a[96:99], a[188:191], v[228:231], a[96:99]\n"
    "ds_write_b128 v3, v[124:127] offset:2048\n"
    "v_mfma_f32_16x16x32_bf16 a[104:107], a[144:147], v[96:99], a[104:107]\n"
    "v_fma_f32 v138, v138, v117, v119\n"
    "v_fma_f32 v139, v139, v117, v119\n"
    "v_mfma_f32_16x16x32_bf16 a[96:99], a[132:135], v[232:235], a[96:99]\n"
    "v_cvt_pk_bf16_f32 v134, v136, v137\n"
    "v_cvt_pk_bf16_f32 v135, v138, v139\n"
    "v_mfma_f32_16x16x32_bf16 a[96:99], a[172:175], v[28:31], a[96:99]\n"
    "ds_write_b128 v2, v[132:135] offset:2048\n"
    "v_mfma_f32_16x16x32_bf16 a[100:103], a[128:131], v[228:231], a[100:103]\n"
    "v_mfma_f32_16x16x32_bf16 a[108:111], a[136:139], v[92:95], a[108:111]\n"
    "v_mfma_f32_16x16x32_bf16 a[108:111], a[176:179], v[232:235], a[108:111]\n"
    "ds_read_b128 v[136:139], v252 offset:3072\n"
    "ds_read_b128 v[128:131], v252 offset:2048\n"
    "v_mfma_f32_16x16x32_bf16 a[96:99], a[116:119], v[32:35], a[96:99]\n"
    "v_mfma_f32_16x16x32_bf16 a[100:103], a[168:171], v[32:35], a[100:103]\n"
    "v_mfma_f32_16x16x32_bf16 a[96:99], a[208:211], v[60:63], a[96:99]\n"
    "v_mfma_f32_16x16x32_bf16 a[96:99], a[212:215], v[64:67], a[96:99]\n"
    "v_mfma_f32_16x16x32_bf16 a[108:111], a[120:123], v[228:231], a[108:111]\n"
    "v_mfma_f32_16x16x32_bf16 a[100:103], a[112:115], v[28:31], a[100:103]\n"
    "v_mfma_f32_16x16x32_bf16 a[100:103], a[236:239], v[64:67], a[100:103]\n"
    "v_mfma_f32_16x16x32_bf16 a[108:111], a[200:203], v[32:35], a[108:111]\n"
    "s_waitcnt lgkmcnt(1)\n"
    "buffer_store_dwordx4 v[136:139], v244, s[24:27], s77 offen offset:3328 sc1\n"
    "v_mfma_f32_16x16x32_bf16 a[100:103], a[232:235], v[60:63], a[100:103]\n"
    "s_nop 0\n"
    "v_accvgpr_read_b32 v148, a98\n"
    "v_mfma_f32_16x16x32_bf16 a[104:107], a[180:183], v[228:231], a[104:107]\n"
    "v_accvgpr_read_b32 v140, a96\n"
    "v_mfma_f32_16x16x32_bf16 a[108:111], a[204:207], v[28:31], a[108:111]\n"
    "s_waitcnt lgkmcnt(0)\n"
    "buffer_store_dwordx4 v[128:131], v244, s[24:27], s77 offen offset:256 sc1\n"
    "s_nop 0\n"
    "v_accvgpr_read_b32 v153, a103\n"
    "v_accvgpr_read_b32 v152, a99\n"
    "v_accvgpr_read_b32 v145, a101\n"
    "v_mfma_f32_16x16x32_bf16 a[104:107], a[124:127], v[232:235], a[104:107]\n"
    "v_accvgpr_read_b32 v149, a102\n"
    "v_mfma_f32_16x16x32_bf16 a[108:111], a[220:223], v[64:67], a[108:111]\n"
    "v_accvgpr_read_b32 v141, a100\n"
    "v_mfma_f32_16x16x32_bf16 a[108:111], a[216:219], v[60:63], a[108:111]\n"
    "v_fma_f32 v152, v152, v121, v123\n"
    "v_fma_f32 v153, v153, v121, v123\n"
    "v_mfma_f32_16x16x32_bf16 a[104:107], a[192:195], v[28:31], a[104:107]\n"
    "v_fma_f32 v148, v148, v121, v123\n"
    "v_fma_f32 v149, v149, v121, v123\n"
    "v_mfma_f32_16x16x32_bf16 a[104:107], a[196:199], v[32:35], a[104:107]\n"
    "v_accvgpr_read_b32 v144, a97\n"
    "v_cvt_pk_bf16_f32 v148, v148, v149\n"
    "v_mfma_f32_16x16x32_bf16 a[104:107], a[228:231], v[60:63], a[104:107]\n"
    "v_accvgpr_read_b32 v143, a108\n"
    "v_mfma_f32_16x16x32_bf16 a[104:107], a[224:227], v[64:67], a[104:107]\n"
    "s_or_b32 %0, s54, s55\n"
    "v_accvgpr_read_b32 v147, a109\n"
    "v_fma_f32 v140, v140, v121, v123\n"
    "v_fma_f32 v141, v141, v121, v123\n"
    "v_cvt_pk_bf16_f32 v140, v140, v141\n"
    "v_accvgpr_read_b32 v151, a110\n"
    "v_accvgpr_read_b32 v155, a111\n"
    "v_pk_fma_f32 v[144:145], v[144:145], v[120:121], v[122:123] op_sel:[0,1,1] op_sel_hi:[1,1,1]\n"
    "v_accvgpr_read_b32 v142, a104\n"
    "v_pk_fma_f32 v[142:143], v[142:143], v[120:121], v[122:123] op_sel:[0,1,1] op_sel_hi:[1,1,1]\n"
    "v_accvgpr_read_b32 v146, a105\n"
    "v_pk_fma_f32 v[146:147], v[146:147], v[120:121], v[122:123] op_sel:[0,1,1] op_sel_hi:[1,1,1]\n"
    "v_cvt_pk_bf16_f32 v141, v142, v143\n"
    "v_cvt_pk_bf16_f32 v143, v146, v147\n"
    "v_cvt_pk_bf16_f32 v142, v144, v145\n"
    "ds_write_b128 v3, v[140:143] offset:4096\n"
    "v_accvgpr_read_b32 v150, a106\n"
    "v_accvgpr_read_b32 v154, a107\n"
    "v_fma_f32 v154, v154, v121, v123\n"
    "v_fma_f32 v155, v155, v121, v123\n"
    "v_pk_fma_f32 v[150:151], v[150:151], v[120:121], v[122:123] op_sel:[0,1,1] op_sel_hi:[1,1,1]\n"
    "v_cvt_pk_bf16_f32 v149, v150, v151\n"
    "v_cvt_pk_bf16_f32 v150, v152, v153\n"
    "v_cvt_pk_bf16_f32 v151, v154, v155\n"
    "ds_write_b128 v2, v[148:151] offset:4096\n"
    "ds_read_b128 v[144:147], v252 offset:4096\n"
    "ds_read_b128 v[152:155], v252 offset:5120\n"
    "s_waitcnt lgkmcnt(1)\n"
    "buffer_store_dwordx4 v[144:147], v244, s[24:27], s78 offen offset:256 sc1\n"
    "s_waitcnt lgkmcnt(0)\n"
    "buffer_store_dwordx4 v[152:155], v244, s[24:27], s78 offen offset:3328 sc1\n"
    "s_cmp_eq_u32 %0, 0\n"
    "s_cbranch_scc1 .Lfin\n"
    "s_waitcnt vmcnt(0)\n"
    ".Lfin:\n"
    ".Lend:\n"
    ".Lend2:\n"
    : "=&s"(bad), "+s"(ka) : "s"(b), "v"(tid)
    : "memory", "vcc", "s4", "s5", "s6", "s7", "s8", "s9", "s10", "s11", "s12", "s13", "s14", "s15", "s16", "s17", "s18", "s19", "s20", "s21", "s22", "s23", "s24", "s25", "s26", "s27", "s28", "s29", "s30", "s31", "s34", "s35", "s36", "s37", "s38", "s39", "s40", "s41", "s42", "s43", "s44", "s45", "s46", "s47", "s48", "s49", "s50", "s51", "s52", "s53", "s54", "s55", "s56", "s57", "s58", "s59", "s60", "s61", "s62", "s63", "s64", "s65", "s66", "s67", "s68", "s69", "s70", "s71", "s72", "s73", "s74", "s75", "s76", "s77", "s78", "s79", "s80", "s81", "s82", "s83", "s84", "s85", "s86", "s87", "s88", "s89", "s90", "s91", "s92", "s93", "s94", "s95", "s96", "s97", "s98", "s99", "v1", "v2", "v3", "v4", "v5", "v6", "v7", "v8", "v9", "v10", "v11", "v12", "v13", "v14", "v15", "v16", "v17", "v18", "v19", "v20", "v21", "v22", "v23", "v24", "v25", "v26", "v27", "v28", "v29", "v30", "v31", "v32", "v33", "v34", "v35", "v36", "v37", "v38", "v39", "v40", "v41", "v42", "v43", "v44", "v45", "v46", "v47", "v48", "v49", "v50", "v51", "v52", "v53", "v54", "v55", "v56", "v57", "v58", "v59", "v60", "v61", "v62", "v63", "v64", "v65", "v66", "v67", "v68", "v69", "v70", "v71", "v72", "v73", "v74", "v75", "v76", "v77", "v78", "v79", "v80", "v81", "v82", "v83", "v84", "v85", "v86", "v87", "v88", "v89", "v90", "v91", "v92", "v93", "v94", "v95", "v96", "v97", "v98", "v99", "v100", "v101", "v102", "v103", "v104", "v105", "v106", "v107", "v108", "v109", "v110", "v111", "v112", "v113", "v114", "v115", "v116", "v117", "v118", "v119", "v120", "v121", "v122", "v123", "v124", "v125", "v126", "v127", "v128", "v129", "v130", "v131", "v132", "v133", "v134", "v135", "v136", "v137", "v138", "v139", "v140", "v141", "v142", "v143", "v144", "v145", "v146", "v147", "v148", "v149", "v150", "v151", "v152", "v153", "v154", "v155", "v156", "v157", "v158", "v159", "v160", "v161", "v162", "v163", "v164", "v165", "v166", "v167", "v168", "v169", "v170", "v171", "v172", "v173", "v174", "v175", "v176", "v177", "v178", "v179", "v180", "v181", "v182", "v183", "v184", "v185", "v186", "v187", "v188", "v189", "v190", "v191", "v192", "v193", "v194", "v195", "v196", "v197", "v198", "v199", "v200", "v201", "v202", "v203", "v204", "v205", "v206", "v207", "v208", "v209", "v210", "v211", "v212", "v213", "v214", "v215", "v216", "v217", "v218", "v219", "v220", "v221", "v222", "v223", "v224", "v225", "v226", "v227", "v228", "v229", "v230", "v231", "v232", "v233", "v234", "v235", "v236", "v237", "v238", "v239", "v240", "v241", "v242", "v243", "v244", "v245", "v246", "v247", "v248", "v249", "v250", "v251", "v252", "v253", "v254", "v255", "a0", "a1", "a2", "a3", "a4", "a5", "a6", "a7", "a8", "a9", "a10", "a11", "a12", "a13", "a14", "a15", "a16", "a17", "a18", "a19", "a20", "a21", "a22", "a23", "a24", "a25", "a26", "a27", "a28", "a29", "a30", "a31", "a32", "a33", "a34", "a35", "a36", "a37", "a38", "a39", "a40", "a41", "a42", "a43", "a44", "a45", "a46", "a47", "a48", "a49", "a50", "a51", "a52", "a53", "a54", "a55", "a56", "a57", "a58", "a59", "a60", "a61", "a62", "a63", "a64", "a65", "a66", "a67", "a68", "a69", "a70", "a71", "a72", "a73", "a74", "a75", "a76", "a77", "a78", "a79", "a80", "a81", "a82", "a83", "a84", "a85", "a86", "a87", "a88", "a89", "a90", "a91", "a92", "a93", "a94", "a95", "a96", "a97", "a98", "a99", "a100", "a101", "a102", "a103", "a104", "a105", "a106", "a107", "a108", "a109", "a110", "a111", "a112", "a113", "a114", "a115", "a116", "a117", "a118", "a119", "a120", "a121", "a122", "a123", "a124", "a125", "a126", "a127", "a128", "a129", "a130", "a131", "a132", "a133", "a134", "a135", "a136", "a137", "a138", "a139", "a140", "a141", "a142", "a143", "a144", "a145", "a146", "a147", "a148", "a149", "a150", "a151", "a152", "a153", "a154", "a155", "a156", "a157", "a158", "a159", "a160", "a161", "a162", "a163", "a164", "a165", "a166", "a167", "a168", "a169", "a170", "a171", "a172", "a173", "a174", "a175", "a176", "a177", "a178", "a179", "a180", "a181", "a182", "a183", "a184", "a185", "a186", "a187", "a188", "a189", "a190", "a191", "a192", "a193", "a194", "a195", "a196", "a197", "a198", "a199", "a200", "a201", "a202", "a203", "a204", "a205", "a206", "a207", "a208", "a209", "a210", "a211", "a212", "a213", "a214", "a215", "a216", "a217", "a218", "a219", "a220", "a221", "a222", "a223", "a224", "a225", "a226", "a227", "a228", "a229", "a230", "a231", "a232", "a233", "a234", "a235", "a236", "a237", "a238", "a239", "a240", "a241", "a242", "a243", "a244", "a245", "a246", "a247", "a248", "a249", "a250", "a251", "a252", "a253", "a254", "a255");
  if (bad) {
    slow_rows((const Args*)ka, (b & 7) * 4 + ((b >> 3) & 3), (b >> 5) * 16 + (tid >> 6) * 128, tid & 63);
  }
}

extern "C" void kernel_launch(void* const* d_in, const int* in_sizes, int n_in,
                              void* d_out, int out_size, void* d_ws, size_t ws_size,
                              hipStream_t stream) {
  (void)hipFuncSetAttribute((const void*)attn_fwd, hipFuncAttributeMaxDynamicSharedMemorySize, SHM);
  attn_fwd<<<256, 256, SHM, stream>>>((const unsigned short*)d_in[0], (const int*)d_in[1], (const int*)d_in[2], (const unsigned short*)d_in[3], (const unsigned short*)d_in[4],
                                      (const unsigned short*)d_in[5], (const unsigned short*)d_in[6], (char*)d_ws, (unsigned short*)d_out);
}
```
